```python
import torch

def _softmax(x, axis=-1):
    return torch.softmax(x, dim=axis)

def _gelu(x):
    return torch.nn.functional.gelu(x, approximate='tanh')

def _silu(x):
    return torch.nn.functional.silu(x)

def _sigmoid(x):
    return torch.sigmoid(x)

def _layernorm(x, axis=-1, eps=1e-05):
    m = x.mean(dim=axis, keepdim=True)
    v = x.var(dim=axis, keepdim=True, unbiased=False)
    return (x - m) / torch.sqrt(v + eps)

def _scatter_add(tgt, idx, src, axis=0):
    return tgt.clone().index_add_(axis, idx, src)

def _segment_sum(src, seg, n):
    out = torch.zeros((n,) + src.shape[1:], dtype=src.dtype)
    return out.index_add_(0, seg, src)

def _pad(x, pw):
    flat = tuple((v for pair in reversed(pw) for v in pair))
    return torch.nn.functional.pad(x, flat)
kernel_name = 'standalone__tree_speculative_verify_tree_attention_over_candidate_token_tree__sampling'

def setup_inputs():

    _w = lambda a: (lambda t: t.to(torch.bfloat16) if t.is_floating_point() else t)(torch.as_tensor(a))
    B, Hq, Hkv, T, C, D = (16, 32, 8, 32, 4096, 128)
    tree_mask = torch.zeros((B, T, T), dtype=float)
    for b in range(B):
        parent = torch.zeros(T, dtype=torch.int64)
        for i in range(1, T):
            parent[i] = int(torch.randint(0, i, (1,)).item())
        for i in range(T):
            j = i
            tree_mask[b, i, j] = 1.0
            while j > 0:
                j = parent[j]
                tree_mask[b, i, j] = 1.0
    return {'q': _w((lambda *s: torch.randn(*s, dtype=torch.bfloat16) * 0.8)(B, Hq, T, D)), 'k_cache': _w((lambda *s: torch.randn(*s, dtype=torch.bfloat16) * 0.8)(B, Hkv, C, D)), 'v_cache': _w((lambda *s: torch.randn(*s, dtype=torch.bfloat16) * 0.05)(B, Hkv, C, D)), 'k_tree': _w((lambda *s: torch.randn(*s, dtype=torch.bfloat16) * 0.8)(B, Hkv, T, D)), 'v_tree': _w((lambda *s: torch.randn(*s, dtype=torch.bfloat16) * 0.05)(B, Hkv, T, D)), 'tree_mask': _w(tree_mask)}

def reference(q, k_cache, v_cache, k_tree, v_tree, tree_mask):
    B, Hq, T, D = q.shape
    Hkv, C = (k_cache.shape[1], k_cache.shape[2])
    rep = Hq // Hkv
    kc = (lambda x, n, axis: torch.repeat_interleave(x, n, dim=axis))(k_cache, rep, axis=1)
    vc = (lambda x, n, axis: torch.repeat_interleave(x, n, dim=axis))(v_cache, rep, axis=1)
    kt = (lambda x, n, axis: torch.repeat_interleave(x, n, dim=axis))(k_tree, rep, axis=1)
    vt = (lambda x, n, axis: torch.repeat_interleave(x, n, dim=axis))(v_tree, rep, axis=1)
    k = torch.concatenate([kc, kt], axis=2)
    v = torch.concatenate([vc, vt], axis=2)
    scale = D ** (-0.5)
    scores = torch.einsum('bhqd,bhkd->bhqk', q, k) * scale
    ctx_mask = torch.ones((B, 1, T, C), dtype=scores.dtype)
    tm = tree_mask[:, None, :, :].clone()
    full_mask = torch.concatenate([ctx_mask, tm], axis=-1)
    scores = scores + (1.0 - full_mask) * -1000000000.0
    p = _softmax(scores, axis=-1)
    return torch.einsum('bhqk,bhkd->bhqd', p, v)
```

```cpp
#include <hip/hip_runtime.h>
#include <hip/hip_bf16.h>
#include <stdint.h>

#pragma clang diagnostic ignored "-Wint-to-pointer-cast"
#pragma clang diagnostic ignored "-Winline-asm"

namespace {

constexpr int B_ = 16, HQ_ = 32, HKV_ = 8, T_ = 32, C_ = 4096, D_ = 128;
constexpr int TILE = 64;
constexpr int KIMG = TILE * D_ * 2;
constexpr int STAGE_BYTES = 2 * KIMG;
constexpr int TREE_OFF = 4 * STAGE_BYTES;
constexpr int Q_OFF = 3 * STAGE_BYTES;
constexpr int LDS_BYTES = TREE_OFF + 8192 + 1024;
constexpr unsigned C2 = 325, WIN = 36;
constexpr int CODE_BYTES = 21504;

constexpr size_t WS_EPOCH = 0;
constexpr size_t WS_REF = WS_EPOCH + 128 * 4 * 128;
constexpr size_t WS_X = WS_REF + 16384;
constexpr size_t XW_BYTES = 4096 + 512;
constexpr size_t WS_END = WS_X + (size_t)128 * 2 * 4 * XW_BYTES;

typedef float    f32x32 __attribute__((ext_vector_type(32)));
typedef float    f32x4  __attribute__((ext_vector_type(4)));
typedef unsigned long long u64x8 __attribute__((ext_vector_type(8)));
typedef unsigned u32x8  __attribute__((ext_vector_type(8)));
typedef unsigned u32x4  __attribute__((ext_vector_type(4)));
typedef unsigned u32x2  __attribute__((ext_vector_type(2)));
typedef __attribute__((address_space(3))) f32x4 lds_f32x4;
typedef __attribute__((address_space(1))) unsigned long long gu64;

#define DI __device__ __forceinline__

DI void dma16(unsigned lds_off, unsigned voff, __amdgpu_buffer_rsrc_t rsrc, unsigned soff) {
  asm volatile("s_mov_b32 m0, %0\n\ts_nop 0\n\tbuffer_load_dwordx4 %1, %2, %3 offen nt lds"
               :: "s"(lds_off), "v"(voff), "s"(rsrc), "s"(soff) : "memory");
}

__global__ __launch_bounds__(256, 1)
void tree_attn_main(const __hip_bfloat16*, const __hip_bfloat16*, const __hip_bfloat16*, const __hip_bfloat16*,
                    const __hip_bfloat16*, const __hip_bfloat16*, __hip_bfloat16*, char*) {
  u64x8 ka;
  asm volatile("s_load_dwordx16 s[4:19], %1, 0x0" : "={s[4:19]}"(ka) : "s"(__builtin_amdgcn_kernarg_segment_ptr()) : "memory");
  __shared__ __attribute__((aligned(1024))) char smem[LDS_BYTES];
  unsigned long long pc;
  asm volatile("s_getpc_b64 %0" : "=s"(pc));
  const int tid = threadIdx.x, lane = tid & 63;
  const int wid = __builtin_amdgcn_readfirstlane(tid >> 6);
  const int rh = wid & 1, kh = wid >> 1;
  const int xg = blockIdx.x & 7, jj = blockIdx.x >> 3;
  const int bk = xg * 16 + (jj >> 1), split = jj & 1;
  const int c16 = lane & 15, g = lane >> 4;
  const unsigned lds0 = (unsigned)(uintptr_t)smem;
  if (wid >= 2) {
    __amdgpu_buffer_rsrc_t srdC = __builtin_amdgcn_make_buffer_rsrc((void*)(pc & ~63ull), 0, CODE_BYTES, 0x00020000);
    asm volatile("s_mov_b32 m0, %0\n\ts_nop 0\n\tbuffer_load_dwordx4 %1, %2, %3 offen lds"
                 :: "s"(lds0 + TREE_OFF + 8192), "v"(lane * 16), "s"(srdC), "s"((unsigned)(wid == 3 ? min(jj, CODE_BYTES / 1024 - 1) : jj & 3) * 1024) : "memory");
  }

  const unsigned voffK = (unsigned)((8 * wid + (lane >> 3)) * 256 + (((lane & 7) ^ (4 * (wid & 1) + g)) << 4));
  const unsigned voffV = (unsigned)((8 * wid + (lane >> 3)) * 256 + (((lane & 7) ^ (g << 1)) << 4));
  const unsigned wbase = lds0 + (unsigned)wid * 2048;

  *(lds_f32x4*)(lds0 + KIMG + (1 - split) * 8192 + tid * 16) = f32x4{0.f, 0.f, 0.f, 0.f};
  *(lds_f32x4*)(lds0 + KIMG + (1 - split) * 8192 + 4096 + tid * 16) = f32x4{0.f, 0.f, 0.f, 0.f};
  u32x8 ad;
#pragma unroll
  for (int j = 0; j < 2; ++j)
    ad[j] = lds0 + (unsigned)(2048 * (c16 >> 3) + 128 * (c16 & 7) + (((4 * j + g) ^ (c16 >> 1)) << 4) + kh * 8192);
  {
    const int qq = c16 >> 2, p = lane & 3;
#pragma unroll
    for (int j = 0; j < 4; ++j)
      ad[2 + j] = lds0 + (unsigned)(2048 * (g >> 1) + 128 * (4 * (g & 1) + qq) + kh * 8192 + KIMG +
                                    (((2 * (j ^ (2 * kh)) + (p >> 1)) ^ ((2 * (g & 1) + (qq >> 1)) << 1)) << 4) + 8 * (p & 1));
  }
  ad[6] = voffK; ad[7] = voffV;
  u32x2 ra = {(unsigned)((lane >> 2) * 512 + (lane & 3) * 16), (unsigned)((blockIdx.x & 15) * 512 + (lane & 7) * 64)};
  unsigned mko = (unsigned)(((bk >> 3) * T_ + c16) * (T_ * 2) + g * 8);
  size_t qo = (size_t)bk * 128 * D_ * 2;
  f32x32 z1, z2, z3;
  asm volatile(
      "v_mov_b64 v[32:33], 0\n"
      "v_mov_b64 v[34:35], 0\n"
      "v_mov_b64 v[36:37], 0\n"
      "v_mov_b64 v[38:39], 0\n"
      "v_mov_b64 v[40:41], 0\n"
      "v_mov_b64 v[42:43], 0\n"
      "v_mov_b64 v[44:45], 0\n"
      "v_mov_b64 v[46:47], 0\n"
      "v_mov_b64 v[48:49], 0\n"
      "v_mov_b64 v[50:51], 0\n"
      "v_mov_b64 v[52:53], 0\n"
      "v_mov_b64 v[54:55], 0\n"
      "v_mov_b64 v[56:57], 0\n"
      "v_mov_b64 v[58:59], 0\n"
      "v_mov_b64 v[60:61], 0\n"
      "v_mov_b64 v[62:63], 0\n"
      "v_mov_b64 v[64:65], 0\n"
      "v_mov_b64 v[66:67], 0\n"
      "v_mov_b64 v[68:69], 0\n"
      "v_mov_b64 v[70:71], 0\n"
      "v_mov_b64 v[72:73], 0\n"
      "v_mov_b64 v[74:75], 0\n"
      "v_mov_b64 v[76:77], 0\n"
      "v_mov_b64 v[78:79], 0\n"
      "v_mov_b64 v[80:81], 0\n"
      "v_mov_b64 v[82:83], 0\n"
      "v_mov_b64 v[84:85], 0\n"
      "v_mov_b64 v[86:87], 0\n"
      "v_mov_b64 v[88:89], 0\n"
      "v_mov_b64 v[90:91], 0\n"
      "v_mov_b64 v[92:93], 0\n"
      "v_mov_b64 v[94:95], 0\n"
      "v_mov_b64 v[96:97], 0\n"
      "v_mov_b64 v[98:99], 0\n"
      "v_mov_b64 v[100:101], 0\n"
      "v_mov_b64 v[102:103], 0\n"
      "v_mov_b64 v[104:105], 0\n"
      "v_mov_b64 v[106:107], 0\n"
      "v_mov_b64 v[108:109], 0\n"
      "v_mov_b64 v[110:111], 0\n"
      "v_mov_b64 v[112:113], 0\n"
      "v_mov_b64 v[114:115], 0\n"
      "v_mov_b64 v[116:117], 0\n"
      "v_mov_b64 v[118:119], 0\n"
      "v_mov_b64 v[120:121], 0\n"
      "v_mov_b64 v[122:123], 0\n"
      "v_mov_b64 v[124:125], 0\n"
      "v_mov_b64 v[126:127], 0\n"
      : "={v[32:63]}"(z1), "={v[64:95]}"(z2), "={v[96:127]}"(z3));
  asm volatile("" : "+{v[242:249]}"(ad), "+{v[250:251]}"(ra), "+v"(mko), "+s"(qo) :: "memory");
  asm volatile("s_waitcnt lgkmcnt(0)" : "+{s[4:19]}"(ka) :: "memory");
  const char *qg = (const char*)ka[0], *kc = (const char*)ka[1], *vc = (const char*)ka[2], *kt = (const char*)ka[3],
             *vt = (const char*)ka[4], *tmask = (const char*)ka[5];
  char *out = (char*)ka[6], *ws = (char*)ka[7];
  __amdgpu_buffer_rsrc_t srdQ = __builtin_amdgcn_make_buffer_rsrc((void*)((const char*)qg + qo), 0, 128 * D_ * 2, 0x00020000);
#pragma unroll
  for (int i = 0; i < 8; ++i) dma16(wbase + Q_OFF + (i >> 1) * 8192 + (i & 1) * 1024, ad[6], srdQ, (i >> 1) * 8192 + (i & 1) * 128);
  u32x8 mk;
  asm volatile("s_cmp_lg_u32 %3, 0\n\ts_cbranch_scc1 1f\n\t"
               "global_load_dwordx2 v[208:209], %1, %2\n\tglobal_load_dwordx2 v[210:211], %1, %2 offset:32\n\t"
               "global_load_dwordx2 v[212:213], %1, %2 offset:1024\n\tglobal_load_dwordx2 v[214:215], %1, %2 offset:1056\n1:"
               : "=&{v[208:215]}"(mk) : "v"(mko), "s"(tmask), "s"(wid ^ (3 * split)) : "memory", "scc");
  unsigned tch;
  asm volatile("s_load_dword s92, %1, 0x0\n\ts_load_dword s92, %2, 0x0"
               : "={s92}"(tch) : "s"(kc + qo * 32), "s"(vc + qo * 32) : "memory");
  const size_t kvoff = ((size_t)bk * C_ + (size_t)split * 2 * TILE) * D_ * 2;
  const int kvbytes = (C_ - split * 2 * TILE) * D_ * 2;
  const unsigned so0 = (bk & 1) * KIMG, inc = KIMG - 2 * so0;
  __amdgpu_buffer_rsrc_t srdK = __builtin_amdgcn_make_buffer_rsrc((void*)((const char*)kc + kvoff), 0, kvbytes, 0x00020000);
  __amdgpu_buffer_rsrc_t srdV = __builtin_amdgcn_make_buffer_rsrc((void*)((const char*)vc + kvoff), 0, kvbytes, 0x00020000);
  __amdgpu_buffer_rsrc_t srdKt = __builtin_amdgcn_make_buffer_rsrc((void*)((const char*)kt + (size_t)bk * T_ * D_ * 2), 0, T_ * D_ * 2, 0x00020000);
  dma16(wbase + TREE_OFF, ad[6], srdKt, 0);
  dma16(wbase + TREE_OFF + 1024, ad[6], srdKt, 128);
  char* line = ws + WS_EPOCH + (size_t)(bk * 4 + wid) * 128;
  u32x2 epoch;
  asm volatile("global_load_dwordx2 v[216:217], %1, off sc1" : "=&{v[216:217]}"(epoch) : "v"(line) : "memory");
  __amdgpu_buffer_rsrc_t srdVt = __builtin_amdgcn_make_buffer_rsrc((void*)((const char*)vt + (size_t)bk * T_ * D_ * 2), 0, T_ * D_ * 2, 0x00020000);
  __amdgpu_buffer_rsrc_t srdR = __builtin_amdgcn_make_buffer_rsrc((void*)(ws + WS_REF), 0, 16384, 0x00020000);
  __amdgpu_buffer_rsrc_t srdX = __builtin_amdgcn_make_buffer_rsrc((void*)(ws + WS_X + (size_t)(bk * 8 + wid) * XW_BYTES), 0, (int)(5 * XW_BYTES), 0x00020000);
  const unsigned v0 = KIMG + 1024 * split, v1 = KIMG + 1024 * (1 - split) - 128;
  const unsigned n1 = 5 + (unsigned)((jj >> 2) + xg * 5) % 9u;
  unsigned e0, e1;
  asm volatile(
      "s_mov_b32 s60, %[so0]\n"
      "s_add_u32 s61, %[so0], 8192\n"
      "s_mov_b32 s79, %[inc]\n"
      "s_mov_b32 s62, %[wb]\n"
      "s_mov_b32 s70, 300\n"
      "s_mov_b32 s72, %[other]\n"
      "s_memrealtime s[82:83]\n"
      "s_mov_b32 s80, 0x443212a\n"
      "s_mov_b32 s81, 0x0\n"
      "s_mov_b32 s86, 108\n"
      "s_mov_b32 s87, 0\n"
      "s_mov_b32 s93, 29\n"
      "s_mov_b32 s89, 0\n"
      "s_lshl_b32 s91, %[wb], 18\n"
      "s_sub_u32 s91, 0x10000000, s91\n"
      "v_mov_b64 v[0:1], 0\n"
      "v_mov_b64 v[2:3], 0\n"
      "v_mov_b64 v[4:5], 0\n"
      "v_mov_b64 v[6:7], 0\n"
      "v_mov_b64 v[8:9], 0\n"
      "v_mov_b64 v[10:11], 0\n"
      "v_mov_b64 v[12:13], 0\n"
      "v_mov_b64 v[14:15], 0\n"
      "v_mov_b64 v[16:17], 0\n"
      "v_mov_b64 v[18:19], 0\n"
      "v_mov_b64 v[20:21], 0\n"
      "v_mov_b64 v[22:23], 0\n"
      "v_mov_b64 v[24:25], 0\n"
      "v_mov_b64 v[26:27], 0\n"
      "v_mov_b64 v[28:29], 0\n"
      "v_mov_b64 v[30:31], 0\n"
      "v_mov_b64 v[192:193], 0\n"
      "v_mov_b64 v[194:195], 0\n"
      "v_mov_b64 v[196:197], 0\n"
      "v_mov_b64 v[198:199], 0\n"
      "v_mov_b64 v[200:201], 0\n"
      "v_mov_b64 v[202:203], 0\n"
      "v_mov_b64 v[204:205], 0\n"
      "v_mov_b64 v[206:207], 0\n"
      "v_mov_b64 v[228:229], 0\n"
      "v_mov_b64 v[230:231], 0\n"
      "v_mov_b64 v[232:233], 0\n"
      "v_mov_b64 v[234:235], 0\n"
      "v_accvgpr_write_b32 a112, 0\n"
      "v_accvgpr_write_b32 a113, 0\n"
      "v_accvgpr_write_b32 a114, 0\n"
      "v_accvgpr_write_b32 a115, 0\n"
      "v_accvgpr_write_b32 a116, 0\n"
      "v_accvgpr_write_b32 a117, 0\n"
      "v_accvgpr_write_b32 a118, 0\n"
      "v_accvgpr_write_b32 a119, 0\n"
      "v_accvgpr_write_b32 a120, 0\n"
      "v_accvgpr_write_b32 a121, 0\n"
      "v_accvgpr_write_b32 a122, 0\n"
      "v_accvgpr_write_b32 a123, 0\n"
      "v_accvgpr_write_b32 a124, 0\n"
      "v_accvgpr_write_b32 a125, 0\n"
      "v_accvgpr_write_b32 a126, 0\n"
      "v_accvgpr_write_b32 a127, 0\n"
      "v_mov_b32 v224, 0x7f800000\n"
      "v_mov_b32 v225, 0x7f800000\n"
      "v_mov_b32 v226, 0x7f800000\n"
      "v_mov_b32 v227, 0x7f800000\n"
      "v_add_u32 v240, %[qoff], v242\n"
      "v_add_u32 v236, %[toff], v242\n"
      "v_add_u32 v241, %[qoff], v243\n"
      "v_add_u32 v237, %[toff], v243\n"
      "s_add_u32 m0, s62, 0\n"
      "s_nop 0\n"
      "buffer_load_dwordx4 v248, %[srdk], s60 offen nt lds\n"
      "s_add_u32 m0, s62, 896\n"
      "s_nop 0\n"
      "buffer_load_dwordx4 v248, %[srdk], s60 offen offset:128 nt lds\n"
      "s_add_u32 m0, s62, 8192\n"
      "s_nop 0\n"
      "buffer_load_dwordx4 v248, %[srdk], s61 offen nt lds\n"
      "s_add_u32 m0, s62, 9088\n"
      "s_nop 0\n"
      "buffer_load_dwordx4 v248, %[srdk], s61 offen offset:128 nt lds\n"
      "s_add_u32 m0, %[tv], %[v0]\n"
      "s_nop 0\n"
      "buffer_load_dwordx4 v249, %[srdt], 0 offen nt lds\n"
      "s_add_u32 m0, %[tv], %[v1]\n"
      "s_nop 0\n"
      "buffer_load_dwordx4 v249, %[srdt], 0 offen offset:128 nt lds\n"
      "s_mov_b32 s77, s60\n"
      "s_mov_b32 s78, s61\n"
      "s_add_u32 s60, s60, s79\n"
      "s_add_u32 s61, s61, s79\n"
      "s_sub_u32 s79, 0x10000, s79\n"
      "s_add_u32 s62, s62, 0x8000\n"
      "s_and_b32 s62, s62, 0x1ffff\n"
      "s_add_u32 m0, s62, 0\n"
      "s_nop 0\n"
      "buffer_load_dwordx4 v248, %[srdk], s60 offen nt lds\n"
      "s_add_u32 m0, s62, 896\n"
      "s_nop 0\n"
      "buffer_load_dwordx4 v248, %[srdk], s60 offen offset:128 nt lds\n"
      "s_add_u32 m0, s62, 8192\n"
      "s_nop 0\n"
      "buffer_load_dwordx4 v248, %[srdk], s61 offen nt lds\n"
      "s_add_u32 m0, s62, 9088\n"
      "s_nop 0\n"
      "buffer_load_dwordx4 v248, %[srdk], s61 offen offset:128 nt lds\n"
      "s_waitcnt vmcnt(11)\n"
      "s_barrier\n"
      "s_memrealtime s[68:69]\n"
      "ds_read_b128 a[64:67], v236 offset:0\n"
      "ds_read_b128 a[68:71], v236 offset:4096\n"
      "ds_read_b128 a[72:75], v237 offset:0\n"
      "ds_read_b128 a[76:79], v237 offset:4096\n"
      "ds_read_b128 a[80:83], v236 offset:1024\n"
      "ds_read_b128 a[84:87], v236 offset:5120\n"
      "ds_read_b128 a[88:91], v237 offset:1024\n"
      "ds_read_b128 a[92:95], v237 offset:5120\n"
      "ds_read_b128 a[0:3], v240 offset:0\n"
      "ds_read_b128 a[4:7], v240 offset:4096\n"
      "s_waitcnt lgkmcnt(7)\n"
      "ds_read_b128 a[8:11], v240 offset:8192\n"
      "ds_read_b128 a[12:15], v240 offset:12288\n"
      "ds_read_b128 a[16:19], v241 offset:0\n"
      "ds_read_b128 a[20:23], v241 offset:4096\n"
      "ds_read_b128 a[24:27], v241 offset:8192\n"
      "ds_read_b128 a[28:31], v241 offset:12288\n"
      "ds_read_b128 a[32:35], v240 offset:1024\n"
      "s_waitcnt lgkmcnt(7)\n"
      "ds_read_b128 a[36:39], v240 offset:5120\n"
      "ds_read_b128 a[40:43], v240 offset:9216\n"
      "ds_read_b128 a[44:47], v240 offset:13312\n"
      "ds_read_b128 a[48:51], v241 offset:1024\n"
      "ds_read_b128 a[52:55], v241 offset:5120\n"
      "ds_read_b128 a[56:59], v241 offset:9216\n"
      "ds_read_b128 a[60:63], v241 offset:13312\n"
      "s_add_u32 m0, s62, %[v0]\n"
      "s_nop 0\n"
      "buffer_load_dwordx4 v249, %[srdv], s77 offen nt lds\n"
      "s_add_u32 m0, s62, %[v1]\n"
      "s_nop 0\n"
      "buffer_load_dwordx4 v249, %[srdv], s77 offen offset:128 nt lds\n"
      "s_add_u32 m0, s62, %[v2]\n"
      "s_nop 0\n"
      "buffer_load_dwordx4 v249, %[srdv], s78 offen nt lds\n"
      "s_add_u32 m0, s62, %[v3]\n"
      "s_nop 0\n"
      "buffer_load_dwordx4 v249, %[srdv], s78 offen offset:128 nt lds\n"
      "s_mov_b32 s77, s60\n"
      "s_mov_b32 s78, s61\n"
      "s_add_u32 s60, s60, s79\n"
      "s_add_u32 s61, s61, s79\n"
      "s_sub_u32 s79, 0x10000, s79\n"
      "s_add_u32 s62, s62, 0x8000\n"
      "s_and_b32 s62, s62, 0x1ffff\n"
      "s_waitcnt lgkmcnt(0)\n"
      "s_mov_b32 s83, 0\n"
      "s_sub_u32 s67, s68, s82\n"
      "s_min_u32 s67, s67, 140\n"
      "s_add_u32 s66, s82, s67\n"
      "s_add_u32 s66, s66, 108\n"
      "s_mov_b32 s88, s66\n"
      "s_add_u32 s90, s88, s91\n"
      "s_sub_u32 s71, s68, 400\n"
      "s_cmp_lt_u32 %[lead], 512\n"
      "s_cbranch_scc0 NOLEAD_%=\n"
      "v_mov_b32 v236, s68\n"
      "v_mov_b32 v237, s68\n"
      "v_mov_b32 v238, s68\n"
      "v_mov_b32 v239, s68\n"
      "buffer_store_dwordx4 v[236:239], v250, %[srdr], %[lead] offen sc1\n"
      "s_nop 1\n"
      "NOLEAD_%=:\n"
      "s_cmp_lg_u32 s72, 0\n"
      "s_cbranch_scc1 NOTREE_%=\n"
      "v_mfma_f32_16x16x32_bf16 v[160:163], a[64:67], a[0:3], 0\n"
      "v_lshlrev_b32 v128, 16, v208\n"
      "v_and_b32 v129, 0xffff0000, v208\n"
      "v_mfma_f32_16x16x32_bf16 v[168:171], a[64:67], a[4:7], 0\n"
      "v_lshlrev_b32 v130, 16, v209\n"
      "v_and_b32 v131, 0xffff0000, v209\n"
      "v_mfma_f32_16x16x32_bf16 v[176:179], a[64:67], a[8:11], 0\n"
      "v_lshlrev_b32 v132, 16, v210\n"
      "v_and_b32 v133, 0xffff0000, v210\n"
      "v_mfma_f32_16x16x32_bf16 v[184:187], a[64:67], a[12:15], 0\n"
      "v_lshlrev_b32 v134, 16, v211\n"
      "v_and_b32 v135, 0xffff0000, v211\n"
      "v_mfma_f32_16x16x32_bf16 v[164:167], a[68:71], a[0:3], 0\n"
      "v_lshlrev_b32 v136, 16, v212\n"
      "v_and_b32 v137, 0xffff0000, v212\n"
      "v_mfma_f32_16x16x32_bf16 v[172:175], a[68:71], a[4:7], 0\n"
      "v_lshlrev_b32 v138, 16, v213\n"
      "v_and_b32 v139, 0xffff0000, v213\n"
      "v_mfma_f32_16x16x32_bf16 v[180:183], a[68:71], a[8:11], 0\n"
      "v_lshlrev_b32 v140, 16, v214\n"
      "v_and_b32 v141, 0xffff0000, v214\n"
      "v_mfma_f32_16x16x32_bf16 v[188:191], a[68:71], a[12:15], 0\n"
      "v_lshlrev_b32 v142, 16, v215\n"
      "v_and_b32 v143, 0xffff0000, v215\n"
      "v_mfma_f32_16x16x32_bf16 v[160:163], a[72:75], a[16:19], v[160:163]\n"
      "v_sub_f32 v128, 1.0, v128\n"
      "v_sub_f32 v129, 1.0, v129\n"
      "v_mfma_f32_16x16x32_bf16 v[168:171], a[72:75], a[20:23], v[168:171]\n"
      "v_sub_f32 v130, 1.0, v130\n"
      "v_sub_f32 v131, 1.0, v131\n"
      "v_mfma_f32_16x16x32_bf16 v[176:179], a[72:75], a[24:27], v[176:179]\n"
      "v_sub_f32 v132, 1.0, v132\n"
      "v_sub_f32 v133, 1.0, v133\n"
      "v_mfma_f32_16x16x32_bf16 v[184:187], a[72:75], a[28:31], v[184:187]\n"
      "v_sub_f32 v134, 1.0, v134\n"
      "v_sub_f32 v135, 1.0, v135\n"
      "v_mfma_f32_16x16x32_bf16 v[164:167], a[76:79], a[16:19], v[164:167]\n"
      "v_sub_f32 v136, 1.0, v136\n"
      "v_sub_f32 v137, 1.0, v137\n"
      "v_mfma_f32_16x16x32_bf16 v[172:175], a[76:79], a[20:23], v[172:175]\n"
      "v_sub_f32 v138, 1.0, v138\n"
      "v_sub_f32 v139, 1.0, v139\n"
      "v_mfma_f32_16x16x32_bf16 v[180:183], a[76:79], a[24:27], v[180:183]\n"
      "v_sub_f32 v140, 1.0, v140\n"
      "v_sub_f32 v141, 1.0, v141\n"
      "v_mfma_f32_16x16x32_bf16 v[188:191], a[76:79], a[28:31], v[188:191]\n"
      "v_sub_f32 v142, 1.0, v142\n"
      "v_sub_f32 v143, 1.0, v143\n"
      "v_mfma_f32_16x16x32_bf16 v[160:163], a[80:83], a[32:35], v[160:163]\n"
      "v_mfma_f32_16x16x32_bf16 v[168:171], a[80:83], a[36:39], v[168:171]\n"
      "v_mfma_f32_16x16x32_bf16 v[176:179], a[80:83], a[40:43], v[176:179]\n"
      "v_mfma_f32_16x16x32_bf16 v[184:187], a[80:83], a[44:47], v[184:187]\n"
      "v_mfma_f32_16x16x32_bf16 v[164:167], a[84:87], a[32:35], v[164:167]\n"
      "v_mfma_f32_16x16x32_bf16 v[172:175], a[84:87], a[36:39], v[172:175]\n"
      "v_mfma_f32_16x16x32_bf16 v[180:183], a[84:87], a[40:43], v[180:183]\n"
      "v_mfma_f32_16x16x32_bf16 v[188:191], a[84:87], a[44:47], v[188:191]\n"
      "v_mfma_f32_16x16x32_bf16 v[160:163], a[88:91], a[48:51], v[160:163]\n"
      "v_mfma_f32_16x16x32_bf16 v[168:171], a[88:91], a[52:55], v[168:171]\n"
      "v_mfma_f32_16x16x32_bf16 v[176:179], a[88:91], a[56:59], v[176:179]\n"
      "v_mfma_f32_16x16x32_bf16 v[184:187], a[88:91], a[60:63], v[184:187]\n"
      "v_mfma_f32_16x16x32_bf16 v[164:167], a[92:95], a[48:51], v[164:167]\n"
      "v_mfma_f32_16x16x32_bf16 v[172:175], a[92:95], a[52:55], v[172:175]\n"
      "v_mfma_f32_16x16x32_bf16 v[180:183], a[92:95], a[56:59], v[180:183]\n"
      "v_mfma_f32_16x16x32_bf16 v[188:191], a[92:95], a[60:63], v[188:191]\n"
      "s_nop 3\n"
      "v_fmamk_f32 v160, v128, 0xd0289660, v160\n"
      "v_fmamk_f32 v161, v129, 0xd0289660, v161\n"
      "v_fmamk_f32 v162, v130, 0xd0289660, v162\n"
      "v_fmamk_f32 v163, v131, 0xd0289660, v163\n"
      "v_fmamk_f32 v168, v136, 0xd0289660, v168\n"
      "v_fmamk_f32 v169, v137, 0xd0289660, v169\n"
      "v_fmamk_f32 v170, v138, 0xd0289660, v170\n"
      "v_fmamk_f32 v171, v139, 0xd0289660, v171\n"
      "v_fmamk_f32 v176, v128, 0xd0289660, v176\n"
      "v_fmamk_f32 v177, v129, 0xd0289660, v177\n"
      "v_fmamk_f32 v178, v130, 0xd0289660, v178\n"
      "v_fmamk_f32 v179, v131, 0xd0289660, v179\n"
      "v_fmamk_f32 v184, v136, 0xd0289660, v184\n"
      "v_fmamk_f32 v185, v137, 0xd0289660, v185\n"
      "v_fmamk_f32 v186, v138, 0xd0289660, v186\n"
      "v_fmamk_f32 v187, v139, 0xd0289660, v187\n"
      "v_fmamk_f32 v164, v132, 0xd0289660, v164\n"
      "v_fmamk_f32 v165, v133, 0xd0289660, v165\n"
      "v_fmamk_f32 v166, v134, 0xd0289660, v166\n"
      "v_fmamk_f32 v167, v135, 0xd0289660, v167\n"
      "v_fmamk_f32 v172, v140, 0xd0289660, v172\n"
      "v_fmamk_f32 v173, v141, 0xd0289660, v173\n"
      "v_fmamk_f32 v174, v142, 0xd0289660, v174\n"
      "v_fmamk_f32 v175, v143, 0xd0289660, v175\n"
      "v_fmamk_f32 v180, v132, 0xd0289660, v180\n"
      "v_fmamk_f32 v181, v133, 0xd0289660, v181\n"
      "v_fmamk_f32 v182, v134, 0xd0289660, v182\n"
      "v_fmamk_f32 v183, v135, 0xd0289660, v183\n"
      "v_fmamk_f32 v188, v140, 0xd0289660, v188\n"
      "v_fmamk_f32 v189, v141, 0xd0289660, v189\n"
      "v_fmamk_f32 v190, v142, 0xd0289660, v190\n"
      "v_fmamk_f32 v191, v143, 0xd0289660, v191\n"
      "v_max3_f32 v236, v160, v161, v162\n"
      "v_max3_f32 v237, v168, v169, v170\n"
      "v_max3_f32 v238, v176, v177, v178\n"
      "v_max3_f32 v239, v184, v185, v186\n"
      "v_max3_f32 v236, v236, v163, v164\n"
      "v_max3_f32 v237, v237, v171, v172\n"
      "v_max3_f32 v238, v238, v179, v180\n"
      "v_max3_f32 v239, v239, v187, v188\n"
      "v_max3_f32 v236, v236, v165, v166\n"
      "v_max3_f32 v237, v237, v173, v174\n"
      "v_max3_f32 v238, v238, v181, v182\n"
      "v_max3_f32 v239, v239, v189, v190\n"
      "v_max_f32 v236, v236, v167\n"
      "v_max_f32 v237, v237, v175\n"
      "v_max_f32 v238, v238, v183\n"
      "v_max_f32 v239, v239, v191\n"
      "v_mov_b32 v208, v236\n"
      "v_mov_b32 v209, v237\n"
      "v_mov_b32 v210, v238\n"
      "v_mov_b32 v211, v239\n"
      "v_permlane16_swap_b32 v236, v208\n"
      "v_permlane16_swap_b32 v237, v209\n"
      "v_permlane16_swap_b32 v238, v210\n"
      "v_permlane16_swap_b32 v239, v211\n"
      "v_max_f32 v236, v236, v208\n"
      "v_max_f32 v237, v237, v209\n"
      "v_max_f32 v238, v238, v210\n"
      "v_max_f32 v239, v239, v211\n"
      "v_mov_b32 v208, v236\n"
      "v_mov_b32 v209, v237\n"
      "v_mov_b32 v210, v238\n"
      "v_mov_b32 v211, v239\n"
      "v_permlane32_swap_b32 v236, v208\n"
      "v_permlane32_swap_b32 v237, v209\n"
      "v_permlane32_swap_b32 v238, v210\n"
      "v_permlane32_swap_b32 v239, v211\n"
      "v_max_f32 v236, v236, v208\n"
      "v_max_f32 v237, v237, v209\n"
      "v_max_f32 v238, v238, v210\n"
      "v_max_f32 v239, v239, v211\n"
      "v_mul_f32 v224, 0xbe0293ee, v236\n"
      "v_mul_f32 v225, 0xbe0293ee, v237\n"
      "v_mul_f32 v226, 0xbe0293ee, v238\n"
      "v_mul_f32 v227, 0xbe0293ee, v239\n"
      "v_fmamk_f32 v240, v160, 0x3e0293ee, v224\n"
      "v_exp_f32 v236, v240\n"
      "v_fmamk_f32 v241, v161, 0x3e0293ee, v224\n"
      "v_add_f32 v228, v228, v232\n"
      "v_exp_f32 v237, v241\n"
      "v_fmamk_f32 v240, v162, 0x3e0293ee, v224\n"
      "v_add_f32 v232, v236, v237\n"
      "v_cvt_pk_bf16_f32 v208, v236, v237\n"
      "v_exp_f32 v238, v240\n"
      "v_fmamk_f32 v241, v163, 0x3e0293ee, v224\n"
      "v_add_f32 v232, v232, v238\n"
      "v_exp_f32 v239, v241\n"
      "v_fmamk_f32 v240, v168, 0x3e0293ee, v225\n"
      "v_add_f32 v232, v232, v239\n"
      "v_cvt_pk_bf16_f32 v209, v238, v239\n"
      "v_exp_f32 v236, v240\n"
      "v_fmamk_f32 v241, v169, 0x3e0293ee, v225\n"
      "v_add_f32 v229, v229, v233\n"
      "v_exp_f32 v237, v241\n"
      "v_fmamk_f32 v240, v170, 0x3e0293ee, v225\n"
      "v_add_f32 v233, v236, v237\n"
      "v_cvt_pk_bf16_f32 v212, v236, v237\n"
      "v_exp_f32 v238, v240\n"
      "v_fmamk_f32 v241, v171, 0x3e0293ee, v225\n"
      "v_add_f32 v233, v233, v238\n"
      "v_exp_f32 v239, v241\n"
      "v_fmamk_f32 v240, v176, 0x3e0293ee, v226\n"
      "v_add_f32 v233, v233, v239\n"
      "v_cvt_pk_bf16_f32 v213, v238, v239\n"
      "NOTREE_%=:\n"
      "s_waitcnt vmcnt(14)\n"
      "v_readfirstlane_b32 %[e0], v216\n"
      "v_readfirstlane_b32 %[e1], v217\n"
      "s_memrealtime s[68:69]\n"
      "s_sub_u32 s63, %[n1], 1\n"
      "LOOP1_%=:\n"
      "s_waitcnt vmcnt(8) lgkmcnt(0)\n"
      "s_sub_u32 s67, s68, s90\n"
      "s_cmp_lt_i32 s67, 0\n"
      "s_cbranch_scc1 GW0_%=\n"
      "GX0_%=:\n"
      "s_barrier\n"
      "v_mfma_f32_16x16x32_bf16 v[64:67], a[112:115], v[192:195], v[64:67]\n"
      "s_memrealtime s[68:69]\n"
      "ds_read_b128 a[64:67], v242 offset:0\n"
      "v_exp_f32 v236, v240\n"
      "v_mfma_f32_16x16x32_bf16 v[68:71], a[112:115], v[196:199], v[68:71]\n"
      "s_add_u32 m0, s62, 0\n"
      "ds_read_b128 a[68:71], v242 offset:4096\n"
      "v_fmamk_f32 v241, v177, 0x3e0293ee, v226\n"
      "v_add_f32 v230, v230, v234\n"
      "v_mfma_f32_16x16x32_bf16 v[72:75], a[112:115], v[200:203], v[72:75]\n"
      "ds_read_b128 a[72:75], v243 offset:0\n"
      "buffer_load_dwordx4 v248, %[srdk], s60 offen nt lds\n"
      "v_exp_f32 v237, v241\n"
      "v_mfma_f32_16x16x32_bf16 v[76:79], a[112:115], v[204:207], v[76:79]\n"
      "s_add_u32 m0, s62, 896\n"
      "ds_read_b128 a[76:79], v243 offset:4096\n"
      "v_fmamk_f32 v240, v178, 0x3e0293ee, v226\n"
      "v_add_f32 v234, v236, v237\n"
      "v_cvt_pk_bf16_f32 v216, v236, v237\n"
      "v_mfma_f32_16x16x32_bf16 v[80:83], a[116:119], v[192:195], v[80:83]\n"
      "ds_read_b128 a[80:83], v242 offset:1024\n"
      "buffer_load_dwordx4 v248, %[srdk], s60 offen offset:128 nt lds\n"
      "v_exp_f32 v238, v240\n"
      "v_mfma_f32_16x16x32_bf16 v[84:87], a[116:119], v[196:199], v[84:87]\n"
      "s_add_u32 m0, s62, 8192\n"
      "ds_read_b128 a[84:87], v242 offset:5120\n"
      "v_fmamk_f32 v241, v179, 0x3e0293ee, v226\n"
      "v_add_f32 v234, v234, v238\n"
      "v_mfma_f32_16x16x32_bf16 v[88:91], a[116:119], v[200:203], v[88:91]\n"
      "ds_read_b128 a[88:91], v243 offset:1024\n"
      "buffer_load_dwordx4 v248, %[srdk], s61 offen nt lds\n"
      "v_exp_f32 v239, v241\n"
      "v_mfma_f32_16x16x32_bf16 v[92:95], a[116:119], v[204:207], v[92:95]\n"
      "s_add_u32 m0, s62, 9088\n"
      "ds_read_b128 a[92:95], v243 offset:5120\n"
      "v_fmamk_f32 v240, v184, 0x3e0293ee, v227\n"
      "v_add_f32 v234, v234, v239\n"
      "v_cvt_pk_bf16_f32 v217, v238, v239\n"
      "v_mfma_f32_16x16x32_bf16 v[96:99], a[120:123], v[192:195], v[96:99]\n"
      "buffer_load_dwordx4 v248, %[srdk], s61 offen offset:128 nt lds\n"
      "v_exp_f32 v236, v240\n"
      "v_mfma_f32_16x16x32_bf16 v[100:103], a[120:123], v[196:199], v[100:103]\n"
      "v_fmamk_f32 v241, v185, 0x3e0293ee, v227\n"
      "v_add_f32 v231, v231, v235\n"
      "v_mfma_f32_16x16x32_bf16 v[104:107], a[120:123], v[200:203], v[104:107]\n"
      "v_exp_f32 v237, v241\n"
      "v_mfma_f32_16x16x32_bf16 v[108:111], a[120:123], v[204:207], v[108:111]\n"
      "v_fmamk_f32 v240, v186, 0x3e0293ee, v227\n"
      "v_add_f32 v235, v236, v237\n"
      "v_cvt_pk_bf16_f32 v220, v236, v237\n"
      "v_mfma_f32_16x16x32_bf16 v[112:115], a[124:127], v[192:195], v[112:115]\n"
      "v_exp_f32 v238, v240\n"
      "v_mfma_f32_16x16x32_bf16 v[116:119], a[124:127], v[196:199], v[116:119]\n"
      "v_fmamk_f32 v241, v187, 0x3e0293ee, v227\n"
      "v_add_f32 v235, v235, v238\n"
      "v_mfma_f32_16x16x32_bf16 v[120:123], a[124:127], v[200:203], v[120:123]\n"
      "v_exp_f32 v239, v241\n"
      "v_mfma_f32_16x16x32_bf16 v[124:127], a[124:127], v[204:207], v[124:127]\n"
      "s_waitcnt lgkmcnt(0)\n"
      "v_fmamk_f32 v240, v164, 0x3e0293ee, v224\n"
      "v_add_f32 v235, v235, v239\n"
      "v_cvt_pk_bf16_f32 v221, v238, v239\n"
      "v_mfma_f32_16x16x32_bf16 v[128:131], a[64:67], a[0:3], 0\n"
      "s_sub_u32 s67, s68, s66\n"
      "v_exp_f32 v236, v240\n"
      "v_mfma_f32_16x16x32_bf16 v[136:139], a[64:67], a[4:7], 0\n"
      "s_cmp_gt_i32 s67, s70\n"
      "ds_read_b64_tr_b16 a[128:129], v244 offset:0\n"
      "v_fmamk_f32 v241, v165, 0x3e0293ee, v224\n"
      "v_add_f32 v232, v232, v236\n"
      "v_mfma_f32_16x16x32_bf16 v[144:147], a[64:67], a[8:11], 0\n"
      "s_cselect_b32 s69, s86, 0\n"
      "ds_read_b64_tr_b16 a[130:131], v244 offset:4096\n"
      "v_exp_f32 v237, v241\n"
      "v_mfma_f32_16x16x32_bf16 v[152:155], a[64:67], a[12:15], 0\n"
      "s_add_u32 s66, s66, s69\n"
      "ds_read_b64_tr_b16 a[132:133], v245 offset:0\n"
      "v_fmamk_f32 v240, v166, 0x3e0293ee, v224\n"
      "v_add_f32 v232, v232, v237\n"
      "v_cvt_pk_bf16_f32 v210, v236, v237\n"
      "v_mfma_f32_16x16x32_bf16 v[132:135], a[68:71], a[0:3], 0\n"
      "s_add_u32 s67, s67, 50\n"
      "ds_read_b64_tr_b16 a[134:135], v245 offset:4096\n"
      "v_exp_f32 v238, v240\n"
      "v_mfma_f32_16x16x32_bf16 v[140:143], a[68:71], a[4:7], 0\n"
      "s_min_i32 s70, s70, s67\n"
      "ds_read_b64_tr_b16 a[136:137], v246 offset:0\n"
      "v_fmamk_f32 v241, v167, 0x3e0293ee, v224\n"
      "v_add_f32 v232, v232, v238\n"
      "v_mfma_f32_16x16x32_bf16 v[148:151], a[68:71], a[8:11], 0\n"
      "s_max_i32 s70, s70, 85\n"
      "ds_read_b64_tr_b16 a[138:139], v246 offset:4096\n"
      "v_exp_f32 v239, v241\n"
      "v_mfma_f32_16x16x32_bf16 v[156:159], a[68:71], a[12:15], 0\n"
      "s_add_u32 s66, s66, s86\n"
      "ds_read_b64_tr_b16 a[140:141], v247 offset:0\n"
      "v_fmamk_f32 v240, v172, 0x3e0293ee, v225\n"
      "v_add_f32 v232, v232, v239\n"
      "v_cvt_pk_bf16_f32 v211, v238, v239\n"
      "v_mfma_f32_16x16x32_bf16 v[128:131], a[72:75], a[16:19], v[128:131]\n"
      "s_and_b32 s76, s80, 31\n"
      "ds_read_b64_tr_b16 a[142:143], v247 offset:4096\n"
      "v_exp_f32 v236, v240\n"
      "v_mfma_f32_16x16x32_bf16 v[136:139], a[72:75], a[20:23], v[136:139]\n"
      "s_lshr_b64 s[80:81], s[80:81], 5\n"
      "ds_read_b64_tr_b16 a[144:145], v244 offset:1024\n"
      "v_fmamk_f32 v241, v173, 0x3e0293ee, v225\n"
      "v_add_f32 v233, v233, v236\n"
      "v_mfma_f32_16x16x32_bf16 v[144:147], a[72:75], a[24:27], v[144:147]\n"
      "s_add_u32 s66, s66, s76\n"
      "ds_read_b64_tr_b16 a[146:147], v244 offset:5120\n"
      "v_exp_f32 v237, v241\n"
      "v_mfma_f32_16x16x32_bf16 v[152:155], a[72:75], a[28:31], v[152:155]\n"
      "s_sub_u32 s67, s68, s82\n"
      "ds_read_b64_tr_b16 a[148:149], v245 offset:1024\n"
      "v_fmamk_f32 v240, v174, 0x3e0293ee, v225\n"
      "v_add_f32 v233, v233, v237\n"
      "v_cvt_pk_bf16_f32 v214, v236, v237\n"
      "v_mfma_f32_16x16x32_bf16 v[132:135], a[76:79], a[16:19], v[132:135]\n"
      "s_min_i32 s76, s67, s83\n"
      "ds_read_b64_tr_b16 a[150:151], v245 offset:5120\n"
      "v_exp_f32 v238, v240\n"
      "v_mfma_f32_16x16x32_bf16 v[140:143], a[76:79], a[20:23], v[140:143]\n"
      "s_mov_b32 s83, s67\n"
      "ds_read_b64_tr_b16 a[152:153], v246 offset:1024\n"
      "v_fmamk_f32 v241, v175, 0x3e0293ee, v225\n"
      "v_add_f32 v233, v233, v238\n"
      "v_mfma_f32_16x16x32_bf16 v[148:151], a[76:79], a[24:27], v[148:151]\n"
      "s_sub_u32 s76, s76, 26\n"
      "ds_read_b64_tr_b16 a[154:155], v246 offset:5120\n"
      "v_exp_f32 v239, v241\n"
      "v_mfma_f32_16x16x32_bf16 v[156:159], a[76:79], a[28:31], v[156:159]\n"
      "s_min_i32 s76, s76, 148\n"
      "ds_read_b64_tr_b16 a[156:157], v247 offset:1024\n"
      "v_fmamk_f32 v240, v180, 0x3e0293ee, v226\n"
      "v_add_f32 v233, v233, v239\n"
      "v_cvt_pk_bf16_f32 v215, v238, v239\n"
      "v_mfma_f32_16x16x32_bf16 v[128:131], a[80:83], a[32:35], v[128:131]\n"
      "s_max_i32 s76, s76, 94\n"
      "ds_read_b64_tr_b16 a[158:159], v247 offset:5120\n"
      "v_exp_f32 v236, v240\n"
      "v_mfma_f32_16x16x32_bf16 v[136:139], a[80:83], a[36:39], v[136:139]\n"
      "s_mov_b32 s82, s68\n"
      "v_fmamk_f32 v241, v181, 0x3e0293ee, v226\n"
      "v_add_f32 v234, v234, v236\n"
      "v_mfma_f32_16x16x32_bf16 v[144:147], a[80:83], a[40:43], v[144:147]\n"
      "s_add_u32 s65, s82, s76\n"
      "v_exp_f32 v237, v241\n"
      "v_mfma_f32_16x16x32_bf16 v[152:155], a[80:83], a[44:47], v[152:155]\n"
      "s_sub_u32 s93, s93, 1\n"
      "v_fmamk_f32 v240, v182, 0x3e0293ee, v226\n"
      "v_add_f32 v234, v234, v237\n"
      "v_cvt_pk_bf16_f32 v218, v236, v237\n"
      "v_mfma_f32_16x16x32_bf16 v[132:135], a[84:87], a[32:35], v[132:135]\n"
      "s_cmp_lt_i32 s93, 10\n"
      "v_exp_f32 v238, v240\n"
      "v_mfma_f32_16x16x32_bf16 v[140:143], a[84:87], a[36:39], v[140:143]\n"
      "s_cselect_b32 s69, 3, 0\n"
      "v_fmamk_f32 v241, v183, 0x3e0293ee, v226\n"
      "v_add_f32 v234, v234, v238\n"
      "v_mfma_f32_16x16x32_bf16 v[148:151], a[84:87], a[40:43], v[148:151]\n"
      "s_sub_u32 s66, s66, s69\n"
      "v_exp_f32 v239, v241\n"
      "v_mfma_f32_16x16x32_bf16 v[156:159], a[84:87], a[44:47], v[156:159]\n"
      "s_sub_u32 s67, s65, s66\n"
      "v_fmamk_f32 v240, v188, 0x3e0293ee, v227\n"
      "v_add_f32 v234, v234, v239\n"
      "v_cvt_pk_bf16_f32 v219, v238, v239\n"
      "v_mfma_f32_16x16x32_bf16 v[128:131], a[88:91], a[48:51], v[128:131]\n"
      "s_cmp_lt_i32 s67, 0\n"
      "v_exp_f32 v236, v240\n"
      "v_mfma_f32_16x16x32_bf16 v[136:139], a[88:91], a[52:55], v[136:139]\n"
      "s_cselect_b32 s88, s66, s65\n"
      "v_fmamk_f32 v241, v189, 0x3e0293ee, v227\n"
      "v_add_f32 v235, v235, v236\n"
      "v_mfma_f32_16x16x32_bf16 v[144:147], a[88:91], a[56:59], v[144:147]\n"
      "s_add_u32 s90, s88, s91\n"
      "v_exp_f32 v237, v241\n"
      "v_mfma_f32_16x16x32_bf16 v[152:155], a[88:91], a[60:63], v[152:155]\n"
      "v_fmamk_f32 v240, v190, 0x3e0293ee, v227\n"
      "v_add_f32 v235, v235, v237\n"
      "v_cvt_pk_bf16_f32 v222, v236, v237\n"
      "v_mfma_f32_16x16x32_bf16 v[132:135], a[92:95], a[48:51], v[132:135]\n"
      "s_mov_b32 s64, 0\n"
      "v_exp_f32 v238, v240\n"
      "v_mfma_f32_16x16x32_bf16 v[140:143], a[92:95], a[52:55], v[140:143]\n"
      "v_fmamk_f32 v241, v191, 0x3e0293ee, v227\n"
      "v_add_f32 v235, v235, v238\n"
      "v_mfma_f32_16x16x32_bf16 v[148:151], a[92:95], a[56:59], v[148:151]\n"
      "v_exp_f32 v239, v241\n"
      "v_mfma_f32_16x16x32_bf16 v[156:159], a[92:95], a[60:63], v[156:159]\n"
      "s_waitcnt lgkmcnt(8)\n"
      "v_add_f32 v235, v235, v239\n"
      "v_cvt_pk_bf16_f32 v223, v238, v239\n"
      "v_max3_f32 v240, v232, v233, v234\n"
      "v_add_f32 v240, v240, v235\n"
      "v_cmp_nge_f32 vcc, 0x44800000, v240\n"
      "s_cbranch_vccnz FB1_%=\n"
      "RET1_0_%=:\n"
      "v_mfma_f32_16x16x32_bf16 v[0:3], a[128:131], v[208:211], v[0:3]\n"
      "s_add_u32 m0, s62, %[v0]\n"
      "v_fmamk_f32 v240, v128, 0x3e0293ee, v224\n"
      "v_exp_f32 v236, v240\n"
      "v_mfma_f32_16x16x32_bf16 v[4:7], a[128:131], v[212:215], v[4:7]\n"
      "buffer_load_dwordx4 v249, %[srdv], s77 offen nt lds\n"
      "v_fmamk_f32 v241, v129, 0x3e0293ee, v224\n"
      "v_add_f32 v228, v228, v232\n"
      "v_mfma_f32_16x16x32_bf16 v[8:11], a[128:131], v[216:219], v[8:11]\n"
      "s_add_u32 m0, s62, %[v1]\n"
      "v_exp_f32 v237, v241\n"
      "v_mfma_f32_16x16x32_bf16 v[12:15], a[128:131], v[220:223], v[12:15]\n"
      "buffer_load_dwordx4 v249, %[srdv], s77 offen offset:128 nt lds\n"
      "v_fmamk_f32 v240, v130, 0x3e0293ee, v224\n"
      "v_add_f32 v232, v236, v237\n"
      "v_cvt_pk_bf16_f32 v192, v236, v237\n"
      "v_mfma_f32_16x16x32_bf16 v[16:19], a[132:135], v[208:211], v[16:19]\n"
      "s_add_u32 m0, s62, %[v2]\n"
      "v_exp_f32 v238, v240\n"
      "v_mfma_f32_16x16x32_bf16 v[20:23], a[132:135], v[212:215], v[20:23]\n"
      "buffer_load_dwordx4 v249, %[srdv], s78 offen nt lds\n"
      "v_fmamk_f32 v241, v131, 0x3e0293ee, v224\n"
      "v_add_f32 v232, v232, v238\n"
      "v_mfma_f32_16x16x32_bf16 v[24:27], a[132:135], v[216:219], v[24:27]\n"
      "s_add_u32 m0, s62, %[v3]\n"
      "v_exp_f32 v239, v241\n"
      "v_mfma_f32_16x16x32_bf16 v[28:31], a[132:135], v[220:223], v[28:31]\n"
      "s_mov_b32 s77, s60\n"
      "buffer_load_dwordx4 v249, %[srdv], s78 offen offset:128 nt lds\n"
      "v_fmamk_f32 v240, v136, 0x3e0293ee, v225\n"
      "v_add_f32 v232, v232, v239\n"
      "v_cvt_pk_bf16_f32 v193, v238, v239\n"
      "v_mfma_f32_16x16x32_bf16 v[32:35], a[136:139], v[208:211], v[32:35]\n"
      "s_mov_b32 s78, s61\n"
      "v_exp_f32 v236, v240\n"
      "v_mfma_f32_16x16x32_bf16 v[36:39], a[136:139], v[212:215], v[36:39]\n"
      "s_add_u32 s60, s60, s79\n"
      "v_fmamk_f32 v241, v137, 0x3e0293ee, v225\n"
      "v_add_f32 v229, v229, v233\n"
      "v_mfma_f32_16x16x32_bf16 v[40:43], a[136:139], v[216:219], v[40:43]\n"
      "s_add_u32 s61, s61, s79\n"
      "v_exp_f32 v237, v241\n"
      "v_mfma_f32_16x16x32_bf16 v[44:47], a[136:139], v[220:223], v[44:47]\n"
      "s_sub_u32 s79, 0x10000, s79\n"
      "v_fmamk_f32 v240, v138, 0x3e0293ee, v225\n"
      "v_add_f32 v233, v236, v237\n"
      "v_cvt_pk_bf16_f32 v196, v236, v237\n"
      "v_mfma_f32_16x16x32_bf16 v[48:51], a[140:143], v[208:211], v[48:51]\n"
      "s_memrealtime s[68:69]\n"
      "v_exp_f32 v238, v240\n"
      "v_mfma_f32_16x16x32_bf16 v[52:55], a[140:143], v[212:215], v[52:55]\n"
      "s_add_u32 s62, s62, 0x8000\n"
      "v_fmamk_f32 v241, v139, 0x3e0293ee, v225\n"
      "v_add_f32 v233, v233, v238\n"
      "v_mfma_f32_16x16x32_bf16 v[56:59], a[140:143], v[216:219], v[56:59]\n"
      "s_and_b32 s62, s62, 0x1ffff\n"
      "v_exp_f32 v239, v241\n"
      "v_mfma_f32_16x16x32_bf16 v[60:63], a[140:143], v[220:223], v[60:63]\n"
      "v_fmamk_f32 v240, v144, 0x3e0293ee, v226\n"
      "v_add_f32 v233, v233, v239\n"
      "v_cvt_pk_bf16_f32 v197, v238, v239\n"
      "s_waitcnt vmcnt(8) lgkmcnt(0)\n"
      "s_sub_u32 s67, s68, s90\n"
      "s_cmp_lt_i32 s67, 0\n"
      "s_cbranch_scc1 GW1_%=\n"
      "GX1_%=:\n"
      "s_barrier\n"
      "v_mfma_f32_16x16x32_bf16 v[64:67], a[144:147], v[208:211], v[64:67]\n"
      "s_memrealtime s[68:69]\n"
      "ds_read_b128 a[64:67], v242 offset:32768\n"
      "v_exp_f32 v236, v240\n"
      "v_mfma_f32_16x16x32_bf16 v[68:71], a[144:147], v[212:215], v[68:71]\n"
      "s_add_u32 m0, s62, 0\n"
      "ds_read_b128 a[68:71], v242 offset:36864\n"
      "v_fmamk_f32 v241, v145, 0x3e0293ee, v226\n"
      "v_add_f32 v230, v230, v234\n"
      "v_mfma_f32_16x16x32_bf16 v[72:75], a[144:147], v[216:219], v[72:75]\n"
      "ds_read_b128 a[72:75], v243 offset:32768\n"
      "buffer_load_dwordx4 v248, %[srdk], s60 offen nt lds\n"
      "v_exp_f32 v237, v241\n"
      "v_mfma_f32_16x16x32_bf16 v[76:79], a[144:147], v[220:223], v[76:79]\n"
      "s_add_u32 m0, s62, 896\n"
      "ds_read_b128 a[76:79], v243 offset:36864\n"
      "v_fmamk_f32 v240, v146, 0x3e0293ee, v226\n"
      "v_add_f32 v234, v236, v237\n"
      "v_cvt_pk_bf16_f32 v200, v236, v237\n"
      "v_mfma_f32_16x16x32_bf16 v[80:83], a[148:151], v[208:211], v[80:83]\n"
      "ds_read_b128 a[80:83], v242 offset:33792\n"
      "buffer_load_dwordx4 v248, %[srdk], s60 offen offset:128 nt lds\n"
      "v_exp_f32 v238, v240\n"
      "v_mfma_f32_16x16x32_bf16 v[84:87], a[148:151], v[212:215], v[84:87]\n"
      "s_add_u32 m0, s62, 8192\n"
      "ds_read_b128 a[84:87], v242 offset:37888\n"
      "v_fmamk_f32 v241, v147, 0x3e0293ee, v226\n"
      "v_add_f32 v234, v234, v238\n"
      "v_mfma_f32_16x16x32_bf16 v[88:91], a[148:151], v[216:219], v[88:91]\n"
      "ds_read_b128 a[88:91], v243 offset:33792\n"
      "buffer_load_dwordx4 v248, %[srdk], s61 offen nt lds\n"
      "v_exp_f32 v239, v241\n"
      "v_mfma_f32_16x16x32_bf16 v[92:95], a[148:151], v[220:223], v[92:95]\n"
      "s_add_u32 m0, s62, 9088\n"
      "ds_read_b128 a[92:95], v243 offset:37888\n"
      "v_fmamk_f32 v240, v152, 0x3e0293ee, v227\n"
      "v_add_f32 v234, v234, v239\n"
      "v_cvt_pk_bf16_f32 v201, v238, v239\n"
      "v_mfma_f32_16x16x32_bf16 v[96:99], a[152:155], v[208:211], v[96:99]\n"
      "buffer_load_dwordx4 v248, %[srdk], s61 offen offset:128 nt lds\n"
      "v_exp_f32 v236, v240\n"
      "v_mfma_f32_16x16x32_bf16 v[100:103], a[152:155], v[212:215], v[100:103]\n"
      "v_fmamk_f32 v241, v153, 0x3e0293ee, v227\n"
      "v_add_f32 v231, v231, v235\n"
      "v_mfma_f32_16x16x32_bf16 v[104:107], a[152:155], v[216:219], v[104:107]\n"
      "v_exp_f32 v237, v241\n"
      "v_mfma_f32_16x16x32_bf16 v[108:111], a[152:155], v[220:223], v[108:111]\n"
      "v_fmamk_f32 v240, v154, 0x3e0293ee, v227\n"
      "v_add_f32 v235, v236, v237\n"
      "v_cvt_pk_bf16_f32 v204, v236, v237\n"
      "v_mfma_f32_16x16x32_bf16 v[112:115], a[156:159], v[208:211], v[112:115]\n"
      "v_exp_f32 v238, v240\n"
      "v_mfma_f32_16x16x32_bf16 v[116:119], a[156:159], v[212:215], v[116:119]\n"
      "v_fmamk_f32 v241, v155, 0x3e0293ee, v227\n"
      "v_add_f32 v235, v235, v238\n"
      "v_mfma_f32_16x16x32_bf16 v[120:123], a[156:159], v[216:219], v[120:123]\n"
      "v_exp_f32 v239, v241\n"
      "v_mfma_f32_16x16x32_bf16 v[124:127], a[156:159], v[220:223], v[124:127]\n"
      "s_waitcnt lgkmcnt(0)\n"
      "v_fmamk_f32 v240, v132, 0x3e0293ee, v224\n"
      "v_add_f32 v235, v235, v239\n"
      "v_cvt_pk_bf16_f32 v205, v238, v239\n"
      "v_mfma_f32_16x16x32_bf16 v[160:163], a[64:67], a[0:3], 0\n"
      "s_sub_u32 s67, s68, s66\n"
      "v_exp_f32 v236, v240\n"
      "v_mfma_f32_16x16x32_bf16 v[168:171], a[64:67], a[4:7], 0\n"
      "s_cmp_gt_i32 s67, s70\n"
      "ds_read_b64_tr_b16 a[96:97], v244 offset:32768\n"
      "v_fmamk_f32 v241, v133, 0x3e0293ee, v224\n"
      "v_add_f32 v232, v232, v236\n"
      "v_mfma_f32_16x16x32_bf16 v[176:179], a[64:67], a[8:11], 0\n"
      "s_cselect_b32 s69, s86, 0\n"
      "ds_read_b64_tr_b16 a[98:99], v244 offset:36864\n"
      "v_exp_f32 v237, v241\n"
      "v_mfma_f32_16x16x32_bf16 v[184:187], a[64:67], a[12:15], 0\n"
      "s_add_u32 s66, s66, s69\n"
      "ds_read_b64_tr_b16 a[100:101], v245 offset:32768\n"
      "v_fmamk_f32 v240, v134, 0x3e0293ee, v224\n"
      "v_add_f32 v232, v232, v237\n"
      "v_cvt_pk_bf16_f32 v194, v236, v237\n"
      "v_mfma_f32_16x16x32_bf16 v[164:167], a[68:71], a[0:3], 0\n"
      "s_add_u32 s67, s67, 50\n"
      "ds_read_b64_tr_b16 a[102:103], v245 offset:36864\n"
      "v_exp_f32 v238, v240\n"
      "v_mfma_f32_16x16x32_bf16 v[172:175], a[68:71], a[4:7], 0\n"
      "s_min_i32 s70, s70, s67\n"
      "ds_read_b64_tr_b16 a[104:105], v246 offset:32768\n"
      "v_fmamk_f32 v241, v135, 0x3e0293ee, v224\n"
      "v_add_f32 v232, v232, v238\n"
      "v_mfma_f32_16x16x32_bf16 v[180:183], a[68:71], a[8:11], 0\n"
      "s_max_i32 s70, s70, 85\n"
      "ds_read_b64_tr_b16 a[106:107], v246 offset:36864\n"
      "v_exp_f32 v239, v241\n"
      "v_mfma_f32_16x16x32_bf16 v[188:191], a[68:71], a[12:15], 0\n"
      "s_add_u32 s66, s66, s86\n"
      "ds_read_b64_tr_b16 a[108:109], v247 offset:32768\n"
      "v_fmamk_f32 v240, v140, 0x3e0293ee, v225\n"
      "v_add_f32 v232, v232, v239\n"
      "v_cvt_pk_bf16_f32 v195, v238, v239\n"
      "v_mfma_f32_16x16x32_bf16 v[160:163], a[72:75], a[16:19], v[160:163]\n"
      "s_and_b32 s76, s80, 31\n"
      "ds_read_b64_tr_b16 a[110:111], v247 offset:36864\n"
      "v_exp_f32 v236, v240\n"
      "v_mfma_f32_16x16x32_bf16 v[168:171], a[72:75], a[20:23], v[168:171]\n"
      "s_lshr_b64 s[80:81], s[80:81], 5\n"
      "ds_read_b64_tr_b16 a[112:113], v244 offset:33792\n"
      "v_fmamk_f32 v241, v141, 0x3e0293ee, v225\n"
      "v_add_f32 v233, v233, v236\n"
      "v_mfma_f32_16x16x32_bf16 v[176:179], a[72:75], a[24:27], v[176:179]\n"
      "s_add_u32 s66, s66, s76\n"
      "ds_read_b64_tr_b16 a[114:115], v244 offset:37888\n"
      "v_exp_f32 v237, v241\n"
      "v_mfma_f32_16x16x32_bf16 v[184:187], a[72:75], a[28:31], v[184:187]\n"
      "s_sub_u32 s67, s68, s82\n"
      "ds_read_b64_tr_b16 a[116:117], v245 offset:33792\n"
      "v_fmamk_f32 v240, v142, 0x3e0293ee, v225\n"
      "v_add_f32 v233, v233, v237\n"
      "v_cvt_pk_bf16_f32 v198, v236, v237\n"
      "v_mfma_f32_16x16x32_bf16 v[164:167], a[76:79], a[16:19], v[164:167]\n"
      "s_min_i32 s76, s67, s83\n"
      "ds_read_b64_tr_b16 a[118:119], v245 offset:37888\n"
      "v_exp_f32 v238, v240\n"
      "v_mfma_f32_16x16x32_bf16 v[172:175], a[76:79], a[20:23], v[172:175]\n"
      "s_mov_b32 s83, s67\n"
      "ds_read_b64_tr_b16 a[120:121], v246 offset:33792\n"
      "v_fmamk_f32 v241, v143, 0x3e0293ee, v225\n"
      "v_add_f32 v233, v233, v238\n"
      "v_mfma_f32_16x16x32_bf16 v[180:183], a[76:79], a[24:27], v[180:183]\n"
      "s_sub_u32 s76, s76, 26\n"
      "ds_read_b64_tr_b16 a[122:123], v246 offset:37888\n"
      "v_exp_f32 v239, v241\n"
      "v_mfma_f32_16x16x32_bf16 v[188:191], a[76:79], a[28:31], v[188:191]\n"
      "s_min_i32 s76, s76, 148\n"
      "ds_read_b64_tr_b16 a[124:125], v247 offset:33792\n"
      "v_fmamk_f32 v240, v148, 0x3e0293ee, v226\n"
      "v_add_f32 v233, v233, v239\n"
      "v_cvt_pk_bf16_f32 v199, v238, v239\n"
      "v_mfma_f32_16x16x32_bf16 v[160:163], a[80:83], a[32:35], v[160:163]\n"
      "s_max_i32 s76, s76, 94\n"
      "ds_read_b64_tr_b16 a[126:127], v247 offset:37888\n"
      "v_exp_f32 v236, v240\n"
      "v_mfma_f32_16x16x32_bf16 v[168:171], a[80:83], a[36:39], v[168:171]\n"
      "s_mov_b32 s82, s68\n"
      "v_fmamk_f32 v241, v149, 0x3e0293ee, v226\n"
      "v_add_f32 v234, v234, v236\n"
      "v_mfma_f32_16x16x32_bf16 v[176:179], a[80:83], a[40:43], v[176:179]\n"
      "s_add_u32 s65, s82, s76\n"
      "v_exp_f32 v237, v241\n"
      "v_mfma_f32_16x16x32_bf16 v[184:187], a[80:83], a[44:47], v[184:187]\n"
      "s_sub_u32 s93, s93, 1\n"
      "v_fmamk_f32 v240, v150, 0x3e0293ee, v226\n"
      "v_add_f32 v234, v234, v237\n"
      "v_cvt_pk_bf16_f32 v202, v236, v237\n"
      "v_mfma_f32_16x16x32_bf16 v[164:167], a[84:87], a[32:35], v[164:167]\n"
      "s_cmp_lt_i32 s93, 10\n"
      "v_exp_f32 v238, v240\n"
      "v_mfma_f32_16x16x32_bf16 v[172:175], a[84:87], a[36:39], v[172:175]\n"
      "s_cselect_b32 s69, 3, 0\n"
      "v_fmamk_f32 v241, v151, 0x3e0293ee, v226\n"
      "v_add_f32 v234, v234, v238\n"
      "v_mfma_f32_16x16x32_bf16 v[180:183], a[84:87], a[40:43], v[180:183]\n"
      "s_sub_u32 s66, s66, s69\n"
      "v_exp_f32 v239, v241\n"
      "v_mfma_f32_16x16x32_bf16 v[188:191], a[84:87], a[44:47], v[188:191]\n"
      "s_sub_u32 s67, s65, s66\n"
      "v_fmamk_f32 v240, v156, 0x3e0293ee, v227\n"
      "v_add_f32 v234, v234, v239\n"
      "v_cvt_pk_bf16_f32 v203, v238, v239\n"
      "v_mfma_f32_16x16x32_bf16 v[160:163], a[88:91], a[48:51], v[160:163]\n"
      "s_cmp_lt_i32 s67, 0\n"
      "v_exp_f32 v236, v240\n"
      "v_mfma_f32_16x16x32_bf16 v[168:171], a[88:91], a[52:55], v[168:171]\n"
      "s_cselect_b32 s88, s66, s65\n"
      "v_fmamk_f32 v241, v157, 0x3e0293ee, v227\n"
      "v_add_f32 v235, v235, v236\n"
      "v_mfma_f32_16x16x32_bf16 v[176:179], a[88:91], a[56:59], v[176:179]\n"
      "s_add_u32 s90, s88, s91\n"
      "v_exp_f32 v237, v241\n"
      "v_mfma_f32_16x16x32_bf16 v[184:187], a[88:91], a[60:63], v[184:187]\n"
      "v_fmamk_f32 v240, v158, 0x3e0293ee, v227\n"
      "v_add_f32 v235, v235, v237\n"
      "v_cvt_pk_bf16_f32 v206, v236, v237\n"
      "v_mfma_f32_16x16x32_bf16 v[164:167], a[92:95], a[48:51], v[164:167]\n"
      "s_mov_b32 s64, 0\n"
      "v_exp_f32 v238, v240\n"
      "v_mfma_f32_16x16x32_bf16 v[172:175], a[92:95], a[52:55], v[172:175]\n"
      "v_fmamk_f32 v241, v159, 0x3e0293ee, v227\n"
      "v_add_f32 v235, v235, v238\n"
      "v_mfma_f32_16x16x32_bf16 v[180:183], a[92:95], a[56:59], v[180:183]\n"
      "v_exp_f32 v239, v241\n"
      "v_mfma_f32_16x16x32_bf16 v[188:191], a[92:95], a[60:63], v[188:191]\n"
      "s_waitcnt lgkmcnt(8)\n"
      "v_add_f32 v235, v235, v239\n"
      "v_cvt_pk_bf16_f32 v207, v238, v239\n"
      "v_max3_f32 v240, v232, v233, v234\n"
      "v_add_f32 v240, v240, v235\n"
      "v_cmp_nge_f32 vcc, 0x44800000, v240\n"
      "s_cbranch_vccnz FB0_%=\n"
      "RET0_0_%=:\n"
      "v_mfma_f32_16x16x32_bf16 v[0:3], a[96:99], v[192:195], v[0:3]\n"
      "s_add_u32 m0, s62, %[v0]\n"
      "v_fmamk_f32 v240, v160, 0x3e0293ee, v224\n"
      "v_exp_f32 v236, v240\n"
      "v_mfma_f32_16x16x32_bf16 v[4:7], a[96:99], v[196:199], v[4:7]\n"
      "buffer_load_dwordx4 v249, %[srdv], s77 offen nt lds\n"
      "v_fmamk_f32 v241, v161, 0x3e0293ee, v224\n"
      "v_add_f32 v228, v228, v232\n"
      "v_mfma_f32_16x16x32_bf16 v[8:11], a[96:99], v[200:203], v[8:11]\n"
      "s_add_u32 m0, s62, %[v1]\n"
      "v_exp_f32 v237, v241\n"
      "v_xor_b32 v242, 0x10000, v242\n"
      "v_mfma_f32_16x16x32_bf16 v[12:15], a[96:99], v[204:207], v[12:15]\n"
      "buffer_load_dwordx4 v249, %[srdv], s77 offen offset:128 nt lds\n"
      "v_fmamk_f32 v240, v162, 0x3e0293ee, v224\n"
      "v_add_f32 v232, v236, v237\n"
      "v_cvt_pk_bf16_f32 v208, v236, v237\n"
      "v_mfma_f32_16x16x32_bf16 v[16:19], a[100:103], v[192:195], v[16:19]\n"
      "s_add_u32 m0, s62, %[v2]\n"
      "v_exp_f32 v238, v240\n"
      "v_xor_b32 v243, 0x10000, v243\n"
      "v_mfma_f32_16x16x32_bf16 v[20:23], a[100:103], v[196:199], v[20:23]\n"
      "buffer_load_dwordx4 v249, %[srdv], s78 offen nt lds\n"
      "v_fmamk_f32 v241, v163, 0x3e0293ee, v224\n"
      "v_add_f32 v232, v232, v238\n"
      "v_mfma_f32_16x16x32_bf16 v[24:27], a[100:103], v[200:203], v[24:27]\n"
      "s_add_u32 m0, s62, %[v3]\n"
      "v_exp_f32 v239, v241\n"
      "v_xor_b32 v244, 0x10000, v244\n"
      "v_mfma_f32_16x16x32_bf16 v[28:31], a[100:103], v[204:207], v[28:31]\n"
      "s_mov_b32 s77, s60\n"
      "buffer_load_dwordx4 v249, %[srdv], s78 offen offset:128 nt lds\n"
      "v_fmamk_f32 v240, v168, 0x3e0293ee, v225\n"
      "v_add_f32 v232, v232, v239\n"
      "v_cvt_pk_bf16_f32 v209, v238, v239\n"
      "v_mfma_f32_16x16x32_bf16 v[32:35], a[104:107], v[192:195], v[32:35]\n"
      "s_mov_b32 s78, s61\n"
      "v_exp_f32 v236, v240\n"
      "v_xor_b32 v245, 0x10000, v245\n"
      "v_mfma_f32_16x16x32_bf16 v[36:39], a[104:107], v[196:199], v[36:39]\n"
      "s_add_u32 s60, s60, s79\n"
      "v_fmamk_f32 v241, v169, 0x3e0293ee, v225\n"
      "v_add_f32 v229, v229, v233\n"
      "v_mfma_f32_16x16x32_bf16 v[40:43], a[104:107], v[200:203], v[40:43]\n"
      "s_add_u32 s61, s61, s79\n"
      "v_exp_f32 v237, v241\n"
      "v_xor_b32 v246, 0x10000, v246\n"
      "v_mfma_f32_16x16x32_bf16 v[44:47], a[104:107], v[204:207], v[44:47]\n"
      "s_sub_u32 s79, 0x10000, s79\n"
      "v_fmamk_f32 v240, v170, 0x3e0293ee, v225\n"
      "v_add_f32 v233, v236, v237\n"
      "v_cvt_pk_bf16_f32 v212, v236, v237\n"
      "v_mfma_f32_16x16x32_bf16 v[48:51], a[108:111], v[192:195], v[48:51]\n"
      "s_memrealtime s[68:69]\n"
      "v_exp_f32 v238, v240\n"
      "v_xor_b32 v247, 0x10000, v247\n"
      "v_mfma_f32_16x16x32_bf16 v[52:55], a[108:111], v[196:199], v[52:55]\n"
      "s_add_u32 s62, s62, 0x8000\n"
      "v_fmamk_f32 v241, v171, 0x3e0293ee, v225\n"
      "v_add_f32 v233, v233, v238\n"
      "v_mfma_f32_16x16x32_bf16 v[56:59], a[108:111], v[200:203], v[56:59]\n"
      "s_and_b32 s62, s62, 0x1ffff\n"
      "v_exp_f32 v239, v241\n"
      "v_mfma_f32_16x16x32_bf16 v[60:63], a[108:111], v[204:207], v[60:63]\n"
      "s_sub_u32 s63, s63, 1\n"
      "v_fmamk_f32 v240, v176, 0x3e0293ee, v226\n"
      "v_add_f32 v233, v233, v239\n"
      "v_cvt_pk_bf16_f32 v213, v238, v239\n"
      "s_cbranch_scc0 LOOP1_%=\n"
      "s_waitcnt vmcnt(8) lgkmcnt(0)\n"
      "s_sub_u32 s67, s68, s90\n"
      "s_cmp_lt_i32 s67, 0\n"
      "s_cbranch_scc1 GW2_%=\n"
      "GX2_%=:\n"
      "s_barrier\n"
      "v_mfma_f32_16x16x32_bf16 v[64:67], a[112:115], v[192:195], v[64:67]\n"
      "s_memrealtime s[68:69]\n"
      "ds_read_b128 a[64:67], v242 offset:0\n"
      "v_exp_f32 v236, v240\n"
      "v_mfma_f32_16x16x32_bf16 v[68:71], a[112:115], v[196:199], v[68:71]\n"
      "s_add_u32 m0, s62, 0\n"
      "ds_read_b128 a[68:71], v242 offset:4096\n"
      "v_fmamk_f32 v241, v177, 0x3e0293ee, v226\n"
      "v_add_f32 v230, v230, v234\n"
      "v_mfma_f32_16x16x32_bf16 v[72:75], a[112:115], v[200:203], v[72:75]\n"
      "ds_read_b128 a[72:75], v243 offset:0\n"
      "buffer_load_dwordx4 v248, %[srdk], s60 offen nt lds\n"
      "v_exp_f32 v237, v241\n"
      "v_mfma_f32_16x16x32_bf16 v[76:79], a[112:115], v[204:207], v[76:79]\n"
      "s_add_u32 m0, s62, 896\n"
      "ds_read_b128 a[76:79], v243 offset:4096\n"
      "v_fmamk_f32 v240, v178, 0x3e0293ee, v226\n"
      "v_add_f32 v234, v236, v237\n"
      "v_cvt_pk_bf16_f32 v216, v236, v237\n"
      "v_mfma_f32_16x16x32_bf16 v[80:83], a[116:119], v[192:195], v[80:83]\n"
      "ds_read_b128 a[80:83], v242 offset:1024\n"
      "buffer_load_dwordx4 v248, %[srdk], s60 offen offset:128 nt lds\n"
      "v_exp_f32 v238, v240\n"
      "v_mfma_f32_16x16x32_bf16 v[84:87], a[116:119], v[196:199], v[84:87]\n"
      "s_add_u32 m0, s62, 8192\n"
      "ds_read_b128 a[84:87], v242 offset:5120\n"
      "v_fmamk_f32 v241, v179, 0x3e0293ee, v226\n"
      "v_add_f32 v234, v234, v238\n"
      "v_mfma_f32_16x16x32_bf16 v[88:91], a[116:119], v[200:203], v[88:91]\n"
      "ds_read_b128 a[88:91], v243 offset:1024\n"
      "buffer_load_dwordx4 v248, %[srdk], s61 offen nt lds\n"
      "v_exp_f32 v239, v241\n"
      "v_mfma_f32_16x16x32_bf16 v[92:95], a[116:119], v[204:207], v[92:95]\n"
      "s_add_u32 m0, s62, 9088\n"
      "ds_read_b128 a[92:95], v243 offset:5120\n"
      "v_fmamk_f32 v240, v184, 0x3e0293ee, v227\n"
      "v_add_f32 v234, v234, v239\n"
      "v_cvt_pk_bf16_f32 v217, v238, v239\n"
      "v_mfma_f32_16x16x32_bf16 v[96:99], a[120:123], v[192:195], v[96:99]\n"
      "buffer_load_dwordx4 v248, %[srdk], s61 offen offset:128 nt lds\n"
      "v_exp_f32 v236, v240\n"
      "v_mfma_f32_16x16x32_bf16 v[100:103], a[120:123], v[196:199], v[100:103]\n"
      "v_fmamk_f32 v241, v185, 0x3e0293ee, v227\n"
      "v_add_f32 v231, v231, v235\n"
      "v_mfma_f32_16x16x32_bf16 v[104:107], a[120:123], v[200:203], v[104:107]\n"
      "v_exp_f32 v237, v241\n"
      "v_mfma_f32_16x16x32_bf16 v[108:111], a[120:123], v[204:207], v[108:111]\n"
      "v_fmamk_f32 v240, v186, 0x3e0293ee, v227\n"
      "v_add_f32 v235, v236, v237\n"
      "v_cvt_pk_bf16_f32 v220, v236, v237\n"
      "v_mfma_f32_16x16x32_bf16 v[112:115], a[124:127], v[192:195], v[112:115]\n"
      "v_exp_f32 v238, v240\n"
      "v_mfma_f32_16x16x32_bf16 v[116:119], a[124:127], v[196:199], v[116:119]\n"
      "v_fmamk_f32 v241, v187, 0x3e0293ee, v227\n"
      "v_add_f32 v235, v235, v238\n"
      "v_mfma_f32_16x16x32_bf16 v[120:123], a[124:127], v[200:203], v[120:123]\n"
      "v_exp_f32 v239, v241\n"
      "v_mfma_f32_16x16x32_bf16 v[124:127], a[124:127], v[204:207], v[124:127]\n"
      "s_waitcnt lgkmcnt(0)\n"
      "v_fmamk_f32 v240, v164, 0x3e0293ee, v224\n"
      "v_add_f32 v235, v235, v239\n"
      "v_cvt_pk_bf16_f32 v221, v238, v239\n"
      "v_mfma_f32_16x16x32_bf16 v[128:131], a[64:67], a[0:3], 0\n"
      "s_sub_u32 s67, s68, s66\n"
      "v_exp_f32 v236, v240\n"
      "v_mfma_f32_16x16x32_bf16 v[136:139], a[64:67], a[4:7], 0\n"
      "s_cmp_gt_i32 s67, s70\n"
      "ds_read_b64_tr_b16 a[128:129], v244 offset:0\n"
      "v_fmamk_f32 v241, v165, 0x3e0293ee, v224\n"
      "v_add_f32 v232, v232, v236\n"
      "v_mfma_f32_16x16x32_bf16 v[144:147], a[64:67], a[8:11], 0\n"
      "s_cselect_b32 s69, s86, 0\n"
      "ds_read_b64_tr_b16 a[130:131], v244 offset:4096\n"
      "v_exp_f32 v237, v241\n"
      "v_mfma_f32_16x16x32_bf16 v[152:155], a[64:67], a[12:15], 0\n"
      "s_add_u32 s66, s66, s69\n"
      "ds_read_b64_tr_b16 a[132:133], v245 offset:0\n"
      "v_fmamk_f32 v240, v166, 0x3e0293ee, v224\n"
      "v_add_f32 v232, v232, v237\n"
      "v_cvt_pk_bf16_f32 v210, v236, v237\n"
      "v_mfma_f32_16x16x32_bf16 v[132:135], a[68:71], a[0:3], 0\n"
      "s_add_u32 s67, s67, 50\n"
      "ds_read_b64_tr_b16 a[134:135], v245 offset:4096\n"
      "v_exp_f32 v238, v240\n"
      "v_mfma_f32_16x16x32_bf16 v[140:143], a[68:71], a[4:7], 0\n"
      "s_min_i32 s70, s70, s67\n"
      "ds_read_b64_tr_b16 a[136:137], v246 offset:0\n"
      "v_fmamk_f32 v241, v167, 0x3e0293ee, v224\n"
      "v_add_f32 v232, v232, v238\n"
      "v_mfma_f32_16x16x32_bf16 v[148:151], a[68:71], a[8:11], 0\n"
      "s_max_i32 s70, s70, 85\n"
      "ds_read_b64_tr_b16 a[138:139], v246 offset:4096\n"
      "v_exp_f32 v239, v241\n"
      "v_mfma_f32_16x16x32_bf16 v[156:159], a[68:71], a[12:15], 0\n"
      "s_add_u32 s66, s66, s86\n"
      "ds_read_b64_tr_b16 a[140:141], v247 offset:0\n"
      "v_fmamk_f32 v240, v172, 0x3e0293ee, v225\n"
      "v_add_f32 v232, v232, v239\n"
      "v_cvt_pk_bf16_f32 v211, v238, v239\n"
      "v_mfma_f32_16x16x32_bf16 v[128:131], a[72:75], a[16:19], v[128:131]\n"
      "s_and_b32 s76, s80, 31\n"
      "ds_read_b64_tr_b16 a[142:143], v247 offset:4096\n"
      "v_exp_f32 v236, v240\n"
      "v_mfma_f32_16x16x32_bf16 v[136:139], a[72:75], a[20:23], v[136:139]\n"
      "s_lshr_b64 s[80:81], s[80:81], 5\n"
      "ds_read_b64_tr_b16 a[144:145], v244 offset:1024\n"
      "v_fmamk_f32 v241, v173, 0x3e0293ee, v225\n"
      "v_add_f32 v233, v233, v236\n"
      "v_mfma_f32_16x16x32_bf16 v[144:147], a[72:75], a[24:27], v[144:147]\n"
      "s_add_u32 s66, s66, s76\n"
      "ds_read_b64_tr_b16 a[146:147], v244 offset:5120\n"
      "v_exp_f32 v237, v241\n"
      "v_mfma_f32_16x16x32_bf16 v[152:155], a[72:75], a[28:31], v[152:155]\n"
      "s_sub_u32 s67, s68, s82\n"
      "ds_read_b64_tr_b16 a[148:149], v245 offset:1024\n"
      "v_fmamk_f32 v240, v174, 0x3e0293ee, v225\n"
      "v_add_f32 v233, v233, v237\n"
      "v_cvt_pk_bf16_f32 v214, v236, v237\n"
      "v_mfma_f32_16x16x32_bf16 v[132:135], a[76:79], a[16:19], v[132:135]\n"
      "s_min_i32 s76, s67, s83\n"
      "ds_read_b64_tr_b16 a[150:151], v245 offset:5120\n"
      "v_exp_f32 v238, v240\n"
      "v_mfma_f32_16x16x32_bf16 v[140:143], a[76:79], a[20:23], v[140:143]\n"
      "s_mov_b32 s83, s67\n"
      "ds_read_b64_tr_b16 a[152:153], v246 offset:1024\n"
      "v_fmamk_f32 v241, v175, 0x3e0293ee, v225\n"
      "v_add_f32 v233, v233, v238\n"
      "v_mfma_f32_16x16x32_bf16 v[148:151], a[76:79], a[24:27], v[148:151]\n"
      "s_sub_u32 s76, s76, 26\n"
      "ds_read_b64_tr_b16 a[154:155], v246 offset:5120\n"
      "v_exp_f32 v239, v241\n"
      "v_mfma_f32_16x16x32_bf16 v[156:159], a[76:79], a[28:31], v[156:159]\n"
      "s_min_i32 s76, s76, 148\n"
      "ds_read_b64_tr_b16 a[156:157], v247 offset:1024\n"
      "v_fmamk_f32 v240, v180, 0x3e0293ee, v226\n"
      "v_add_f32 v233, v233, v239\n"
      "v_cvt_pk_bf16_f32 v215, v238, v239\n"
      "v_mfma_f32_16x16x32_bf16 v[128:131], a[80:83], a[32:35], v[128:131]\n"
      "s_max_i32 s76, s76, 94\n"
      "ds_read_b64_tr_b16 a[158:159], v247 offset:5120\n"
      "v_exp_f32 v236, v240\n"
      "v_mfma_f32_16x16x32_bf16 v[136:139], a[80:83], a[36:39], v[136:139]\n"
      "s_mov_b32 s82, s68\n"
      "v_fmamk_f32 v241, v181, 0x3e0293ee, v226\n"
      "v_add_f32 v234, v234, v236\n"
      "v_mfma_f32_16x16x32_bf16 v[144:147], a[80:83], a[40:43], v[144:147]\n"
      "s_lshr_b32 s69, s86, 1\n"
      "v_exp_f32 v237, v241\n"
      "v_mfma_f32_16x16x32_bf16 v[152:155], a[80:83], a[44:47], v[152:155]\n"
      "s_add_u32 s66, s66, s69\n"
      "v_fmamk_f32 v240, v182, 0x3e0293ee, v226\n"
      "v_add_f32 v234, v234, v237\n"
      "v_cvt_pk_bf16_f32 v218, v236, v237\n"
      "v_mfma_f32_16x16x32_bf16 v[132:135], a[84:87], a[32:35], v[132:135]\n"
      "s_add_u32 s82, s82, s69\n"
      "v_exp_f32 v238, v240\n"
      "v_mfma_f32_16x16x32_bf16 v[140:143], a[84:87], a[36:39], v[140:143]\n"
      "s_add_u32 s65, s82, s76\n"
      "v_fmamk_f32 v241, v183, 0x3e0293ee, v226\n"
      "v_add_f32 v234, v234, v238\n"
      "v_mfma_f32_16x16x32_bf16 v[148:151], a[84:87], a[40:43], v[148:151]\n"
      "s_sub_u32 s93, s93, 1\n"
      "v_exp_f32 v239, v241\n"
      "v_mfma_f32_16x16x32_bf16 v[156:159], a[84:87], a[44:47], v[156:159]\n"
      "s_cmp_lt_i32 s93, 10\n"
      "v_fmamk_f32 v240, v188, 0x3e0293ee, v227\n"
      "v_add_f32 v234, v234, v239\n"
      "v_cvt_pk_bf16_f32 v219, v238, v239\n"
      "v_mfma_f32_16x16x32_bf16 v[128:131], a[88:91], a[48:51], v[128:131]\n"
      "s_cselect_b32 s69, 3, 0\n"
      "v_exp_f32 v236, v240\n"
      "v_mfma_f32_16x16x32_bf16 v[136:139], a[88:91], a[52:55], v[136:139]\n"
      "s_sub_u32 s66, s66, s69\n"
      "v_fmamk_f32 v241, v189, 0x3e0293ee, v227\n"
      "v_add_f32 v235, v235, v236\n"
      "v_mfma_f32_16x16x32_bf16 v[144:147], a[88:91], a[56:59], v[144:147]\n"
      "s_sub_u32 s67, s65, s66\n"
      "v_exp_f32 v237, v241\n"
      "v_mfma_f32_16x16x32_bf16 v[152:155], a[88:91], a[60:63], v[152:155]\n"
      "s_cmp_lt_i32 s67, 0\n"
      "s_cselect_b32 s88, s66, s65\n"
      "v_fmamk_f32 v240, v190, 0x3e0293ee, v227\n"
      "v_add_f32 v235, v235, v237\n"
      "v_cvt_pk_bf16_f32 v222, v236, v237\n"
      "v_mfma_f32_16x16x32_bf16 v[132:135], a[92:95], a[48:51], v[132:135]\n"
      "s_mov_b32 s64, 1\n"
      "v_exp_f32 v238, v240\n"
      "v_mfma_f32_16x16x32_bf16 v[140:143], a[92:95], a[52:55], v[140:143]\n"
      "s_add_u32 s90, s88, s91\n"
      "v_fmamk_f32 v241, v191, 0x3e0293ee, v227\n"
      "v_add_f32 v235, v235, v238\n"
      "v_mfma_f32_16x16x32_bf16 v[148:151], a[92:95], a[56:59], v[148:151]\n"
      "v_exp_f32 v239, v241\n"
      "v_mfma_f32_16x16x32_bf16 v[156:159], a[92:95], a[60:63], v[156:159]\n"
      "s_waitcnt lgkmcnt(8)\n"
      "v_add_f32 v235, v235, v239\n"
      "v_cvt_pk_bf16_f32 v223, v238, v239\n"
      "v_max3_f32 v240, v232, v233, v234\n"
      "v_add_f32 v240, v240, v235\n"
      "v_cmp_nge_f32 vcc, 0x44800000, v240\n"
      "s_cbranch_vccnz FB1_%=\n"
      "RET1_1_%=:\n"
      "v_mfma_f32_16x16x32_bf16 v[0:3], a[128:131], v[208:211], v[0:3]\n"
      "s_add_u32 m0, s62, %[v0]\n"
      "v_fmamk_f32 v240, v128, 0x3e0293ee, v224\n"
      "v_exp_f32 v236, v240\n"
      "v_mfma_f32_16x16x32_bf16 v[4:7], a[128:131], v[212:215], v[4:7]\n"
      "buffer_load_dwordx4 v249, %[srdv], s77 offen nt lds\n"
      "v_fmamk_f32 v241, v129, 0x3e0293ee, v224\n"
      "v_add_f32 v228, v228, v232\n"
      "v_mfma_f32_16x16x32_bf16 v[8:11], a[128:131], v[216:219], v[8:11]\n"
      "s_add_u32 m0, s62, %[v1]\n"
      "v_exp_f32 v237, v241\n"
      "v_mfma_f32_16x16x32_bf16 v[12:15], a[128:131], v[220:223], v[12:15]\n"
      "buffer_load_dwordx4 v249, %[srdv], s77 offen offset:128 nt lds\n"
      "v_fmamk_f32 v240, v130, 0x3e0293ee, v224\n"
      "v_add_f32 v232, v236, v237\n"
      "v_cvt_pk_bf16_f32 v192, v236, v237\n"
      "v_mfma_f32_16x16x32_bf16 v[16:19], a[132:135], v[208:211], v[16:19]\n"
      "s_add_u32 m0, s62, %[v2]\n"
      "v_exp_f32 v238, v240\n"
      "v_mfma_f32_16x16x32_bf16 v[20:23], a[132:135], v[212:215], v[20:23]\n"
      "buffer_load_dwordx4 v249, %[srdv], s78 offen nt lds\n"
      "v_fmamk_f32 v241, v131, 0x3e0293ee, v224\n"
      "v_add_f32 v232, v232, v238\n"
      "v_mfma_f32_16x16x32_bf16 v[24:27], a[132:135], v[216:219], v[24:27]\n"
      "s_add_u32 m0, s62, %[v3]\n"
      "v_exp_f32 v239, v241\n"
      "v_mfma_f32_16x16x32_bf16 v[28:31], a[132:135], v[220:223], v[28:31]\n"
      "s_mov_b32 s77, s60\n"
      "buffer_load_dwordx4 v249, %[srdv], s78 offen offset:128 nt lds\n"
      "v_fmamk_f32 v240, v136, 0x3e0293ee, v225\n"
      "v_add_f32 v232, v232, v239\n"
      "v_cvt_pk_bf16_f32 v193, v238, v239\n"
      "v_mfma_f32_16x16x32_bf16 v[32:35], a[136:139], v[208:211], v[32:35]\n"
      "s_mov_b32 s78, s61\n"
      "v_exp_f32 v236, v240\n"
      "v_mfma_f32_16x16x32_bf16 v[36:39], a[136:139], v[212:215], v[36:39]\n"
      "s_add_u32 s60, s60, s79\n"
      "v_fmamk_f32 v241, v137, 0x3e0293ee, v225\n"
      "v_add_f32 v229, v229, v233\n"
      "v_mfma_f32_16x16x32_bf16 v[40:43], a[136:139], v[216:219], v[40:43]\n"
      "s_add_u32 s61, s61, s79\n"
      "v_exp_f32 v237, v241\n"
      "v_mfma_f32_16x16x32_bf16 v[44:47], a[136:139], v[220:223], v[44:47]\n"
      "s_sub_u32 s79, 0x10000, s79\n"
      "v_fmamk_f32 v240, v138, 0x3e0293ee, v225\n"
      "v_add_f32 v233, v236, v237\n"
      "v_cvt_pk_bf16_f32 v196, v236, v237\n"
      "v_mfma_f32_16x16x32_bf16 v[48:51], a[140:143], v[208:211], v[48:51]\n"
      "s_memrealtime s[68:69]\n"
      "v_exp_f32 v238, v240\n"
      "v_mfma_f32_16x16x32_bf16 v[52:55], a[140:143], v[212:215], v[52:55]\n"
      "s_add_u32 s62, s62, 0x8000\n"
      "v_fmamk_f32 v241, v139, 0x3e0293ee, v225\n"
      "v_add_f32 v233, v233, v238\n"
      "v_mfma_f32_16x16x32_bf16 v[56:59], a[140:143], v[216:219], v[56:59]\n"
      "s_and_b32 s62, s62, 0x1ffff\n"
      "v_exp_f32 v239, v241\n"
      "v_mfma_f32_16x16x32_bf16 v[60:63], a[140:143], v[220:223], v[60:63]\n"
      "v_fmamk_f32 v240, v144, 0x3e0293ee, v226\n"
      "v_add_f32 v233, v233, v239\n"
      "v_cvt_pk_bf16_f32 v197, v238, v239\n"
      "s_waitcnt vmcnt(8) lgkmcnt(0)\n"
      "s_barrier\n"
      "v_mfma_f32_16x16x32_bf16 v[64:67], a[144:147], v[208:211], v[64:67]\n"
      "ds_read_b128 a[64:67], v242 offset:32768\n"
      "v_exp_f32 v236, v240\n"
      "v_mfma_f32_16x16x32_bf16 v[68:71], a[144:147], v[212:215], v[68:71]\n"
      "ds_read_b128 a[68:71], v242 offset:36864\n"
      "v_fmamk_f32 v241, v145, 0x3e0293ee, v226\n"
      "v_add_f32 v230, v230, v234\n"
      "v_mfma_f32_16x16x32_bf16 v[72:75], a[144:147], v[216:219], v[72:75]\n"
      "ds_read_b128 a[72:75], v243 offset:32768\n"
      "v_exp_f32 v237, v241\n"
      "v_mfma_f32_16x16x32_bf16 v[76:79], a[144:147], v[220:223], v[76:79]\n"
      "ds_read_b128 a[76:79], v243 offset:36864\n"
      "v_fmamk_f32 v240, v146, 0x3e0293ee, v226\n"
      "v_add_f32 v234, v236, v237\n"
      "v_cvt_pk_bf16_f32 v200, v236, v237\n"
      "v_mfma_f32_16x16x32_bf16 v[80:83], a[148:151], v[208:211], v[80:83]\n"
      "ds_read_b128 a[80:83], v242 offset:33792\n"
      "v_exp_f32 v238, v240\n"
      "v_mfma_f32_16x16x32_bf16 v[84:87], a[148:151], v[212:215], v[84:87]\n"
      "ds_read_b128 a[84:87], v242 offset:37888\n"
      "v_fmamk_f32 v241, v147, 0x3e0293ee, v226\n"
      "v_add_f32 v234, v234, v238\n"
      "v_mfma_f32_16x16x32_bf16 v[88:91], a[148:151], v[216:219], v[88:91]\n"
      "ds_read_b128 a[88:91], v243 offset:33792\n"
      "v_exp_f32 v239, v241\n"
      "v_mfma_f32_16x16x32_bf16 v[92:95], a[148:151], v[220:223], v[92:95]\n"
      "ds_read_b128 a[92:95], v243 offset:37888\n"
      "v_fmamk_f32 v240, v152, 0x3e0293ee, v227\n"
      "v_add_f32 v234, v234, v239\n"
      "v_cvt_pk_bf16_f32 v201, v238, v239\n"
      "v_mfma_f32_16x16x32_bf16 v[96:99], a[152:155], v[208:211], v[96:99]\n"
      "v_exp_f32 v236, v240\n"
      "v_mfma_f32_16x16x32_bf16 v[100:103], a[152:155], v[212:215], v[100:103]\n"
      "v_fmamk_f32 v241, v153, 0x3e0293ee, v227\n"
      "v_add_f32 v231, v231, v235\n"
      "v_mfma_f32_16x16x32_bf16 v[104:107], a[152:155], v[216:219], v[104:107]\n"
      "v_exp_f32 v237, v241\n"
      "v_mfma_f32_16x16x32_bf16 v[108:111], a[152:155], v[220:223], v[108:111]\n"
      "v_fmamk_f32 v240, v154, 0x3e0293ee, v227\n"
      "v_add_f32 v235, v236, v237\n"
      "v_cvt_pk_bf16_f32 v204, v236, v237\n"
      "v_mfma_f32_16x16x32_bf16 v[112:115], a[156:159], v[208:211], v[112:115]\n"
      "v_exp_f32 v238, v240\n"
      "v_mfma_f32_16x16x32_bf16 v[116:119], a[156:159], v[212:215], v[116:119]\n"
      "v_fmamk_f32 v241, v155, 0x3e0293ee, v227\n"
      "v_add_f32 v235, v235, v238\n"
      "v_mfma_f32_16x16x32_bf16 v[120:123], a[156:159], v[216:219], v[120:123]\n"
      "v_exp_f32 v239, v241\n"
      "v_mfma_f32_16x16x32_bf16 v[124:127], a[156:159], v[220:223], v[124:127]\n"
      "s_waitcnt lgkmcnt(0)\n"
      "v_fmamk_f32 v240, v132, 0x3e0293ee, v224\n"
      "v_add_f32 v235, v235, v239\n"
      "v_cvt_pk_bf16_f32 v205, v238, v239\n"
      "v_mfma_f32_16x16x32_bf16 v[160:163], a[64:67], a[0:3], 0\n"
      "v_exp_f32 v236, v240\n"
      "v_mfma_f32_16x16x32_bf16 v[168:171], a[64:67], a[4:7], 0\n"
      "ds_read_b64_tr_b16 a[96:97], v244 offset:32768\n"
      "v_fmamk_f32 v241, v133, 0x3e0293ee, v224\n"
      "v_add_f32 v232, v232, v236\n"
      "v_mfma_f32_16x16x32_bf16 v[176:179], a[64:67], a[8:11], 0\n"
      "ds_read_b64_tr_b16 a[98:99], v244 offset:36864\n"
      "v_exp_f32 v237, v241\n"
      "v_mfma_f32_16x16x32_bf16 v[184:187], a[64:67], a[12:15], 0\n"
      "ds_read_b64_tr_b16 a[100:101], v245 offset:32768\n"
      "v_fmamk_f32 v240, v134, 0x3e0293ee, v224\n"
      "v_add_f32 v232, v232, v237\n"
      "v_cvt_pk_bf16_f32 v194, v236, v237\n"
      "v_mfma_f32_16x16x32_bf16 v[164:167], a[68:71], a[0:3], 0\n"
      "ds_read_b64_tr_b16 a[102:103], v245 offset:36864\n"
      "v_exp_f32 v238, v240\n"
      "v_mfma_f32_16x16x32_bf16 v[172:175], a[68:71], a[4:7], 0\n"
      "ds_read_b64_tr_b16 a[104:105], v246 offset:32768\n"
      "v_fmamk_f32 v241, v135, 0x3e0293ee, v224\n"
      "v_add_f32 v232, v232, v238\n"
      "v_mfma_f32_16x16x32_bf16 v[180:183], a[68:71], a[8:11], 0\n"
      "ds_read_b64_tr_b16 a[106:107], v246 offset:36864\n"
      "v_exp_f32 v239, v241\n"
      "v_mfma_f32_16x16x32_bf16 v[188:191], a[68:71], a[12:15], 0\n"
      "ds_read_b64_tr_b16 a[108:109], v247 offset:32768\n"
      "v_fmamk_f32 v240, v140, 0x3e0293ee, v225\n"
      "v_add_f32 v232, v232, v239\n"
      "v_cvt_pk_bf16_f32 v195, v238, v239\n"
      "v_mfma_f32_16x16x32_bf16 v[160:163], a[72:75], a[16:19], v[160:163]\n"
      "ds_read_b64_tr_b16 a[110:111], v247 offset:36864\n"
      "v_exp_f32 v236, v240\n"
      "v_mfma_f32_16x16x32_bf16 v[168:171], a[72:75], a[20:23], v[168:171]\n"
      "ds_read_b64_tr_b16 a[112:113], v244 offset:33792\n"
      "v_fmamk_f32 v241, v141, 0x3e0293ee, v225\n"
      "v_add_f32 v233, v233, v236\n"
      "v_mfma_f32_16x16x32_bf16 v[176:179], a[72:75], a[24:27], v[176:179]\n"
      "ds_read_b64_tr_b16 a[114:115], v244 offset:37888\n"
      "v_exp_f32 v237, v241\n"
      "v_mfma_f32_16x16x32_bf16 v[184:187], a[72:75], a[28:31], v[184:187]\n"
      "ds_read_b64_tr_b16 a[116:117], v245 offset:33792\n"
      "v_fmamk_f32 v240, v142, 0x3e0293ee, v225\n"
      "v_add_f32 v233, v233, v237\n"
      "v_cvt_pk_bf16_f32 v198, v236, v237\n"
      "v_mfma_f32_16x16x32_bf16 v[164:167], a[76:79], a[16:19], v[164:167]\n"
      "ds_read_b64_tr_b16 a[118:119], v245 offset:37888\n"
      "v_exp_f32 v238, v240\n"
      "v_mfma_f32_16x16x32_bf16 v[172:175], a[76:79], a[20:23], v[172:175]\n"
      "ds_read_b64_tr_b16 a[120:121], v246 offset:33792\n"
      "v_fmamk_f32 v241, v143, 0x3e0293ee, v225\n"
      "v_add_f32 v233, v233, v238\n"
      "v_mfma_f32_16x16x32_bf16 v[180:183], a[76:79], a[24:27], v[180:183]\n"
      "ds_read_b64_tr_b16 a[122:123], v246 offset:37888\n"
      "v_exp_f32 v239, v241\n"
      "v_mfma_f32_16x16x32_bf16 v[188:191], a[76:79], a[28:31], v[188:191]\n"
      "ds_read_b64_tr_b16 a[124:125], v247 offset:33792\n"
      "v_fmamk_f32 v240, v148, 0x3e0293ee, v226\n"
      "v_add_f32 v233, v233, v239\n"
      "v_cvt_pk_bf16_f32 v199, v238, v239\n"
      "v_mfma_f32_16x16x32_bf16 v[160:163], a[80:83], a[32:35], v[160:163]\n"
      "ds_read_b64_tr_b16 a[126:127], v247 offset:37888\n"
      "v_exp_f32 v236, v240\n"
      "v_mfma_f32_16x16x32_bf16 v[168:171], a[80:83], a[36:39], v[168:171]\n"
      "s_add_u32 m0, s62, 0\n"
      "v_fmamk_f32 v241, v149, 0x3e0293ee, v226\n"
      "v_add_f32 v234, v234, v236\n"
      "v_mfma_f32_16x16x32_bf16 v[176:179], a[80:83], a[40:43], v[176:179]\n"
      "buffer_load_dwordx4 v248, %[srdk], s60 offen nt lds\n"
      "v_exp_f32 v237, v241\n"
      "v_mfma_f32_16x16x32_bf16 v[184:187], a[80:83], a[44:47], v[184:187]\n"
      "s_add_u32 m0, s62, 896\n"
      "v_fmamk_f32 v240, v150, 0x3e0293ee, v226\n"
      "v_add_f32 v234, v234, v237\n"
      "v_cvt_pk_bf16_f32 v202, v236, v237\n"
      "v_mfma_f32_16x16x32_bf16 v[164:167], a[84:87], a[32:35], v[164:167]\n"
      "buffer_load_dwordx4 v248, %[srdk], s60 offen offset:128 nt lds\n"
      "v_exp_f32 v238, v240\n"
      "v_mfma_f32_16x16x32_bf16 v[172:175], a[84:87], a[36:39], v[172:175]\n"
      "s_add_u32 m0, s62, 8192\n"
      "v_fmamk_f32 v241, v151, 0x3e0293ee, v226\n"
      "v_add_f32 v234, v234, v238\n"
      "v_mfma_f32_16x16x32_bf16 v[180:183], a[84:87], a[40:43], v[180:183]\n"
      "buffer_load_dwordx4 v248, %[srdk], s61 offen nt lds\n"
      "v_exp_f32 v239, v241\n"
      "v_mfma_f32_16x16x32_bf16 v[188:191], a[84:87], a[44:47], v[188:191]\n"
      "s_add_u32 m0, s62, 9088\n"
      "v_fmamk_f32 v240, v156, 0x3e0293ee, v227\n"
      "v_add_f32 v234, v234, v239\n"
      "v_cvt_pk_bf16_f32 v203, v238, v239\n"
      "v_mfma_f32_16x16x32_bf16 v[160:163], a[88:91], a[48:51], v[160:163]\n"
      "buffer_load_dwordx4 v248, %[srdk], s61 offen offset:128 nt lds\n"
      "v_exp_f32 v236, v240\n"
      "v_mfma_f32_16x16x32_bf16 v[168:171], a[88:91], a[52:55], v[168:171]\n"
      "v_fmamk_f32 v241, v157, 0x3e0293ee, v227\n"
      "v_add_f32 v235, v235, v236\n"
      "v_mfma_f32_16x16x32_bf16 v[176:179], a[88:91], a[56:59], v[176:179]\n"
      "v_exp_f32 v237, v241\n"
      "v_mfma_f32_16x16x32_bf16 v[184:187], a[88:91], a[60:63], v[184:187]\n"
      "v_fmamk_f32 v240, v158, 0x3e0293ee, v227\n"
      "v_add_f32 v235, v235, v237\n"
      "v_cvt_pk_bf16_f32 v206, v236, v237\n"
      "v_mfma_f32_16x16x32_bf16 v[164:167], a[92:95], a[48:51], v[164:167]\n"
      "s_mov_b32 s64, 1\n"
      "v_exp_f32 v238, v240\n"
      "v_mfma_f32_16x16x32_bf16 v[172:175], a[92:95], a[52:55], v[172:175]\n"
      "v_fmamk_f32 v241, v159, 0x3e0293ee, v227\n"
      "v_add_f32 v235, v235, v238\n"
      "v_mfma_f32_16x16x32_bf16 v[180:183], a[92:95], a[56:59], v[180:183]\n"
      "v_exp_f32 v239, v241\n"
      "v_mfma_f32_16x16x32_bf16 v[188:191], a[92:95], a[60:63], v[188:191]\n"
      "s_waitcnt lgkmcnt(8)\n"
      "v_add_f32 v235, v235, v239\n"
      "v_cvt_pk_bf16_f32 v207, v238, v239\n"
      "v_max3_f32 v240, v232, v233, v234\n"
      "v_add_f32 v240, v240, v235\n"
      "v_cmp_nge_f32 vcc, 0x44800000, v240\n"
      "s_cbranch_vccnz FB0_%=\n"
      "RET0_1_%=:\n"
      "v_mfma_f32_16x16x32_bf16 v[0:3], a[96:99], v[192:195], v[0:3]\n"
      "v_fmamk_f32 v240, v160, 0x3e0293ee, v224\n"
      "v_exp_f32 v236, v240\n"
      "v_mfma_f32_16x16x32_bf16 v[4:7], a[96:99], v[196:199], v[4:7]\n"
      "v_fmamk_f32 v241, v161, 0x3e0293ee, v224\n"
      "v_add_f32 v228, v228, v232\n"
      "v_mfma_f32_16x16x32_bf16 v[8:11], a[96:99], v[200:203], v[8:11]\n"
      "v_exp_f32 v237, v241\n"
      "v_xor_b32 v242, 0x10000, v242\n"
      "v_mfma_f32_16x16x32_bf16 v[12:15], a[96:99], v[204:207], v[12:15]\n"
      "v_fmamk_f32 v240, v162, 0x3e0293ee, v224\n"
      "v_add_f32 v232, v236, v237\n"
      "v_cvt_pk_bf16_f32 v208, v236, v237\n"
      "v_mfma_f32_16x16x32_bf16 v[16:19], a[100:103], v[192:195], v[16:19]\n"
      "v_exp_f32 v238, v240\n"
      "v_xor_b32 v243, 0x10000, v243\n"
      "v_mfma_f32_16x16x32_bf16 v[20:23], a[100:103], v[196:199], v[20:23]\n"
      "v_fmamk_f32 v241, v163, 0x3e0293ee, v224\n"
      "v_add_f32 v232, v232, v238\n"
      "v_mfma_f32_16x16x32_bf16 v[24:27], a[100:103], v[200:203], v[24:27]\n"
      "v_exp_f32 v239, v241\n"
      "v_xor_b32 v244, 0x10000, v244\n"
      "v_mfma_f32_16x16x32_bf16 v[28:31], a[100:103], v[204:207], v[28:31]\n"
      "v_fmamk_f32 v240, v168, 0x3e0293ee, v225\n"
      "v_add_f32 v232, v232, v239\n"
      "v_cvt_pk_bf16_f32 v209, v238, v239\n"
      "v_mfma_f32_16x16x32_bf16 v[32:35], a[104:107], v[192:195], v[32:35]\n"
      "v_exp_f32 v236, v240\n"
      "v_xor_b32 v245, 0x10000, v245\n"
      "v_mfma_f32_16x16x32_bf16 v[36:39], a[104:107], v[196:199], v[36:39]\n"
      "v_fmamk_f32 v241, v169, 0x3e0293ee, v225\n"
      "v_add_f32 v229, v229, v233\n"
      "v_mfma_f32_16x16x32_bf16 v[40:43], a[104:107], v[200:203], v[40:43]\n"
      "v_exp_f32 v237, v241\n"
      "v_xor_b32 v246, 0x10000, v246\n"
      "v_mfma_f32_16x16x32_bf16 v[44:47], a[104:107], v[204:207], v[44:47]\n"
      "v_fmamk_f32 v240, v170, 0x3e0293ee, v225\n"
      "v_add_f32 v233, v236, v237\n"
      "v_cvt_pk_bf16_f32 v212, v236, v237\n"
      "v_mfma_f32_16x16x32_bf16 v[48:51], a[108:111], v[192:195], v[48:51]\n"
      "s_memrealtime s[68:69]\n"
      "v_exp_f32 v238, v240\n"
      "v_xor_b32 v247, 0x10000, v247\n"
      "v_mfma_f32_16x16x32_bf16 v[52:55], a[108:111], v[196:199], v[52:55]\n"
      "v_fmamk_f32 v241, v171, 0x3e0293ee, v225\n"
      "v_add_f32 v233, v233, v238\n"
      "v_mfma_f32_16x16x32_bf16 v[56:59], a[108:111], v[200:203], v[56:59]\n"
      "v_exp_f32 v239, v241\n"
      "v_mfma_f32_16x16x32_bf16 v[60:63], a[108:111], v[204:207], v[60:63]\n"
      "v_fmamk_f32 v240, v176, 0x3e0293ee, v226\n"
      "v_add_f32 v233, v233, v239\n"
      "v_cvt_pk_bf16_f32 v213, v238, v239\n"
      "s_sub_u32 s63, %[n2], 1\n"
      "LOOP2_%=:\n"
      "s_waitcnt vmcnt(8) lgkmcnt(0)\n"
      "s_sub_u32 s67, s68, s90\n"
      "s_cmp_lt_i32 s67, 0\n"
      "s_cbranch_scc1 GW3_%=\n"
      "GX3_%=:\n"
      "s_barrier\n"
      "v_mfma_f32_16x16x32_bf16 v[64:67], a[112:115], v[192:195], v[64:67]\n"
      "s_memrealtime s[68:69]\n"
      "ds_read_b128 a[64:67], v242 offset:0\n"
      "v_exp_f32 v236, v240\n"
      "v_mfma_f32_16x16x32_bf16 v[68:71], a[112:115], v[196:199], v[68:71]\n"
      "s_add_u32 m0, s62, %[v0]\n"
      "ds_read_b128 a[68:71], v242 offset:4096\n"
      "v_fmamk_f32 v241, v177, 0x3e0293ee, v226\n"
      "v_add_f32 v230, v230, v234\n"
      "v_mfma_f32_16x16x32_bf16 v[72:75], a[112:115], v[200:203], v[72:75]\n"
      "ds_read_b128 a[72:75], v243 offset:0\n"
      "buffer_load_dwordx4 v249, %[srdv], s77 offen nt lds\n"
      "v_exp_f32 v237, v241\n"
      "v_mfma_f32_16x16x32_bf16 v[76:79], a[112:115], v[204:207], v[76:79]\n"
      "s_add_u32 m0, s62, %[v1]\n"
      "ds_read_b128 a[76:79], v243 offset:4096\n"
      "v_fmamk_f32 v240, v178, 0x3e0293ee, v226\n"
      "v_add_f32 v234, v236, v237\n"
      "v_cvt_pk_bf16_f32 v216, v236, v237\n"
      "v_mfma_f32_16x16x32_bf16 v[80:83], a[116:119], v[192:195], v[80:83]\n"
      "ds_read_b128 a[80:83], v242 offset:1024\n"
      "buffer_load_dwordx4 v249, %[srdv], s77 offen offset:128 nt lds\n"
      "v_exp_f32 v238, v240\n"
      "v_mfma_f32_16x16x32_bf16 v[84:87], a[116:119], v[196:199], v[84:87]\n"
      "s_add_u32 m0, s62, %[v2]\n"
      "ds_read_b128 a[84:87], v242 offset:5120\n"
      "v_fmamk_f32 v241, v179, 0x3e0293ee, v226\n"
      "v_add_f32 v234, v234, v238\n"
      "v_mfma_f32_16x16x32_bf16 v[88:91], a[116:119], v[200:203], v[88:91]\n"
      "ds_read_b128 a[88:91], v243 offset:1024\n"
      "buffer_load_dwordx4 v249, %[srdv], s78 offen nt lds\n"
      "v_exp_f32 v239, v241\n"
      "v_mfma_f32_16x16x32_bf16 v[92:95], a[116:119], v[204:207], v[92:95]\n"
      "s_add_u32 m0, s62, %[v3]\n"
      "ds_read_b128 a[92:95], v243 offset:5120\n"
      "v_fmamk_f32 v240, v184, 0x3e0293ee, v227\n"
      "v_add_f32 v234, v234, v239\n"
      "v_cvt_pk_bf16_f32 v217, v238, v239\n"
      "v_mfma_f32_16x16x32_bf16 v[96:99], a[120:123], v[192:195], v[96:99]\n"
      "s_mov_b32 s77, s60\n"
      "buffer_load_dwordx4 v249, %[srdv], s78 offen offset:128 nt lds\n"
      "v_exp_f32 v236, v240\n"
      "v_mfma_f32_16x16x32_bf16 v[100:103], a[120:123], v[196:199], v[100:103]\n"
      "s_mov_b32 s78, s61\n"
      "v_fmamk_f32 v241, v185, 0x3e0293ee, v227\n"
      "v_add_f32 v231, v231, v235\n"
      "v_mfma_f32_16x16x32_bf16 v[104:107], a[120:123], v[200:203], v[104:107]\n"
      "s_add_u32 s60, s60, s79\n"
      "v_exp_f32 v237, v241\n"
      "v_mfma_f32_16x16x32_bf16 v[108:111], a[120:123], v[204:207], v[108:111]\n"
      "s_add_u32 s61, s61, s79\n"
      "v_fmamk_f32 v240, v186, 0x3e0293ee, v227\n"
      "v_add_f32 v235, v236, v237\n"
      "v_cvt_pk_bf16_f32 v220, v236, v237\n"
      "v_mfma_f32_16x16x32_bf16 v[112:115], a[124:127], v[192:195], v[112:115]\n"
      "s_sub_u32 s79, 0x10000, s79\n"
      "v_exp_f32 v238, v240\n"
      "v_mfma_f32_16x16x32_bf16 v[116:119], a[124:127], v[196:199], v[116:119]\n"
      "s_add_u32 s62, s62, 0x8000\n"
      "v_fmamk_f32 v241, v187, 0x3e0293ee, v227\n"
      "v_add_f32 v235, v235, v238\n"
      "v_mfma_f32_16x16x32_bf16 v[120:123], a[124:127], v[200:203], v[120:123]\n"
      "s_and_b32 s62, s62, 0x1ffff\n"
      "v_exp_f32 v239, v241\n"
      "v_mfma_f32_16x16x32_bf16 v[124:127], a[124:127], v[204:207], v[124:127]\n"
      "s_waitcnt lgkmcnt(0)\n"
      "v_fmamk_f32 v240, v164, 0x3e0293ee, v224\n"
      "v_add_f32 v235, v235, v239\n"
      "v_cvt_pk_bf16_f32 v221, v238, v239\n"
      "v_mfma_f32_16x16x32_bf16 v[128:131], a[64:67], a[0:3], 0\n"
      "s_sub_u32 s67, s68, s66\n"
      "v_exp_f32 v236, v240\n"
      "v_mfma_f32_16x16x32_bf16 v[136:139], a[64:67], a[4:7], 0\n"
      "s_cmp_gt_i32 s67, s70\n"
      "v_fmamk_f32 v241, v165, 0x3e0293ee, v224\n"
      "v_add_f32 v232, v232, v236\n"
      "v_mfma_f32_16x16x32_bf16 v[144:147], a[64:67], a[8:11], 0\n"
      "s_cselect_b32 s69, s86, 0\n"
      "v_exp_f32 v237, v241\n"
      "v_mfma_f32_16x16x32_bf16 v[152:155], a[64:67], a[12:15], 0\n"
      "s_add_u32 s66, s66, s69\n"
      "v_fmamk_f32 v240, v166, 0x3e0293ee, v224\n"
      "v_add_f32 v232, v232, v237\n"
      "v_cvt_pk_bf16_f32 v210, v236, v237\n"
      "v_mfma_f32_16x16x32_bf16 v[132:135], a[68:71], a[0:3], 0\n"
      "s_add_u32 s67, s67, 50\n"
      "v_exp_f32 v238, v240\n"
      "v_mfma_f32_16x16x32_bf16 v[140:143], a[68:71], a[4:7], 0\n"
      "s_min_i32 s70, s70, s67\n"
      "v_fmamk_f32 v241, v167, 0x3e0293ee, v224\n"
      "v_add_f32 v232, v232, v238\n"
      "v_mfma_f32_16x16x32_bf16 v[148:151], a[68:71], a[8:11], 0\n"
      "s_max_i32 s70, s70, 85\n"
      "v_exp_f32 v239, v241\n"
      "v_mfma_f32_16x16x32_bf16 v[156:159], a[68:71], a[12:15], 0\n"
      "s_add_u32 s66, s66, s86\n"
      "v_fmamk_f32 v240, v172, 0x3e0293ee, v225\n"
      "v_add_f32 v232, v232, v239\n"
      "v_cvt_pk_bf16_f32 v211, v238, v239\n"
      "v_mfma_f32_16x16x32_bf16 v[128:131], a[72:75], a[16:19], v[128:131]\n"
      "s_and_b32 s76, s80, 31\n"
      "v_exp_f32 v236, v240\n"
      "v_mfma_f32_16x16x32_bf16 v[136:139], a[72:75], a[20:23], v[136:139]\n"
      "s_lshr_b64 s[80:81], s[80:81], 5\n"
      "v_fmamk_f32 v241, v173, 0x3e0293ee, v225\n"
      "v_add_f32 v233, v233, v236\n"
      "v_mfma_f32_16x16x32_bf16 v[144:147], a[72:75], a[24:27], v[144:147]\n"
      "s_add_u32 s66, s66, s76\n"
      "v_exp_f32 v237, v241\n"
      "v_mfma_f32_16x16x32_bf16 v[152:155], a[72:75], a[28:31], v[152:155]\n"
      "s_sub_u32 s67, s68, s82\n"
      "v_fmamk_f32 v240, v174, 0x3e0293ee, v225\n"
      "v_add_f32 v233, v233, v237\n"
      "v_cvt_pk_bf16_f32 v214, v236, v237\n"
      "v_mfma_f32_16x16x32_bf16 v[132:135], a[76:79], a[16:19], v[132:135]\n"
      "s_min_i32 s76, s67, s83\n"
      "v_exp_f32 v238, v240\n"
      "v_mfma_f32_16x16x32_bf16 v[140:143], a[76:79], a[20:23], v[140:143]\n"
      "s_mov_b32 s83, s67\n"
      "v_fmamk_f32 v241, v175, 0x3e0293ee, v225\n"
      "v_add_f32 v233, v233, v238\n"
      "s_waitcnt vmcnt(8)\n"
      "s_barrier\n"
      "v_mfma_f32_16x16x32_bf16 v[148:151], a[76:79], a[24:27], v[148:151]\n"
      "s_sub_u32 s76, s76, 26\n"
      "ds_read_b64_tr_b16 a[128:129], v244 offset:0\n"
      "v_exp_f32 v239, v241\n"
      "v_mfma_f32_16x16x32_bf16 v[156:159], a[76:79], a[28:31], v[156:159]\n"
      "s_min_i32 s76, s76, 148\n"
      "ds_read_b64_tr_b16 a[130:131], v244 offset:4096\n"
      "v_fmamk_f32 v240, v180, 0x3e0293ee, v226\n"
      "v_add_f32 v233, v233, v239\n"
      "v_cvt_pk_bf16_f32 v215, v238, v239\n"
      "v_mfma_f32_16x16x32_bf16 v[128:131], a[80:83], a[32:35], v[128:131]\n"
      "s_max_i32 s76, s76, 94\n"
      "ds_read_b64_tr_b16 a[132:133], v245 offset:0\n"
      "v_exp_f32 v236, v240\n"
      "v_mfma_f32_16x16x32_bf16 v[136:139], a[80:83], a[36:39], v[136:139]\n"
      "s_mov_b32 s82, s68\n"
      "ds_read_b64_tr_b16 a[134:135], v245 offset:4096\n"
      "v_fmamk_f32 v241, v181, 0x3e0293ee, v226\n"
      "v_add_f32 v234, v234, v236\n"
      "v_mfma_f32_16x16x32_bf16 v[144:147], a[80:83], a[40:43], v[144:147]\n"
      "s_add_u32 s65, s82, s76\n"
      "ds_read_b64_tr_b16 a[136:137], v246 offset:0\n"
      "v_exp_f32 v237, v241\n"
      "v_mfma_f32_16x16x32_bf16 v[152:155], a[80:83], a[44:47], v[152:155]\n"
      "s_sub_u32 s93, s93, 1\n"
      "ds_read_b64_tr_b16 a[138:139], v246 offset:4096\n"
      "v_fmamk_f32 v240, v182, 0x3e0293ee, v226\n"
      "v_add_f32 v234, v234, v237\n"
      "v_cvt_pk_bf16_f32 v218, v236, v237\n"
      "v_mfma_f32_16x16x32_bf16 v[132:135], a[84:87], a[32:35], v[132:135]\n"
      "s_cmp_lt_i32 s93, 10\n"
      "ds_read_b64_tr_b16 a[140:141], v247 offset:0\n"
      "v_exp_f32 v238, v240\n"
      "v_mfma_f32_16x16x32_bf16 v[140:143], a[84:87], a[36:39], v[140:143]\n"
      "s_cselect_b32 s69, 3, 0\n"
      "ds_read_b64_tr_b16 a[142:143], v247 offset:4096\n"
      "v_fmamk_f32 v241, v183, 0x3e0293ee, v226\n"
      "v_add_f32 v234, v234, v238\n"
      "v_mfma_f32_16x16x32_bf16 v[148:151], a[84:87], a[40:43], v[148:151]\n"
      "s_sub_u32 s66, s66, s69\n"
      "ds_read_b64_tr_b16 a[144:145], v244 offset:1024\n"
      "v_exp_f32 v239, v241\n"
      "v_mfma_f32_16x16x32_bf16 v[156:159], a[84:87], a[44:47], v[156:159]\n"
      "s_sub_u32 s67, s65, s66\n"
      "ds_read_b64_tr_b16 a[146:147], v244 offset:5120\n"
      "v_fmamk_f32 v240, v188, 0x3e0293ee, v227\n"
      "v_add_f32 v234, v234, v239\n"
      "v_cvt_pk_bf16_f32 v219, v238, v239\n"
      "v_mfma_f32_16x16x32_bf16 v[128:131], a[88:91], a[48:51], v[128:131]\n"
      "s_cmp_lt_i32 s67, 0\n"
      "ds_read_b64_tr_b16 a[148:149], v245 offset:1024\n"
      "v_exp_f32 v236, v240\n"
      "v_mfma_f32_16x16x32_bf16 v[136:139], a[88:91], a[52:55], v[136:139]\n"
      "s_cselect_b32 s88, s66, s65\n"
      "ds_read_b64_tr_b16 a[150:151], v245 offset:5120\n"
      "v_fmamk_f32 v241, v189, 0x3e0293ee, v227\n"
      "v_add_f32 v235, v235, v236\n"
      "v_mfma_f32_16x16x32_bf16 v[144:147], a[88:91], a[56:59], v[144:147]\n"
      "s_add_u32 s90, s88, s91\n"
      "ds_read_b64_tr_b16 a[152:153], v246 offset:1024\n"
      "v_exp_f32 v237, v241\n"
      "v_mfma_f32_16x16x32_bf16 v[152:155], a[88:91], a[60:63], v[152:155]\n"
      "ds_read_b64_tr_b16 a[154:155], v246 offset:5120\n"
      "v_fmamk_f32 v240, v190, 0x3e0293ee, v227\n"
      "v_add_f32 v235, v235, v237\n"
      "v_cvt_pk_bf16_f32 v222, v236, v237\n"
      "v_mfma_f32_16x16x32_bf16 v[132:135], a[92:95], a[48:51], v[132:135]\n"
      "s_mov_b32 s64, 2\n"
      "ds_read_b64_tr_b16 a[156:157], v247 offset:1024\n"
      "v_exp_f32 v238, v240\n"
      "v_mfma_f32_16x16x32_bf16 v[140:143], a[92:95], a[52:55], v[140:143]\n"
      "ds_read_b64_tr_b16 a[158:159], v247 offset:5120\n"
      "v_fmamk_f32 v241, v191, 0x3e0293ee, v227\n"
      "v_add_f32 v235, v235, v238\n"
      "v_mfma_f32_16x16x32_bf16 v[148:151], a[92:95], a[56:59], v[148:151]\n"
      "v_exp_f32 v239, v241\n"
      "v_mfma_f32_16x16x32_bf16 v[156:159], a[92:95], a[60:63], v[156:159]\n"
      "s_waitcnt lgkmcnt(8)\n"
      "v_add_f32 v235, v235, v239\n"
      "v_cvt_pk_bf16_f32 v223, v238, v239\n"
      "v_max3_f32 v240, v232, v233, v234\n"
      "v_add_f32 v240, v240, v235\n"
      "v_cmp_nge_f32 vcc, 0x44800000, v240\n"
      "s_cbranch_vccnz FB1_%=\n"
      "RET1_2_%=:\n"
      "v_mfma_f32_16x16x32_bf16 v[0:3], a[128:131], v[208:211], v[0:3]\n"
      "s_add_u32 m0, s62, 0\n"
      "v_fmamk_f32 v240, v128, 0x3e0293ee, v224\n"
      "v_exp_f32 v236, v240\n"
      "v_mfma_f32_16x16x32_bf16 v[4:7], a[128:131], v[212:215], v[4:7]\n"
      "buffer_load_dwordx4 v248, %[srdk], s60 offen nt lds\n"
      "v_fmamk_f32 v241, v129, 0x3e0293ee, v224\n"
      "v_add_f32 v228, v228, v232\n"
      "v_mfma_f32_16x16x32_bf16 v[8:11], a[128:131], v[216:219], v[8:11]\n"
      "s_add_u32 m0, s62, 896\n"
      "v_exp_f32 v237, v241\n"
      "v_mfma_f32_16x16x32_bf16 v[12:15], a[128:131], v[220:223], v[12:15]\n"
      "buffer_load_dwordx4 v248, %[srdk], s60 offen offset:128 nt lds\n"
      "v_fmamk_f32 v240, v130, 0x3e0293ee, v224\n"
      "v_add_f32 v232, v236, v237\n"
      "v_cvt_pk_bf16_f32 v192, v236, v237\n"
      "v_mfma_f32_16x16x32_bf16 v[16:19], a[132:135], v[208:211], v[16:19]\n"
      "s_add_u32 m0, s62, 8192\n"
      "v_exp_f32 v238, v240\n"
      "v_mfma_f32_16x16x32_bf16 v[20:23], a[132:135], v[212:215], v[20:23]\n"
      "buffer_load_dwordx4 v248, %[srdk], s61 offen nt lds\n"
      "v_fmamk_f32 v241, v131, 0x3e0293ee, v224\n"
      "v_add_f32 v232, v232, v238\n"
      "v_mfma_f32_16x16x32_bf16 v[24:27], a[132:135], v[216:219], v[24:27]\n"
      "s_add_u32 m0, s62, 9088\n"
      "v_exp_f32 v239, v241\n"
      "v_mfma_f32_16x16x32_bf16 v[28:31], a[132:135], v[220:223], v[28:31]\n"
      "buffer_load_dwordx4 v248, %[srdk], s61 offen offset:128 nt lds\n"
      "v_fmamk_f32 v240, v136, 0x3e0293ee, v225\n"
      "v_add_f32 v232, v232, v239\n"
      "v_cvt_pk_bf16_f32 v193, v238, v239\n"
      "v_mfma_f32_16x16x32_bf16 v[32:35], a[136:139], v[208:211], v[32:35]\n"
      "v_exp_f32 v236, v240\n"
      "v_mfma_f32_16x16x32_bf16 v[36:39], a[136:139], v[212:215], v[36:39]\n"
      "v_fmamk_f32 v241, v137, 0x3e0293ee, v225\n"
      "v_add_f32 v229, v229, v233\n"
      "v_mfma_f32_16x16x32_bf16 v[40:43], a[136:139], v[216:219], v[40:43]\n"
      "v_exp_f32 v237, v241\n"
      "v_mfma_f32_16x16x32_bf16 v[44:47], a[136:139], v[220:223], v[44:47]\n"
      "v_fmamk_f32 v240, v138, 0x3e0293ee, v225\n"
      "v_add_f32 v233, v236, v237\n"
      "v_cvt_pk_bf16_f32 v196, v236, v237\n"
      "v_mfma_f32_16x16x32_bf16 v[48:51], a[140:143], v[208:211], v[48:51]\n"
      "s_memrealtime s[68:69]\n"
      "v_exp_f32 v238, v240\n"
      "v_mfma_f32_16x16x32_bf16 v[52:55], a[140:143], v[212:215], v[52:55]\n"
      "v_fmamk_f32 v241, v139, 0x3e0293ee, v225\n"
      "v_add_f32 v233, v233, v238\n"
      "v_mfma_f32_16x16x32_bf16 v[56:59], a[140:143], v[216:219], v[56:59]\n"
      "v_exp_f32 v239, v241\n"
      "v_mfma_f32_16x16x32_bf16 v[60:63], a[140:143], v[220:223], v[60:63]\n"
      "v_fmamk_f32 v240, v144, 0x3e0293ee, v226\n"
      "v_add_f32 v233, v233, v239\n"
      "v_cvt_pk_bf16_f32 v197, v238, v239\n"
      "s_waitcnt vmcnt(8) lgkmcnt(0)\n"
      "s_sub_u32 s67, s68, s90\n"
      "s_cmp_lt_i32 s67, 0\n"
      "s_cbranch_scc1 GW4_%=\n"
      "GX4_%=:\n"
      "s_barrier\n"
      "v_mfma_f32_16x16x32_bf16 v[64:67], a[144:147], v[208:211], v[64:67]\n"
      "s_memrealtime s[68:69]\n"
      "ds_read_b128 a[64:67], v242 offset:32768\n"
      "v_exp_f32 v236, v240\n"
      "v_mfma_f32_16x16x32_bf16 v[68:71], a[144:147], v[212:215], v[68:71]\n"
      "s_add_u32 m0, s62, %[v0]\n"
      "ds_read_b128 a[68:71], v242 offset:36864\n"
      "v_fmamk_f32 v241, v145, 0x3e0293ee, v226\n"
      "v_add_f32 v230, v230, v234\n"
      "v_mfma_f32_16x16x32_bf16 v[72:75], a[144:147], v[216:219], v[72:75]\n"
      "ds_read_b128 a[72:75], v243 offset:32768\n"
      "buffer_load_dwordx4 v249, %[srdv], s77 offen nt lds\n"
      "v_exp_f32 v237, v241\n"
      "v_mfma_f32_16x16x32_bf16 v[76:79], a[144:147], v[220:223], v[76:79]\n"
      "s_add_u32 m0, s62, %[v1]\n"
      "ds_read_b128 a[76:79], v243 offset:36864\n"
      "v_fmamk_f32 v240, v146, 0x3e0293ee, v226\n"
      "v_add_f32 v234, v236, v237\n"
      "v_cvt_pk_bf16_f32 v200, v236, v237\n"
      "v_mfma_f32_16x16x32_bf16 v[80:83], a[148:151], v[208:211], v[80:83]\n"
      "ds_read_b128 a[80:83], v242 offset:33792\n"
      "buffer_load_dwordx4 v249, %[srdv], s77 offen offset:128 nt lds\n"
      "v_exp_f32 v238, v240\n"
      "v_mfma_f32_16x16x32_bf16 v[84:87], a[148:151], v[212:215], v[84:87]\n"
      "s_add_u32 m0, s62, %[v2]\n"
      "ds_read_b128 a[84:87], v242 offset:37888\n"
      "v_fmamk_f32 v241, v147, 0x3e0293ee, v226\n"
      "v_add_f32 v234, v234, v238\n"
      "v_mfma_f32_16x16x32_bf16 v[88:91], a[148:151], v[216:219], v[88:91]\n"
      "ds_read_b128 a[88:91], v243 offset:33792\n"
      "buffer_load_dwordx4 v249, %[srdv], s78 offen nt lds\n"
      "v_exp_f32 v239, v241\n"
      "v_mfma_f32_16x16x32_bf16 v[92:95], a[148:151], v[220:223], v[92:95]\n"
      "s_add_u32 m0, s62, %[v3]\n"
      "ds_read_b128 a[92:95], v243 offset:37888\n"
      "v_fmamk_f32 v240, v152, 0x3e0293ee, v227\n"
      "v_add_f32 v234, v234, v239\n"
      "v_cvt_pk_bf16_f32 v201, v238, v239\n"
      "v_mfma_f32_16x16x32_bf16 v[96:99], a[152:155], v[208:211], v[96:99]\n"
      "s_mov_b32 s77, s60\n"
      "buffer_load_dwordx4 v249, %[srdv], s78 offen offset:128 nt lds\n"
      "v_exp_f32 v236, v240\n"
      "v_mfma_f32_16x16x32_bf16 v[100:103], a[152:155], v[212:215], v[100:103]\n"
      "s_mov_b32 s78, s61\n"
      "v_fmamk_f32 v241, v153, 0x3e0293ee, v227\n"
      "v_add_f32 v231, v231, v235\n"
      "v_mfma_f32_16x16x32_bf16 v[104:107], a[152:155], v[216:219], v[104:107]\n"
      "s_add_u32 s60, s60, s79\n"
      "v_exp_f32 v237, v241\n"
      "v_mfma_f32_16x16x32_bf16 v[108:111], a[152:155], v[220:223], v[108:111]\n"
      "s_add_u32 s61, s61, s79\n"
      "v_fmamk_f32 v240, v154, 0x3e0293ee, v227\n"
      "v_add_f32 v235, v236, v237\n"
      "v_cvt_pk_bf16_f32 v204, v236, v237\n"
      "v_mfma_f32_16x16x32_bf16 v[112:115], a[156:159], v[208:211], v[112:115]\n"
      "s_sub_u32 s79, 0x10000, s79\n"
      "v_exp_f32 v238, v240\n"
      "v_mfma_f32_16x16x32_bf16 v[116:119], a[156:159], v[212:215], v[116:119]\n"
      "s_add_u32 s62, s62, 0x8000\n"
      "v_fmamk_f32 v241, v155, 0x3e0293ee, v227\n"
      "v_add_f32 v235, v235, v238\n"
      "v_mfma_f32_16x16x32_bf16 v[120:123], a[156:159], v[216:219], v[120:123]\n"
      "s_and_b32 s62, s62, 0x1ffff\n"
      "v_exp_f32 v239, v241\n"
      "v_mfma_f32_16x16x32_bf16 v[124:127], a[156:159], v[220:223], v[124:127]\n"
      "s_waitcnt lgkmcnt(0)\n"
      "v_fmamk_f32 v240, v132, 0x3e0293ee, v224\n"
      "v_add_f32 v235, v235, v239\n"
      "v_cvt_pk_bf16_f32 v205, v238, v239\n"
      "v_mfma_f32_16x16x32_bf16 v[160:163], a[64:67], a[0:3], 0\n"
      "s_sub_u32 s67, s68, s66\n"
      "v_exp_f32 v236, v240\n"
      "v_mfma_f32_16x16x32_bf16 v[168:171], a[64:67], a[4:7], 0\n"
      "s_cmp_gt_i32 s67, s70\n"
      "v_fmamk_f32 v241, v133, 0x3e0293ee, v224\n"
      "v_add_f32 v232, v232, v236\n"
      "v_mfma_f32_16x16x32_bf16 v[176:179], a[64:67], a[8:11], 0\n"
      "s_cselect_b32 s69, s86, 0\n"
      "v_exp_f32 v237, v241\n"
      "v_mfma_f32_16x16x32_bf16 v[184:187], a[64:67], a[12:15], 0\n"
      "s_add_u32 s66, s66, s69\n"
      "v_fmamk_f32 v240, v134, 0x3e0293ee, v224\n"
      "v_add_f32 v232, v232, v237\n"
      "v_cvt_pk_bf16_f32 v194, v236, v237\n"
      "v_mfma_f32_16x16x32_bf16 v[164:167], a[68:71], a[0:3], 0\n"
      "s_add_u32 s67, s67, 50\n"
      "v_exp_f32 v238, v240\n"
      "v_mfma_f32_16x16x32_bf16 v[172:175], a[68:71], a[4:7], 0\n"
      "s_min_i32 s70, s70, s67\n"
      "v_fmamk_f32 v241, v135, 0x3e0293ee, v224\n"
      "v_add_f32 v232, v232, v238\n"
      "v_mfma_f32_16x16x32_bf16 v[180:183], a[68:71], a[8:11], 0\n"
      "s_max_i32 s70, s70, 85\n"
      "v_exp_f32 v239, v241\n"
      "v_mfma_f32_16x16x32_bf16 v[188:191], a[68:71], a[12:15], 0\n"
      "s_add_u32 s66, s66, s86\n"
      "v_fmamk_f32 v240, v140, 0x3e0293ee, v225\n"
      "v_add_f32 v232, v232, v239\n"
      "v_cvt_pk_bf16_f32 v195, v238, v239\n"
      "v_mfma_f32_16x16x32_bf16 v[160:163], a[72:75], a[16:19], v[160:163]\n"
      "s_and_b32 s76, s80, 31\n"
      "v_exp_f32 v236, v240\n"
      "v_mfma_f32_16x16x32_bf16 v[168:171], a[72:75], a[20:23], v[168:171]\n"
      "s_lshr_b64 s[80:81], s[80:81], 5\n"
      "v_fmamk_f32 v241, v141, 0x3e0293ee, v225\n"
      "v_add_f32 v233, v233, v236\n"
      "v_mfma_f32_16x16x32_bf16 v[176:179], a[72:75], a[24:27], v[176:179]\n"
      "s_add_u32 s66, s66, s76\n"
      "v_exp_f32 v237, v241\n"
      "v_mfma_f32_16x16x32_bf16 v[184:187], a[72:75], a[28:31], v[184:187]\n"
      "s_sub_u32 s67, s68, s82\n"
      "v_fmamk_f32 v240, v142, 0x3e0293ee, v225\n"
      "v_add_f32 v233, v233, v237\n"
      "v_cvt_pk_bf16_f32 v198, v236, v237\n"
      "v_mfma_f32_16x16x32_bf16 v[164:167], a[76:79], a[16:19], v[164:167]\n"
      "s_min_i32 s76, s67, s83\n"
      "v_exp_f32 v238, v240\n"
      "v_mfma_f32_16x16x32_bf16 v[172:175], a[76:79], a[20:23], v[172:175]\n"
      "s_mov_b32 s83, s67\n"
      "v_fmamk_f32 v241, v143, 0x3e0293ee, v225\n"
      "v_add_f32 v233, v233, v238\n"
      "s_waitcnt vmcnt(8)\n"
      "s_barrier\n"
      "v_mfma_f32_16x16x32_bf16 v[180:183], a[76:79], a[24:27], v[180:183]\n"
      "s_sub_u32 s76, s76, 26\n"
      "ds_read_b64_tr_b16 a[96:97], v244 offset:32768\n"
      "v_exp_f32 v239, v241\n"
      "v_mfma_f32_16x16x32_bf16 v[188:191], a[76:79], a[28:31], v[188:191]\n"
      "s_min_i32 s76, s76, 148\n"
      "ds_read_b64_tr_b16 a[98:99], v244 offset:36864\n"
      "v_fmamk_f32 v240, v148, 0x3e0293ee, v226\n"
      "v_add_f32 v233, v233, v239\n"
      "v_cvt_pk_bf16_f32 v199, v238, v239\n"
      "v_mfma_f32_16x16x32_bf16 v[160:163], a[80:83], a[32:35], v[160:163]\n"
      "s_max_i32 s76, s76, 94\n"
      "ds_read_b64_tr_b16 a[100:101], v245 offset:32768\n"
      "v_exp_f32 v236, v240\n"
      "v_mfma_f32_16x16x32_bf16 v[168:171], a[80:83], a[36:39], v[168:171]\n"
      "s_mov_b32 s82, s68\n"
      "ds_read_b64_tr_b16 a[102:103], v245 offset:36864\n"
      "v_fmamk_f32 v241, v149, 0x3e0293ee, v226\n"
      "v_add_f32 v234, v234, v236\n"
      "v_mfma_f32_16x16x32_bf16 v[176:179], a[80:83], a[40:43], v[176:179]\n"
      "s_add_u32 s65, s82, s76\n"
      "ds_read_b64_tr_b16 a[104:105], v246 offset:32768\n"
      "v_exp_f32 v237, v241\n"
      "v_mfma_f32_16x16x32_bf16 v[184:187], a[80:83], a[44:47], v[184:187]\n"
      "s_sub_u32 s93, s93, 1\n"
      "ds_read_b64_tr_b16 a[106:107], v246 offset:36864\n"
      "v_fmamk_f32 v240, v150, 0x3e0293ee, v226\n"
      "v_add_f32 v234, v234, v237\n"
      "v_cvt_pk_bf16_f32 v202, v236, v237\n"
      "v_mfma_f32_16x16x32_bf16 v[164:167], a[84:87], a[32:35], v[164:167]\n"
      "s_cmp_lt_i32 s93, 10\n"
      "ds_read_b64_tr_b16 a[108:109], v247 offset:32768\n"
      "v_exp_f32 v238, v240\n"
      "v_mfma_f32_16x16x32_bf16 v[172:175], a[84:87], a[36:39], v[172:175]\n"
      "s_cselect_b32 s69, 3, 0\n"
      "ds_read_b64_tr_b16 a[110:111], v247 offset:36864\n"
      "v_fmamk_f32 v241, v151, 0x3e0293ee, v226\n"
      "v_add_f32 v234, v234, v238\n"
      "v_mfma_f32_16x16x32_bf16 v[180:183], a[84:87], a[40:43], v[180:183]\n"
      "s_sub_u32 s66, s66, s69\n"
      "ds_read_b64_tr_b16 a[112:113], v244 offset:33792\n"
      "v_exp_f32 v239, v241\n"
      "v_mfma_f32_16x16x32_bf16 v[188:191], a[84:87], a[44:47], v[188:191]\n"
      "s_sub_u32 s67, s65, s66\n"
      "ds_read_b64_tr_b16 a[114:115], v244 offset:37888\n"
      "v_fmamk_f32 v240, v156, 0x3e0293ee, v227\n"
      "v_add_f32 v234, v234, v239\n"
      "v_cvt_pk_bf16_f32 v203, v238, v239\n"
      "v_mfma_f32_16x16x32_bf16 v[160:163], a[88:91], a[48:51], v[160:163]\n"
      "s_cmp_lt_i32 s67, 0\n"
      "ds_read_b64_tr_b16 a[116:117], v245 offset:33792\n"
      "v_exp_f32 v236, v240\n"
      "v_mfma_f32_16x16x32_bf16 v[168:171], a[88:91], a[52:55], v[168:171]\n"
      "s_cselect_b32 s88, s66, s65\n"
      "ds_read_b64_tr_b16 a[118:119], v245 offset:37888\n"
      "v_fmamk_f32 v241, v157, 0x3e0293ee, v227\n"
      "v_add_f32 v235, v235, v236\n"
      "v_mfma_f32_16x16x32_bf16 v[176:179], a[88:91], a[56:59], v[176:179]\n"
      "s_add_u32 s90, s88, s91\n"
      "ds_read_b64_tr_b16 a[120:121], v246 offset:33792\n"
      "v_exp_f32 v237, v241\n"
      "v_mfma_f32_16x16x32_bf16 v[184:187], a[88:91], a[60:63], v[184:187]\n"
      "ds_read_b64_tr_b16 a[122:123], v246 offset:37888\n"
      "v_fmamk_f32 v240, v158, 0x3e0293ee, v227\n"
      "v_add_f32 v235, v235, v237\n"
      "v_cvt_pk_bf16_f32 v206, v236, v237\n"
      "v_mfma_f32_16x16x32_bf16 v[164:167], a[92:95], a[48:51], v[164:167]\n"
      "s_mov_b32 s64, 2\n"
      "ds_read_b64_tr_b16 a[124:125], v247 offset:33792\n"
      "v_exp_f32 v238, v240\n"
      "v_mfma_f32_16x16x32_bf16 v[172:175], a[92:95], a[52:55], v[172:175]\n"
      "ds_read_b64_tr_b16 a[126:127], v247 offset:37888\n"
      "v_fmamk_f32 v241, v159, 0x3e0293ee, v227\n"
      "v_add_f32 v235, v235, v238\n"
      "v_mfma_f32_16x16x32_bf16 v[180:183], a[92:95], a[56:59], v[180:183]\n"
      "v_exp_f32 v239, v241\n"
      "v_mfma_f32_16x16x32_bf16 v[188:191], a[92:95], a[60:63], v[188:191]\n"
      "s_waitcnt lgkmcnt(8)\n"
      "v_add_f32 v235, v235, v239\n"
      "v_cvt_pk_bf16_f32 v207, v238, v239\n"
      "v_max3_f32 v240, v232, v233, v234\n"
      "v_add_f32 v240, v240, v235\n"
      "v_cmp_nge_f32 vcc, 0x44800000, v240\n"
      "s_cbranch_vccnz FB0_%=\n"
      "RET0_2_%=:\n"
      "v_mfma_f32_16x16x32_bf16 v[0:3], a[96:99], v[192:195], v[0:3]\n"
      "s_add_u32 m0, s62, 0\n"
      "v_fmamk_f32 v240, v160, 0x3e0293ee, v224\n"
      "v_exp_f32 v236, v240\n"
      "v_mfma_f32_16x16x32_bf16 v[4:7], a[96:99], v[196:199], v[4:7]\n"
      "buffer_load_dwordx4 v248, %[srdk], s60 offen nt lds\n"
      "v_fmamk_f32 v241, v161, 0x3e0293ee, v224\n"
      "v_add_f32 v228, v228, v232\n"
      "v_mfma_f32_16x16x32_bf16 v[8:11], a[96:99], v[200:203], v[8:11]\n"
      "s_add_u32 m0, s62, 896\n"
      "v_exp_f32 v237, v241\n"
      "v_xor_b32 v242, 0x10000, v242\n"
      "v_mfma_f32_16x16x32_bf16 v[12:15], a[96:99], v[204:207], v[12:15]\n"
      "buffer_load_dwordx4 v248, %[srdk], s60 offen offset:128 nt lds\n"
      "v_fmamk_f32 v240, v162, 0x3e0293ee, v224\n"
      "v_add_f32 v232, v236, v237\n"
      "v_cvt_pk_bf16_f32 v208, v236, v237\n"
      "v_mfma_f32_16x16x32_bf16 v[16:19], a[100:103], v[192:195], v[16:19]\n"
      "s_add_u32 m0, s62, 8192\n"
      "v_exp_f32 v238, v240\n"
      "v_xor_b32 v243, 0x10000, v243\n"
      "v_mfma_f32_16x16x32_bf16 v[20:23], a[100:103], v[196:199], v[20:23]\n"
      "buffer_load_dwordx4 v248, %[srdk], s61 offen nt lds\n"
      "v_fmamk_f32 v241, v163, 0x3e0293ee, v224\n"
      "v_add_f32 v232, v232, v238\n"
      "v_mfma_f32_16x16x32_bf16 v[24:27], a[100:103], v[200:203], v[24:27]\n"
      "s_add_u32 m0, s62, 9088\n"
      "v_exp_f32 v239, v241\n"
      "v_xor_b32 v244, 0x10000, v244\n"
      "v_mfma_f32_16x16x32_bf16 v[28:31], a[100:103], v[204:207], v[28:31]\n"
      "buffer_load_dwordx4 v248, %[srdk], s61 offen offset:128 nt lds\n"
      "v_fmamk_f32 v240, v168, 0x3e0293ee, v225\n"
      "v_add_f32 v232, v232, v239\n"
      "v_cvt_pk_bf16_f32 v209, v238, v239\n"
      "v_mfma_f32_16x16x32_bf16 v[32:35], a[104:107], v[192:195], v[32:35]\n"
      "v_exp_f32 v236, v240\n"
      "v_xor_b32 v245, 0x10000, v245\n"
      "v_mfma_f32_16x16x32_bf16 v[36:39], a[104:107], v[196:199], v[36:39]\n"
      "v_fmamk_f32 v241, v169, 0x3e0293ee, v225\n"
      "v_add_f32 v229, v229, v233\n"
      "v_mfma_f32_16x16x32_bf16 v[40:43], a[104:107], v[200:203], v[40:43]\n"
      "v_exp_f32 v237, v241\n"
      "v_xor_b32 v246, 0x10000, v246\n"
      "v_mfma_f32_16x16x32_bf16 v[44:47], a[104:107], v[204:207], v[44:47]\n"
      "v_fmamk_f32 v240, v170, 0x3e0293ee, v225\n"
      "v_add_f32 v233, v236, v237\n"
      "v_cvt_pk_bf16_f32 v212, v236, v237\n"
      "v_mfma_f32_16x16x32_bf16 v[48:51], a[108:111], v[192:195], v[48:51]\n"
      "s_memrealtime s[68:69]\n"
      "v_exp_f32 v238, v240\n"
      "v_xor_b32 v247, 0x10000, v247\n"
      "v_mfma_f32_16x16x32_bf16 v[52:55], a[108:111], v[196:199], v[52:55]\n"
      "v_fmamk_f32 v241, v171, 0x3e0293ee, v225\n"
      "v_add_f32 v233, v233, v238\n"
      "v_mfma_f32_16x16x32_bf16 v[56:59], a[108:111], v[200:203], v[56:59]\n"
      "v_exp_f32 v239, v241\n"
      "v_mfma_f32_16x16x32_bf16 v[60:63], a[108:111], v[204:207], v[60:63]\n"
      "s_sub_u32 s63, s63, 1\n"
      "v_fmamk_f32 v240, v176, 0x3e0293ee, v226\n"
      "v_add_f32 v233, v233, v239\n"
      "v_cvt_pk_bf16_f32 v213, v238, v239\n"
      "s_cbranch_scc0 LOOP2_%=\n"
      "s_waitcnt vmcnt(8) lgkmcnt(0)\n"
      "s_sub_u32 s67, s68, s90\n"
      "s_cmp_lt_i32 s67, 0\n"
      "s_cbranch_scc1 GW5_%=\n"
      "GX5_%=:\n"
      "s_barrier\n"
      "v_mfma_f32_16x16x32_bf16 v[64:67], a[112:115], v[192:195], v[64:67]\n"
      "ds_read_b128 a[64:67], v242 offset:0\n"
      "v_exp_f32 v236, v240\n"
      "v_mfma_f32_16x16x32_bf16 v[68:71], a[112:115], v[196:199], v[68:71]\n"
      "s_add_u32 m0, s62, %[v0]\n"
      "ds_read_b128 a[68:71], v242 offset:4096\n"
      "v_fmamk_f32 v241, v177, 0x3e0293ee, v226\n"
      "v_add_f32 v230, v230, v234\n"
      "v_mfma_f32_16x16x32_bf16 v[72:75], a[112:115], v[200:203], v[72:75]\n"
      "ds_read_b128 a[72:75], v243 offset:0\n"
      "buffer_load_dwordx4 v249, %[srdv], s77 offen nt lds\n"
      "v_exp_f32 v237, v241\n"
      "v_mfma_f32_16x16x32_bf16 v[76:79], a[112:115], v[204:207], v[76:79]\n"
      "s_add_u32 m0, s62, %[v1]\n"
      "ds_read_b128 a[76:79], v243 offset:4096\n"
      "v_fmamk_f32 v240, v178, 0x3e0293ee, v226\n"
      "v_add_f32 v234, v236, v237\n"
      "v_cvt_pk_bf16_f32 v216, v236, v237\n"
      "v_mfma_f32_16x16x32_bf16 v[80:83], a[116:119], v[192:195], v[80:83]\n"
      "ds_read_b128 a[80:83], v242 offset:1024\n"
      "buffer_load_dwordx4 v249, %[srdv], s77 offen offset:128 nt lds\n"
      "v_exp_f32 v238, v240\n"
      "v_mfma_f32_16x16x32_bf16 v[84:87], a[116:119], v[196:199], v[84:87]\n"
      "s_add_u32 m0, s62, %[v2]\n"
      "ds_read_b128 a[84:87], v242 offset:5120\n"
      "v_fmamk_f32 v241, v179, 0x3e0293ee, v226\n"
      "v_add_f32 v234, v234, v238\n"
      "v_mfma_f32_16x16x32_bf16 v[88:91], a[116:119], v[200:203], v[88:91]\n"
      "ds_read_b128 a[88:91], v243 offset:1024\n"
      "buffer_load_dwordx4 v249, %[srdv], s78 offen nt lds\n"
      "v_exp_f32 v239, v241\n"
      "v_mfma_f32_16x16x32_bf16 v[92:95], a[116:119], v[204:207], v[92:95]\n"
      "s_add_u32 m0, s62, %[v3]\n"
      "ds_read_b128 a[92:95], v243 offset:5120\n"
      "v_fmamk_f32 v240, v184, 0x3e0293ee, v227\n"
      "v_add_f32 v234, v234, v239\n"
      "v_cvt_pk_bf16_f32 v217, v238, v239\n"
      "v_mfma_f32_16x16x32_bf16 v[96:99], a[120:123], v[192:195], v[96:99]\n"
      "s_mov_b32 s77, s60\n"
      "buffer_load_dwordx4 v249, %[srdv], s78 offen offset:128 nt lds\n"
      "v_exp_f32 v236, v240\n"
      "v_mfma_f32_16x16x32_bf16 v[100:103], a[120:123], v[196:199], v[100:103]\n"
      "s_mov_b32 s78, s61\n"
      "v_fmamk_f32 v241, v185, 0x3e0293ee, v227\n"
      "v_add_f32 v231, v231, v235\n"
      "v_mfma_f32_16x16x32_bf16 v[104:107], a[120:123], v[200:203], v[104:107]\n"
      "s_add_u32 s60, s60, s79\n"
      "v_exp_f32 v237, v241\n"
      "v_mfma_f32_16x16x32_bf16 v[108:111], a[120:123], v[204:207], v[108:111]\n"
      "s_add_u32 s61, s61, s79\n"
      "v_fmamk_f32 v240, v186, 0x3e0293ee, v227\n"
      "v_add_f32 v235, v236, v237\n"
      "v_cvt_pk_bf16_f32 v220, v236, v237\n"
      "v_mfma_f32_16x16x32_bf16 v[112:115], a[124:127], v[192:195], v[112:115]\n"
      "s_sub_u32 s79, 0x10000, s79\n"
      "v_exp_f32 v238, v240\n"
      "v_mfma_f32_16x16x32_bf16 v[116:119], a[124:127], v[196:199], v[116:119]\n"
      "s_add_u32 s62, s62, 0x8000\n"
      "v_fmamk_f32 v241, v187, 0x3e0293ee, v227\n"
      "v_add_f32 v235, v235, v238\n"
      "v_mfma_f32_16x16x32_bf16 v[120:123], a[124:127], v[200:203], v[120:123]\n"
      "s_and_b32 s62, s62, 0x1ffff\n"
      "v_exp_f32 v239, v241\n"
      "v_mfma_f32_16x16x32_bf16 v[124:127], a[124:127], v[204:207], v[124:127]\n"
      "s_waitcnt lgkmcnt(0)\n"
      "v_fmamk_f32 v240, v164, 0x3e0293ee, v224\n"
      "v_add_f32 v235, v235, v239\n"
      "v_cvt_pk_bf16_f32 v221, v238, v239\n"
      "v_mfma_f32_16x16x32_bf16 v[128:131], a[64:67], a[0:3], 0\n"
      "v_exp_f32 v236, v240\n"
      "v_mfma_f32_16x16x32_bf16 v[136:139], a[64:67], a[4:7], 0\n"
      "v_fmamk_f32 v241, v165, 0x3e0293ee, v224\n"
      "v_add_f32 v232, v232, v236\n"
      "v_mfma_f32_16x16x32_bf16 v[144:147], a[64:67], a[8:11], 0\n"
      "v_exp_f32 v237, v241\n"
      "v_mfma_f32_16x16x32_bf16 v[152:155], a[64:67], a[12:15], 0\n"
      "v_fmamk_f32 v240, v166, 0x3e0293ee, v224\n"
      "v_add_f32 v232, v232, v237\n"
      "v_cvt_pk_bf16_f32 v210, v236, v237\n"
      "v_mfma_f32_16x16x32_bf16 v[132:135], a[68:71], a[0:3], 0\n"
      "v_exp_f32 v238, v240\n"
      "v_mfma_f32_16x16x32_bf16 v[140:143], a[68:71], a[4:7], 0\n"
      "v_fmamk_f32 v241, v167, 0x3e0293ee, v224\n"
      "v_add_f32 v232, v232, v238\n"
      "v_mfma_f32_16x16x32_bf16 v[148:151], a[68:71], a[8:11], 0\n"
      "v_exp_f32 v239, v241\n"
      "v_mfma_f32_16x16x32_bf16 v[156:159], a[68:71], a[12:15], 0\n"
      "v_fmamk_f32 v240, v172, 0x3e0293ee, v225\n"
      "v_add_f32 v232, v232, v239\n"
      "v_cvt_pk_bf16_f32 v211, v238, v239\n"
      "v_mfma_f32_16x16x32_bf16 v[128:131], a[72:75], a[16:19], v[128:131]\n"
      "v_exp_f32 v236, v240\n"
      "v_mfma_f32_16x16x32_bf16 v[136:139], a[72:75], a[20:23], v[136:139]\n"
      "v_fmamk_f32 v241, v173, 0x3e0293ee, v225\n"
      "v_add_f32 v233, v233, v236\n"
      "v_mfma_f32_16x16x32_bf16 v[144:147], a[72:75], a[24:27], v[144:147]\n"
      "v_exp_f32 v237, v241\n"
      "v_mfma_f32_16x16x32_bf16 v[152:155], a[72:75], a[28:31], v[152:155]\n"
      "v_fmamk_f32 v240, v174, 0x3e0293ee, v225\n"
      "v_add_f32 v233, v233, v237\n"
      "v_cvt_pk_bf16_f32 v214, v236, v237\n"
      "v_mfma_f32_16x16x32_bf16 v[132:135], a[76:79], a[16:19], v[132:135]\n"
      "v_exp_f32 v238, v240\n"
      "v_mfma_f32_16x16x32_bf16 v[140:143], a[76:79], a[20:23], v[140:143]\n"
      "v_fmamk_f32 v241, v175, 0x3e0293ee, v225\n"
      "v_add_f32 v233, v233, v238\n"
      "s_waitcnt vmcnt(8)\n"
      "s_barrier\n"
      "v_mfma_f32_16x16x32_bf16 v[148:151], a[76:79], a[24:27], v[148:151]\n"
      "ds_read_b64_tr_b16 a[128:129], v244 offset:0\n"
      "v_exp_f32 v239, v241\n"
      "v_mfma_f32_16x16x32_bf16 v[156:159], a[76:79], a[28:31], v[156:159]\n"
      "ds_read_b64_tr_b16 a[130:131], v244 offset:4096\n"
      "v_fmamk_f32 v240, v180, 0x3e0293ee, v226\n"
      "v_add_f32 v233, v233, v239\n"
      "v_cvt_pk_bf16_f32 v215, v238, v239\n"
      "v_mfma_f32_16x16x32_bf16 v[128:131], a[80:83], a[32:35], v[128:131]\n"
      "ds_read_b64_tr_b16 a[132:133], v245 offset:0\n"
      "v_exp_f32 v236, v240\n"
      "v_mfma_f32_16x16x32_bf16 v[136:139], a[80:83], a[36:39], v[136:139]\n"
      "ds_read_b64_tr_b16 a[134:135], v245 offset:4096\n"
      "v_fmamk_f32 v241, v181, 0x3e0293ee, v226\n"
      "v_add_f32 v234, v234, v236\n"
      "v_mfma_f32_16x16x32_bf16 v[144:147], a[80:83], a[40:43], v[144:147]\n"
      "ds_read_b64_tr_b16 a[136:137], v246 offset:0\n"
      "v_exp_f32 v237, v241\n"
      "v_mfma_f32_16x16x32_bf16 v[152:155], a[80:83], a[44:47], v[152:155]\n"
      "ds_read_b64_tr_b16 a[138:139], v246 offset:4096\n"
      "v_fmamk_f32 v240, v182, 0x3e0293ee, v226\n"
      "v_add_f32 v234, v234, v237\n"
      "v_cvt_pk_bf16_f32 v218, v236, v237\n"
      "v_mfma_f32_16x16x32_bf16 v[132:135], a[84:87], a[32:35], v[132:135]\n"
      "ds_read_b64_tr_b16 a[140:141], v247 offset:0\n"
      "v_exp_f32 v238, v240\n"
      "v_mfma_f32_16x16x32_bf16 v[140:143], a[84:87], a[36:39], v[140:143]\n"
      "ds_read_b64_tr_b16 a[142:143], v247 offset:4096\n"
      "v_fmamk_f32 v241, v183, 0x3e0293ee, v226\n"
      "v_add_f32 v234, v234, v238\n"
      "v_mfma_f32_16x16x32_bf16 v[148:151], a[84:87], a[40:43], v[148:151]\n"
      "ds_read_b64_tr_b16 a[144:145], v244 offset:1024\n"
      "v_exp_f32 v239, v241\n"
      "v_mfma_f32_16x16x32_bf16 v[156:159], a[84:87], a[44:47], v[156:159]\n"
      "ds_read_b64_tr_b16 a[146:147], v244 offset:5120\n"
      "v_fmamk_f32 v240, v188, 0x3e0293ee, v227\n"
      "v_add_f32 v234, v234, v239\n"
      "v_cvt_pk_bf16_f32 v219, v238, v239\n"
      "v_mfma_f32_16x16x32_bf16 v[128:131], a[88:91], a[48:51], v[128:131]\n"
      "ds_read_b64_tr_b16 a[148:149], v245 offset:1024\n"
      "v_exp_f32 v236, v240\n"
      "v_mfma_f32_16x16x32_bf16 v[136:139], a[88:91], a[52:55], v[136:139]\n"
      "ds_read_b64_tr_b16 a[150:151], v245 offset:5120\n"
      "v_fmamk_f32 v241, v189, 0x3e0293ee, v227\n"
      "v_add_f32 v235, v235, v236\n"
      "v_mfma_f32_16x16x32_bf16 v[144:147], a[88:91], a[56:59], v[144:147]\n"
      "ds_read_b64_tr_b16 a[152:153], v246 offset:1024\n"
      "v_exp_f32 v237, v241\n"
      "v_mfma_f32_16x16x32_bf16 v[152:155], a[88:91], a[60:63], v[152:155]\n"
      "ds_read_b64_tr_b16 a[154:155], v246 offset:5120\n"
      "v_fmamk_f32 v240, v190, 0x3e0293ee, v227\n"
      "v_add_f32 v235, v235, v237\n"
      "v_cvt_pk_bf16_f32 v222, v236, v237\n"
      "v_mfma_f32_16x16x32_bf16 v[132:135], a[92:95], a[48:51], v[132:135]\n"
      "s_mov_b32 s64, 3\n"
      "ds_read_b64_tr_b16 a[156:157], v247 offset:1024\n"
      "v_exp_f32 v238, v240\n"
      "v_mfma_f32_16x16x32_bf16 v[140:143], a[92:95], a[52:55], v[140:143]\n"
      "ds_read_b64_tr_b16 a[158:159], v247 offset:5120\n"
      "v_fmamk_f32 v241, v191, 0x3e0293ee, v227\n"
      "v_add_f32 v235, v235, v238\n"
      "v_mfma_f32_16x16x32_bf16 v[148:151], a[92:95], a[56:59], v[148:151]\n"
      "v_exp_f32 v239, v241\n"
      "v_mfma_f32_16x16x32_bf16 v[156:159], a[92:95], a[60:63], v[156:159]\n"
      "s_waitcnt lgkmcnt(8)\n"
      "v_add_f32 v235, v235, v239\n"
      "v_cvt_pk_bf16_f32 v223, v238, v239\n"
      "v_max3_f32 v240, v232, v233, v234\n"
      "v_add_f32 v240, v240, v235\n"
      "v_cmp_nge_f32 vcc, 0x44800000, v240\n"
      "s_cbranch_vccnz FB1_%=\n"
      "RET1_3_%=:\n"
      "v_mfma_f32_16x16x32_bf16 v[0:3], a[128:131], v[208:211], v[0:3]\n"
      "s_add_u32 m0, s62, %[v0]\n"
      "v_fmamk_f32 v240, v128, 0x3e0293ee, v224\n"
      "v_exp_f32 v236, v240\n"
      "v_mfma_f32_16x16x32_bf16 v[4:7], a[128:131], v[212:215], v[4:7]\n"
      "buffer_load_dwordx4 v249, %[srdv], s77 offen nt lds\n"
      "v_fmamk_f32 v241, v129, 0x3e0293ee, v224\n"
      "v_add_f32 v228, v228, v232\n"
      "v_mfma_f32_16x16x32_bf16 v[8:11], a[128:131], v[216:219], v[8:11]\n"
      "s_add_u32 m0, s62, %[v1]\n"
      "v_exp_f32 v237, v241\n"
      "v_mfma_f32_16x16x32_bf16 v[12:15], a[128:131], v[220:223], v[12:15]\n"
      "buffer_load_dwordx4 v249, %[srdv], s77 offen offset:128 nt lds\n"
      "v_fmamk_f32 v240, v130, 0x3e0293ee, v224\n"
      "v_add_f32 v232, v236, v237\n"
      "v_cvt_pk_bf16_f32 v192, v236, v237\n"
      "v_mfma_f32_16x16x32_bf16 v[16:19], a[132:135], v[208:211], v[16:19]\n"
      "s_add_u32 m0, s62, %[v2]\n"
      "v_exp_f32 v238, v240\n"
      "v_mfma_f32_16x16x32_bf16 v[20:23], a[132:135], v[212:215], v[20:23]\n"
      "buffer_load_dwordx4 v249, %[srdv], s78 offen nt lds\n"
      "v_fmamk_f32 v241, v131, 0x3e0293ee, v224\n"
      "v_add_f32 v232, v232, v238\n"
      "v_mfma_f32_16x16x32_bf16 v[24:27], a[132:135], v[216:219], v[24:27]\n"
      "s_add_u32 m0, s62, %[v3]\n"
      "v_exp_f32 v239, v241\n"
      "v_mfma_f32_16x16x32_bf16 v[28:31], a[132:135], v[220:223], v[28:31]\n"
      "buffer_load_dwordx4 v249, %[srdv], s78 offen offset:128 nt lds\n"
      "v_fmamk_f32 v240, v136, 0x3e0293ee, v225\n"
      "v_add_f32 v232, v232, v239\n"
      "v_cvt_pk_bf16_f32 v193, v238, v239\n"
      "v_mfma_f32_16x16x32_bf16 v[32:35], a[136:139], v[208:211], v[32:35]\n"
      "v_exp_f32 v236, v240\n"
      "v_mfma_f32_16x16x32_bf16 v[36:39], a[136:139], v[212:215], v[36:39]\n"
      "v_fmamk_f32 v241, v137, 0x3e0293ee, v225\n"
      "v_add_f32 v229, v229, v233\n"
      "v_mfma_f32_16x16x32_bf16 v[40:43], a[136:139], v[216:219], v[40:43]\n"
      "v_exp_f32 v237, v241\n"
      "v_mfma_f32_16x16x32_bf16 v[44:47], a[136:139], v[220:223], v[44:47]\n"
      "v_fmamk_f32 v240, v138, 0x3e0293ee, v225\n"
      "v_add_f32 v233, v236, v237\n"
      "v_cvt_pk_bf16_f32 v196, v236, v237\n"
      "v_mfma_f32_16x16x32_bf16 v[48:51], a[140:143], v[208:211], v[48:51]\n"
      "s_memrealtime s[68:69]\n"
      "v_exp_f32 v238, v240\n"
      "v_mfma_f32_16x16x32_bf16 v[52:55], a[140:143], v[212:215], v[52:55]\n"
      "v_fmamk_f32 v241, v139, 0x3e0293ee, v225\n"
      "v_add_f32 v233, v233, v238\n"
      "v_mfma_f32_16x16x32_bf16 v[56:59], a[140:143], v[216:219], v[56:59]\n"
      "v_exp_f32 v239, v241\n"
      "v_mfma_f32_16x16x32_bf16 v[60:63], a[140:143], v[220:223], v[60:63]\n"
      "v_fmamk_f32 v240, v144, 0x3e0293ee, v226\n"
      "v_add_f32 v233, v233, v239\n"
      "v_cvt_pk_bf16_f32 v197, v238, v239\n"
      "s_waitcnt vmcnt(8) lgkmcnt(0)\n"
      "s_barrier\n"
      "v_mfma_f32_16x16x32_bf16 v[64:67], a[144:147], v[208:211], v[64:67]\n"
      "ds_read_b128 a[64:67], v242 offset:32768\n"
      "v_exp_f32 v236, v240\n"
      "v_mfma_f32_16x16x32_bf16 v[68:71], a[144:147], v[212:215], v[68:71]\n"
      "ds_read_b128 a[68:71], v242 offset:36864\n"
      "v_fmamk_f32 v241, v145, 0x3e0293ee, v226\n"
      "v_add_f32 v230, v230, v234\n"
      "v_mfma_f32_16x16x32_bf16 v[72:75], a[144:147], v[216:219], v[72:75]\n"
      "ds_read_b128 a[72:75], v243 offset:32768\n"
      "v_exp_f32 v237, v241\n"
      "v_mfma_f32_16x16x32_bf16 v[76:79], a[144:147], v[220:223], v[76:79]\n"
      "ds_read_b128 a[76:79], v243 offset:36864\n"
      "v_fmamk_f32 v240, v146, 0x3e0293ee, v226\n"
      "v_add_f32 v234, v236, v237\n"
      "v_cvt_pk_bf16_f32 v200, v236, v237\n"
      "v_mfma_f32_16x16x32_bf16 v[80:83], a[148:151], v[208:211], v[80:83]\n"
      "ds_read_b128 a[80:83], v242 offset:33792\n"
      "v_exp_f32 v238, v240\n"
      "v_mfma_f32_16x16x32_bf16 v[84:87], a[148:151], v[212:215], v[84:87]\n"
      "ds_read_b128 a[84:87], v242 offset:37888\n"
      "v_fmamk_f32 v241, v147, 0x3e0293ee, v226\n"
      "v_add_f32 v234, v234, v238\n"
      "v_mfma_f32_16x16x32_bf16 v[88:91], a[148:151], v[216:219], v[88:91]\n"
      "ds_read_b128 a[88:91], v243 offset:33792\n"
      "v_exp_f32 v239, v241\n"
      "v_mfma_f32_16x16x32_bf16 v[92:95], a[148:151], v[220:223], v[92:95]\n"
      "ds_read_b128 a[92:95], v243 offset:37888\n"
      "v_fmamk_f32 v240, v152, 0x3e0293ee, v227\n"
      "v_add_f32 v234, v234, v239\n"
      "v_cvt_pk_bf16_f32 v201, v238, v239\n"
      "v_mfma_f32_16x16x32_bf16 v[96:99], a[152:155], v[208:211], v[96:99]\n"
      "v_exp_f32 v236, v240\n"
      "v_mfma_f32_16x16x32_bf16 v[100:103], a[152:155], v[212:215], v[100:103]\n"
      "v_fmamk_f32 v241, v153, 0x3e0293ee, v227\n"
      "v_add_f32 v231, v231, v235\n"
      "v_mfma_f32_16x16x32_bf16 v[104:107], a[152:155], v[216:219], v[104:107]\n"
      "v_exp_f32 v237, v241\n"
      "v_mfma_f32_16x16x32_bf16 v[108:111], a[152:155], v[220:223], v[108:111]\n"
      "v_fmamk_f32 v240, v154, 0x3e0293ee, v227\n"
      "v_add_f32 v235, v236, v237\n"
      "v_cvt_pk_bf16_f32 v204, v236, v237\n"
      "v_mfma_f32_16x16x32_bf16 v[112:115], a[156:159], v[208:211], v[112:115]\n"
      "v_exp_f32 v238, v240\n"
      "v_mfma_f32_16x16x32_bf16 v[116:119], a[156:159], v[212:215], v[116:119]\n"
      "v_fmamk_f32 v241, v155, 0x3e0293ee, v227\n"
      "v_add_f32 v235, v235, v238\n"
      "v_mfma_f32_16x16x32_bf16 v[120:123], a[156:159], v[216:219], v[120:123]\n"
      "v_exp_f32 v239, v241\n"
      "v_mfma_f32_16x16x32_bf16 v[124:127], a[156:159], v[220:223], v[124:127]\n"
      "s_waitcnt lgkmcnt(0)\n"
      "v_fmamk_f32 v240, v132, 0x3e0293ee, v224\n"
      "v_add_f32 v235, v235, v239\n"
      "v_cvt_pk_bf16_f32 v205, v238, v239\n"
      "v_mfma_f32_16x16x32_bf16 v[160:163], a[64:67], a[0:3], 0\n"
      "v_exp_f32 v236, v240\n"
      "v_mfma_f32_16x16x32_bf16 v[168:171], a[64:67], a[4:7], 0\n"
      "v_fmamk_f32 v241, v133, 0x3e0293ee, v224\n"
      "v_add_f32 v232, v232, v236\n"
      "v_mfma_f32_16x16x32_bf16 v[176:179], a[64:67], a[8:11], 0\n"
      "v_exp_f32 v237, v241\n"
      "v_mfma_f32_16x16x32_bf16 v[184:187], a[64:67], a[12:15], 0\n"
      "v_fmamk_f32 v240, v134, 0x3e0293ee, v224\n"
      "v_add_f32 v232, v232, v237\n"
      "v_cvt_pk_bf16_f32 v194, v236, v237\n"
      "v_mfma_f32_16x16x32_bf16 v[164:167], a[68:71], a[0:3], 0\n"
      "v_exp_f32 v238, v240\n"
      "v_mbcnt_lo_u32_b32 v248, -1, 0\n"
      "v_mfma_f32_16x16x32_bf16 v[172:175], a[68:71], a[4:7], 0\n"
      "v_fmamk_f32 v241, v135, 0x3e0293ee, v224\n"
      "v_add_f32 v232, v232, v238\n"
      "v_mfma_f32_16x16x32_bf16 v[180:183], a[68:71], a[8:11], 0\n"
      "v_exp_f32 v239, v241\n"
      "v_mbcnt_hi_u32_b32 v248, -1, v248\n"
      "v_mfma_f32_16x16x32_bf16 v[188:191], a[68:71], a[12:15], 0\n"
      "v_fmamk_f32 v240, v140, 0x3e0293ee, v225\n"
      "v_add_f32 v232, v232, v239\n"
      "v_cvt_pk_bf16_f32 v195, v238, v239\n"
      "v_mfma_f32_16x16x32_bf16 v[160:163], a[72:75], a[16:19], v[160:163]\n"
      "v_exp_f32 v236, v240\n"
      "v_lshlrev_b32 v248, 4, v248\n"
      "v_mfma_f32_16x16x32_bf16 v[168:171], a[72:75], a[20:23], v[168:171]\n"
      "v_fmamk_f32 v241, v141, 0x3e0293ee, v225\n"
      "v_add_f32 v233, v233, v236\n"
      "v_mfma_f32_16x16x32_bf16 v[176:179], a[72:75], a[24:27], v[176:179]\n"
      "v_exp_f32 v237, v241\n"
      "v_add_u32 v242, %[xw], v248\n"
      "v_mfma_f32_16x16x32_bf16 v[184:187], a[72:75], a[28:31], v[184:187]\n"
      "v_fmamk_f32 v240, v142, 0x3e0293ee, v225\n"
      "v_add_f32 v233, v233, v237\n"
      "v_cvt_pk_bf16_f32 v198, v236, v237\n"
      "v_mfma_f32_16x16x32_bf16 v[164:167], a[76:79], a[16:19], v[164:167]\n"
      "v_exp_f32 v238, v240\n"
      "v_xor_b32 v243, 0x4000, v242\n"
      "v_mfma_f32_16x16x32_bf16 v[172:175], a[76:79], a[20:23], v[172:175]\n"
      "v_fmamk_f32 v241, v143, 0x3e0293ee, v225\n"
      "v_add_f32 v233, v233, v238\n"
      "v_mfma_f32_16x16x32_bf16 v[180:183], a[76:79], a[24:27], v[180:183]\n"
      "v_exp_f32 v239, v241\n"
      "v_mfma_f32_16x16x32_bf16 v[188:191], a[76:79], a[28:31], v[188:191]\n"
      "v_fmamk_f32 v240, v148, 0x3e0293ee, v226\n"
      "v_add_f32 v233, v233, v239\n"
      "v_cvt_pk_bf16_f32 v199, v238, v239\n"
      "v_mfma_f32_16x16x32_bf16 v[160:163], a[80:83], a[32:35], v[160:163]\n"
      "v_exp_f32 v236, v240\n"
      "v_mfma_f32_16x16x32_bf16 v[168:171], a[80:83], a[36:39], v[168:171]\n"
      "v_fmamk_f32 v241, v149, 0x3e0293ee, v226\n"
      "v_add_f32 v234, v234, v236\n"
      "v_mfma_f32_16x16x32_bf16 v[176:179], a[80:83], a[40:43], v[176:179]\n"
      "v_exp_f32 v237, v241\n"
      "v_mfma_f32_16x16x32_bf16 v[184:187], a[80:83], a[44:47], v[184:187]\n"
      "v_fmamk_f32 v240, v150, 0x3e0293ee, v226\n"
      "v_add_f32 v234, v234, v237\n"
      "v_cvt_pk_bf16_f32 v202, v236, v237\n"
      "s_waitcnt vmcnt(4)\n"
      "s_barrier\n"
      "v_mfma_f32_16x16x32_bf16 v[164:167], a[84:87], a[32:35], v[164:167]\n"
      "ds_read_b64_tr_b16 a[120:121], v246 offset:33792\n"
      "v_exp_f32 v238, v240\n"
      "v_mfma_f32_16x16x32_bf16 v[172:175], a[84:87], a[36:39], v[172:175]\n"
      "ds_read_b64_tr_b16 a[122:123], v246 offset:37888\n"
      "v_fmamk_f32 v241, v151, 0x3e0293ee, v226\n"
      "v_add_f32 v234, v234, v238\n"
      "v_mfma_f32_16x16x32_bf16 v[180:183], a[84:87], a[40:43], v[180:183]\n"
      "ds_read_b64_tr_b16 a[124:125], v247 offset:33792\n"
      "v_exp_f32 v239, v241\n"
      "v_mfma_f32_16x16x32_bf16 v[188:191], a[84:87], a[44:47], v[188:191]\n"
      "ds_read_b64_tr_b16 a[126:127], v247 offset:37888\n"
      "v_fmamk_f32 v240, v156, 0x3e0293ee, v227\n"
      "v_add_f32 v234, v234, v239\n"
      "v_cvt_pk_bf16_f32 v203, v238, v239\n"
      "v_mfma_f32_16x16x32_bf16 v[160:163], a[88:91], a[48:51], v[160:163]\n"
      "ds_read_b64_tr_b16 a[96:97], v244 offset:32768\n"
      "v_exp_f32 v236, v240\n"
      "v_mfma_f32_16x16x32_bf16 v[168:171], a[88:91], a[52:55], v[168:171]\n"
      "ds_read_b64_tr_b16 a[98:99], v244 offset:36864\n"
      "v_fmamk_f32 v241, v157, 0x3e0293ee, v227\n"
      "v_add_f32 v235, v235, v236\n"
      "v_mfma_f32_16x16x32_bf16 v[176:179], a[88:91], a[56:59], v[176:179]\n"
      "ds_read_b64_tr_b16 a[100:101], v245 offset:32768\n"
      "v_exp_f32 v237, v241\n"
      "v_mfma_f32_16x16x32_bf16 v[184:187], a[88:91], a[60:63], v[184:187]\n"
      "ds_read_b64_tr_b16 a[102:103], v245 offset:36864\n"
      "v_fmamk_f32 v240, v158, 0x3e0293ee, v227\n"
      "v_add_f32 v235, v235, v237\n"
      "v_cvt_pk_bf16_f32 v206, v236, v237\n"
      "v_mfma_f32_16x16x32_bf16 v[164:167], a[92:95], a[48:51], v[164:167]\n"
      "s_mov_b32 s64, 3\n"
      "ds_read_b64_tr_b16 a[104:105], v246 offset:32768\n"
      "v_exp_f32 v238, v240\n"
      "v_mfma_f32_16x16x32_bf16 v[172:175], a[92:95], a[52:55], v[172:175]\n"
      "ds_read_b64_tr_b16 a[106:107], v246 offset:36864\n"
      "v_fmamk_f32 v241, v159, 0x3e0293ee, v227\n"
      "v_add_f32 v235, v235, v238\n"
      "v_mfma_f32_16x16x32_bf16 v[180:183], a[92:95], a[56:59], v[180:183]\n"
      "v_exp_f32 v239, v241\n"
      "v_mfma_f32_16x16x32_bf16 v[188:191], a[92:95], a[60:63], v[188:191]\n"
      "v_add_f32 v235, v235, v239\n"
      "v_cvt_pk_bf16_f32 v207, v238, v239\n"
      "v_max3_f32 v240, v232, v233, v234\n"
      "v_add_f32 v240, v240, v235\n"
      "v_cmp_nge_f32 vcc, 0x44800000, v240\n"
      "s_cbranch_vccnz FB0_%=\n"
      "RET0_3_%=:\n"
      "s_waitcnt lgkmcnt(2)\n"
      "v_mfma_f32_16x16x32_bf16 v[96:99], a[120:123], v[192:195], v[96:99]\n"
      "ds_read_b64_tr_b16 a[108:109], v247 offset:32768\n"
      "v_fmamk_f32 v240, v160, 0x3e0293ee, v224\n"
      "v_exp_f32 v236, v240\n"
      "v_fmamk_f32 v241, v161, 0x3e0293ee, v224\n"
      "v_mfma_f32_16x16x32_bf16 v[100:103], a[120:123], v[196:199], v[100:103]\n"
      "ds_read_b64_tr_b16 a[110:111], v247 offset:36864\n"
      "v_exp_f32 v237, v241\n"
      "v_fmamk_f32 v240, v162, 0x3e0293ee, v224\n"
      "v_add_f32 v228, v228, v232\n"
      "v_mfma_f32_16x16x32_bf16 v[104:107], a[120:123], v[200:203], v[104:107]\n"
      "ds_read_b64_tr_b16 a[112:113], v244 offset:33792\n"
      "v_exp_f32 v238, v240\n"
      "v_fmamk_f32 v241, v163, 0x3e0293ee, v224\n"
      "v_add_f32 v232, v236, v237\n"
      "v_cvt_pk_bf16_f32 v208, v236, v237\n"
      "v_mfma_f32_16x16x32_bf16 v[108:111], a[120:123], v[204:207], v[108:111]\n"
      "ds_read_b64_tr_b16 a[114:115], v244 offset:37888\n"
      "v_exp_f32 v239, v241\n"
      "v_fmamk_f32 v240, v168, 0x3e0293ee, v225\n"
      "v_add_f32 v232, v232, v238\n"
      "v_mfma_f32_16x16x32_bf16 v[112:115], a[124:127], v[192:195], v[112:115]\n"
      "ds_read_b64_tr_b16 a[116:117], v245 offset:33792\n"
      "v_exp_f32 v236, v240\n"
      "v_fmamk_f32 v241, v169, 0x3e0293ee, v225\n"
      "v_add_f32 v232, v232, v239\n"
      "v_cvt_pk_bf16_f32 v209, v238, v239\n"
      "v_mfma_f32_16x16x32_bf16 v[116:119], a[124:127], v[196:199], v[116:119]\n"
      "ds_read_b64_tr_b16 a[118:119], v245 offset:37888\n"
      "v_exp_f32 v237, v241\n"
      "v_fmamk_f32 v240, v170, 0x3e0293ee, v225\n"
      "v_add_f32 v229, v229, v233\n"
      "v_mfma_f32_16x16x32_bf16 v[120:123], a[124:127], v[200:203], v[120:123]\n"
      "v_exp_f32 v238, v240\n"
      "v_fmamk_f32 v241, v171, 0x3e0293ee, v225\n"
      "v_add_f32 v233, v236, v237\n"
      "v_cvt_pk_bf16_f32 v212, v236, v237\n"
      "v_mfma_f32_16x16x32_bf16 v[124:127], a[124:127], v[204:207], v[124:127]\n"
      "v_exp_f32 v239, v241\n"
      "v_fmamk_f32 v240, v176, 0x3e0293ee, v226\n"
      "v_add_f32 v233, v233, v238\n"
      "v_mfma_f32_16x16x32_bf16 v[0:3], a[96:99], v[192:195], v[0:3]\n"
      "v_exp_f32 v236, v240\n"
      "v_fmamk_f32 v241, v177, 0x3e0293ee, v226\n"
      "v_add_f32 v233, v233, v239\n"
      "v_cvt_pk_bf16_f32 v213, v238, v239\n"
      "v_mfma_f32_16x16x32_bf16 v[4:7], a[96:99], v[196:199], v[4:7]\n"
      "ds_write_b128 v242, v[96:99] offset:32768\n"
      "v_exp_f32 v237, v241\n"
      "v_fmamk_f32 v240, v178, 0x3e0293ee, v226\n"
      "v_add_f32 v230, v230, v234\n"
      "v_mfma_f32_16x16x32_bf16 v[8:11], a[96:99], v[200:203], v[8:11]\n"
      "v_exp_f32 v238, v240\n"
      "v_fmamk_f32 v241, v179, 0x3e0293ee, v226\n"
      "v_add_f32 v234, v236, v237\n"
      "v_cvt_pk_bf16_f32 v216, v236, v237\n"
      "v_xor_b32 v244, 0x10000, v244\n"
      "v_mfma_f32_16x16x32_bf16 v[12:15], a[96:99], v[204:207], v[12:15]\n"
      "ds_write_b128 v242, v[100:103] offset:33792\n"
      "v_exp_f32 v239, v241\n"
      "v_fmamk_f32 v240, v184, 0x3e0293ee, v227\n"
      "v_add_f32 v234, v234, v238\n"
      "v_mfma_f32_16x16x32_bf16 v[16:19], a[100:103], v[192:195], v[16:19]\n"
      "v_exp_f32 v236, v240\n"
      "v_fmamk_f32 v241, v185, 0x3e0293ee, v227\n"
      "v_add_f32 v234, v234, v239\n"
      "v_cvt_pk_bf16_f32 v217, v238, v239\n"
      "v_xor_b32 v245, 0x10000, v245\n"
      "v_mfma_f32_16x16x32_bf16 v[20:23], a[100:103], v[196:199], v[20:23]\n"
      "ds_write_b128 v242, v[104:107] offset:34816\n"
      "v_exp_f32 v237, v241\n"
      "v_fmamk_f32 v240, v186, 0x3e0293ee, v227\n"
      "v_add_f32 v231, v231, v235\n"
      "v_mfma_f32_16x16x32_bf16 v[24:27], a[100:103], v[200:203], v[24:27]\n"
      "v_exp_f32 v238, v240\n"
      "v_fmamk_f32 v241, v187, 0x3e0293ee, v227\n"
      "v_add_f32 v235, v236, v237\n"
      "v_cvt_pk_bf16_f32 v220, v236, v237\n"
      "v_xor_b32 v246, 0x10000, v246\n"
      "v_mfma_f32_16x16x32_bf16 v[28:31], a[100:103], v[204:207], v[28:31]\n"
      "ds_write_b128 v242, v[108:111] offset:35840\n"
      "v_exp_f32 v239, v241\n"
      "v_fmamk_f32 v240, v164, 0x3e0293ee, v224\n"
      "v_add_f32 v235, v235, v238\n"
      "s_waitcnt lgkmcnt(4)\n"
      "v_mfma_f32_16x16x32_bf16 v[32:35], a[104:107], v[192:195], v[32:35]\n"
      "v_exp_f32 v236, v240\n"
      "v_fmamk_f32 v241, v165, 0x3e0293ee, v224\n"
      "v_add_f32 v235, v235, v239\n"
      "v_cvt_pk_bf16_f32 v221, v238, v239\n"
      "v_xor_b32 v247, 0x10000, v247\n"
      "v_mfma_f32_16x16x32_bf16 v[36:39], a[104:107], v[196:199], v[36:39]\n"
      "ds_write_b128 v242, v[112:115] offset:36864\n"
      "v_exp_f32 v237, v241\n"
      "v_fmamk_f32 v240, v166, 0x3e0293ee, v224\n"
      "v_add_f32 v232, v232, v236\n"
      "v_mfma_f32_16x16x32_bf16 v[40:43], a[104:107], v[200:203], v[40:43]\n"
      "v_exp_f32 v238, v240\n"
      "v_fmamk_f32 v241, v167, 0x3e0293ee, v224\n"
      "v_add_f32 v232, v232, v237\n"
      "v_cvt_pk_bf16_f32 v210, v236, v237\n"
      "v_mfma_f32_16x16x32_bf16 v[44:47], a[104:107], v[204:207], v[44:47]\n"
      "ds_write_b128 v242, v[116:119] offset:37888\n"
      "v_exp_f32 v239, v241\n"
      "v_fmamk_f32 v240, v172, 0x3e0293ee, v225\n"
      "v_add_f32 v232, v232, v238\n"
      "v_mfma_f32_16x16x32_bf16 v[48:51], a[108:111], v[192:195], v[48:51]\n"
      "v_exp_f32 v236, v240\n"
      "v_fmamk_f32 v241, v173, 0x3e0293ee, v225\n"
      "v_add_f32 v232, v232, v239\n"
      "v_cvt_pk_bf16_f32 v211, v238, v239\n"
      "v_xor_b32 v250, 0x2000, v244\n"
      "v_mfma_f32_16x16x32_bf16 v[52:55], a[108:111], v[196:199], v[52:55]\n"
      "ds_write_b128 v242, v[120:123] offset:38912\n"
      "ds_write_b128 v242, v[208:211] offset:2048\n"
      "v_exp_f32 v237, v241\n"
      "v_fmamk_f32 v240, v174, 0x3e0293ee, v225\n"
      "v_add_f32 v233, v233, v236\n"
      "v_mfma_f32_16x16x32_bf16 v[56:59], a[108:111], v[200:203], v[56:59]\n"
      "v_exp_f32 v238, v240\n"
      "v_fmamk_f32 v241, v175, 0x3e0293ee, v225\n"
      "v_add_f32 v233, v233, v237\n"
      "v_cvt_pk_bf16_f32 v214, v236, v237\n"
      "v_xor_b32 v251, 0x2000, v245\n"
      "v_mfma_f32_16x16x32_bf16 v[60:63], a[108:111], v[204:207], v[60:63]\n"
      "ds_write_b128 v242, v[124:127] offset:39936\n"
      "v_exp_f32 v239, v241\n"
      "v_fmamk_f32 v240, v180, 0x3e0293ee, v226\n"
      "v_add_f32 v233, v233, v238\n"
      "v_mfma_f32_16x16x32_bf16 v[64:67], a[112:115], v[192:195], v[64:67]\n"
      "v_exp_f32 v236, v240\n"
      "v_fmamk_f32 v241, v181, 0x3e0293ee, v226\n"
      "v_add_f32 v233, v233, v239\n"
      "v_cvt_pk_bf16_f32 v215, v238, v239\n"
      "v_mfma_f32_16x16x32_bf16 v[68:71], a[112:115], v[196:199], v[68:71]\n"
      "ds_write_b128 v242, v[212:215] offset:3072\n"
      "v_exp_f32 v237, v241\n"
      "v_fmamk_f32 v240, v182, 0x3e0293ee, v226\n"
      "v_add_f32 v234, v234, v236\n"
      "v_mfma_f32_16x16x32_bf16 v[72:75], a[112:115], v[200:203], v[72:75]\n"
      "v_exp_f32 v238, v240\n"
      "v_fmamk_f32 v241, v183, 0x3e0293ee, v226\n"
      "v_add_f32 v234, v234, v237\n"
      "v_cvt_pk_bf16_f32 v218, v236, v237\n"
      "v_mfma_f32_16x16x32_bf16 v[76:79], a[112:115], v[204:207], v[76:79]\n"
      "v_exp_f32 v239, v241\n"
      "v_fmamk_f32 v240, v188, 0x3e0293ee, v227\n"
      "v_add_f32 v234, v234, v238\n"
      "v_mfma_f32_16x16x32_bf16 v[80:83], a[116:119], v[192:195], v[80:83]\n"
      "s_mov_b32 s64, 4\n"
      "v_exp_f32 v236, v240\n"
      "v_fmamk_f32 v241, v189, 0x3e0293ee, v227\n"
      "v_add_f32 v234, v234, v239\n"
      "v_cvt_pk_bf16_f32 v219, v238, v239\n"
      "v_mfma_f32_16x16x32_bf16 v[84:87], a[116:119], v[196:199], v[84:87]\n"
      "ds_write_b128 v242, v[216:219] offset:4096\n"
      "v_exp_f32 v237, v241\n"
      "v_fmamk_f32 v240, v190, 0x3e0293ee, v227\n"
      "v_add_f32 v235, v235, v236\n"
      "v_mfma_f32_16x16x32_bf16 v[88:91], a[116:119], v[200:203], v[88:91]\n"
      "v_exp_f32 v238, v240\n"
      "v_fmamk_f32 v241, v191, 0x3e0293ee, v227\n"
      "v_add_f32 v235, v235, v237\n"
      "v_cvt_pk_bf16_f32 v222, v236, v237\n"
      "v_mfma_f32_16x16x32_bf16 v[92:95], a[116:119], v[204:207], v[92:95]\n"
      "v_exp_f32 v239, v241\n"
      "v_add_f32 v235, v235, v238\n"
      "v_add_f32 v235, v235, v239\n"
      "v_cvt_pk_bf16_f32 v223, v238, v239\n"
      "v_max3_f32 v240, v232, v233, v234\n"
      "v_add_f32 v240, v240, v235\n"
      "v_cmp_nge_f32 vcc, 0x44800000, v240\n"
      "s_cbranch_vccnz FB1_%=\n"
      "RET1_4_%=:\n"
      "ds_write_b128 v242, v[220:223] offset:5120\n"
      "ds_write_b128 v242, v[224:227]\n"
      "v_add_f32 v228, v228, v232\n"
      "v_add_f32 v229, v229, v233\n"
      "v_add_f32 v230, v230, v234\n"
      "v_add_f32 v231, v231, v235\n"
      "ds_write_b128 v242, v[228:231] offset:1024\n"
      "s_waitcnt vmcnt(0) lgkmcnt(0)\n"
      "s_barrier\n"
      "ds_read_b64_tr_b16 a[144:145], v244 offset:1024\n"
      "ds_read_b64_tr_b16 a[146:147], v244 offset:5120\n"
      "ds_read_b64_tr_b16 a[148:149], v245 offset:1024\n"
      "ds_read_b64_tr_b16 a[150:151], v245 offset:5120\n"
      "ds_read_b128 v[128:131], v243\n"
      "ds_read_b128 v[132:135], v243 offset:1024\n"
      "ds_read_b128 v[192:195], v243 offset:2048\n"
      "ds_read_b128 v[196:199], v243 offset:3072\n"
      "ds_read_b128 v[200:203], v243 offset:4096\n"
      "ds_read_b128 v[204:207], v243 offset:5120\n"
      "s_waitcnt lgkmcnt(8)\n"
      "v_mfma_f32_16x16x32_bf16 v[64:67], a[144:147], v[208:211], v[64:67]\n"
      "ds_read_b64_tr_b16 a[152:153], v250 offset:1024\n"
      "ds_read_b64_tr_b16 a[154:155], v250 offset:5120\n"
      "v_mfma_f32_16x16x32_bf16 v[68:71], a[144:147], v[212:215], v[68:71]\n"
      "ds_read_b128 v[96:99], v243 offset:32768\n"
      "ds_read_b128 v[100:103], v243 offset:33792\n"
      "v_mfma_f32_16x16x32_bf16 v[72:75], a[144:147], v[216:219], v[72:75]\n"
      "ds_read_b128 v[104:107], v243 offset:34816\n"
      "ds_read_b128 v[108:111], v243 offset:35840\n"
      "v_mfma_f32_16x16x32_bf16 v[76:79], a[144:147], v[220:223], v[76:79]\n"
      "ds_read_b64_tr_b16 a[156:157], v251 offset:1024\n"
      "ds_read_b64_tr_b16 a[158:159], v251 offset:5120\n"
      "s_waitcnt lgkmcnt(12)\n"
      "v_mfma_f32_16x16x32_bf16 v[80:83], a[148:151], v[208:211], v[80:83]\n"
      "ds_read_b128 v[112:115], v243 offset:36864\n"
      "ds_read_b128 v[116:119], v243 offset:37888\n"
      "v_sub_f32 v232, v128, v224\n"
      "v_sub_f32 v233, v129, v225\n"
      "v_mfma_f32_16x16x32_bf16 v[84:87], a[148:151], v[212:215], v[84:87]\n"
      "ds_read_b128 v[120:123], v243 offset:38912\n"
      "ds_read_b128 v[124:127], v243 offset:39936\n"
      "v_sub_f32 v234, v130, v226\n"
      "v_sub_f32 v235, v131, v227\n"
      "v_mfma_f32_16x16x32_bf16 v[88:91], a[148:151], v[216:219], v[88:91]\n"
      "v_min_f32 v236, 0, -v232\n"
      "v_min_f32 v237, 0, -v233\n"
      "v_mfma_f32_16x16x32_bf16 v[92:95], a[148:151], v[220:223], v[92:95]\n"
      "v_min_f32 v238, 0, -v234\n"
      "v_min_f32 v239, 0, -v235\n"
      "s_waitcnt lgkmcnt(6)\n"
      "v_mfma_f32_16x16x32_bf16 v[96:99], a[152:155], v[192:195], v[96:99]\n"
      "ds_read_b64_tr_b16 a[136:137], v246 offset:0\n"
      "v_min_f32 v232, 0, v232\n"
      "v_min_f32 v233, 0, v233\n"
      "v_mfma_f32_16x16x32_bf16 v[100:103], a[152:155], v[196:199], v[100:103]\n"
      "ds_read_b64_tr_b16 a[138:139], v246 offset:4096\n"
      "v_min_f32 v234, 0, v234\n"
      "v_min_f32 v235, 0, v235\n"
      "v_mfma_f32_16x16x32_bf16 v[104:107], a[152:155], v[200:203], v[104:107]\n"
      "ds_read_b64_tr_b16 a[140:141], v247 offset:0\n"
      "v_exp_f32 v232, v232\n"
      "v_exp_f32 v233, v233\n"
      "v_mfma_f32_16x16x32_bf16 v[108:111], a[152:155], v[204:207], v[108:111]\n"
      "ds_read_b64_tr_b16 a[142:143], v247 offset:4096\n"
      "v_exp_f32 v234, v234\n"
      "v_exp_f32 v235, v235\n"
      "s_waitcnt lgkmcnt(4)\n"
      "v_mfma_f32_16x16x32_bf16 v[112:115], a[156:159], v[192:195], v[112:115]\n"
      "ds_read_b64_tr_b16 a[128:129], v244 offset:0\n"
      "v_exp_f32 v236, v236\n"
      "v_exp_f32 v237, v237\n"
      "v_mfma_f32_16x16x32_bf16 v[116:119], a[156:159], v[196:199], v[116:119]\n"
      "ds_read_b64_tr_b16 a[130:131], v244 offset:4096\n"
      "v_exp_f32 v238, v238\n"
      "v_exp_f32 v239, v239\n"
      "v_mfma_f32_16x16x32_bf16 v[120:123], a[156:159], v[200:203], v[120:123]\n"
      "ds_read_b64_tr_b16 a[132:133], v245 offset:0\n"
      "v_min_f32 v224, v224, v128\n"
      "v_min_f32 v225, v225, v129\n"
      "v_mfma_f32_16x16x32_bf16 v[124:127], a[156:159], v[204:207], v[124:127]\n"
      "ds_read_b64_tr_b16 a[134:135], v245 offset:4096\n"
      "v_min_f32 v226, v226, v130\n"
      "v_min_f32 v227, v227, v131\n"
      "v_pk_mul_f32 v[64:65], v[64:65], v[232:233] op_sel:[0,0] op_sel_hi:[1,0]\n"
      "v_pk_mul_f32 v[66:67], v[66:67], v[232:233] op_sel:[0,0] op_sel_hi:[1,0]\n"
      "v_pk_mul_f32 v[68:69], v[68:69], v[232:233] op_sel:[0,1] op_sel_hi:[1,1]\n"
      "v_pk_mul_f32 v[70:71], v[70:71], v[232:233] op_sel:[0,1] op_sel_hi:[1,1]\n"
      "v_pk_mul_f32 v[72:73], v[72:73], v[234:235] op_sel:[0,0] op_sel_hi:[1,0]\n"
      "v_pk_mul_f32 v[74:75], v[74:75], v[234:235] op_sel:[0,0] op_sel_hi:[1,0]\n"
      "v_pk_mul_f32 v[76:77], v[76:77], v[234:235] op_sel:[0,1] op_sel_hi:[1,1]\n"
      "v_pk_mul_f32 v[78:79], v[78:79], v[234:235] op_sel:[0,1] op_sel_hi:[1,1]\n"
      "v_pk_mul_f32 v[80:81], v[80:81], v[232:233] op_sel:[0,0] op_sel_hi:[1,0]\n"
      "v_pk_mul_f32 v[82:83], v[82:83], v[232:233] op_sel:[0,0] op_sel_hi:[1,0]\n"
      "v_pk_mul_f32 v[84:85], v[84:85], v[232:233] op_sel:[0,1] op_sel_hi:[1,1]\n"
      "v_pk_mul_f32 v[86:87], v[86:87], v[232:233] op_sel:[0,1] op_sel_hi:[1,1]\n"
      "v_pk_mul_f32 v[88:89], v[88:89], v[234:235] op_sel:[0,0] op_sel_hi:[1,0]\n"
      "v_pk_mul_f32 v[90:91], v[90:91], v[234:235] op_sel:[0,0] op_sel_hi:[1,0]\n"
      "v_pk_mul_f32 v[92:93], v[92:93], v[234:235] op_sel:[0,1] op_sel_hi:[1,1]\n"
      "v_pk_mul_f32 v[94:95], v[94:95], v[234:235] op_sel:[0,1] op_sel_hi:[1,1]\n"
      "v_pk_fma_f32 v[64:65], v[96:97], v[236:237], v[64:65] op_sel:[0,0,0] op_sel_hi:[1,0,1]\n"
      "v_pk_fma_f32 v[66:67], v[98:99], v[236:237], v[66:67] op_sel:[0,0,0] op_sel_hi:[1,0,1]\n"
      "v_pk_fma_f32 v[68:69], v[100:101], v[236:237], v[68:69] op_sel:[0,1,0] op_sel_hi:[1,1,1]\n"
      "v_pk_fma_f32 v[70:71], v[102:103], v[236:237], v[70:71] op_sel:[0,1,0] op_sel_hi:[1,1,1]\n"
      "v_cvt_pk_bf16_f32 v160, v64, v65\n"
      "v_cvt_pk_bf16_f32 v161, v66, v67\n"
      "v_cvt_pk_bf16_f32 v162, v68, v69\n"
      "v_cvt_pk_bf16_f32 v163, v70, v71\n"
      "buffer_store_dwordx4 v[160:163], v248, %[srdx], %[xo] offen offset:0 sc1\n"
      "v_pk_fma_f32 v[72:73], v[104:105], v[238:239], v[72:73] op_sel:[0,0,0] op_sel_hi:[1,0,1]\n"
      "v_pk_fma_f32 v[74:75], v[106:107], v[238:239], v[74:75] op_sel:[0,0,0] op_sel_hi:[1,0,1]\n"
      "v_pk_fma_f32 v[76:77], v[108:109], v[238:239], v[76:77] op_sel:[0,1,0] op_sel_hi:[1,1,1]\n"
      "v_pk_fma_f32 v[78:79], v[110:111], v[238:239], v[78:79] op_sel:[0,1,0] op_sel_hi:[1,1,1]\n"
      "v_cvt_pk_bf16_f32 v164, v72, v73\n"
      "v_cvt_pk_bf16_f32 v165, v74, v75\n"
      "v_cvt_pk_bf16_f32 v166, v76, v77\n"
      "v_cvt_pk_bf16_f32 v167, v78, v79\n"
      "buffer_store_dwordx4 v[164:167], v248, %[srdx], %[xo] offen offset:1024 sc1\n"
      "v_pk_fma_f32 v[80:81], v[112:113], v[236:237], v[80:81] op_sel:[0,0,0] op_sel_hi:[1,0,1]\n"
      "v_pk_fma_f32 v[82:83], v[114:115], v[236:237], v[82:83] op_sel:[0,0,0] op_sel_hi:[1,0,1]\n"
      "v_pk_fma_f32 v[84:85], v[116:117], v[236:237], v[84:85] op_sel:[0,1,0] op_sel_hi:[1,1,1]\n"
      "v_pk_fma_f32 v[86:87], v[118:119], v[236:237], v[86:87] op_sel:[0,1,0] op_sel_hi:[1,1,1]\n"
      "v_cvt_pk_bf16_f32 v168, v80, v81\n"
      "v_cvt_pk_bf16_f32 v169, v82, v83\n"
      "v_cvt_pk_bf16_f32 v170, v84, v85\n"
      "v_cvt_pk_bf16_f32 v171, v86, v87\n"
      "buffer_store_dwordx4 v[168:171], v248, %[srdx], %[xo] offen offset:2048 sc1\n"
      "v_pk_fma_f32 v[88:89], v[120:121], v[238:239], v[88:89] op_sel:[0,0,0] op_sel_hi:[1,0,1]\n"
      "v_pk_fma_f32 v[90:91], v[122:123], v[238:239], v[90:91] op_sel:[0,0,0] op_sel_hi:[1,0,1]\n"
      "v_pk_fma_f32 v[92:93], v[124:125], v[238:239], v[92:93] op_sel:[0,1,0] op_sel_hi:[1,1,1]\n"
      "v_pk_fma_f32 v[94:95], v[126:127], v[238:239], v[94:95] op_sel:[0,1,0] op_sel_hi:[1,1,1]\n"
      "v_cvt_pk_bf16_f32 v172, v88, v89\n"
      "v_cvt_pk_bf16_f32 v173, v90, v91\n"
      "v_cvt_pk_bf16_f32 v174, v92, v93\n"
      "v_cvt_pk_bf16_f32 v175, v94, v95\n"
      "buffer_store_dwordx4 v[172:175], v248, %[srdx], %[xo] offen offset:3072 sc1\n"
      "v_mul_f32 v228, v228, v232\n"
      "v_mul_f32 v229, v229, v233\n"
      "v_mul_f32 v230, v230, v234\n"
      "v_mul_f32 v231, v231, v235\n"
      "v_fmac_f32 v228, v132, v236\n"
      "v_fmac_f32 v229, v133, v237\n"
      "v_fmac_f32 v230, v134, v238\n"
      "v_fmac_f32 v231, v135, v239\n"
      "v_mov_b32 v128, v228\n"
      "v_mov_b32 v129, v229\n"
      "v_mov_b32 v130, v230\n"
      "v_mov_b32 v131, v231\n"
      "v_permlane16_swap_b32 v228, v128\n"
      "v_permlane16_swap_b32 v229, v129\n"
      "v_permlane16_swap_b32 v230, v130\n"
      "v_permlane16_swap_b32 v231, v131\n"
      "v_add_f32 v228, v228, v128\n"
      "v_add_f32 v229, v229, v129\n"
      "v_add_f32 v230, v230, v130\n"
      "v_add_f32 v231, v231, v131\n"
      "v_mov_b32 v128, v228\n"
      "v_mov_b32 v129, v229\n"
      "v_mov_b32 v130, v230\n"
      "v_mov_b32 v131, v231\n"
      "v_permlane32_swap_b32 v228, v128\n"
      "v_permlane32_swap_b32 v229, v129\n"
      "v_permlane32_swap_b32 v230, v130\n"
      "v_permlane32_swap_b32 v231, v131\n"
      "v_add_f32 v228, v228, v128\n"
      "v_add_f32 v229, v229, v129\n"
      "v_add_f32 v230, v230, v130\n"
      "v_add_f32 v231, v231, v131\n"
      "s_add_u32 s67, %[xo], 0x1000\n"
      "s_mov_b64 exec, 0xffff\n"
      "buffer_store_dwordx4 v[224:227], v248, %[srdx], s67 offen sc1\n"
      "buffer_store_dwordx4 v[228:231], v248, %[srdx], s67 offen offset:256 sc1\n"
      "s_mov_b64 exec, -1\n"
      "s_waitcnt lgkmcnt(0)\n"
      "v_mfma_f32_16x16x32_bf16 v[32:35], a[136:139], v[208:211], v[32:35]\n"
      "s_add_u32 s74, %[e0], 0x7f4a7c15\n"
      "s_addc_u32 s75, %[e1], 0x9e3779b9\n"
      "v_mov_b32 v128, s74\n"
      "v_mfma_f32_16x16x32_bf16 v[36:39], a[136:139], v[212:215], v[36:39]\n"
      "v_mov_b32 v129, s75\n"
      "v_mov_b32 v130, %[fo]\n"
      "v_sub_u32 v131, 24, v130\n"
      "v_mfma_f32_16x16x32_bf16 v[40:43], a[136:139], v[216:219], v[40:43]\n"
      "v_mov_b32 v138, 0\n"
      "v_add_u32 v242, 0x10000, v242\n"
      "v_add_u32 v243, 0x10000, v243\n"
      "v_mfma_f32_16x16x32_bf16 v[44:47], a[136:139], v[220:223], v[44:47]\n"
      "v_and_b32 v137, 0xf0, v248\n"
      "v_lshrrev_b32 v136, 3, v248\n"
      "v_and_b32 v136, 32, v136\n"
      "v_mfma_f32_16x16x32_bf16 v[48:51], a[140:143], v[208:211], v[48:51]\n"
      "v_lshl_or_b32 v136, v137, 4, v136\n"
      "v_lshrrev_b32 v240, 5, v248\n"
      "v_and_or_b32 v136, v240, 16, v136\n"
      "v_mfma_f32_16x16x32_bf16 v[52:55], a[140:143], v[212:215], v[52:55]\n"
      "s_sub_u32 s76, 18432, %[xo]\n"
      "s_add_u32 s69, s76, 0x1000\n"
      "s_mov_b32 s63, 0x100000\n"
      "v_mfma_f32_16x16x32_bf16 v[56:59], a[140:143], v[216:219], v[56:59]\n"
      "v_mfma_f32_16x16x32_bf16 v[60:63], a[140:143], v[220:223], v[60:63]\n"
      "v_mfma_f32_16x16x32_bf16 v[0:3], a[128:131], v[208:211], v[0:3]\n"
      "ds_write_b128 v242, v[32:35] offset:0\n"
      "v_mfma_f32_16x16x32_bf16 v[4:7], a[128:131], v[212:215], v[4:7]\n"
      "ds_write_b128 v242, v[36:39] offset:1024\n"
      "v_mfma_f32_16x16x32_bf16 v[8:11], a[128:131], v[216:219], v[8:11]\n"
      "ds_write_b128 v242, v[40:43] offset:2048\n"
      "v_mfma_f32_16x16x32_bf16 v[12:15], a[128:131], v[220:223], v[12:15]\n"
      "ds_write_b128 v242, v[44:47] offset:3072\n"
      "v_mfma_f32_16x16x32_bf16 v[16:19], a[132:135], v[208:211], v[16:19]\n"
      "ds_write_b128 v242, v[48:51] offset:4096\n"
      "v_mfma_f32_16x16x32_bf16 v[20:23], a[132:135], v[212:215], v[20:23]\n"
      "ds_write_b128 v242, v[52:55] offset:5120\n"
      "v_mfma_f32_16x16x32_bf16 v[24:27], a[132:135], v[216:219], v[24:27]\n"
      "ds_write_b128 v242, v[56:59] offset:6144\n"
      "v_mfma_f32_16x16x32_bf16 v[28:31], a[132:135], v[220:223], v[28:31]\n"
      "ds_write_b128 v242, v[60:63] offset:7168\n"
      "s_waitcnt vmcnt(0)\n"
      "s_mov_b64 exec, 1\n"
      "global_store_dwordx2 v130, v[128:129], %[line] sc1\n"
      "s_mov_b64 exec, -1\n"
      "s_waitcnt lgkmcnt(0)\n"
      "s_barrier\n"
      "ds_read_b128 v[32:35], v243 offset:0\n"
      "ds_read_b128 v[36:39], v243 offset:1024\n"
      "ds_read_b128 v[40:43], v243 offset:2048\n"
      "ds_read_b128 v[44:47], v243 offset:3072\n"
      "ds_read_b128 v[48:51], v243 offset:4096\n"
      "ds_read_b128 v[52:55], v243 offset:5120\n"
      "ds_read_b128 v[56:59], v243 offset:6144\n"
      "ds_read_b128 v[60:63], v243 offset:7168\n"
      "v_pk_mul_f32 v[0:1], v[0:1], v[232:233] op_sel:[0,0] op_sel_hi:[1,0]\n"
      "v_pk_mul_f32 v[2:3], v[2:3], v[232:233] op_sel:[0,0] op_sel_hi:[1,0]\n"
      "v_pk_mul_f32 v[4:5], v[4:5], v[232:233] op_sel:[0,1] op_sel_hi:[1,1]\n"
      "v_pk_mul_f32 v[6:7], v[6:7], v[232:233] op_sel:[0,1] op_sel_hi:[1,1]\n"
      "v_pk_mul_f32 v[8:9], v[8:9], v[234:235] op_sel:[0,0] op_sel_hi:[1,0]\n"
      "v_pk_mul_f32 v[10:11], v[10:11], v[234:235] op_sel:[0,0] op_sel_hi:[1,0]\n"
      "v_pk_mul_f32 v[12:13], v[12:13], v[234:235] op_sel:[0,1] op_sel_hi:[1,1]\n"
      "v_pk_mul_f32 v[14:15], v[14:15], v[234:235] op_sel:[0,1] op_sel_hi:[1,1]\n"
      "v_pk_mul_f32 v[16:17], v[16:17], v[232:233] op_sel:[0,0] op_sel_hi:[1,0]\n"
      "v_pk_mul_f32 v[18:19], v[18:19], v[232:233] op_sel:[0,0] op_sel_hi:[1,0]\n"
      "v_pk_mul_f32 v[20:21], v[20:21], v[232:233] op_sel:[0,1] op_sel_hi:[1,1]\n"
      "v_pk_mul_f32 v[22:23], v[22:23], v[232:233] op_sel:[0,1] op_sel_hi:[1,1]\n"
      "v_pk_mul_f32 v[24:25], v[24:25], v[234:235] op_sel:[0,0] op_sel_hi:[1,0]\n"
      "v_pk_mul_f32 v[26:27], v[26:27], v[234:235] op_sel:[0,0] op_sel_hi:[1,0]\n"
      "v_pk_mul_f32 v[28:29], v[28:29], v[234:235] op_sel:[0,1] op_sel_hi:[1,1]\n"
      "v_pk_mul_f32 v[30:31], v[30:31], v[234:235] op_sel:[0,1] op_sel_hi:[1,1]\n"
      "global_load_dwordx2 v[132:133], v131, %[line] sc1\n"
      "s_waitcnt lgkmcnt(6)\n"
      "v_pk_fma_f32 v[0:1], v[32:33], v[236:237], v[0:1] op_sel:[0,0,0] op_sel_hi:[1,0,1]\n"
      "v_pk_fma_f32 v[2:3], v[34:35], v[236:237], v[2:3] op_sel:[0,0,0] op_sel_hi:[1,0,1]\n"
      "v_pk_fma_f32 v[4:5], v[36:37], v[236:237], v[4:5] op_sel:[0,1,0] op_sel_hi:[1,1,1]\n"
      "v_pk_fma_f32 v[6:7], v[38:39], v[236:237], v[6:7] op_sel:[0,1,0] op_sel_hi:[1,1,1]\n"
      "s_waitcnt lgkmcnt(4)\n"
      "v_pk_fma_f32 v[8:9], v[40:41], v[238:239], v[8:9] op_sel:[0,0,0] op_sel_hi:[1,0,1]\n"
      "v_pk_fma_f32 v[10:11], v[42:43], v[238:239], v[10:11] op_sel:[0,0,0] op_sel_hi:[1,0,1]\n"
      "v_pk_fma_f32 v[12:13], v[44:45], v[238:239], v[12:13] op_sel:[0,1,0] op_sel_hi:[1,1,1]\n"
      "v_pk_fma_f32 v[14:15], v[46:47], v[238:239], v[14:15] op_sel:[0,1,0] op_sel_hi:[1,1,1]\n"
      "s_waitcnt lgkmcnt(2)\n"
      "v_pk_fma_f32 v[16:17], v[48:49], v[236:237], v[16:17] op_sel:[0,0,0] op_sel_hi:[1,0,1]\n"
      "v_pk_fma_f32 v[18:19], v[50:51], v[236:237], v[18:19] op_sel:[0,0,0] op_sel_hi:[1,0,1]\n"
      "v_pk_fma_f32 v[20:21], v[52:53], v[236:237], v[20:21] op_sel:[0,1,0] op_sel_hi:[1,1,1]\n"
      "v_pk_fma_f32 v[22:23], v[54:55], v[236:237], v[22:23] op_sel:[0,1,0] op_sel_hi:[1,1,1]\n"
      "s_waitcnt lgkmcnt(0)\n"
      "v_pk_fma_f32 v[24:25], v[56:57], v[238:239], v[24:25] op_sel:[0,0,0] op_sel_hi:[1,0,1]\n"
      "v_pk_fma_f32 v[26:27], v[58:59], v[238:239], v[26:27] op_sel:[0,0,0] op_sel_hi:[1,0,1]\n"
      "v_pk_fma_f32 v[28:29], v[60:61], v[238:239], v[28:29] op_sel:[0,1,0] op_sel_hi:[1,1,1]\n"
      "v_pk_fma_f32 v[30:31], v[62:63], v[238:239], v[30:31] op_sel:[0,1,0] op_sel_hi:[1,1,1]\n"
      "POLL_%=:\n"
      "global_load_dwordx2 v[134:135], v131, %[line] sc1\n"
      "s_waitcnt vmcnt(1)\n"
      "v_cmp_ne_u64 vcc, v[132:133], v[128:129]\n"
      "s_cbranch_vccz GOT_%=\n"
      "global_load_dwordx2 v[132:133], v131, %[line] sc1\n"
      "s_waitcnt vmcnt(1)\n"
      "v_cmp_ne_u64 vcc, v[134:135], v[128:129]\n"
      "s_cbranch_vccz GOT_%=\n"
      "s_sub_u32 s63, s63, 1\n"
      "s_cbranch_scc0 POLL_%=\n"
      "GOT_%=:\n"
      "buffer_load_dwordx4 v[192:195], v137, %[srdx], s69 offen sc1\n"
      "buffer_load_dwordx4 v[196:199], v137, %[srdx], s69 offen offset:256 sc1\n"
      "buffer_load_dwordx4 v[96:99], v248, %[srdx], s76 offen offset:0 sc1\n"
      "buffer_load_dwordx4 v[104:107], v248, %[srdx], s76 offen offset:2048 sc1\n"
      "buffer_load_dwordx4 v[100:103], v248, %[srdx], s76 offen offset:1024 sc1\n"
      "buffer_load_dwordx4 v[108:111], v248, %[srdx], s76 offen offset:3072 sc1\n"
      "s_waitcnt vmcnt(4)\n"
      "v_sub_f32 v200, v192, v224\n"
      "v_sub_f32 v201, v193, v225\n"
      "v_sub_f32 v202, v194, v226\n"
      "v_sub_f32 v203, v195, v227\n"
      "v_min_f32 v204, 0, -v200\n"
      "v_min_f32 v205, 0, -v201\n"
      "v_min_f32 v206, 0, -v202\n"
      "v_min_f32 v207, 0, -v203\n"
      "v_min_f32 v200, 0, v200\n"
      "v_min_f32 v201, 0, v201\n"
      "v_min_f32 v202, 0, v202\n"
      "v_min_f32 v203, 0, v203\n"
      "v_exp_f32 v200, v200\n"
      "v_exp_f32 v201, v201\n"
      "v_exp_f32 v202, v202\n"
      "v_exp_f32 v203, v203\n"
      "v_exp_f32 v204, v204\n"
      "v_exp_f32 v205, v205\n"
      "v_exp_f32 v206, v206\n"
      "v_exp_f32 v207, v207\n"
      "v_mul_f32 v228, v228, v200\n"
      "v_mul_f32 v229, v229, v201\n"
      "v_mul_f32 v230, v230, v202\n"
      "v_mul_f32 v231, v231, v203\n"
      "v_fmac_f32 v228, v196, v204\n"
      "v_fmac_f32 v229, v197, v205\n"
      "v_fmac_f32 v230, v198, v206\n"
      "v_fmac_f32 v231, v199, v207\n"
      "v_rcp_f32 v228, v228\n"
      "v_rcp_f32 v229, v229\n"
      "v_rcp_f32 v230, v230\n"
      "v_rcp_f32 v231, v231\n"
      "s_waitcnt vmcnt(2)\n"
      "v_lshlrev_b32 v64, 16, v96\n"
      "v_and_b32 v65, 0xffff0000, v96\n"
      "v_lshlrev_b32 v66, 16, v97\n"
      "v_and_b32 v67, 0xffff0000, v97\n"
      "v_lshlrev_b32 v68, 16, v98\n"
      "v_and_b32 v69, 0xffff0000, v98\n"
      "v_lshlrev_b32 v70, 16, v99\n"
      "v_and_b32 v71, 0xffff0000, v99\n"
      "v_lshlrev_b32 v80, 16, v104\n"
      "v_and_b32 v81, 0xffff0000, v104\n"
      "v_lshlrev_b32 v82, 16, v105\n"
      "v_and_b32 v83, 0xffff0000, v105\n"
      "v_lshlrev_b32 v84, 16, v106\n"
      "v_and_b32 v85, 0xffff0000, v106\n"
      "v_lshlrev_b32 v86, 16, v107\n"
      "v_and_b32 v87, 0xffff0000, v107\n"
      "v_pk_mul_f32 v[200:201], v[200:201], v[228:229]\n"
      "v_pk_mul_f32 v[204:205], v[204:205], v[228:229]\n"
      "v_pk_mul_f32 v[202:203], v[202:203], v[230:231]\n"
      "v_pk_mul_f32 v[206:207], v[206:207], v[230:231]\n"
      "v_pk_mul_f32 v[0:1], v[0:1], v[200:201] op_sel:[0,0] op_sel_hi:[1,0]\n"
      "v_pk_mul_f32 v[2:3], v[2:3], v[200:201] op_sel:[0,0] op_sel_hi:[1,0]\n"
      "v_pk_mul_f32 v[16:17], v[16:17], v[200:201] op_sel:[0,0] op_sel_hi:[1,0]\n"
      "v_pk_mul_f32 v[18:19], v[18:19], v[200:201] op_sel:[0,0] op_sel_hi:[1,0]\n"
      "v_pk_fma_f32 v[0:1], v[64:65], v[204:205], v[0:1] op_sel:[0,0,0] op_sel_hi:[1,0,1]\n"
      "v_pk_fma_f32 v[2:3], v[66:67], v[204:205], v[2:3] op_sel:[0,0,0] op_sel_hi:[1,0,1]\n"
      "v_pk_fma_f32 v[16:17], v[80:81], v[204:205], v[16:17] op_sel:[0,0,0] op_sel_hi:[1,0,1]\n"
      "v_pk_fma_f32 v[18:19], v[82:83], v[204:205], v[18:19] op_sel:[0,0,0] op_sel_hi:[1,0,1]\n"
      "v_cvt_pk_bf16_f32 v176, v0, v1\n"
      "v_cvt_pk_bf16_f32 v177, v2, v3\n"
      "v_cvt_pk_bf16_f32 v178, v16, v17\n"
      "v_cvt_pk_bf16_f32 v179, v18, v19\n"
      "v_add_u32 v240, 0, v136\n"
      "v_permlane16_swap_b32 v176, v178\n"
      "v_permlane16_swap_b32 v177, v179\n"
      "global_store_dwordx4 v240, v[176:179], %[outp] sc1\n"
      "v_pk_mul_f32 v[4:5], v[4:5], v[200:201] op_sel:[0,1] op_sel_hi:[1,1]\n"
      "v_pk_mul_f32 v[6:7], v[6:7], v[200:201] op_sel:[0,1] op_sel_hi:[1,1]\n"
      "v_pk_mul_f32 v[20:21], v[20:21], v[200:201] op_sel:[0,1] op_sel_hi:[1,1]\n"
      "v_pk_mul_f32 v[22:23], v[22:23], v[200:201] op_sel:[0,1] op_sel_hi:[1,1]\n"
      "v_pk_fma_f32 v[4:5], v[68:69], v[204:205], v[4:5] op_sel:[0,1,0] op_sel_hi:[1,1,1]\n"
      "v_pk_fma_f32 v[6:7], v[70:71], v[204:205], v[6:7] op_sel:[0,1,0] op_sel_hi:[1,1,1]\n"
      "v_pk_fma_f32 v[20:21], v[84:85], v[204:205], v[20:21] op_sel:[0,1,0] op_sel_hi:[1,1,1]\n"
      "v_pk_fma_f32 v[22:23], v[86:87], v[204:205], v[22:23] op_sel:[0,1,0] op_sel_hi:[1,1,1]\n"
      "v_cvt_pk_bf16_f32 v180, v4, v5\n"
      "v_cvt_pk_bf16_f32 v181, v6, v7\n"
      "v_cvt_pk_bf16_f32 v182, v20, v21\n"
      "v_cvt_pk_bf16_f32 v183, v22, v23\n"
      "v_add_u32 v241, 4096, v136\n"
      "v_permlane16_swap_b32 v180, v182\n"
      "v_permlane16_swap_b32 v181, v183\n"
      "global_store_dwordx4 v241, v[180:183], %[outp] sc1\n"
      "s_waitcnt vmcnt(2)\n"
      "v_lshlrev_b32 v72, 16, v100\n"
      "v_and_b32 v73, 0xffff0000, v100\n"
      "v_lshlrev_b32 v74, 16, v101\n"
      "v_and_b32 v75, 0xffff0000, v101\n"
      "v_lshlrev_b32 v76, 16, v102\n"
      "v_and_b32 v77, 0xffff0000, v102\n"
      "v_lshlrev_b32 v78, 16, v103\n"
      "v_and_b32 v79, 0xffff0000, v103\n"
      "v_lshlrev_b32 v88, 16, v108\n"
      "v_and_b32 v89, 0xffff0000, v108\n"
      "v_lshlrev_b32 v90, 16, v109\n"
      "v_and_b32 v91, 0xffff0000, v109\n"
      "v_lshlrev_b32 v92, 16, v110\n"
      "v_and_b32 v93, 0xffff0000, v110\n"
      "v_lshlrev_b32 v94, 16, v111\n"
      "v_and_b32 v95, 0xffff0000, v111\n"
      "v_pk_mul_f32 v[8:9], v[8:9], v[202:203] op_sel:[0,0] op_sel_hi:[1,0]\n"
      "v_pk_mul_f32 v[10:11], v[10:11], v[202:203] op_sel:[0,0] op_sel_hi:[1,0]\n"
      "v_pk_mul_f32 v[24:25], v[24:25], v[202:203] op_sel:[0,0] op_sel_hi:[1,0]\n"
      "v_pk_mul_f32 v[26:27], v[26:27], v[202:203] op_sel:[0,0] op_sel_hi:[1,0]\n"
      "v_pk_fma_f32 v[8:9], v[72:73], v[206:207], v[8:9] op_sel:[0,0,0] op_sel_hi:[1,0,1]\n"
      "v_pk_fma_f32 v[10:11], v[74:75], v[206:207], v[10:11] op_sel:[0,0,0] op_sel_hi:[1,0,1]\n"
      "v_pk_fma_f32 v[24:25], v[88:89], v[206:207], v[24:25] op_sel:[0,0,0] op_sel_hi:[1,0,1]\n"
      "v_pk_fma_f32 v[26:27], v[90:91], v[206:207], v[26:27] op_sel:[0,0,0] op_sel_hi:[1,0,1]\n"
      "v_cvt_pk_bf16_f32 v184, v8, v9\n"
      "v_cvt_pk_bf16_f32 v185, v10, v11\n"
      "v_cvt_pk_bf16_f32 v186, v24, v25\n"
      "v_cvt_pk_bf16_f32 v187, v26, v27\n"
      "v_add_u32 v240, 8192, v136\n"
      "v_permlane16_swap_b32 v184, v186\n"
      "v_permlane16_swap_b32 v185, v187\n"
      "global_store_dwordx4 v240, v[184:187], %[outp] sc1\n"
      "v_pk_mul_f32 v[12:13], v[12:13], v[202:203] op_sel:[0,1] op_sel_hi:[1,1]\n"
      "v_pk_mul_f32 v[14:15], v[14:15], v[202:203] op_sel:[0,1] op_sel_hi:[1,1]\n"
      "v_pk_mul_f32 v[28:29], v[28:29], v[202:203] op_sel:[0,1] op_sel_hi:[1,1]\n"
      "v_pk_mul_f32 v[30:31], v[30:31], v[202:203] op_sel:[0,1] op_sel_hi:[1,1]\n"
      "v_pk_fma_f32 v[12:13], v[76:77], v[206:207], v[12:13] op_sel:[0,1,0] op_sel_hi:[1,1,1]\n"
      "v_pk_fma_f32 v[14:15], v[78:79], v[206:207], v[14:15] op_sel:[0,1,0] op_sel_hi:[1,1,1]\n"
      "v_pk_fma_f32 v[28:29], v[92:93], v[206:207], v[28:29] op_sel:[0,1,0] op_sel_hi:[1,1,1]\n"
      "v_pk_fma_f32 v[30:31], v[94:95], v[206:207], v[30:31] op_sel:[0,1,0] op_sel_hi:[1,1,1]\n"
      "v_cvt_pk_bf16_f32 v188, v12, v13\n"
      "v_cvt_pk_bf16_f32 v189, v14, v15\n"
      "v_cvt_pk_bf16_f32 v190, v28, v29\n"
      "v_cvt_pk_bf16_f32 v191, v30, v31\n"
      "v_add_u32 v241, 12288, v136\n"
      "v_permlane16_swap_b32 v188, v190\n"
      "v_permlane16_swap_b32 v189, v191\n"
      "global_store_dwordx4 v241, v[188:191], %[outp] sc1\n"
      "s_mov_b64 exec, 1\n"
      "global_store_dwordx2 v138, v[128:129], %[line] sc1\n"
      "s_mov_b64 exec, -1\n"
      ".subsection 1\n"
      "GW0_%=:\n"
      "s_call_b64 s[84:85], WAIT_%=\n"
      "s_branch GX0_%=\n"
      "GW1_%=:\n"
      "s_call_b64 s[84:85], WAIT_%=\n"
      "s_branch GX1_%=\n"
      "GW2_%=:\n"
      "s_call_b64 s[84:85], WAIT_%=\n"
      "s_branch GX2_%=\n"
      "GW3_%=:\n"
      "s_call_b64 s[84:85], WAIT_%=\n"
      "s_branch GX3_%=\n"
      "GW4_%=:\n"
      "s_call_b64 s[84:85], WAIT_%=\n"
      "s_branch GX4_%=\n"
      "GW5_%=:\n"
      "s_call_b64 s[84:85], WAIT_%=\n"
      "s_branch GX5_%=\n"
      "WAIT_%=:\n"
      "s_cmp_eq_u32 s91, 0\n"
      "s_cbranch_scc1 SPIN_%=\n"
      "s_cmp_le_u32 s87, 12\n"
      "s_cselect_b32 s70, 300, s70\n"
      "s_cmp_gt_u32 s87, 2\n"
      "s_cbranch_scc1 H3_%=\n"
      "s_cmp_lg_u32 s87, 1\n"
      "s_cbranch_scc1 H2_%=\n"
      "buffer_load_dword v250, v251, %[srdr], 0 offen sc1\n"
      "s_branch HR_%=\n"
      "H2_%=:\n"
      "s_cmp_lg_u32 s87, 2\n"
      "s_cbranch_scc1 HR_%=\n"
      "s_waitcnt vmcnt(8)\n"
      "v_subrev_u32 v250, s71, v250\n"
      "v_min_u32 v250, 0x258, v250\n"
      "s_mov_b32 s66, 0\n"
      "v_readlane_b32 s67, v250, 0\n"
      "s_nop 0\n"
      "s_add_u32 s66, s66, s67\n"
      "v_readlane_b32 s67, v250, 1\n"
      "s_nop 0\n"
      "s_add_u32 s66, s66, s67\n"
      "v_readlane_b32 s67, v250, 2\n"
      "s_nop 0\n"
      "s_add_u32 s66, s66, s67\n"
      "v_readlane_b32 s67, v250, 3\n"
      "s_nop 0\n"
      "s_add_u32 s66, s66, s67\n"
      "v_readlane_b32 s67, v250, 4\n"
      "s_nop 0\n"
      "s_add_u32 s66, s66, s67\n"
      "v_readlane_b32 s67, v250, 5\n"
      "s_nop 0\n"
      "s_add_u32 s66, s66, s67\n"
      "v_readlane_b32 s67, v250, 6\n"
      "s_nop 0\n"
      "s_add_u32 s66, s66, s67\n"
      "v_readlane_b32 s67, v250, 7\n"
      "s_nop 0\n"
      "s_add_u32 s66, s66, s67\n"
      "s_lshr_b32 s66, s66, 3\n"
      "s_add_u32 s66, s66, s71\n"
      "s_add_u32 s66, s66, %[cph]\n"
      "s_sub_u32 s67, s65, s66\n"
      "s_cmp_lt_i32 s67, 0\n"
      "s_cselect_b32 s88, s66, s65\n"
      "s_branch H5_%=\n"
      "H3_%=:\n"
      "s_bitcmp1_b32 0x8880, s87\n"
      "s_cbranch_scc0 H4_%=\n"
      "v_mbcnt_lo_u32_b32 v251, -1, 0\n"
      "v_mbcnt_hi_u32_b32 v251, -1, v251\n"
      "v_and_b32 v251, 31, v251\n"
      "v_lshlrev_b32 v251, 6, v251\n"
      "v_add_u32 v251, %[vrd], v251\n"
      "buffer_load_dword v250, v251, %[srdr], 0 offen sc1\n"
      "s_branch HR_%=\n"
      "H4_%=:\n"
      "s_bitcmp1_b32 0x11100, s87\n"
      "s_cbranch_scc0 H5_%=\n"
      "s_waitcnt vmcnt(8)\n"
      "v_sub_u32 v250, s68, v250\n"
      "v_cmp_gt_u32 vcc, 1600, v250\n"
      "s_nop 3\n"
      "s_bcnt1_i32_b32 s67, vcc_lo\n"
      "s_cmp_lt_u32 s67, 13\n"
      "s_cbranch_scc1 H5_%=\n"
      "s_mov_b32 s87, 30\n"
      "s_add_u32 s66, s66, 70\n"
      "s_mov_b32 s86, 113\n"
      "s_mov_b64 s[80:81], 0x2a9f4\n"
      "s_sub_u32 s67, s65, s66\n"
      "s_cmp_lt_i32 s67, 0\n"
      "s_cselect_b32 s88, s66, s65\n"
      "s_branch HR_%=\n"
      "H5_%=:\n"
      "s_cmp_lt_u32 %[rep], 2048\n"
      "s_cbranch_scc0 HR_%=\n"
      "s_bitcmp1_b32 0x3330, s87\n"
      "s_cbranch_scc0 HR_%=\n"
      "s_memrealtime s[68:69]\n"
      "s_waitcnt lgkmcnt(0)\n"
      "s_sub_u32 s76, s68, s82\n"
      "s_add_u32 s76, s76, 12\n"
      "s_max_i32 s76, s76, 102\n"
      "s_waitcnt vmcnt(4)\n"
      "s_memrealtime s[68:69]\n"
      "s_waitcnt lgkmcnt(0)\n"
      "s_sub_u32 s67, s68, s82\n"
      "s_sub_u32 s67, s67, s76\n"
      "s_max_i32 s67, s67, 0\n"
      "s_min_i32 s67, s67, 12\n"
      "s_add_u32 s89, s89, s67\n"
      "s_bitcmp1_b32 0x2220, s87\n"
      "s_cbranch_scc0 HR_%=\n"
      "s_cmp_lt_u32 s89, 14\n"
      "s_mov_b32 s89, 0\n"
      "s_cbranch_scc1 HR_%=\n"
      "v_mbcnt_lo_u32_b32 v251, -1, 0\n"
      "v_mbcnt_hi_u32_b32 v251, -1, v251\n"
      "v_lshlrev_b32 v250, 7, v251\n"
      "v_and_b32 v250, 0x1800, v250\n"
      "v_and_b32 v251, 15, v251\n"
      "v_lshl_or_b32 v251, v251, 2, v250\n"
      "v_mov_b32 v250, s68\n"
      "s_add_u32 s67, %[rep], 8192\n"
      "buffer_store_dword v250, v251, %[srdr], s67 offen sc1\n"
      "HR_%=:\n"
      "s_add_u32 s87, s87, 1\n"
      "s_cmp_gt_u32 s87, 16\n"
      "s_cselect_b32 s91, 0, s91\n"
      "SPIN_%=:\n"
      "s_sub_u32 s67, s68, s88\n"
      "s_cmp_ge_i32 s67, 0\n"
      "s_cbranch_scc1 GO_%=\n"
      "s_sleep 1\n"
      "s_memrealtime s[68:69]\n"
      "s_waitcnt lgkmcnt(0)\n"
      "s_branch SPIN_%=\n"
      "GO_%=:\n"
      "s_setpc_b64 s[84:85]\n"
      "FB1_%=:\n"
      "s_cmp_eq_u32 s72, 0\n"
      "s_cbranch_scc1 FULL1_%=\n"
      "s_mov_b32 s72, 0\n"
      "s_nop 7\n"
      "v_max3_f32 v236, v128, v129, v130\n"
      "v_max3_f32 v237, v136, v137, v138\n"
      "v_max3_f32 v238, v144, v145, v146\n"
      "v_max3_f32 v239, v152, v153, v154\n"
      "v_max3_f32 v236, v236, v131, v132\n"
      "v_max3_f32 v237, v237, v139, v140\n"
      "v_max3_f32 v238, v238, v147, v148\n"
      "v_max3_f32 v239, v239, v155, v156\n"
      "v_max3_f32 v236, v236, v133, v134\n"
      "v_max3_f32 v237, v237, v141, v142\n"
      "v_max3_f32 v238, v238, v149, v150\n"
      "v_max3_f32 v239, v239, v157, v158\n"
      "v_max_f32 v236, v236, v135\n"
      "v_max_f32 v237, v237, v143\n"
      "v_max_f32 v238, v238, v151\n"
      "v_max_f32 v239, v239, v159\n"
      "v_mov_b32 v192, v236\n"
      "v_mov_b32 v193, v237\n"
      "v_mov_b32 v194, v238\n"
      "v_mov_b32 v195, v239\n"
      "v_permlane16_swap_b32 v236, v192\n"
      "v_permlane16_swap_b32 v237, v193\n"
      "v_permlane16_swap_b32 v238, v194\n"
      "v_permlane16_swap_b32 v239, v195\n"
      "v_max_f32 v236, v236, v192\n"
      "v_max_f32 v237, v237, v193\n"
      "v_max_f32 v238, v238, v194\n"
      "v_max_f32 v239, v239, v195\n"
      "v_mov_b32 v192, v236\n"
      "v_mov_b32 v193, v237\n"
      "v_mov_b32 v194, v238\n"
      "v_mov_b32 v195, v239\n"
      "v_permlane32_swap_b32 v236, v192\n"
      "v_permlane32_swap_b32 v237, v193\n"
      "v_permlane32_swap_b32 v238, v194\n"
      "v_permlane32_swap_b32 v239, v195\n"
      "v_max_f32 v236, v236, v192\n"
      "v_max_f32 v237, v237, v193\n"
      "v_max_f32 v238, v238, v194\n"
      "v_max_f32 v239, v239, v195\n"
      "v_mul_f32 v224, 0xbe0293ee, v236\n"
      "v_mul_f32 v225, 0xbe0293ee, v237\n"
      "v_mul_f32 v226, 0xbe0293ee, v238\n"
      "v_mul_f32 v227, 0xbe0293ee, v239\n"
      "v_mov_b64 v[208:209], 0\n"
      "v_mov_b64 v[210:211], 0\n"
      "v_mov_b64 v[212:213], 0\n"
      "v_mov_b64 v[214:215], 0\n"
      "v_mov_b64 v[216:217], 0\n"
      "v_mov_b64 v[218:219], 0\n"
      "v_mov_b64 v[220:221], 0\n"
      "v_mov_b64 v[222:223], 0\n"
      "v_mov_b64 v[232:233], 0\n"
      "v_mov_b64 v[234:235], 0\n"
      "s_nop 1\n"
      "s_branch RET1_0_%=\n"
      "FB0_%=:\n"
      "s_nop 7\n"
      "s_nop 7\n"
      "v_max3_f32 v236, v128, v129, v130\n"
      "v_max3_f32 v237, v136, v137, v138\n"
      "v_max3_f32 v238, v144, v145, v146\n"
      "v_max3_f32 v239, v152, v153, v154\n"
      "v_max3_f32 v236, v236, v131, v132\n"
      "v_max3_f32 v237, v237, v139, v140\n"
      "v_max3_f32 v238, v238, v147, v148\n"
      "v_max3_f32 v239, v239, v155, v156\n"
      "v_max3_f32 v236, v236, v133, v134\n"
      "v_max3_f32 v237, v237, v141, v142\n"
      "v_max3_f32 v238, v238, v149, v150\n"
      "v_max3_f32 v239, v239, v157, v158\n"
      "v_max_f32 v236, v236, v135\n"
      "v_max_f32 v237, v237, v143\n"
      "v_max_f32 v238, v238, v151\n"
      "v_max_f32 v239, v239, v159\n"
      "v_mov_b32 v192, v236\n"
      "v_mov_b32 v193, v237\n"
      "v_mov_b32 v194, v238\n"
      "v_mov_b32 v195, v239\n"
      "v_permlane16_swap_b32 v236, v192\n"
      "v_permlane16_swap_b32 v237, v193\n"
      "v_permlane16_swap_b32 v238, v194\n"
      "v_permlane16_swap_b32 v239, v195\n"
      "v_max_f32 v236, v236, v192\n"
      "v_max_f32 v237, v237, v193\n"
      "v_max_f32 v238, v238, v194\n"
      "v_max_f32 v239, v239, v195\n"
      "v_mov_b32 v192, v236\n"
      "v_mov_b32 v193, v237\n"
      "v_mov_b32 v194, v238\n"
      "v_mov_b32 v195, v239\n"
      "v_permlane32_swap_b32 v236, v192\n"
      "v_permlane32_swap_b32 v237, v193\n"
      "v_permlane32_swap_b32 v238, v194\n"
      "v_permlane32_swap_b32 v239, v195\n"
      "v_max_f32 v236, v236, v192\n"
      "v_max_f32 v237, v237, v193\n"
      "v_max_f32 v238, v238, v194\n"
      "v_max_f32 v239, v239, v195\n"
      "v_mul_f32 v236, 0xbe0293ee, v236\n"
      "v_mul_f32 v237, 0xbe0293ee, v237\n"
      "v_mul_f32 v238, 0xbe0293ee, v238\n"
      "v_mul_f32 v239, 0xbe0293ee, v239\n"
      "v_min_f32 v236, v236, v224\n"
      "v_min_f32 v237, v237, v225\n"
      "v_min_f32 v238, v238, v226\n"
      "v_min_f32 v239, v239, v227\n"
      "v_sub_f32 v232, v236, v224\n"
      "v_sub_f32 v233, v237, v225\n"
      "v_sub_f32 v234, v238, v226\n"
      "v_sub_f32 v235, v239, v227\n"
      "v_exp_f32 v232, v232\n"
      "v_exp_f32 v233, v233\n"
      "v_exp_f32 v234, v234\n"
      "v_exp_f32 v235, v235\n"
      "v_mov_b32 v224, v236\n"
      "v_mov_b32 v225, v237\n"
      "v_mov_b32 v226, v238\n"
      "v_mov_b32 v227, v239\n"
      "v_mul_f32 v0, v0, v232\n"
      "v_mul_f32 v1, v1, v232\n"
      "v_mul_f32 v2, v2, v232\n"
      "v_mul_f32 v3, v3, v232\n"
      "v_mul_f32 v4, v4, v233\n"
      "v_mul_f32 v5, v5, v233\n"
      "v_mul_f32 v6, v6, v233\n"
      "v_mul_f32 v7, v7, v233\n"
      "v_mul_f32 v8, v8, v234\n"
      "v_mul_f32 v9, v9, v234\n"
      "v_mul_f32 v10, v10, v234\n"
      "v_mul_f32 v11, v11, v234\n"
      "v_mul_f32 v12, v12, v235\n"
      "v_mul_f32 v13, v13, v235\n"
      "v_mul_f32 v14, v14, v235\n"
      "v_mul_f32 v15, v15, v235\n"
      "v_mul_f32 v16, v16, v232\n"
      "v_mul_f32 v17, v17, v232\n"
      "v_mul_f32 v18, v18, v232\n"
      "v_mul_f32 v19, v19, v232\n"
      "v_mul_f32 v20, v20, v233\n"
      "v_mul_f32 v21, v21, v233\n"
      "v_mul_f32 v22, v22, v233\n"
      "v_mul_f32 v23, v23, v233\n"
      "v_mul_f32 v24, v24, v234\n"
      "v_mul_f32 v25, v25, v234\n"
      "v_mul_f32 v26, v26, v234\n"
      "v_mul_f32 v27, v27, v234\n"
      "v_mul_f32 v28, v28, v235\n"
      "v_mul_f32 v29, v29, v235\n"
      "v_mul_f32 v30, v30, v235\n"
      "v_mul_f32 v31, v31, v235\n"
      "v_mul_f32 v32, v32, v232\n"
      "v_mul_f32 v33, v33, v232\n"
      "v_mul_f32 v34, v34, v232\n"
      "v_mul_f32 v35, v35, v232\n"
      "v_mul_f32 v36, v36, v233\n"
      "v_mul_f32 v37, v37, v233\n"
      "v_mul_f32 v38, v38, v233\n"
      "v_mul_f32 v39, v39, v233\n"
      "v_mul_f32 v40, v40, v234\n"
      "v_mul_f32 v41, v41, v234\n"
      "v_mul_f32 v42, v42, v234\n"
      "v_mul_f32 v43, v43, v234\n"
      "v_mul_f32 v44, v44, v235\n"
      "v_mul_f32 v45, v45, v235\n"
      "v_mul_f32 v46, v46, v235\n"
      "v_mul_f32 v47, v47, v235\n"
      "v_mul_f32 v48, v48, v232\n"
      "v_mul_f32 v49, v49, v232\n"
      "v_mul_f32 v50, v50, v232\n"
      "v_mul_f32 v51, v51, v232\n"
      "v_mul_f32 v52, v52, v233\n"
      "v_mul_f32 v53, v53, v233\n"
      "v_mul_f32 v54, v54, v233\n"
      "v_mul_f32 v55, v55, v233\n"
      "v_mul_f32 v56, v56, v234\n"
      "v_mul_f32 v57, v57, v234\n"
      "v_mul_f32 v58, v58, v234\n"
      "v_mul_f32 v59, v59, v234\n"
      "v_mul_f32 v60, v60, v235\n"
      "v_mul_f32 v61, v61, v235\n"
      "v_mul_f32 v62, v62, v235\n"
      "v_mul_f32 v63, v63, v235\n"
      "v_mul_f32 v64, v64, v232\n"
      "v_mul_f32 v65, v65, v232\n"
      "v_mul_f32 v66, v66, v232\n"
      "v_mul_f32 v67, v67, v232\n"
      "v_mul_f32 v68, v68, v233\n"
      "v_mul_f32 v69, v69, v233\n"
      "v_mul_f32 v70, v70, v233\n"
      "v_mul_f32 v71, v71, v233\n"
      "v_mul_f32 v72, v72, v234\n"
      "v_mul_f32 v73, v73, v234\n"
      "v_mul_f32 v74, v74, v234\n"
      "v_mul_f32 v75, v75, v234\n"
      "v_mul_f32 v76, v76, v235\n"
      "v_mul_f32 v77, v77, v235\n"
      "v_mul_f32 v78, v78, v235\n"
      "v_mul_f32 v79, v79, v235\n"
      "v_mul_f32 v80, v80, v232\n"
      "v_mul_f32 v81, v81, v232\n"
      "v_mul_f32 v82, v82, v232\n"
      "v_mul_f32 v83, v83, v232\n"
      "v_mul_f32 v84, v84, v233\n"
      "v_mul_f32 v85, v85, v233\n"
      "v_mul_f32 v86, v86, v233\n"
      "v_mul_f32 v87, v87, v233\n"
      "v_mul_f32 v88, v88, v234\n"
      "v_mul_f32 v89, v89, v234\n"
      "v_mul_f32 v90, v90, v234\n"
      "v_mul_f32 v91, v91, v234\n"
      "v_mul_f32 v92, v92, v235\n"
      "v_mul_f32 v93, v93, v235\n"
      "v_mul_f32 v94, v94, v235\n"
      "v_mul_f32 v95, v95, v235\n"
      "v_mul_f32 v96, v96, v232\n"
      "v_mul_f32 v97, v97, v232\n"
      "v_mul_f32 v98, v98, v232\n"
      "v_mul_f32 v99, v99, v232\n"
      "v_mul_f32 v100, v100, v233\n"
      "v_mul_f32 v101, v101, v233\n"
      "v_mul_f32 v102, v102, v233\n"
      "v_mul_f32 v103, v103, v233\n"
      "v_mul_f32 v104, v104, v234\n"
      "v_mul_f32 v105, v105, v234\n"
      "v_mul_f32 v106, v106, v234\n"
      "v_mul_f32 v107, v107, v234\n"
      "v_mul_f32 v108, v108, v235\n"
      "v_mul_f32 v109, v109, v235\n"
      "v_mul_f32 v110, v110, v235\n"
      "v_mul_f32 v111, v111, v235\n"
      "v_mul_f32 v112, v112, v232\n"
      "v_mul_f32 v113, v113, v232\n"
      "v_mul_f32 v114, v114, v232\n"
      "v_mul_f32 v115, v115, v232\n"
      "v_mul_f32 v116, v116, v233\n"
      "v_mul_f32 v117, v117, v233\n"
      "v_mul_f32 v118, v118, v233\n"
      "v_mul_f32 v119, v119, v233\n"
      "v_mul_f32 v120, v120, v234\n"
      "v_mul_f32 v121, v121, v234\n"
      "v_mul_f32 v122, v122, v234\n"
      "v_mul_f32 v123, v123, v234\n"
      "v_mul_f32 v124, v124, v235\n"
      "v_mul_f32 v125, v125, v235\n"
      "v_mul_f32 v126, v126, v235\n"
      "v_mul_f32 v127, v127, v235\n"
      "v_mul_f32 v228, v228, v232\n"
      "v_mul_f32 v229, v229, v233\n"
      "v_mul_f32 v230, v230, v234\n"
      "v_mul_f32 v231, v231, v235\n"
      "v_fmamk_f32 v240, v128, 0x3e0293ee, v224\n"
      "v_exp_f32 v236, v240\n"
      "v_fmamk_f32 v241, v129, 0x3e0293ee, v224\n"
      "v_exp_f32 v237, v241\n"
      "v_fmamk_f32 v240, v130, 0x3e0293ee, v224\n"
      "v_exp_f32 v238, v240\n"
      "v_fmamk_f32 v241, v131, 0x3e0293ee, v224\n"
      "v_add_f32 v232, v236, v237\n"
      "v_cvt_pk_bf16_f32 v192, v236, v237\n"
      "v_exp_f32 v239, v241\n"
      "v_fmamk_f32 v240, v136, 0x3e0293ee, v225\n"
      "v_add_f32 v232, v232, v238\n"
      "v_exp_f32 v236, v240\n"
      "v_fmamk_f32 v241, v137, 0x3e0293ee, v225\n"
      "v_add_f32 v232, v232, v239\n"
      "v_cvt_pk_bf16_f32 v193, v238, v239\n"
      "v_exp_f32 v237, v241\n"
      "v_fmamk_f32 v240, v138, 0x3e0293ee, v225\n"
      "v_exp_f32 v238, v240\n"
      "v_fmamk_f32 v241, v139, 0x3e0293ee, v225\n"
      "v_add_f32 v233, v236, v237\n"
      "v_cvt_pk_bf16_f32 v196, v236, v237\n"
      "v_exp_f32 v239, v241\n"
      "v_fmamk_f32 v240, v144, 0x3e0293ee, v226\n"
      "v_add_f32 v233, v233, v238\n"
      "v_exp_f32 v236, v240\n"
      "v_fmamk_f32 v241, v145, 0x3e0293ee, v226\n"
      "v_add_f32 v233, v233, v239\n"
      "v_cvt_pk_bf16_f32 v197, v238, v239\n"
      "v_exp_f32 v237, v241\n"
      "v_fmamk_f32 v240, v146, 0x3e0293ee, v226\n"
      "v_exp_f32 v238, v240\n"
      "v_fmamk_f32 v241, v147, 0x3e0293ee, v226\n"
      "v_add_f32 v234, v236, v237\n"
      "v_cvt_pk_bf16_f32 v200, v236, v237\n"
      "v_exp_f32 v239, v241\n"
      "v_fmamk_f32 v240, v152, 0x3e0293ee, v227\n"
      "v_add_f32 v234, v234, v238\n"
      "v_exp_f32 v236, v240\n"
      "v_fmamk_f32 v241, v153, 0x3e0293ee, v227\n"
      "v_add_f32 v234, v234, v239\n"
      "v_cvt_pk_bf16_f32 v201, v238, v239\n"
      "v_exp_f32 v237, v241\n"
      "v_fmamk_f32 v240, v154, 0x3e0293ee, v227\n"
      "v_exp_f32 v238, v240\n"
      "v_fmamk_f32 v241, v155, 0x3e0293ee, v227\n"
      "v_add_f32 v235, v236, v237\n"
      "v_cvt_pk_bf16_f32 v204, v236, v237\n"
      "v_exp_f32 v239, v241\n"
      "v_fmamk_f32 v240, v132, 0x3e0293ee, v224\n"
      "v_add_f32 v235, v235, v238\n"
      "v_exp_f32 v236, v240\n"
      "v_fmamk_f32 v241, v133, 0x3e0293ee, v224\n"
      "v_add_f32 v235, v235, v239\n"
      "v_cvt_pk_bf16_f32 v205, v238, v239\n"
      "v_exp_f32 v237, v241\n"
      "v_fmamk_f32 v240, v134, 0x3e0293ee, v224\n"
      "v_add_f32 v232, v232, v236\n"
      "v_exp_f32 v238, v240\n"
      "v_fmamk_f32 v241, v135, 0x3e0293ee, v224\n"
      "v_add_f32 v232, v232, v237\n"
      "v_cvt_pk_bf16_f32 v194, v236, v237\n"
      "v_exp_f32 v239, v241\n"
      "v_fmamk_f32 v240, v140, 0x3e0293ee, v225\n"
      "v_add_f32 v232, v232, v238\n"
      "v_exp_f32 v236, v240\n"
      "v_fmamk_f32 v241, v141, 0x3e0293ee, v225\n"
      "v_add_f32 v232, v232, v239\n"
      "v_cvt_pk_bf16_f32 v195, v238, v239\n"
      "v_exp_f32 v237, v241\n"
      "v_fmamk_f32 v240, v142, 0x3e0293ee, v225\n"
      "v_add_f32 v233, v233, v236\n"
      "v_exp_f32 v238, v240\n"
      "v_fmamk_f32 v241, v143, 0x3e0293ee, v225\n"
      "v_add_f32 v233, v233, v237\n"
      "v_cvt_pk_bf16_f32 v198, v236, v237\n"
      "v_exp_f32 v239, v241\n"
      "v_fmamk_f32 v240, v148, 0x3e0293ee, v226\n"
      "v_add_f32 v233, v233, v238\n"
      "v_exp_f32 v236, v240\n"
      "v_fmamk_f32 v241, v149, 0x3e0293ee, v226\n"
      "v_add_f32 v233, v233, v239\n"
      "v_cvt_pk_bf16_f32 v199, v238, v239\n"
      "v_exp_f32 v237, v241\n"
      "v_fmamk_f32 v240, v150, 0x3e0293ee, v226\n"
      "v_add_f32 v234, v234, v236\n"
      "v_exp_f32 v238, v240\n"
      "v_fmamk_f32 v241, v151, 0x3e0293ee, v226\n"
      "v_add_f32 v234, v234, v237\n"
      "v_cvt_pk_bf16_f32 v202, v236, v237\n"
      "v_exp_f32 v239, v241\n"
      "v_fmamk_f32 v240, v156, 0x3e0293ee, v227\n"
      "v_add_f32 v234, v234, v238\n"
      "v_exp_f32 v236, v240\n"
      "v_fmamk_f32 v241, v157, 0x3e0293ee, v227\n"
      "v_add_f32 v234, v234, v239\n"
      "v_cvt_pk_bf16_f32 v203, v238, v239\n"
      "v_exp_f32 v237, v241\n"
      "v_fmamk_f32 v240, v158, 0x3e0293ee, v227\n"
      "v_add_f32 v235, v235, v236\n"
      "v_exp_f32 v238, v240\n"
      "v_fmamk_f32 v241, v159, 0x3e0293ee, v227\n"
      "v_add_f32 v235, v235, v237\n"
      "v_cvt_pk_bf16_f32 v206, v236, v237\n"
      "v_exp_f32 v239, v241\n"
      "v_add_f32 v235, v235, v238\n"
      "v_add_f32 v235, v235, v239\n"
      "v_cvt_pk_bf16_f32 v207, v238, v239\n"
      "s_nop 1\n"
      "s_cmp_eq_u32 s64, 0\n"
      "s_cbranch_scc1 RET0_0_%=\n"
      "s_cmp_eq_u32 s64, 1\n"
      "s_cbranch_scc1 RET0_1_%=\n"
      "s_cmp_eq_u32 s64, 2\n"
      "s_cbranch_scc1 RET0_2_%=\n"
      "s_branch RET0_3_%=\n"
      "FULL1_%=:\n"
      "s_nop 7\n"
      "s_nop 7\n"
      "v_max3_f32 v236, v160, v161, v162\n"
      "v_max3_f32 v237, v168, v169, v170\n"
      "v_max3_f32 v238, v176, v177, v178\n"
      "v_max3_f32 v239, v184, v185, v186\n"
      "v_max3_f32 v236, v236, v163, v164\n"
      "v_max3_f32 v237, v237, v171, v172\n"
      "v_max3_f32 v238, v238, v179, v180\n"
      "v_max3_f32 v239, v239, v187, v188\n"
      "v_max3_f32 v236, v236, v165, v166\n"
      "v_max3_f32 v237, v237, v173, v174\n"
      "v_max3_f32 v238, v238, v181, v182\n"
      "v_max3_f32 v239, v239, v189, v190\n"
      "v_max_f32 v236, v236, v167\n"
      "v_max_f32 v237, v237, v175\n"
      "v_max_f32 v238, v238, v183\n"
      "v_max_f32 v239, v239, v191\n"
      "v_mov_b32 v208, v236\n"
      "v_mov_b32 v209, v237\n"
      "v_mov_b32 v210, v238\n"
      "v_mov_b32 v211, v239\n"
      "v_permlane16_swap_b32 v236, v208\n"
      "v_permlane16_swap_b32 v237, v209\n"
      "v_permlane16_swap_b32 v238, v210\n"
      "v_permlane16_swap_b32 v239, v211\n"
      "v_max_f32 v236, v236, v208\n"
      "v_max_f32 v237, v237, v209\n"
      "v_max_f32 v238, v238, v210\n"
      "v_max_f32 v239, v239, v211\n"
      "v_mov_b32 v208, v236\n"
      "v_mov_b32 v209, v237\n"
      "v_mov_b32 v210, v238\n"
      "v_mov_b32 v211, v239\n"
      "v_permlane32_swap_b32 v236, v208\n"
      "v_permlane32_swap_b32 v237, v209\n"
      "v_permlane32_swap_b32 v238, v210\n"
      "v_permlane32_swap_b32 v239, v211\n"
      "v_max_f32 v236, v236, v208\n"
      "v_max_f32 v237, v237, v209\n"
      "v_max_f32 v238, v238, v210\n"
      "v_max_f32 v239, v239, v211\n"
      "v_mul_f32 v236, 0xbe0293ee, v236\n"
      "v_mul_f32 v237, 0xbe0293ee, v237\n"
      "v_mul_f32 v238, 0xbe0293ee, v238\n"
      "v_mul_f32 v239, 0xbe0293ee, v239\n"
      "v_min_f32 v236, v236, v224\n"
      "v_min_f32 v237, v237, v225\n"
      "v_min_f32 v238, v238, v226\n"
      "v_min_f32 v239, v239, v227\n"
      "v_sub_f32 v232, v236, v224\n"
      "v_sub_f32 v233, v237, v225\n"
      "v_sub_f32 v234, v238, v226\n"
      "v_sub_f32 v235, v239, v227\n"
      "v_exp_f32 v232, v232\n"
      "v_exp_f32 v233, v233\n"
      "v_exp_f32 v234, v234\n"
      "v_exp_f32 v235, v235\n"
      "v_mov_b32 v224, v236\n"
      "v_mov_b32 v225, v237\n"
      "v_mov_b32 v226, v238\n"
      "v_mov_b32 v227, v239\n"
      "v_mul_f32 v0, v0, v232\n"
      "v_mul_f32 v1, v1, v232\n"
      "v_mul_f32 v2, v2, v232\n"
      "v_mul_f32 v3, v3, v232\n"
      "v_mul_f32 v4, v4, v233\n"
      "v_mul_f32 v5, v5, v233\n"
      "v_mul_f32 v6, v6, v233\n"
      "v_mul_f32 v7, v7, v233\n"
      "v_mul_f32 v8, v8, v234\n"
      "v_mul_f32 v9, v9, v234\n"
      "v_mul_f32 v10, v10, v234\n"
      "v_mul_f32 v11, v11, v234\n"
      "v_mul_f32 v12, v12, v235\n"
      "v_mul_f32 v13, v13, v235\n"
      "v_mul_f32 v14, v14, v235\n"
      "v_mul_f32 v15, v15, v235\n"
      "v_mul_f32 v16, v16, v232\n"
      "v_mul_f32 v17, v17, v232\n"
      "v_mul_f32 v18, v18, v232\n"
      "v_mul_f32 v19, v19, v232\n"
      "v_mul_f32 v20, v20, v233\n"
      "v_mul_f32 v21, v21, v233\n"
      "v_mul_f32 v22, v22, v233\n"
      "v_mul_f32 v23, v23, v233\n"
      "v_mul_f32 v24, v24, v234\n"
      "v_mul_f32 v25, v25, v234\n"
      "v_mul_f32 v26, v26, v234\n"
      "v_mul_f32 v27, v27, v234\n"
      "v_mul_f32 v28, v28, v235\n"
      "v_mul_f32 v29, v29, v235\n"
      "v_mul_f32 v30, v30, v235\n"
      "v_mul_f32 v31, v31, v235\n"
      "v_mul_f32 v32, v32, v232\n"
      "v_mul_f32 v33, v33, v232\n"
      "v_mul_f32 v34, v34, v232\n"
      "v_mul_f32 v35, v35, v232\n"
      "v_mul_f32 v36, v36, v233\n"
      "v_mul_f32 v37, v37, v233\n"
      "v_mul_f32 v38, v38, v233\n"
      "v_mul_f32 v39, v39, v233\n"
      "v_mul_f32 v40, v40, v234\n"
      "v_mul_f32 v41, v41, v234\n"
      "v_mul_f32 v42, v42, v234\n"
      "v_mul_f32 v43, v43, v234\n"
      "v_mul_f32 v44, v44, v235\n"
      "v_mul_f32 v45, v45, v235\n"
      "v_mul_f32 v46, v46, v235\n"
      "v_mul_f32 v47, v47, v235\n"
      "v_mul_f32 v48, v48, v232\n"
      "v_mul_f32 v49, v49, v232\n"
      "v_mul_f32 v50, v50, v232\n"
      "v_mul_f32 v51, v51, v232\n"
      "v_mul_f32 v52, v52, v233\n"
      "v_mul_f32 v53, v53, v233\n"
      "v_mul_f32 v54, v54, v233\n"
      "v_mul_f32 v55, v55, v233\n"
      "v_mul_f32 v56, v56, v234\n"
      "v_mul_f32 v57, v57, v234\n"
      "v_mul_f32 v58, v58, v234\n"
      "v_mul_f32 v59, v59, v234\n"
      "v_mul_f32 v60, v60, v235\n"
      "v_mul_f32 v61, v61, v235\n"
      "v_mul_f32 v62, v62, v235\n"
      "v_mul_f32 v63, v63, v235\n"
      "v_mul_f32 v64, v64, v232\n"
      "v_mul_f32 v65, v65, v232\n"
      "v_mul_f32 v66, v66, v232\n"
      "v_mul_f32 v67, v67, v232\n"
      "v_mul_f32 v68, v68, v233\n"
      "v_mul_f32 v69, v69, v233\n"
      "v_mul_f32 v70, v70, v233\n"
      "v_mul_f32 v71, v71, v233\n"
      "v_mul_f32 v72, v72, v234\n"
      "v_mul_f32 v73, v73, v234\n"
      "v_mul_f32 v74, v74, v234\n"
      "v_mul_f32 v75, v75, v234\n"
      "v_mul_f32 v76, v76, v235\n"
      "v_mul_f32 v77, v77, v235\n"
      "v_mul_f32 v78, v78, v235\n"
      "v_mul_f32 v79, v79, v235\n"
      "v_mul_f32 v80, v80, v232\n"
      "v_mul_f32 v81, v81, v232\n"
      "v_mul_f32 v82, v82, v232\n"
      "v_mul_f32 v83, v83, v232\n"
      "v_mul_f32 v84, v84, v233\n"
      "v_mul_f32 v85, v85, v233\n"
      "v_mul_f32 v86, v86, v233\n"
      "v_mul_f32 v87, v87, v233\n"
      "v_mul_f32 v88, v88, v234\n"
      "v_mul_f32 v89, v89, v234\n"
      "v_mul_f32 v90, v90, v234\n"
      "v_mul_f32 v91, v91, v234\n"
      "v_mul_f32 v92, v92, v235\n"
      "v_mul_f32 v93, v93, v235\n"
      "v_mul_f32 v94, v94, v235\n"
      "v_mul_f32 v95, v95, v235\n"
      "v_mul_f32 v96, v96, v232\n"
      "v_mul_f32 v97, v97, v232\n"
      "v_mul_f32 v98, v98, v232\n"
      "v_mul_f32 v99, v99, v232\n"
      "v_mul_f32 v100, v100, v233\n"
      "v_mul_f32 v101, v101, v233\n"
      "v_mul_f32 v102, v102, v233\n"
      "v_mul_f32 v103, v103, v233\n"
      "v_mul_f32 v104, v104, v234\n"
      "v_mul_f32 v105, v105, v234\n"
      "v_mul_f32 v106, v106, v234\n"
      "v_mul_f32 v107, v107, v234\n"
      "v_mul_f32 v108, v108, v235\n"
      "v_mul_f32 v109, v109, v235\n"
      "v_mul_f32 v110, v110, v235\n"
      "v_mul_f32 v111, v111, v235\n"
      "v_mul_f32 v112, v112, v232\n"
      "v_mul_f32 v113, v113, v232\n"
      "v_mul_f32 v114, v114, v232\n"
      "v_mul_f32 v115, v115, v232\n"
      "v_mul_f32 v116, v116, v233\n"
      "v_mul_f32 v117, v117, v233\n"
      "v_mul_f32 v118, v118, v233\n"
      "v_mul_f32 v119, v119, v233\n"
      "v_mul_f32 v120, v120, v234\n"
      "v_mul_f32 v121, v121, v234\n"
      "v_mul_f32 v122, v122, v234\n"
      "v_mul_f32 v123, v123, v234\n"
      "v_mul_f32 v124, v124, v235\n"
      "v_mul_f32 v125, v125, v235\n"
      "v_mul_f32 v126, v126, v235\n"
      "v_mul_f32 v127, v127, v235\n"
      "v_mul_f32 v228, v228, v232\n"
      "v_mul_f32 v229, v229, v233\n"
      "v_mul_f32 v230, v230, v234\n"
      "v_mul_f32 v231, v231, v235\n"
      "v_fmamk_f32 v240, v160, 0x3e0293ee, v224\n"
      "v_exp_f32 v236, v240\n"
      "v_fmamk_f32 v241, v161, 0x3e0293ee, v224\n"
      "v_exp_f32 v237, v241\n"
      "v_fmamk_f32 v240, v162, 0x3e0293ee, v224\n"
      "v_exp_f32 v238, v240\n"
      "v_fmamk_f32 v241, v163, 0x3e0293ee, v224\n"
      "v_add_f32 v232, v236, v237\n"
      "v_cvt_pk_bf16_f32 v208, v236, v237\n"
      "v_exp_f32 v239, v241\n"
      "v_fmamk_f32 v240, v168, 0x3e0293ee, v225\n"
      "v_add_f32 v232, v232, v238\n"
      "v_exp_f32 v236, v240\n"
      "v_fmamk_f32 v241, v169, 0x3e0293ee, v225\n"
      "v_add_f32 v232, v232, v239\n"
      "v_cvt_pk_bf16_f32 v209, v238, v239\n"
      "v_exp_f32 v237, v241\n"
      "v_fmamk_f32 v240, v170, 0x3e0293ee, v225\n"
      "v_exp_f32 v238, v240\n"
      "v_fmamk_f32 v241, v171, 0x3e0293ee, v225\n"
      "v_add_f32 v233, v236, v237\n"
      "v_cvt_pk_bf16_f32 v212, v236, v237\n"
      "v_exp_f32 v239, v241\n"
      "v_fmamk_f32 v240, v176, 0x3e0293ee, v226\n"
      "v_add_f32 v233, v233, v238\n"
      "v_exp_f32 v236, v240\n"
      "v_fmamk_f32 v241, v177, 0x3e0293ee, v226\n"
      "v_add_f32 v233, v233, v239\n"
      "v_cvt_pk_bf16_f32 v213, v238, v239\n"
      "v_exp_f32 v237, v241\n"
      "v_fmamk_f32 v240, v178, 0x3e0293ee, v226\n"
      "v_exp_f32 v238, v240\n"
      "v_fmamk_f32 v241, v179, 0x3e0293ee, v226\n"
      "v_add_f32 v234, v236, v237\n"
      "v_cvt_pk_bf16_f32 v216, v236, v237\n"
      "v_exp_f32 v239, v241\n"
      "v_fmamk_f32 v240, v184, 0x3e0293ee, v227\n"
      "v_add_f32 v234, v234, v238\n"
      "v_exp_f32 v236, v240\n"
      "v_fmamk_f32 v241, v185, 0x3e0293ee, v227\n"
      "v_add_f32 v234, v234, v239\n"
      "v_cvt_pk_bf16_f32 v217, v238, v239\n"
      "v_exp_f32 v237, v241\n"
      "v_fmamk_f32 v240, v186, 0x3e0293ee, v227\n"
      "v_exp_f32 v238, v240\n"
      "v_fmamk_f32 v241, v187, 0x3e0293ee, v227\n"
      "v_add_f32 v235, v236, v237\n"
      "v_cvt_pk_bf16_f32 v220, v236, v237\n"
      "v_exp_f32 v239, v241\n"
      "v_fmamk_f32 v240, v164, 0x3e0293ee, v224\n"
      "v_add_f32 v235, v235, v238\n"
      "v_exp_f32 v236, v240\n"
      "v_fmamk_f32 v241, v165, 0x3e0293ee, v224\n"
      "v_add_f32 v235, v235, v239\n"
      "v_cvt_pk_bf16_f32 v221, v238, v239\n"
      "v_exp_f32 v237, v241\n"
      "v_fmamk_f32 v240, v166, 0x3e0293ee, v224\n"
      "v_add_f32 v232, v232, v236\n"
      "v_exp_f32 v238, v240\n"
      "v_fmamk_f32 v241, v167, 0x3e0293ee, v224\n"
      "v_add_f32 v232, v232, v237\n"
      "v_cvt_pk_bf16_f32 v210, v236, v237\n"
      "v_exp_f32 v239, v241\n"
      "v_fmamk_f32 v240, v172, 0x3e0293ee, v225\n"
      "v_add_f32 v232, v232, v238\n"
      "v_exp_f32 v236, v240\n"
      "v_fmamk_f32 v241, v173, 0x3e0293ee, v225\n"
      "v_add_f32 v232, v232, v239\n"
      "v_cvt_pk_bf16_f32 v211, v238, v239\n"
      "v_exp_f32 v237, v241\n"
      "v_fmamk_f32 v240, v174, 0x3e0293ee, v225\n"
      "v_add_f32 v233, v233, v236\n"
      "v_exp_f32 v238, v240\n"
      "v_fmamk_f32 v241, v175, 0x3e0293ee, v225\n"
      "v_add_f32 v233, v233, v237\n"
      "v_cvt_pk_bf16_f32 v214, v236, v237\n"
      "v_exp_f32 v239, v241\n"
      "v_fmamk_f32 v240, v180, 0x3e0293ee, v226\n"
      "v_add_f32 v233, v233, v238\n"
      "v_exp_f32 v236, v240\n"
      "v_fmamk_f32 v241, v181, 0x3e0293ee, v226\n"
      "v_add_f32 v233, v233, v239\n"
      "v_cvt_pk_bf16_f32 v215, v238, v239\n"
      "v_exp_f32 v237, v241\n"
      "v_fmamk_f32 v240, v182, 0x3e0293ee, v226\n"
      "v_add_f32 v234, v234, v236\n"
      "v_exp_f32 v238, v240\n"
      "v_fmamk_f32 v241, v183, 0x3e0293ee, v226\n"
      "v_add_f32 v234, v234, v237\n"
      "v_cvt_pk_bf16_f32 v218, v236, v237\n"
      "v_exp_f32 v239, v241\n"
      "v_fmamk_f32 v240, v188, 0x3e0293ee, v227\n"
      "v_add_f32 v234, v234, v238\n"
      "v_exp_f32 v236, v240\n"
      "v_fmamk_f32 v241, v189, 0x3e0293ee, v227\n"
      "v_add_f32 v234, v234, v239\n"
      "v_cvt_pk_bf16_f32 v219, v238, v239\n"
      "v_exp_f32 v237, v241\n"
      "v_fmamk_f32 v240, v190, 0x3e0293ee, v227\n"
      "v_add_f32 v235, v235, v236\n"
      "v_exp_f32 v238, v240\n"
      "v_fmamk_f32 v241, v191, 0x3e0293ee, v227\n"
      "v_add_f32 v235, v235, v237\n"
      "v_cvt_pk_bf16_f32 v222, v236, v237\n"
      "v_exp_f32 v239, v241\n"
      "v_add_f32 v235, v235, v238\n"
      "v_add_f32 v235, v235, v239\n"
      "v_cvt_pk_bf16_f32 v223, v238, v239\n"
      "s_nop 1\n"
      "s_cmp_eq_u32 s64, 0\n"
      "s_cbranch_scc1 RET1_0_%=\n"
      "s_cmp_eq_u32 s64, 1\n"
      "s_cbranch_scc1 RET1_1_%=\n"
      "s_cmp_eq_u32 s64, 2\n"
      "s_cbranch_scc1 RET1_2_%=\n"
      "s_cmp_eq_u32 s64, 3\n"
      "s_cbranch_scc1 RET1_3_%=\n"
      "ds_write_b128 v242, v[96:99] offset:32768\n"
      "ds_write_b128 v242, v[100:103] offset:33792\n"
      "ds_write_b128 v242, v[104:107] offset:34816\n"
      "ds_write_b128 v242, v[108:111] offset:35840\n"
      "ds_write_b128 v242, v[112:115] offset:36864\n"
      "ds_write_b128 v242, v[116:119] offset:37888\n"
      "ds_write_b128 v242, v[120:123] offset:38912\n"
      "ds_write_b128 v242, v[124:127] offset:39936\n"
      "ds_write_b128 v242, v[208:211] offset:2048\n"
      "ds_write_b128 v242, v[212:215] offset:3072\n"
      "ds_write_b128 v242, v[216:219] offset:4096\n"
      "s_branch RET1_4_%=\n"
      ".subsection 0\n"
      : "+{v[32:63]}"(z1), "+{v[64:95]}"(z2), "+{v[96:127]}"(z3), "+{s92}"(tch), "+{v[208:215]}"(mk), "+{v[216:217]}"(epoch), "+{v[242:249]}"(ad), "+{v[250:251]}"(ra), [e0] "=&s"(e0), [e1] "=&s"(e1)
      : [srdk] "s"(srdK), [srdv] "s"(srdV), [srdr] "s"(srdR), [srdx] "s"(srdX), [srdt] "s"(srdVt), [line] "s"(line),
        [outp] "s"((char*)out + ((size_t)bk * 128 + rh * 64) * (D_ * 2) + (2 * split + kh) * 64),
        [fo] "s"(8 + split * 8), [xo] "s"(split * 4 * (unsigned)XW_BYTES), [xw] "s"(lds0 + STAGE_BYTES + wid * 8192),
        [so0] "s"(so0), [inc] "s"(inc), [wb] "s"(wbase), [tv] "s"(wbase + split * 8192),
        [v0] "s"(v0), [v1] "s"(v1), [v2] "s"(v0 + 8192), [v3] "s"(v1 + 8192), [n1] "s"(n1), [n2] "s"(14 - n1),
        [qoff] "s"(Q_OFF + rh * 16384 - kh * 8192), [toff] "s"(TREE_OFF - kh * 8192), [other] "s"(wid ^ (3 * split)),
        [lead] "s"(blockIdx.x * 64 + wid * 512), [cph] "s"(C2 + (unsigned)((jj & ~3) * 8 + xg * 4) * WIN / 256u),
        [rep] "s"((unsigned)(blockIdx.x - 224) * 64), [vrd] "s"(8192 + (blockIdx.x & 3) * 2048)
      : "v0", "v1", "v2", "v3", "v4", "v5", "v6", "v7", "v8", "v9", "v10", "v11", "v12", "v13", "v14", "v15", "v16", "v17", "v18", "v19", "v20", "v21", "v22", "v23", "v24", "v25", "v26", "v27", "v28", "v29", "v30", "v31", "v128", "v129", "v130", "v131", "v132", "v133", "v134", "v135", "v136", "v137", "v138", "v139", "v140", "v141", "v142", "v143", "v144", "v145", "v146", "v147", "v148", "v149", "v150", "v151", "v152", "v153", "v154", "v155", "v156", "v157", "v158", "v159", "v160", "v161", "v162", "v163", "v164", "v165", "v166", "v167", "v168", "v169", "v170", "v171", "v172", "v173", "v174", "v175", "v176", "v177", "v178", "v179", "v180", "v181", "v182", "v183", "v184", "v185", "v186", "v187", "v188", "v189", "v190", "v191", "v192", "v193", "v194", "v195", "v196", "v197", "v198", "v199", "v200", "v201", "v202", "v203", "v204", "v205", "v206", "v207", "v218", "v219", "v220", "v221", "v222", "v223", "v224", "v225", "v226", "v227", "v228", "v229", "v230", "v231", "v232", "v233", "v234", "v235", "v236", "v237", "v238", "v239", "v240", "v241", "a0", "a1", "a2", "a3", "a4", "a5", "a6", "a7", "a8", "a9", "a10", "a11", "a12", "a13", "a14", "a15", "a16", "a17", "a18", "a19", "a20", "a21", "a22", "a23", "a24", "a25", "a26", "a27", "a28", "a29", "a30", "a31", "a32", "a33", "a34", "a35", "a36", "a37", "a38", "a39", "a40", "a41", "a42", "a43", "a44", "a45", "a46", "a47", "a48", "a49", "a50", "a51", "a52", "a53", "a54", "a55", "a56", "a57", "a58", "a59", "a60", "a61", "a62", "a63", "a64", "a65", "a66", "a67", "a68", "a69", "a70", "a71", "a72", "a73", "a74", "a75", "a76", "a77", "a78", "a79", "a80", "a81", "a82", "a83", "a84", "a85", "a86", "a87", "a88", "a89", "a90", "a91", "a92", "a93", "a94", "a95", "a96", "a97", "a98", "a99", "a100", "a101", "a102", "a103", "a104", "a105", "a106", "a107", "a108", "a109", "a110", "a111", "a112", "a113", "a114", "a115", "a116", "a117", "a118", "a119", "a120", "a121", "a122", "a123", "a124", "a125", "a126", "a127", "a128", "a129", "a130", "a131", "a132", "a133", "a134", "a135", "a136", "a137", "a138", "a139", "a140", "a141", "a142", "a143", "a144", "a145", "a146", "a147", "a148", "a149", "a150", "a151", "a152", "a153", "a154", "a155", "a156", "a157", "a158", "a159", "s60", "s61", "s62", "s63", "s64", "s65", "s66", "s67", "s68", "s69", "s70", "s71", "s72", "s73", "s74", "s75", "s76", "s77", "s78", "s79", "s80", "s81", "s82", "s83", "s84", "s85", "s86", "s87", "s88", "s89", "s90", "s91", "s93", "vcc", "scc", "memory");
}

}

extern "C" void kernel_launch(void* const* d_in, const int* in_sizes, int n_in, void* d_out, int out_size,
                              void* d_ws, size_t ws_size, hipStream_t stream) {
  if (n_in != 6 || ws_size < WS_END) return;
  const __hip_bfloat16* q = (const __hip_bfloat16*)d_in[0];
  const __hip_bfloat16* kc = (const __hip_bfloat16*)d_in[1];
  const __hip_bfloat16* vc = (const __hip_bfloat16*)d_in[2];
  const __hip_bfloat16* kt = (const __hip_bfloat16*)d_in[3];
  const __hip_bfloat16* vt = (const __hip_bfloat16*)d_in[4];
  const __hip_bfloat16* tm = (const __hip_bfloat16*)d_in[5];
  hipLaunchKernelGGL(tree_attn_main, dim3(B_ * HKV_ * 2), dim3(256), 0, stream, q, kc, vc, kt, vt, tm,
                     (__hip_bfloat16*)d_out, (char*)d_ws + ((size_t)0  __H0__ ));
}
```

```cpp
#include <hip/hip_runtime.h>
#include <hip/hip_bf16.h>
#include <stdint.h>

#pragma clang diagnostic ignored "-Wint-to-pointer-cast"
#pragma clang diagnostic ignored "-Winline-asm"

namespace {

constexpr int B_ = 16, HQ_ = 32, HKV_ = 8, T_ = 32, C_ = 4096, D_ = 128;
constexpr int SLOT = 64 * D_ * 2;
constexpr int TREE_OFF = 7 * SLOT;
constexpr int REF_OFF = TREE_OFF + 8192, DUMP_OFF = TREE_OFF + 12288;
constexpr int Q_OFF = 8 * SLOT;
constexpr int LDS_BYTES = 10 * SLOT;
constexpr unsigned C2 = 125, WIN = 36;
constexpr int CODE_BYTES = 15360;

constexpr size_t WS_EPOCH = 0;
constexpr size_t WS_REF = WS_EPOCH + 128 * 4 * 128;
constexpr size_t WS_X = WS_REF + 16384;
constexpr size_t XW_BYTES = 4864;
constexpr size_t WS_END = WS_X + (size_t)128 * 2 * 4 * XW_BYTES;

typedef float    f32x32 __attribute__((ext_vector_type(32)));
typedef float    f32x16 __attribute__((ext_vector_type(16)));
typedef float    f32x4  __attribute__((ext_vector_type(4)));
typedef unsigned long long u64x8 __attribute__((ext_vector_type(8)));
typedef unsigned u32x8  __attribute__((ext_vector_type(8)));
typedef unsigned u32x4  __attribute__((ext_vector_type(4)));
typedef unsigned u32x2  __attribute__((ext_vector_type(2)));
typedef __attribute__((address_space(3))) f32x4 lds_f32x4;
typedef __attribute__((address_space(1))) unsigned long long gu64;

#define DI __device__ __forceinline__

DI void dma_rows(unsigned m, unsigned v_lo, unsigned v_hi, __amdgpu_buffer_rsrc_t rsrc, unsigned soff) {
  asm volatile("s_mov_b32 m0, %0\n\ts_nop 0\n\tbuffer_load_dwordx4 %1, %3, %4 offen nt lds\n\tbuffer_load_dwordx4 %1, %3, %4 offen offset:2048 nt lds\n\t"
               "s_bitset1_b32 m0, 10\n\ts_nop 0\n\tbuffer_load_dwordx4 %2, %3, %4 offen nt lds\n\tbuffer_load_dwordx4 %2, %3, %4 offen offset:2048 nt lds"
               :: "s"(m), "v"(v_lo), "v"(v_hi), "s"(rsrc), "s"(soff) : "memory");
}

__global__ __launch_bounds__(256, 1)
void tree_attn_main(const __hip_bfloat16*, const __hip_bfloat16*, const __hip_bfloat16*, const __hip_bfloat16*,
                    const __hip_bfloat16*, const __hip_bfloat16*, __hip_bfloat16*, char*) {
  u64x8 ka;
  asm volatile("s_load_dwordx16 s[4:19], %1, 0x0" : "={s[4:19]}"(ka) : "s"(__builtin_amdgcn_kernarg_segment_ptr()) : "memory");
  __shared__ __attribute__((aligned(1024))) char smem[LDS_BYTES];
  unsigned long long pc;
  asm volatile("s_getpc_b64 %0" : "=s"(pc));
  const int tid = threadIdx.x, lane = tid & 63;
  const int wid = __builtin_amdgcn_readfirstlane(tid >> 6);
  const int rh = wid & 1, kh = wid >> 1;
  const int xg = blockIdx.x & 7, jj = blockIdx.x >> 3;
  const int bk = xg * 16 + (jj >> 1), split = jj & 1;
  const int c16 = lane & 15, g = lane >> 4;
  const unsigned lds0 = (unsigned)(uintptr_t)smem;
  if (wid >= 2) {
    __amdgpu_buffer_rsrc_t srdC = __builtin_amdgcn_make_buffer_rsrc((void*)(pc & ~63ull), 0, CODE_BYTES, 0x00020000);
    asm volatile("s_mov_b32 m0, %0\n\ts_nop 0\n\tbuffer_load_dwordx4 %1, %2, %3 offen lds"
                 :: "s"(lds0 + DUMP_OFF), "v"(lane * 16), "s"(srdC), "s"((unsigned)(wid == 3 ? min(jj, CODE_BYTES / 1024 - 1) : jj & 3) * 1024) : "memory");
  }

  const unsigned wbase = lds0 + (unsigned)wid * 4096;
#pragma unroll
  for (int i = 0; i < 4; ++i) *(lds_f32x4*)(lds0 + SLOT + i * 4096 + tid * 16) = f32x4{0.f, 0.f, 0.f, 0.f};
#pragma unroll
  for (int i = 0; i < 2; ++i) *(lds_f32x4*)(lds0 + 3 * SLOT + (1 - split) * 8192 + i * 4096 + tid * 16) = f32x4{0.f, 0.f, 0.f, 0.f};
  u32x8 ad;
#pragma unroll
  for (int j = 0; j < 2; ++j)
    ad[j] = lds0 + (unsigned)(2048 * (c16 >> 3) + 128 * (c16 & 7) + (((4 * j + g) ^ (c16 & 6)) << 4) + kh * 8192);
  {
    const int qq = c16 >> 2, p = lane & 3;
#pragma unroll
    for (int j = 0; j < 4; ++j)
      ad[2 + j] = lds0 + (unsigned)(2048 * (g >> 1) + 128 * (4 * (g & 1) + qq) + kh * 8192 + SLOT +
                                    (((2 * (j ^ (2 * kh)) + (p >> 1)) ^ ((2 * (g & 1) + (qq >> 1)) << 1)) << 4) + 8 * (p & 1));
  }
  ad[6] = (unsigned)((16 * wid + (lane >> 3)) * 256 + (((lane & 7) ^ (g << 1)) << 4) + 128 * split);
  ad[7] = ad[6] ^ 128;
  unsigned l16 = (unsigned)lane * 16;
  u32x2 ra = {(unsigned)((lane >> 2) * 512 + (lane & 3) * 16), (unsigned)((blockIdx.x & 15) * 512 + (lane & 7) * 64)};
  unsigned mko = (unsigned)(((bk >> 3) * T_ + c16) * (T_ * 2) + g * 8);
  size_t qo = (size_t)bk * 128 * D_ * 2;
  const unsigned cls = ((jj >> 1) + xg * 5) & 15;
  unsigned mA = wid ? 1u : 0x54440001u, mB = wid ? 3u : 0x2aaa8883u;
  unsigned long long mN = 0, mR = 0;
  u32x2 mH = {wid ? 0u : 0x98989990u, wid ? 0u : 0x80808u};
  { const unsigned k = 7 + (cls * 16 / 16 & ~3u); mB |= 0x80000000u >> (k >> 1); mN |= 4ull << k; }
  { const unsigned k = 23 + (cls * 20 / 16 & ~3u); mB |= 0x80000000u >> (k >> 1); mN |= 4ull << k; }
  { const unsigned k = 43 + (cls * 20 / 16 & ~3u); mB |= 0x80000000u >> (k >> 1); mN |= 4ull << k; }
  f32x32 z1, z2, z3;
  f32x16 z4, z5;
  u32x8 z6;
  asm volatile(
      "v_mov_b64 v[32:33], 0\n"
      "v_mov_b64 v[34:35], 0\n"
      "v_mov_b64 v[36:37], 0\n"
      "v_mov_b64 v[38:39], 0\n"
      "v_mov_b64 v[40:41], 0\n"
      "v_mov_b64 v[42:43], 0\n"
      "v_mov_b64 v[44:45], 0\n"
      "v_mov_b64 v[46:47], 0\n"
      "v_mov_b64 v[48:49], 0\n"
      "v_mov_b64 v[50:51], 0\n"
      "v_mov_b64 v[52:53], 0\n"
      "v_mov_b64 v[54:55], 0\n"
      "v_mov_b64 v[56:57], 0\n"
      "v_mov_b64 v[58:59], 0\n"
      "v_mov_b64 v[60:61], 0\n"
      "v_mov_b64 v[62:63], 0\n"
      "v_mov_b64 v[128:129], 0\n"
      "v_mov_b64 v[130:131], 0\n"
      "v_mov_b64 v[132:133], 0\n"
      "v_mov_b64 v[134:135], 0\n"
      "v_mov_b64 v[136:137], 0\n"
      "v_mov_b64 v[138:139], 0\n"
      "v_mov_b64 v[140:141], 0\n"
      "v_mov_b64 v[142:143], 0\n"
      "v_mov_b64 v[180:181], 0\n"
      "v_mov_b64 v[182:183], 0\n"
      "v_mov_b64 v[184:185], 0\n"
      "v_mov_b64 v[186:187], 0\n"
      "v_accvgpr_write_b32 a144, 0\n"
      "v_accvgpr_write_b32 a145, 0\n"
      "v_accvgpr_write_b32 a146, 0\n"
      "v_accvgpr_write_b32 a147, 0\n"
      "v_accvgpr_write_b32 a148, 0\n"
      "v_accvgpr_write_b32 a149, 0\n"
      "v_accvgpr_write_b32 a150, 0\n"
      "v_accvgpr_write_b32 a151, 0\n"
      "v_accvgpr_write_b32 a152, 0\n"
      "v_accvgpr_write_b32 a153, 0\n"
      "v_accvgpr_write_b32 a154, 0\n"
      "v_accvgpr_write_b32 a155, 0\n"
      "v_accvgpr_write_b32 a156, 0\n"
      "v_accvgpr_write_b32 a157, 0\n"
      "v_accvgpr_write_b32 a158, 0\n"
      "v_accvgpr_write_b32 a159, 0\n"
      "v_accvgpr_write_b32 a160, 0\n"
      "v_accvgpr_write_b32 a161, 0\n"
      "v_accvgpr_write_b32 a162, 0\n"
      "v_accvgpr_write_b32 a163, 0\n"
      "v_accvgpr_write_b32 a164, 0\n"
      "v_accvgpr_write_b32 a165, 0\n"
      "v_accvgpr_write_b32 a166, 0\n"
      "v_accvgpr_write_b32 a167, 0\n"
      "v_accvgpr_write_b32 a168, 0\n"
      "v_accvgpr_write_b32 a169, 0\n"
      "v_accvgpr_write_b32 a170, 0\n"
      "v_accvgpr_write_b32 a171, 0\n"
      "v_accvgpr_write_b32 a172, 0\n"
      "v_accvgpr_write_b32 a173, 0\n"
      "v_accvgpr_write_b32 a174, 0\n"
      "v_accvgpr_write_b32 a175, 0\n"
      "v_accvgpr_write_b32 a176, 0\n"
      "v_accvgpr_write_b32 a177, 0\n"
      "v_accvgpr_write_b32 a178, 0\n"
      "v_accvgpr_write_b32 a179, 0\n"
      "v_accvgpr_write_b32 a180, 0\n"
      "v_accvgpr_write_b32 a181, 0\n"
      "v_accvgpr_write_b32 a182, 0\n"
      "v_accvgpr_write_b32 a183, 0\n"
      "v_accvgpr_write_b32 a184, 0\n"
      "v_accvgpr_write_b32 a185, 0\n"
      "v_accvgpr_write_b32 a186, 0\n"
      "v_accvgpr_write_b32 a187, 0\n"
      "v_accvgpr_write_b32 a188, 0\n"
      "v_accvgpr_write_b32 a189, 0\n"
      "v_accvgpr_write_b32 a190, 0\n"
      "v_accvgpr_write_b32 a191, 0\n"
      "v_accvgpr_write_b32 a192, 0\n"
      "v_accvgpr_write_b32 a193, 0\n"
      "v_accvgpr_write_b32 a194, 0\n"
      "v_accvgpr_write_b32 a195, 0\n"
      "v_accvgpr_write_b32 a196, 0\n"
      "v_accvgpr_write_b32 a197, 0\n"
      "v_accvgpr_write_b32 a198, 0\n"
      "v_accvgpr_write_b32 a199, 0\n"
      "v_accvgpr_write_b32 a200, 0\n"
      "v_accvgpr_write_b32 a201, 0\n"
      "v_accvgpr_write_b32 a202, 0\n"
      "v_accvgpr_write_b32 a203, 0\n"
      "v_accvgpr_write_b32 a204, 0\n"
      "v_accvgpr_write_b32 a205, 0\n"
      "v_accvgpr_write_b32 a206, 0\n"
      "v_accvgpr_write_b32 a207, 0\n"
      "v_bfe_u32 v188, v241, 4, 2\n"
      "v_mov_b32 v189, 0x3f803f80\n"
      "v_cmp_eq_u32 vcc, 0, v188\n"
      "s_nop 1\n"
      "v_cndmask_b32 v190, 0, v189, vcc\n"
      "v_accvgpr_write_b32 a128, v190\n"
      "v_accvgpr_write_b32 a129, v190\n"
      "v_accvgpr_write_b32 a130, v190\n"
      "v_accvgpr_write_b32 a131, v190\n"
      "v_cmp_eq_u32 vcc, 1, v188\n"
      "s_nop 1\n"
      "v_cndmask_b32 v190, 0, v189, vcc\n"
      "v_accvgpr_write_b32 a132, v190\n"
      "v_accvgpr_write_b32 a133, v190\n"
      "v_accvgpr_write_b32 a134, v190\n"
      "v_accvgpr_write_b32 a135, v190\n"
      "v_cmp_eq_u32 vcc, 2, v188\n"
      "s_nop 1\n"
      "v_cndmask_b32 v190, 0, v189, vcc\n"
      "v_accvgpr_write_b32 a136, v190\n"
      "v_accvgpr_write_b32 a137, v190\n"
      "v_accvgpr_write_b32 a138, v190\n"
      "v_accvgpr_write_b32 a139, v190\n"
      "v_cmp_eq_u32 vcc, 3, v188\n"
      "s_nop 1\n"
      "v_cndmask_b32 v190, 0, v189, vcc\n"
      "v_accvgpr_write_b32 a140, v190\n"
      "v_accvgpr_write_b32 a141, v190\n"
      "v_accvgpr_write_b32 a142, v190\n"
      "v_accvgpr_write_b32 a143, v190\n"
      : "={v[32:63]}"(z1), "={a[144:175]}"(z2), "={a[176:207]}"(z3), "={v[128:143]}"(z4), "={a[128:143]}"(z5), "={v[180:187]}"(z6)
      : "{v241}"(l16) : "v188", "v189", "v190", "vcc");
  asm volatile("" : "+{v[242:249]}"(ad), "+{v[250:251]}"(ra), "+{v241}"(l16), "+v"(mko), "+s"(qo), "+s"(mA), "+s"(mB), "+s"(mN), "+s"(mR), "+{s[52:53]}"(mH) :: "memory");
  asm volatile("s_waitcnt lgkmcnt(0)" : "+{s[4:19]}"(ka) :: "memory");
  const char *qg = (const char*)ka[0], *kc = (const char*)ka[1], *vc = (const char*)ka[2], *kt = (const char*)ka[3],
             *vt = (const char*)ka[4], *tmask = (const char*)ka[5];
  char *out = (char*)ka[6], *ws = (char*)ka[7];
  __amdgpu_buffer_rsrc_t srdQ = __builtin_amdgcn_make_buffer_rsrc((void*)(qg + qo), 0, 128 * D_ * 2, 0x00020000);
  dma_rows(wbase + Q_OFF, ad[6], ad[7], srdQ, 0);
  dma_rows(wbase + Q_OFF + SLOT, ad[6], ad[7], srdQ, SLOT);
  unsigned tch;
  asm volatile("s_load_dword s92, %1, 0x0\n\ts_load_dword s92, %2, 0x0" : "={s92}"(tch) : "s"(kc + qo * 32), "s"(vc + qo * 32) : "memory");
  const size_t hoff = qo * 32;
  {
    __amdgpu_buffer_rsrc_t srdF = __builtin_amdgcn_make_buffer_rsrc((void*)(kc + hoff + (kh * 2048 + ((rh ^ split) - split) * 128 - wid * 4096)), 0, 16384, 0x00020000);
    asm volatile("s_mov_b32 m0, %0\n\ts_nop 0\n\tbuffer_load_dwordx4 %1, %2, 0 offen nt lds"
                 :: "s"(lds0 + REF_OFF + kh * 2048 + rh * 1024), "v"(ad[6]), "s"(srdF) : "memory");
  }
  {
    __amdgpu_buffer_rsrc_t srdK = __builtin_amdgcn_make_buffer_rsrc((void*)(kt + qo / 4 - wid * 2048), 0, 16384, 0x00020000);
    asm volatile("s_mov_b32 m0, %0\n\ts_nop 0\n\tbuffer_load_dwordx4 %1, %3, 0 offen nt lds\n\ts_bitset1_b32 m0, 10\n\ts_nop 0\n\tbuffer_load_dwordx4 %2, %3, 0 offen nt lds"
                 :: "s"(lds0 + TREE_OFF + wid * 2048), "v"(ad[6]), "v"(ad[7]), "s"(srdK) : "memory");
  }
  u32x8 mk;
  asm volatile("s_cmp_lg_u32 %3, 0\n\ts_cbranch_scc1 1f\n\t"
               "global_load_dwordx2 v[200:201], %1, %2\n\tglobal_load_dwordx2 v[202:203], %1, %2 offset:32\n\t"
               "global_load_dwordx2 v[204:205], %1, %2 offset:1024\n\tglobal_load_dwordx2 v[206:207], %1, %2 offset:1056\n1:"
               : "=&{v[200:207]}"(mk) : "v"(mko), "s"(tmask), "s"(wid ^ (3 * split)) : "memory", "scc");
  char* line = ws + WS_EPOCH + (size_t)(bk * 4 + wid) * 128;
  unsigned epoch;
  asm volatile("global_load_dword v208, %1, off sc1" : "=&{v208}"(epoch) : "v"(line) : "memory");
  const unsigned long long kb = (unsigned long long)(kc + hoff + split * 2 * SLOT), vb = (unsigned long long)(vc + hoff + split * 2 * SLOT) - 4 * SLOT - 8192;
  u32x4 srd = {(unsigned)kb, (unsigned)(kb >> 32), 0x120000u, 0x00020000u};
  __amdgpu_buffer_rsrc_t srdT = __builtin_amdgcn_make_buffer_rsrc((void*)(vt + qo / 4 - wid * 2048), 0, 16384, 0x00020000);
  __amdgpu_buffer_rsrc_t srdR = __builtin_amdgcn_make_buffer_rsrc((void*)(ws + WS_REF), 0, 16384, 0x00020000);
  __amdgpu_buffer_rsrc_t srdX = __builtin_amdgcn_make_buffer_rsrc((void*)(ws + WS_X + (size_t)(bk * 8 + wid) * XW_BYTES), 0, (int)(5 * XW_BYTES), 0x00020000);
  unsigned e0, par = (bk & 1) * SLOT;
  asm volatile(
      "s_memrealtime s[82:83]\n"
      "s_mov_b32 s61, 0\n"
      "s_mov_b32 s60, %[par]\n"
      "s_mov_b32 s62, %[wb]\n"
      "s_and_b32 s74, %[wb], 8192\n"
      "s_and_b32 s76, %[wb], 4096\n"
      "s_lshl_b32 s76, s76, 2\n"
      "s_sub_u32 s76, s76, s74\n"
      "s_add_u32 s76, s76, 131072\n"
      "s_sub_u32 s74, 114688, s74\n"
      "v_add_u32 v196, s76, v242\n"
      "v_add_u32 v198, s74, v242\n"
      "v_add_u32 v197, s76, v243\n"
      "v_add_u32 v199, s74, v243\n"
      "v_add_u32 v210, %[wb], v241\n"
      "v_add_u32 v210, 131072, v210\n"
      "v_mov_b64 v[0:1], 0\n"
      "v_mov_b64 v[2:3], 0\n"
      "v_mov_b64 v[4:5], 0\n"
      "v_mov_b64 v[6:7], 0\n"
      "v_mov_b64 v[8:9], 0\n"
      "v_mov_b64 v[10:11], 0\n"
      "v_mov_b64 v[12:13], 0\n"
      "v_mov_b64 v[14:15], 0\n"
      "v_mov_b64 v[16:17], 0\n"
      "v_mov_b64 v[18:19], 0\n"
      "v_mov_b64 v[20:21], 0\n"
      "v_mov_b64 v[22:23], 0\n"
      "v_mov_b64 v[24:25], 0\n"
      "v_mov_b64 v[26:27], 0\n"
      "v_mov_b64 v[28:29], 0\n"
      "v_mov_b64 v[30:31], 0\n"
      "s_mov_b32 m0, s62\n"
      "s_nop 0\n"
      "buffer_load_dwordx4 v248, s[56:59], s60 offen nt lds\n"
      "buffer_load_dwordx4 v248, s[56:59], s60 offen offset:2048 nt lds\n"
      "s_bitset1_b32 m0, 10\n"
      "s_nop 0\n"
      "buffer_load_dwordx4 v249, s[56:59], s60 offen nt lds\n"
      "buffer_load_dwordx4 v249, s[56:59], s60 offen offset:2048 nt lds\n"
      "s_xor_b64 s[56:57], s[56:57], %[dlt]\n"
      "s_bitset1_b32 s61, 15\n"
      "s_add_u32 s61, s61, 0x2000\n"
      "s_bitset0_b32 s61, 15\n"
      "s_xor_b32 s60, s61, %[par]\n"
      "s_add_u32 s62, s62, 0x4000\n"
      "s_and_b32 s62, s62, 0x1ffff\n"
      "s_xor_b64 s[56:57], s[56:57], %[dlt]\n"
      "s_bitset1_b32 s61, 15\n"
      "s_add_u32 s61, s61, 0x2000\n"
      "s_bitset0_b32 s61, 15\n"
      "s_xor_b32 s60, s61, %[par]\n"
      "s_add_u32 s62, s62, 0x4000\n"
      "s_and_b32 s62, s62, 0x1ffff\n"
      "s_mov_b32 m0, %[tv]\n"
      "s_mov_b32 s94, 0x3e0293ee\n"
      "buffer_load_dwordx4 v248, %[srdt], 0 offen nt lds\n"
      "s_bitset1_b32 m0, 10\n"
      "s_mov_b32 s95, 0x3e0293ee\n"
      "buffer_load_dwordx4 v249, %[srdt], 0 offen nt lds\n"
      "s_mov_b32 s54, 3\n"
      "s_mov_b32 s70, 300\n"
      "s_mov_b32 s80, 300\n"
      "s_mov_b32 s86, 59\n"
      "s_mov_b32 s77, 177\n"
      "s_lshl_b32 s91, %[wb], 16\n"
      "s_sub_u32 s87, 70, s91\n"
      "s_sub_u32 s90, 34, s91\n"
      "s_lshl_b32 s75, %[blk], 6\n"
      "s_sub_u32 s72, s75, 14336\n"
      "s_add_u32 s75, s75, %[wb]\n"
      "s_and_b32 s79, %[blk], 3\n"
      "s_lshl_b32 s79, s79, 11\n"
      "s_add_u32 s79, s79, 8192\n"
      "s_mul_i32 s78, %[spl], 19456\n"
      "s_getpc_b64 s[84:85]\n"
      "PC_%=:\n"
      "s_add_u32 s84, s84, HOOK_%=-PC_%=\n"
      "s_addc_u32 s85, s85, 0\n"
      "s_mov_b32 m0, s62\n"
      "s_nop 0\n"
      "buffer_load_dwordx4 v248, s[56:59], s60 offen nt lds\n"
      "buffer_load_dwordx4 v248, s[56:59], s60 offen offset:2048 nt lds\n"
      "s_bitset1_b32 m0, 10\n"
      "s_nop 0\n"
      "buffer_load_dwordx4 v249, s[56:59], s60 offen nt lds\n"
      "buffer_load_dwordx4 v249, s[56:59], s60 offen offset:2048 nt lds\n"
      "s_xor_b64 s[56:57], s[56:57], %[dlt]\n"
      "s_bitset1_b32 s61, 15\n"
      "s_add_u32 s61, s61, 0x2000\n"
      "s_bitset0_b32 s61, 15\n"
      "s_xor_b32 s60, s61, %[par]\n"
      "s_add_u32 s62, s62, 0x4000\n"
      "s_and_b32 s62, s62, 0x1ffff\n"
      "s_xor_b64 s[56:57], s[56:57], %[dlt]\n"
      "s_bitset1_b32 s61, 15\n"
      "s_add_u32 s61, s61, 0x2000\n"
      "s_bitset0_b32 s61, 15\n"
      "s_xor_b32 s60, s61, %[par]\n"
      "s_add_u32 s62, s62, 0x4000\n"
      "s_and_b32 s62, s62, 0x1ffff\n"
      "s_waitcnt vmcnt(14)\n"
      "s_memrealtime s[68:69]\n"
      "ds_read_b128 v[64:67], v210 offset:0\n"
      "ds_read_b128 v[68:71], v210 offset:1024\n"
      "ds_read_b128 v[72:75], v210 offset:2048\n"
      "ds_read_b128 v[76:79], v210 offset:3072\n"
      "ds_read_b128 v[80:83], v210 offset:16384\n"
      "ds_read_b128 v[84:87], v210 offset:17408\n"
      "ds_read_b128 v[88:91], v210 offset:18432\n"
      "ds_read_b128 v[92:95], v210 offset:19456\n"
      "s_mov_b32 m0, s62\n"
      "s_nop 0\n"
      "buffer_load_dwordx4 v248, s[56:59], s60 offen nt lds\n"
      "buffer_load_dwordx4 v248, s[56:59], s60 offen offset:2048 nt lds\n"
      "s_bitset1_b32 m0, 10\n"
      "s_nop 0\n"
      "buffer_load_dwordx4 v249, s[56:59], s60 offen nt lds\n"
      "buffer_load_dwordx4 v249, s[56:59], s60 offen offset:2048 nt lds\n"
      "s_xor_b64 s[56:57], s[56:57], %[dlt]\n"
      "s_bitset1_b32 s61, 15\n"
      "s_add_u32 s61, s61, 0x2000\n"
      "s_bitset0_b32 s61, 15\n"
      "s_xor_b32 s60, s61, %[par]\n"
      "s_add_u32 s62, s62, 0x4000\n"
      "s_and_b32 s62, s62, 0x1ffff\n"
      "s_waitcnt lgkmcnt(0)\n"
      "s_mov_b32 s83, 0\n"
      "s_sub_u32 s67, s68, s82\n"
      "s_min_u32 s67, s67, 140\n"
      "s_add_u32 s93, s82, s67\n"
      "s_add_u32 s66, s93, 135\n"
      "s_sub_u32 s71, s68, 400\n"
      "s_sub_u32 s81, s66, s71\n"
      "s_sub_u32 s81, s81, %[cph]\n"
      "s_sub_u32 s66, s66, s91\n"
      "s_mov_b32 s88, s66\n"
      "s_mov_b32 s65, s66\n"
      "s_cmp_lt_u32 s75, 512\n"
      "s_cbranch_scc0 NOLEAD_%=\n"
      "v_mov_b32 v188, s68\n"
      "v_mov_b32 v189, s68\n"
      "v_mov_b32 v190, s68\n"
      "v_mov_b32 v191, s68\n"
      "buffer_store_dwordx4 v[188:191], v250, %[srdr], s75 offen sc1\n"
      "s_nop 1\n"
      "NOLEAD_%=:\n"
      "v_lshlrev_b32 v188, 16, v64\n"
      "v_and_b32 v189, 0xffff0000, v64\n"
      "v_lshlrev_b32 v190, 16, v65\n"
      "v_and_b32 v191, 0xffff0000, v65\n"
      "v_lshlrev_b32 v192, 16, v66\n"
      "v_and_b32 v193, 0xffff0000, v66\n"
      "v_lshlrev_b32 v194, 16, v67\n"
      "v_and_b32 v195, 0xffff0000, v67\n"
      "v_pk_mul_f32 v[188:189], v[188:189], s[94:95]\n"
      "v_pk_mul_f32 v[190:191], v[190:191], s[94:95]\n"
      "v_pk_mul_f32 v[192:193], v[192:193], s[94:95]\n"
      "v_pk_mul_f32 v[194:195], v[194:195], s[94:95]\n"
      "v_cvt_pk_bf16_f32 v64, v188, v189\n"
      "v_cvt_pk_bf16_f32 v65, v190, v191\n"
      "v_cvt_pk_bf16_f32 v66, v192, v193\n"
      "v_cvt_pk_bf16_f32 v67, v194, v195\n"
      "ds_write_b128 v210, v[64:67] offset:0\n"
      "v_lshlrev_b32 v188, 16, v68\n"
      "v_and_b32 v189, 0xffff0000, v68\n"
      "v_lshlrev_b32 v190, 16, v69\n"
      "v_and_b32 v191, 0xffff0000, v69\n"
      "v_lshlrev_b32 v192, 16, v70\n"
      "v_and_b32 v193, 0xffff0000, v70\n"
      "v_lshlrev_b32 v194, 16, v71\n"
      "v_and_b32 v195, 0xffff0000, v71\n"
      "v_pk_mul_f32 v[188:189], v[188:189], s[94:95]\n"
      "v_pk_mul_f32 v[190:191], v[190:191], s[94:95]\n"
      "v_pk_mul_f32 v[192:193], v[192:193], s[94:95]\n"
      "v_pk_mul_f32 v[194:195], v[194:195], s[94:95]\n"
      "v_cvt_pk_bf16_f32 v68, v188, v189\n"
      "v_cvt_pk_bf16_f32 v69, v190, v191\n"
      "v_cvt_pk_bf16_f32 v70, v192, v193\n"
      "v_cvt_pk_bf16_f32 v71, v194, v195\n"
      "ds_write_b128 v210, v[68:71] offset:1024\n"
      "v_lshlrev_b32 v188, 16, v72\n"
      "v_and_b32 v189, 0xffff0000, v72\n"
      "v_lshlrev_b32 v190, 16, v73\n"
      "v_and_b32 v191, 0xffff0000, v73\n"
      "v_lshlrev_b32 v192, 16, v74\n"
      "v_and_b32 v193, 0xffff0000, v74\n"
      "v_lshlrev_b32 v194, 16, v75\n"
      "v_and_b32 v195, 0xffff0000, v75\n"
      "v_pk_mul_f32 v[188:189], v[188:189], s[94:95]\n"
      "v_pk_mul_f32 v[190:191], v[190:191], s[94:95]\n"
      "v_pk_mul_f32 v[192:193], v[192:193], s[94:95]\n"
      "v_pk_mul_f32 v[194:195], v[194:195], s[94:95]\n"
      "v_cvt_pk_bf16_f32 v72, v188, v189\n"
      "v_cvt_pk_bf16_f32 v73, v190, v191\n"
      "v_cvt_pk_bf16_f32 v74, v192, v193\n"
      "v_cvt_pk_bf16_f32 v75, v194, v195\n"
      "ds_write_b128 v210, v[72:75] offset:2048\n"
      "v_lshlrev_b32 v188, 16, v76\n"
      "v_and_b32 v189, 0xffff0000, v76\n"
      "v_lshlrev_b32 v190, 16, v77\n"
      "v_and_b32 v191, 0xffff0000, v77\n"
      "v_lshlrev_b32 v192, 16, v78\n"
      "v_and_b32 v193, 0xffff0000, v78\n"
      "v_lshlrev_b32 v194, 16, v79\n"
      "v_and_b32 v195, 0xffff0000, v79\n"
      "v_pk_mul_f32 v[188:189], v[188:189], s[94:95]\n"
      "v_pk_mul_f32 v[190:191], v[190:191], s[94:95]\n"
      "v_pk_mul_f32 v[192:193], v[192:193], s[94:95]\n"
      "v_pk_mul_f32 v[194:195], v[194:195], s[94:95]\n"
      "v_cvt_pk_bf16_f32 v76, v188, v189\n"
      "v_cvt_pk_bf16_f32 v77, v190, v191\n"
      "v_cvt_pk_bf16_f32 v78, v192, v193\n"
      "v_cvt_pk_bf16_f32 v79, v194, v195\n"
      "ds_write_b128 v210, v[76:79] offset:3072\n"
      "v_lshlrev_b32 v188, 16, v80\n"
      "v_and_b32 v189, 0xffff0000, v80\n"
      "v_lshlrev_b32 v190, 16, v81\n"
      "v_and_b32 v191, 0xffff0000, v81\n"
      "v_lshlrev_b32 v192, 16, v82\n"
      "v_and_b32 v193, 0xffff0000, v82\n"
      "v_lshlrev_b32 v194, 16, v83\n"
      "v_and_b32 v195, 0xffff0000, v83\n"
      "v_pk_mul_f32 v[188:189], v[188:189], s[94:95]\n"
      "v_pk_mul_f32 v[190:191], v[190:191], s[94:95]\n"
      "v_pk_mul_f32 v[192:193], v[192:193], s[94:95]\n"
      "v_pk_mul_f32 v[194:195], v[194:195], s[94:95]\n"
      "v_cvt_pk_bf16_f32 v80, v188, v189\n"
      "v_cvt_pk_bf16_f32 v81, v190, v191\n"
      "v_cvt_pk_bf16_f32 v82, v192, v193\n"
      "v_cvt_pk_bf16_f32 v83, v194, v195\n"
      "ds_write_b128 v210, v[80:83] offset:16384\n"
      "v_lshlrev_b32 v188, 16, v84\n"
      "v_and_b32 v189, 0xffff0000, v84\n"
      "v_lshlrev_b32 v190, 16, v85\n"
      "v_and_b32 v191, 0xffff0000, v85\n"
      "v_lshlrev_b32 v192, 16, v86\n"
      "v_and_b32 v193, 0xffff0000, v86\n"
      "v_lshlrev_b32 v194, 16, v87\n"
      "v_and_b32 v195, 0xffff0000, v87\n"
      "v_pk_mul_f32 v[188:189], v[188:189], s[94:95]\n"
      "v_pk_mul_f32 v[190:191], v[190:191], s[94:95]\n"
      "v_pk_mul_f32 v[192:193], v[192:193], s[94:95]\n"
      "v_pk_mul_f32 v[194:195], v[194:195], s[94:95]\n"
      "v_cvt_pk_bf16_f32 v84, v188, v189\n"
      "v_cvt_pk_bf16_f32 v85, v190, v191\n"
      "v_cvt_pk_bf16_f32 v86, v192, v193\n"
      "v_cvt_pk_bf16_f32 v87, v194, v195\n"
      "ds_write_b128 v210, v[84:87] offset:17408\n"
      "v_lshlrev_b32 v188, 16, v88\n"
      "v_and_b32 v189, 0xffff0000, v88\n"
      "v_lshlrev_b32 v190, 16, v89\n"
      "v_and_b32 v191, 0xffff0000, v89\n"
      "v_lshlrev_b32 v192, 16, v90\n"
      "v_and_b32 v193, 0xffff0000, v90\n"
      "v_lshlrev_b32 v194, 16, v91\n"
      "v_and_b32 v195, 0xffff0000, v91\n"
      "v_pk_mul_f32 v[188:189], v[188:189], s[94:95]\n"
      "v_pk_mul_f32 v[190:191], v[190:191], s[94:95]\n"
      "v_pk_mul_f32 v[192:193], v[192:193], s[94:95]\n"
      "v_pk_mul_f32 v[194:195], v[194:195], s[94:95]\n"
      "v_cvt_pk_bf16_f32 v88, v188, v189\n"
      "v_cvt_pk_bf16_f32 v89, v190, v191\n"
      "v_cvt_pk_bf16_f32 v90, v192, v193\n"
      "v_cvt_pk_bf16_f32 v91, v194, v195\n"
      "ds_write_b128 v210, v[88:91] offset:18432\n"
      "v_lshlrev_b32 v188, 16, v92\n"
      "v_and_b32 v189, 0xffff0000, v92\n"
      "v_lshlrev_b32 v190, 16, v93\n"
      "v_and_b32 v191, 0xffff0000, v93\n"
      "v_lshlrev_b32 v192, 16, v94\n"
      "v_and_b32 v193, 0xffff0000, v94\n"
      "v_lshlrev_b32 v194, 16, v95\n"
      "v_and_b32 v195, 0xffff0000, v95\n"
      "v_pk_mul_f32 v[188:189], v[188:189], s[94:95]\n"
      "v_pk_mul_f32 v[190:191], v[190:191], s[94:95]\n"
      "v_pk_mul_f32 v[192:193], v[192:193], s[94:95]\n"
      "v_pk_mul_f32 v[194:195], v[194:195], s[94:95]\n"
      "v_cvt_pk_bf16_f32 v92, v188, v189\n"
      "v_cvt_pk_bf16_f32 v93, v190, v191\n"
      "v_cvt_pk_bf16_f32 v94, v192, v193\n"
      "v_cvt_pk_bf16_f32 v95, v194, v195\n"
      "ds_write_b128 v210, v[92:95] offset:19456\n"
      "s_waitcnt vmcnt(14) lgkmcnt(0)\n"
      "s_barrier\n"
      "ds_read_b128 a[64:67], v198 offset:8192\n"
      "ds_read_b128 a[72:75], v199 offset:8192\n"
      "ds_read_b128 a[80:83], v198 offset:9216\n"
      "ds_read_b128 a[88:91], v199 offset:9216\n"
      "ds_read_b128 a[0:3], v196 offset:0\n"
      "ds_read_b128 a[16:19], v197 offset:0\n"
      "ds_read_b128 a[32:35], v196 offset:1024\n"
      "ds_read_b128 a[48:51], v197 offset:1024\n"
      "ds_read_b128 a[4:7], v196 offset:4096\n"
      "ds_read_b128 a[20:23], v197 offset:4096\n"
      "ds_read_b128 a[36:39], v196 offset:5120\n"
      "ds_read_b128 a[52:55], v197 offset:5120\n"
      "s_waitcnt lgkmcnt(7)\n"
      "ds_read_b128 a[8:11], v196 offset:8192\n"
      "ds_read_b128 a[24:27], v197 offset:8192\n"
      "ds_read_b128 a[40:43], v196 offset:9216\n"
      "ds_read_b128 a[56:59], v197 offset:9216\n"
      "ds_read_b128 a[12:15], v196 offset:12288\n"
      "ds_read_b128 a[28:31], v197 offset:12288\n"
      "ds_read_b128 a[44:47], v196 offset:13312\n"
      "ds_read_b128 a[60:63], v197 offset:13312\n"
      "v_readfirstlane_b32 %[e0], v208\n"
      "s_waitcnt lgkmcnt(12)\n"
      "v_mfma_f32_16x16x32_bf16 v[64:67], a[64:67], a[0:3], 0\n"
      "v_mov_b32 v96, 0xf149f2ca\n"
      "v_mov_b32 v97, 0xf149f2ca\n"
      "v_mfma_f32_16x16x32_bf16 v[64:67], a[72:75], a[16:19], v[64:67]\n"
      "v_mov_b32 v98, 0xf149f2ca\n"
      "v_mov_b32 v99, 0xf149f2ca\n"
      "v_mfma_f32_16x16x32_bf16 v[64:67], a[80:83], a[32:35], v[64:67]\n"
      "v_mov_b32 v100, 0xf149f2ca\n"
      "v_mov_b32 v101, 0xf149f2ca\n"
      "v_mfma_f32_16x16x32_bf16 v[64:67], a[88:91], a[48:51], v[64:67]\n"
      "v_mov_b32 v102, 0xf149f2ca\n"
      "v_mov_b32 v103, 0xf149f2ca\n"
      "s_waitcnt lgkmcnt(8)\n"
      "v_mfma_f32_16x16x32_bf16 v[72:75], a[64:67], a[4:7], 0\n"
      "v_mov_b32 v104, 0xf149f2ca\n"
      "v_mov_b32 v105, 0xf149f2ca\n"
      "v_mfma_f32_16x16x32_bf16 v[72:75], a[72:75], a[20:23], v[72:75]\n"
      "v_mov_b32 v106, 0xf149f2ca\n"
      "v_mov_b32 v107, 0xf149f2ca\n"
      "v_mfma_f32_16x16x32_bf16 v[72:75], a[80:83], a[36:39], v[72:75]\n"
      "v_mov_b32 v108, 0xf149f2ca\n"
      "v_mov_b32 v109, 0xf149f2ca\n"
      "v_mfma_f32_16x16x32_bf16 v[72:75], a[88:91], a[52:55], v[72:75]\n"
      "v_mov_b32 v110, 0xf149f2ca\n"
      "v_mov_b32 v111, 0xf149f2ca\n"
      "s_waitcnt lgkmcnt(4)\n"
      "v_mfma_f32_16x16x32_bf16 v[80:83], a[64:67], a[8:11], 0\n"
      "v_mov_b32 v112, 0xf149f2ca\n"
      "v_mov_b32 v113, 0xf149f2ca\n"
      "v_mfma_f32_16x16x32_bf16 v[80:83], a[72:75], a[24:27], v[80:83]\n"
      "v_mov_b32 v114, 0xf149f2ca\n"
      "v_mov_b32 v115, 0xf149f2ca\n"
      "v_mfma_f32_16x16x32_bf16 v[80:83], a[80:83], a[40:43], v[80:83]\n"
      "v_mov_b32 v116, 0xf149f2ca\n"
      "v_mov_b32 v117, 0xf149f2ca\n"
      "v_mfma_f32_16x16x32_bf16 v[80:83], a[88:91], a[56:59], v[80:83]\n"
      "v_mov_b32 v118, 0xf149f2ca\n"
      "v_mov_b32 v119, 0xf149f2ca\n"
      "s_waitcnt lgkmcnt(0)\n"
      "v_mfma_f32_16x16x32_bf16 v[88:91], a[64:67], a[12:15], 0\n"
      "v_mov_b32 v120, 0xf149f2ca\n"
      "v_mov_b32 v121, 0xf149f2ca\n"
      "v_mfma_f32_16x16x32_bf16 v[88:91], a[72:75], a[28:31], v[88:91]\n"
      "v_mov_b32 v122, 0xf149f2ca\n"
      "v_mov_b32 v123, 0xf149f2ca\n"
      "v_mfma_f32_16x16x32_bf16 v[88:91], a[80:83], a[44:47], v[88:91]\n"
      "v_mov_b32 v124, 0xf149f2ca\n"
      "v_mov_b32 v125, 0xf149f2ca\n"
      "v_mfma_f32_16x16x32_bf16 v[88:91], a[88:91], a[60:63], v[88:91]\n"
      "v_mov_b32 v126, 0xf149f2ca\n"
      "v_mov_b32 v127, 0xf149f2ca\n"
      "ds_read_b128 a[64:67], v198 offset:0\n"
      "ds_read_b128 a[68:71], v198 offset:4096\n"
      "ds_read_b128 a[72:75], v199 offset:0\n"
      "ds_read_b128 a[76:79], v199 offset:4096\n"
      "ds_read_b128 a[80:83], v198 offset:1024\n"
      "ds_read_b128 a[84:87], v198 offset:5120\n"
      "ds_read_b128 a[88:91], v199 offset:1024\n"
      "ds_read_b128 a[92:95], v199 offset:5120\n"
      "v_max3_f32 v188, v64, v65, v66\n"
      "v_max3_f32 v189, v72, v73, v74\n"
      "v_max3_f32 v190, v80, v81, v82\n"
      "v_max3_f32 v191, v88, v89, v90\n"
      "v_max_f32 v188, v188, v67\n"
      "v_max_f32 v189, v189, v75\n"
      "v_max_f32 v190, v190, v83\n"
      "v_max_f32 v191, v191, v91\n"
      "v_mov_b32 v192, v188\n"
      "v_mov_b32 v193, v189\n"
      "v_mov_b32 v194, v190\n"
      "v_mov_b32 v195, v191\n"
      "v_permlane16_swap_b32 v188, v192\n"
      "v_permlane16_swap_b32 v189, v193\n"
      "v_permlane16_swap_b32 v190, v194\n"
      "v_permlane16_swap_b32 v191, v195\n"
      "v_max_f32 v188, v188, v192\n"
      "v_max_f32 v189, v189, v193\n"
      "v_max_f32 v190, v190, v194\n"
      "v_max_f32 v191, v191, v195\n"
      "v_mov_b32 v192, v188\n"
      "v_mov_b32 v193, v189\n"
      "v_mov_b32 v194, v190\n"
      "v_mov_b32 v195, v191\n"
      "v_permlane32_swap_b32 v188, v192\n"
      "v_permlane32_swap_b32 v189, v193\n"
      "v_permlane32_swap_b32 v190, v194\n"
      "v_permlane32_swap_b32 v191, v195\n"
      "v_max_f32 v188, v188, v192\n"
      "v_max_f32 v189, v189, v193\n"
      "v_max_f32 v190, v190, v194\n"
      "v_max_f32 v191, v191, v195\n"
      "v_xor_b32 v176, 0x80000000, v188\n"
      "v_xor_b32 v177, 0x80000000, v189\n"
      "v_xor_b32 v178, 0x80000000, v190\n"
      "v_xor_b32 v179, 0x80000000, v191\n"
      "v_mov_b32 v160, v176\n"
      "v_mov_b32 v161, v176\n"
      "v_mov_b32 v162, v176\n"
      "v_mov_b32 v163, v176\n"
      "v_mov_b32 v164, v177\n"
      "v_mov_b32 v165, v177\n"
      "v_mov_b32 v166, v177\n"
      "v_mov_b32 v167, v177\n"
      "v_mov_b32 v168, v178\n"
      "v_mov_b32 v169, v178\n"
      "v_mov_b32 v170, v178\n"
      "v_mov_b32 v171, v178\n"
      "v_mov_b32 v172, v179\n"
      "v_mov_b32 v173, v179\n"
      "v_mov_b32 v174, v179\n"
      "v_mov_b32 v175, v179\n"
      "s_cmp_lg_u32 %[other], 0\n"
      "s_cbranch_scc1 NOTREE_%=\n"
      "s_waitcnt lgkmcnt(0)\n"
      "v_mfma_f32_16x16x32_bf16 v[96:99], a[64:67], a[0:3], v[160:163]\n"
      "v_lshlrev_b32 v64, 16, v200\n"
      "v_and_b32 v65, 0xffff0000, v200\n"
      "v_mfma_f32_16x16x32_bf16 v[96:99], a[72:75], a[16:19], v[96:99]\n"
      "v_lshlrev_b32 v66, 16, v201\n"
      "v_and_b32 v67, 0xffff0000, v201\n"
      "v_mfma_f32_16x16x32_bf16 v[96:99], a[80:83], a[32:35], v[96:99]\n"
      "v_lshlrev_b32 v68, 16, v202\n"
      "v_and_b32 v69, 0xffff0000, v202\n"
      "v_mfma_f32_16x16x32_bf16 v[96:99], a[88:91], a[48:51], v[96:99]\n"
      "v_lshlrev_b32 v70, 16, v203\n"
      "v_and_b32 v71, 0xffff0000, v203\n"
      "v_mfma_f32_16x16x32_bf16 v[104:107], a[64:67], a[4:7], v[164:167]\n"
      "v_lshlrev_b32 v72, 16, v204\n"
      "v_and_b32 v73, 0xffff0000, v204\n"
      "v_mfma_f32_16x16x32_bf16 v[104:107], a[72:75], a[20:23], v[104:107]\n"
      "v_lshlrev_b32 v74, 16, v205\n"
      "v_and_b32 v75, 0xffff0000, v205\n"
      "v_mfma_f32_16x16x32_bf16 v[104:107], a[80:83], a[36:39], v[104:107]\n"
      "v_lshlrev_b32 v76, 16, v206\n"
      "v_and_b32 v77, 0xffff0000, v206\n"
      "v_mfma_f32_16x16x32_bf16 v[104:107], a[88:91], a[52:55], v[104:107]\n"
      "v_lshlrev_b32 v78, 16, v207\n"
      "v_and_b32 v79, 0xffff0000, v207\n"
      "v_mfma_f32_16x16x32_bf16 v[112:115], a[64:67], a[8:11], v[168:171]\n"
      "v_sub_f32 v64, 1.0, v64\n"
      "v_sub_f32 v65, 1.0, v65\n"
      "v_mfma_f32_16x16x32_bf16 v[112:115], a[72:75], a[24:27], v[112:115]\n"
      "v_sub_f32 v66, 1.0, v66\n"
      "v_sub_f32 v67, 1.0, v67\n"
      "v_mfma_f32_16x16x32_bf16 v[112:115], a[80:83], a[40:43], v[112:115]\n"
      "v_sub_f32 v68, 1.0, v68\n"
      "v_sub_f32 v69, 1.0, v69\n"
      "v_mfma_f32_16x16x32_bf16 v[112:115], a[88:91], a[56:59], v[112:115]\n"
      "v_sub_f32 v70, 1.0, v70\n"
      "v_sub_f32 v71, 1.0, v71\n"
      "v_mfma_f32_16x16x32_bf16 v[120:123], a[64:67], a[12:15], v[172:175]\n"
      "v_sub_f32 v72, 1.0, v72\n"
      "v_sub_f32 v73, 1.0, v73\n"
      "v_mfma_f32_16x16x32_bf16 v[120:123], a[72:75], a[28:31], v[120:123]\n"
      "v_sub_f32 v74, 1.0, v74\n"
      "v_sub_f32 v75, 1.0, v75\n"
      "v_mfma_f32_16x16x32_bf16 v[120:123], a[80:83], a[44:47], v[120:123]\n"
      "v_sub_f32 v76, 1.0, v76\n"
      "v_sub_f32 v77, 1.0, v77\n"
      "v_mfma_f32_16x16x32_bf16 v[120:123], a[88:91], a[60:63], v[120:123]\n"
      "v_sub_f32 v78, 1.0, v78\n"
      "v_sub_f32 v79, 1.0, v79\n"
      "v_mfma_f32_16x16x32_bf16 v[100:103], a[68:71], a[0:3], v[160:163]\n"
      "v_mfma_f32_16x16x32_bf16 v[100:103], a[76:79], a[16:19], v[100:103]\n"
      "v_mfma_f32_16x16x32_bf16 v[100:103], a[84:87], a[32:35], v[100:103]\n"
      "v_mfma_f32_16x16x32_bf16 v[100:103], a[92:95], a[48:51], v[100:103]\n"
      "v_mfma_f32_16x16x32_bf16 v[108:111], a[68:71], a[4:7], v[164:167]\n"
      "v_mfma_f32_16x16x32_bf16 v[108:111], a[76:79], a[20:23], v[108:111]\n"
      "v_mfma_f32_16x16x32_bf16 v[108:111], a[84:87], a[36:39], v[108:111]\n"
      "v_mfma_f32_16x16x32_bf16 v[108:111], a[92:95], a[52:55], v[108:111]\n"
      "v_mfma_f32_16x16x32_bf16 v[116:119], a[68:71], a[8:11], v[168:171]\n"
      "v_mfma_f32_16x16x32_bf16 v[116:119], a[76:79], a[24:27], v[116:119]\n"
      "v_mfma_f32_16x16x32_bf16 v[116:119], a[84:87], a[40:43], v[116:119]\n"
      "v_mfma_f32_16x16x32_bf16 v[116:119], a[92:95], a[56:59], v[116:119]\n"
      "v_mfma_f32_16x16x32_bf16 v[124:127], a[68:71], a[12:15], v[172:175]\n"
      "v_mfma_f32_16x16x32_bf16 v[124:127], a[76:79], a[28:31], v[124:127]\n"
      "v_mfma_f32_16x16x32_bf16 v[124:127], a[84:87], a[44:47], v[124:127]\n"
      "v_mfma_f32_16x16x32_bf16 v[124:127], a[92:95], a[60:63], v[124:127]\n"
      "v_fmamk_f32 v96, v64, 0xceabfb8f, v96\n"
      "v_fmamk_f32 v97, v65, 0xceabfb8f, v97\n"
      "v_fmamk_f32 v98, v66, 0xceabfb8f, v98\n"
      "v_fmamk_f32 v99, v67, 0xceabfb8f, v99\n"
      "v_fmamk_f32 v100, v68, 0xceabfb8f, v100\n"
      "v_fmamk_f32 v101, v69, 0xceabfb8f, v101\n"
      "v_fmamk_f32 v102, v70, 0xceabfb8f, v102\n"
      "v_fmamk_f32 v103, v71, 0xceabfb8f, v103\n"
      "v_fmamk_f32 v104, v72, 0xceabfb8f, v104\n"
      "v_fmamk_f32 v105, v73, 0xceabfb8f, v105\n"
      "v_fmamk_f32 v106, v74, 0xceabfb8f, v106\n"
      "v_fmamk_f32 v107, v75, 0xceabfb8f, v107\n"
      "v_fmamk_f32 v108, v76, 0xceabfb8f, v108\n"
      "v_fmamk_f32 v109, v77, 0xceabfb8f, v109\n"
      "v_fmamk_f32 v110, v78, 0xceabfb8f, v110\n"
      "v_fmamk_f32 v111, v79, 0xceabfb8f, v111\n"
      "v_fmamk_f32 v112, v64, 0xceabfb8f, v112\n"
      "v_fmamk_f32 v113, v65, 0xceabfb8f, v113\n"
      "v_fmamk_f32 v114, v66, 0xceabfb8f, v114\n"
      "v_fmamk_f32 v115, v67, 0xceabfb8f, v115\n"
      "v_fmamk_f32 v116, v68, 0xceabfb8f, v116\n"
      "v_fmamk_f32 v117, v69, 0xceabfb8f, v117\n"
      "v_fmamk_f32 v118, v70, 0xceabfb8f, v118\n"
      "v_fmamk_f32 v119, v71, 0xceabfb8f, v119\n"
      "v_fmamk_f32 v120, v72, 0xceabfb8f, v120\n"
      "v_fmamk_f32 v121, v73, 0xceabfb8f, v121\n"
      "v_fmamk_f32 v122, v74, 0xceabfb8f, v122\n"
      "v_fmamk_f32 v123, v75, 0xceabfb8f, v123\n"
      "v_fmamk_f32 v124, v76, 0xceabfb8f, v124\n"
      "v_fmamk_f32 v125, v77, 0xceabfb8f, v125\n"
      "v_fmamk_f32 v126, v78, 0xceabfb8f, v126\n"
      "v_fmamk_f32 v127, v79, 0xceabfb8f, v127\n"
      "NOTREE_%=:\n"
      "P5_%=:\n"
      "s_memrealtime s[68:69]\n"
      "s_waitcnt lgkmcnt(0)\n"
      "s_sub_u32 s67, s68, s93\n"
      "s_cmp_lt_i32 s67, 108\n"
      "s_cbranch_scc1 P5_%=\n"
      "s_mov_b32 m0, s62\n"
      "s_nop 0\n"
      "buffer_load_dwordx4 v248, s[56:59], s60 offen nt lds\n"
      "buffer_load_dwordx4 v248, s[56:59], s60 offen offset:2048 nt lds\n"
      "s_bitset1_b32 m0, 10\n"
      "s_nop 0\n"
      "buffer_load_dwordx4 v249, s[56:59], s60 offen nt lds\n"
      "buffer_load_dwordx4 v249, s[56:59], s60 offen offset:2048 nt lds\n"
      "v_mov_b32 v209, 139248\n"
      "v_mov_b32 v196, 0\n"
      "v_lshl_add_u32 v232, %[wb], 1, v241\n"
      "v_add_u32 v232, 0x10000, v232\n"
      "v_xor_b32 v233, 0x4000, v232\n"
      "v_add_u32 v234, 0x10000, v232\n"
      "v_xor_b32 v235, 0x4000, v234\n"
      "v_lshrrev_b32 v240, 2, v241\n"
      "v_sub_u32 v240, v241, v240\n"
      "v_add_u32 v240, 0x1000, v240\n"
      "s_waitcnt vmcnt(14) lgkmcnt(0)\n"
      "s_sub_u32 s67, s68, s88\n"
      "s_cmp_lt_i32 s67, 0\n"
      "s_cbranch_scc1 GW0_%=\n"
      "GX0_%=:\n"
      "s_barrier\n"
      "ds_read_b128 a[64:67], v242 offset:0\n"
      "ds_read_b128 a[68:71], v242 offset:4096\n"
      "ds_read_b128 a[72:75], v243 offset:0\n"
      "ds_read_b128 a[76:79], v243 offset:4096\n"
      "ds_read_b128 a[80:83], v242 offset:1024\n"
      "ds_read_b128 a[84:87], v242 offset:5120\n"
      "ds_read_b128 a[88:91], v243 offset:1024\n"
      "ds_read_b128 a[92:95], v243 offset:5120\n"
      "ds_write_b32 v209, v196\n"
      "s_add_u32 %[mA], %[mA], %[mA]\n"
      "s_memrealtime s[68:69]\n"
      "s_xor_b64 s[56:57], s[56:57], %[dlt]\n"
      "s_bitset1_b32 s61, 15\n"
      "s_add_u32 s61, s61, 0x2000\n"
      "s_bitset0_b32 s61, 15\n"
      "s_xor_b32 s60, s61, %[par]\n"
      "s_add_u32 s62, s62, 0x4000\n"
      "s_and_b32 s62, s62, 0x1ffff\n"
      "s_waitcnt lgkmcnt(0)\n"
      "s_sub_u32 s67, s68, s66\n"
      "s_cmp_gt_i32 s67, s70\n"
      "s_cselect_b32 s69, s77, s86\n"
      "s_add_u32 s66, s66, s69\n"
      "s_add_u32 s67, s67, 50\n"
      "s_min_i32 s70, s70, s67\n"
      "s_max_i32 s70, s70, s80\n"
      "LOOP_%=:\n"
      "v_mfma_f32_16x16x32_bf16 v[64:67], a[64:67], a[0:3], v[160:163]\n"
      "s_sub_u32 s67, s68, s82\n"
      "v_exp_f32 v188, v96\n"
      "v_mfma_f32_16x16x32_bf16 v[64:67], a[72:75], a[16:19], v[64:67]\n"
      "s_min_i32 s76, s67, s83\n"
      "v_add_f32 v180, v180, v184\n"
      "v_add_f32 v181, v181, v185\n"
      "v_mfma_f32_16x16x32_bf16 v[64:67], a[80:83], a[32:35], v[64:67]\n"
      "s_mov_b32 s83, s67\n"
      "v_exp_f32 v189, v97\n"
      "v_mfma_f32_16x16x32_bf16 v[64:67], a[88:91], a[48:51], v[64:67]\n"
      "s_sub_u32 s76, s76, 14\n"
      "v_add_f32 v182, v182, v186\n"
      "v_add_f32 v183, v183, v187\n"
      "v_mfma_f32_16x16x32_bf16 v[72:75], a[64:67], a[4:7], v[164:167]\n"
      "s_min_i32 s76, s76, s87\n"
      "v_exp_f32 v190, v98\n"
      "v_mfma_f32_16x16x32_bf16 v[72:75], a[72:75], a[20:23], v[72:75]\n"
      "s_max_i32 s76, s76, s90\n"
      "v_exp_f32 v191, v99\n"
      "v_mfma_f32_16x16x32_bf16 v[72:75], a[80:83], a[36:39], v[72:75]\n"
      "s_mov_b32 s82, s68\n"
      "v_cvt_pk_bf16_f32 v144, v188, v189\n"
      "v_cvt_pk_bf16_f32 v145, v190, v191\n"
      "v_mfma_f32_16x16x32_bf16 v[72:75], a[88:91], a[52:55], v[72:75]\n"
      "s_add_u32 s65, s68, s76\n"
      "v_exp_f32 v192, v100\n"
      "v_mfma_f32_16x16x32_bf16 v[80:83], a[64:67], a[8:11], v[168:171]\n"
      "s_max_u32 s88, s66, s65\n"
      "v_exp_f32 v193, v101\n"
      "v_mfma_f32_16x16x32_bf16 v[80:83], a[72:75], a[24:27], v[80:83]\n"
      "s_memrealtime s[68:69]\n"
      "v_exp_f32 v194, v102\n"
      "v_mfma_f32_16x16x32_bf16 v[80:83], a[80:83], a[40:43], v[80:83]\n"
      "s_mov_b32 m0, s62\n"
      "v_exp_f32 v195, v103\n"
      "v_mfma_f32_16x16x32_bf16 v[80:83], a[88:91], a[56:59], v[80:83]\n"
      "buffer_load_dwordx4 v248, s[56:59], s60 offen nt lds\n"
      "v_mfma_f32_16x16x32_bf16 v[88:91], a[64:67], a[12:15], v[172:175]\n"
      "buffer_load_dwordx4 v248, s[56:59], s60 offen offset:2048 nt lds\n"
      "v_mfma_f32_16x16x32_bf16 v[88:91], a[72:75], a[28:31], v[88:91]\n"
      "s_bitset1_b32 m0, 10\n"
      "s_add_u32 %[mB], %[mB], %[mB]\n"
      "v_cvt_pk_bf16_f32 v146, v192, v193\n"
      "v_mfma_f32_16x16x32_bf16 v[88:91], a[80:83], a[44:47], v[88:91]\n"
      "buffer_load_dwordx4 v249, s[56:59], s60 offen nt lds\n"
      "v_mfma_f32_16x16x32_bf16 v[88:91], a[88:91], a[60:63], v[88:91]\n"
      "buffer_load_dwordx4 v249, s[56:59], s60 offen offset:2048 nt lds\n"
      "s_cbranch_scc1 SP1_%=\n"
      "HD1_%=:\n"
      "s_cmp_lg_u32 s54, 0\n"
      "s_waitcnt vmcnt(12) lgkmcnt(0)\n"
      "s_cbranch_scc1 W1_%=\n"
      "s_waitcnt vmcnt(8)\n"
      "W1_%=:\n"
      "s_sub_u32 s67, s68, s88\n"
      "s_cmp_lt_i32 s67, 0\n"
      "s_cbranch_scc1 GW4_%=\n"
      "GX4_%=:\n"
      "s_barrier\n"
      "v_mfma_f32_16x16x32_bf16 v[68:71], a[68:71], a[0:3], v[160:163]\n"
      "s_memrealtime s[68:69]\n"
      "ds_read_b64_tr_b16 a[96:97], v244 offset:0\n"
      "ds_read_b64_tr_b16 a[98:99], v244 offset:4096\n"
      "v_mfma_f32_16x16x32_bf16 v[68:71], a[76:79], a[16:19], v[68:71]\n"
      "s_xor_b64 s[56:57], s[56:57], %[dlt]\n"
      "ds_read_b64_tr_b16 a[100:101], v245 offset:0\n"
      "ds_read_b64_tr_b16 a[102:103], v245 offset:4096\n"
      "v_mfma_f32_16x16x32_bf16 v[68:71], a[84:87], a[32:35], v[68:71]\n"
      "s_bitset1_b32 s61, 15\n"
      "ds_read_b64_tr_b16 a[104:105], v246 offset:0\n"
      "ds_read_b64_tr_b16 a[106:107], v246 offset:4096\n"
      "v_mfma_f32_16x16x32_bf16 v[68:71], a[92:95], a[48:51], v[68:71]\n"
      "s_add_u32 s61, s61, 0x2000\n"
      "ds_read_b64_tr_b16 a[108:109], v247 offset:0\n"
      "ds_read_b64_tr_b16 a[110:111], v247 offset:4096\n"
      "v_mfma_f32_16x16x32_bf16 v[76:79], a[68:71], a[4:7], v[164:167]\n"
      "s_bitset0_b32 s61, 15\n"
      "v_exp_f32 v188, v104\n"
      "v_mfma_f32_16x16x32_bf16 v[76:79], a[76:79], a[20:23], v[76:79]\n"
      "s_xor_b32 s60, s61, %[par]\n"
      "v_exp_f32 v189, v105\n"
      "v_mfma_f32_16x16x32_bf16 v[76:79], a[84:87], a[36:39], v[76:79]\n"
      "s_add_u32 s62, s62, 0x4000\n"
      "v_cvt_pk_bf16_f32 v147, v194, v195\n"
      "v_cvt_pk_bf16_f32 v148, v188, v189\n"
      "v_mfma_f32_16x16x32_bf16 v[76:79], a[92:95], a[52:55], v[76:79]\n"
      "s_and_b32 s62, s62, 0x1ffff\n"
      "v_exp_f32 v190, v106\n"
      "v_mfma_f32_16x16x32_bf16 v[184:187], a[128:131], v[144:147], 0\n"
      "v_exp_f32 v191, v107\n"
      "v_mfma_f32_16x16x32_bf16 v[84:87], a[68:71], a[8:11], v[168:171]\n"
      "v_exp_f32 v192, v108\n"
      "v_mfma_f32_16x16x32_bf16 v[84:87], a[76:79], a[24:27], v[84:87]\n"
      "v_exp_f32 v193, v109\n"
      "s_waitcnt lgkmcnt(0)\n"
      "v_mfma_f32_16x16x32_bf16 v[84:87], a[84:87], a[40:43], v[84:87]\n"
      "s_sub_u32 s67, s68, s66\n"
      "ds_read_b64_tr_b16 a[112:113], v244 offset:1024\n"
      "v_cvt_pk_bf16_f32 v149, v190, v191\n"
      "v_mfma_f32_16x16x32_bf16 v[84:87], a[92:95], a[56:59], v[84:87]\n"
      "s_cmp_gt_i32 s67, s70\n"
      "v_exp_f32 v194, v110\n"
      "v_mfma_f32_16x16x32_bf16 v[92:95], a[68:71], a[12:15], v[172:175]\n"
      "s_cselect_b32 s69, s77, s86\n"
      "ds_read_b64_tr_b16 a[114:115], v244 offset:5120\n"
      "v_cvt_pk_bf16_f32 v150, v192, v193\n"
      "v_mfma_f32_16x16x32_bf16 v[92:95], a[76:79], a[28:31], v[92:95]\n"
      "s_add_u32 s66, s66, s69\n"
      "v_exp_f32 v195, v111\n"
      "v_mfma_f32_16x16x32_bf16 v[92:95], a[84:87], a[44:47], v[92:95]\n"
      "s_add_u32 s67, s67, 50\n"
      "ds_read_b64_tr_b16 a[116:117], v245 offset:1024\n"
      "v_cvt_pk_bf16_f32 v151, v194, v195\n"
      "v_mfma_f32_16x16x32_bf16 v[92:95], a[92:95], a[60:63], v[92:95]\n"
      "s_min_i32 s70, s70, s67\n"
      "v_exp_f32 v188, v112\n"
      "v_mfma_f32_16x16x32_bf16 v[184:187], a[132:135], v[148:151], v[184:187]\n"
      "s_max_i32 s70, s70, s80\n"
      "ds_read_b64_tr_b16 a[118:119], v245 offset:5120\n"
      "ds_read_b64_tr_b16 a[120:121], v246 offset:1024\n"
      "v_mfma_f32_16x16x32_bf16 v[0:3], a[96:99], v[128:131], v[0:3]\n"
      "s_sub_u32 s67, s68, s82\n"
      "v_exp_f32 v189, v113\n"
      "v_mfma_f32_16x16x32_bf16 v[4:7], a[96:99], v[132:135], v[4:7]\n"
      "s_min_i32 s76, s67, s83\n"
      "ds_read_b64_tr_b16 a[122:123], v246 offset:5120\n"
      "v_cvt_pk_bf16_f32 v152, v188, v189\n"
      "v_mfma_f32_16x16x32_bf16 v[8:11], a[96:99], v[136:139], v[8:11]\n"
      "s_mov_b32 s83, s67\n"
      "v_exp_f32 v190, v114\n"
      "v_mfma_f32_16x16x32_bf16 v[12:15], a[96:99], v[140:143], v[12:15]\n"
      "s_sub_u32 s76, s76, 14\n"
      "ds_read_b64_tr_b16 a[124:125], v247 offset:1024\n"
      "ds_read_b64_tr_b16 a[126:127], v247 offset:5120\n"
      "v_mfma_f32_16x16x32_bf16 v[28:31], a[100:103], v[140:143], v[28:31]\n"
      "s_min_i32 s76, s76, s87\n"
      "v_exp_f32 v191, v115\n"
      "v_mfma_f32_16x16x32_bf16 v[24:27], a[100:103], v[136:139], v[24:27]\n"
      "s_max_i32 s76, s76, s90\n"
      "v_exp_f32 v192, v116\n"
      "v_mfma_f32_16x16x32_bf16 v[20:23], a[100:103], v[132:135], v[20:23]\n"
      "s_mov_b32 s82, s68\n"
      "v_exp_f32 v193, v117\n"
      "v_mfma_f32_16x16x32_bf16 v[16:19], a[100:103], v[128:131], v[16:19]\n"
      "s_add_u32 s65, s68, s76\n"
      "v_cvt_pk_bf16_f32 v153, v190, v191\n"
      "v_cvt_pk_bf16_f32 v154, v192, v193\n"
      "v_mfma_f32_16x16x32_bf16 a[144:147], a[104:107], v[128:131], a[144:147]\n"
      "s_max_u32 s88, s66, s65\n"
      "v_exp_f32 v194, v118\n"
      "v_mfma_f32_16x16x32_bf16 a[148:151], a[104:107], v[132:135], a[148:151]\n"
      "s_memrealtime s[68:69]\n"
      "v_exp_f32 v195, v119\n"
      "v_mfma_f32_16x16x32_bf16 a[152:155], a[104:107], v[136:139], a[152:155]\n"
      "s_mov_b32 m0, s62\n"
      "v_exp_f32 v188, v120\n"
      "v_mfma_f32_16x16x32_bf16 a[156:159], a[104:107], v[140:143], a[156:159]\n"
      "buffer_load_dwordx4 v248, s[56:59], s60 offen nt lds\n"
      "v_mfma_f32_16x16x32_bf16 a[172:175], a[108:111], v[140:143], a[172:175]\n"
      "buffer_load_dwordx4 v248, s[56:59], s60 offen offset:2048 nt lds\n"
      "v_mfma_f32_16x16x32_bf16 a[168:171], a[108:111], v[136:139], a[168:171]\n"
      "s_bitset1_b32 m0, 10\n"
      "s_add_u32 %[mA], %[mA], %[mA]\n"
      "v_cvt_pk_bf16_f32 v155, v194, v195\n"
      "v_mfma_f32_16x16x32_bf16 a[164:167], a[108:111], v[132:135], a[164:167]\n"
      "buffer_load_dwordx4 v249, s[56:59], s60 offen nt lds\n"
      "v_mfma_f32_16x16x32_bf16 v[184:187], a[136:139], v[152:155], v[184:187]\n"
      "buffer_load_dwordx4 v249, s[56:59], s60 offen offset:2048 nt lds\n"
      "s_cbranch_scc1 SP2_%=\n"
      "HD2_%=:\n"
      "s_cmp_lg_u32 s54, 0\n"
      "s_waitcnt vmcnt(12) lgkmcnt(0)\n"
      "s_cbranch_scc1 W2_%=\n"
      "s_waitcnt vmcnt(8)\n"
      "W2_%=:\n"
      "s_sub_u32 s67, s68, s88\n"
      "s_cmp_lt_i32 s67, 0\n"
      "s_cbranch_scc1 GW5_%=\n"
      "GX5_%=:\n"
      "s_barrier\n"
      "v_mfma_f32_16x16x32_bf16 a[160:163], a[108:111], v[128:131], a[160:163]\n"
      "s_memrealtime s[68:69]\n"
      "ds_read_b128 a[64:67], v242 offset:32768\n"
      "ds_read_b128 a[72:75], v243 offset:32768\n"
      "v_mfma_f32_16x16x32_bf16 v[32:35], a[112:115], v[128:131], v[32:35]\n"
      "s_xor_b64 s[56:57], s[56:57], %[dlt]\n"
      "ds_read_b128 a[80:83], v242 offset:33792\n"
      "ds_read_b128 a[88:91], v243 offset:33792\n"
      "v_mfma_f32_16x16x32_bf16 v[36:39], a[112:115], v[132:135], v[36:39]\n"
      "s_bitset1_b32 s61, 15\n"
      "v_exp_f32 v189, v121\n"
      "v_mfma_f32_16x16x32_bf16 v[40:43], a[112:115], v[136:139], v[40:43]\n"
      "s_add_u32 s61, s61, 0x2000\n"
      "v_exp_f32 v190, v122\n"
      "v_mfma_f32_16x16x32_bf16 v[44:47], a[112:115], v[140:143], v[44:47]\n"
      "s_bitset0_b32 s61, 15\n"
      "v_exp_f32 v191, v123\n"
      "v_mfma_f32_16x16x32_bf16 v[60:63], a[116:119], v[140:143], v[60:63]\n"
      "s_xor_b32 s60, s61, %[par]\n"
      "v_cvt_pk_bf16_f32 v156, v188, v189\n"
      "v_cvt_pk_bf16_f32 v157, v190, v191\n"
      "v_mfma_f32_16x16x32_bf16 v[56:59], a[116:119], v[136:139], v[56:59]\n"
      "s_add_u32 s62, s62, 0x4000\n"
      "v_exp_f32 v192, v124\n"
      "v_mfma_f32_16x16x32_bf16 v[52:55], a[116:119], v[132:135], v[52:55]\n"
      "s_and_b32 s62, s62, 0x1ffff\n"
      "v_exp_f32 v193, v125\n"
      "v_mfma_f32_16x16x32_bf16 v[48:51], a[116:119], v[128:131], v[48:51]\n"
      "v_exp_f32 v194, v126\n"
      "v_mfma_f32_16x16x32_bf16 a[176:179], a[120:123], v[128:131], a[176:179]\n"
      "v_exp_f32 v195, v127\n"
      "v_mfma_f32_16x16x32_bf16 a[180:183], a[120:123], v[132:135], a[180:183]\n"
      "v_cvt_pk_bf16_f32 v158, v192, v193\n"
      "v_cvt_pk_bf16_f32 v159, v194, v195\n"
      "s_waitcnt lgkmcnt(0)\n"
      "v_mfma_f32_16x16x32_bf16 a[184:187], a[120:123], v[136:139], a[184:187]\n"
      "s_sub_u32 s67, s68, s66\n"
      "ds_read_b128 a[68:71], v242 offset:36864\n"
      "ds_read_b128 a[76:79], v243 offset:36864\n"
      "v_mfma_f32_16x16x32_bf16 v[184:187], a[140:143], v[156:159], v[184:187]\n"
      "s_cmp_gt_i32 s67, s70\n"
      "ds_read_b128 a[84:87], v242 offset:37888\n"
      "ds_read_b128 a[92:95], v243 offset:37888\n"
      "v_mfma_f32_16x16x32_bf16 a[188:191], a[120:123], v[140:143], a[188:191]\n"
      "s_cselect_b32 s69, s77, s86\n"
      "v_mfma_f32_16x16x32_bf16 a[204:207], a[124:127], v[140:143], a[204:207]\n"
      "s_add_u32 s66, s66, s69\n"
      "v_mfma_f32_16x16x32_bf16 a[200:203], a[124:127], v[136:139], a[200:203]\n"
      "s_add_u32 s67, s67, 50\n"
      "v_max3_f32 v196, v184, v185, v186\n"
      "v_max_f32 v196, v196, v187\n"
      "v_mfma_f32_16x16x32_bf16 a[196:199], a[124:127], v[132:135], a[196:199]\n"
      "s_min_i32 s70, s70, s67\n"
      "v_cmp_nge_f32 vcc, 0x4b800000, v196\n"
      "v_mfma_f32_16x16x32_bf16 a[192:195], a[124:127], v[128:131], a[192:195]\n"
      "s_max_i32 s70, s70, s80\n"
      "s_cbranch_vccnz FS1_%=\n"
      "FR1_%=:\n"
      "v_mfma_f32_16x16x32_bf16 v[96:99], a[64:67], a[0:3], v[160:163]\n"
      "s_sub_u32 s67, s68, s82\n"
      "v_exp_f32 v188, v64\n"
      "v_mfma_f32_16x16x32_bf16 v[96:99], a[72:75], a[16:19], v[96:99]\n"
      "s_min_i32 s76, s67, s83\n"
      "v_add_f32 v180, v180, v184\n"
      "v_add_f32 v181, v181, v185\n"
      "v_mfma_f32_16x16x32_bf16 v[96:99], a[80:83], a[32:35], v[96:99]\n"
      "s_mov_b32 s83, s67\n"
      "v_exp_f32 v189, v65\n"
      "v_mfma_f32_16x16x32_bf16 v[96:99], a[88:91], a[48:51], v[96:99]\n"
      "s_sub_u32 s76, s76, 14\n"
      "v_add_f32 v182, v182, v186\n"
      "v_add_f32 v183, v183, v187\n"
      "v_mfma_f32_16x16x32_bf16 v[104:107], a[64:67], a[4:7], v[164:167]\n"
      "s_min_i32 s76, s76, s87\n"
      "v_exp_f32 v190, v66\n"
      "v_mfma_f32_16x16x32_bf16 v[104:107], a[72:75], a[20:23], v[104:107]\n"
      "s_max_i32 s76, s76, s90\n"
      "v_exp_f32 v191, v67\n"
      "v_mfma_f32_16x16x32_bf16 v[104:107], a[80:83], a[36:39], v[104:107]\n"
      "s_mov_b32 s82, s68\n"
      "v_cvt_pk_bf16_f32 v128, v188, v189\n"
      "v_cvt_pk_bf16_f32 v129, v190, v191\n"
      "v_mfma_f32_16x16x32_bf16 v[104:107], a[88:91], a[52:55], v[104:107]\n"
      "s_add_u32 s65, s68, s76\n"
      "v_exp_f32 v192, v68\n"
      "v_mfma_f32_16x16x32_bf16 v[112:115], a[64:67], a[8:11], v[168:171]\n"
      "s_max_u32 s88, s66, s65\n"
      "v_exp_f32 v193, v69\n"
      "v_mfma_f32_16x16x32_bf16 v[112:115], a[72:75], a[24:27], v[112:115]\n"
      "s_memrealtime s[68:69]\n"
      "v_exp_f32 v194, v70\n"
      "v_mfma_f32_16x16x32_bf16 v[112:115], a[80:83], a[40:43], v[112:115]\n"
      "s_mov_b32 m0, s62\n"
      "v_exp_f32 v195, v71\n"
      "v_mfma_f32_16x16x32_bf16 v[112:115], a[88:91], a[56:59], v[112:115]\n"
      "buffer_load_dwordx4 v248, s[56:59], s60 offen nt lds\n"
      "v_mfma_f32_16x16x32_bf16 v[120:123], a[64:67], a[12:15], v[172:175]\n"
      "buffer_load_dwordx4 v248, s[56:59], s60 offen offset:2048 nt lds\n"
      "v_mfma_f32_16x16x32_bf16 v[120:123], a[72:75], a[28:31], v[120:123]\n"
      "s_bitset1_b32 m0, 10\n"
      "s_add_u32 %[mB], %[mB], %[mB]\n"
      "v_cvt_pk_bf16_f32 v130, v192, v193\n"
      "v_mfma_f32_16x16x32_bf16 v[120:123], a[80:83], a[44:47], v[120:123]\n"
      "buffer_load_dwordx4 v249, s[56:59], s60 offen nt lds\n"
      "v_mfma_f32_16x16x32_bf16 v[120:123], a[88:91], a[60:63], v[120:123]\n"
      "buffer_load_dwordx4 v249, s[56:59], s60 offen offset:2048 nt lds\n"
      "s_cbranch_scc1 SP3_%=\n"
      "HD3_%=:\n"
      "s_cmp_lg_u32 s54, 0\n"
      "s_waitcnt vmcnt(12) lgkmcnt(0)\n"
      "s_cbranch_scc1 W3_%=\n"
      "s_waitcnt vmcnt(8)\n"
      "W3_%=:\n"
      "s_sub_u32 s67, s68, s88\n"
      "s_cmp_lt_i32 s67, 0\n"
      "s_cbranch_scc1 GW6_%=\n"
      "GX6_%=:\n"
      "s_barrier\n"
      "v_mfma_f32_16x16x32_bf16 v[100:103], a[68:71], a[0:3], v[160:163]\n"
      "s_memrealtime s[68:69]\n"
      "ds_read_b64_tr_b16 a[96:97], v244 offset:32768\n"
      "ds_read_b64_tr_b16 a[98:99], v244 offset:36864\n"
      "v_mfma_f32_16x16x32_bf16 v[100:103], a[76:79], a[16:19], v[100:103]\n"
      "s_xor_b64 s[56:57], s[56:57], %[dlt]\n"
      "ds_read_b64_tr_b16 a[100:101], v245 offset:32768\n"
      "ds_read_b64_tr_b16 a[102:103], v245 offset:36864\n"
      "v_mfma_f32_16x16x32_bf16 v[100:103], a[84:87], a[32:35], v[100:103]\n"
      "s_bitset1_b32 s61, 15\n"
      "ds_read_b64_tr_b16 a[104:105], v246 offset:32768\n"
      "ds_read_b64_tr_b16 a[106:107], v246 offset:36864\n"
      "v_mfma_f32_16x16x32_bf16 v[100:103], a[92:95], a[48:51], v[100:103]\n"
      "s_add_u32 s61, s61, 0x2000\n"
      "ds_read_b64_tr_b16 a[108:109], v247 offset:32768\n"
      "ds_read_b64_tr_b16 a[110:111], v247 offset:36864\n"
      "v_mfma_f32_16x16x32_bf16 v[108:111], a[68:71], a[4:7], v[164:167]\n"
      "s_bitset0_b32 s61, 15\n"
      "v_exp_f32 v188, v72\n"
      "v_mfma_f32_16x16x32_bf16 v[108:111], a[76:79], a[20:23], v[108:111]\n"
      "s_xor_b32 s60, s61, %[par]\n"
      "v_exp_f32 v189, v73\n"
      "v_mfma_f32_16x16x32_bf16 v[108:111], a[84:87], a[36:39], v[108:111]\n"
      "s_add_u32 s62, s62, 0x4000\n"
      "v_xor_b32 v242, 0x10000, v242\n"
      "v_xor_b32 v243, 0x10000, v243\n"
      "v_mfma_f32_16x16x32_bf16 v[108:111], a[92:95], a[52:55], v[108:111]\n"
      "s_and_b32 s62, s62, 0x1ffff\n"
      "v_exp_f32 v190, v74\n"
      "v_mfma_f32_16x16x32_bf16 v[116:119], a[68:71], a[8:11], v[168:171]\n"
      "v_cvt_pk_bf16_f32 v131, v194, v195\n"
      "v_cvt_pk_bf16_f32 v132, v188, v189\n"
      "v_mfma_f32_16x16x32_bf16 v[116:119], a[76:79], a[24:27], v[116:119]\n"
      "v_exp_f32 v191, v75\n"
      "v_mfma_f32_16x16x32_bf16 v[184:187], a[128:131], v[128:131], 0\n"
      "v_exp_f32 v192, v76\n"
      "s_waitcnt lgkmcnt(0)\n"
      "v_mfma_f32_16x16x32_bf16 v[116:119], a[84:87], a[40:43], v[116:119]\n"
      "s_sub_u32 s67, s68, s66\n"
      "ds_read_b64_tr_b16 a[112:113], v244 offset:33792\n"
      "v_cvt_pk_bf16_f32 v133, v190, v191\n"
      "v_mfma_f32_16x16x32_bf16 v[116:119], a[92:95], a[56:59], v[116:119]\n"
      "s_cmp_gt_i32 s67, s70\n"
      "v_exp_f32 v193, v77\n"
      "v_mfma_f32_16x16x32_bf16 v[124:127], a[68:71], a[12:15], v[172:175]\n"
      "s_cselect_b32 s69, s77, s86\n"
      "ds_read_b64_tr_b16 a[114:115], v244 offset:37888\n"
      "v_cvt_pk_bf16_f32 v134, v192, v193\n"
      "v_mfma_f32_16x16x32_bf16 v[124:127], a[76:79], a[28:31], v[124:127]\n"
      "s_add_u32 s66, s66, s69\n"
      "v_exp_f32 v194, v78\n"
      "v_mfma_f32_16x16x32_bf16 v[124:127], a[84:87], a[44:47], v[124:127]\n"
      "s_add_u32 s67, s67, 50\n"
      "ds_read_b64_tr_b16 a[116:117], v245 offset:33792\n"
      "ds_read_b64_tr_b16 a[118:119], v245 offset:37888\n"
      "v_mfma_f32_16x16x32_bf16 v[124:127], a[92:95], a[60:63], v[124:127]\n"
      "s_min_i32 s70, s70, s67\n"
      "v_exp_f32 v195, v79\n"
      "v_mfma_f32_16x16x32_bf16 v[0:3], a[96:99], v[144:147], v[0:3]\n"
      "s_max_i32 s70, s70, s80\n"
      "ds_read_b64_tr_b16 a[120:121], v246 offset:33792\n"
      "v_cvt_pk_bf16_f32 v135, v194, v195\n"
      "v_mfma_f32_16x16x32_bf16 v[4:7], a[96:99], v[148:151], v[4:7]\n"
      "s_sub_u32 s67, s68, s82\n"
      "v_exp_f32 v188, v80\n"
      "v_mfma_f32_16x16x32_bf16 v[184:187], a[132:135], v[132:135], v[184:187]\n"
      "s_min_i32 s76, s67, s83\n"
      "ds_read_b64_tr_b16 a[122:123], v246 offset:37888\n"
      "ds_read_b64_tr_b16 a[124:125], v247 offset:33792\n"
      "v_mfma_f32_16x16x32_bf16 v[8:11], a[96:99], v[152:155], v[8:11]\n"
      "s_mov_b32 s83, s67\n"
      "v_exp_f32 v189, v81\n"
      "v_mfma_f32_16x16x32_bf16 v[12:15], a[96:99], v[156:159], v[12:15]\n"
      "s_sub_u32 s76, s76, 14\n"
      "ds_read_b64_tr_b16 a[126:127], v247 offset:37888\n"
      "v_cvt_pk_bf16_f32 v136, v188, v189\n"
      "v_mfma_f32_16x16x32_bf16 v[28:31], a[100:103], v[156:159], v[28:31]\n"
      "s_min_i32 s76, s76, s87\n"
      "v_exp_f32 v190, v82\n"
      "v_mfma_f32_16x16x32_bf16 v[24:27], a[100:103], v[152:155], v[24:27]\n"
      "s_max_i32 s76, s76, s90\n"
      "v_exp_f32 v191, v83\n"
      "v_mfma_f32_16x16x32_bf16 v[20:23], a[100:103], v[148:151], v[20:23]\n"
      "s_mov_b32 s82, s68\n"
      "v_exp_f32 v192, v84\n"
      "v_mfma_f32_16x16x32_bf16 v[16:19], a[100:103], v[144:147], v[16:19]\n"
      "s_add_u32 s65, s68, s76\n"
      "v_exp_f32 v193, v85\n"
      "v_mfma_f32_16x16x32_bf16 a[144:147], a[104:107], v[144:147], a[144:147]\n"
      "s_max_u32 s88, s66, s65\n"
      "v_cvt_pk_bf16_f32 v137, v190, v191\n"
      "v_cvt_pk_bf16_f32 v138, v192, v193\n"
      "v_mfma_f32_16x16x32_bf16 a[148:151], a[104:107], v[148:151], a[148:151]\n"
      "s_memrealtime s[68:69]\n"
      "v_exp_f32 v194, v86\n"
      "v_mfma_f32_16x16x32_bf16 a[152:155], a[104:107], v[152:155], a[152:155]\n"
      "s_mov_b32 m0, s62\n"
      "v_exp_f32 v195, v87\n"
      "v_mfma_f32_16x16x32_bf16 a[156:159], a[104:107], v[156:159], a[156:159]\n"
      "buffer_load_dwordx4 v248, s[56:59], s60 offen nt lds\n"
      "v_mfma_f32_16x16x32_bf16 a[172:175], a[108:111], v[156:159], a[172:175]\n"
      "buffer_load_dwordx4 v248, s[56:59], s60 offen offset:2048 nt lds\n"
      "v_mfma_f32_16x16x32_bf16 a[168:171], a[108:111], v[152:155], a[168:171]\n"
      "s_bitset1_b32 m0, 10\n"
      "s_add_u32 %[mA], %[mA], %[mA]\n"
      "v_cvt_pk_bf16_f32 v139, v194, v195\n"
      "v_mfma_f32_16x16x32_bf16 a[164:167], a[108:111], v[148:151], a[164:167]\n"
      "buffer_load_dwordx4 v249, s[56:59], s60 offen nt lds\n"
      "v_mfma_f32_16x16x32_bf16 v[184:187], a[136:139], v[136:139], v[184:187]\n"
      "buffer_load_dwordx4 v249, s[56:59], s60 offen offset:2048 nt lds\n"
      "HD0_%=:\n"
      "s_cmp_lg_u32 s54, 0\n"
      "s_waitcnt vmcnt(12) lgkmcnt(0)\n"
      "s_cbranch_scc1 W4_%=\n"
      "s_waitcnt vmcnt(8)\n"
      "W4_%=:\n"
      "s_sub_u32 s67, s68, s88\n"
      "s_cmp_lt_i32 s67, 0\n"
      "s_cbranch_scc1 GW7_%=\n"
      "GX7_%=:\n"
      "s_barrier\n"
      "v_mfma_f32_16x16x32_bf16 a[160:163], a[108:111], v[144:147], a[160:163]\n"
      "s_memrealtime s[68:69]\n"
      "ds_read_b128 a[64:67], v242 offset:0\n"
      "ds_read_b128 a[72:75], v243 offset:0\n"
      "v_mfma_f32_16x16x32_bf16 v[32:35], a[112:115], v[144:147], v[32:35]\n"
      "s_xor_b64 s[56:57], s[56:57], %[dlt]\n"
      "ds_read_b128 a[80:83], v242 offset:1024\n"
      "ds_read_b128 a[88:91], v243 offset:1024\n"
      "v_mfma_f32_16x16x32_bf16 v[36:39], a[112:115], v[148:151], v[36:39]\n"
      "s_bitset1_b32 s61, 15\n"
      "v_exp_f32 v188, v88\n"
      "v_mfma_f32_16x16x32_bf16 v[40:43], a[112:115], v[152:155], v[40:43]\n"
      "s_add_u32 s61, s61, 0x2000\n"
      "v_exp_f32 v189, v89\n"
      "v_mfma_f32_16x16x32_bf16 v[44:47], a[112:115], v[156:159], v[44:47]\n"
      "s_bitset0_b32 s61, 15\n"
      "v_exp_f32 v190, v90\n"
      "v_mfma_f32_16x16x32_bf16 v[60:63], a[116:119], v[156:159], v[60:63]\n"
      "s_xor_b32 s60, s61, %[par]\n"
      "v_exp_f32 v191, v91\n"
      "v_mfma_f32_16x16x32_bf16 v[56:59], a[116:119], v[152:155], v[56:59]\n"
      "s_add_u32 s62, s62, 0x4000\n"
      "v_cvt_pk_bf16_f32 v140, v188, v189\n"
      "v_cvt_pk_bf16_f32 v141, v190, v191\n"
      "v_mfma_f32_16x16x32_bf16 v[52:55], a[116:119], v[148:151], v[52:55]\n"
      "s_and_b32 s62, s62, 0x1ffff\n"
      "v_exp_f32 v192, v92\n"
      "v_mfma_f32_16x16x32_bf16 v[48:51], a[116:119], v[144:147], v[48:51]\n"
      "v_exp_f32 v193, v93\n"
      "v_mfma_f32_16x16x32_bf16 a[176:179], a[120:123], v[144:147], a[176:179]\n"
      "v_exp_f32 v194, v94\n"
      "v_mfma_f32_16x16x32_bf16 a[180:183], a[120:123], v[148:151], a[180:183]\n"
      "v_exp_f32 v195, v95\n"
      "s_waitcnt lgkmcnt(0)\n"
      "v_mfma_f32_16x16x32_bf16 a[184:187], a[120:123], v[152:155], a[184:187]\n"
      "s_sub_u32 s67, s68, s66\n"
      "v_cvt_pk_bf16_f32 v142, v192, v193\n"
      "v_cvt_pk_bf16_f32 v143, v194, v195\n"
      "v_mfma_f32_16x16x32_bf16 a[188:191], a[120:123], v[156:159], a[188:191]\n"
      "s_cmp_gt_i32 s67, s70\n"
      "ds_read_b128 a[68:71], v242 offset:4096\n"
      "ds_read_b128 a[76:79], v243 offset:4096\n"
      "v_mfma_f32_16x16x32_bf16 v[184:187], a[140:143], v[140:143], v[184:187]\n"
      "s_cselect_b32 s69, s77, s86\n"
      "ds_read_b128 a[84:87], v242 offset:5120\n"
      "ds_read_b128 a[92:95], v243 offset:5120\n"
      "v_mfma_f32_16x16x32_bf16 a[204:207], a[124:127], v[156:159], a[204:207]\n"
      "s_add_u32 s66, s66, s69\n"
      "v_xor_b32 v244, 0x10000, v244\n"
      "v_xor_b32 v245, 0x10000, v245\n"
      "v_mfma_f32_16x16x32_bf16 a[200:203], a[124:127], v[152:155], a[200:203]\n"
      "s_add_u32 s67, s67, 50\n"
      "v_xor_b32 v246, 0x10000, v246\n"
      "v_xor_b32 v247, 0x10000, v247\n"
      "v_mfma_f32_16x16x32_bf16 a[196:199], a[124:127], v[148:151], a[196:199]\n"
      "s_min_i32 s70, s70, s67\n"
      "v_max3_f32 v196, v184, v185, v186\n"
      "v_max_f32 v196, v196, v187\n"
      "v_mfma_f32_16x16x32_bf16 a[192:195], a[124:127], v[144:147], a[192:195]\n"
      "s_max_i32 s70, s70, s80\n"
      "v_cmp_nge_f32 vcc, 0x4b800000, v196\n"
      "s_cbranch_vccnz FS2_%=\n"
      "FR2_%=:\n"
      "s_branch LOOP_%=\n"
      "SP1_%=:\n"
      "s_ff1_i32_b32 s64, %[mB]\n"
      "s_lshl1_add_u32 s64, s64, 1\n"
      "s_bitcmp1_b64 s[52:53], s64\n"
      "s_cbranch_scc0 SH1_%=\n"
      "s_swappc_b64 s[84:85], s[84:85]\n"
      "SH1_%=:\n"
      "s_cmp_lt_u32 s64, 63\n"
      "s_cbranch_scc1 HD1_%=\n"
      "s_xor_b64 s[56:57], s[56:57], %[dlt]\n"
      "s_bitset1_b32 s61, 15\n"
      "s_add_u32 s61, s61, 0x2000\n"
      "s_bitset0_b32 s61, 15\n"
      "s_xor_b32 s60, s61, %[par]\n"
      "s_add_u32 s62, s62, 0x4000\n"
      "s_and_b32 s62, s62, 0x1ffff\n"
      "v_add_u32 v248, %[par], v249\n"
      "s_mov_b32 %[par], 0\n"
      "v_xor_b32 v249, 0x4000, v248\n"
      "s_branch HD1_%=\n"
      "SP2_%=:\n"
      "s_cmp_eq_u32 %[mA], 0\n"
      "s_cbranch_scc1 L62_%=\n"
      "s_ff1_i32_b32 s64, %[mA]\n"
      "s_lshl1_add_u32 s64, s64, 0\n"
      "s_bitcmp1_b64 s[52:53], s64\n"
      "s_cbranch_scc0 SH2_%=\n"
      "s_swappc_b64 s[84:85], s[84:85]\n"
      "SH2_%=:\n"
      "s_branch HD2_%=\n"
      "L62_%=:\n"
      "s_xor_b64 s[56:57], s[56:57], %[dlt]\n"
      "s_sub_u32 s61, s61, 0x2000\n"
      "v_xor_b32 v248, 128, v248\n"
      "v_xor_b32 v249, 128, v249\n"
      "s_mov_b32 %[mA], 0x80000000\n"
      "s_branch HD2_%=\n"
      "SP3_%=:\n"
      "s_cmp_eq_u32 %[mB], 0\n"
      "s_cbranch_scc1 NH_%=\n"
      "s_ff1_i32_b32 s64, %[mB]\n"
      "s_lshl1_add_u32 s64, s64, 1\n"
      "s_bitcmp1_b64 s[52:53], s64\n"
      "s_cbranch_scc0 SH3_%=\n"
      "s_swappc_b64 s[84:85], s[84:85]\n"
      "SH3_%=:\n"
      "s_bitcmp1_b64 %[mN], s64\n"
      "s_cbranch_scc0 HD3_%=\n"
      "NH_%=:\n"
      "s_cmp_lg_u32 s54, 0\n"
      "s_waitcnt vmcnt(12) lgkmcnt(0)\n"
      "s_cbranch_scc1 W5_%=\n"
      "s_waitcnt vmcnt(8)\n"
      "W5_%=:\n"
      "s_barrier\n"
      "v_mfma_f32_16x16x32_bf16 v[100:103], a[68:71], a[0:3], v[160:163]\n"
      "s_sub_u32 s54, s54, 1\n"
      "ds_read_b64_tr_b16 a[96:97], v244 offset:32768\n"
      "ds_read_b64_tr_b16 a[98:99], v244 offset:36864\n"
      "v_mfma_f32_16x16x32_bf16 v[100:103], a[76:79], a[16:19], v[100:103]\n"
      "ds_read_b64_tr_b16 a[100:101], v245 offset:32768\n"
      "ds_read_b64_tr_b16 a[102:103], v245 offset:36864\n"
      "v_mfma_f32_16x16x32_bf16 v[100:103], a[84:87], a[32:35], v[100:103]\n"
      "ds_read_b64_tr_b16 a[104:105], v246 offset:32768\n"
      "ds_read_b64_tr_b16 a[106:107], v246 offset:36864\n"
      "v_mfma_f32_16x16x32_bf16 v[100:103], a[92:95], a[48:51], v[100:103]\n"
      "ds_read_b64_tr_b16 a[108:109], v247 offset:32768\n"
      "ds_read_b64_tr_b16 a[110:111], v247 offset:36864\n"
      "v_mfma_f32_16x16x32_bf16 v[108:111], a[68:71], a[4:7], v[164:167]\n"
      "v_exp_f32 v188, v72\n"
      "v_mfma_f32_16x16x32_bf16 v[108:111], a[76:79], a[20:23], v[108:111]\n"
      "v_exp_f32 v189, v73\n"
      "v_mfma_f32_16x16x32_bf16 v[108:111], a[84:87], a[36:39], v[108:111]\n"
      "v_xor_b32 v242, 0x10000, v242\n"
      "v_xor_b32 v243, 0x10000, v243\n"
      "v_mfma_f32_16x16x32_bf16 v[108:111], a[92:95], a[52:55], v[108:111]\n"
      "v_exp_f32 v190, v74\n"
      "v_mfma_f32_16x16x32_bf16 v[116:119], a[68:71], a[8:11], v[168:171]\n"
      "v_cvt_pk_bf16_f32 v131, v194, v195\n"
      "v_cvt_pk_bf16_f32 v132, v188, v189\n"
      "v_mfma_f32_16x16x32_bf16 v[116:119], a[76:79], a[24:27], v[116:119]\n"
      "v_exp_f32 v191, v75\n"
      "v_mfma_f32_16x16x32_bf16 v[184:187], a[128:131], v[128:131], 0\n"
      "v_exp_f32 v192, v76\n"
      "s_waitcnt lgkmcnt(0)\n"
      "v_mfma_f32_16x16x32_bf16 v[116:119], a[84:87], a[40:43], v[116:119]\n"
      "ds_read_b64_tr_b16 a[112:113], v244 offset:33792\n"
      "v_cvt_pk_bf16_f32 v133, v190, v191\n"
      "v_mfma_f32_16x16x32_bf16 v[116:119], a[92:95], a[56:59], v[116:119]\n"
      "v_exp_f32 v193, v77\n"
      "v_mfma_f32_16x16x32_bf16 v[124:127], a[68:71], a[12:15], v[172:175]\n"
      "ds_read_b64_tr_b16 a[114:115], v244 offset:37888\n"
      "v_cvt_pk_bf16_f32 v134, v192, v193\n"
      "v_mfma_f32_16x16x32_bf16 v[124:127], a[76:79], a[28:31], v[124:127]\n"
      "v_exp_f32 v194, v78\n"
      "v_mfma_f32_16x16x32_bf16 v[124:127], a[84:87], a[44:47], v[124:127]\n"
      "ds_read_b64_tr_b16 a[116:117], v245 offset:33792\n"
      "ds_read_b64_tr_b16 a[118:119], v245 offset:37888\n"
      "v_mfma_f32_16x16x32_bf16 v[124:127], a[92:95], a[60:63], v[124:127]\n"
      "v_exp_f32 v195, v79\n"
      "v_mfma_f32_16x16x32_bf16 v[0:3], a[96:99], v[144:147], v[0:3]\n"
      "ds_read_b64_tr_b16 a[120:121], v246 offset:33792\n"
      "v_cvt_pk_bf16_f32 v135, v194, v195\n"
      "v_mfma_f32_16x16x32_bf16 v[4:7], a[96:99], v[148:151], v[4:7]\n"
      "v_exp_f32 v188, v80\n"
      "v_mfma_f32_16x16x32_bf16 v[184:187], a[132:135], v[132:135], v[184:187]\n"
      "ds_read_b64_tr_b16 a[122:123], v246 offset:37888\n"
      "ds_read_b64_tr_b16 a[124:125], v247 offset:33792\n"
      "v_mfma_f32_16x16x32_bf16 v[8:11], a[96:99], v[152:155], v[8:11]\n"
      "v_exp_f32 v189, v81\n"
      "v_mfma_f32_16x16x32_bf16 v[12:15], a[96:99], v[156:159], v[12:15]\n"
      "ds_read_b64_tr_b16 a[126:127], v247 offset:37888\n"
      "v_cvt_pk_bf16_f32 v136, v188, v189\n"
      "v_mfma_f32_16x16x32_bf16 v[28:31], a[100:103], v[156:159], v[28:31]\n"
      "v_exp_f32 v190, v82\n"
      "v_mfma_f32_16x16x32_bf16 v[24:27], a[100:103], v[152:155], v[24:27]\n"
      "v_exp_f32 v191, v83\n"
      "v_mfma_f32_16x16x32_bf16 v[20:23], a[100:103], v[148:151], v[20:23]\n"
      "v_exp_f32 v192, v84\n"
      "v_mfma_f32_16x16x32_bf16 v[16:19], a[100:103], v[144:147], v[16:19]\n"
      "v_exp_f32 v193, v85\n"
      "v_mfma_f32_16x16x32_bf16 a[144:147], a[104:107], v[144:147], a[144:147]\n"
      "v_cvt_pk_bf16_f32 v137, v190, v191\n"
      "v_cvt_pk_bf16_f32 v138, v192, v193\n"
      "v_mfma_f32_16x16x32_bf16 a[148:151], a[104:107], v[148:151], a[148:151]\n"
      "s_memrealtime s[68:69]\n"
      "v_exp_f32 v194, v86\n"
      "v_mfma_f32_16x16x32_bf16 a[152:155], a[104:107], v[152:155], a[152:155]\n"
      "v_exp_f32 v195, v87\n"
      "v_mfma_f32_16x16x32_bf16 a[156:159], a[104:107], v[156:159], a[156:159]\n"
      "s_nop 0\n"
      "v_mfma_f32_16x16x32_bf16 a[172:175], a[108:111], v[156:159], a[172:175]\n"
      "s_nop 0\n"
      "v_mfma_f32_16x16x32_bf16 a[168:171], a[108:111], v[152:155], a[168:171]\n"
      "s_add_u32 %[mA], %[mA], %[mA]\n"
      "v_cvt_pk_bf16_f32 v139, v194, v195\n"
      "v_mfma_f32_16x16x32_bf16 a[164:167], a[108:111], v[148:151], a[164:167]\n"
      "s_nop 0\n"
      "v_mfma_f32_16x16x32_bf16 v[184:187], a[136:139], v[136:139], v[184:187]\n"
      "s_nop 0\n"
      "s_cbranch_scc0 MH_%=\n"
      "END_%=:\n"
      "v_xor_b32 v244, 0x10000, v244\n"
      "v_xor_b32 v245, 0x10000, v245\n"
      "v_xor_b32 v246, 0x10000, v246\n"
      "v_xor_b32 v247, 0x10000, v247\n"
      "v_xor_b32 v250, 0x2000, v244\n"
      "v_xor_b32 v251, 0x2000, v245\n"
      "s_waitcnt lgkmcnt(0)\n"
      "v_mfma_f32_16x16x32_bf16 a[160:163], a[108:111], v[144:147], a[160:163]\n"
      "s_mov_b32 s74, 0xffff0000\n"
      "v_exp_f32 v188, v88\n"
      "v_mfma_f32_16x16x32_bf16 v[32:35], a[112:115], v[144:147], v[32:35]\n"
      "s_mov_b32 s75, 0xffff0000\n"
      "v_exp_f32 v189, v89\n"
      "v_mfma_f32_16x16x32_bf16 v[36:39], a[112:115], v[148:151], v[36:39]\n"
      "s_mov_b32 s94, 0\n"
      "v_exp_f32 v190, v90\n"
      "v_mfma_f32_16x16x32_bf16 v[40:43], a[112:115], v[152:155], v[40:43]\n"
      "s_mov_b32 s95, -1\n"
      "v_exp_f32 v191, v91\n"
      "v_mfma_f32_16x16x32_bf16 v[44:47], a[112:115], v[156:159], v[44:47]\n"
      "s_add_u32 %[e0], %[e0], 0x7f4a7c15\n"
      "v_cvt_pk_bf16_f32 v140, v188, v189\n"
      "v_cvt_pk_bf16_f32 v141, v190, v191\n"
      "v_mfma_f32_16x16x32_bf16 v[60:63], a[116:119], v[156:159], v[60:63]\n"
      "v_exp_f32 v192, v92\n"
      "s_waitcnt vmcnt(4)\n"
      "s_barrier\n"
      "s_memrealtime s[68:69]\n"
      "v_mfma_f32_16x16x32_bf16 v[56:59], a[116:119], v[152:155], v[56:59]\n"
      "ds_read_b64_tr_b16 a[88:89], v246 offset:0\n"
      "ds_read_b64_tr_b16 a[90:91], v246 offset:4096\n"
      "v_mfma_f32_16x16x32_bf16 v[52:55], a[116:119], v[148:151], v[52:55]\n"
      "ds_read_b64_tr_b16 a[92:93], v247 offset:0\n"
      "ds_read_b64_tr_b16 a[94:95], v247 offset:4096\n"
      "v_mfma_f32_16x16x32_bf16 v[48:51], a[116:119], v[144:147], v[48:51]\n"
      "ds_read_b64_tr_b16 a[80:81], v244 offset:0\n"
      "ds_read_b64_tr_b16 a[82:83], v244 offset:4096\n"
      "v_mfma_f32_16x16x32_bf16 a[176:179], a[120:123], v[144:147], a[176:179]\n"
      "ds_read_b64_tr_b16 a[84:85], v245 offset:0\n"
      "ds_read_b64_tr_b16 a[86:87], v245 offset:4096\n"
      "v_mfma_f32_16x16x32_bf16 a[180:183], a[120:123], v[148:151], a[180:183]\n"
      "v_exp_f32 v193, v93\n"
      "v_mfma_f32_16x16x32_bf16 a[184:187], a[120:123], v[152:155], a[184:187]\n"
      "v_exp_f32 v194, v94\n"
      "v_mfma_f32_16x16x32_bf16 a[188:191], a[120:123], v[156:159], a[188:191]\n"
      "v_exp_f32 v195, v95\n"
      "v_mfma_f32_16x16x32_bf16 a[204:207], a[124:127], v[156:159], a[204:207]\n"
      "v_cvt_pk_bf16_f32 v142, v192, v193\n"
      "v_cvt_pk_bf16_f32 v143, v194, v195\n"
      "v_mfma_f32_16x16x32_bf16 a[200:203], a[124:127], v[152:155], a[200:203]\n"
      "v_mfma_f32_16x16x32_bf16 a[196:199], a[124:127], v[148:151], a[196:199]\n"
      "v_mfma_f32_16x16x32_bf16 v[184:187], a[140:143], v[140:143], v[184:187]\n"
      "v_mfma_f32_16x16x32_bf16 a[192:195], a[124:127], v[144:147], a[192:195]\n"
      "s_nop 6\n"
      "v_max3_f32 v196, v184, v185, v186\n"
      "v_max_f32 v196, v196, v187\n"
      "v_cmp_nge_f32 vcc, 0x4b800000, v196\n"
      "s_cbranch_vccnz FS3_%=\n"
      "FR3_%=:\n"
      "s_lshl_b32 s76, %[spl], 3\n"
      "s_waitcnt lgkmcnt(0)\n"
      "v_mov_b32 v236, s68\n"
      "v_mov_b32 v237, %[e0]\n"
      "v_mov_b32 v238, s76\n"
      "s_mov_b64 exec, 1\n"
      "global_store_dwordx2 v238, v[236:237], %[line] offset:8 sc1\n"
      "s_mov_b64 exec, -1\n"
      "s_mov_b32 s82, s68\n"
      "v_xor_b32 v239, 8, v238\n"
      "v_mfma_f32_16x16x32_bf16 a[176:179], a[88:91], v[128:131], a[176:179]\n"
      "ds_read_b64_tr_b16 a[116:117], v245 offset:1024\n"
      "ds_read_b64_tr_b16 a[118:119], v245 offset:5120\n"
      "v_mfma_f32_16x16x32_bf16 a[180:183], a[88:91], v[132:135], a[180:183]\n"
      "v_add_f32 v180, v180, v184\n"
      "v_add_f32 v181, v181, v185\n"
      "v_mfma_f32_16x16x32_bf16 a[184:187], a[88:91], v[136:139], a[184:187]\n"
      "v_add_f32 v182, v182, v186\n"
      "v_add_f32 v183, v183, v187\n"
      "v_mfma_f32_16x16x32_bf16 a[188:191], a[88:91], v[140:143], a[188:191]\n"
      "v_exp_f32 v188, v96\n"
      "v_mfma_f32_16x16x32_bf16 a[204:207], a[92:95], v[140:143], a[204:207]\n"
      "v_exp_f32 v189, v97\n"
      "v_mfma_f32_16x16x32_bf16 a[200:203], a[92:95], v[136:139], a[200:203]\n"
      "v_exp_f32 v190, v98\n"
      "v_mfma_f32_16x16x32_bf16 a[196:199], a[92:95], v[132:135], a[196:199]\n"
      "ds_write_b128 v232, a[176:179] offset:0\n"
      "v_cvt_pk_bf16_f32 v144, v188, v189\n"
      "v_exp_f32 v191, v99\n"
      "v_mfma_f32_16x16x32_bf16 a[192:195], a[92:95], v[128:131], a[192:195]\n"
      "ds_read_b64_tr_b16 a[112:113], v244 offset:1024\n"
      "ds_read_b64_tr_b16 a[114:115], v244 offset:5120\n"
      "v_exp_f32 v192, v100\n"
      "v_cvt_pk_bf16_f32 v145, v190, v191\n"
      "v_exp_f32 v193, v101\n"
      "v_mfma_f32_16x16x32_bf16 v[32:35], a[80:83], v[128:131], v[32:35]\n"
      "ds_write_b128 v232, a[180:183] offset:1024\n"
      "v_exp_f32 v194, v102\n"
      "v_cvt_pk_bf16_f32 v146, v192, v193\n"
      "v_exp_f32 v195, v103\n"
      "v_exp_f32 v188, v104\n"
      "v_cvt_pk_bf16_f32 v147, v194, v195\n"
      "v_exp_f32 v189, v105\n"
      "v_mfma_f32_16x16x32_bf16 v[36:39], a[80:83], v[132:135], v[36:39]\n"
      "ds_write_b128 v234, v[144:147] offset:0\n"
      "v_exp_f32 v190, v106\n"
      "v_cvt_pk_bf16_f32 v148, v188, v189\n"
      "v_mfma_f32_16x16x32_bf16 v[184:187], a[128:131], v[144:147], 0\n"
      "ds_read_b64_tr_b16 a[124:125], v251 offset:1024\n"
      "ds_read_b64_tr_b16 a[126:127], v251 offset:5120\n"
      "v_exp_f32 v191, v107\n"
      "v_mfma_f32_16x16x32_bf16 v[40:43], a[80:83], v[136:139], v[40:43]\n"
      "ds_write_b128 v232, a[184:187] offset:2048\n"
      "v_exp_f32 v192, v108\n"
      "v_cvt_pk_bf16_f32 v149, v190, v191\n"
      "v_exp_f32 v193, v109\n"
      "v_exp_f32 v194, v110\n"
      "v_cvt_pk_bf16_f32 v150, v192, v193\n"
      "v_exp_f32 v195, v111\n"
      "v_mfma_f32_16x16x32_bf16 v[44:47], a[80:83], v[140:143], v[44:47]\n"
      "ds_write_b128 v232, a[188:191] offset:3072\n"
      "v_exp_f32 v188, v112\n"
      "v_cvt_pk_bf16_f32 v151, v194, v195\n"
      "v_exp_f32 v189, v113\n"
      "v_exp_f32 v190, v114\n"
      "v_mfma_f32_16x16x32_bf16 v[184:187], a[132:135], v[148:151], v[184:187]\n"
      "ds_read_b64_tr_b16 a[120:121], v250 offset:1024\n"
      "ds_read_b64_tr_b16 a[122:123], v250 offset:5120\n"
      "v_mfma_f32_16x16x32_bf16 v[60:63], a[84:87], v[140:143], v[60:63]\n"
      "ds_write_b128 v234, v[148:151] offset:1024\n"
      "v_cvt_pk_bf16_f32 v152, v188, v189\n"
      "v_exp_f32 v191, v115\n"
      "v_exp_f32 v192, v116\n"
      "v_cvt_pk_bf16_f32 v153, v190, v191\n"
      "v_exp_f32 v193, v117\n"
      "v_exp_f32 v194, v118\n"
      "v_mfma_f32_16x16x32_bf16 v[56:59], a[84:87], v[136:139], v[56:59]\n"
      "ds_write_b128 v232, a[204:207] offset:7168\n"
      "v_cvt_pk_bf16_f32 v154, v192, v193\n"
      "v_exp_f32 v195, v119\n"
      "v_exp_f32 v188, v120\n"
      "v_cvt_pk_bf16_f32 v155, v194, v195\n"
      "v_exp_f32 v189, v121\n"
      "v_exp_f32 v190, v122\n"
      "v_mfma_f32_16x16x32_bf16 v[184:187], a[136:139], v[152:155], v[184:187]\n"
      "ds_write_b128 v234, v[152:155] offset:2048\n"
      "v_cvt_pk_bf16_f32 v156, v188, v189\n"
      "v_exp_f32 v191, v123\n"
      "v_exp_f32 v192, v124\n"
      "v_cvt_pk_bf16_f32 v157, v190, v191\n"
      "v_exp_f32 v193, v125\n"
      "v_exp_f32 v194, v126\n"
      "v_mfma_f32_16x16x32_bf16 v[52:55], a[84:87], v[132:135], v[52:55]\n"
      "ds_write_b128 v232, a[200:203] offset:6144\n"
      "v_cvt_pk_bf16_f32 v158, v192, v193\n"
      "v_exp_f32 v195, v127\n"
      "v_mfma_f32_16x16x32_bf16 v[48:51], a[84:87], v[128:131], v[48:51]\n"
      "v_cvt_pk_bf16_f32 v159, v194, v195\n"
      "v_cndmask_b32 v229, v180, v181, s[74:75]\n"
      "v_cndmask_b32 v196, v182, v183, s[74:75]\n"
      "v_cndmask_b32 v229, v229, v196, s[94:95]\n"
      "v_mfma_f32_16x16x32_bf16 v[184:187], a[140:143], v[156:159], v[184:187]\n"
      "ds_write_b128 v234, v[156:159] offset:3072\n"
      "v_cndmask_b32 v228, v176, v177, s[74:75]\n"
      "v_cndmask_b32 v196, v178, v179, s[74:75]\n"
      "v_cndmask_b32 v228, v228, v196, s[94:95]\n"
      "s_nop 3\n"
      "v_max3_f32 v196, v184, v185, v186\n"
      "v_max_f32 v196, v196, v187\n"
      "v_cmp_nge_f32 vcc, 0x4b800000, v196\n"
      "s_cbranch_vccnz FS8_%=\n"
      "FR8_%=:\n"
      "s_waitcnt lgkmcnt(13)\n"
      "v_mfma_f32_16x16x32_bf16 v[48:51], a[116:119], v[144:147], v[48:51]\n"
      "ds_write_b128 v232, a[196:199] offset:5120\n"
      "v_cndmask_b32 v200, v184, v185, s[74:75]\n"
      "v_cndmask_b32 v196, v186, v187, s[74:75]\n"
      "v_cndmask_b32 v200, v200, v196, s[94:95]\n"
      "v_add_f32 v229, v229, v200\n"
      "v_mfma_f32_16x16x32_bf16 v[52:55], a[116:119], v[148:151], v[52:55]\n"
      "ds_write_b32 v234, v229 offset:4096\n"
      "v_mfma_f32_16x16x32_bf16 v[56:59], a[116:119], v[152:155], v[56:59]\n"
      "ds_write_b128 v232, a[192:195] offset:4096\n"
      "s_waitcnt lgkmcnt(0)\n"
      "s_barrier\n"
      "v_mfma_f32_16x16x32_bf16 v[60:63], a[116:119], v[156:159], v[60:63]\n"
      "ds_read_b32 v208, v209\n"
      "ds_read_b128 v[160:163], v235 offset:0\n"
      "ds_read_b128 v[164:167], v235 offset:1024\n"
      "v_mfma_f32_16x16x32_bf16 v[44:47], a[112:115], v[156:159], v[44:47]\n"
      "ds_read_b128 v[168:171], v235 offset:2048\n"
      "ds_read_b128 v[172:175], v235 offset:3072\n"
      "ds_read_b128 v[64:67], v233 offset:0\n"
      "v_mfma_f32_16x16x32_bf16 v[40:43], a[112:115], v[152:155], v[40:43]\n"
      "ds_read_b128 v[68:71], v233 offset:1024\n"
      "ds_read_b128 v[72:75], v233 offset:2048\n"
      "ds_read_b128 v[76:79], v233 offset:3072\n"
      "v_mfma_f32_16x16x32_bf16 v[36:39], a[112:115], v[148:151], v[36:39]\n"
      "ds_read_b128 v[80:83], v233 offset:4096\n"
      "ds_read_b128 v[84:87], v233 offset:5120\n"
      "ds_read_b128 v[88:91], v233 offset:6144\n"
      "v_mfma_f32_16x16x32_bf16 v[32:35], a[112:115], v[144:147], v[32:35]\n"
      "ds_read_b128 v[92:95], v233 offset:7168\n"
      "ds_read_b32 v204, v235 offset:4096\n"
      "s_waitcnt lgkmcnt(13)\n"
      "v_cmp_ne_u32 vcc, 0, v208\n"
      "s_cbranch_vccnz SLOW_%=\n"
      "AGAIN_%=:\n"
      "s_waitcnt lgkmcnt(12)\n"
      "v_mfma_f32_16x16x32_bf16 v[48:51], a[124:127], v[160:163], v[48:51]\n"
      "s_waitcnt lgkmcnt(11)\n"
      "v_mfma_f32_16x16x32_bf16 v[52:55], a[124:127], v[164:167], v[52:55]\n"
      "s_waitcnt lgkmcnt(10)\n"
      "v_mfma_f32_16x16x32_bf16 v[56:59], a[124:127], v[168:171], v[56:59]\n"
      "s_waitcnt lgkmcnt(9)\n"
      "v_mfma_f32_16x16x32_bf16 v[60:63], a[124:127], v[172:175], v[60:63]\n"
      "s_waitcnt lgkmcnt(8)\n"
      "v_add_f32 v32, v32, v64\n"
      "v_add_f32 v33, v33, v65\n"
      "v_add_f32 v34, v34, v66\n"
      "v_add_f32 v35, v35, v67\n"
      "s_waitcnt lgkmcnt(7)\n"
      "v_add_f32 v36, v36, v68\n"
      "v_add_f32 v37, v37, v69\n"
      "v_add_f32 v38, v38, v70\n"
      "v_add_f32 v39, v39, v71\n"
      "v_mfma_f32_16x16x32_bf16 v[32:35], a[120:123], v[160:163], v[32:35]\n"
      "s_waitcnt lgkmcnt(6)\n"
      "v_add_f32 v40, v40, v72\n"
      "v_add_f32 v41, v41, v73\n"
      "v_add_f32 v42, v42, v74\n"
      "v_add_f32 v43, v43, v75\n"
      "v_mfma_f32_16x16x32_bf16 v[36:39], a[120:123], v[164:167], v[36:39]\n"
      "s_waitcnt lgkmcnt(5)\n"
      "v_add_f32 v44, v44, v76\n"
      "v_add_f32 v45, v45, v77\n"
      "v_add_f32 v46, v46, v78\n"
      "v_add_f32 v47, v47, v79\n"
      "v_mfma_f32_16x16x32_bf16 v[40:43], a[120:123], v[168:171], v[40:43]\n"
      "s_waitcnt lgkmcnt(4)\n"
      "v_add_f32 v48, v48, v80\n"
      "v_add_f32 v49, v49, v81\n"
      "v_add_f32 v50, v50, v82\n"
      "v_add_f32 v51, v51, v83\n"
      "v_mfma_f32_16x16x32_bf16 v[44:47], a[120:123], v[172:175], v[44:47]\n"
      "v_cvt_pk_bf16_f32 v220, v48, v49\n"
      "v_cvt_pk_bf16_f32 v221, v50, v51\n"
      "s_waitcnt lgkmcnt(3)\n"
      "v_add_f32 v52, v52, v84\n"
      "v_add_f32 v53, v53, v85\n"
      "v_add_f32 v54, v54, v86\n"
      "v_add_f32 v55, v55, v87\n"
      "v_cvt_pk_bf16_f32 v222, v52, v53\n"
      "v_cvt_pk_bf16_f32 v223, v54, v55\n"
      "buffer_store_dwordx4 v[220:223], v241, %[srdx], s78 offen offset:2048 sc1\n"
      "s_waitcnt lgkmcnt(2)\n"
      "v_add_f32 v56, v56, v88\n"
      "v_add_f32 v57, v57, v89\n"
      "v_add_f32 v58, v58, v90\n"
      "v_add_f32 v59, v59, v91\n"
      "v_cvt_pk_bf16_f32 v224, v56, v57\n"
      "v_cvt_pk_bf16_f32 v225, v58, v59\n"
      "s_waitcnt lgkmcnt(1)\n"
      "v_add_f32 v60, v60, v92\n"
      "v_add_f32 v61, v61, v93\n"
      "v_add_f32 v62, v62, v94\n"
      "v_add_f32 v63, v63, v95\n"
      "v_cvt_pk_bf16_f32 v226, v60, v61\n"
      "v_cvt_pk_bf16_f32 v227, v62, v63\n"
      "buffer_store_dwordx4 v[224:227], v241, %[srdx], s78 offen offset:3072 sc1\n"
      "v_cvt_pk_bf16_f32 v212, v32, v33\n"
      "v_cvt_pk_bf16_f32 v213, v34, v35\n"
      "v_cvt_pk_bf16_f32 v214, v36, v37\n"
      "v_cvt_pk_bf16_f32 v215, v38, v39\n"
      "buffer_store_dwordx4 v[212:215], v241, %[srdx], s78 offen offset:0 sc1\n"
      "v_cvt_pk_bf16_f32 v216, v40, v41\n"
      "v_cvt_pk_bf16_f32 v217, v42, v43\n"
      "v_cvt_pk_bf16_f32 v218, v44, v45\n"
      "v_cvt_pk_bf16_f32 v219, v46, v47\n"
      "buffer_store_dwordx4 v[216:219], v241, %[srdx], s78 offen offset:1024 sc1\n"
      "s_waitcnt lgkmcnt(0)\n"
      "v_add_f32 v229, v229, v204\n"
      "v_add3_u32 v230, v228, v229, %[e0]\n"
      "v_add3_u32 v230, v230, v212, v213\n"
      "v_add3_u32 v230, v230, v214, v215\n"
      "v_add3_u32 v230, v230, v216, v217\n"
      "v_add3_u32 v230, v230, v218, v219\n"
      "v_add3_u32 v230, v230, v220, v221\n"
      "v_add3_u32 v230, v230, v222, v223\n"
      "v_add3_u32 v230, v230, v224, v225\n"
      "v_add3_u32 v230, v230, v226, v227\n"
      "buffer_store_dwordx3 v[228:230], v240, %[srdx], s78 offen sc1\n"
      "global_load_dwordx2 v[206:207], v239, %[line] offset:8 sc1\n"
      "s_waitcnt vmcnt(7)\n"
      "s_barrier\n"
      "ds_read_b64_tr_b16 a[72:73], v246 offset:16384\n"
      "ds_read_b64_tr_b16 a[74:75], v246 offset:20480\n"
      "ds_read_b64_tr_b16 a[76:77], v247 offset:16384\n"
      "ds_read_b64_tr_b16 a[78:79], v247 offset:20480\n"
      "ds_read_b64_tr_b16 a[104:105], v246 offset:17408\n"
      "ds_read_b64_tr_b16 a[106:107], v246 offset:21504\n"
      "ds_read_b64_tr_b16 a[108:109], v247 offset:17408\n"
      "ds_read_b64_tr_b16 a[110:111], v247 offset:21504\n"
      "s_waitcnt lgkmcnt(4)\n"
      "v_mfma_f32_16x16x32_bf16 a[144:147], a[72:75], v[128:131], a[144:147]\n"
      "ds_read_b64_tr_b16 a[64:65], v244 offset:16384\n"
      "v_mov_b32 v198, 0\n"
      "v_mfma_f32_16x16x32_bf16 a[148:151], a[72:75], v[132:135], a[148:151]\n"
      "ds_read_b64_tr_b16 a[66:67], v244 offset:20480\n"
      "v_and_b32 v197, 0xf0, v241\n"
      "v_mfma_f32_16x16x32_bf16 a[152:155], a[72:75], v[136:139], a[152:155]\n"
      "ds_read_b64_tr_b16 a[68:69], v245 offset:16384\n"
      "v_lshrrev_b32 v196, 3, v241\n"
      "v_mfma_f32_16x16x32_bf16 a[156:159], a[72:75], v[140:143], a[156:159]\n"
      "ds_read_b64_tr_b16 a[70:71], v245 offset:20480\n"
      "v_and_b32 v196, 32, v196\n"
      "v_mfma_f32_16x16x32_bf16 a[172:175], a[76:79], v[140:143], a[172:175]\n"
      "ds_read_b64_tr_b16 a[96:97], v244 offset:17408\n"
      "v_lshl_or_b32 v196, v197, 4, v196\n"
      "v_mfma_f32_16x16x32_bf16 a[168:171], a[76:79], v[136:139], a[168:171]\n"
      "ds_read_b64_tr_b16 a[98:99], v244 offset:21504\n"
      "v_lshrrev_b32 v199, 5, v241\n"
      "v_mfma_f32_16x16x32_bf16 a[164:167], a[76:79], v[132:135], a[164:167]\n"
      "ds_read_b64_tr_b16 a[100:101], v245 offset:17408\n"
      "v_and_or_b32 v196, v199, 16, v196\n"
      "v_mfma_f32_16x16x32_bf16 a[160:163], a[76:79], v[128:131], a[160:163]\n"
      "ds_read_b64_tr_b16 a[102:103], v245 offset:21504\n"
      "v_mov_b32 v191, %[e0]\n"
      "s_waitcnt lgkmcnt(8)\n"
      "v_mfma_f32_16x16x32_bf16 a[144:147], a[104:107], v[144:147], a[144:147]\n"
      "s_sub_u32 s76, 19456, s78\n"
      "v_mfma_f32_16x16x32_bf16 a[148:151], a[104:107], v[148:151], a[148:151]\n"
      "s_mov_b32 s63, 0x40000\n"
      "v_mfma_f32_16x16x32_bf16 a[152:155], a[104:107], v[152:155], a[152:155]\n"
      "v_mfma_f32_16x16x32_bf16 a[156:159], a[104:107], v[156:159], a[156:159]\n"
      "v_mfma_f32_16x16x32_bf16 a[172:175], a[108:111], v[156:159], a[172:175]\n"
      "v_mfma_f32_16x16x32_bf16 a[168:171], a[108:111], v[152:155], a[168:171]\n"
      "v_mfma_f32_16x16x32_bf16 a[164:167], a[108:111], v[148:151], a[164:167]\n"
      "v_mfma_f32_16x16x32_bf16 a[160:163], a[108:111], v[144:147], a[160:163]\n"
      "s_waitcnt lgkmcnt(0)\n"
      "v_mfma_f32_16x16x32_bf16 v[0:3], a[64:67], v[128:131], v[0:3]\n"
      "v_mfma_f32_16x16x32_bf16 v[4:7], a[64:67], v[132:135], v[4:7]\n"
      "v_mfma_f32_16x16x32_bf16 v[8:11], a[64:67], v[136:139], v[8:11]\n"
      "ds_write_b128 v232, a[144:147] offset:32768\n"
      "v_mfma_f32_16x16x32_bf16 v[12:15], a[64:67], v[140:143], v[12:15]\n"
      "ds_write_b128 v232, a[148:151] offset:33792\n"
      "v_mfma_f32_16x16x32_bf16 v[28:31], a[68:71], v[140:143], v[28:31]\n"
      "ds_write_b128 v232, a[152:155] offset:34816\n"
      "v_mfma_f32_16x16x32_bf16 v[24:27], a[68:71], v[136:139], v[24:27]\n"
      "ds_write_b128 v232, a[156:159] offset:35840\n"
      "v_mfma_f32_16x16x32_bf16 v[20:23], a[68:71], v[132:135], v[20:23]\n"
      "ds_write_b128 v232, a[172:175] offset:39936\n"
      "v_mfma_f32_16x16x32_bf16 v[16:19], a[68:71], v[128:131], v[16:19]\n"
      "ds_write_b128 v232, a[168:171] offset:38912\n"
      "v_mfma_f32_16x16x32_bf16 v[0:3], a[96:99], v[144:147], v[0:3]\n"
      "ds_write_b128 v232, a[164:167] offset:37888\n"
      "v_mfma_f32_16x16x32_bf16 v[4:7], a[96:99], v[148:151], v[4:7]\n"
      "ds_write_b128 v232, a[160:163] offset:36864\n"
      "v_mfma_f32_16x16x32_bf16 v[8:11], a[96:99], v[152:155], v[8:11]\n"
      "v_mfma_f32_16x16x32_bf16 v[12:15], a[96:99], v[156:159], v[12:15]\n"
      "v_mfma_f32_16x16x32_bf16 v[28:31], a[100:103], v[156:159], v[28:31]\n"
      "v_mfma_f32_16x16x32_bf16 v[24:27], a[100:103], v[152:155], v[24:27]\n"
      "v_mfma_f32_16x16x32_bf16 v[20:23], a[100:103], v[148:151], v[20:23]\n"
      "v_mfma_f32_16x16x32_bf16 v[16:19], a[100:103], v[144:147], v[16:19]\n"
      "s_waitcnt lgkmcnt(0)\n"
      "s_barrier\n"
      "ds_read_b128 v[64:67], v233 offset:32768\n"
      "ds_read_b128 v[68:71], v233 offset:33792\n"
      "ds_read_b128 v[72:75], v233 offset:34816\n"
      "ds_read_b128 v[76:79], v233 offset:35840\n"
      "ds_read_b128 v[80:83], v233 offset:36864\n"
      "ds_read_b128 v[84:87], v233 offset:37888\n"
      "ds_read_b128 v[88:91], v233 offset:38912\n"
      "ds_read_b128 v[92:95], v233 offset:39936\n"
      "s_waitcnt vmcnt(0)\n"
      "v_subrev_u32 v205, s82, v206\n"
      "v_cmp_ne_u32 vcc, %[e0], v207\n"
      "s_cbranch_vccnz LAG_%=\n"
      "v_cmp_lt_i32 vcc, 16, v205\n"
      "s_cbranch_vccnz LAG_%=\n"
      "READ_%=:\n"
      "buffer_load_dwordx3 v[128:130], v240, %[srdx], s76 offen sc1\n"
      "buffer_load_dwordx4 v[104:107], v241, %[srdx], s76 offen offset:2048 sc1\n"
      "buffer_load_dwordx4 v[108:111], v241, %[srdx], s76 offen offset:3072 sc1\n"
      "buffer_load_dwordx4 v[96:99], v241, %[srdx], s76 offen offset:0 sc1\n"
      "buffer_load_dwordx4 v[100:103], v241, %[srdx], s76 offen offset:1024 sc1\n"
      "s_waitcnt lgkmcnt(6)\n"
      "v_pk_add_f32 v[0:1], v[0:1], v[64:65]\n"
      "v_pk_add_f32 v[2:3], v[2:3], v[66:67]\n"
      "v_pk_add_f32 v[4:5], v[4:5], v[68:69]\n"
      "v_pk_add_f32 v[6:7], v[6:7], v[70:71]\n"
      "s_waitcnt lgkmcnt(4)\n"
      "v_pk_add_f32 v[8:9], v[8:9], v[72:73]\n"
      "v_pk_add_f32 v[10:11], v[10:11], v[74:75]\n"
      "v_pk_add_f32 v[12:13], v[12:13], v[76:77]\n"
      "v_pk_add_f32 v[14:15], v[14:15], v[78:79]\n"
      "s_waitcnt lgkmcnt(2)\n"
      "v_pk_add_f32 v[16:17], v[16:17], v[80:81]\n"
      "v_pk_add_f32 v[18:19], v[18:19], v[82:83]\n"
      "v_pk_add_f32 v[20:21], v[20:21], v[84:85]\n"
      "v_pk_add_f32 v[22:23], v[22:23], v[86:87]\n"
      "s_waitcnt lgkmcnt(0)\n"
      "v_pk_add_f32 v[24:25], v[24:25], v[88:89]\n"
      "v_pk_add_f32 v[26:27], v[26:27], v[90:91]\n"
      "v_pk_add_f32 v[28:29], v[28:29], v[92:93]\n"
      "v_pk_add_f32 v[30:31], v[30:31], v[94:95]\n"
      "TRY_%=:\n"
      "s_waitcnt vmcnt(4)\n"
      "v_sub_f32 v136, v128, v228\n"
      "v_add3_u32 v230, v128, v129, %[e0]\n"
      "v_min_f32 v140, 0, -v136\n"
      "v_min_f32 v136, 0, v136\n"
      "v_exp_f32 v136, v136\n"
      "v_exp_f32 v140, v140\n"
      "v_mul_f32 v131, v229, v136\n"
      "v_fmac_f32 v131, v129, v140\n"
      "v_rcp_f32 v131, v131\n"
      "s_nop 0\n"
      "v_mul_f32 v136, v136, v131\n"
      "v_mul_f32 v140, v140, v131\n"
      "v_mov_b32 v137, v136\n"
      "v_mov_b32 v141, v140\n"
      "s_nop 0\n"
      "v_permlane16_swap_b32 v136, v137\n"
      "v_permlane16_swap_b32 v140, v141\n"
      "v_mov_b32 v138, v136\n"
      "v_mov_b32 v139, v137\n"
      "v_mov_b32 v142, v140\n"
      "v_mov_b32 v143, v141\n"
      "v_permlane32_swap_b32 v136, v138\n"
      "v_permlane32_swap_b32 v137, v139\n"
      "v_permlane32_swap_b32 v140, v142\n"
      "v_permlane32_swap_b32 v141, v143\n"
      "s_waitcnt vmcnt(3)\n"
      "v_add3_u32 v230, v230, v104, v105\n"
      "v_add3_u32 v230, v230, v106, v107\n"
      "v_lshlrev_b32 v48, 16, v104\n"
      "v_and_b32 v49, 0xffff0000, v104\n"
      "v_lshlrev_b32 v50, 16, v105\n"
      "v_and_b32 v51, 0xffff0000, v105\n"
      "v_lshlrev_b32 v52, 16, v106\n"
      "v_and_b32 v53, 0xffff0000, v106\n"
      "v_lshlrev_b32 v54, 16, v107\n"
      "v_and_b32 v55, 0xffff0000, v107\n"
      "s_waitcnt vmcnt(2)\n"
      "v_add3_u32 v230, v230, v108, v109\n"
      "v_add3_u32 v230, v230, v110, v111\n"
      "v_lshlrev_b32 v56, 16, v108\n"
      "v_and_b32 v57, 0xffff0000, v108\n"
      "v_lshlrev_b32 v58, 16, v109\n"
      "v_and_b32 v59, 0xffff0000, v109\n"
      "v_lshlrev_b32 v60, 16, v110\n"
      "v_and_b32 v61, 0xffff0000, v110\n"
      "v_lshlrev_b32 v62, 16, v111\n"
      "v_and_b32 v63, 0xffff0000, v111\n"
      "s_waitcnt vmcnt(1)\n"
      "v_add3_u32 v230, v230, v96, v97\n"
      "v_add3_u32 v230, v230, v98, v99\n"
      "v_lshlrev_b32 v32, 16, v96\n"
      "v_and_b32 v33, 0xffff0000, v96\n"
      "v_lshlrev_b32 v34, 16, v97\n"
      "v_and_b32 v35, 0xffff0000, v97\n"
      "v_lshlrev_b32 v36, 16, v98\n"
      "v_and_b32 v37, 0xffff0000, v98\n"
      "v_lshlrev_b32 v38, 16, v99\n"
      "v_and_b32 v39, 0xffff0000, v99\n"
      "s_waitcnt vmcnt(0)\n"
      "v_add3_u32 v230, v230, v100, v101\n"
      "v_add3_u32 v230, v230, v102, v103\n"
      "v_cmp_ne_u32 vcc, v230, v130\n"
      "s_cbranch_vccnz RETRY_%=\n"
      "GOT_%=:\n"
      "v_pk_mul_f32 v[0:1], v[0:1], v[136:137] op_sel:[0,0] op_sel_hi:[1,0]\n"
      "v_pk_mul_f32 v[2:3], v[2:3], v[136:137] op_sel:[0,0] op_sel_hi:[1,0]\n"
      "v_pk_mul_f32 v[16:17], v[16:17], v[136:137] op_sel:[0,0] op_sel_hi:[1,0]\n"
      "v_pk_mul_f32 v[18:19], v[18:19], v[136:137] op_sel:[0,0] op_sel_hi:[1,0]\n"
      "v_pk_fma_f32 v[0:1], v[32:33], v[140:141], v[0:1] op_sel:[0,0,0] op_sel_hi:[1,0,1]\n"
      "v_pk_fma_f32 v[2:3], v[34:35], v[140:141], v[2:3] op_sel:[0,0,0] op_sel_hi:[1,0,1]\n"
      "v_pk_fma_f32 v[16:17], v[48:49], v[140:141], v[16:17] op_sel:[0,0,0] op_sel_hi:[1,0,1]\n"
      "v_pk_fma_f32 v[18:19], v[50:51], v[140:141], v[18:19] op_sel:[0,0,0] op_sel_hi:[1,0,1]\n"
      "v_cvt_pk_bf16_f32 v112, v0, v1\n"
      "v_cvt_pk_bf16_f32 v113, v2, v3\n"
      "v_cvt_pk_bf16_f32 v114, v16, v17\n"
      "v_cvt_pk_bf16_f32 v115, v18, v19\n"
      "v_add_u32 v192, 0, v196\n"
      "v_permlane16_swap_b32 v112, v114\n"
      "v_permlane16_swap_b32 v113, v115\n"
      "global_store_dwordx4 v192, v[112:115], %[outp] sc1\n"
      "v_pk_mul_f32 v[4:5], v[4:5], v[136:137] op_sel:[0,1] op_sel_hi:[1,1]\n"
      "v_pk_mul_f32 v[6:7], v[6:7], v[136:137] op_sel:[0,1] op_sel_hi:[1,1]\n"
      "v_pk_mul_f32 v[20:21], v[20:21], v[136:137] op_sel:[0,1] op_sel_hi:[1,1]\n"
      "v_pk_mul_f32 v[22:23], v[22:23], v[136:137] op_sel:[0,1] op_sel_hi:[1,1]\n"
      "v_pk_fma_f32 v[4:5], v[36:37], v[140:141], v[4:5] op_sel:[0,1,0] op_sel_hi:[1,1,1]\n"
      "v_pk_fma_f32 v[6:7], v[38:39], v[140:141], v[6:7] op_sel:[0,1,0] op_sel_hi:[1,1,1]\n"
      "v_pk_fma_f32 v[20:21], v[52:53], v[140:141], v[20:21] op_sel:[0,1,0] op_sel_hi:[1,1,1]\n"
      "v_pk_fma_f32 v[22:23], v[54:55], v[140:141], v[22:23] op_sel:[0,1,0] op_sel_hi:[1,1,1]\n"
      "v_cvt_pk_bf16_f32 v116, v4, v5\n"
      "v_cvt_pk_bf16_f32 v117, v6, v7\n"
      "v_cvt_pk_bf16_f32 v118, v20, v21\n"
      "v_cvt_pk_bf16_f32 v119, v22, v23\n"
      "v_add_u32 v193, 4096, v196\n"
      "v_permlane16_swap_b32 v116, v118\n"
      "v_permlane16_swap_b32 v117, v119\n"
      "global_store_dwordx4 v193, v[116:119], %[outp] sc1\n"
      "v_lshlrev_b32 v40, 16, v100\n"
      "v_and_b32 v41, 0xffff0000, v100\n"
      "v_lshlrev_b32 v42, 16, v101\n"
      "v_and_b32 v43, 0xffff0000, v101\n"
      "v_lshlrev_b32 v44, 16, v102\n"
      "v_and_b32 v45, 0xffff0000, v102\n"
      "v_lshlrev_b32 v46, 16, v103\n"
      "v_and_b32 v47, 0xffff0000, v103\n"
      "v_pk_mul_f32 v[8:9], v[8:9], v[138:139] op_sel:[0,0] op_sel_hi:[1,0]\n"
      "v_pk_mul_f32 v[10:11], v[10:11], v[138:139] op_sel:[0,0] op_sel_hi:[1,0]\n"
      "v_pk_mul_f32 v[24:25], v[24:25], v[138:139] op_sel:[0,0] op_sel_hi:[1,0]\n"
      "v_pk_mul_f32 v[26:27], v[26:27], v[138:139] op_sel:[0,0] op_sel_hi:[1,0]\n"
      "v_pk_fma_f32 v[8:9], v[40:41], v[142:143], v[8:9] op_sel:[0,0,0] op_sel_hi:[1,0,1]\n"
      "v_pk_fma_f32 v[10:11], v[42:43], v[142:143], v[10:11] op_sel:[0,0,0] op_sel_hi:[1,0,1]\n"
      "v_pk_fma_f32 v[24:25], v[56:57], v[142:143], v[24:25] op_sel:[0,0,0] op_sel_hi:[1,0,1]\n"
      "v_pk_fma_f32 v[26:27], v[58:59], v[142:143], v[26:27] op_sel:[0,0,0] op_sel_hi:[1,0,1]\n"
      "v_cvt_pk_bf16_f32 v120, v8, v9\n"
      "v_cvt_pk_bf16_f32 v121, v10, v11\n"
      "v_cvt_pk_bf16_f32 v122, v24, v25\n"
      "v_cvt_pk_bf16_f32 v123, v26, v27\n"
      "v_add_u32 v192, 8192, v196\n"
      "v_permlane16_swap_b32 v120, v122\n"
      "v_permlane16_swap_b32 v121, v123\n"
      "global_store_dwordx4 v192, v[120:123], %[outp] sc1\n"
      "v_pk_mul_f32 v[12:13], v[12:13], v[138:139] op_sel:[0,1] op_sel_hi:[1,1]\n"
      "v_pk_mul_f32 v[14:15], v[14:15], v[138:139] op_sel:[0,1] op_sel_hi:[1,1]\n"
      "v_pk_mul_f32 v[28:29], v[28:29], v[138:139] op_sel:[0,1] op_sel_hi:[1,1]\n"
      "v_pk_mul_f32 v[30:31], v[30:31], v[138:139] op_sel:[0,1] op_sel_hi:[1,1]\n"
      "v_pk_fma_f32 v[12:13], v[44:45], v[142:143], v[12:13] op_sel:[0,1,0] op_sel_hi:[1,1,1]\n"
      "v_pk_fma_f32 v[14:15], v[46:47], v[142:143], v[14:15] op_sel:[0,1,0] op_sel_hi:[1,1,1]\n"
      "v_pk_fma_f32 v[28:29], v[60:61], v[142:143], v[28:29] op_sel:[0,1,0] op_sel_hi:[1,1,1]\n"
      "v_pk_fma_f32 v[30:31], v[62:63], v[142:143], v[30:31] op_sel:[0,1,0] op_sel_hi:[1,1,1]\n"
      "v_cvt_pk_bf16_f32 v124, v12, v13\n"
      "v_cvt_pk_bf16_f32 v125, v14, v15\n"
      "v_cvt_pk_bf16_f32 v126, v28, v29\n"
      "v_cvt_pk_bf16_f32 v127, v30, v31\n"
      "v_add_u32 v193, 12288, v196\n"
      "v_permlane16_swap_b32 v124, v126\n"
      "v_permlane16_swap_b32 v125, v127\n"
      "global_store_dwordx4 v193, v[124:127], %[outp] sc1\n"
      "s_mov_b64 exec, 1\n"
      "global_store_dword v198, v191, %[line] sc1\n"
      "s_mov_b64 exec, -1\n"
      "ENDE_%=:\n"
      ".subsection 1\n"
      "MH_%=:\n"
      "s_cmp_lg_u32 s54, 0\n"
      "s_waitcnt vmcnt(12) lgkmcnt(0)\n"
      "s_cbranch_scc1 W6_%=\n"
      "s_waitcnt vmcnt(8)\n"
      "W6_%=:\n"
      "s_barrier\n"
      "v_mfma_f32_16x16x32_bf16 a[160:163], a[108:111], v[144:147], a[160:163]\n"
      "s_xor_b64 s[56:57], s[56:57], %[dlt]\n"
      "ds_read_b128 a[64:67], v242 offset:0\n"
      "ds_read_b128 a[72:75], v243 offset:0\n"
      "v_mfma_f32_16x16x32_bf16 v[32:35], a[112:115], v[144:147], v[32:35]\n"
      "s_bitset1_b32 s61, 15\n"
      "ds_read_b128 a[80:83], v242 offset:1024\n"
      "ds_read_b128 a[88:91], v243 offset:1024\n"
      "v_mfma_f32_16x16x32_bf16 v[36:39], a[112:115], v[148:151], v[36:39]\n"
      "s_add_u32 s61, s61, 0x2000\n"
      "v_exp_f32 v188, v88\n"
      "v_mfma_f32_16x16x32_bf16 v[40:43], a[112:115], v[152:155], v[40:43]\n"
      "s_bitset0_b32 s61, 15\n"
      "v_exp_f32 v189, v89\n"
      "v_mfma_f32_16x16x32_bf16 v[44:47], a[112:115], v[156:159], v[44:47]\n"
      "s_xor_b32 s60, s61, %[par]\n"
      "v_exp_f32 v190, v90\n"
      "v_mfma_f32_16x16x32_bf16 v[60:63], a[116:119], v[156:159], v[60:63]\n"
      "s_add_u32 s62, s62, 0x4000\n"
      "v_exp_f32 v191, v91\n"
      "v_mfma_f32_16x16x32_bf16 v[56:59], a[116:119], v[152:155], v[56:59]\n"
      "s_and_b32 s62, s62, 0x1ffff\n"
      "v_cvt_pk_bf16_f32 v140, v188, v189\n"
      "v_cvt_pk_bf16_f32 v141, v190, v191\n"
      "v_mfma_f32_16x16x32_bf16 v[52:55], a[116:119], v[148:151], v[52:55]\n"
      "s_add_u32 s66, s66, s86\n"
      "v_exp_f32 v192, v92\n"
      "v_mfma_f32_16x16x32_bf16 v[48:51], a[116:119], v[144:147], v[48:51]\n"
      "s_add_u32 s82, s82, s86\n"
      "v_exp_f32 v193, v93\n"
      "v_mfma_f32_16x16x32_bf16 a[176:179], a[120:123], v[144:147], a[176:179]\n"
      "s_add_u32 s65, s65, s86\n"
      "v_exp_f32 v194, v94\n"
      "v_mfma_f32_16x16x32_bf16 a[180:183], a[120:123], v[148:151], a[180:183]\n"
      "s_max_u32 s88, s66, s65\n"
      "v_exp_f32 v195, v95\n"
      "s_waitcnt lgkmcnt(0)\n"
      "v_mfma_f32_16x16x32_bf16 a[184:187], a[120:123], v[152:155], a[184:187]\n"
      "v_cvt_pk_bf16_f32 v142, v192, v193\n"
      "v_cvt_pk_bf16_f32 v143, v194, v195\n"
      "v_mfma_f32_16x16x32_bf16 a[188:191], a[120:123], v[156:159], a[188:191]\n"
      "s_mov_b32 m0, s62\n"
      "ds_read_b128 a[68:71], v242 offset:4096\n"
      "ds_read_b128 a[76:79], v243 offset:4096\n"
      "v_mfma_f32_16x16x32_bf16 v[184:187], a[140:143], v[140:143], v[184:187]\n"
      "buffer_load_dwordx4 v248, s[56:59], s60 offen nt lds\n"
      "ds_read_b128 a[84:87], v242 offset:5120\n"
      "ds_read_b128 a[92:95], v243 offset:5120\n"
      "v_mfma_f32_16x16x32_bf16 a[204:207], a[124:127], v[156:159], a[204:207]\n"
      "buffer_load_dwordx4 v248, s[56:59], s60 offen offset:2048 nt lds\n"
      "v_xor_b32 v244, 0x10000, v244\n"
      "v_xor_b32 v245, 0x10000, v245\n"
      "v_mfma_f32_16x16x32_bf16 a[200:203], a[124:127], v[152:155], a[200:203]\n"
      "s_bitset1_b32 m0, 10\n"
      "v_xor_b32 v246, 0x10000, v246\n"
      "v_xor_b32 v247, 0x10000, v247\n"
      "v_mfma_f32_16x16x32_bf16 a[196:199], a[124:127], v[148:151], a[196:199]\n"
      "buffer_load_dwordx4 v249, s[56:59], s60 offen nt lds\n"
      "v_max3_f32 v196, v184, v185, v186\n"
      "v_max_f32 v196, v196, v187\n"
      "v_mfma_f32_16x16x32_bf16 a[192:195], a[124:127], v[144:147], a[192:195]\n"
      "buffer_load_dwordx4 v249, s[56:59], s60 offen offset:2048 nt lds\n"
      "v_cmp_nge_f32 vcc, 0x4b800000, v196\n"
      "s_cbranch_vccnz FS11_%=\n"
      "FR11_%=:\n"
      "v_mfma_f32_16x16x32_bf16 v[64:67], a[64:67], a[0:3], v[160:163]\n"
      "v_exp_f32 v188, v96\n"
      "v_mfma_f32_16x16x32_bf16 v[64:67], a[72:75], a[16:19], v[64:67]\n"
      "v_add_f32 v180, v180, v184\n"
      "v_add_f32 v181, v181, v185\n"
      "v_mfma_f32_16x16x32_bf16 v[64:67], a[80:83], a[32:35], v[64:67]\n"
      "v_exp_f32 v189, v97\n"
      "v_mfma_f32_16x16x32_bf16 v[64:67], a[88:91], a[48:51], v[64:67]\n"
      "v_add_f32 v182, v182, v186\n"
      "v_add_f32 v183, v183, v187\n"
      "v_mfma_f32_16x16x32_bf16 v[72:75], a[64:67], a[4:7], v[164:167]\n"
      "v_exp_f32 v190, v98\n"
      "v_mfma_f32_16x16x32_bf16 v[72:75], a[72:75], a[20:23], v[72:75]\n"
      "v_exp_f32 v191, v99\n"
      "v_mfma_f32_16x16x32_bf16 v[72:75], a[80:83], a[36:39], v[72:75]\n"
      "v_cvt_pk_bf16_f32 v144, v188, v189\n"
      "v_cvt_pk_bf16_f32 v145, v190, v191\n"
      "v_mfma_f32_16x16x32_bf16 v[72:75], a[88:91], a[52:55], v[72:75]\n"
      "v_exp_f32 v192, v100\n"
      "v_mfma_f32_16x16x32_bf16 v[80:83], a[64:67], a[8:11], v[168:171]\n"
      "v_exp_f32 v193, v101\n"
      "v_mfma_f32_16x16x32_bf16 v[80:83], a[72:75], a[24:27], v[80:83]\n"
      "s_memrealtime s[68:69]\n"
      "v_exp_f32 v194, v102\n"
      "v_mfma_f32_16x16x32_bf16 v[80:83], a[80:83], a[40:43], v[80:83]\n"
      "v_exp_f32 v195, v103\n"
      "v_mfma_f32_16x16x32_bf16 v[80:83], a[88:91], a[56:59], v[80:83]\n"
      "s_nop 0\n"
      "v_mfma_f32_16x16x32_bf16 v[88:91], a[64:67], a[12:15], v[172:175]\n"
      "s_nop 0\n"
      "v_mfma_f32_16x16x32_bf16 v[88:91], a[72:75], a[28:31], v[88:91]\n"
      "s_add_u32 %[mB], %[mB], %[mB]\n"
      "v_cvt_pk_bf16_f32 v146, v192, v193\n"
      "v_mfma_f32_16x16x32_bf16 v[88:91], a[80:83], a[44:47], v[88:91]\n"
      "s_nop 0\n"
      "v_mfma_f32_16x16x32_bf16 v[88:91], a[88:91], a[60:63], v[88:91]\n"
      "s_nop 0\n"
      "s_cbranch_scc1 SP1_%=\n"
      "s_branch HD1_%=\n"
      "HOOK_%=:\n"
      "buffer_load_dword v250, v251, %[srdr], 0 offen sc1\n"
      "s_swappc_b64 s[84:85], s[84:85]\n"
      "s_waitcnt vmcnt(12)\n"
      "v_subrev_u32 v250, s71, v250\n"
      "v_min_u32 v250, 0x258, v250\n"
      "s_nop 1\n"
      "v_add_u32_dpp v250, v250, v250 quad_perm:[1,0,3,2] row_mask:0xf bank_mask:0xf\n"
      "s_nop 1\n"
      "v_add_u32_dpp v250, v250, v250 quad_perm:[2,3,0,1] row_mask:0xf bank_mask:0xf\n"
      "s_nop 1\n"
      "v_add_u32_dpp v250, v250, v250 row_half_mirror row_mask:0xf bank_mask:0xf\n"
      "s_nop 0\n"
      "v_readfirstlane_b32 s67, v250\n"
      "s_sub_u32 s66, s66, s81\n"
      "s_lshr_b32 s67, s67, 3\n"
      "s_add_u32 s66, s66, s67\n"
      "s_max_u32 s88, s66, s65\n"
      "s_mov_b32 s86, 57\n"
      "s_mov_b32 s77, 171\n"
      "s_swappc_b64 s[84:85], s[84:85]\n"
      "s_mov_b32 s89, 0\n"
      "s_swappc_b64 s[84:85], s[84:85]\n"
      "s_mov_b32 s86, 55\n"
      "s_mov_b32 s77, 165\n"
      "s_swappc_b64 s[84:85], s[84:85]\n"
      "s_cmp_lt_u32 s89, 1\n"
      "s_cselect_b32 s67, 2048, s72\n"
      "s_mov_b32 s89, 0\n"
      "s_cmp_lt_u32 s67, 2048\n"
      "s_cbranch_scc0 NV0_%=\n"
      "v_lshrrev_b32 v251, 4, v241\n"
      "v_lshlrev_b32 v250, 7, v251\n"
      "v_and_b32 v250, 0x1800, v250\n"
      "v_and_b32 v251, 15, v251\n"
      "v_lshl_or_b32 v251, v251, 2, v250\n"
      "v_mov_b32 v250, s82\n"
      "s_add_u32 s67, s67, 8192\n"
      "buffer_store_dword v250, v251, %[srdr], s67 offen sc1\n"
      "NV0_%=:\n"
      "s_swappc_b64 s[84:85], s[84:85]\n"
      "s_mov_b32 s86, 54\n"
      "s_mov_b32 s77, 162\n"
      "v_and_b32 v251, 0x1f0, v241\n"
      "v_lshl_add_u32 v251, v251, 2, s79\n"
      "buffer_load_dword v250, v251, %[srdr], 0 offen sc1\n"
      "s_swappc_b64 s[84:85], s[84:85]\n"
      "s_waitcnt vmcnt(12)\n"
      "v_sub_u32 v250, s82, v250\n"
      "v_cmp_gt_u32 vcc, 1600, v250\n"
      "s_nop 3\n"
      "s_bcnt1_i32_b32 s67, vcc_lo\n"
      "s_cmp_ge_u32 s67, 7\n"
      "s_cbranch_scc1 SM_%=\n"
      "s_swappc_b64 s[84:85], s[84:85]\n"
      "s_cmp_lt_u32 s89, 3\n"
      "s_cselect_b32 s67, 2048, s72\n"
      "s_mov_b32 s89, 0\n"
      "s_cmp_lt_u32 s67, 2048\n"
      "s_cbranch_scc0 NV1_%=\n"
      "v_lshrrev_b32 v251, 4, v241\n"
      "v_lshlrev_b32 v250, 7, v251\n"
      "v_and_b32 v250, 0x1800, v250\n"
      "v_and_b32 v251, 15, v251\n"
      "v_lshl_or_b32 v251, v251, 2, v250\n"
      "v_mov_b32 v250, s82\n"
      "s_add_u32 s67, s67, 8192\n"
      "buffer_store_dword v250, v251, %[srdr], s67 offen sc1\n"
      "NV1_%=:\n"
      "s_swappc_b64 s[84:85], s[84:85]\n"
      "v_and_b32 v251, 0x1f0, v241\n"
      "v_lshl_add_u32 v251, v251, 2, s79\n"
      "buffer_load_dword v250, v251, %[srdr], 0 offen sc1\n"
      "s_swappc_b64 s[84:85], s[84:85]\n"
      "s_mov_b32 s80, 85\n"
      "s_waitcnt vmcnt(12)\n"
      "v_sub_u32 v250, s82, v250\n"
      "v_cmp_gt_u32 vcc, 1600, v250\n"
      "s_nop 3\n"
      "s_bcnt1_i32_b32 s67, vcc_lo\n"
      "s_cmp_ge_u32 s67, 7\n"
      "s_cbranch_scc1 SM_%=\n"
      "s_swappc_b64 s[84:85], s[84:85]\n"
      "s_cmp_lt_u32 s89, 3\n"
      "s_cselect_b32 s67, 2048, s72\n"
      "s_mov_b32 s89, 0\n"
      "s_cmp_lt_u32 s67, 2048\n"
      "s_cbranch_scc0 NV2_%=\n"
      "v_lshrrev_b32 v251, 4, v241\n"
      "v_lshlrev_b32 v250, 7, v251\n"
      "v_and_b32 v250, 0x1800, v250\n"
      "v_and_b32 v251, 15, v251\n"
      "v_lshl_or_b32 v251, v251, 2, v250\n"
      "v_mov_b32 v250, s82\n"
      "s_add_u32 s67, s67, 8192\n"
      "buffer_store_dword v250, v251, %[srdr], s67 offen sc1\n"
      "NV2_%=:\n"
      "s_swappc_b64 s[84:85], s[84:85]\n"
      "v_and_b32 v251, 0x1f0, v241\n"
      "v_lshl_add_u32 v251, v251, 2, s79\n"
      "buffer_load_dword v250, v251, %[srdr], 0 offen sc1\n"
      "s_swappc_b64 s[84:85], s[84:85]\n"
      "s_waitcnt vmcnt(12)\n"
      "v_sub_u32 v250, s82, v250\n"
      "v_cmp_gt_u32 vcc, 1600, v250\n"
      "s_nop 3\n"
      "s_bcnt1_i32_b32 s67, vcc_lo\n"
      "s_cmp_ge_u32 s67, 7\n"
      "s_cbranch_scc1 SM_%=\n"
      "s_swappc_b64 s[84:85], s[84:85]\n"
      "s_mov_b32 s86, 53\n"
      "s_mov_b32 s77, 159\n"
      "s_swappc_b64 s[84:85], s[84:85]\n"
      "s_getpc_b64 s[74:75]\n"
      "PF_%=:\n"
      "s_sub_u32 s74, s74, PF_%=-END_%=\n"
      "s_subb_u32 s75, s75, 0\n"
      "s_lshl_b32 s76, %[blk], 7\n"
      "s_and_b32 s76, s76, 0x1c00\n"
      "s_min_u32 s76, s76, ENDE_%=-END_%=-1024\n"
      "v_add_u32 v236, s76, v241\n"
      "global_load_dwordx4 v[236:239], v236, s[74:75]\n"
      "s_swappc_b64 s[84:85], s[84:85]\n"
      "SM_%=:\n"
      "s_or_b32 %[mB], %[mB], 0xe0000000\n"
      "s_mov_b32 s52, 0xaaaaaaaa\n"
      "s_mov_b32 s53, 0xaaaaaaaa\n"
      "s_add_u32 s66, s66, 110\n"
      "s_max_u32 s88, s66, s65\n"
      "s_mov_b32 s80, 85\n"
      "s_mov_b32 s86, 66\n"
      "s_mov_b32 s77, 198\n"
      "s_swappc_b64 s[84:85], s[84:85]\n"
      "s_mov_b32 s86, 62\n"
      "s_mov_b32 s77, 186\n"
      "s_swappc_b64 s[84:85], s[84:85]\n"
      "s_mov_b32 s86, 59\n"
      "s_mov_b32 s77, 177\n"
      "s_swappc_b64 s[84:85], s[84:85]\n"
      "s_mov_b32 s86, 56\n"
      "s_mov_b32 s77, 168\n"
      "s_mov_b32 s52, 0x0\n"
      "s_mov_b32 s53, 0x80000\n"
      "s_swappc_b64 s[84:85], s[84:85]\n"
      "s_getpc_b64 s[74:75]\n"
      "PG_%=:\n"
      "s_sub_u32 s74, s74, PG_%=-END_%=\n"
      "s_subb_u32 s75, s75, 0\n"
      "s_lshl_b32 s76, %[blk], 7\n"
      "s_and_b32 s76, s76, 0x1c00\n"
      "s_min_u32 s76, s76, ENDE_%=-END_%=-1024\n"
      "v_add_u32 v236, s76, v241\n"
      "global_load_dwordx4 v[236:239], v236, s[74:75]\n"
      "s_swappc_b64 s[84:85], s[84:85]\n"
      "GW0_%=:\n"
      "s_memrealtime s[68:69]\n"
      "s_waitcnt lgkmcnt(0)\n"
      "s_getreg_b32 s67, hwreg(HW_REG_IB_STS, 0, 4)\n"
      "s_cmp_gt_u32 s67, 9\n"
      "s_addc_u32 s89, s89, 0\n"
      "GS0_%=:\n"
      "s_sub_u32 s67, s68, s88\n"
      "s_cmp_ge_i32 s67, 0\n"
      "s_cbranch_scc1 GX0_%=\n"
      "s_memrealtime s[68:69]\n"
      "s_waitcnt lgkmcnt(0)\n"
      "s_branch GS0_%=\n"
      "FS1_%=:\n"
      "s_call_b64 s[96:97], FB1_%=\n"
      "s_branch FR1_%=\n"
      "FS2_%=:\n"
      "s_call_b64 s[96:97], FB0_%=\n"
      "s_branch FR2_%=\n"
      "FS3_%=:\n"
      "s_call_b64 s[96:97], FB0_%=\n"
      "s_branch FR3_%=\n"
      "GW4_%=:\n"
      "s_memrealtime s[68:69]\n"
      "s_waitcnt lgkmcnt(0)\n"
      "s_getreg_b32 s67, hwreg(HW_REG_IB_STS, 0, 4)\n"
      "s_cmp_gt_u32 s67, 9\n"
      "s_addc_u32 s89, s89, 0\n"
      "GS4_%=:\n"
      "s_sub_u32 s67, s68, s88\n"
      "s_cmp_ge_i32 s67, 0\n"
      "s_cbranch_scc1 GX4_%=\n"
      "s_memrealtime s[68:69]\n"
      "s_waitcnt lgkmcnt(0)\n"
      "s_branch GS4_%=\n"
      "GW5_%=:\n"
      "s_memrealtime s[68:69]\n"
      "s_waitcnt lgkmcnt(0)\n"
      "s_getreg_b32 s67, hwreg(HW_REG_IB_STS, 0, 4)\n"
      "s_cmp_gt_u32 s67, 9\n"
      "s_addc_u32 s89, s89, 0\n"
      "GS5_%=:\n"
      "s_sub_u32 s67, s68, s88\n"
      "s_cmp_ge_i32 s67, 0\n"
      "s_cbranch_scc1 GX5_%=\n"
      "s_memrealtime s[68:69]\n"
      "s_waitcnt lgkmcnt(0)\n"
      "s_branch GS5_%=\n"
      "GW6_%=:\n"
      "s_memrealtime s[68:69]\n"
      "s_waitcnt lgkmcnt(0)\n"
      "s_getreg_b32 s67, hwreg(HW_REG_IB_STS, 0, 4)\n"
      "s_cmp_gt_u32 s67, 9\n"
      "s_addc_u32 s89, s89, 0\n"
      "GS6_%=:\n"
      "s_sub_u32 s67, s68, s88\n"
      "s_cmp_ge_i32 s67, 0\n"
      "s_cbranch_scc1 GX6_%=\n"
      "s_memrealtime s[68:69]\n"
      "s_waitcnt lgkmcnt(0)\n"
      "s_branch GS6_%=\n"
      "GW7_%=:\n"
      "s_memrealtime s[68:69]\n"
      "s_waitcnt lgkmcnt(0)\n"
      "s_getreg_b32 s67, hwreg(HW_REG_IB_STS, 0, 4)\n"
      "s_cmp_gt_u32 s67, 9\n"
      "s_addc_u32 s89, s89, 0\n"
      "GS7_%=:\n"
      "s_sub_u32 s67, s68, s88\n"
      "s_cmp_ge_i32 s67, 0\n"
      "s_cbranch_scc1 GX7_%=\n"
      "s_memrealtime s[68:69]\n"
      "s_waitcnt lgkmcnt(0)\n"
      "s_branch GS7_%=\n"
      "FS8_%=:\n"
      "s_call_b64 s[96:97], FB1_%=\n"
      "s_branch FR8_%=\n"
      "LAG_%=:\n"
      "s_memrealtime s[68:69]\n"
      "s_waitcnt lgkmcnt(0)\n"
      "s_mov_b32 s88, s68\n"
      "LG1_%=:\n"
      "v_cmp_ne_u32 vcc, %[e0], v207\n"
      "s_cbranch_vccz LG2_%=\n"
      "s_sub_u32 s63, s63, 1\n"
      "s_cmp_eq_u32 s63, 0\n"
      "s_cbranch_scc1 READ_%=\n"
      "s_sleep 4\n"
      "global_load_dwordx2 v[206:207], v239, %[line] offset:8 sc1\n"
      "s_waitcnt vmcnt(0)\n"
      "s_branch LG1_%=\n"
      "LG2_%=:\n"
      "v_readfirstlane_b32 s67, v206\n"
      "s_sub_u32 s88, s88, s82\n"
      "s_min_u32 s88, s88, 400\n"
      "s_add_u32 s88, s88, s67\n"
      "LG3_%=:\n"
      "s_memrealtime s[68:69]\n"
      "s_waitcnt lgkmcnt(0)\n"
      "s_sub_u32 s67, s88, s68\n"
      "s_sub_u32 s67, s67, 17\n"
      "s_cmp_lt_u32 s67, 400\n"
      "s_cbranch_scc1 LG3_%=\n"
      "s_branch READ_%=\n"
      "RETRY_%=:\n"
      "s_sub_u32 s63, s63, 1\n"
      "s_cbranch_scc1 GOT_%=\n"
      "buffer_load_dwordx3 v[128:130], v240, %[srdx], s76 offen sc1\n"
      "buffer_load_dwordx4 v[104:107], v241, %[srdx], s76 offen offset:2048 sc1\n"
      "buffer_load_dwordx4 v[108:111], v241, %[srdx], s76 offen offset:3072 sc1\n"
      "buffer_load_dwordx4 v[96:99], v241, %[srdx], s76 offen offset:0 sc1\n"
      "buffer_load_dwordx4 v[100:103], v241, %[srdx], s76 offen offset:1024 sc1\n"
      "s_branch TRY_%=\n"
      "FS11_%=:\n"
      "s_call_b64 s[96:97], FB0_%=\n"
      "s_branch FR11_%=\n"
      "COLD_%=:\n"
      "FB0_%=:\n"
      "v_mov_b32 v196, 139248\n"
      "s_nop 7\n"
      "ds_write_b32 v196, v196\n"
      "v_max3_f32 v188, v64, v65, 0\n"
      "v_max3_f32 v189, v72, v73, 0\n"
      "v_max3_f32 v190, v80, v81, 0\n"
      "v_max3_f32 v191, v88, v89, 0\n"
      "v_max3_f32 v188, v188, v66, v67\n"
      "v_max3_f32 v189, v189, v74, v75\n"
      "v_max3_f32 v190, v190, v82, v83\n"
      "v_max3_f32 v191, v191, v90, v91\n"
      "v_max3_f32 v188, v188, v68, v69\n"
      "v_max3_f32 v189, v189, v76, v77\n"
      "v_max3_f32 v190, v190, v84, v85\n"
      "v_max3_f32 v191, v191, v92, v93\n"
      "v_max3_f32 v188, v188, v70, v71\n"
      "v_max3_f32 v189, v189, v78, v79\n"
      "v_max3_f32 v190, v190, v86, v87\n"
      "v_max3_f32 v191, v191, v94, v95\n"
      "v_mov_b32 v192, v188\n"
      "v_mov_b32 v193, v189\n"
      "v_mov_b32 v194, v190\n"
      "v_mov_b32 v195, v191\n"
      "v_permlane16_swap_b32 v188, v192\n"
      "v_permlane16_swap_b32 v189, v193\n"
      "v_permlane16_swap_b32 v190, v194\n"
      "v_permlane16_swap_b32 v191, v195\n"
      "v_max_f32 v188, v188, v192\n"
      "v_max_f32 v189, v189, v193\n"
      "v_max_f32 v190, v190, v194\n"
      "v_max_f32 v191, v191, v195\n"
      "v_mov_b32 v192, v188\n"
      "v_mov_b32 v193, v189\n"
      "v_mov_b32 v194, v190\n"
      "v_mov_b32 v195, v191\n"
      "v_permlane32_swap_b32 v188, v192\n"
      "v_permlane32_swap_b32 v189, v193\n"
      "v_permlane32_swap_b32 v190, v194\n"
      "v_permlane32_swap_b32 v191, v195\n"
      "v_max_f32 v188, v188, v192\n"
      "v_max_f32 v189, v189, v193\n"
      "v_max_f32 v190, v190, v194\n"
      "v_max_f32 v191, v191, v195\n"
      "RS0_%=:\n"
      "v_exp_f32 v192, -v188\n"
      "v_exp_f32 v193, -v189\n"
      "v_exp_f32 v194, -v190\n"
      "v_exp_f32 v195, -v191\n"
      "v_sub_f32 v176, v176, v188\n"
      "v_sub_f32 v177, v177, v189\n"
      "v_sub_f32 v178, v178, v190\n"
      "v_sub_f32 v179, v179, v191\n"
      "v_mov_b32 v160, v176\n"
      "v_mov_b32 v161, v176\n"
      "v_mov_b32 v162, v176\n"
      "v_mov_b32 v163, v176\n"
      "v_mov_b32 v164, v177\n"
      "v_mov_b32 v165, v177\n"
      "v_mov_b32 v166, v177\n"
      "v_mov_b32 v167, v177\n"
      "v_mov_b32 v168, v178\n"
      "v_mov_b32 v169, v178\n"
      "v_mov_b32 v170, v178\n"
      "v_mov_b32 v171, v178\n"
      "v_mov_b32 v172, v179\n"
      "v_mov_b32 v173, v179\n"
      "v_mov_b32 v174, v179\n"
      "v_mov_b32 v175, v179\n"
      "v_sub_f32 v64, v64, v188\n"
      "v_sub_f32 v65, v65, v188\n"
      "v_sub_f32 v66, v66, v188\n"
      "v_sub_f32 v67, v67, v188\n"
      "v_sub_f32 v68, v68, v188\n"
      "v_sub_f32 v69, v69, v188\n"
      "v_sub_f32 v70, v70, v188\n"
      "v_sub_f32 v71, v71, v188\n"
      "v_sub_f32 v72, v72, v189\n"
      "v_sub_f32 v73, v73, v189\n"
      "v_sub_f32 v74, v74, v189\n"
      "v_sub_f32 v75, v75, v189\n"
      "v_sub_f32 v76, v76, v189\n"
      "v_sub_f32 v77, v77, v189\n"
      "v_sub_f32 v78, v78, v189\n"
      "v_sub_f32 v79, v79, v189\n"
      "v_sub_f32 v80, v80, v190\n"
      "v_sub_f32 v81, v81, v190\n"
      "v_sub_f32 v82, v82, v190\n"
      "v_sub_f32 v83, v83, v190\n"
      "v_sub_f32 v84, v84, v190\n"
      "v_sub_f32 v85, v85, v190\n"
      "v_sub_f32 v86, v86, v190\n"
      "v_sub_f32 v87, v87, v190\n"
      "v_sub_f32 v88, v88, v191\n"
      "v_sub_f32 v89, v89, v191\n"
      "v_sub_f32 v90, v90, v191\n"
      "v_sub_f32 v91, v91, v191\n"
      "v_sub_f32 v92, v92, v191\n"
      "v_sub_f32 v93, v93, v191\n"
      "v_sub_f32 v94, v94, v191\n"
      "v_sub_f32 v95, v95, v191\n"
      "v_sub_f32 v96, v96, v188\n"
      "v_sub_f32 v97, v97, v188\n"
      "v_sub_f32 v98, v98, v188\n"
      "v_sub_f32 v99, v99, v188\n"
      "v_sub_f32 v100, v100, v188\n"
      "v_sub_f32 v101, v101, v188\n"
      "v_sub_f32 v102, v102, v188\n"
      "v_sub_f32 v103, v103, v188\n"
      "v_sub_f32 v104, v104, v189\n"
      "v_sub_f32 v105, v105, v189\n"
      "v_sub_f32 v106, v106, v189\n"
      "v_sub_f32 v107, v107, v189\n"
      "v_sub_f32 v108, v108, v189\n"
      "v_sub_f32 v109, v109, v189\n"
      "v_sub_f32 v110, v110, v189\n"
      "v_sub_f32 v111, v111, v189\n"
      "v_sub_f32 v112, v112, v190\n"
      "v_sub_f32 v113, v113, v190\n"
      "v_sub_f32 v114, v114, v190\n"
      "v_sub_f32 v115, v115, v190\n"
      "v_sub_f32 v116, v116, v190\n"
      "v_sub_f32 v117, v117, v190\n"
      "v_sub_f32 v118, v118, v190\n"
      "v_sub_f32 v119, v119, v190\n"
      "v_sub_f32 v120, v120, v191\n"
      "v_sub_f32 v121, v121, v191\n"
      "v_sub_f32 v122, v122, v191\n"
      "v_sub_f32 v123, v123, v191\n"
      "v_sub_f32 v124, v124, v191\n"
      "v_sub_f32 v125, v125, v191\n"
      "v_sub_f32 v126, v126, v191\n"
      "v_sub_f32 v127, v127, v191\n"
      "v_mul_f32 v0, v0, v192\n"
      "v_mul_f32 v1, v1, v192\n"
      "v_mul_f32 v2, v2, v192\n"
      "v_mul_f32 v3, v3, v192\n"
      "v_mul_f32 v4, v4, v193\n"
      "v_mul_f32 v5, v5, v193\n"
      "v_mul_f32 v6, v6, v193\n"
      "v_mul_f32 v7, v7, v193\n"
      "v_mul_f32 v8, v8, v194\n"
      "v_mul_f32 v9, v9, v194\n"
      "v_mul_f32 v10, v10, v194\n"
      "v_mul_f32 v11, v11, v194\n"
      "v_mul_f32 v12, v12, v195\n"
      "v_mul_f32 v13, v13, v195\n"
      "v_mul_f32 v14, v14, v195\n"
      "v_mul_f32 v15, v15, v195\n"
      "v_mul_f32 v16, v16, v192\n"
      "v_mul_f32 v17, v17, v192\n"
      "v_mul_f32 v18, v18, v192\n"
      "v_mul_f32 v19, v19, v192\n"
      "v_mul_f32 v20, v20, v193\n"
      "v_mul_f32 v21, v21, v193\n"
      "v_mul_f32 v22, v22, v193\n"
      "v_mul_f32 v23, v23, v193\n"
      "v_mul_f32 v24, v24, v194\n"
      "v_mul_f32 v25, v25, v194\n"
      "v_mul_f32 v26, v26, v194\n"
      "v_mul_f32 v27, v27, v194\n"
      "v_mul_f32 v28, v28, v195\n"
      "v_mul_f32 v29, v29, v195\n"
      "v_mul_f32 v30, v30, v195\n"
      "v_mul_f32 v31, v31, v195\n"
      "v_mul_f32 v32, v32, v192\n"
      "v_mul_f32 v33, v33, v192\n"
      "v_mul_f32 v34, v34, v192\n"
      "v_mul_f32 v35, v35, v192\n"
      "v_mul_f32 v36, v36, v193\n"
      "v_mul_f32 v37, v37, v193\n"
      "v_mul_f32 v38, v38, v193\n"
      "v_mul_f32 v39, v39, v193\n"
      "v_mul_f32 v40, v40, v194\n"
      "v_mul_f32 v41, v41, v194\n"
      "v_mul_f32 v42, v42, v194\n"
      "v_mul_f32 v43, v43, v194\n"
      "v_mul_f32 v44, v44, v195\n"
      "v_mul_f32 v45, v45, v195\n"
      "v_mul_f32 v46, v46, v195\n"
      "v_mul_f32 v47, v47, v195\n"
      "v_mul_f32 v48, v48, v192\n"
      "v_mul_f32 v49, v49, v192\n"
      "v_mul_f32 v50, v50, v192\n"
      "v_mul_f32 v51, v51, v192\n"
      "v_mul_f32 v52, v52, v193\n"
      "v_mul_f32 v53, v53, v193\n"
      "v_mul_f32 v54, v54, v193\n"
      "v_mul_f32 v55, v55, v193\n"
      "v_mul_f32 v56, v56, v194\n"
      "v_mul_f32 v57, v57, v194\n"
      "v_mul_f32 v58, v58, v194\n"
      "v_mul_f32 v59, v59, v194\n"
      "v_mul_f32 v60, v60, v195\n"
      "v_mul_f32 v61, v61, v195\n"
      "v_mul_f32 v62, v62, v195\n"
      "v_mul_f32 v63, v63, v195\n"
      "v_accvgpr_read_b32 v196, a144\n"
      "v_accvgpr_read_b32 v197, a145\n"
      "v_accvgpr_read_b32 v198, a146\n"
      "v_accvgpr_read_b32 v199, a147\n"
      "v_mul_f32 v196, v196, v192\n"
      "v_mul_f32 v197, v197, v192\n"
      "v_mul_f32 v198, v198, v192\n"
      "v_mul_f32 v199, v199, v192\n"
      "v_accvgpr_write_b32 a144, v196\n"
      "v_accvgpr_write_b32 a145, v197\n"
      "v_accvgpr_write_b32 a146, v198\n"
      "v_accvgpr_write_b32 a147, v199\n"
      "v_accvgpr_read_b32 v196, a148\n"
      "v_accvgpr_read_b32 v197, a149\n"
      "v_accvgpr_read_b32 v198, a150\n"
      "v_accvgpr_read_b32 v199, a151\n"
      "v_mul_f32 v196, v196, v193\n"
      "v_mul_f32 v197, v197, v193\n"
      "v_mul_f32 v198, v198, v193\n"
      "v_mul_f32 v199, v199, v193\n"
      "v_accvgpr_write_b32 a148, v196\n"
      "v_accvgpr_write_b32 a149, v197\n"
      "v_accvgpr_write_b32 a150, v198\n"
      "v_accvgpr_write_b32 a151, v199\n"
      "v_accvgpr_read_b32 v196, a152\n"
      "v_accvgpr_read_b32 v197, a153\n"
      "v_accvgpr_read_b32 v198, a154\n"
      "v_accvgpr_read_b32 v199, a155\n"
      "v_mul_f32 v196, v196, v194\n"
      "v_mul_f32 v197, v197, v194\n"
      "v_mul_f32 v198, v198, v194\n"
      "v_mul_f32 v199, v199, v194\n"
      "v_accvgpr_write_b32 a152, v196\n"
      "v_accvgpr_write_b32 a153, v197\n"
      "v_accvgpr_write_b32 a154, v198\n"
      "v_accvgpr_write_b32 a155, v199\n"
      "v_accvgpr_read_b32 v196, a156\n"
      "v_accvgpr_read_b32 v197, a157\n"
      "v_accvgpr_read_b32 v198, a158\n"
      "v_accvgpr_read_b32 v199, a159\n"
      "v_mul_f32 v196, v196, v195\n"
      "v_mul_f32 v197, v197, v195\n"
      "v_mul_f32 v198, v198, v195\n"
      "v_mul_f32 v199, v199, v195\n"
      "v_accvgpr_write_b32 a156, v196\n"
      "v_accvgpr_write_b32 a157, v197\n"
      "v_accvgpr_write_b32 a158, v198\n"
      "v_accvgpr_write_b32 a159, v199\n"
      "v_accvgpr_read_b32 v196, a160\n"
      "v_accvgpr_read_b32 v197, a161\n"
      "v_accvgpr_read_b32 v198, a162\n"
      "v_accvgpr_read_b32 v199, a163\n"
      "v_mul_f32 v196, v196, v192\n"
      "v_mul_f32 v197, v197, v192\n"
      "v_mul_f32 v198, v198, v192\n"
      "v_mul_f32 v199, v199, v192\n"
      "v_accvgpr_write_b32 a160, v196\n"
      "v_accvgpr_write_b32 a161, v197\n"
      "v_accvgpr_write_b32 a162, v198\n"
      "v_accvgpr_write_b32 a163, v199\n"
      "v_accvgpr_read_b32 v196, a164\n"
      "v_accvgpr_read_b32 v197, a165\n"
      "v_accvgpr_read_b32 v198, a166\n"
      "v_accvgpr_read_b32 v199, a167\n"
      "v_mul_f32 v196, v196, v193\n"
      "v_mul_f32 v197, v197, v193\n"
      "v_mul_f32 v198, v198, v193\n"
      "v_mul_f32 v199, v199, v193\n"
      "v_accvgpr_write_b32 a164, v196\n"
      "v_accvgpr_write_b32 a165, v197\n"
      "v_accvgpr_write_b32 a166, v198\n"
      "v_accvgpr_write_b32 a167, v199\n"
      "v_accvgpr_read_b32 v196, a168\n"
      "v_accvgpr_read_b32 v197, a169\n"
      "v_accvgpr_read_b32 v198, a170\n"
      "v_accvgpr_read_b32 v199, a171\n"
      "v_mul_f32 v196, v196, v194\n"
      "v_mul_f32 v197, v197, v194\n"
      "v_mul_f32 v198, v198, v194\n"
      "v_mul_f32 v199, v199, v194\n"
      "v_accvgpr_write_b32 a168, v196\n"
      "v_accvgpr_write_b32 a169, v197\n"
      "v_accvgpr_write_b32 a170, v198\n"
      "v_accvgpr_write_b32 a171, v199\n"
      "v_accvgpr_read_b32 v196, a172\n"
      "v_accvgpr_read_b32 v197, a173\n"
      "v_accvgpr_read_b32 v198, a174\n"
      "v_accvgpr_read_b32 v199, a175\n"
      "v_mul_f32 v196, v196, v195\n"
      "v_mul_f32 v197, v197, v195\n"
      "v_mul_f32 v198, v198, v195\n"
      "v_mul_f32 v199, v199, v195\n"
      "v_accvgpr_write_b32 a172, v196\n"
      "v_accvgpr_write_b32 a173, v197\n"
      "v_accvgpr_write_b32 a174, v198\n"
      "v_accvgpr_write_b32 a175, v199\n"
      "v_accvgpr_read_b32 v196, a176\n"
      "v_accvgpr_read_b32 v197, a177\n"
      "v_accvgpr_read_b32 v198, a178\n"
      "v_accvgpr_read_b32 v199, a179\n"
      "v_mul_f32 v196, v196, v192\n"
      "v_mul_f32 v197, v197, v192\n"
      "v_mul_f32 v198, v198, v192\n"
      "v_mul_f32 v199, v199, v192\n"
      "v_accvgpr_write_b32 a176, v196\n"
      "v_accvgpr_write_b32 a177, v197\n"
      "v_accvgpr_write_b32 a178, v198\n"
      "v_accvgpr_write_b32 a179, v199\n"
      "v_accvgpr_read_b32 v196, a180\n"
      "v_accvgpr_read_b32 v197, a181\n"
      "v_accvgpr_read_b32 v198, a182\n"
      "v_accvgpr_read_b32 v199, a183\n"
      "v_mul_f32 v196, v196, v193\n"
      "v_mul_f32 v197, v197, v193\n"
      "v_mul_f32 v198, v198, v193\n"
      "v_mul_f32 v199, v199, v193\n"
      "v_accvgpr_write_b32 a180, v196\n"
      "v_accvgpr_write_b32 a181, v197\n"
      "v_accvgpr_write_b32 a182, v198\n"
      "v_accvgpr_write_b32 a183, v199\n"
      "v_accvgpr_read_b32 v196, a184\n"
      "v_accvgpr_read_b32 v197, a185\n"
      "v_accvgpr_read_b32 v198, a186\n"
      "v_accvgpr_read_b32 v199, a187\n"
      "v_mul_f32 v196, v196, v194\n"
      "v_mul_f32 v197, v197, v194\n"
      "v_mul_f32 v198, v198, v194\n"
      "v_mul_f32 v199, v199, v194\n"
      "v_accvgpr_write_b32 a184, v196\n"
      "v_accvgpr_write_b32 a185, v197\n"
      "v_accvgpr_write_b32 a186, v198\n"
      "v_accvgpr_write_b32 a187, v199\n"
      "v_accvgpr_read_b32 v196, a188\n"
      "v_accvgpr_read_b32 v197, a189\n"
      "v_accvgpr_read_b32 v198, a190\n"
      "v_accvgpr_read_b32 v199, a191\n"
      "v_mul_f32 v196, v196, v195\n"
      "v_mul_f32 v197, v197, v195\n"
      "v_mul_f32 v198, v198, v195\n"
      "v_mul_f32 v199, v199, v195\n"
      "v_accvgpr_write_b32 a188, v196\n"
      "v_accvgpr_write_b32 a189, v197\n"
      "v_accvgpr_write_b32 a190, v198\n"
      "v_accvgpr_write_b32 a191, v199\n"
      "v_accvgpr_read_b32 v196, a192\n"
      "v_accvgpr_read_b32 v197, a193\n"
      "v_accvgpr_read_b32 v198, a194\n"
      "v_accvgpr_read_b32 v199, a195\n"
      "v_mul_f32 v196, v196, v192\n"
      "v_mul_f32 v197, v197, v192\n"
      "v_mul_f32 v198, v198, v192\n"
      "v_mul_f32 v199, v199, v192\n"
      "v_accvgpr_write_b32 a192, v196\n"
      "v_accvgpr_write_b32 a193, v197\n"
      "v_accvgpr_write_b32 a194, v198\n"
      "v_accvgpr_write_b32 a195, v199\n"
      "v_accvgpr_read_b32 v196, a196\n"
      "v_accvgpr_read_b32 v197, a197\n"
      "v_accvgpr_read_b32 v198, a198\n"
      "v_accvgpr_read_b32 v199, a199\n"
      "v_mul_f32 v196, v196, v193\n"
      "v_mul_f32 v197, v197, v193\n"
      "v_mul_f32 v198, v198, v193\n"
      "v_mul_f32 v199, v199, v193\n"
      "v_accvgpr_write_b32 a196, v196\n"
      "v_accvgpr_write_b32 a197, v197\n"
      "v_accvgpr_write_b32 a198, v198\n"
      "v_accvgpr_write_b32 a199, v199\n"
      "v_accvgpr_read_b32 v196, a200\n"
      "v_accvgpr_read_b32 v197, a201\n"
      "v_accvgpr_read_b32 v198, a202\n"
      "v_accvgpr_read_b32 v199, a203\n"
      "v_mul_f32 v196, v196, v194\n"
      "v_mul_f32 v197, v197, v194\n"
      "v_mul_f32 v198, v198, v194\n"
      "v_mul_f32 v199, v199, v194\n"
      "v_accvgpr_write_b32 a200, v196\n"
      "v_accvgpr_write_b32 a201, v197\n"
      "v_accvgpr_write_b32 a202, v198\n"
      "v_accvgpr_write_b32 a203, v199\n"
      "v_accvgpr_read_b32 v196, a204\n"
      "v_accvgpr_read_b32 v197, a205\n"
      "v_accvgpr_read_b32 v198, a206\n"
      "v_accvgpr_read_b32 v199, a207\n"
      "v_mul_f32 v196, v196, v195\n"
      "v_mul_f32 v197, v197, v195\n"
      "v_mul_f32 v198, v198, v195\n"
      "v_mul_f32 v199, v199, v195\n"
      "v_accvgpr_write_b32 a204, v196\n"
      "v_accvgpr_write_b32 a205, v197\n"
      "v_accvgpr_write_b32 a206, v198\n"
      "v_accvgpr_write_b32 a207, v199\n"
      "v_mul_f32 v180, v180, v192\n"
      "v_mul_f32 v181, v181, v193\n"
      "v_mul_f32 v182, v182, v194\n"
      "v_mul_f32 v183, v183, v195\n"
      "v_exp_f32 v188, v64\n"
      "v_exp_f32 v189, v65\n"
      "v_exp_f32 v190, v66\n"
      "v_exp_f32 v191, v67\n"
      "v_cvt_pk_bf16_f32 v128, v188, v189\n"
      "v_cvt_pk_bf16_f32 v129, v190, v191\n"
      "v_exp_f32 v192, v68\n"
      "v_exp_f32 v193, v69\n"
      "v_exp_f32 v194, v70\n"
      "v_exp_f32 v195, v71\n"
      "v_cvt_pk_bf16_f32 v130, v192, v193\n"
      "v_cvt_pk_bf16_f32 v131, v194, v195\n"
      "v_exp_f32 v188, v72\n"
      "v_exp_f32 v189, v73\n"
      "v_exp_f32 v190, v74\n"
      "v_exp_f32 v191, v75\n"
      "v_cvt_pk_bf16_f32 v132, v188, v189\n"
      "v_cvt_pk_bf16_f32 v133, v190, v191\n"
      "v_exp_f32 v192, v76\n"
      "v_exp_f32 v193, v77\n"
      "v_exp_f32 v194, v78\n"
      "v_exp_f32 v195, v79\n"
      "v_cvt_pk_bf16_f32 v134, v192, v193\n"
      "v_cvt_pk_bf16_f32 v135, v194, v195\n"
      "v_exp_f32 v188, v80\n"
      "v_exp_f32 v189, v81\n"
      "v_exp_f32 v190, v82\n"
      "v_exp_f32 v191, v83\n"
      "v_cvt_pk_bf16_f32 v136, v188, v189\n"
      "v_cvt_pk_bf16_f32 v137, v190, v191\n"
      "v_exp_f32 v192, v84\n"
      "v_exp_f32 v193, v85\n"
      "v_exp_f32 v194, v86\n"
      "v_exp_f32 v195, v87\n"
      "v_cvt_pk_bf16_f32 v138, v192, v193\n"
      "v_cvt_pk_bf16_f32 v139, v194, v195\n"
      "v_exp_f32 v188, v88\n"
      "v_exp_f32 v189, v89\n"
      "v_exp_f32 v190, v90\n"
      "v_exp_f32 v191, v91\n"
      "v_cvt_pk_bf16_f32 v140, v188, v189\n"
      "v_cvt_pk_bf16_f32 v141, v190, v191\n"
      "v_exp_f32 v192, v92\n"
      "v_exp_f32 v193, v93\n"
      "v_exp_f32 v194, v94\n"
      "v_exp_f32 v195, v95\n"
      "v_cvt_pk_bf16_f32 v142, v192, v193\n"
      "v_cvt_pk_bf16_f32 v143, v194, v195\n"
      "s_nop 1\n"
      "v_mfma_f32_16x16x32_bf16 v[184:187], a[128:131], v[128:131], 0\n"
      "v_mfma_f32_16x16x32_bf16 v[184:187], a[132:135], v[132:135], v[184:187]\n"
      "v_mfma_f32_16x16x32_bf16 v[184:187], a[136:139], v[136:139], v[184:187]\n"
      "v_mfma_f32_16x16x32_bf16 v[184:187], a[140:143], v[140:143], v[184:187]\n"
      "s_nop 7\n"
      "s_setpc_b64 s[96:97]\n"
      "FB1_%=:\n"
      "v_mov_b32 v196, 139248\n"
      "s_nop 7\n"
      "ds_write_b32 v196, v196\n"
      "v_max3_f32 v188, v96, v97, 0\n"
      "v_max3_f32 v189, v104, v105, 0\n"
      "v_max3_f32 v190, v112, v113, 0\n"
      "v_max3_f32 v191, v120, v121, 0\n"
      "v_max3_f32 v188, v188, v98, v99\n"
      "v_max3_f32 v189, v189, v106, v107\n"
      "v_max3_f32 v190, v190, v114, v115\n"
      "v_max3_f32 v191, v191, v122, v123\n"
      "v_max3_f32 v188, v188, v100, v101\n"
      "v_max3_f32 v189, v189, v108, v109\n"
      "v_max3_f32 v190, v190, v116, v117\n"
      "v_max3_f32 v191, v191, v124, v125\n"
      "v_max3_f32 v188, v188, v102, v103\n"
      "v_max3_f32 v189, v189, v110, v111\n"
      "v_max3_f32 v190, v190, v118, v119\n"
      "v_max3_f32 v191, v191, v126, v127\n"
      "v_mov_b32 v192, v188\n"
      "v_mov_b32 v193, v189\n"
      "v_mov_b32 v194, v190\n"
      "v_mov_b32 v195, v191\n"
      "v_permlane16_swap_b32 v188, v192\n"
      "v_permlane16_swap_b32 v189, v193\n"
      "v_permlane16_swap_b32 v190, v194\n"
      "v_permlane16_swap_b32 v191, v195\n"
      "v_max_f32 v188, v188, v192\n"
      "v_max_f32 v189, v189, v193\n"
      "v_max_f32 v190, v190, v194\n"
      "v_max_f32 v191, v191, v195\n"
      "v_mov_b32 v192, v188\n"
      "v_mov_b32 v193, v189\n"
      "v_mov_b32 v194, v190\n"
      "v_mov_b32 v195, v191\n"
      "v_permlane32_swap_b32 v188, v192\n"
      "v_permlane32_swap_b32 v189, v193\n"
      "v_permlane32_swap_b32 v190, v194\n"
      "v_permlane32_swap_b32 v191, v195\n"
      "v_max_f32 v188, v188, v192\n"
      "v_max_f32 v189, v189, v193\n"
      "v_max_f32 v190, v190, v194\n"
      "v_max_f32 v191, v191, v195\n"
      "RS1_%=:\n"
      "v_exp_f32 v192, -v188\n"
      "v_exp_f32 v193, -v189\n"
      "v_exp_f32 v194, -v190\n"
      "v_exp_f32 v195, -v191\n"
      "v_sub_f32 v176, v176, v188\n"
      "v_sub_f32 v177, v177, v189\n"
      "v_sub_f32 v178, v178, v190\n"
      "v_sub_f32 v179, v179, v191\n"
      "v_mov_b32 v160, v176\n"
      "v_mov_b32 v161, v176\n"
      "v_mov_b32 v162, v176\n"
      "v_mov_b32 v163, v176\n"
      "v_mov_b32 v164, v177\n"
      "v_mov_b32 v165, v177\n"
      "v_mov_b32 v166, v177\n"
      "v_mov_b32 v167, v177\n"
      "v_mov_b32 v168, v178\n"
      "v_mov_b32 v169, v178\n"
      "v_mov_b32 v170, v178\n"
      "v_mov_b32 v171, v178\n"
      "v_mov_b32 v172, v179\n"
      "v_mov_b32 v173, v179\n"
      "v_mov_b32 v174, v179\n"
      "v_mov_b32 v175, v179\n"
      "v_sub_f32 v64, v64, v188\n"
      "v_sub_f32 v65, v65, v188\n"
      "v_sub_f32 v66, v66, v188\n"
      "v_sub_f32 v67, v67, v188\n"
      "v_sub_f32 v68, v68, v188\n"
      "v_sub_f32 v69, v69, v188\n"
      "v_sub_f32 v70, v70, v188\n"
      "v_sub_f32 v71, v71, v188\n"
      "v_sub_f32 v72, v72, v189\n"
      "v_sub_f32 v73, v73, v189\n"
      "v_sub_f32 v74, v74, v189\n"
      "v_sub_f32 v75, v75, v189\n"
      "v_sub_f32 v76, v76, v189\n"
      "v_sub_f32 v77, v77, v189\n"
      "v_sub_f32 v78, v78, v189\n"
      "v_sub_f32 v79, v79, v189\n"
      "v_sub_f32 v80, v80, v190\n"
      "v_sub_f32 v81, v81, v190\n"
      "v_sub_f32 v82, v82, v190\n"
      "v_sub_f32 v83, v83, v190\n"
      "v_sub_f32 v84, v84, v190\n"
      "v_sub_f32 v85, v85, v190\n"
      "v_sub_f32 v86, v86, v190\n"
      "v_sub_f32 v87, v87, v190\n"
      "v_sub_f32 v88, v88, v191\n"
      "v_sub_f32 v89, v89, v191\n"
      "v_sub_f32 v90, v90, v191\n"
      "v_sub_f32 v91, v91, v191\n"
      "v_sub_f32 v92, v92, v191\n"
      "v_sub_f32 v93, v93, v191\n"
      "v_sub_f32 v94, v94, v191\n"
      "v_sub_f32 v95, v95, v191\n"
      "v_sub_f32 v96, v96, v188\n"
      "v_sub_f32 v97, v97, v188\n"
      "v_sub_f32 v98, v98, v188\n"
      "v_sub_f32 v99, v99, v188\n"
      "v_sub_f32 v100, v100, v188\n"
      "v_sub_f32 v101, v101, v188\n"
      "v_sub_f32 v102, v102, v188\n"
      "v_sub_f32 v103, v103, v188\n"
      "v_sub_f32 v104, v104, v189\n"
      "v_sub_f32 v105, v105, v189\n"
      "v_sub_f32 v106, v106, v189\n"
      "v_sub_f32 v107, v107, v189\n"
      "v_sub_f32 v108, v108, v189\n"
      "v_sub_f32 v109, v109, v189\n"
      "v_sub_f32 v110, v110, v189\n"
      "v_sub_f32 v111, v111, v189\n"
      "v_sub_f32 v112, v112, v190\n"
      "v_sub_f32 v113, v113, v190\n"
      "v_sub_f32 v114, v114, v190\n"
      "v_sub_f32 v115, v115, v190\n"
      "v_sub_f32 v116, v116, v190\n"
      "v_sub_f32 v117, v117, v190\n"
      "v_sub_f32 v118, v118, v190\n"
      "v_sub_f32 v119, v119, v190\n"
      "v_sub_f32 v120, v120, v191\n"
      "v_sub_f32 v121, v121, v191\n"
      "v_sub_f32 v122, v122, v191\n"
      "v_sub_f32 v123, v123, v191\n"
      "v_sub_f32 v124, v124, v191\n"
      "v_sub_f32 v125, v125, v191\n"
      "v_sub_f32 v126, v126, v191\n"
      "v_sub_f32 v127, v127, v191\n"
      "v_mul_f32 v0, v0, v192\n"
      "v_mul_f32 v1, v1, v192\n"
      "v_mul_f32 v2, v2, v192\n"
      "v_mul_f32 v3, v3, v192\n"
      "v_mul_f32 v4, v4, v193\n"
      "v_mul_f32 v5, v5, v193\n"
      "v_mul_f32 v6, v6, v193\n"
      "v_mul_f32 v7, v7, v193\n"
      "v_mul_f32 v8, v8, v194\n"
      "v_mul_f32 v9, v9, v194\n"
      "v_mul_f32 v10, v10, v194\n"
      "v_mul_f32 v11, v11, v194\n"
      "v_mul_f32 v12, v12, v195\n"
      "v_mul_f32 v13, v13, v195\n"
      "v_mul_f32 v14, v14, v195\n"
      "v_mul_f32 v15, v15, v195\n"
      "v_mul_f32 v16, v16, v192\n"
      "v_mul_f32 v17, v17, v192\n"
      "v_mul_f32 v18, v18, v192\n"
      "v_mul_f32 v19, v19, v192\n"
      "v_mul_f32 v20, v20, v193\n"
      "v_mul_f32 v21, v21, v193\n"
      "v_mul_f32 v22, v22, v193\n"
      "v_mul_f32 v23, v23, v193\n"
      "v_mul_f32 v24, v24, v194\n"
      "v_mul_f32 v25, v25, v194\n"
      "v_mul_f32 v26, v26, v194\n"
      "v_mul_f32 v27, v27, v194\n"
      "v_mul_f32 v28, v28, v195\n"
      "v_mul_f32 v29, v29, v195\n"
      "v_mul_f32 v30, v30, v195\n"
      "v_mul_f32 v31, v31, v195\n"
      "v_mul_f32 v32, v32, v192\n"
      "v_mul_f32 v33, v33, v192\n"
      "v_mul_f32 v34, v34, v192\n"
      "v_mul_f32 v35, v35, v192\n"
      "v_mul_f32 v36, v36, v193\n"
      "v_mul_f32 v37, v37, v193\n"
      "v_mul_f32 v38, v38, v193\n"
      "v_mul_f32 v39, v39, v193\n"
      "v_mul_f32 v40, v40, v194\n"
      "v_mul_f32 v41, v41, v194\n"
      "v_mul_f32 v42, v42, v194\n"
      "v_mul_f32 v43, v43, v194\n"
      "v_mul_f32 v44, v44, v195\n"
      "v_mul_f32 v45, v45, v195\n"
      "v_mul_f32 v46, v46, v195\n"
      "v_mul_f32 v47, v47, v195\n"
      "v_mul_f32 v48, v48, v192\n"
      "v_mul_f32 v49, v49, v192\n"
      "v_mul_f32 v50, v50, v192\n"
      "v_mul_f32 v51, v51, v192\n"
      "v_mul_f32 v52, v52, v193\n"
      "v_mul_f32 v53, v53, v193\n"
      "v_mul_f32 v54, v54, v193\n"
      "v_mul_f32 v55, v55, v193\n"
      "v_mul_f32 v56, v56, v194\n"
      "v_mul_f32 v57, v57, v194\n"
      "v_mul_f32 v58, v58, v194\n"
      "v_mul_f32 v59, v59, v194\n"
      "v_mul_f32 v60, v60, v195\n"
      "v_mul_f32 v61, v61, v195\n"
      "v_mul_f32 v62, v62, v195\n"
      "v_mul_f32 v63, v63, v195\n"
      "v_accvgpr_read_b32 v196, a144\n"
      "v_accvgpr_read_b32 v197, a145\n"
      "v_accvgpr_read_b32 v198, a146\n"
      "v_accvgpr_read_b32 v199, a147\n"
      "v_mul_f32 v196, v196, v192\n"
      "v_mul_f32 v197, v197, v192\n"
      "v_mul_f32 v198, v198, v192\n"
      "v_mul_f32 v199, v199, v192\n"
      "v_accvgpr_write_b32 a144, v196\n"
      "v_accvgpr_write_b32 a145, v197\n"
      "v_accvgpr_write_b32 a146, v198\n"
      "v_accvgpr_write_b32 a147, v199\n"
      "v_accvgpr_read_b32 v196, a148\n"
      "v_accvgpr_read_b32 v197, a149\n"
      "v_accvgpr_read_b32 v198, a150\n"
      "v_accvgpr_read_b32 v199, a151\n"
      "v_mul_f32 v196, v196, v193\n"
      "v_mul_f32 v197, v197, v193\n"
      "v_mul_f32 v198, v198, v193\n"
      "v_mul_f32 v199, v199, v193\n"
      "v_accvgpr_write_b32 a148, v196\n"
      "v_accvgpr_write_b32 a149, v197\n"
      "v_accvgpr_write_b32 a150, v198\n"
      "v_accvgpr_write_b32 a151, v199\n"
      "v_accvgpr_read_b32 v196, a152\n"
      "v_accvgpr_read_b32 v197, a153\n"
      "v_accvgpr_read_b32 v198, a154\n"
      "v_accvgpr_read_b32 v199, a155\n"
      "v_mul_f32 v196, v196, v194\n"
      "v_mul_f32 v197, v197, v194\n"
      "v_mul_f32 v198, v198, v194\n"
      "v_mul_f32 v199, v199, v194\n"
      "v_accvgpr_write_b32 a152, v196\n"
      "v_accvgpr_write_b32 a153, v197\n"
      "v_accvgpr_write_b32 a154, v198\n"
      "v_accvgpr_write_b32 a155, v199\n"
      "v_accvgpr_read_b32 v196, a156\n"
      "v_accvgpr_read_b32 v197, a157\n"
      "v_accvgpr_read_b32 v198, a158\n"
      "v_accvgpr_read_b32 v199, a159\n"
      "v_mul_f32 v196, v196, v195\n"
      "v_mul_f32 v197, v197, v195\n"
      "v_mul_f32 v198, v198, v195\n"
      "v_mul_f32 v199, v199, v195\n"
      "v_accvgpr_write_b32 a156, v196\n"
      "v_accvgpr_write_b32 a157, v197\n"
      "v_accvgpr_write_b32 a158, v198\n"
      "v_accvgpr_write_b32 a159, v199\n"
      "v_accvgpr_read_b32 v196, a160\n"
      "v_accvgpr_read_b32 v197, a161\n"
      "v_accvgpr_read_b32 v198, a162\n"
      "v_accvgpr_read_b32 v199, a163\n"
      "v_mul_f32 v196, v196, v192\n"
      "v_mul_f32 v197, v197, v192\n"
      "v_mul_f32 v198, v198, v192\n"
      "v_mul_f32 v199, v199, v192\n"
      "v_accvgpr_write_b32 a160, v196\n"
      "v_accvgpr_write_b32 a161, v197\n"
      "v_accvgpr_write_b32 a162, v198\n"
      "v_accvgpr_write_b32 a163, v199\n"
      "v_accvgpr_read_b32 v196, a164\n"
      "v_accvgpr_read_b32 v197, a165\n"
      "v_accvgpr_read_b32 v198, a166\n"
      "v_accvgpr_read_b32 v199, a167\n"
      "v_mul_f32 v196, v196, v193\n"
      "v_mul_f32 v197, v197, v193\n"
      "v_mul_f32 v198, v198, v193\n"
      "v_mul_f32 v199, v199, v193\n"
      "v_accvgpr_write_b32 a164, v196\n"
      "v_accvgpr_write_b32 a165, v197\n"
      "v_accvgpr_write_b32 a166, v198\n"
      "v_accvgpr_write_b32 a167, v199\n"
      "v_accvgpr_read_b32 v196, a168\n"
      "v_accvgpr_read_b32 v197, a169\n"
      "v_accvgpr_read_b32 v198, a170\n"
      "v_accvgpr_read_b32 v199, a171\n"
      "v_mul_f32 v196, v196, v194\n"
      "v_mul_f32 v197, v197, v194\n"
      "v_mul_f32 v198, v198, v194\n"
      "v_mul_f32 v199, v199, v194\n"
      "v_accvgpr_write_b32 a168, v196\n"
      "v_accvgpr_write_b32 a169, v197\n"
      "v_accvgpr_write_b32 a170, v198\n"
      "v_accvgpr_write_b32 a171, v199\n"
      "v_accvgpr_read_b32 v196, a172\n"
      "v_accvgpr_read_b32 v197, a173\n"
      "v_accvgpr_read_b32 v198, a174\n"
      "v_accvgpr_read_b32 v199, a175\n"
      "v_mul_f32 v196, v196, v195\n"
      "v_mul_f32 v197, v197, v195\n"
      "v_mul_f32 v198, v198, v195\n"
      "v_mul_f32 v199, v199, v195\n"
      "v_accvgpr_write_b32 a172, v196\n"
      "v_accvgpr_write_b32 a173, v197\n"
      "v_accvgpr_write_b32 a174, v198\n"
      "v_accvgpr_write_b32 a175, v199\n"
      "v_accvgpr_read_b32 v196, a176\n"
      "v_accvgpr_read_b32 v197, a177\n"
      "v_accvgpr_read_b32 v198, a178\n"
      "v_accvgpr_read_b32 v199, a179\n"
      "v_mul_f32 v196, v196, v192\n"
      "v_mul_f32 v197, v197, v192\n"
      "v_mul_f32 v198, v198, v192\n"
      "v_mul_f32 v199, v199, v192\n"
      "v_accvgpr_write_b32 a176, v196\n"
      "v_accvgpr_write_b32 a177, v197\n"
      "v_accvgpr_write_b32 a178, v198\n"
      "v_accvgpr_write_b32 a179, v199\n"
      "v_accvgpr_read_b32 v196, a180\n"
      "v_accvgpr_read_b32 v197, a181\n"
      "v_accvgpr_read_b32 v198, a182\n"
      "v_accvgpr_read_b32 v199, a183\n"
      "v_mul_f32 v196, v196, v193\n"
      "v_mul_f32 v197, v197, v193\n"
      "v_mul_f32 v198, v198, v193\n"
      "v_mul_f32 v199, v199, v193\n"
      "v_accvgpr_write_b32 a180, v196\n"
      "v_accvgpr_write_b32 a181, v197\n"
      "v_accvgpr_write_b32 a182, v198\n"
      "v_accvgpr_write_b32 a183, v199\n"
      "v_accvgpr_read_b32 v196, a184\n"
      "v_accvgpr_read_b32 v197, a185\n"
      "v_accvgpr_read_b32 v198, a186\n"
      "v_accvgpr_read_b32 v199, a187\n"
      "v_mul_f32 v196, v196, v194\n"
      "v_mul_f32 v197, v197, v194\n"
      "v_mul_f32 v198, v198, v194\n"
      "v_mul_f32 v199, v199, v194\n"
      "v_accvgpr_write_b32 a184, v196\n"
      "v_accvgpr_write_b32 a185, v197\n"
      "v_accvgpr_write_b32 a186, v198\n"
      "v_accvgpr_write_b32 a187, v199\n"
      "v_accvgpr_read_b32 v196, a188\n"
      "v_accvgpr_read_b32 v197, a189\n"
      "v_accvgpr_read_b32 v198, a190\n"
      "v_accvgpr_read_b32 v199, a191\n"
      "v_mul_f32 v196, v196, v195\n"
      "v_mul_f32 v197, v197, v195\n"
      "v_mul_f32 v198, v198, v195\n"
      "v_mul_f32 v199, v199, v195\n"
      "v_accvgpr_write_b32 a188, v196\n"
      "v_accvgpr_write_b32 a189, v197\n"
      "v_accvgpr_write_b32 a190, v198\n"
      "v_accvgpr_write_b32 a191, v199\n"
      "v_accvgpr_read_b32 v196, a192\n"
      "v_accvgpr_read_b32 v197, a193\n"
      "v_accvgpr_read_b32 v198, a194\n"
      "v_accvgpr_read_b32 v199, a195\n"
      "v_mul_f32 v196, v196, v192\n"
      "v_mul_f32 v197, v197, v192\n"
      "v_mul_f32 v198, v198, v192\n"
      "v_mul_f32 v199, v199, v192\n"
      "v_accvgpr_write_b32 a192, v196\n"
      "v_accvgpr_write_b32 a193, v197\n"
      "v_accvgpr_write_b32 a194, v198\n"
      "v_accvgpr_write_b32 a195, v199\n"
      "v_accvgpr_read_b32 v196, a196\n"
      "v_accvgpr_read_b32 v197, a197\n"
      "v_accvgpr_read_b32 v198, a198\n"
      "v_accvgpr_read_b32 v199, a199\n"
      "v_mul_f32 v196, v196, v193\n"
      "v_mul_f32 v197, v197, v193\n"
      "v_mul_f32 v198, v198, v193\n"
      "v_mul_f32 v199, v199, v193\n"
      "v_accvgpr_write_b32 a196, v196\n"
      "v_accvgpr_write_b32 a197, v197\n"
      "v_accvgpr_write_b32 a198, v198\n"
      "v_accvgpr_write_b32 a199, v199\n"
      "v_accvgpr_read_b32 v196, a200\n"
      "v_accvgpr_read_b32 v197, a201\n"
      "v_accvgpr_read_b32 v198, a202\n"
      "v_accvgpr_read_b32 v199, a203\n"
      "v_mul_f32 v196, v196, v194\n"
      "v_mul_f32 v197, v197, v194\n"
      "v_mul_f32 v198, v198, v194\n"
      "v_mul_f32 v199, v199, v194\n"
      "v_accvgpr_write_b32 a200, v196\n"
      "v_accvgpr_write_b32 a201, v197\n"
      "v_accvgpr_write_b32 a202, v198\n"
      "v_accvgpr_write_b32 a203, v199\n"
      "v_accvgpr_read_b32 v196, a204\n"
      "v_accvgpr_read_b32 v197, a205\n"
      "v_accvgpr_read_b32 v198, a206\n"
      "v_accvgpr_read_b32 v199, a207\n"
      "v_mul_f32 v196, v196, v195\n"
      "v_mul_f32 v197, v197, v195\n"
      "v_mul_f32 v198, v198, v195\n"
      "v_mul_f32 v199, v199, v195\n"
      "v_accvgpr_write_b32 a204, v196\n"
      "v_accvgpr_write_b32 a205, v197\n"
      "v_accvgpr_write_b32 a206, v198\n"
      "v_accvgpr_write_b32 a207, v199\n"
      "v_mul_f32 v180, v180, v192\n"
      "v_mul_f32 v181, v181, v193\n"
      "v_mul_f32 v182, v182, v194\n"
      "v_mul_f32 v183, v183, v195\n"
      "v_lshlrev_b32 v196, 16, v128\n"
      "v_and_b32 v197, 0xffff0000, v128\n"
      "v_mul_f32 v196, v196, v192\n"
      "v_mul_f32 v197, v197, v192\n"
      "v_cvt_pk_bf16_f32 v128, v196, v197\n"
      "v_lshlrev_b32 v196, 16, v129\n"
      "v_and_b32 v197, 0xffff0000, v129\n"
      "v_mul_f32 v196, v196, v192\n"
      "v_mul_f32 v197, v197, v192\n"
      "v_cvt_pk_bf16_f32 v129, v196, v197\n"
      "v_lshlrev_b32 v196, 16, v130\n"
      "v_and_b32 v197, 0xffff0000, v130\n"
      "v_mul_f32 v196, v196, v192\n"
      "v_mul_f32 v197, v197, v192\n"
      "v_cvt_pk_bf16_f32 v130, v196, v197\n"
      "v_lshlrev_b32 v196, 16, v131\n"
      "v_and_b32 v197, 0xffff0000, v131\n"
      "v_mul_f32 v196, v196, v192\n"
      "v_mul_f32 v197, v197, v192\n"
      "v_cvt_pk_bf16_f32 v131, v196, v197\n"
      "v_lshlrev_b32 v196, 16, v132\n"
      "v_and_b32 v197, 0xffff0000, v132\n"
      "v_mul_f32 v196, v196, v193\n"
      "v_mul_f32 v197, v197, v193\n"
      "v_cvt_pk_bf16_f32 v132, v196, v197\n"
      "v_lshlrev_b32 v196, 16, v133\n"
      "v_and_b32 v197, 0xffff0000, v133\n"
      "v_mul_f32 v196, v196, v193\n"
      "v_mul_f32 v197, v197, v193\n"
      "v_cvt_pk_bf16_f32 v133, v196, v197\n"
      "v_lshlrev_b32 v196, 16, v134\n"
      "v_and_b32 v197, 0xffff0000, v134\n"
      "v_mul_f32 v196, v196, v193\n"
      "v_mul_f32 v197, v197, v193\n"
      "v_cvt_pk_bf16_f32 v134, v196, v197\n"
      "v_lshlrev_b32 v196, 16, v135\n"
      "v_and_b32 v197, 0xffff0000, v135\n"
      "v_mul_f32 v196, v196, v193\n"
      "v_mul_f32 v197, v197, v193\n"
      "v_cvt_pk_bf16_f32 v135, v196, v197\n"
      "v_lshlrev_b32 v196, 16, v136\n"
      "v_and_b32 v197, 0xffff0000, v136\n"
      "v_mul_f32 v196, v196, v194\n"
      "v_mul_f32 v197, v197, v194\n"
      "v_cvt_pk_bf16_f32 v136, v196, v197\n"
      "v_lshlrev_b32 v196, 16, v137\n"
      "v_and_b32 v197, 0xffff0000, v137\n"
      "v_mul_f32 v196, v196, v194\n"
      "v_mul_f32 v197, v197, v194\n"
      "v_cvt_pk_bf16_f32 v137, v196, v197\n"
      "v_lshlrev_b32 v196, 16, v138\n"
      "v_and_b32 v197, 0xffff0000, v138\n"
      "v_mul_f32 v196, v196, v194\n"
      "v_mul_f32 v197, v197, v194\n"
      "v_cvt_pk_bf16_f32 v138, v196, v197\n"
      "v_lshlrev_b32 v196, 16, v139\n"
      "v_and_b32 v197, 0xffff0000, v139\n"
      "v_mul_f32 v196, v196, v194\n"
      "v_mul_f32 v197, v197, v194\n"
      "v_cvt_pk_bf16_f32 v139, v196, v197\n"
      "v_lshlrev_b32 v196, 16, v140\n"
      "v_and_b32 v197, 0xffff0000, v140\n"
      "v_mul_f32 v196, v196, v195\n"
      "v_mul_f32 v197, v197, v195\n"
      "v_cvt_pk_bf16_f32 v140, v196, v197\n"
      "v_lshlrev_b32 v196, 16, v141\n"
      "v_and_b32 v197, 0xffff0000, v141\n"
      "v_mul_f32 v196, v196, v195\n"
      "v_mul_f32 v197, v197, v195\n"
      "v_cvt_pk_bf16_f32 v141, v196, v197\n"
      "v_lshlrev_b32 v196, 16, v142\n"
      "v_and_b32 v197, 0xffff0000, v142\n"
      "v_mul_f32 v196, v196, v195\n"
      "v_mul_f32 v197, v197, v195\n"
      "v_cvt_pk_bf16_f32 v142, v196, v197\n"
      "v_lshlrev_b32 v196, 16, v143\n"
      "v_and_b32 v197, 0xffff0000, v143\n"
      "v_mul_f32 v196, v196, v195\n"
      "v_mul_f32 v197, v197, v195\n"
      "v_cvt_pk_bf16_f32 v143, v196, v197\n"
      "v_exp_f32 v188, v96\n"
      "v_exp_f32 v189, v97\n"
      "v_exp_f32 v190, v98\n"
      "v_exp_f32 v191, v99\n"
      "v_cvt_pk_bf16_f32 v144, v188, v189\n"
      "v_cvt_pk_bf16_f32 v145, v190, v191\n"
      "v_exp_f32 v192, v100\n"
      "v_exp_f32 v193, v101\n"
      "v_exp_f32 v194, v102\n"
      "v_exp_f32 v195, v103\n"
      "v_cvt_pk_bf16_f32 v146, v192, v193\n"
      "v_cvt_pk_bf16_f32 v147, v194, v195\n"
      "v_exp_f32 v188, v104\n"
      "v_exp_f32 v189, v105\n"
      "v_exp_f32 v190, v106\n"
      "v_exp_f32 v191, v107\n"
      "v_cvt_pk_bf16_f32 v148, v188, v189\n"
      "v_cvt_pk_bf16_f32 v149, v190, v191\n"
      "v_exp_f32 v192, v108\n"
      "v_exp_f32 v193, v109\n"
      "v_exp_f32 v194, v110\n"
      "v_exp_f32 v195, v111\n"
      "v_cvt_pk_bf16_f32 v150, v192, v193\n"
      "v_cvt_pk_bf16_f32 v151, v194, v195\n"
      "v_exp_f32 v188, v112\n"
      "v_exp_f32 v189, v113\n"
      "v_exp_f32 v190, v114\n"
      "v_exp_f32 v191, v115\n"
      "v_cvt_pk_bf16_f32 v152, v188, v189\n"
      "v_cvt_pk_bf16_f32 v153, v190, v191\n"
      "v_exp_f32 v192, v116\n"
      "v_exp_f32 v193, v117\n"
      "v_exp_f32 v194, v118\n"
      "v_exp_f32 v195, v119\n"
      "v_cvt_pk_bf16_f32 v154, v192, v193\n"
      "v_cvt_pk_bf16_f32 v155, v194, v195\n"
      "v_exp_f32 v188, v120\n"
      "v_exp_f32 v189, v121\n"
      "v_exp_f32 v190, v122\n"
      "v_exp_f32 v191, v123\n"
      "v_cvt_pk_bf16_f32 v156, v188, v189\n"
      "v_cvt_pk_bf16_f32 v157, v190, v191\n"
      "v_exp_f32 v192, v124\n"
      "v_exp_f32 v193, v125\n"
      "v_exp_f32 v194, v126\n"
      "v_exp_f32 v195, v127\n"
      "v_cvt_pk_bf16_f32 v158, v192, v193\n"
      "v_cvt_pk_bf16_f32 v159, v194, v195\n"
      "s_nop 1\n"
      "v_mfma_f32_16x16x32_bf16 v[184:187], a[128:131], v[144:147], 0\n"
      "v_mfma_f32_16x16x32_bf16 v[184:187], a[132:135], v[148:151], v[184:187]\n"
      "v_mfma_f32_16x16x32_bf16 v[184:187], a[136:139], v[152:155], v[184:187]\n"
      "v_mfma_f32_16x16x32_bf16 v[184:187], a[140:143], v[156:159], v[184:187]\n"
      "s_nop 7\n"
      "s_setpc_b64 s[96:97]\n"
      "SLOW_%=:\n"
      "ds_write_b128 v234, v[176:179] offset:5120\n"
      "s_waitcnt lgkmcnt(0)\n"
      "s_barrier\n"
      "ds_read_b128 v[192:195], v235 offset:5120\n"
      "s_waitcnt lgkmcnt(0)\n"
      "v_sub_f32 v188, v176, v192\n"
      "v_sub_f32 v189, v177, v193\n"
      "v_sub_f32 v190, v178, v194\n"
      "v_sub_f32 v191, v179, v195\n"
      "v_max_f32 v188, 0, v188\n"
      "v_max_f32 v189, 0, v189\n"
      "v_max_f32 v190, 0, v190\n"
      "v_max_f32 v191, 0, v191\n"
      "s_call_b64 s[96:97], RS1_%=\n"
      "ds_write_b128 v232, a[176:179] offset:0\n"
      "ds_write_b128 v232, a[180:183] offset:1024\n"
      "ds_write_b128 v232, a[184:187] offset:2048\n"
      "ds_write_b128 v232, a[188:191] offset:3072\n"
      "ds_write_b128 v232, a[192:195] offset:4096\n"
      "ds_write_b128 v232, a[196:199] offset:5120\n"
      "ds_write_b128 v232, a[200:203] offset:6144\n"
      "ds_write_b128 v232, a[204:207] offset:7168\n"
      "ds_write_b128 v234, v[144:147] offset:0\n"
      "ds_write_b128 v234, v[148:151] offset:1024\n"
      "ds_write_b128 v234, v[152:155] offset:2048\n"
      "ds_write_b128 v234, v[156:159] offset:3072\n"
      "v_cndmask_b32 v200, v184, v185, s[74:75]\n"
      "v_cndmask_b32 v196, v186, v187, s[74:75]\n"
      "v_cndmask_b32 v200, v200, v196, s[94:95]\n"
      "v_cndmask_b32 v229, v180, v181, s[74:75]\n"
      "v_cndmask_b32 v196, v182, v183, s[74:75]\n"
      "v_cndmask_b32 v229, v229, v196, s[94:95]\n"
      "v_add_f32 v229, v229, v200\n"
      "ds_write_b32 v234, v229 offset:4096\n"
      "v_cndmask_b32 v228, v176, v177, s[74:75]\n"
      "v_cndmask_b32 v196, v178, v179, s[74:75]\n"
      "v_cndmask_b32 v228, v228, v196, s[94:95]\n"
      "s_waitcnt lgkmcnt(0)\n"
      "s_barrier\n"
      "ds_read_b32 v208, v209\n"
      "ds_read_b128 v[160:163], v235 offset:0\n"
      "ds_read_b128 v[164:167], v235 offset:1024\n"
      "ds_read_b128 v[168:171], v235 offset:2048\n"
      "ds_read_b128 v[172:175], v235 offset:3072\n"
      "ds_read_b128 v[64:67], v233 offset:0\n"
      "ds_read_b128 v[68:71], v233 offset:1024\n"
      "ds_read_b128 v[72:75], v233 offset:2048\n"
      "ds_read_b128 v[76:79], v233 offset:3072\n"
      "ds_read_b128 v[80:83], v233 offset:4096\n"
      "ds_read_b128 v[84:87], v233 offset:5120\n"
      "ds_read_b128 v[88:91], v233 offset:6144\n"
      "ds_read_b128 v[92:95], v233 offset:7168\n"
      "ds_read_b32 v204, v235 offset:4096\n"
      "s_branch AGAIN_%=\n"
      ".subsection 0\n"
      : "+{v[32:63]}"(z1), "+{a[144:175]}"(z2), "+{a[176:207]}"(z3), "+{v[128:143]}"(z4), "+{a[128:143]}"(z5), "+{v[180:187]}"(z6), "+{s92}"(tch), "+{v[200:207]}"(mk), "+{v208}"(epoch), "+{v[242:249]}"(ad), "+{v[250:251]}"(ra), "+{v241}"(l16), "+{s[56:59]}"(srd),
        [e0] "=&s"(e0), [par] "+s"(par), [mA] "+s"(mA), [mB] "+s"(mB), "+{s[52:53]}"(mH)
      : [dlt] "s"(kb ^ vb), [mN] "s"(mN), [mR] "s"(mR), [srdt] "s"(srdT), [tv] "s"(lds0 + 3 * SLOT + split * 8192 + wid * 2048), [srdr] "s"(srdR), [srdx] "s"(srdX), [line] "s"(line),
        [outp] "s"((char*)out + ((size_t)bk * 128 + rh * 64) * (D_ * 2) + (2 * split + kh) * 64),
        [wb] "s"(wbase), [spl] "s"(split), [other] "s"(wid ^ (3 * split)),
        [blk] "s"(blockIdx.x), [cph] "s"(C2 + (unsigned)((jj & ~3) * 8 + xg * 4) * WIN / 256u)
      : "v0", "v1", "v2", "v3", "v4", "v5", "v6", "v7", "v8", "v9", "v10", "v11", "v12", "v13", "v14", "v15", "v16", "v17", "v18", "v19", "v20", "v21", "v22", "v23", "v24", "v25", "v26", "v27", "v28", "v29", "v30", "v31", "v64", "v65", "v66", "v67", "v68", "v69", "v70", "v71", "v72", "v73", "v74", "v75", "v76", "v77", "v78", "v79", "v80", "v81", "v82", "v83", "v84", "v85", "v86", "v87", "v88", "v89", "v90", "v91", "v92", "v93", "v94", "v95", "v96", "v97", "v98", "v99", "v100", "v101", "v102", "v103", "v104", "v105", "v106", "v107", "v108", "v109", "v110", "v111", "v112", "v113", "v114", "v115", "v116", "v117", "v118", "v119", "v120", "v121", "v122", "v123", "v124", "v125", "v126", "v127", "v144", "v145", "v146", "v147", "v148", "v149", "v150", "v151", "v152", "v153", "v154", "v155", "v156", "v157", "v158", "v159", "v160", "v161", "v162", "v163", "v164", "v165", "v166", "v167", "v168", "v169", "v170", "v171", "v172", "v173", "v174", "v175", "v176", "v177", "v178", "v179", "v188", "v189", "v190", "v191", "v192", "v193", "v194", "v195", "v196", "v197", "v198", "v199", "v209", "v210", "v211", "v212", "v213", "v214", "v215", "v216", "v217", "v218", "v219", "v220", "v221", "v222", "v223", "v224", "v225", "v226", "v227", "v228", "v229", "v230", "v231", "v232", "v233", "v234", "v235", "v236", "v237", "v238", "v239", "v240", "a0", "a1", "a2", "a3", "a4", "a5", "a6", "a7", "a8", "a9", "a10", "a11", "a12", "a13", "a14", "a15", "a16", "a17", "a18", "a19", "a20", "a21", "a22", "a23", "a24", "a25", "a26", "a27", "a28", "a29", "a30", "a31", "a32", "a33", "a34", "a35", "a36", "a37", "a38", "a39", "a40", "a41", "a42", "a43", "a44", "a45", "a46", "a47", "a48", "a49", "a50", "a51", "a52", "a53", "a54", "a55", "a56", "a57", "a58", "a59", "a60", "a61", "a62", "a63", "a64", "a65", "a66", "a67", "a68", "a69", "a70", "a71", "a72", "a73", "a74", "a75", "a76", "a77", "a78", "a79", "a80", "a81", "a82", "a83", "a84", "a85", "a86", "a87", "a88", "a89", "a90", "a91", "a92", "a93", "a94", "a95", "a96", "a97", "a98", "a99", "a100", "a101", "a102", "a103", "a104", "a105", "a106", "a107", "a108", "a109", "a110", "a111", "a112", "a113", "a114", "a115", "a116", "a117", "a118", "a119", "a120", "a121", "a122", "a123", "a124", "a125", "a126", "a127", "s54", "s60", "s61", "s62", "s63", "s64", "s65", "s66", "s67", "s68", "s69", "s70", "s71", "s72", "s73", "s74", "s75", "s76", "s77", "s78", "s79", "s80", "s81", "s82", "s83", "s84", "s85", "s86", "s87", "s88", "s89", "s90", "s91", "s93", "s94", "s95", "s96", "s97", "vcc", "scc", "memory");
}

}

extern "C" void kernel_launch(void* const* d_in, const int* in_sizes, int n_in, void* d_out, int out_size,
                              void* d_ws, size_t ws_size, hipStream_t stream) {
  if (n_in != 6 || ws_size < WS_END) return;
  const __hip_bfloat16* q = (const __hip_bfloat16*)d_in[0];
  const __hip_bfloat16* kc = (const __hip_bfloat16*)d_in[1];
  const __hip_bfloat16* vc = (const __hip_bfloat16*)d_in[2];
  const __hip_bfloat16* kt = (const __hip_bfloat16*)d_in[3];
  const __hip_bfloat16* vt = (const __hip_bfloat16*)d_in[4];
  const __hip_bfloat16* tm = (const __hip_bfloat16*)d_in[5];
  hipLaunchKernelGGL(tree_attn_main, dim3(B_ * HKV_ * 2), dim3(256), 0, stream, q, kc, vc, kt, vt, tm,
                     (__hip_bfloat16*)d_out, (char*)d_ws);
}
```

```cpp
#include <hip/hip_runtime.h>
#include <hip/hip_bf16.h>
#include <stdint.h>

#pragma clang diagnostic ignored "-Wint-to-pointer-cast"
#pragma clang diagnostic ignored "-Winline-asm"

namespace {

constexpr int B_ = 16, HQ_ = 32, HKV_ = 8, T_ = 32, C_ = 4096, D_ = 128;
constexpr int SLOT = 64 * D_ * 2;
constexpr int LDS_BYTES = 10 * SLOT;

constexpr size_t WS_EPOCH = 0;
constexpr size_t WS_REF = WS_EPOCH + 128 * 4 * 128;
constexpr size_t WS_X = WS_REF + 16384;
constexpr size_t XW_BYTES = 4864;
constexpr size_t WS_END = WS_X + (size_t)128 * 2 * 4 * XW_BYTES;

__global__ __launch_bounds__(256, 1)
void tree_attn_main(const __hip_bfloat16*, const __hip_bfloat16*, const __hip_bfloat16*, const __hip_bfloat16*,
                    const __hip_bfloat16*, const __hip_bfloat16*, __hip_bfloat16*, char*) {
  __shared__ __attribute__((aligned(1024))) char smem[LDS_BYTES];
  const unsigned lds0 = (unsigned)(uintptr_t)smem;
  unsigned tid = threadIdx.x;
  asm volatile(
      "s_load_dwordx16 s[4:19], %[kp], 0x0\n"
      "s_getpc_b64 s[60:61]\n"
      "v_lshlrev_b32 v241, 4, v0\n"
      "s_lshl_b32 s62, %[blk], 9\n"
      "s_and_b32 s62, s62, 0x3000\n"
      "s_add_u32 s60, s60, s62\n"
      "s_addc_u32 s61, s61, 0\n"
      "global_load_dwordx4 v[236:239], v241, s[60:61]\n"
      "v_and_b32 v1, 0xc0, v0\n"
      "v_lshlrev_b32 v248, 1, v0\n"
      "v_readfirstlane_b32 s50, v1\n"
      "v_and_b32 v248, 0x60, v248\n"
      "v_and_b32 v249, 0x70, v241\n"
      "v_xor_b32 v248, v248, v249\n"
      "v_lshl_or_b32 v248, v1, 6, v248\n"
      "v_and_b32 v1, 56, v0\n"
      "v_lshl_or_b32 v248, v1, 5, v248\n"
      "s_lshl_b32 s62, %[blk], 4\n"
      "s_and_b32 s63, s62, 0x80\n"
      "v_or_b32 v248, s63, v248\n"
      "v_xor_b32 v249, 0x80, v248\n"
      "s_and_b32 s62, s62, 0x70\n"
      "s_lshr_b32 s64, %[blk], 4\n"
      "s_add_u32 s44, s62, s64\n"
      "s_lshl_b32 s51, s64, 14\n"
      "s_and_b32 s51, s51, 0x4000\n"
      "s_lshl_b32 s64, s44, 20\n"
      "s_lshl_b32 s65, s63, 8\n"
      "s_add_u32 s64, s64, s65\n"
      "s_lshl_b32 s62, s44, 15\n"
      "s_lshl_b32 s50, s50, 6\n"
      "s_add_u32 m0, s50, 131072\n"
      "s_mov_b32 s58, 0x120000\n"
      "s_mov_b32 s59, 0x20000\n"
      "s_movk_i32 s22, 0x4000\n"
      "s_waitcnt lgkmcnt(0)\n"
      "s_add_u32 s56, s4, s62\n"
      "s_addc_u32 s57, s5, 0\n"
      "buffer_load_dwordx4 v248, s[56:59], 0 offen nt lds\n"
      "buffer_load_dwordx4 v248, s[56:59], 0 offen offset:2048 nt lds\n"
      "s_bitset1_b32 m0, 10\n"
      "s_memrealtime s[82:83]\n"
      "s_add_u32 s20, s6, s64\n"
      "s_addc_u32 s21, s7, 0\n"
      "s_add_u32 s32, s8, s64\n"
      "s_addc_u32 s33, s9, 0\n"
      "s_load_dword s92, s[20:21], 0x0\n"
      "s_load_dword s92, s[10:11], 0x0\n"
      "s_load_dword s92, s[32:33], 0x0\n"
      "buffer_load_dwordx4 v249, s[56:59], 0 offen nt lds\n"
      "buffer_load_dwordx4 v249, s[56:59], 0 offen offset:2048 nt lds\n"
      "s_add_u32 m0, s50, 147456\n"
      "s_load_dword s92, s[18:19], 0x0\n"
      "buffer_load_dwordx4 v248, s[56:59], s22 offen nt lds\n"
      "buffer_load_dwordx4 v248, s[56:59], s22 offen offset:2048 nt lds\n"
      "s_bitset1_b32 m0, 10\n"
      "s_nop 0\n"
      "buffer_load_dwordx4 v249, s[56:59], s22 offen nt lds\n"
      "buffer_load_dwordx4 v249, s[56:59], s22 offen offset:2048 nt lds\n"
      "s_mov_b64 s[56:57], s[20:21]\n"
      "s_load_dword s92, s[14:15], 0x0\n"
      "s_load_dword s92, s[12:13], 0x0\n"
      "s_lshr_b32 s74, s50, 2\n"
      "s_lshr_b32 s75, s50, 5\n"
      "s_xor_b32 s75, s75, s63\n"
      "s_and_b32 s75, s75, 0x80\n"
      "s_and_b32 s76, s74, 0x800\n"
      "s_add_u32 s75, s75, s76\n"
      "s_add_u32 s70, s65, s63\n"
      "s_add_u32 s70, s70, s50\n"
      "s_sub_u32 s70, s70, s75\n"
      "s_lshr_b32 s75, s50, 1\n"
      "s_lshr_b32 s71, s62, 2\n"
      "s_sub_u32 s71, s71, s75\n"
      "s_ashr_i32 s72, s71, 31\n"
      "s_mov_b32 s23, s59\n"
      "s_mov_b64 s[26:27], s[22:23]\n"
      "s_sub_u32 s20, s56, s70\n"
      "s_subb_u32 s21, s57, 0\n"
      "s_add_u32 m0, s74, 24576\n"
      "s_add_u32 s24, s10, s71\n"
      "buffer_load_dwordx4 v248, s[20:23], 0 offen nt lds\n"
      "s_addc_u32 s25, s11, s72\n"
      "s_add_u32 m0, s75, 16384\n"
      "s_nop 0\n"
      "buffer_load_dwordx4 v248, s[24:27], 0 offen nt lds\n"
      "s_bitset1_b32 m0, 10\n"
      "s_nop 0\n"
      "buffer_load_dwordx4 v249, s[24:27], 0 offen nt lds\n"
      "s_lshr_b32 s43, s63, 7\n"
      "s_lshl_b32 s76, s44, 9\n"
      "s_lshr_b32 s69, s50, 5\n"
      "s_add_u32 s76, s76, s69\n"
      "s_add_u32 s46, s18, s76\n"
      "s_addc_u32 s47, s19, 0\n"
      "s_mul_i32 s76, s43, 0x3000\n"
      "s_lshl_b32 s69, s44, 8\n"
      "s_and_b32 s69, s69, 0x7800\n"
      "s_add_u32 s14, s14, s69\n"
      "s_addc_u32 s15, s15, 0\n"
      "v_mov_b64 v[192:193], 0\n"
      "v_mov_b64 v[194:195], 0\n"
      "v_lshlrev_b32 v3, 6, v0\n"
      "v_and_b32 v3, 0x3c0, v3\n"
      "v_lshrrev_b32 v4, 1, v0\n"
      "v_and_or_b32 v211, v4, 24, v3\n"
      "v_mov_b64 v[200:201], 0\n"
      "v_mov_b64 v[202:203], 0\n"
      "v_mov_b64 v[204:205], 0\n"
      "v_mov_b64 v[206:207], 0\n"
      "s_cmp_lg_u32 s76, s50\n"
      "s_cbranch_scc1 NOMASK_%=\n"
      "global_load_dwordx2 v[200:201], v211, s[14:15] offset:0\n"
      "global_load_dwordx2 v[202:203], v211, s[14:15] offset:32\n"
      "global_load_dwordx2 v[204:205], v211, s[14:15] offset:1024\n"
      "global_load_dwordx2 v[206:207], v211, s[14:15] offset:1056\n"
      "NOMASK_%=:\n"
      "global_load_dword v208, v192, s[46:47] sc1\n"
      "s_mov_b64 s[30:31], s[22:23]\n"
      "s_add_u32 s28, s12, s71\n"
      "s_addc_u32 s29, s13, s72\n"
      "s_sub_u32 s32, s32, 73728\n"
      "s_subb_u32 s33, s33, 0\n"
      "s_xor_b64 s[32:33], s[32:33], s[56:57]\n"
      "s_lshl_b32 s69, s43, 13\n"
      "s_xor_b32 s69, s69, 0x2000\n"
      "v_add_u32 v2, s69, v241\n"
      "ds_write_b128 v241, v[192:195] offset:28672\n"
      "ds_write_b128 v2, v[192:195] offset:49152\n"
      "ds_write_b128 v2, v[192:195] offset:53248\n"
      "v_and_b32 v241, 0x3f0, v241\n"
      "s_and_b32 s91, s50, 8192\n"
      "v_lshlrev_b32 v4, 8, v0\n"
      "v_and_b32 v4, 0x800, v4\n"
      "v_lshlrev_b32 v5, 7, v0\n"
      "v_and_b32 v6, 0x380, v5\n"
      "v_or3_b32 v4, v4, v6, s91\n"
      "v_and_b32 v6, 0x60, v241\n"
      "v_and_b32 v2, 48, v0\n"
      "v_xor_b32 v6, v6, v2\n"
      "v_or_b32 v242, v4, v6\n"
      "v_xor_b32 v243, 64, v242\n"
      "v_lshlrev_b32 v4, 6, v0\n"
      "v_and_b32 v6, 0x800, v4\n"
      "v_lshlrev_b32 v2, 5, v0\n"
      "v_and_b32 v2, 0x380, v2\n"
      "s_lshr_b32 s69, s91, 7\n"
      "s_add_u32 s69, s69, s91\n"
      "s_add_u32 s69, s69, 16384\n"
      "v_or3_b32 v6, v6, v2, s69\n"
      "v_lshlrev_b32 v2, 3, v0\n"
      "v_and_or_b32 v6, v2, 24, v6\n"
      "v_lshlrev_b32 v2, 2, v0\n"
      "v_and_b32 v2, 0x60, v2\n"
      "v_xor_b32 v244, v6, v2\n"
      "v_xor_b32 v245, 0x20, v244\n"
      "v_xor_b32 v246, 0x40, v244\n"
      "v_xor_b32 v247, 0x60, v244\n"
      "v_and_b32 v5, 0x1e00, v5\n"
      "v_and_or_b32 v250, v241, 48, v5\n"
      "s_lshl_b32 s69, %[blk], 9\n"
      "s_and_b32 s69, s69, 0x1e00\n"
      "v_and_b32 v4, 0x1c0, v4\n"
      "v_or_b32 v251, s69, v4\n"
      "s_and_b32 s69, s50, 4096\n"
      "s_lshl_b32 s69, s69, 2\n"
      "s_sub_u32 s69, s69, s91\n"
      "s_add_u32 s69, s69, 131072\n"
      "s_sub_u32 s91, 16384, s91\n"
      "v_add_u32 v196, s69, v242\n"
      "v_add_u32 v198, s91, v242\n"
      "v_add_u32 v197, s69, v243\n"
      "v_add_u32 v199, s91, v243\n"
      "s_add_u32 s69, s50, 131072\n"
      "v_add_u32 v210, s69, v241\n"
      "v_xor_b32 v212, 0x4000, v210\n"
      "v_and_b32 v2, 3, v0\n"
      "v_mov_b32 v4, 0x3f803f80\n"
      "v_cmp_eq_u32 vcc, 0, v2\n"
      "s_nop 1\n"
      "v_cndmask_b32 v5, 0, v4, vcc\n"
      "v_accvgpr_write_b32 a128, v5\n"
      "v_accvgpr_write_b32 a129, v5\n"
      "v_accvgpr_write_b32 a130, v5\n"
      "v_accvgpr_write_b32 a131, v5\n"
      "v_cmp_eq_u32 vcc, 1, v2\n"
      "s_nop 1\n"
      "v_cndmask_b32 v5, 0, v4, vcc\n"
      "v_accvgpr_write_b32 a132, v5\n"
      "v_accvgpr_write_b32 a133, v5\n"
      "v_accvgpr_write_b32 a134, v5\n"
      "v_accvgpr_write_b32 a135, v5\n"
      "v_cmp_eq_u32 vcc, 2, v2\n"
      "s_nop 1\n"
      "v_cndmask_b32 v5, 0, v4, vcc\n"
      "v_accvgpr_write_b32 a136, v5\n"
      "v_accvgpr_write_b32 a137, v5\n"
      "v_accvgpr_write_b32 a138, v5\n"
      "v_accvgpr_write_b32 a139, v5\n"
      "v_cmp_eq_u32 vcc, 3, v2\n"
      "s_nop 1\n"
      "v_cndmask_b32 v5, 0, v4, vcc\n"
      "v_accvgpr_write_b32 a140, v5\n"
      "v_accvgpr_write_b32 a141, v5\n"
      "v_accvgpr_write_b32 a142, v5\n"
      "v_accvgpr_write_b32 a143, v5\n"
      "v_mfma_f32_32x32x16_bf16 a[144:159], v[192:195], v[192:195], 0\n"
      "v_mfma_f32_32x32x16_bf16 a[160:175], v[192:195], v[192:195], 0\n"
      "v_mfma_f32_32x32x16_bf16 a[176:191], v[192:195], v[192:195], 0\n"
      "v_mfma_f32_32x32x16_bf16 a[192:207], v[192:195], v[192:195], 0\n"
      "v_mfma_f32_32x32x16_bf16 v[0:15], v[192:195], v[192:195], 0\n"
      "v_mfma_f32_32x32x16_bf16 v[16:31], v[192:195], v[192:195], 0\n"
      "v_mfma_f32_32x32x16_bf16 v[32:47], v[192:195], v[192:195], 0\n"
      "v_mfma_f32_32x32x16_bf16 v[48:63], v[192:195], v[192:195], 0\n"
      "v_mfma_f32_32x32x16_bf16 v[128:143], v[192:195], v[192:195], 0\n"
      "v_mov_b64 v[180:181], 0\n"
      "v_mov_b64 v[182:183], 0\n"
      "v_mov_b64 v[184:185], 0\n"
      "v_mov_b64 v[186:187], 0\n"
      "s_mov_b32 s61, 0x4000\n"
      "s_xor_b32 s60, s61, s51\n"
      "s_add_u32 s62, s50, 32768\n"
      "s_mov_b32 s94, 0x3e0293ee\n"
      "s_mov_b32 s95, s94\n"
      "s_lshl_b32 s69, s43, 13\n"
      "s_add_u32 s45, s69, s75\n"
      "s_mov_b32 m0, s50\n"
      "s_nop 0\n"
      "buffer_load_dwordx4 v248, s[56:59], s51 offen nt lds\n"
      "buffer_load_dwordx4 v248, s[56:59], s51 offen offset:2048 nt lds\n"
      "s_bitset1_b32 m0, 10\n"
      "s_nop 0\n"
      "buffer_load_dwordx4 v249, s[56:59], s51 offen nt lds\n"
      "buffer_load_dwordx4 v249, s[56:59], s51 offen offset:2048 nt lds\n"
      "s_waitcnt vmcnt(8)\n"
      "s_memrealtime s[68:69]\n"
      "ds_read_b128 v[64:67], v210 offset:0\n"
      "ds_read_b128 v[68:71], v210 offset:1024\n"
      "ds_read_b128 v[72:75], v210 offset:2048\n"
      "ds_read_b128 v[76:79], v210 offset:3072\n"
      "ds_read_b128 v[80:83], v212 offset:0\n"
      "ds_read_b128 v[84:87], v212 offset:1024\n"
      "ds_read_b128 v[88:91], v212 offset:2048\n"
      "ds_read_b128 v[92:95], v212 offset:3072\n"
      "s_add_u32 s20, s18, 65536\n"
      "s_addc_u32 s21, s19, 0\n"
      "s_lshl_b32 s75, %[blk], 6\n"
      "s_add_u32 s75, s75, s50\n"
      "s_waitcnt lgkmcnt(0)\n"
      "s_sub_u32 s67, s68, s82\n"
      "s_min_u32 s67, s67, 165\n"
      "s_add_u32 s93, s82, s67\n"
      "s_sub_u32 s71, s68, 400\n"
      "s_cmp_lt_u32 s75, 512\n"
      "s_cbranch_scc0 NOLEAD_%=\n"
      "v_mov_b32 v188, s68\n"
      "v_mov_b32 v189, s68\n"
      "v_mov_b32 v190, s68\n"
      "v_mov_b32 v191, s68\n"
      "buffer_store_dwordx4 v[188:191], v250, s[20:23], s75 offen sc1\n"
      "s_nop 1\n"
      "NOLEAD_%=:\n"
      "s_mov_b32 m0, s62\n"
      "s_nop 0\n"
      "buffer_load_dwordx4 v248, s[56:59], s60 offen nt lds\n"
      "buffer_load_dwordx4 v248, s[56:59], s60 offen offset:2048 nt lds\n"
      "s_bitset1_b32 m0, 10\n"
      "s_nop 0\n"
      "buffer_load_dwordx4 v249, s[56:59], s60 offen nt lds\n"
      "buffer_load_dwordx4 v249, s[56:59], s60 offen offset:2048 nt lds\n"
      "s_xor_b64 s[56:57], s[56:57], s[32:33]\n"
      "s_bitset1_b32 s61, 15\n"
      "s_add_u32 s61, s61, 0x2000\n"
      "s_bitset0_b32 s61, 15\n"
      "s_xor_b32 s60, s61, s51\n"
      "s_add_u32 s62, s62, 0x4000\n"
      "s_and_b32 s62, s62, 0x1ffff\n"
      "s_xor_b64 s[56:57], s[56:57], s[32:33]\n"
      "s_bitset1_b32 s61, 15\n"
      "s_add_u32 s61, s61, 0x2000\n"
      "s_bitset0_b32 s61, 15\n"
      "s_xor_b32 s60, s61, s51\n"
      "s_add_u32 s62, s62, 0x4000\n"
      "s_and_b32 s62, s62, 0x1ffff\n"
      "s_add_u32 m0, s45, 49152\n"
      "s_nop 0\n"
      "buffer_load_dwordx4 v248, s[28:31], 0 offen nt lds\n"
      "s_bitset1_b32 m0, 10\n"
      "s_nop 0\n"
      "buffer_load_dwordx4 v249, s[28:31], 0 offen nt lds\n"
      "v_lshlrev_b32 v188, 16, v64\n"
      "v_and_b32 v189, 0xffff0000, v64\n"
      "v_lshlrev_b32 v190, 16, v65\n"
      "v_and_b32 v191, 0xffff0000, v65\n"
      "v_lshlrev_b32 v192, 16, v66\n"
      "v_and_b32 v193, 0xffff0000, v66\n"
      "v_lshlrev_b32 v194, 16, v67\n"
      "v_and_b32 v195, 0xffff0000, v67\n"
      "v_pk_mul_f32 v[188:189], v[188:189], s[94:95]\n"
      "v_pk_mul_f32 v[190:191], v[190:191], s[94:95]\n"
      "v_pk_mul_f32 v[192:193], v[192:193], s[94:95]\n"
      "v_pk_mul_f32 v[194:195], v[194:195], s[94:95]\n"
      "v_cvt_pk_bf16_f32 v64, v188, v189\n"
      "v_cvt_pk_bf16_f32 v65, v190, v191\n"
      "v_cvt_pk_bf16_f32 v66, v192, v193\n"
      "v_cvt_pk_bf16_f32 v67, v194, v195\n"
      "ds_write_b128 v210, v[64:67] offset:0\n"
      "v_lshlrev_b32 v188, 16, v68\n"
      "v_and_b32 v189, 0xffff0000, v68\n"
      "v_lshlrev_b32 v190, 16, v69\n"
      "v_and_b32 v191, 0xffff0000, v69\n"
      "v_lshlrev_b32 v192, 16, v70\n"
      "v_and_b32 v193, 0xffff0000, v70\n"
      "v_lshlrev_b32 v194, 16, v71\n"
      "v_and_b32 v195, 0xffff0000, v71\n"
      "v_pk_mul_f32 v[188:189], v[188:189], s[94:95]\n"
      "v_pk_mul_f32 v[190:191], v[190:191], s[94:95]\n"
      "v_pk_mul_f32 v[192:193], v[192:193], s[94:95]\n"
      "v_pk_mul_f32 v[194:195], v[194:195], s[94:95]\n"
      "v_cvt_pk_bf16_f32 v68, v188, v189\n"
      "v_cvt_pk_bf16_f32 v69, v190, v191\n"
      "v_cvt_pk_bf16_f32 v70, v192, v193\n"
      "v_cvt_pk_bf16_f32 v71, v194, v195\n"
      "ds_write_b128 v210, v[68:71] offset:1024\n"
      "v_lshlrev_b32 v188, 16, v72\n"
      "v_and_b32 v189, 0xffff0000, v72\n"
      "v_lshlrev_b32 v190, 16, v73\n"
      "v_and_b32 v191, 0xffff0000, v73\n"
      "v_lshlrev_b32 v192, 16, v74\n"
      "v_and_b32 v193, 0xffff0000, v74\n"
      "v_lshlrev_b32 v194, 16, v75\n"
      "v_and_b32 v195, 0xffff0000, v75\n"
      "v_pk_mul_f32 v[188:189], v[188:189], s[94:95]\n"
      "v_pk_mul_f32 v[190:191], v[190:191], s[94:95]\n"
      "v_pk_mul_f32 v[192:193], v[192:193], s[94:95]\n"
      "v_pk_mul_f32 v[194:195], v[194:195], s[94:95]\n"
      "v_cvt_pk_bf16_f32 v72, v188, v189\n"
      "v_cvt_pk_bf16_f32 v73, v190, v191\n"
      "v_cvt_pk_bf16_f32 v74, v192, v193\n"
      "v_cvt_pk_bf16_f32 v75, v194, v195\n"
      "ds_write_b128 v210, v[72:75] offset:2048\n"
      "v_lshlrev_b32 v188, 16, v76\n"
      "v_and_b32 v189, 0xffff0000, v76\n"
      "v_lshlrev_b32 v190, 16, v77\n"
      "v_and_b32 v191, 0xffff0000, v77\n"
      "v_lshlrev_b32 v192, 16, v78\n"
      "v_and_b32 v193, 0xffff0000, v78\n"
      "v_lshlrev_b32 v194, 16, v79\n"
      "v_and_b32 v195, 0xffff0000, v79\n"
      "v_pk_mul_f32 v[188:189], v[188:189], s[94:95]\n"
      "v_pk_mul_f32 v[190:191], v[190:191], s[94:95]\n"
      "v_pk_mul_f32 v[192:193], v[192:193], s[94:95]\n"
      "v_pk_mul_f32 v[194:195], v[194:195], s[94:95]\n"
      "v_cvt_pk_bf16_f32 v76, v188, v189\n"
      "v_cvt_pk_bf16_f32 v77, v190, v191\n"
      "v_cvt_pk_bf16_f32 v78, v192, v193\n"
      "v_cvt_pk_bf16_f32 v79, v194, v195\n"
      "ds_write_b128 v210, v[76:79] offset:3072\n"
      "v_lshlrev_b32 v188, 16, v80\n"
      "v_and_b32 v189, 0xffff0000, v80\n"
      "v_lshlrev_b32 v190, 16, v81\n"
      "v_and_b32 v191, 0xffff0000, v81\n"
      "v_lshlrev_b32 v192, 16, v82\n"
      "v_and_b32 v193, 0xffff0000, v82\n"
      "v_lshlrev_b32 v194, 16, v83\n"
      "v_and_b32 v195, 0xffff0000, v83\n"
      "v_pk_mul_f32 v[188:189], v[188:189], s[94:95]\n"
      "v_pk_mul_f32 v[190:191], v[190:191], s[94:95]\n"
      "v_pk_mul_f32 v[192:193], v[192:193], s[94:95]\n"
      "v_pk_mul_f32 v[194:195], v[194:195], s[94:95]\n"
      "v_cvt_pk_bf16_f32 v80, v188, v189\n"
      "v_cvt_pk_bf16_f32 v81, v190, v191\n"
      "v_cvt_pk_bf16_f32 v82, v192, v193\n"
      "v_cvt_pk_bf16_f32 v83, v194, v195\n"
      "ds_write_b128 v212, v[80:83] offset:0\n"
      "v_lshlrev_b32 v188, 16, v84\n"
      "v_and_b32 v189, 0xffff0000, v84\n"
      "v_lshlrev_b32 v190, 16, v85\n"
      "v_and_b32 v191, 0xffff0000, v85\n"
      "v_lshlrev_b32 v192, 16, v86\n"
      "v_and_b32 v193, 0xffff0000, v86\n"
      "v_lshlrev_b32 v194, 16, v87\n"
      "v_and_b32 v195, 0xffff0000, v87\n"
      "v_pk_mul_f32 v[188:189], v[188:189], s[94:95]\n"
      "v_pk_mul_f32 v[190:191], v[190:191], s[94:95]\n"
      "v_pk_mul_f32 v[192:193], v[192:193], s[94:95]\n"
      "v_pk_mul_f32 v[194:195], v[194:195], s[94:95]\n"
      "v_cvt_pk_bf16_f32 v84, v188, v189\n"
      "v_cvt_pk_bf16_f32 v85, v190, v191\n"
      "v_cvt_pk_bf16_f32 v86, v192, v193\n"
      "v_cvt_pk_bf16_f32 v87, v194, v195\n"
      "ds_write_b128 v212, v[84:87] offset:1024\n"
      "v_lshlrev_b32 v188, 16, v88\n"
      "v_and_b32 v189, 0xffff0000, v88\n"
      "v_lshlrev_b32 v190, 16, v89\n"
      "v_and_b32 v191, 0xffff0000, v89\n"
      "v_lshlrev_b32 v192, 16, v90\n"
      "v_and_b32 v193, 0xffff0000, v90\n"
      "v_lshlrev_b32 v194, 16, v91\n"
      "v_and_b32 v195, 0xffff0000, v91\n"
      "v_pk_mul_f32 v[188:189], v[188:189], s[94:95]\n"
      "v_pk_mul_f32 v[190:191], v[190:191], s[94:95]\n"
      "v_pk_mul_f32 v[192:193], v[192:193], s[94:95]\n"
      "v_pk_mul_f32 v[194:195], v[194:195], s[94:95]\n"
      "v_cvt_pk_bf16_f32 v88, v188, v189\n"
      "v_cvt_pk_bf16_f32 v89, v190, v191\n"
      "v_cvt_pk_bf16_f32 v90, v192, v193\n"
      "v_cvt_pk_bf16_f32 v91, v194, v195\n"
      "ds_write_b128 v212, v[88:91] offset:2048\n"
      "v_lshlrev_b32 v188, 16, v92\n"
      "v_and_b32 v189, 0xffff0000, v92\n"
      "v_lshlrev_b32 v190, 16, v93\n"
      "v_and_b32 v191, 0xffff0000, v93\n"
      "v_lshlrev_b32 v192, 16, v94\n"
      "v_and_b32 v193, 0xffff0000, v94\n"
      "v_lshlrev_b32 v194, 16, v95\n"
      "v_and_b32 v195, 0xffff0000, v95\n"
      "v_pk_mul_f32 v[188:189], v[188:189], s[94:95]\n"
      "v_pk_mul_f32 v[190:191], v[190:191], s[94:95]\n"
      "v_pk_mul_f32 v[192:193], v[192:193], s[94:95]\n"
      "v_pk_mul_f32 v[194:195], v[194:195], s[94:95]\n"
      "v_cvt_pk_bf16_f32 v92, v188, v189\n"
      "v_cvt_pk_bf16_f32 v93, v190, v191\n"
      "v_cvt_pk_bf16_f32 v94, v192, v193\n"
      "v_cvt_pk_bf16_f32 v95, v194, v195\n"
      "ds_write_b128 v212, v[92:95] offset:3072\n"
      "s_mov_b32 m0, s62\n"
      "s_nop 0\n"
      "buffer_load_dwordx4 v248, s[56:59], s60 offen nt lds\n"
      "buffer_load_dwordx4 v248, s[56:59], s60 offen offset:2048 nt lds\n"
      "s_bitset1_b32 m0, 10\n"
      "s_nop 0\n"
      "buffer_load_dwordx4 v249, s[56:59], s60 offen nt lds\n"
      "buffer_load_dwordx4 v249, s[56:59], s60 offen offset:2048 nt lds\n"
      "s_xor_b64 s[56:57], s[56:57], s[32:33]\n"
      "s_bitset1_b32 s61, 15\n"
      "s_add_u32 s61, s61, 0x2000\n"
      "s_bitset0_b32 s61, 15\n"
      "s_xor_b32 s60, s61, s51\n"
      "s_add_u32 s62, s62, 0x4000\n"
      "s_and_b32 s62, s62, 0x1ffff\n"
      "s_cmp_eq_u32 s50, 0\n"
      "s_cselect_b32 s38, 0x41554001, 1\n"
      "s_cselect_b32 s39, 0x3e4c0883, 3\n"
      "s_cselect_b32 s52, 0x3931aa90, 0\n"
      "s_cselect_b32 s53, 0x80811, 0\n"
      "s_mov_b64 s[34:35], 0\n"
      "s_and_b32 s69, %[blk], 7\n"
      "s_mul_i32 s69, s69, 5\n"
      "s_add_u32 s69, s69, s44\n"
      "s_and_b32 s69, s69, 15\n"
      "s_mul_i32 s74, s69, 16\n"
      "s_lshr_b32 s74, s74, 6\n"
      "s_lshl2_add_u32 s76, s74, 9\n"
      "s_bitset1_b64 s[34:35], s76\n"
      "s_lshl_b32 s74, s74, 1\n"
      "s_sub_u32 s74, 28, s74\n"
      "s_bitset1_b32 s39, s74\n"
      "s_mul_i32 s74, s69, 20\n"
      "s_lshr_b32 s74, s74, 6\n"
      "s_lshl2_add_u32 s76, s74, 25\n"
      "s_bitset1_b64 s[34:35], s76\n"
      "s_lshl_b32 s74, s74, 1\n"
      "s_sub_u32 s74, 20, s74\n"
      "s_bitset1_b32 s39, s74\n"
      "s_mul_i32 s74, s69, 20\n"
      "s_lshr_b32 s74, s74, 6\n"
      "s_lshl2_add_u32 s76, s74, 45\n"
      "s_bitset1_b64 s[34:35], s76\n"
      "s_lshl_b32 s74, s74, 1\n"
      "s_sub_u32 s74, 10, s74\n"
      "s_bitset1_b32 s39, s74\n"
      "s_mov_b32 s83, 0\n"
      "s_mov_b32 s54, 3\n"
      "s_mov_b32 s70, 300\n"
      "s_mov_b32 s80, 300\n"
      "s_mov_b32 s86, 59\n"
      "s_mov_b32 s77, 177\n"
      "s_lshl_b32 s91, s50, 16\n"
      "s_sub_u32 s87, 70, s91\n"
      "s_sub_u32 s90, 34, s91\n"
      "s_sub_u32 s72, s75, 14336\n"
      "s_and_b32 s79, %[blk], 3\n"
      "s_lshl_b32 s79, s79, 11\n"
      "s_add_u32 s79, s79, 8192\n"
      "s_and_b32 s69, %[blk], 7\n"
      "s_lshl_b32 s69, s69, 2\n"
      "s_and_b32 s74, %[blk], 0xe0\n"
      "s_add_u32 s69, s69, s74\n"
      "s_mul_i32 s69, s69, 36\n"
      "s_lshr_b32 s69, s69, 8\n"
      "s_add_u32 s66, s93, 135\n"
      "s_sub_u32 s81, s93, s71\n"
      "s_sub_u32 s81, s81, s69\n"
      "s_add_u32 s81, s81, 15\n"
      "s_sub_u32 s66, s66, s91\n"
      "s_mov_b32 s88, s66\n"
      "s_mov_b32 s65, s66\n"
      "s_getpc_b64 s[84:85]\n"
      "PC_%=:\n"
      "s_add_u32 s84, s84, HOOK_%=-PC_%=\n"
      "s_addc_u32 s85, s85, 0\n"
      "s_waitcnt vmcnt(14) lgkmcnt(0)\n"
      "s_barrier\n"
      "ds_read_b128 a[64:67], v198 offset:8192\n"
      "ds_read_b128 a[72:75], v199 offset:8192\n"
      "ds_read_b128 a[80:83], v198 offset:9216\n"
      "ds_read_b128 a[88:91], v199 offset:9216\n"
      "ds_read_b128 a[0:3], v196 offset:0\n"
      "ds_read_b128 a[16:19], v197 offset:0\n"
      "ds_read_b128 a[32:35], v196 offset:1024\n"
      "ds_read_b128 a[48:51], v197 offset:1024\n"
      "ds_read_b128 a[4:7], v196 offset:4096\n"
      "ds_read_b128 a[20:23], v197 offset:4096\n"
      "ds_read_b128 a[36:39], v196 offset:5120\n"
      "ds_read_b128 a[52:55], v197 offset:5120\n"
      "v_readfirstlane_b32 s40, v208\n"
      "s_mov_b32 s94, 0xceabfb8f\n"
      "v_lshlrev_b32 v144, 16, v200\n"
      "v_and_b32 v145, 0xffff0000, v200\n"
      "v_lshlrev_b32 v146, 16, v201\n"
      "v_and_b32 v147, 0xffff0000, v201\n"
      "v_lshlrev_b32 v148, 16, v202\n"
      "v_and_b32 v149, 0xffff0000, v202\n"
      "v_lshlrev_b32 v150, 16, v203\n"
      "v_and_b32 v151, 0xffff0000, v203\n"
      "v_lshlrev_b32 v152, 16, v204\n"
      "v_and_b32 v153, 0xffff0000, v204\n"
      "v_lshlrev_b32 v154, 16, v205\n"
      "v_and_b32 v155, 0xffff0000, v205\n"
      "v_lshlrev_b32 v156, 16, v206\n"
      "v_and_b32 v157, 0xffff0000, v206\n"
      "v_lshlrev_b32 v158, 16, v207\n"
      "v_and_b32 v159, 0xffff0000, v207\n"
      "s_waitcnt lgkmcnt(4)\n"
      "v_mfma_f32_16x16x32_bf16 v[64:67], a[64:67], a[0:3], 0\n"
      "ds_read_b128 a[8:11], v196 offset:8192\n"
      "v_fma_f32 v144, -v144, s94, s94\n"
      "v_mfma_f32_16x16x32_bf16 v[64:67], a[72:75], a[16:19], v[64:67]\n"
      "ds_read_b128 a[24:27], v197 offset:8192\n"
      "v_fma_f32 v145, -v145, s94, s94\n"
      "v_mfma_f32_16x16x32_bf16 v[64:67], a[80:83], a[32:35], v[64:67]\n"
      "ds_read_b128 a[40:43], v196 offset:9216\n"
      "v_fma_f32 v146, -v146, s94, s94\n"
      "v_mfma_f32_16x16x32_bf16 v[64:67], a[88:91], a[48:51], v[64:67]\n"
      "ds_read_b128 a[56:59], v197 offset:9216\n"
      "v_fma_f32 v147, -v147, s94, s94\n"
      "s_waitcnt lgkmcnt(4)\n"
      "v_mfma_f32_16x16x32_bf16 v[72:75], a[64:67], a[4:7], 0\n"
      "ds_read_b128 a[12:15], v196 offset:12288\n"
      "v_fma_f32 v148, -v148, s94, s94\n"
      "v_mfma_f32_16x16x32_bf16 v[72:75], a[72:75], a[20:23], v[72:75]\n"
      "ds_read_b128 a[28:31], v197 offset:12288\n"
      "v_fma_f32 v149, -v149, s94, s94\n"
      "v_mfma_f32_16x16x32_bf16 v[72:75], a[80:83], a[36:39], v[72:75]\n"
      "ds_read_b128 a[44:47], v196 offset:13312\n"
      "v_fma_f32 v150, -v150, s94, s94\n"
      "v_mfma_f32_16x16x32_bf16 v[72:75], a[88:91], a[52:55], v[72:75]\n"
      "ds_read_b128 a[60:63], v197 offset:13312\n"
      "v_fma_f32 v151, -v151, s94, s94\n"
      "s_waitcnt lgkmcnt(4)\n"
      "v_mfma_f32_16x16x32_bf16 v[80:83], a[64:67], a[8:11], 0\n"
      "ds_read_b128 a[96:99], v198 offset:0\n"
      "v_fma_f32 v152, -v152, s94, s94\n"
      "v_mfma_f32_16x16x32_bf16 v[80:83], a[72:75], a[24:27], v[80:83]\n"
      "ds_read_b128 a[100:103], v198 offset:4096\n"
      "v_fma_f32 v153, -v153, s94, s94\n"
      "v_mfma_f32_16x16x32_bf16 v[80:83], a[80:83], a[40:43], v[80:83]\n"
      "ds_read_b128 a[104:107], v199 offset:0\n"
      "v_fma_f32 v154, -v154, s94, s94\n"
      "v_mfma_f32_16x16x32_bf16 v[80:83], a[88:91], a[56:59], v[80:83]\n"
      "ds_read_b128 a[108:111], v199 offset:4096\n"
      "v_fma_f32 v155, -v155, s94, s94\n"
      "s_waitcnt lgkmcnt(4)\n"
      "v_mfma_f32_16x16x32_bf16 v[88:91], a[64:67], a[12:15], 0\n"
      "ds_read_b128 a[112:115], v198 offset:1024\n"
      "v_fma_f32 v156, -v156, s94, s94\n"
      "v_mfma_f32_16x16x32_bf16 v[88:91], a[72:75], a[28:31], v[88:91]\n"
      "ds_read_b128 a[116:119], v198 offset:5120\n"
      "v_fma_f32 v157, -v157, s94, s94\n"
      "v_mfma_f32_16x16x32_bf16 v[88:91], a[80:83], a[44:47], v[88:91]\n"
      "ds_read_b128 a[120:123], v199 offset:1024\n"
      "v_fma_f32 v158, -v158, s94, s94\n"
      "v_mfma_f32_16x16x32_bf16 v[88:91], a[88:91], a[60:63], v[88:91]\n"
      "ds_read_b128 a[124:127], v199 offset:5120\n"
      "v_fma_f32 v159, -v159, s94, s94\n"
      "s_mov_b32 m0, s62\n"
      "s_nop 0\n"
      "buffer_load_dwordx4 v248, s[56:59], s60 offen nt lds\n"
      "buffer_load_dwordx4 v248, s[56:59], s60 offen offset:2048 nt lds\n"
      "s_bitset1_b32 m0, 10\n"
      "s_nop 0\n"
      "buffer_load_dwordx4 v249, s[56:59], s60 offen nt lds\n"
      "buffer_load_dwordx4 v249, s[56:59], s60 offen offset:2048 nt lds\n"
      "s_waitcnt lgkmcnt(0)\n"
      "v_mfma_f32_16x16x32_bf16 v[96:99], a[96:99], a[0:3], v[144:147]\n"
      "s_lshl_b32 s69, s44, 3\n"
      "v_mfma_f32_16x16x32_bf16 v[96:99], a[104:107], a[16:19], v[96:99]\n"
      "s_lshr_b32 s74, s50, 12\n"
      "v_max3_f32 v188, v64, v65, v66\n"
      "v_max3_f32 v189, v72, v73, v74\n"
      "v_mfma_f32_16x16x32_bf16 v[96:99], a[112:115], a[32:35], v[96:99]\n"
      "s_add_u32 s69, s69, s74\n"
      "v_max3_f32 v190, v80, v81, v82\n"
      "v_max3_f32 v191, v88, v89, v90\n"
      "v_mfma_f32_16x16x32_bf16 v[96:99], a[120:123], a[48:51], v[96:99]\n"
      "s_mul_i32 s69, s69, 4864\n"
      "v_max_f32 v188, v188, v67\n"
      "v_max_f32 v189, v189, v75\n"
      "v_mfma_f32_16x16x32_bf16 v[104:107], a[96:99], a[4:7], v[152:155]\n"
      "s_add_u32 s69, s69, 81920\n"
      "v_max_f32 v190, v190, v83\n"
      "v_max_f32 v191, v191, v91\n"
      "v_mfma_f32_16x16x32_bf16 v[104:107], a[104:107], a[20:23], v[104:107]\n"
      "s_add_u32 s24, s18, s69\n"
      "v_mov_b32 v192, v188\n"
      "v_mov_b32 v193, v189\n"
      "v_mfma_f32_16x16x32_bf16 v[104:107], a[112:115], a[36:39], v[104:107]\n"
      "s_addc_u32 s25, s19, 0\n"
      "v_mov_b32 v194, v190\n"
      "v_mov_b32 v195, v191\n"
      "v_mfma_f32_16x16x32_bf16 v[104:107], a[120:123], a[52:55], v[104:107]\n"
      "s_movk_i32 s26, 24320\n"
      "v_permlane16_swap_b32 v188, v192\n"
      "v_permlane16_swap_b32 v189, v193\n"
      "v_mfma_f32_16x16x32_bf16 v[112:115], a[96:99], a[8:11], v[144:147]\n"
      "s_mul_i32 s78, s43, 19456\n"
      "v_permlane16_swap_b32 v190, v194\n"
      "v_permlane16_swap_b32 v191, v195\n"
      "v_mfma_f32_16x16x32_bf16 v[112:115], a[104:107], a[24:27], v[112:115]\n"
      "s_and_b32 s69, s50, 4096\n"
      "v_max_f32 v188, v188, v192\n"
      "v_max_f32 v189, v189, v193\n"
      "v_mfma_f32_16x16x32_bf16 v[112:115], a[112:115], a[40:43], v[112:115]\n"
      "s_lshl_b32 s69, s69, 2\n"
      "v_max_f32 v190, v190, v194\n"
      "v_max_f32 v191, v191, v195\n"
      "v_mfma_f32_16x16x32_bf16 v[112:115], a[120:123], a[56:59], v[112:115]\n"
      "s_lshr_b32 s74, s50, 7\n"
      "v_mov_b32 v192, v188\n"
      "v_mov_b32 v193, v189\n"
      "v_mfma_f32_16x16x32_bf16 v[120:123], a[96:99], a[12:15], v[152:155]\n"
      "s_and_b32 s74, s74, 64\n"
      "v_mov_b32 v194, v190\n"
      "v_mov_b32 v195, v191\n"
      "v_mfma_f32_16x16x32_bf16 v[120:123], a[104:107], a[28:31], v[120:123]\n"
      "s_add_u32 s69, s69, s74\n"
      "v_permlane32_swap_b32 v188, v192\n"
      "v_permlane32_swap_b32 v189, v193\n"
      "v_mfma_f32_16x16x32_bf16 v[120:123], a[112:115], a[44:47], v[120:123]\n"
      "s_lshl_b32 s74, s43, 7\n"
      "v_permlane32_swap_b32 v190, v194\n"
      "v_permlane32_swap_b32 v191, v195\n"
      "v_mfma_f32_16x16x32_bf16 v[120:123], a[120:123], a[60:63], v[120:123]\n"
      "s_add_u32 s69, s69, s74\n"
      "v_max_f32 v188, v188, v192\n"
      "v_max_f32 v189, v189, v193\n"
      "v_mfma_f32_16x16x32_bf16 v[100:103], a[100:103], a[0:3], v[148:151]\n"
      "s_lshl_b32 s74, s44, 15\n"
      "v_max_f32 v190, v190, v194\n"
      "v_max_f32 v191, v191, v195\n"
      "v_mfma_f32_16x16x32_bf16 v[100:103], a[108:111], a[16:19], v[100:103]\n"
      "s_add_u32 s69, s69, s74\n"
      "v_xor_b32 v176, 0x80000000, v188\n"
      "v_xor_b32 v177, 0x80000000, v189\n"
      "v_mfma_f32_16x16x32_bf16 v[100:103], a[116:119], a[32:35], v[100:103]\n"
      "s_add_u32 s30, s16, s69\n"
      "v_xor_b32 v178, 0x80000000, v190\n"
      "v_xor_b32 v179, 0x80000000, v191\n"
      "v_mfma_f32_16x16x32_bf16 v[100:103], a[124:127], a[48:51], v[100:103]\n"
      "s_addc_u32 s31, s17, 0\n"
      "v_mov_b32 v160, v176\n"
      "v_mov_b32 v161, v176\n"
      "v_mfma_f32_16x16x32_bf16 v[108:111], a[100:103], a[4:7], v[156:159]\n"
      "v_mov_b32 v162, v176\n"
      "v_mov_b32 v163, v176\n"
      "v_mfma_f32_16x16x32_bf16 v[108:111], a[108:111], a[20:23], v[108:111]\n"
      "v_mov_b32 v164, v177\n"
      "v_mov_b32 v165, v177\n"
      "v_mfma_f32_16x16x32_bf16 v[108:111], a[116:119], a[36:39], v[108:111]\n"
      "v_mov_b32 v166, v177\n"
      "v_mov_b32 v167, v177\n"
      "v_mfma_f32_16x16x32_bf16 v[108:111], a[124:127], a[52:55], v[108:111]\n"
      "v_mov_b32 v168, v178\n"
      "v_mov_b32 v169, v178\n"
      "v_mfma_f32_16x16x32_bf16 v[116:119], a[100:103], a[8:11], v[148:151]\n"
      "v_mov_b32 v170, v178\n"
      "v_mov_b32 v171, v178\n"
      "v_mfma_f32_16x16x32_bf16 v[116:119], a[108:111], a[24:27], v[116:119]\n"
      "v_mov_b32 v172, v179\n"
      "v_mov_b32 v173, v179\n"
      "v_mfma_f32_16x16x32_bf16 v[116:119], a[116:119], a[40:43], v[116:119]\n"
      "v_mov_b32 v174, v179\n"
      "v_mov_b32 v175, v179\n"
      "v_mfma_f32_16x16x32_bf16 v[116:119], a[124:127], a[56:59], v[116:119]\n"
      "v_mov_b32 v209, 139248\n"
      "v_lshl_add_u32 v232, s50, 1, v241\n"
      "v_mfma_f32_16x16x32_bf16 v[124:127], a[100:103], a[12:15], v[156:159]\n"
      "v_add_u32 v232, 0x10000, v232\n"
      "v_xor_b32 v233, 0x4000, v232\n"
      "v_mfma_f32_16x16x32_bf16 v[124:127], a[108:111], a[28:31], v[124:127]\n"
      "v_add_u32 v234, 0x10000, v232\n"
      "v_xor_b32 v235, 0x4000, v234\n"
      "v_mfma_f32_16x16x32_bf16 v[124:127], a[116:119], a[44:47], v[124:127]\n"
      "v_lshrrev_b32 v240, 2, v241\n"
      "v_sub_u32 v240, v241, v240\n"
      "v_mfma_f32_16x16x32_bf16 v[124:127], a[124:127], a[60:63], v[124:127]\n"
      "v_add_u32 v240, 0x1000, v240\n"
      "v_mov_b32 v196, 0\n"
      "v_pk_add_f32 v[96:97], v[96:97], v[160:161]\n"
      "v_pk_add_f32 v[98:99], v[98:99], v[160:161]\n"
      "v_pk_add_f32 v[104:105], v[104:105], v[164:165]\n"
      "v_pk_add_f32 v[106:107], v[106:107], v[164:165]\n"
      "v_pk_add_f32 v[112:113], v[112:113], v[168:169]\n"
      "v_pk_add_f32 v[114:115], v[114:115], v[168:169]\n"
      "v_pk_add_f32 v[120:121], v[120:121], v[172:173]\n"
      "v_pk_add_f32 v[122:123], v[122:123], v[172:173]\n"
      "v_pk_add_f32 v[100:101], v[100:101], v[160:161]\n"
      "v_pk_add_f32 v[102:103], v[102:103], v[160:161]\n"
      "v_pk_add_f32 v[108:109], v[108:109], v[164:165]\n"
      "v_pk_add_f32 v[110:111], v[110:111], v[164:165]\n"
      "v_pk_add_f32 v[116:117], v[116:117], v[168:169]\n"
      "v_pk_add_f32 v[118:119], v[118:119], v[168:169]\n"
      "v_pk_add_f32 v[124:125], v[124:125], v[172:173]\n"
      "v_pk_add_f32 v[126:127], v[126:127], v[172:173]\n"
      "s_memrealtime s[68:69]\n"
      "s_waitcnt vmcnt(14) lgkmcnt(0)\n"
      "s_sub_u32 s67, s68, s88\n"
      "s_cmp_lt_i32 s67, -8\n"
      "s_cbranch_scc1 GW0_%=\n"
      "GX0_%=:\n"
      "s_barrier\n"
      "ds_read_b128 a[64:67], v242 offset:0\n"
      "ds_read_b128 a[68:71], v242 offset:4096\n"
      "ds_read_b128 a[72:75], v243 offset:0\n"
      "ds_read_b128 a[76:79], v243 offset:4096\n"
      "ds_read_b128 a[80:83], v242 offset:1024\n"
      "ds_read_b128 a[84:87], v242 offset:5120\n"
      "ds_read_b128 a[88:91], v243 offset:1024\n"
      "ds_read_b128 a[92:95], v243 offset:5120\n"
      "ds_write_b32 v209, v196\n"
      "s_add_u32 s38, s38, s38\n"
      "s_memrealtime s[68:69]\n"
      "s_xor_b64 s[56:57], s[56:57], s[32:33]\n"
      "s_bitset1_b32 s61, 15\n"
      "s_add_u32 s61, s61, 0x2000\n"
      "s_bitset0_b32 s61, 15\n"
      "s_xor_b32 s60, s61, s51\n"
      "s_add_u32 s62, s62, 0x4000\n"
      "s_and_b32 s62, s62, 0x1ffff\n"
      "s_waitcnt lgkmcnt(0)\n"
      "s_sub_u32 s67, s68, s66\n"
      "s_cmp_gt_i32 s67, s70\n"
      "s_cselect_b32 s69, s77, s86\n"
      "s_add_u32 s66, s66, s69\n"
      "s_add_u32 s67, s67, 50\n"
      "s_min_i32 s70, s70, s67\n"
      "s_max_i32 s70, s70, s80\n"
      "LOOP_%=:\n"
      "v_mfma_f32_16x16x32_bf16 v[64:67], a[64:67], a[0:3], v[160:163]\n"
      "s_sub_u32 s67, s68, s82\n"
      "v_exp_f32 v188, v96\n"
      "v_mfma_f32_16x16x32_bf16 v[64:67], a[72:75], a[16:19], v[64:67]\n"
      "s_min_i32 s76, s67, s83\n"
      "v_add_f32 v180, v180, v184\n"
      "v_add_f32 v181, v181, v185\n"
      "v_mfma_f32_16x16x32_bf16 v[64:67], a[80:83], a[32:35], v[64:67]\n"
      "s_mov_b32 s83, s67\n"
      "v_exp_f32 v189, v97\n"
      "v_mfma_f32_16x16x32_bf16 v[64:67], a[88:91], a[48:51], v[64:67]\n"
      "s_sub_u32 s76, s76, 14\n"
      "v_add_f32 v182, v182, v186\n"
      "v_add_f32 v183, v183, v187\n"
      "v_mfma_f32_16x16x32_bf16 v[72:75], a[64:67], a[4:7], v[164:167]\n"
      "s_min_i32 s76, s76, s87\n"
      "v_exp_f32 v190, v98\n"
      "v_mfma_f32_16x16x32_bf16 v[72:75], a[72:75], a[20:23], v[72:75]\n"
      "s_max_i32 s76, s76, s90\n"
      "v_exp_f32 v191, v99\n"
      "v_mfma_f32_16x16x32_bf16 v[72:75], a[80:83], a[36:39], v[72:75]\n"
      "s_mov_b32 s82, s68\n"
      "v_cvt_pk_bf16_f32 v144, v188, v189\n"
      "v_cvt_pk_bf16_f32 v145, v190, v191\n"
      "v_mfma_f32_16x16x32_bf16 v[72:75], a[88:91], a[52:55], v[72:75]\n"
      "s_add_u32 s65, s68, s76\n"
      "v_exp_f32 v192, v100\n"
      "v_mfma_f32_16x16x32_bf16 v[80:83], a[64:67], a[8:11], v[168:171]\n"
      "s_max_u32 s88, s66, s65\n"
      "v_exp_f32 v193, v101\n"
      "v_mfma_f32_16x16x32_bf16 v[80:83], a[72:75], a[24:27], v[80:83]\n"
      "s_memrealtime s[68:69]\n"
      "v_exp_f32 v194, v102\n"
      "v_mfma_f32_16x16x32_bf16 v[80:83], a[80:83], a[40:43], v[80:83]\n"
      "s_mov_b32 m0, s62\n"
      "v_exp_f32 v195, v103\n"
      "v_mfma_f32_16x16x32_bf16 v[80:83], a[88:91], a[56:59], v[80:83]\n"
      "buffer_load_dwordx4 v248, s[56:59], s60 offen nt lds\n"
      "v_mfma_f32_16x16x32_bf16 v[88:91], a[64:67], a[12:15], v[172:175]\n"
      "buffer_load_dwordx4 v248, s[56:59], s60 offen offset:2048 nt lds\n"
      "v_mfma_f32_16x16x32_bf16 v[88:91], a[72:75], a[28:31], v[88:91]\n"
      "s_bitset1_b32 m0, 10\n"
      "s_add_u32 s39, s39, s39\n"
      "v_cvt_pk_bf16_f32 v146, v192, v193\n"
      "v_mfma_f32_16x16x32_bf16 v[88:91], a[80:83], a[44:47], v[88:91]\n"
      "buffer_load_dwordx4 v249, s[56:59], s60 offen nt lds\n"
      "v_mfma_f32_16x16x32_bf16 v[88:91], a[88:91], a[60:63], v[88:91]\n"
      "buffer_load_dwordx4 v249, s[56:59], s60 offen offset:2048 nt lds\n"
      "s_cbranch_scc1 SP1_%=\n"
      "HD1_%=:\n"
      "s_cmp_lg_u32 s54, 0\n"
      "s_waitcnt vmcnt(12) lgkmcnt(0)\n"
      "s_cbranch_scc1 W1_%=\n"
      "s_waitcnt vmcnt(8)\n"
      "W1_%=:\n"
      "s_sub_u32 s67, s68, s88\n"
      "s_cmp_lt_i32 s67, -8\n"
      "s_cbranch_scc1 GW4_%=\n"
      "GX4_%=:\n"
      "s_barrier\n"
      "v_mfma_f32_16x16x32_bf16 v[68:71], a[68:71], a[0:3], v[160:163]\n"
      "s_memrealtime s[68:69]\n"
      "ds_read_b64_tr_b16 a[96:97], v244 offset:0\n"
      "ds_read_b64_tr_b16 a[98:99], v244 offset:4096\n"
      "v_mfma_f32_16x16x32_bf16 v[68:71], a[76:79], a[16:19], v[68:71]\n"
      "s_xor_b64 s[56:57], s[56:57], s[32:33]\n"
      "ds_read_b64_tr_b16 a[100:101], v245 offset:0\n"
      "ds_read_b64_tr_b16 a[102:103], v245 offset:4096\n"
      "v_mfma_f32_16x16x32_bf16 v[68:71], a[84:87], a[32:35], v[68:71]\n"
      "s_bitset1_b32 s61, 15\n"
      "ds_read_b64_tr_b16 a[104:105], v246 offset:0\n"
      "ds_read_b64_tr_b16 a[106:107], v246 offset:4096\n"
      "v_mfma_f32_16x16x32_bf16 v[68:71], a[92:95], a[48:51], v[68:71]\n"
      "s_add_u32 s61, s61, 0x2000\n"
      "ds_read_b64_tr_b16 a[108:109], v247 offset:0\n"
      "ds_read_b64_tr_b16 a[110:111], v247 offset:4096\n"
      "v_mfma_f32_16x16x32_bf16 v[76:79], a[68:71], a[4:7], v[164:167]\n"
      "s_bitset0_b32 s61, 15\n"
      "v_exp_f32 v188, v104\n"
      "v_mfma_f32_16x16x32_bf16 v[76:79], a[76:79], a[20:23], v[76:79]\n"
      "s_xor_b32 s60, s61, s51\n"
      "v_exp_f32 v189, v105\n"
      "v_mfma_f32_16x16x32_bf16 v[76:79], a[84:87], a[36:39], v[76:79]\n"
      "s_add_u32 s62, s62, 0x4000\n"
      "v_cvt_pk_bf16_f32 v147, v194, v195\n"
      "v_cvt_pk_bf16_f32 v148, v188, v189\n"
      "v_mfma_f32_16x16x32_bf16 v[76:79], a[92:95], a[52:55], v[76:79]\n"
      "s_and_b32 s62, s62, 0x1ffff\n"
      "v_exp_f32 v190, v106\n"
      "v_mfma_f32_16x16x32_bf16 v[184:187], a[128:131], v[144:147], 0\n"
      "v_exp_f32 v191, v107\n"
      "v_mfma_f32_16x16x32_bf16 v[84:87], a[68:71], a[8:11], v[168:171]\n"
      "v_exp_f32 v192, v108\n"
      "v_mfma_f32_16x16x32_bf16 v[84:87], a[76:79], a[24:27], v[84:87]\n"
      "v_exp_f32 v193, v109\n"
      "s_waitcnt lgkmcnt(0)\n"
      "v_mfma_f32_16x16x32_bf16 v[84:87], a[84:87], a[40:43], v[84:87]\n"
      "s_sub_u32 s67, s68, s66\n"
      "ds_read_b64_tr_b16 a[112:113], v244 offset:1024\n"
      "v_cvt_pk_bf16_f32 v149, v190, v191\n"
      "v_mfma_f32_16x16x32_bf16 v[84:87], a[92:95], a[56:59], v[84:87]\n"
      "s_cmp_gt_i32 s67, s70\n"
      "v_exp_f32 v194, v110\n"
      "v_mfma_f32_16x16x32_bf16 v[92:95], a[68:71], a[12:15], v[172:175]\n"
      "s_cselect_b32 s69, s77, s86\n"
      "ds_read_b64_tr_b16 a[114:115], v244 offset:5120\n"
      "v_cvt_pk_bf16_f32 v150, v192, v193\n"
      "v_mfma_f32_16x16x32_bf16 v[92:95], a[76:79], a[28:31], v[92:95]\n"
      "s_add_u32 s66, s66, s69\n"
      "v_exp_f32 v195, v111\n"
      "v_mfma_f32_16x16x32_bf16 v[92:95], a[84:87], a[44:47], v[92:95]\n"
      "s_add_u32 s67, s67, 50\n"
      "ds_read_b64_tr_b16 a[116:117], v245 offset:1024\n"
      "v_cvt_pk_bf16_f32 v151, v194, v195\n"
      "v_mfma_f32_16x16x32_bf16 v[92:95], a[92:95], a[60:63], v[92:95]\n"
      "s_min_i32 s70, s70, s67\n"
      "v_exp_f32 v188, v112\n"
      "v_mfma_f32_16x16x32_bf16 v[184:187], a[132:135], v[148:151], v[184:187]\n"
      "s_max_i32 s70, s70, s80\n"
      "ds_read_b64_tr_b16 a[118:119], v245 offset:5120\n"
      "ds_read_b64_tr_b16 a[120:121], v246 offset:1024\n"
      "v_mfma_f32_16x16x32_bf16 v[0:3], a[96:99], v[128:131], v[0:3]\n"
      "s_sub_u32 s67, s68, s82\n"
      "v_exp_f32 v189, v113\n"
      "v_mfma_f32_16x16x32_bf16 v[4:7], a[96:99], v[132:135], v[4:7]\n"
      "s_min_i32 s76, s67, s83\n"
      "ds_read_b64_tr_b16 a[122:123], v246 offset:5120\n"
      "v_cvt_pk_bf16_f32 v152, v188, v189\n"
      "v_mfma_f32_16x16x32_bf16 v[8:11], a[96:99], v[136:139], v[8:11]\n"
      "s_mov_b32 s83, s67\n"
      "v_exp_f32 v190, v114\n"
      "v_mfma_f32_16x16x32_bf16 v[12:15], a[96:99], v[140:143], v[12:15]\n"
      "s_sub_u32 s76, s76, 14\n"
      "ds_read_b64_tr_b16 a[124:125], v247 offset:1024\n"
      "ds_read_b64_tr_b16 a[126:127], v247 offset:5120\n"
      "v_mfma_f32_16x16x32_bf16 v[28:31], a[100:103], v[140:143], v[28:31]\n"
      "s_min_i32 s76, s76, s87\n"
      "v_exp_f32 v191, v115\n"
      "v_mfma_f32_16x16x32_bf16 v[24:27], a[100:103], v[136:139], v[24:27]\n"
      "s_max_i32 s76, s76, s90\n"
      "v_exp_f32 v192, v116\n"
      "v_mfma_f32_16x16x32_bf16 v[20:23], a[100:103], v[132:135], v[20:23]\n"
      "s_mov_b32 s82, s68\n"
      "v_exp_f32 v193, v117\n"
      "v_mfma_f32_16x16x32_bf16 v[16:19], a[100:103], v[128:131], v[16:19]\n"
      "s_add_u32 s65, s68, s76\n"
      "v_cvt_pk_bf16_f32 v153, v190, v191\n"
      "v_cvt_pk_bf16_f32 v154, v192, v193\n"
      "v_mfma_f32_16x16x32_bf16 a[144:147], a[104:107], v[128:131], a[144:147]\n"
      "s_max_u32 s88, s66, s65\n"
      "v_exp_f32 v194, v118\n"
      "v_mfma_f32_16x16x32_bf16 a[148:151], a[104:107], v[132:135], a[148:151]\n"
      "s_memrealtime s[68:69]\n"
      "v_exp_f32 v195, v119\n"
      "v_mfma_f32_16x16x32_bf16 a[152:155], a[104:107], v[136:139], a[152:155]\n"
      "s_mov_b32 m0, s62\n"
      "v_exp_f32 v188, v120\n"
      "v_mfma_f32_16x16x32_bf16 a[156:159], a[104:107], v[140:143], a[156:159]\n"
      "buffer_load_dwordx4 v248, s[56:59], s60 offen nt lds\n"
      "v_mfma_f32_16x16x32_bf16 a[172:175], a[108:111], v[140:143], a[172:175]\n"
      "buffer_load_dwordx4 v248, s[56:59], s60 offen offset:2048 nt lds\n"
      "v_mfma_f32_16x16x32_bf16 a[168:171], a[108:111], v[136:139], a[168:171]\n"
      "s_bitset1_b32 m0, 10\n"
      "s_add_u32 s38, s38, s38\n"
      "v_cvt_pk_bf16_f32 v155, v194, v195\n"
      "v_mfma_f32_16x16x32_bf16 a[164:167], a[108:111], v[132:135], a[164:167]\n"
      "buffer_load_dwordx4 v249, s[56:59], s60 offen nt lds\n"
      "v_mfma_f32_16x16x32_bf16 v[184:187], a[136:139], v[152:155], v[184:187]\n"
      "buffer_load_dwordx4 v249, s[56:59], s60 offen offset:2048 nt lds\n"
      "s_cbranch_scc1 SP2_%=\n"
      "HD2_%=:\n"
      "s_cmp_lg_u32 s54, 0\n"
      "s_waitcnt vmcnt(12) lgkmcnt(0)\n"
      "s_cbranch_scc1 W2_%=\n"
      "s_waitcnt vmcnt(8)\n"
      "W2_%=:\n"
      "s_sub_u32 s67, s68, s88\n"
      "s_cmp_lt_i32 s67, -8\n"
      "s_cbranch_scc1 GW5_%=\n"
      "GX5_%=:\n"
      "s_barrier\n"
      "v_mfma_f32_16x16x32_bf16 a[160:163], a[108:111], v[128:131], a[160:163]\n"
      "s_memrealtime s[68:69]\n"
      "ds_read_b128 a[64:67], v242 offset:32768\n"
      "ds_read_b128 a[72:75], v243 offset:32768\n"
      "v_mfma_f32_16x16x32_bf16 v[32:35], a[112:115], v[128:131], v[32:35]\n"
      "s_xor_b64 s[56:57], s[56:57], s[32:33]\n"
      "ds_read_b128 a[80:83], v242 offset:33792\n"
      "ds_read_b128 a[88:91], v243 offset:33792\n"
      "v_mfma_f32_16x16x32_bf16 v[36:39], a[112:115], v[132:135], v[36:39]\n"
      "s_bitset1_b32 s61, 15\n"
      "v_exp_f32 v189, v121\n"
      "v_mfma_f32_16x16x32_bf16 v[40:43], a[112:115], v[136:139], v[40:43]\n"
      "s_add_u32 s61, s61, 0x2000\n"
      "v_exp_f32 v190, v122\n"
      "v_mfma_f32_16x16x32_bf16 v[44:47], a[112:115], v[140:143], v[44:47]\n"
      "s_bitset0_b32 s61, 15\n"
      "v_exp_f32 v191, v123\n"
      "v_mfma_f32_16x16x32_bf16 v[60:63], a[116:119], v[140:143], v[60:63]\n"
      "s_xor_b32 s60, s61, s51\n"
      "v_cvt_pk_bf16_f32 v156, v188, v189\n"
      "v_cvt_pk_bf16_f32 v157, v190, v191\n"
      "v_mfma_f32_16x16x32_bf16 v[56:59], a[116:119], v[136:139], v[56:59]\n"
      "s_add_u32 s62, s62, 0x4000\n"
      "v_exp_f32 v192, v124\n"
      "v_mfma_f32_16x16x32_bf16 v[52:55], a[116:119], v[132:135], v[52:55]\n"
      "s_and_b32 s62, s62, 0x1ffff\n"
      "v_exp_f32 v193, v125\n"
      "v_mfma_f32_16x16x32_bf16 v[48:51], a[116:119], v[128:131], v[48:51]\n"
      "v_exp_f32 v194, v126\n"
      "v_mfma_f32_16x16x32_bf16 a[176:179], a[120:123], v[128:131], a[176:179]\n"
      "v_exp_f32 v195, v127\n"
      "v_mfma_f32_16x16x32_bf16 a[180:183], a[120:123], v[132:135], a[180:183]\n"
      "v_cvt_pk_bf16_f32 v158, v192, v193\n"
      "v_cvt_pk_bf16_f32 v159, v194, v195\n"
      "s_waitcnt lgkmcnt(0)\n"
      "v_mfma_f32_16x16x32_bf16 a[184:187], a[120:123], v[136:139], a[184:187]\n"
      "s_sub_u32 s67, s68, s66\n"
      "ds_read_b128 a[68:71], v242 offset:36864\n"
      "ds_read_b128 a[76:79], v243 offset:36864\n"
      "v_mfma_f32_16x16x32_bf16 v[184:187], a[140:143], v[156:159], v[184:187]\n"
      "s_cmp_gt_i32 s67, s70\n"
      "ds_read_b128 a[84:87], v242 offset:37888\n"
      "ds_read_b128 a[92:95], v243 offset:37888\n"
      "v_mfma_f32_16x16x32_bf16 a[188:191], a[120:123], v[140:143], a[188:191]\n"
      "s_cselect_b32 s69, s77, s86\n"
      "v_mfma_f32_16x16x32_bf16 a[204:207], a[124:127], v[140:143], a[204:207]\n"
      "s_add_u32 s66, s66, s69\n"
      "v_mfma_f32_16x16x32_bf16 a[200:203], a[124:127], v[136:139], a[200:203]\n"
      "s_add_u32 s67, s67, 50\n"
      "v_max3_f32 v196, v184, v185, v186\n"
      "v_max_f32 v196, v196, v187\n"
      "v_mfma_f32_16x16x32_bf16 a[196:199], a[124:127], v[132:135], a[196:199]\n"
      "s_min_i32 s70, s70, s67\n"
      "v_cmp_nge_f32 vcc, 0x4b800000, v196\n"
      "v_mfma_f32_16x16x32_bf16 a[192:195], a[124:127], v[128:131], a[192:195]\n"
      "s_max_i32 s70, s70, s80\n"
      "s_cbranch_vccnz FS1_%=\n"
      "FR1_%=:\n"
      "v_mfma_f32_16x16x32_bf16 v[96:99], a[64:67], a[0:3], v[160:163]\n"
      "s_sub_u32 s67, s68, s82\n"
      "v_exp_f32 v188, v64\n"
      "v_mfma_f32_16x16x32_bf16 v[96:99], a[72:75], a[16:19], v[96:99]\n"
      "s_min_i32 s76, s67, s83\n"
      "v_add_f32 v180, v180, v184\n"
      "v_add_f32 v181, v181, v185\n"
      "v_mfma_f32_16x16x32_bf16 v[96:99], a[80:83], a[32:35], v[96:99]\n"
      "s_mov_b32 s83, s67\n"
      "v_exp_f32 v189, v65\n"
      "v_mfma_f32_16x16x32_bf16 v[96:99], a[88:91], a[48:51], v[96:99]\n"
      "s_sub_u32 s76, s76, 14\n"
      "v_add_f32 v182, v182, v186\n"
      "v_add_f32 v183, v183, v187\n"
      "v_mfma_f32_16x16x32_bf16 v[104:107], a[64:67], a[4:7], v[164:167]\n"
      "s_min_i32 s76, s76, s87\n"
      "v_exp_f32 v190, v66\n"
      "v_mfma_f32_16x16x32_bf16 v[104:107], a[72:75], a[20:23], v[104:107]\n"
      "s_max_i32 s76, s76, s90\n"
      "v_exp_f32 v191, v67\n"
      "v_mfma_f32_16x16x32_bf16 v[104:107], a[80:83], a[36:39], v[104:107]\n"
      "s_mov_b32 s82, s68\n"
      "v_cvt_pk_bf16_f32 v128, v188, v189\n"
      "v_cvt_pk_bf16_f32 v129, v190, v191\n"
      "v_mfma_f32_16x16x32_bf16 v[104:107], a[88:91], a[52:55], v[104:107]\n"
      "s_add_u32 s65, s68, s76\n"
      "v_exp_f32 v192, v68\n"
      "v_mfma_f32_16x16x32_bf16 v[112:115], a[64:67], a[8:11], v[168:171]\n"
      "s_max_u32 s88, s66, s65\n"
      "v_exp_f32 v193, v69\n"
      "v_mfma_f32_16x16x32_bf16 v[112:115], a[72:75], a[24:27], v[112:115]\n"
      "s_memrealtime s[68:69]\n"
      "v_exp_f32 v194, v70\n"
      "v_mfma_f32_16x16x32_bf16 v[112:115], a[80:83], a[40:43], v[112:115]\n"
      "s_mov_b32 m0, s62\n"
      "v_exp_f32 v195, v71\n"
      "v_mfma_f32_16x16x32_bf16 v[112:115], a[88:91], a[56:59], v[112:115]\n"
      "buffer_load_dwordx4 v248, s[56:59], s60 offen nt lds\n"
      "v_mfma_f32_16x16x32_bf16 v[120:123], a[64:67], a[12:15], v[172:175]\n"
      "buffer_load_dwordx4 v248, s[56:59], s60 offen offset:2048 nt lds\n"
      "v_mfma_f32_16x16x32_bf16 v[120:123], a[72:75], a[28:31], v[120:123]\n"
      "s_bitset1_b32 m0, 10\n"
      "s_add_u32 s39, s39, s39\n"
      "v_cvt_pk_bf16_f32 v130, v192, v193\n"
      "v_mfma_f32_16x16x32_bf16 v[120:123], a[80:83], a[44:47], v[120:123]\n"
      "buffer_load_dwordx4 v249, s[56:59], s60 offen nt lds\n"
      "v_mfma_f32_16x16x32_bf16 v[120:123], a[88:91], a[60:63], v[120:123]\n"
      "buffer_load_dwordx4 v249, s[56:59], s60 offen offset:2048 nt lds\n"
      "s_cbranch_scc1 SP3_%=\n"
      "HD3_%=:\n"
      "s_cmp_lg_u32 s54, 0\n"
      "s_waitcnt vmcnt(12) lgkmcnt(0)\n"
      "s_cbranch_scc1 W3_%=\n"
      "s_waitcnt vmcnt(8)\n"
      "W3_%=:\n"
      "s_sub_u32 s67, s68, s88\n"
      "s_cmp_lt_i32 s67, -8\n"
      "s_cbranch_scc1 GW6_%=\n"
      "GX6_%=:\n"
      "s_barrier\n"
      "v_mfma_f32_16x16x32_bf16 v[100:103], a[68:71], a[0:3], v[160:163]\n"
      "s_memrealtime s[68:69]\n"
      "ds_read_b64_tr_b16 a[96:97], v244 offset:32768\n"
      "ds_read_b64_tr_b16 a[98:99], v244 offset:36864\n"
      "v_mfma_f32_16x16x32_bf16 v[100:103], a[76:79], a[16:19], v[100:103]\n"
      "s_xor_b64 s[56:57], s[56:57], s[32:33]\n"
      "ds_read_b64_tr_b16 a[100:101], v245 offset:32768\n"
      "ds_read_b64_tr_b16 a[102:103], v245 offset:36864\n"
      "v_mfma_f32_16x16x32_bf16 v[100:103], a[84:87], a[32:35], v[100:103]\n"
      "s_bitset1_b32 s61, 15\n"
      "ds_read_b64_tr_b16 a[104:105], v246 offset:32768\n"
      "ds_read_b64_tr_b16 a[106:107], v246 offset:36864\n"
      "v_mfma_f32_16x16x32_bf16 v[100:103], a[92:95], a[48:51], v[100:103]\n"
      "s_add_u32 s61, s61, 0x2000\n"
      "ds_read_b64_tr_b16 a[108:109], v247 offset:32768\n"
      "ds_read_b64_tr_b16 a[110:111], v247 offset:36864\n"
      "v_mfma_f32_16x16x32_bf16 v[108:111], a[68:71], a[4:7], v[164:167]\n"
      "s_bitset0_b32 s61, 15\n"
      "v_exp_f32 v188, v72\n"
      "v_mfma_f32_16x16x32_bf16 v[108:111], a[76:79], a[20:23], v[108:111]\n"
      "s_xor_b32 s60, s61, s51\n"
      "v_exp_f32 v189, v73\n"
      "v_mfma_f32_16x16x32_bf16 v[108:111], a[84:87], a[36:39], v[108:111]\n"
      "s_add_u32 s62, s62, 0x4000\n"
      "v_xor_b32 v242, 0x10000, v242\n"
      "v_xor_b32 v243, 0x10000, v243\n"
      "v_mfma_f32_16x16x32_bf16 v[108:111], a[92:95], a[52:55], v[108:111]\n"
      "s_and_b32 s62, s62, 0x1ffff\n"
      "v_exp_f32 v190, v74\n"
      "v_mfma_f32_16x16x32_bf16 v[116:119], a[68:71], a[8:11], v[168:171]\n"
      "v_cvt_pk_bf16_f32 v131, v194, v195\n"
      "v_cvt_pk_bf16_f32 v132, v188, v189\n"
      "v_mfma_f32_16x16x32_bf16 v[116:119], a[76:79], a[24:27], v[116:119]\n"
      "v_exp_f32 v191, v75\n"
      "v_mfma_f32_16x16x32_bf16 v[184:187], a[128:131], v[128:131], 0\n"
      "v_exp_f32 v192, v76\n"
      "s_waitcnt lgkmcnt(0)\n"
      "v_mfma_f32_16x16x32_bf16 v[116:119], a[84:87], a[40:43], v[116:119]\n"
      "s_sub_u32 s67, s68, s66\n"
      "ds_read_b64_tr_b16 a[112:113], v244 offset:33792\n"
      "v_cvt_pk_bf16_f32 v133, v190, v191\n"
      "v_mfma_f32_16x16x32_bf16 v[116:119], a[92:95], a[56:59], v[116:119]\n"
      "s_cmp_gt_i32 s67, s70\n"
      "v_exp_f32 v193, v77\n"
      "v_mfma_f32_16x16x32_bf16 v[124:127], a[68:71], a[12:15], v[172:175]\n"
      "s_cselect_b32 s69, s77, s86\n"
      "ds_read_b64_tr_b16 a[114:115], v244 offset:37888\n"
      "v_cvt_pk_bf16_f32 v134, v192, v193\n"
      "v_mfma_f32_16x16x32_bf16 v[124:127], a[76:79], a[28:31], v[124:127]\n"
      "s_add_u32 s66, s66, s69\n"
      "v_exp_f32 v194, v78\n"
      "v_mfma_f32_16x16x32_bf16 v[124:127], a[84:87], a[44:47], v[124:127]\n"
      "s_add_u32 s67, s67, 50\n"
      "ds_read_b64_tr_b16 a[116:117], v245 offset:33792\n"
      "ds_read_b64_tr_b16 a[118:119], v245 offset:37888\n"
      "v_mfma_f32_16x16x32_bf16 v[124:127], a[92:95], a[60:63], v[124:127]\n"
      "s_min_i32 s70, s70, s67\n"
      "v_exp_f32 v195, v79\n"
      "v_mfma_f32_16x16x32_bf16 v[0:3], a[96:99], v[144:147], v[0:3]\n"
      "s_max_i32 s70, s70, s80\n"
      "ds_read_b64_tr_b16 a[120:121], v246 offset:33792\n"
      "v_cvt_pk_bf16_f32 v135, v194, v195\n"
      "v_mfma_f32_16x16x32_bf16 v[4:7], a[96:99], v[148:151], v[4:7]\n"
      "s_sub_u32 s67, s68, s82\n"
      "v_exp_f32 v188, v80\n"
      "v_mfma_f32_16x16x32_bf16 v[184:187], a[132:135], v[132:135], v[184:187]\n"
      "s_min_i32 s76, s67, s83\n"
      "ds_read_b64_tr_b16 a[122:123], v246 offset:37888\n"
      "ds_read_b64_tr_b16 a[124:125], v247 offset:33792\n"
      "v_mfma_f32_16x16x32_bf16 v[8:11], a[96:99], v[152:155], v[8:11]\n"
      "s_mov_b32 s83, s67\n"
      "v_exp_f32 v189, v81\n"
      "v_mfma_f32_16x16x32_bf16 v[12:15], a[96:99], v[156:159], v[12:15]\n"
      "s_sub_u32 s76, s76, 14\n"
      "ds_read_b64_tr_b16 a[126:127], v247 offset:37888\n"
      "v_cvt_pk_bf16_f32 v136, v188, v189\n"
      "v_mfma_f32_16x16x32_bf16 v[28:31], a[100:103], v[156:159], v[28:31]\n"
      "s_min_i32 s76, s76, s87\n"
      "v_exp_f32 v190, v82\n"
      "v_mfma_f32_16x16x32_bf16 v[24:27], a[100:103], v[152:155], v[24:27]\n"
      "s_max_i32 s76, s76, s90\n"
      "v_exp_f32 v191, v83\n"
      "v_mfma_f32_16x16x32_bf16 v[20:23], a[100:103], v[148:151], v[20:23]\n"
      "s_mov_b32 s82, s68\n"
      "v_exp_f32 v192, v84\n"
      "v_mfma_f32_16x16x32_bf16 v[16:19], a[100:103], v[144:147], v[16:19]\n"
      "s_add_u32 s65, s68, s76\n"
      "v_exp_f32 v193, v85\n"
      "v_mfma_f32_16x16x32_bf16 a[144:147], a[104:107], v[144:147], a[144:147]\n"
      "s_max_u32 s88, s66, s65\n"
      "v_cvt_pk_bf16_f32 v137, v190, v191\n"
      "v_cvt_pk_bf16_f32 v138, v192, v193\n"
      "v_mfma_f32_16x16x32_bf16 a[148:151], a[104:107], v[148:151], a[148:151]\n"
      "s_memrealtime s[68:69]\n"
      "v_exp_f32 v194, v86\n"
      "v_mfma_f32_16x16x32_bf16 a[152:155], a[104:107], v[152:155], a[152:155]\n"
      "s_mov_b32 m0, s62\n"
      "v_exp_f32 v195, v87\n"
      "v_mfma_f32_16x16x32_bf16 a[156:159], a[104:107], v[156:159], a[156:159]\n"
      "buffer_load_dwordx4 v248, s[56:59], s60 offen nt lds\n"
      "v_mfma_f32_16x16x32_bf16 a[172:175], a[108:111], v[156:159], a[172:175]\n"
      "buffer_load_dwordx4 v248, s[56:59], s60 offen offset:2048 nt lds\n"
      "v_mfma_f32_16x16x32_bf16 a[168:171], a[108:111], v[152:155], a[168:171]\n"
      "s_bitset1_b32 m0, 10\n"
      "s_add_u32 s38, s38, s38\n"
      "v_cvt_pk_bf16_f32 v139, v194, v195\n"
      "v_mfma_f32_16x16x32_bf16 a[164:167], a[108:111], v[148:151], a[164:167]\n"
      "buffer_load_dwordx4 v249, s[56:59], s60 offen nt lds\n"
      "v_mfma_f32_16x16x32_bf16 v[184:187], a[136:139], v[136:139], v[184:187]\n"
      "buffer_load_dwordx4 v249, s[56:59], s60 offen offset:2048 nt lds\n"
      "HD0_%=:\n"
      "s_cmp_lg_u32 s54, 0\n"
      "s_waitcnt vmcnt(12) lgkmcnt(0)\n"
      "s_cbranch_scc1 W4_%=\n"
      "s_waitcnt vmcnt(8)\n"
      "W4_%=:\n"
      "s_sub_u32 s67, s68, s88\n"
      "s_cmp_lt_i32 s67, -8\n"
      "s_cbranch_scc1 GW7_%=\n"
      "GX7_%=:\n"
      "s_barrier\n"
      "v_mfma_f32_16x16x32_bf16 a[160:163], a[108:111], v[144:147], a[160:163]\n"
      "s_memrealtime s[68:69]\n"
      "ds_read_b128 a[64:67], v242 offset:0\n"
      "ds_read_b128 a[72:75], v243 offset:0\n"
      "v_mfma_f32_16x16x32_bf16 v[32:35], a[112:115], v[144:147], v[32:35]\n"
      "s_xor_b64 s[56:57], s[56:57], s[32:33]\n"
      "ds_read_b128 a[80:83], v242 offset:1024\n"
      "ds_read_b128 a[88:91], v243 offset:1024\n"
      "v_mfma_f32_16x16x32_bf16 v[36:39], a[112:115], v[148:151], v[36:39]\n"
      "s_bitset1_b32 s61, 15\n"
      "v_exp_f32 v188, v88\n"
      "v_mfma_f32_16x16x32_bf16 v[40:43], a[112:115], v[152:155], v[40:43]\n"
      "s_add_u32 s61, s61, 0x2000\n"
      "v_exp_f32 v189, v89\n"
      "v_mfma_f32_16x16x32_bf16 v[44:47], a[112:115], v[156:159], v[44:47]\n"
      "s_bitset0_b32 s61, 15\n"
      "v_exp_f32 v190, v90\n"
      "v_mfma_f32_16x16x32_bf16 v[60:63], a[116:119], v[156:159], v[60:63]\n"
      "s_xor_b32 s60, s61, s51\n"
      "v_exp_f32 v191, v91\n"
      "v_mfma_f32_16x16x32_bf16 v[56:59], a[116:119], v[152:155], v[56:59]\n"
      "s_add_u32 s62, s62, 0x4000\n"
      "v_cvt_pk_bf16_f32 v140, v188, v189\n"
      "v_cvt_pk_bf16_f32 v141, v190, v191\n"
      "v_mfma_f32_16x16x32_bf16 v[52:55], a[116:119], v[148:151], v[52:55]\n"
      "s_and_b32 s62, s62, 0x1ffff\n"
      "v_exp_f32 v192, v92\n"
      "v_mfma_f32_16x16x32_bf16 v[48:51], a[116:119], v[144:147], v[48:51]\n"
      "v_exp_f32 v193, v93\n"
      "v_mfma_f32_16x16x32_bf16 a[176:179], a[120:123], v[144:147], a[176:179]\n"
      "v_exp_f32 v194, v94\n"
      "v_mfma_f32_16x16x32_bf16 a[180:183], a[120:123], v[148:151], a[180:183]\n"
      "v_exp_f32 v195, v95\n"
      "s_waitcnt lgkmcnt(0)\n"
      "v_mfma_f32_16x16x32_bf16 a[184:187], a[120:123], v[152:155], a[184:187]\n"
      "s_sub_u32 s67, s68, s66\n"
      "v_cvt_pk_bf16_f32 v142, v192, v193\n"
      "v_cvt_pk_bf16_f32 v143, v194, v195\n"
      "v_mfma_f32_16x16x32_bf16 a[188:191], a[120:123], v[156:159], a[188:191]\n"
      "s_cmp_gt_i32 s67, s70\n"
      "ds_read_b128 a[68:71], v242 offset:4096\n"
      "ds_read_b128 a[76:79], v243 offset:4096\n"
      "v_mfma_f32_16x16x32_bf16 v[184:187], a[140:143], v[140:143], v[184:187]\n"
      "s_cselect_b32 s69, s77, s86\n"
      "ds_read_b128 a[84:87], v242 offset:5120\n"
      "ds_read_b128 a[92:95], v243 offset:5120\n"
      "v_mfma_f32_16x16x32_bf16 a[204:207], a[124:127], v[156:159], a[204:207]\n"
      "s_add_u32 s66, s66, s69\n"
      "v_xor_b32 v244, 0x10000, v244\n"
      "v_xor_b32 v245, 0x10000, v245\n"
      "v_mfma_f32_16x16x32_bf16 a[200:203], a[124:127], v[152:155], a[200:203]\n"
      "s_add_u32 s67, s67, 50\n"
      "v_xor_b32 v246, 0x10000, v246\n"
      "v_xor_b32 v247, 0x10000, v247\n"
      "v_mfma_f32_16x16x32_bf16 a[196:199], a[124:127], v[148:151], a[196:199]\n"
      "s_min_i32 s70, s70, s67\n"
      "v_max3_f32 v196, v184, v185, v186\n"
      "v_max_f32 v196, v196, v187\n"
      "v_mfma_f32_16x16x32_bf16 a[192:195], a[124:127], v[144:147], a[192:195]\n"
      "s_max_i32 s70, s70, s80\n"
      "v_cmp_nge_f32 vcc, 0x4b800000, v196\n"
      "s_cbranch_vccnz FS2_%=\n"
      "FR2_%=:\n"
      "s_branch LOOP_%=\n"
      "SP1_%=:\n"
      "s_ff1_i32_b32 s64, s39\n"
      "s_lshl1_add_u32 s64, s64, 1\n"
      "s_bitcmp1_b64 s[52:53], s64\n"
      "s_cbranch_scc0 SH1_%=\n"
      "s_swappc_b64 s[84:85], s[84:85]\n"
      "SH1_%=:\n"
      "s_cmp_lt_u32 s64, 63\n"
      "s_cbranch_scc1 HD1_%=\n"
      "s_xor_b64 s[56:57], s[56:57], s[32:33]\n"
      "s_bitset1_b32 s61, 15\n"
      "s_add_u32 s61, s61, 0x2000\n"
      "s_bitset0_b32 s61, 15\n"
      "s_xor_b32 s60, s61, s51\n"
      "s_add_u32 s62, s62, 0x4000\n"
      "s_and_b32 s62, s62, 0x1ffff\n"
      "v_add_u32 v248, s51, v249\n"
      "s_mov_b32 s51, 0\n"
      "v_xor_b32 v249, 0x4000, v248\n"
      "s_branch HD1_%=\n"
      "SP2_%=:\n"
      "s_cmp_eq_u32 s38, 0\n"
      "s_cbranch_scc1 L62_%=\n"
      "s_ff1_i32_b32 s64, s38\n"
      "s_lshl1_add_u32 s64, s64, 0\n"
      "s_bitcmp1_b64 s[52:53], s64\n"
      "s_cbranch_scc0 SH2_%=\n"
      "s_swappc_b64 s[84:85], s[84:85]\n"
      "SH2_%=:\n"
      "s_branch HD2_%=\n"
      "L62_%=:\n"
      "s_xor_b64 s[56:57], s[56:57], s[32:33]\n"
      "s_sub_u32 s61, s61, 0x2000\n"
      "v_xor_b32 v248, 128, v248\n"
      "v_xor_b32 v249, 128, v249\n"
      "s_mov_b32 s38, 0x80000000\n"
      "s_branch HD2_%=\n"
      "SP3_%=:\n"
      "s_cmp_eq_u32 s39, 0\n"
      "s_cbranch_scc1 NH_%=\n"
      "s_ff1_i32_b32 s64, s39\n"
      "s_lshl1_add_u32 s64, s64, 1\n"
      "s_bitcmp1_b64 s[52:53], s64\n"
      "s_cbranch_scc0 SH3_%=\n"
      "s_swappc_b64 s[84:85], s[84:85]\n"
      "SH3_%=:\n"
      "s_bitcmp1_b64 s[34:35], s64\n"
      "s_cbranch_scc0 HD3_%=\n"
      "NH_%=:\n"
      "s_cmp_lg_u32 s54, 0\n"
      "s_waitcnt vmcnt(12) lgkmcnt(0)\n"
      "s_cbranch_scc1 W5_%=\n"
      "s_waitcnt vmcnt(8)\n"
      "W5_%=:\n"
      "s_barrier\n"
      "v_mfma_f32_16x16x32_bf16 v[100:103], a[68:71], a[0:3], v[160:163]\n"
      "s_sub_u32 s54, s54, 1\n"
      "ds_read_b64_tr_b16 a[96:97], v244 offset:32768\n"
      "ds_read_b64_tr_b16 a[98:99], v244 offset:36864\n"
      "v_mfma_f32_16x16x32_bf16 v[100:103], a[76:79], a[16:19], v[100:103]\n"
      "ds_read_b64_tr_b16 a[100:101], v245 offset:32768\n"
      "ds_read_b64_tr_b16 a[102:103], v245 offset:36864\n"
      "v_mfma_f32_16x16x32_bf16 v[100:103], a[84:87], a[32:35], v[100:103]\n"
      "ds_read_b64_tr_b16 a[104:105], v246 offset:32768\n"
      "ds_read_b64_tr_b16 a[106:107], v246 offset:36864\n"
      "v_mfma_f32_16x16x32_bf16 v[100:103], a[92:95], a[48:51], v[100:103]\n"
      "ds_read_b64_tr_b16 a[108:109], v247 offset:32768\n"
      "ds_read_b64_tr_b16 a[110:111], v247 offset:36864\n"
      "v_mfma_f32_16x16x32_bf16 v[108:111], a[68:71], a[4:7], v[164:167]\n"
      "v_exp_f32 v188, v72\n"
      "v_mfma_f32_16x16x32_bf16 v[108:111], a[76:79], a[20:23], v[108:111]\n"
      "v_exp_f32 v189, v73\n"
      "v_mfma_f32_16x16x32_bf16 v[108:111], a[84:87], a[36:39], v[108:111]\n"
      "v_xor_b32 v242, 0x10000, v242\n"
      "v_xor_b32 v243, 0x10000, v243\n"
      "v_mfma_f32_16x16x32_bf16 v[108:111], a[92:95], a[52:55], v[108:111]\n"
      "v_exp_f32 v190, v74\n"
      "v_mfma_f32_16x16x32_bf16 v[116:119], a[68:71], a[8:11], v[168:171]\n"
      "v_cvt_pk_bf16_f32 v131, v194, v195\n"
      "v_cvt_pk_bf16_f32 v132, v188, v189\n"
      "v_mfma_f32_16x16x32_bf16 v[116:119], a[76:79], a[24:27], v[116:119]\n"
      "v_exp_f32 v191, v75\n"
      "v_mfma_f32_16x16x32_bf16 v[184:187], a[128:131], v[128:131], 0\n"
      "v_exp_f32 v192, v76\n"
      "s_waitcnt lgkmcnt(0)\n"
      "v_mfma_f32_16x16x32_bf16 v[116:119], a[84:87], a[40:43], v[116:119]\n"
      "ds_read_b64_tr_b16 a[112:113], v244 offset:33792\n"
      "v_cvt_pk_bf16_f32 v133, v190, v191\n"
      "v_mfma_f32_16x16x32_bf16 v[116:119], a[92:95], a[56:59], v[116:119]\n"
      "v_exp_f32 v193, v77\n"
      "v_mfma_f32_16x16x32_bf16 v[124:127], a[68:71], a[12:15], v[172:175]\n"
      "ds_read_b64_tr_b16 a[114:115], v244 offset:37888\n"
      "v_cvt_pk_bf16_f32 v134, v192, v193\n"
      "v_mfma_f32_16x16x32_bf16 v[124:127], a[76:79], a[28:31], v[124:127]\n"
      "v_exp_f32 v194, v78\n"
      "v_mfma_f32_16x16x32_bf16 v[124:127], a[84:87], a[44:47], v[124:127]\n"
      "ds_read_b64_tr_b16 a[116:117], v245 offset:33792\n"
      "ds_read_b64_tr_b16 a[118:119], v245 offset:37888\n"
      "v_mfma_f32_16x16x32_bf16 v[124:127], a[92:95], a[60:63], v[124:127]\n"
      "v_exp_f32 v195, v79\n"
      "v_mfma_f32_16x16x32_bf16 v[0:3], a[96:99], v[144:147], v[0:3]\n"
      "ds_read_b64_tr_b16 a[120:121], v246 offset:33792\n"
      "v_cvt_pk_bf16_f32 v135, v194, v195\n"
      "v_mfma_f32_16x16x32_bf16 v[4:7], a[96:99], v[148:151], v[4:7]\n"
      "v_exp_f32 v188, v80\n"
      "v_mfma_f32_16x16x32_bf16 v[184:187], a[132:135], v[132:135], v[184:187]\n"
      "ds_read_b64_tr_b16 a[122:123], v246 offset:37888\n"
      "ds_read_b64_tr_b16 a[124:125], v247 offset:33792\n"
      "v_mfma_f32_16x16x32_bf16 v[8:11], a[96:99], v[152:155], v[8:11]\n"
      "v_exp_f32 v189, v81\n"
      "v_mfma_f32_16x16x32_bf16 v[12:15], a[96:99], v[156:159], v[12:15]\n"
      "ds_read_b64_tr_b16 a[126:127], v247 offset:37888\n"
      "v_cvt_pk_bf16_f32 v136, v188, v189\n"
      "v_mfma_f32_16x16x32_bf16 v[28:31], a[100:103], v[156:159], v[28:31]\n"
      "v_exp_f32 v190, v82\n"
      "v_mfma_f32_16x16x32_bf16 v[24:27], a[100:103], v[152:155], v[24:27]\n"
      "v_exp_f32 v191, v83\n"
      "v_mfma_f32_16x16x32_bf16 v[20:23], a[100:103], v[148:151], v[20:23]\n"
      "v_exp_f32 v192, v84\n"
      "v_mfma_f32_16x16x32_bf16 v[16:19], a[100:103], v[144:147], v[16:19]\n"
      "v_exp_f32 v193, v85\n"
      "v_mfma_f32_16x16x32_bf16 a[144:147], a[104:107], v[144:147], a[144:147]\n"
      "v_cvt_pk_bf16_f32 v137, v190, v191\n"
      "v_cvt_pk_bf16_f32 v138, v192, v193\n"
      "v_mfma_f32_16x16x32_bf16 a[148:151], a[104:107], v[148:151], a[148:151]\n"
      "s_memrealtime s[68:69]\n"
      "v_exp_f32 v194, v86\n"
      "v_mfma_f32_16x16x32_bf16 a[152:155], a[104:107], v[152:155], a[152:155]\n"
      "v_exp_f32 v195, v87\n"
      "v_mfma_f32_16x16x32_bf16 a[156:159], a[104:107], v[156:159], a[156:159]\n"
      "s_nop 0\n"
      "v_mfma_f32_16x16x32_bf16 a[172:175], a[108:111], v[156:159], a[172:175]\n"
      "s_nop 0\n"
      "v_mfma_f32_16x16x32_bf16 a[168:171], a[108:111], v[152:155], a[168:171]\n"
      "s_add_u32 s38, s38, s38\n"
      "v_cvt_pk_bf16_f32 v139, v194, v195\n"
      "v_mfma_f32_16x16x32_bf16 a[164:167], a[108:111], v[148:151], a[164:167]\n"
      "s_nop 0\n"
      "v_mfma_f32_16x16x32_bf16 v[184:187], a[136:139], v[136:139], v[184:187]\n"
      "s_nop 0\n"
      "s_cbranch_scc0 MH_%=\n"
      "END_%=:\n"
      "v_xor_b32 v244, 0x10000, v244\n"
      "v_xor_b32 v245, 0x10000, v245\n"
      "v_xor_b32 v246, 0x10000, v246\n"
      "v_xor_b32 v247, 0x10000, v247\n"
      "v_xor_b32 v250, 0x2000, v244\n"
      "v_xor_b32 v251, 0x2000, v245\n"
      "s_waitcnt lgkmcnt(0)\n"
      "v_mfma_f32_16x16x32_bf16 a[160:163], a[108:111], v[144:147], a[160:163]\n"
      "s_mov_b32 s74, 0xffff0000\n"
      "v_exp_f32 v188, v88\n"
      "v_mfma_f32_16x16x32_bf16 v[32:35], a[112:115], v[144:147], v[32:35]\n"
      "s_mov_b32 s75, 0xffff0000\n"
      "v_exp_f32 v189, v89\n"
      "v_mfma_f32_16x16x32_bf16 v[36:39], a[112:115], v[148:151], v[36:39]\n"
      "s_mov_b32 s94, 0\n"
      "v_exp_f32 v190, v90\n"
      "v_mfma_f32_16x16x32_bf16 v[40:43], a[112:115], v[152:155], v[40:43]\n"
      "s_mov_b32 s95, -1\n"
      "v_exp_f32 v191, v91\n"
      "v_mfma_f32_16x16x32_bf16 v[44:47], a[112:115], v[156:159], v[44:47]\n"
      "s_add_u32 s40, s40, 0x7f4a7c15\n"
      "v_cvt_pk_bf16_f32 v140, v188, v189\n"
      "v_cvt_pk_bf16_f32 v141, v190, v191\n"
      "v_mfma_f32_16x16x32_bf16 v[60:63], a[116:119], v[156:159], v[60:63]\n"
      "v_exp_f32 v192, v92\n"
      "s_waitcnt vmcnt(4)\n"
      "s_barrier\n"
      "s_memrealtime s[68:69]\n"
      "v_mfma_f32_16x16x32_bf16 v[56:59], a[116:119], v[152:155], v[56:59]\n"
      "ds_read_b64_tr_b16 a[88:89], v246 offset:0\n"
      "ds_read_b64_tr_b16 a[90:91], v246 offset:4096\n"
      "v_mfma_f32_16x16x32_bf16 v[52:55], a[116:119], v[148:151], v[52:55]\n"
      "ds_read_b64_tr_b16 a[92:93], v247 offset:0\n"
      "ds_read_b64_tr_b16 a[94:95], v247 offset:4096\n"
      "v_mfma_f32_16x16x32_bf16 v[48:51], a[116:119], v[144:147], v[48:51]\n"
      "ds_read_b64_tr_b16 a[80:81], v244 offset:0\n"
      "ds_read_b64_tr_b16 a[82:83], v244 offset:4096\n"
      "v_mfma_f32_16x16x32_bf16 a[176:179], a[120:123], v[144:147], a[176:179]\n"
      "ds_read_b64_tr_b16 a[84:85], v245 offset:0\n"
      "ds_read_b64_tr_b16 a[86:87], v245 offset:4096\n"
      "v_mfma_f32_16x16x32_bf16 a[180:183], a[120:123], v[148:151], a[180:183]\n"
      "v_exp_f32 v193, v93\n"
      "v_mfma_f32_16x16x32_bf16 a[184:187], a[120:123], v[152:155], a[184:187]\n"
      "v_exp_f32 v194, v94\n"
      "v_mfma_f32_16x16x32_bf16 a[188:191], a[120:123], v[156:159], a[188:191]\n"
      "v_exp_f32 v195, v95\n"
      "v_mfma_f32_16x16x32_bf16 a[204:207], a[124:127], v[156:159], a[204:207]\n"
      "v_cvt_pk_bf16_f32 v142, v192, v193\n"
      "v_cvt_pk_bf16_f32 v143, v194, v195\n"
      "v_mfma_f32_16x16x32_bf16 a[200:203], a[124:127], v[152:155], a[200:203]\n"
      "v_mfma_f32_16x16x32_bf16 a[196:199], a[124:127], v[148:151], a[196:199]\n"
      "v_mfma_f32_16x16x32_bf16 v[184:187], a[140:143], v[140:143], v[184:187]\n"
      "v_mfma_f32_16x16x32_bf16 a[192:195], a[124:127], v[144:147], a[192:195]\n"
      "s_nop 6\n"
      "v_max3_f32 v196, v184, v185, v186\n"
      "v_max_f32 v196, v196, v187\n"
      "v_cmp_nge_f32 vcc, 0x4b800000, v196\n"
      "s_cbranch_vccnz FS3_%=\n"
      "FR3_%=:\n"
      "s_lshl_b32 s76, s43, 3\n"
      "s_waitcnt lgkmcnt(0)\n"
      "v_mov_b32 v236, s68\n"
      "v_mov_b32 v237, s40\n"
      "v_mov_b32 v238, s76\n"
      "s_mov_b64 exec, 1\n"
      "global_store_dwordx2 v238, v[236:237], s[46:47] offset:8 sc1\n"
      "s_mov_b64 exec, -1\n"
      "s_mov_b32 s82, s68\n"
      "v_xor_b32 v239, 8, v238\n"
      "v_mfma_f32_16x16x32_bf16 a[176:179], a[88:91], v[128:131], a[176:179]\n"
      "ds_read_b64_tr_b16 a[116:117], v245 offset:1024\n"
      "ds_read_b64_tr_b16 a[118:119], v245 offset:5120\n"
      "v_mfma_f32_16x16x32_bf16 a[180:183], a[88:91], v[132:135], a[180:183]\n"
      "v_add_f32 v180, v180, v184\n"
      "v_add_f32 v181, v181, v185\n"
      "v_mfma_f32_16x16x32_bf16 a[184:187], a[88:91], v[136:139], a[184:187]\n"
      "v_add_f32 v182, v182, v186\n"
      "v_add_f32 v183, v183, v187\n"
      "v_mfma_f32_16x16x32_bf16 a[188:191], a[88:91], v[140:143], a[188:191]\n"
      "v_exp_f32 v188, v96\n"
      "v_mfma_f32_16x16x32_bf16 a[204:207], a[92:95], v[140:143], a[204:207]\n"
      "v_exp_f32 v189, v97\n"
      "v_mfma_f32_16x16x32_bf16 a[200:203], a[92:95], v[136:139], a[200:203]\n"
      "v_exp_f32 v190, v98\n"
      "v_mfma_f32_16x16x32_bf16 a[196:199], a[92:95], v[132:135], a[196:199]\n"
      "ds_write_b128 v232, a[176:179] offset:0\n"
      "v_cvt_pk_bf16_f32 v144, v188, v189\n"
      "v_exp_f32 v191, v99\n"
      "v_mfma_f32_16x16x32_bf16 a[192:195], a[92:95], v[128:131], a[192:195]\n"
      "ds_read_b64_tr_b16 a[112:113], v244 offset:1024\n"
      "ds_read_b64_tr_b16 a[114:115], v244 offset:5120\n"
      "v_exp_f32 v192, v100\n"
      "v_cvt_pk_bf16_f32 v145, v190, v191\n"
      "v_exp_f32 v193, v101\n"
      "v_mfma_f32_16x16x32_bf16 v[32:35], a[80:83], v[128:131], v[32:35]\n"
      "ds_write_b128 v232, a[180:183] offset:1024\n"
      "v_exp_f32 v194, v102\n"
      "v_cvt_pk_bf16_f32 v146, v192, v193\n"
      "v_exp_f32 v195, v103\n"
      "v_exp_f32 v188, v104\n"
      "v_cvt_pk_bf16_f32 v147, v194, v195\n"
      "v_exp_f32 v189, v105\n"
      "v_mfma_f32_16x16x32_bf16 v[36:39], a[80:83], v[132:135], v[36:39]\n"
      "ds_write_b128 v234, v[144:147] offset:0\n"
      "v_exp_f32 v190, v106\n"
      "v_cvt_pk_bf16_f32 v148, v188, v189\n"
      "v_mfma_f32_16x16x32_bf16 v[184:187], a[128:131], v[144:147], 0\n"
      "ds_read_b64_tr_b16 a[124:125], v251 offset:1024\n"
      "ds_read_b64_tr_b16 a[126:127], v251 offset:5120\n"
      "v_exp_f32 v191, v107\n"
      "v_mfma_f32_16x16x32_bf16 v[40:43], a[80:83], v[136:139], v[40:43]\n"
      "ds_write_b128 v232, a[184:187] offset:2048\n"
      "v_exp_f32 v192, v108\n"
      "v_cvt_pk_bf16_f32 v149, v190, v191\n"
      "v_exp_f32 v193, v109\n"
      "v_exp_f32 v194, v110\n"
      "v_cvt_pk_bf16_f32 v150, v192, v193\n"
      "v_exp_f32 v195, v111\n"
      "v_mfma_f32_16x16x32_bf16 v[44:47], a[80:83], v[140:143], v[44:47]\n"
      "ds_write_b128 v232, a[188:191] offset:3072\n"
      "v_exp_f32 v188, v112\n"
      "v_cvt_pk_bf16_f32 v151, v194, v195\n"
      "v_exp_f32 v189, v113\n"
      "v_exp_f32 v190, v114\n"
      "v_mfma_f32_16x16x32_bf16 v[184:187], a[132:135], v[148:151], v[184:187]\n"
      "ds_read_b64_tr_b16 a[120:121], v250 offset:1024\n"
      "ds_read_b64_tr_b16 a[122:123], v250 offset:5120\n"
      "v_mfma_f32_16x16x32_bf16 v[60:63], a[84:87], v[140:143], v[60:63]\n"
      "ds_write_b128 v234, v[148:151] offset:1024\n"
      "v_cvt_pk_bf16_f32 v152, v188, v189\n"
      "v_exp_f32 v191, v115\n"
      "v_exp_f32 v192, v116\n"
      "v_cvt_pk_bf16_f32 v153, v190, v191\n"
      "v_exp_f32 v193, v117\n"
      "v_exp_f32 v194, v118\n"
      "v_mfma_f32_16x16x32_bf16 v[56:59], a[84:87], v[136:139], v[56:59]\n"
      "ds_write_b128 v232, a[204:207] offset:7168\n"
      "v_cvt_pk_bf16_f32 v154, v192, v193\n"
      "v_exp_f32 v195, v119\n"
      "v_exp_f32 v188, v120\n"
      "v_cvt_pk_bf16_f32 v155, v194, v195\n"
      "v_exp_f32 v189, v121\n"
      "v_exp_f32 v190, v122\n"
      "v_mfma_f32_16x16x32_bf16 v[184:187], a[136:139], v[152:155], v[184:187]\n"
      "ds_write_b128 v234, v[152:155] offset:2048\n"
      "v_cvt_pk_bf16_f32 v156, v188, v189\n"
      "v_exp_f32 v191, v123\n"
      "v_exp_f32 v192, v124\n"
      "v_cvt_pk_bf16_f32 v157, v190, v191\n"
      "v_exp_f32 v193, v125\n"
      "v_exp_f32 v194, v126\n"
      "v_mfma_f32_16x16x32_bf16 v[52:55], a[84:87], v[132:135], v[52:55]\n"
      "ds_write_b128 v232, a[200:203] offset:6144\n"
      "v_cvt_pk_bf16_f32 v158, v192, v193\n"
      "v_exp_f32 v195, v127\n"
      "v_mfma_f32_16x16x32_bf16 v[48:51], a[84:87], v[128:131], v[48:51]\n"
      "v_cvt_pk_bf16_f32 v159, v194, v195\n"
      "v_cndmask_b32 v229, v180, v181, s[74:75]\n"
      "v_cndmask_b32 v196, v182, v183, s[74:75]\n"
      "v_cndmask_b32 v229, v229, v196, s[94:95]\n"
      "v_mfma_f32_16x16x32_bf16 v[184:187], a[140:143], v[156:159], v[184:187]\n"
      "ds_write_b128 v234, v[156:159] offset:3072\n"
      "v_cndmask_b32 v228, v176, v177, s[74:75]\n"
      "v_cndmask_b32 v196, v178, v179, s[74:75]\n"
      "v_cndmask_b32 v228, v228, v196, s[94:95]\n"
      "s_nop 3\n"
      "v_max3_f32 v196, v184, v185, v186\n"
      "v_max_f32 v196, v196, v187\n"
      "v_cmp_nge_f32 vcc, 0x4b800000, v196\n"
      "s_cbranch_vccnz FS8_%=\n"
      "FR8_%=:\n"
      "s_waitcnt lgkmcnt(13)\n"
      "v_mfma_f32_16x16x32_bf16 v[48:51], a[116:119], v[144:147], v[48:51]\n"
      "ds_write_b128 v232, a[196:199] offset:5120\n"
      "v_cndmask_b32 v200, v184, v185, s[74:75]\n"
      "v_cndmask_b32 v196, v186, v187, s[74:75]\n"
      "v_cndmask_b32 v200, v200, v196, s[94:95]\n"
      "v_add_f32 v229, v229, v200\n"
      "v_mfma_f32_16x16x32_bf16 v[52:55], a[116:119], v[148:151], v[52:55]\n"
      "ds_write_b32 v234, v229 offset:4096\n"
      "v_mfma_f32_16x16x32_bf16 v[56:59], a[116:119], v[152:155], v[56:59]\n"
      "ds_write_b128 v232, a[192:195] offset:4096\n"
      "s_waitcnt lgkmcnt(0)\n"
      "s_barrier\n"
      "v_mfma_f32_16x16x32_bf16 v[60:63], a[116:119], v[156:159], v[60:63]\n"
      "ds_read_b32 v208, v209\n"
      "ds_read_b128 v[160:163], v235 offset:0\n"
      "ds_read_b128 v[164:167], v235 offset:1024\n"
      "v_mfma_f32_16x16x32_bf16 v[44:47], a[112:115], v[156:159], v[44:47]\n"
      "ds_read_b128 v[168:171], v235 offset:2048\n"
      "ds_read_b128 v[172:175], v235 offset:3072\n"
      "ds_read_b128 v[64:67], v233 offset:0\n"
      "v_mfma_f32_16x16x32_bf16 v[40:43], a[112:115], v[152:155], v[40:43]\n"
      "ds_read_b128 v[68:71], v233 offset:1024\n"
      "ds_read_b128 v[72:75], v233 offset:2048\n"
      "ds_read_b128 v[76:79], v233 offset:3072\n"
      "v_mfma_f32_16x16x32_bf16 v[36:39], a[112:115], v[148:151], v[36:39]\n"
      "ds_read_b128 v[80:83], v233 offset:4096\n"
      "ds_read_b128 v[84:87], v233 offset:5120\n"
      "ds_read_b128 v[88:91], v233 offset:6144\n"
      "v_mfma_f32_16x16x32_bf16 v[32:35], a[112:115], v[144:147], v[32:35]\n"
      "ds_read_b128 v[92:95], v233 offset:7168\n"
      "ds_read_b32 v204, v235 offset:4096\n"
      "s_waitcnt lgkmcnt(13)\n"
      "v_cmp_ne_u32 vcc, 0, v208\n"
      "s_cbranch_vccnz SLOW_%=\n"
      "AGAIN_%=:\n"
      "s_waitcnt lgkmcnt(12)\n"
      "v_mfma_f32_16x16x32_bf16 v[48:51], a[124:127], v[160:163], v[48:51]\n"
      "s_waitcnt lgkmcnt(11)\n"
      "v_mfma_f32_16x16x32_bf16 v[52:55], a[124:127], v[164:167], v[52:55]\n"
      "s_waitcnt lgkmcnt(10)\n"
      "v_mfma_f32_16x16x32_bf16 v[56:59], a[124:127], v[168:171], v[56:59]\n"
      "s_waitcnt lgkmcnt(9)\n"
      "v_mfma_f32_16x16x32_bf16 v[60:63], a[124:127], v[172:175], v[60:63]\n"
      "s_waitcnt lgkmcnt(8)\n"
      "v_add_f32 v32, v32, v64\n"
      "v_add_f32 v33, v33, v65\n"
      "v_add_f32 v34, v34, v66\n"
      "v_add_f32 v35, v35, v67\n"
      "s_waitcnt lgkmcnt(7)\n"
      "v_add_f32 v36, v36, v68\n"
      "v_add_f32 v37, v37, v69\n"
      "v_add_f32 v38, v38, v70\n"
      "v_add_f32 v39, v39, v71\n"
      "v_mfma_f32_16x16x32_bf16 v[32:35], a[120:123], v[160:163], v[32:35]\n"
      "s_waitcnt lgkmcnt(6)\n"
      "v_add_f32 v40, v40, v72\n"
      "v_add_f32 v41, v41, v73\n"
      "v_add_f32 v42, v42, v74\n"
      "v_add_f32 v43, v43, v75\n"
      "v_mfma_f32_16x16x32_bf16 v[36:39], a[120:123], v[164:167], v[36:39]\n"
      "s_waitcnt lgkmcnt(5)\n"
      "v_add_f32 v44, v44, v76\n"
      "v_add_f32 v45, v45, v77\n"
      "v_add_f32 v46, v46, v78\n"
      "v_add_f32 v47, v47, v79\n"
      "v_mfma_f32_16x16x32_bf16 v[40:43], a[120:123], v[168:171], v[40:43]\n"
      "s_waitcnt lgkmcnt(4)\n"
      "v_add_f32 v48, v48, v80\n"
      "v_add_f32 v49, v49, v81\n"
      "v_add_f32 v50, v50, v82\n"
      "v_add_f32 v51, v51, v83\n"
      "v_mfma_f32_16x16x32_bf16 v[44:47], a[120:123], v[172:175], v[44:47]\n"
      "v_cvt_pk_bf16_f32 v220, v48, v49\n"
      "v_cvt_pk_bf16_f32 v221, v50, v51\n"
      "s_waitcnt lgkmcnt(3)\n"
      "v_add_f32 v52, v52, v84\n"
      "v_add_f32 v53, v53, v85\n"
      "v_add_f32 v54, v54, v86\n"
      "v_add_f32 v55, v55, v87\n"
      "v_cvt_pk_bf16_f32 v222, v52, v53\n"
      "v_cvt_pk_bf16_f32 v223, v54, v55\n"
      "buffer_store_dwordx4 v[220:223], v241, s[24:27], s78 offen offset:2048 sc1\n"
      "s_waitcnt lgkmcnt(2)\n"
      "v_add_f32 v56, v56, v88\n"
      "v_add_f32 v57, v57, v89\n"
      "v_add_f32 v58, v58, v90\n"
      "v_add_f32 v59, v59, v91\n"
      "v_cvt_pk_bf16_f32 v224, v56, v57\n"
      "v_cvt_pk_bf16_f32 v225, v58, v59\n"
      "s_waitcnt lgkmcnt(1)\n"
      "v_add_f32 v60, v60, v92\n"
      "v_add_f32 v61, v61, v93\n"
      "v_add_f32 v62, v62, v94\n"
      "v_add_f32 v63, v63, v95\n"
      "v_cvt_pk_bf16_f32 v226, v60, v61\n"
      "v_cvt_pk_bf16_f32 v227, v62, v63\n"
      "buffer_store_dwordx4 v[224:227], v241, s[24:27], s78 offen offset:3072 sc1\n"
      "v_cvt_pk_bf16_f32 v212, v32, v33\n"
      "v_cvt_pk_bf16_f32 v213, v34, v35\n"
      "v_cvt_pk_bf16_f32 v214, v36, v37\n"
      "v_cvt_pk_bf16_f32 v215, v38, v39\n"
      "buffer_store_dwordx4 v[212:215], v241, s[24:27], s78 offen offset:0 sc1\n"
      "v_cvt_pk_bf16_f32 v216, v40, v41\n"
      "v_cvt_pk_bf16_f32 v217, v42, v43\n"
      "v_cvt_pk_bf16_f32 v218, v44, v45\n"
      "v_cvt_pk_bf16_f32 v219, v46, v47\n"
      "buffer_store_dwordx4 v[216:219], v241, s[24:27], s78 offen offset:1024 sc1\n"
      "s_waitcnt lgkmcnt(0)\n"
      "v_add_f32 v229, v229, v204\n"
      "v_add3_u32 v230, v228, v229, s40\n"
      "v_add3_u32 v230, v230, v212, v213\n"
      "v_add3_u32 v230, v230, v214, v215\n"
      "v_add3_u32 v230, v230, v216, v217\n"
      "v_add3_u32 v230, v230, v218, v219\n"
      "v_add3_u32 v230, v230, v220, v221\n"
      "v_add3_u32 v230, v230, v222, v223\n"
      "v_add3_u32 v230, v230, v224, v225\n"
      "v_add3_u32 v230, v230, v226, v227\n"
      "buffer_store_dwordx3 v[228:230], v240, s[24:27], s78 offen sc1\n"
      "global_load_dwordx2 v[206:207], v239, s[46:47] offset:8 sc1\n"
      "s_waitcnt vmcnt(7)\n"
      "s_barrier\n"
      "ds_read_b64_tr_b16 a[72:73], v246 offset:16384\n"
      "ds_read_b64_tr_b16 a[74:75], v246 offset:20480\n"
      "ds_read_b64_tr_b16 a[76:77], v247 offset:16384\n"
      "ds_read_b64_tr_b16 a[78:79], v247 offset:20480\n"
      "ds_read_b64_tr_b16 a[104:105], v246 offset:17408\n"
      "ds_read_b64_tr_b16 a[106:107], v246 offset:21504\n"
      "ds_read_b64_tr_b16 a[108:109], v247 offset:17408\n"
      "ds_read_b64_tr_b16 a[110:111], v247 offset:21504\n"
      "s_waitcnt lgkmcnt(4)\n"
      "v_mfma_f32_16x16x32_bf16 a[144:147], a[72:75], v[128:131], a[144:147]\n"
      "ds_read_b64_tr_b16 a[64:65], v244 offset:16384\n"
      "v_mov_b32 v198, 0\n"
      "v_mfma_f32_16x16x32_bf16 a[148:151], a[72:75], v[132:135], a[148:151]\n"
      "ds_read_b64_tr_b16 a[66:67], v244 offset:20480\n"
      "v_and_b32 v197, 0xf0, v241\n"
      "v_mfma_f32_16x16x32_bf16 a[152:155], a[72:75], v[136:139], a[152:155]\n"
      "ds_read_b64_tr_b16 a[68:69], v245 offset:16384\n"
      "v_lshrrev_b32 v196, 3, v241\n"
      "v_mfma_f32_16x16x32_bf16 a[156:159], a[72:75], v[140:143], a[156:159]\n"
      "ds_read_b64_tr_b16 a[70:71], v245 offset:20480\n"
      "v_and_b32 v196, 32, v196\n"
      "v_mfma_f32_16x16x32_bf16 a[172:175], a[76:79], v[140:143], a[172:175]\n"
      "ds_read_b64_tr_b16 a[96:97], v244 offset:17408\n"
      "v_lshl_or_b32 v196, v197, 4, v196\n"
      "v_mfma_f32_16x16x32_bf16 a[168:171], a[76:79], v[136:139], a[168:171]\n"
      "ds_read_b64_tr_b16 a[98:99], v244 offset:21504\n"
      "v_lshrrev_b32 v199, 5, v241\n"
      "v_mfma_f32_16x16x32_bf16 a[164:167], a[76:79], v[132:135], a[164:167]\n"
      "ds_read_b64_tr_b16 a[100:101], v245 offset:17408\n"
      "v_and_or_b32 v196, v199, 16, v196\n"
      "v_mfma_f32_16x16x32_bf16 a[160:163], a[76:79], v[128:131], a[160:163]\n"
      "ds_read_b64_tr_b16 a[102:103], v245 offset:21504\n"
      "v_mov_b32 v191, s40\n"
      "s_waitcnt lgkmcnt(8)\n"
      "v_mfma_f32_16x16x32_bf16 a[144:147], a[104:107], v[144:147], a[144:147]\n"
      "s_sub_u32 s76, 19456, s78\n"
      "v_mfma_f32_16x16x32_bf16 a[148:151], a[104:107], v[148:151], a[148:151]\n"
      "s_mov_b32 s63, 0x40000\n"
      "v_mfma_f32_16x16x32_bf16 a[152:155], a[104:107], v[152:155], a[152:155]\n"
      "v_mfma_f32_16x16x32_bf16 a[156:159], a[104:107], v[156:159], a[156:159]\n"
      "v_mfma_f32_16x16x32_bf16 a[172:175], a[108:111], v[156:159], a[172:175]\n"
      "v_mfma_f32_16x16x32_bf16 a[168:171], a[108:111], v[152:155], a[168:171]\n"
      "v_mfma_f32_16x16x32_bf16 a[164:167], a[108:111], v[148:151], a[164:167]\n"
      "v_mfma_f32_16x16x32_bf16 a[160:163], a[108:111], v[144:147], a[160:163]\n"
      "s_waitcnt lgkmcnt(0)\n"
      "v_mfma_f32_16x16x32_bf16 v[0:3], a[64:67], v[128:131], v[0:3]\n"
      "v_mfma_f32_16x16x32_bf16 v[4:7], a[64:67], v[132:135], v[4:7]\n"
      "v_mfma_f32_16x16x32_bf16 v[8:11], a[64:67], v[136:139], v[8:11]\n"
      "ds_write_b128 v232, a[144:147] offset:32768\n"
      "v_mfma_f32_16x16x32_bf16 v[12:15], a[64:67], v[140:143], v[12:15]\n"
      "ds_write_b128 v232, a[148:151] offset:33792\n"
      "v_mfma_f32_16x16x32_bf16 v[28:31], a[68:71], v[140:143], v[28:31]\n"
      "ds_write_b128 v232, a[152:155] offset:34816\n"
      "v_mfma_f32_16x16x32_bf16 v[24:27], a[68:71], v[136:139], v[24:27]\n"
      "ds_write_b128 v232, a[156:159] offset:35840\n"
      "v_mfma_f32_16x16x32_bf16 v[20:23], a[68:71], v[132:135], v[20:23]\n"
      "ds_write_b128 v232, a[172:175] offset:39936\n"
      "v_mfma_f32_16x16x32_bf16 v[16:19], a[68:71], v[128:131], v[16:19]\n"
      "ds_write_b128 v232, a[168:171] offset:38912\n"
      "v_mfma_f32_16x16x32_bf16 v[0:3], a[96:99], v[144:147], v[0:3]\n"
      "ds_write_b128 v232, a[164:167] offset:37888\n"
      "v_mfma_f32_16x16x32_bf16 v[4:7], a[96:99], v[148:151], v[4:7]\n"
      "ds_write_b128 v232, a[160:163] offset:36864\n"
      "v_mfma_f32_16x16x32_bf16 v[8:11], a[96:99], v[152:155], v[8:11]\n"
      "v_mfma_f32_16x16x32_bf16 v[12:15], a[96:99], v[156:159], v[12:15]\n"
      "v_mfma_f32_16x16x32_bf16 v[28:31], a[100:103], v[156:159], v[28:31]\n"
      "v_mfma_f32_16x16x32_bf16 v[24:27], a[100:103], v[152:155], v[24:27]\n"
      "v_mfma_f32_16x16x32_bf16 v[20:23], a[100:103], v[148:151], v[20:23]\n"
      "v_mfma_f32_16x16x32_bf16 v[16:19], a[100:103], v[144:147], v[16:19]\n"
      "s_waitcnt lgkmcnt(0)\n"
      "s_barrier\n"
      "ds_read_b128 v[64:67], v233 offset:32768\n"
      "ds_read_b128 v[68:71], v233 offset:33792\n"
      "ds_read_b128 v[72:75], v233 offset:34816\n"
      "ds_read_b128 v[76:79], v233 offset:35840\n"
      "ds_read_b128 v[80:83], v233 offset:36864\n"
      "ds_read_b128 v[84:87], v233 offset:37888\n"
      "ds_read_b128 v[88:91], v233 offset:38912\n"
      "ds_read_b128 v[92:95], v233 offset:39936\n"
      "s_waitcnt vmcnt(0)\n"
      "v_subrev_u32 v205, s82, v206\n"
      "v_cmp_ne_u32 vcc, s40, v207\n"
      "s_cbranch_vccnz LAG_%=\n"
      "v_cmp_lt_i32 vcc, 16, v205\n"
      "s_cbranch_vccnz LAG_%=\n"
      "READ_%=:\n"
      "buffer_load_dwordx3 v[128:130], v240, s[24:27], s76 offen sc1\n"
      "buffer_load_dwordx4 v[104:107], v241, s[24:27], s76 offen offset:2048 sc1\n"
      "buffer_load_dwordx4 v[108:111], v241, s[24:27], s76 offen offset:3072 sc1\n"
      "buffer_load_dwordx4 v[96:99], v241, s[24:27], s76 offen offset:0 sc1\n"
      "buffer_load_dwordx4 v[100:103], v241, s[24:27], s76 offen offset:1024 sc1\n"
      "s_waitcnt lgkmcnt(6)\n"
      "v_pk_add_f32 v[0:1], v[0:1], v[64:65]\n"
      "v_pk_add_f32 v[2:3], v[2:3], v[66:67]\n"
      "v_pk_add_f32 v[4:5], v[4:5], v[68:69]\n"
      "v_pk_add_f32 v[6:7], v[6:7], v[70:71]\n"
      "s_waitcnt lgkmcnt(4)\n"
      "v_pk_add_f32 v[8:9], v[8:9], v[72:73]\n"
      "v_pk_add_f32 v[10:11], v[10:11], v[74:75]\n"
      "v_pk_add_f32 v[12:13], v[12:13], v[76:77]\n"
      "v_pk_add_f32 v[14:15], v[14:15], v[78:79]\n"
      "s_waitcnt lgkmcnt(2)\n"
      "v_pk_add_f32 v[16:17], v[16:17], v[80:81]\n"
      "v_pk_add_f32 v[18:19], v[18:19], v[82:83]\n"
      "v_pk_add_f32 v[20:21], v[20:21], v[84:85]\n"
      "v_pk_add_f32 v[22:23], v[22:23], v[86:87]\n"
      "s_waitcnt lgkmcnt(0)\n"
      "v_pk_add_f32 v[24:25], v[24:25], v[88:89]\n"
      "v_pk_add_f32 v[26:27], v[26:27], v[90:91]\n"
      "v_pk_add_f32 v[28:29], v[28:29], v[92:93]\n"
      "v_pk_add_f32 v[30:31], v[30:31], v[94:95]\n"
      "TRY_%=:\n"
      "s_waitcnt vmcnt(4)\n"
      "v_sub_f32 v136, v128, v228\n"
      "v_add3_u32 v230, v128, v129, s40\n"
      "v_min_f32 v140, 0, -v136\n"
      "v_min_f32 v136, 0, v136\n"
      "v_exp_f32 v136, v136\n"
      "v_exp_f32 v140, v140\n"
      "v_mul_f32 v131, v229, v136\n"
      "v_fmac_f32 v131, v129, v140\n"
      "v_rcp_f32 v131, v131\n"
      "s_nop 0\n"
      "v_mul_f32 v136, v136, v131\n"
      "v_mul_f32 v140, v140, v131\n"
      "v_mov_b32 v137, v136\n"
      "v_mov_b32 v141, v140\n"
      "s_nop 0\n"
      "v_permlane16_swap_b32 v136, v137\n"
      "v_permlane16_swap_b32 v140, v141\n"
      "v_mov_b32 v138, v136\n"
      "v_mov_b32 v139, v137\n"
      "v_mov_b32 v142, v140\n"
      "v_mov_b32 v143, v141\n"
      "v_permlane32_swap_b32 v136, v138\n"
      "v_permlane32_swap_b32 v137, v139\n"
      "v_permlane32_swap_b32 v140, v142\n"
      "v_permlane32_swap_b32 v141, v143\n"
      "s_waitcnt vmcnt(3)\n"
      "v_add3_u32 v230, v230, v104, v105\n"
      "v_add3_u32 v230, v230, v106, v107\n"
      "v_lshlrev_b32 v48, 16, v104\n"
      "v_and_b32 v49, 0xffff0000, v104\n"
      "v_lshlrev_b32 v50, 16, v105\n"
      "v_and_b32 v51, 0xffff0000, v105\n"
      "v_lshlrev_b32 v52, 16, v106\n"
      "v_and_b32 v53, 0xffff0000, v106\n"
      "v_lshlrev_b32 v54, 16, v107\n"
      "v_and_b32 v55, 0xffff0000, v107\n"
      "s_waitcnt vmcnt(2)\n"
      "v_add3_u32 v230, v230, v108, v109\n"
      "v_add3_u32 v230, v230, v110, v111\n"
      "v_lshlrev_b32 v56, 16, v108\n"
      "v_and_b32 v57, 0xffff0000, v108\n"
      "v_lshlrev_b32 v58, 16, v109\n"
      "v_and_b32 v59, 0xffff0000, v109\n"
      "v_lshlrev_b32 v60, 16, v110\n"
      "v_and_b32 v61, 0xffff0000, v110\n"
      "v_lshlrev_b32 v62, 16, v111\n"
      "v_and_b32 v63, 0xffff0000, v111\n"
      "s_waitcnt vmcnt(1)\n"
      "v_add3_u32 v230, v230, v96, v97\n"
      "v_add3_u32 v230, v230, v98, v99\n"
      "v_lshlrev_b32 v32, 16, v96\n"
      "v_and_b32 v33, 0xffff0000, v96\n"
      "v_lshlrev_b32 v34, 16, v97\n"
      "v_and_b32 v35, 0xffff0000, v97\n"
      "v_lshlrev_b32 v36, 16, v98\n"
      "v_and_b32 v37, 0xffff0000, v98\n"
      "v_lshlrev_b32 v38, 16, v99\n"
      "v_and_b32 v39, 0xffff0000, v99\n"
      "s_waitcnt vmcnt(0)\n"
      "v_add3_u32 v230, v230, v100, v101\n"
      "v_add3_u32 v230, v230, v102, v103\n"
      "v_cmp_ne_u32 vcc, v230, v130\n"
      "s_cbranch_vccnz RETRY_%=\n"
      "GOT_%=:\n"
      "v_pk_mul_f32 v[0:1], v[0:1], v[136:137] op_sel:[0,0] op_sel_hi:[1,0]\n"
      "v_pk_mul_f32 v[2:3], v[2:3], v[136:137] op_sel:[0,0] op_sel_hi:[1,0]\n"
      "v_pk_mul_f32 v[16:17], v[16:17], v[136:137] op_sel:[0,0] op_sel_hi:[1,0]\n"
      "v_pk_mul_f32 v[18:19], v[18:19], v[136:137] op_sel:[0,0] op_sel_hi:[1,0]\n"
      "v_pk_fma_f32 v[0:1], v[32:33], v[140:141], v[0:1] op_sel:[0,0,0] op_sel_hi:[1,0,1]\n"
      "v_pk_fma_f32 v[2:3], v[34:35], v[140:141], v[2:3] op_sel:[0,0,0] op_sel_hi:[1,0,1]\n"
      "v_pk_fma_f32 v[16:17], v[48:49], v[140:141], v[16:17] op_sel:[0,0,0] op_sel_hi:[1,0,1]\n"
      "v_pk_fma_f32 v[18:19], v[50:51], v[140:141], v[18:19] op_sel:[0,0,0] op_sel_hi:[1,0,1]\n"
      "v_cvt_pk_bf16_f32 v112, v0, v1\n"
      "v_cvt_pk_bf16_f32 v113, v2, v3\n"
      "v_cvt_pk_bf16_f32 v114, v16, v17\n"
      "v_cvt_pk_bf16_f32 v115, v18, v19\n"
      "v_add_u32 v192, 0, v196\n"
      "v_permlane16_swap_b32 v112, v114\n"
      "v_permlane16_swap_b32 v113, v115\n"
      "global_store_dwordx4 v192, v[112:115], s[30:31] sc1\n"
      "v_pk_mul_f32 v[4:5], v[4:5], v[136:137] op_sel:[0,1] op_sel_hi:[1,1]\n"
      "v_pk_mul_f32 v[6:7], v[6:7], v[136:137] op_sel:[0,1] op_sel_hi:[1,1]\n"
      "v_pk_mul_f32 v[20:21], v[20:21], v[136:137] op_sel:[0,1] op_sel_hi:[1,1]\n"
      "v_pk_mul_f32 v[22:23], v[22:23], v[136:137] op_sel:[0,1] op_sel_hi:[1,1]\n"
      "v_pk_fma_f32 v[4:5], v[36:37], v[140:141], v[4:5] op_sel:[0,1,0] op_sel_hi:[1,1,1]\n"
      "v_pk_fma_f32 v[6:7], v[38:39], v[140:141], v[6:7] op_sel:[0,1,0] op_sel_hi:[1,1,1]\n"
      "v_pk_fma_f32 v[20:21], v[52:53], v[140:141], v[20:21] op_sel:[0,1,0] op_sel_hi:[1,1,1]\n"
      "v_pk_fma_f32 v[22:23], v[54:55], v[140:141], v[22:23] op_sel:[0,1,0] op_sel_hi:[1,1,1]\n"
      "v_cvt_pk_bf16_f32 v116, v4, v5\n"
      "v_cvt_pk_bf16_f32 v117, v6, v7\n"
      "v_cvt_pk_bf16_f32 v118, v20, v21\n"
      "v_cvt_pk_bf16_f32 v119, v22, v23\n"
      "v_add_u32 v193, 4096, v196\n"
      "v_permlane16_swap_b32 v116, v118\n"
      "v_permlane16_swap_b32 v117, v119\n"
      "global_store_dwordx4 v193, v[116:119], s[30:31] sc1\n"
      "v_lshlrev_b32 v40, 16, v100\n"
      "v_and_b32 v41, 0xffff0000, v100\n"
      "v_lshlrev_b32 v42, 16, v101\n"
      "v_and_b32 v43, 0xffff0000, v101\n"
      "v_lshlrev_b32 v44, 16, v102\n"
      "v_and_b32 v45, 0xffff0000, v102\n"
      "v_lshlrev_b32 v46, 16, v103\n"
      "v_and_b32 v47, 0xffff0000, v103\n"
      "v_pk_mul_f32 v[8:9], v[8:9], v[138:139] op_sel:[0,0] op_sel_hi:[1,0]\n"
      "v_pk_mul_f32 v[10:11], v[10:11], v[138:139] op_sel:[0,0] op_sel_hi:[1,0]\n"
      "v_pk_mul_f32 v[24:25], v[24:25], v[138:139] op_sel:[0,0] op_sel_hi:[1,0]\n"
      "v_pk_mul_f32 v[26:27], v[26:27], v[138:139] op_sel:[0,0] op_sel_hi:[1,0]\n"
      "v_pk_fma_f32 v[8:9], v[40:41], v[142:143], v[8:9] op_sel:[0,0,0] op_sel_hi:[1,0,1]\n"
      "v_pk_fma_f32 v[10:11], v[42:43], v[142:143], v[10:11] op_sel:[0,0,0] op_sel_hi:[1,0,1]\n"
      "v_pk_fma_f32 v[24:25], v[56:57], v[142:143], v[24:25] op_sel:[0,0,0] op_sel_hi:[1,0,1]\n"
      "v_pk_fma_f32 v[26:27], v[58:59], v[142:143], v[26:27] op_sel:[0,0,0] op_sel_hi:[1,0,1]\n"
      "v_cvt_pk_bf16_f32 v120, v8, v9\n"
      "v_cvt_pk_bf16_f32 v121, v10, v11\n"
      "v_cvt_pk_bf16_f32 v122, v24, v25\n"
      "v_cvt_pk_bf16_f32 v123, v26, v27\n"
      "v_add_u32 v192, 8192, v196\n"
      "v_permlane16_swap_b32 v120, v122\n"
      "v_permlane16_swap_b32 v121, v123\n"
      "global_store_dwordx4 v192, v[120:123], s[30:31] sc1\n"
      "v_pk_mul_f32 v[12:13], v[12:13], v[138:139] op_sel:[0,1] op_sel_hi:[1,1]\n"
      "v_pk_mul_f32 v[14:15], v[14:15], v[138:139] op_sel:[0,1] op_sel_hi:[1,1]\n"
      "v_pk_mul_f32 v[28:29], v[28:29], v[138:139] op_sel:[0,1] op_sel_hi:[1,1]\n"
      "v_pk_mul_f32 v[30:31], v[30:31], v[138:139] op_sel:[0,1] op_sel_hi:[1,1]\n"
      "v_pk_fma_f32 v[12:13], v[44:45], v[142:143], v[12:13] op_sel:[0,1,0] op_sel_hi:[1,1,1]\n"
      "v_pk_fma_f32 v[14:15], v[46:47], v[142:143], v[14:15] op_sel:[0,1,0] op_sel_hi:[1,1,1]\n"
      "v_pk_fma_f32 v[28:29], v[60:61], v[142:143], v[28:29] op_sel:[0,1,0] op_sel_hi:[1,1,1]\n"
      "v_pk_fma_f32 v[30:31], v[62:63], v[142:143], v[30:31] op_sel:[0,1,0] op_sel_hi:[1,1,1]\n"
      "v_cvt_pk_bf16_f32 v124, v12, v13\n"
      "v_cvt_pk_bf16_f32 v125, v14, v15\n"
      "v_cvt_pk_bf16_f32 v126, v28, v29\n"
      "v_cvt_pk_bf16_f32 v127, v30, v31\n"
      "v_add_u32 v193, 12288, v196\n"
      "v_permlane16_swap_b32 v124, v126\n"
      "v_permlane16_swap_b32 v125, v127\n"
      "global_store_dwordx4 v193, v[124:127], s[30:31] sc1\n"
      "s_mov_b64 exec, 1\n"
      "global_store_dword v198, v191, s[46:47] sc1\n"
      "s_mov_b64 exec, -1\n"
      "ENDE_%=:\n"
      ".subsection 1\n"
      "MH_%=:\n"
      "s_cmp_lg_u32 s54, 0\n"
      "s_waitcnt vmcnt(12) lgkmcnt(0)\n"
      "s_cbranch_scc1 W6_%=\n"
      "s_waitcnt vmcnt(8)\n"
      "W6_%=:\n"
      "s_barrier\n"
      "v_mfma_f32_16x16x32_bf16 a[160:163], a[108:111], v[144:147], a[160:163]\n"
      "s_xor_b64 s[56:57], s[56:57], s[32:33]\n"
      "ds_read_b128 a[64:67], v242 offset:0\n"
      "ds_read_b128 a[72:75], v243 offset:0\n"
      "v_mfma_f32_16x16x32_bf16 v[32:35], a[112:115], v[144:147], v[32:35]\n"
      "s_bitset1_b32 s61, 15\n"
      "ds_read_b128 a[80:83], v242 offset:1024\n"
      "ds_read_b128 a[88:91], v243 offset:1024\n"
      "v_mfma_f32_16x16x32_bf16 v[36:39], a[112:115], v[148:151], v[36:39]\n"
      "s_add_u32 s61, s61, 0x2000\n"
      "v_exp_f32 v188, v88\n"
      "v_mfma_f32_16x16x32_bf16 v[40:43], a[112:115], v[152:155], v[40:43]\n"
      "s_bitset0_b32 s61, 15\n"
      "v_exp_f32 v189, v89\n"
      "v_mfma_f32_16x16x32_bf16 v[44:47], a[112:115], v[156:159], v[44:47]\n"
      "s_xor_b32 s60, s61, s51\n"
      "v_exp_f32 v190, v90\n"
      "v_mfma_f32_16x16x32_bf16 v[60:63], a[116:119], v[156:159], v[60:63]\n"
      "s_add_u32 s62, s62, 0x4000\n"
      "v_exp_f32 v191, v91\n"
      "v_mfma_f32_16x16x32_bf16 v[56:59], a[116:119], v[152:155], v[56:59]\n"
      "s_and_b32 s62, s62, 0x1ffff\n"
      "v_cvt_pk_bf16_f32 v140, v188, v189\n"
      "v_cvt_pk_bf16_f32 v141, v190, v191\n"
      "v_mfma_f32_16x16x32_bf16 v[52:55], a[116:119], v[148:151], v[52:55]\n"
      "s_add_u32 s66, s66, s86\n"
      "v_exp_f32 v192, v92\n"
      "v_mfma_f32_16x16x32_bf16 v[48:51], a[116:119], v[144:147], v[48:51]\n"
      "s_add_u32 s82, s82, s86\n"
      "v_exp_f32 v193, v93\n"
      "v_mfma_f32_16x16x32_bf16 a[176:179], a[120:123], v[144:147], a[176:179]\n"
      "s_add_u32 s65, s65, s86\n"
      "v_exp_f32 v194, v94\n"
      "v_mfma_f32_16x16x32_bf16 a[180:183], a[120:123], v[148:151], a[180:183]\n"
      "s_max_u32 s88, s66, s65\n"
      "v_exp_f32 v195, v95\n"
      "s_waitcnt lgkmcnt(0)\n"
      "v_mfma_f32_16x16x32_bf16 a[184:187], a[120:123], v[152:155], a[184:187]\n"
      "v_cvt_pk_bf16_f32 v142, v192, v193\n"
      "v_cvt_pk_bf16_f32 v143, v194, v195\n"
      "v_mfma_f32_16x16x32_bf16 a[188:191], a[120:123], v[156:159], a[188:191]\n"
      "s_mov_b32 m0, s62\n"
      "ds_read_b128 a[68:71], v242 offset:4096\n"
      "ds_read_b128 a[76:79], v243 offset:4096\n"
      "v_mfma_f32_16x16x32_bf16 v[184:187], a[140:143], v[140:143], v[184:187]\n"
      "buffer_load_dwordx4 v248, s[56:59], s60 offen nt lds\n"
      "ds_read_b128 a[84:87], v242 offset:5120\n"
      "ds_read_b128 a[92:95], v243 offset:5120\n"
      "v_mfma_f32_16x16x32_bf16 a[204:207], a[124:127], v[156:159], a[204:207]\n"
      "buffer_load_dwordx4 v248, s[56:59], s60 offen offset:2048 nt lds\n"
      "v_xor_b32 v244, 0x10000, v244\n"
      "v_xor_b32 v245, 0x10000, v245\n"
      "v_mfma_f32_16x16x32_bf16 a[200:203], a[124:127], v[152:155], a[200:203]\n"
      "s_bitset1_b32 m0, 10\n"
      "v_xor_b32 v246, 0x10000, v246\n"
      "v_xor_b32 v247, 0x10000, v247\n"
      "v_mfma_f32_16x16x32_bf16 a[196:199], a[124:127], v[148:151], a[196:199]\n"
      "buffer_load_dwordx4 v249, s[56:59], s60 offen nt lds\n"
      "v_max3_f32 v196, v184, v185, v186\n"
      "v_max_f32 v196, v196, v187\n"
      "v_mfma_f32_16x16x32_bf16 a[192:195], a[124:127], v[144:147], a[192:195]\n"
      "buffer_load_dwordx4 v249, s[56:59], s60 offen offset:2048 nt lds\n"
      "v_cmp_nge_f32 vcc, 0x4b800000, v196\n"
      "s_cbranch_vccnz FS11_%=\n"
      "FR11_%=:\n"
      "v_mfma_f32_16x16x32_bf16 v[64:67], a[64:67], a[0:3], v[160:163]\n"
      "v_exp_f32 v188, v96\n"
      "v_mfma_f32_16x16x32_bf16 v[64:67], a[72:75], a[16:19], v[64:67]\n"
      "v_add_f32 v180, v180, v184\n"
      "v_add_f32 v181, v181, v185\n"
      "v_mfma_f32_16x16x32_bf16 v[64:67], a[80:83], a[32:35], v[64:67]\n"
      "v_exp_f32 v189, v97\n"
      "v_mfma_f32_16x16x32_bf16 v[64:67], a[88:91], a[48:51], v[64:67]\n"
      "v_add_f32 v182, v182, v186\n"
      "v_add_f32 v183, v183, v187\n"
      "v_mfma_f32_16x16x32_bf16 v[72:75], a[64:67], a[4:7], v[164:167]\n"
      "v_exp_f32 v190, v98\n"
      "v_mfma_f32_16x16x32_bf16 v[72:75], a[72:75], a[20:23], v[72:75]\n"
      "v_exp_f32 v191, v99\n"
      "v_mfma_f32_16x16x32_bf16 v[72:75], a[80:83], a[36:39], v[72:75]\n"
      "v_cvt_pk_bf16_f32 v144, v188, v189\n"
      "v_cvt_pk_bf16_f32 v145, v190, v191\n"
      "v_mfma_f32_16x16x32_bf16 v[72:75], a[88:91], a[52:55], v[72:75]\n"
      "v_exp_f32 v192, v100\n"
      "v_mfma_f32_16x16x32_bf16 v[80:83], a[64:67], a[8:11], v[168:171]\n"
      "v_exp_f32 v193, v101\n"
      "v_mfma_f32_16x16x32_bf16 v[80:83], a[72:75], a[24:27], v[80:83]\n"
      "s_memrealtime s[68:69]\n"
      "v_exp_f32 v194, v102\n"
      "v_mfma_f32_16x16x32_bf16 v[80:83], a[80:83], a[40:43], v[80:83]\n"
      "v_exp_f32 v195, v103\n"
      "v_mfma_f32_16x16x32_bf16 v[80:83], a[88:91], a[56:59], v[80:83]\n"
      "s_nop 0\n"
      "v_mfma_f32_16x16x32_bf16 v[88:91], a[64:67], a[12:15], v[172:175]\n"
      "s_nop 0\n"
      "v_mfma_f32_16x16x32_bf16 v[88:91], a[72:75], a[28:31], v[88:91]\n"
      "s_add_u32 s39, s39, s39\n"
      "v_cvt_pk_bf16_f32 v146, v192, v193\n"
      "v_mfma_f32_16x16x32_bf16 v[88:91], a[80:83], a[44:47], v[88:91]\n"
      "s_nop 0\n"
      "v_mfma_f32_16x16x32_bf16 v[88:91], a[88:91], a[60:63], v[88:91]\n"
      "s_nop 0\n"
      "s_cbranch_scc1 SP1_%=\n"
      "s_branch HD1_%=\n"
      "HOOK_%=:\n"
      "buffer_load_dword v250, v251, s[20:23], 0 offen sc1\n"
      "s_swappc_b64 s[84:85], s[84:85]\n"
      "s_waitcnt vmcnt(12)\n"
      "v_subrev_u32 v250, s71, v250\n"
      "v_min_u32 v250, 0x258, v250\n"
      "s_nop 1\n"
      "v_add_u32_dpp v250, v250, v250 quad_perm:[1,0,3,2] row_mask:0xf bank_mask:0xf\n"
      "s_nop 1\n"
      "v_add_u32_dpp v250, v250, v250 quad_perm:[2,3,0,1] row_mask:0xf bank_mask:0xf\n"
      "s_nop 1\n"
      "v_add_u32_dpp v250, v250, v250 row_half_mirror row_mask:0xf bank_mask:0xf\n"
      "s_nop 0\n"
      "v_readfirstlane_b32 s67, v250\n"
      "s_sub_u32 s66, s66, s81\n"
      "s_lshr_b32 s67, s67, 3\n"
      "s_add_u32 s66, s66, s67\n"
      "s_max_u32 s88, s66, s65\n"
      "s_mov_b32 s86, 57\n"
      "s_mov_b32 s77, 171\n"
      "s_swappc_b64 s[84:85], s[84:85]\n"
      "s_mov_b32 s89, 0\n"
      "s_swappc_b64 s[84:85], s[84:85]\n"
      "s_mov_b32 s86, 55\n"
      "s_mov_b32 s77, 165\n"
      "s_swappc_b64 s[84:85], s[84:85]\n"
      "s_cmp_lt_u32 s89, 1\n"
      "s_cselect_b32 s67, 2048, s72\n"
      "s_mov_b32 s89, 0\n"
      "s_cmp_lt_u32 s67, 2048\n"
      "s_cbranch_scc0 NV0_%=\n"
      "v_lshrrev_b32 v251, 4, v241\n"
      "v_lshlrev_b32 v250, 7, v251\n"
      "v_and_b32 v250, 0x1800, v250\n"
      "v_and_b32 v251, 15, v251\n"
      "v_lshl_or_b32 v251, v251, 2, v250\n"
      "v_mov_b32 v250, s82\n"
      "s_add_u32 s67, s67, 8192\n"
      "buffer_store_dword v250, v251, s[20:23], s67 offen sc1\n"
      "NV0_%=:\n"
      "s_swappc_b64 s[84:85], s[84:85]\n"
      "s_mov_b32 s86, 54\n"
      "s_mov_b32 s77, 162\n"
      "s_swappc_b64 s[84:85], s[84:85]\n"
      "v_and_b32 v251, 0x1f0, v241\n"
      "v_lshl_add_u32 v251, v251, 2, s79\n"
      "buffer_load_dword v250, v251, s[20:23], 0 offen sc1\n"
      "s_swappc_b64 s[84:85], s[84:85]\n"
      "s_waitcnt vmcnt(12)\n"
      "v_sub_u32 v250, s82, v250\n"
      "v_cmp_gt_u32 vcc, 1600, v250\n"
      "s_nop 3\n"
      "s_bcnt1_i32_b32 s67, vcc_lo\n"
      "s_cmp_ge_u32 s67, 9\n"
      "s_cbranch_scc1 SM_%=\n"
      "s_swappc_b64 s[84:85], s[84:85]\n"
      "s_cmp_lt_u32 s89, 3\n"
      "s_cselect_b32 s67, 2048, s72\n"
      "s_mov_b32 s89, 0\n"
      "s_cmp_lt_u32 s67, 2048\n"
      "s_cbranch_scc0 NV1_%=\n"
      "v_lshrrev_b32 v251, 4, v241\n"
      "v_lshlrev_b32 v250, 7, v251\n"
      "v_and_b32 v250, 0x1800, v250\n"
      "v_and_b32 v251, 15, v251\n"
      "v_lshl_or_b32 v251, v251, 2, v250\n"
      "v_mov_b32 v250, s82\n"
      "s_add_u32 s67, s67, 8192\n"
      "buffer_store_dword v250, v251, s[20:23], s67 offen sc1\n"
      "NV1_%=:\n"
      "s_swappc_b64 s[84:85], s[84:85]\n"
      "v_and_b32 v251, 0x1f0, v241\n"
      "v_lshl_add_u32 v251, v251, 2, s79\n"
      "buffer_load_dword v250, v251, s[20:23], 0 offen sc1\n"
      "s_swappc_b64 s[84:85], s[84:85]\n"
      "s_mov_b32 s80, 85\n"
      "s_swappc_b64 s[84:85], s[84:85]\n"
      "s_waitcnt vmcnt(12)\n"
      "v_sub_u32 v250, s82, v250\n"
      "v_cmp_gt_u32 vcc, 1600, v250\n"
      "s_nop 3\n"
      "s_bcnt1_i32_b32 s67, vcc_lo\n"
      "s_cmp_ge_u32 s67, 9\n"
      "s_cbranch_scc1 SM_%=\n"
      "s_swappc_b64 s[84:85], s[84:85]\n"
      "s_cmp_lt_u32 s89, 3\n"
      "s_cselect_b32 s67, 2048, s72\n"
      "s_mov_b32 s89, 0\n"
      "s_cmp_lt_u32 s67, 2048\n"
      "s_cbranch_scc0 NV2_%=\n"
      "v_lshrrev_b32 v251, 4, v241\n"
      "v_lshlrev_b32 v250, 7, v251\n"
      "v_and_b32 v250, 0x1800, v250\n"
      "v_and_b32 v251, 15, v251\n"
      "v_lshl_or_b32 v251, v251, 2, v250\n"
      "v_mov_b32 v250, s82\n"
      "s_add_u32 s67, s67, 8192\n"
      "buffer_store_dword v250, v251, s[20:23], s67 offen sc1\n"
      "NV2_%=:\n"
      "s_swappc_b64 s[84:85], s[84:85]\n"
      "v_and_b32 v251, 0x1f0, v241\n"
      "v_lshl_add_u32 v251, v251, 2, s79\n"
      "buffer_load_dword v250, v251, s[20:23], 0 offen sc1\n"
      "s_swappc_b64 s[84:85], s[84:85]\n"
      "s_waitcnt vmcnt(12)\n"
      "v_sub_u32 v250, s82, v250\n"
      "v_cmp_gt_u32 vcc, 1600, v250\n"
      "s_nop 3\n"
      "s_bcnt1_i32_b32 s67, vcc_lo\n"
      "s_cmp_ge_u32 s67, 9\n"
      "s_cbranch_scc1 SM_%=\n"
      "s_swappc_b64 s[84:85], s[84:85]\n"
      "s_mov_b32 s86, 53\n"
      "s_mov_b32 s77, 159\n"
      "s_swappc_b64 s[84:85], s[84:85]\n"
      "s_getpc_b64 s[74:75]\n"
      "PF_%=:\n"
      "s_sub_u32 s74, s74, PF_%=-END_%=\n"
      "s_subb_u32 s75, s75, 0\n"
      "s_lshl_b32 s76, %[blk], 7\n"
      "s_and_b32 s76, s76, 0x1c00\n"
      "s_min_u32 s76, s76, ENDE_%=-END_%=-1024\n"
      "v_add_u32 v236, s76, v241\n"
      "global_load_dwordx4 v[236:239], v236, s[74:75]\n"
      "s_swappc_b64 s[84:85], s[84:85]\n"
      "SM_%=:\n"
      "s_or_b32 s39, s39, 0xe0000000\n"
      "s_mov_b32 s52, 0xaaaaaaaa\n"
      "s_mov_b32 s53, 0xaaaaaaaa\n"
      "s_add_u32 s66, s66, 110\n"
      "s_max_u32 s88, s66, s65\n"
      "s_mov_b32 s80, 85\n"
      "s_mov_b32 s86, 66\n"
      "s_mov_b32 s77, 198\n"
      "s_swappc_b64 s[84:85], s[84:85]\n"
      "s_mov_b32 s86, 62\n"
      "s_mov_b32 s77, 186\n"
      "s_swappc_b64 s[84:85], s[84:85]\n"
      "s_mov_b32 s86, 59\n"
      "s_mov_b32 s77, 177\n"
      "s_swappc_b64 s[84:85], s[84:85]\n"
      "s_mov_b32 s86, 56\n"
      "s_mov_b32 s77, 168\n"
      "s_mov_b32 s52, 0x0\n"
      "s_mov_b32 s53, 0x80000\n"
      "s_swappc_b64 s[84:85], s[84:85]\n"
      "s_getpc_b64 s[74:75]\n"
      "PG_%=:\n"
      "s_sub_u32 s74, s74, PG_%=-END_%=\n"
      "s_subb_u32 s75, s75, 0\n"
      "s_lshl_b32 s76, %[blk], 7\n"
      "s_and_b32 s76, s76, 0x1c00\n"
      "s_min_u32 s76, s76, ENDE_%=-END_%=-1024\n"
      "v_add_u32 v236, s76, v241\n"
      "global_load_dwordx4 v[236:239], v236, s[74:75]\n"
      "s_swappc_b64 s[84:85], s[84:85]\n"
      "GW0_%=:\n"
      "s_memrealtime s[68:69]\n"
      "s_waitcnt lgkmcnt(0)\n"
      "s_getreg_b32 s67, hwreg(HW_REG_IB_STS, 0, 4)\n"
      "s_cmp_gt_u32 s67, 9\n"
      "s_addc_u32 s89, s89, 0\n"
      "GS0_%=:\n"
      "s_sub_u32 s67, s68, s88\n"
      "s_cmp_ge_i32 s67, 0\n"
      "s_cbranch_scc1 GX0_%=\n"
      "s_memrealtime s[68:69]\n"
      "s_waitcnt lgkmcnt(0)\n"
      "s_branch GS0_%=\n"
      "FS1_%=:\n"
      "s_call_b64 s[96:97], FB1_%=\n"
      "s_branch FR1_%=\n"
      "FS2_%=:\n"
      "s_call_b64 s[96:97], FB0_%=\n"
      "s_branch FR2_%=\n"
      "FS3_%=:\n"
      "s_call_b64 s[96:97], FB0_%=\n"
      "s_branch FR3_%=\n"
      "GW4_%=:\n"
      "s_memrealtime s[68:69]\n"
      "s_waitcnt lgkmcnt(0)\n"
      "s_getreg_b32 s67, hwreg(HW_REG_IB_STS, 0, 4)\n"
      "s_cmp_gt_u32 s67, 9\n"
      "s_addc_u32 s89, s89, 0\n"
      "GS4_%=:\n"
      "s_sub_u32 s67, s68, s88\n"
      "s_cmp_ge_i32 s67, 0\n"
      "s_cbranch_scc1 GX4_%=\n"
      "s_memrealtime s[68:69]\n"
      "s_waitcnt lgkmcnt(0)\n"
      "s_branch GS4_%=\n"
      "GW5_%=:\n"
      "s_memrealtime s[68:69]\n"
      "s_waitcnt lgkmcnt(0)\n"
      "s_getreg_b32 s67, hwreg(HW_REG_IB_STS, 0, 4)\n"
      "s_cmp_gt_u32 s67, 9\n"
      "s_addc_u32 s89, s89, 0\n"
      "GS5_%=:\n"
      "s_sub_u32 s67, s68, s88\n"
      "s_cmp_ge_i32 s67, 0\n"
      "s_cbranch_scc1 GX5_%=\n"
      "s_memrealtime s[68:69]\n"
      "s_waitcnt lgkmcnt(0)\n"
      "s_branch GS5_%=\n"
      "GW6_%=:\n"
      "s_memrealtime s[68:69]\n"
      "s_waitcnt lgkmcnt(0)\n"
      "s_getreg_b32 s67, hwreg(HW_REG_IB_STS, 0, 4)\n"
      "s_cmp_gt_u32 s67, 9\n"
      "s_addc_u32 s89, s89, 0\n"
      "GS6_%=:\n"
      "s_sub_u32 s67, s68, s88\n"
      "s_cmp_ge_i32 s67, 0\n"
      "s_cbranch_scc1 GX6_%=\n"
      "s_memrealtime s[68:69]\n"
      "s_waitcnt lgkmcnt(0)\n"
      "s_branch GS6_%=\n"
      "GW7_%=:\n"
      "s_memrealtime s[68:69]\n"
      "s_waitcnt lgkmcnt(0)\n"
      "s_getreg_b32 s67, hwreg(HW_REG_IB_STS, 0, 4)\n"
      "s_cmp_gt_u32 s67, 9\n"
      "s_addc_u32 s89, s89, 0\n"
      "GS7_%=:\n"
      "s_sub_u32 s67, s68, s88\n"
      "s_cmp_ge_i32 s67, 0\n"
      "s_cbranch_scc1 GX7_%=\n"
      "s_memrealtime s[68:69]\n"
      "s_waitcnt lgkmcnt(0)\n"
      "s_branch GS7_%=\n"
      "FS8_%=:\n"
      "s_call_b64 s[96:97], FB1_%=\n"
      "s_branch FR8_%=\n"
      "LAG_%=:\n"
      "s_memrealtime s[68:69]\n"
      "s_waitcnt lgkmcnt(0)\n"
      "s_mov_b32 s88, s68\n"
      "LG1_%=:\n"
      "v_cmp_ne_u32 vcc, s40, v207\n"
      "s_cbranch_vccz LG2_%=\n"
      "s_sub_u32 s63, s63, 1\n"
      "s_cmp_eq_u32 s63, 0\n"
      "s_cbranch_scc1 READ_%=\n"
      "s_sleep 4\n"
      "global_load_dwordx2 v[206:207], v239, s[46:47] offset:8 sc1\n"
      "s_waitcnt vmcnt(0)\n"
      "s_branch LG1_%=\n"
      "LG2_%=:\n"
      "v_readfirstlane_b32 s67, v206\n"
      "s_sub_u32 s88, s88, s82\n"
      "s_min_u32 s88, s88, 400\n"
      "s_add_u32 s88, s88, s67\n"
      "LG3_%=:\n"
      "s_memrealtime s[68:69]\n"
      "s_waitcnt lgkmcnt(0)\n"
      "s_sub_u32 s67, s88, s68\n"
      "s_sub_u32 s67, s67, 17\n"
      "s_cmp_lt_u32 s67, 400\n"
      "s_cbranch_scc1 LG3_%=\n"
      "s_branch READ_%=\n"
      "RETRY_%=:\n"
      "s_sub_u32 s63, s63, 1\n"
      "s_cbranch_scc1 GOT_%=\n"
      "buffer_load_dwordx3 v[128:130], v240, s[24:27], s76 offen sc1\n"
      "buffer_load_dwordx4 v[104:107], v241, s[24:27], s76 offen offset:2048 sc1\n"
      "buffer_load_dwordx4 v[108:111], v241, s[24:27], s76 offen offset:3072 sc1\n"
      "buffer_load_dwordx4 v[96:99], v241, s[24:27], s76 offen offset:0 sc1\n"
      "buffer_load_dwordx4 v[100:103], v241, s[24:27], s76 offen offset:1024 sc1\n"
      "s_branch TRY_%=\n"
      "FS11_%=:\n"
      "s_call_b64 s[96:97], FB0_%=\n"
      "s_branch FR11_%=\n"
      "COLD_%=:\n"
      "FB0_%=:\n"
      "v_mov_b32 v196, 139248\n"
      "s_nop 7\n"
      "ds_write_b32 v196, v196\n"
      "v_max3_f32 v188, v64, v65, 0\n"
      "v_max3_f32 v189, v72, v73, 0\n"
      "v_max3_f32 v190, v80, v81, 0\n"
      "v_max3_f32 v191, v88, v89, 0\n"
      "v_max3_f32 v188, v188, v66, v67\n"
      "v_max3_f32 v189, v189, v74, v75\n"
      "v_max3_f32 v190, v190, v82, v83\n"
      "v_max3_f32 v191, v191, v90, v91\n"
      "v_max3_f32 v188, v188, v68, v69\n"
      "v_max3_f32 v189, v189, v76, v77\n"
      "v_max3_f32 v190, v190, v84, v85\n"
      "v_max3_f32 v191, v191, v92, v93\n"
      "v_max3_f32 v188, v188, v70, v71\n"
      "v_max3_f32 v189, v189, v78, v79\n"
      "v_max3_f32 v190, v190, v86, v87\n"
      "v_max3_f32 v191, v191, v94, v95\n"
      "v_mov_b32 v192, v188\n"
      "v_mov_b32 v193, v189\n"
      "v_mov_b32 v194, v190\n"
      "v_mov_b32 v195, v191\n"
      "v_permlane16_swap_b32 v188, v192\n"
      "v_permlane16_swap_b32 v189, v193\n"
      "v_permlane16_swap_b32 v190, v194\n"
      "v_permlane16_swap_b32 v191, v195\n"
      "v_max_f32 v188, v188, v192\n"
      "v_max_f32 v189, v189, v193\n"
      "v_max_f32 v190, v190, v194\n"
      "v_max_f32 v191, v191, v195\n"
      "v_mov_b32 v192, v188\n"
      "v_mov_b32 v193, v189\n"
      "v_mov_b32 v194, v190\n"
      "v_mov_b32 v195, v191\n"
      "v_permlane32_swap_b32 v188, v192\n"
      "v_permlane32_swap_b32 v189, v193\n"
      "v_permlane32_swap_b32 v190, v194\n"
      "v_permlane32_swap_b32 v191, v195\n"
      "v_max_f32 v188, v188, v192\n"
      "v_max_f32 v189, v189, v193\n"
      "v_max_f32 v190, v190, v194\n"
      "v_max_f32 v191, v191, v195\n"
      "RS0_%=:\n"
      "v_exp_f32 v192, -v188\n"
      "v_exp_f32 v193, -v189\n"
      "v_exp_f32 v194, -v190\n"
      "v_exp_f32 v195, -v191\n"
      "v_sub_f32 v176, v176, v188\n"
      "v_sub_f32 v177, v177, v189\n"
      "v_sub_f32 v178, v178, v190\n"
      "v_sub_f32 v179, v179, v191\n"
      "v_mov_b32 v160, v176\n"
      "v_mov_b32 v161, v176\n"
      "v_mov_b32 v162, v176\n"
      "v_mov_b32 v163, v176\n"
      "v_mov_b32 v164, v177\n"
      "v_mov_b32 v165, v177\n"
      "v_mov_b32 v166, v177\n"
      "v_mov_b32 v167, v177\n"
      "v_mov_b32 v168, v178\n"
      "v_mov_b32 v169, v178\n"
      "v_mov_b32 v170, v178\n"
      "v_mov_b32 v171, v178\n"
      "v_mov_b32 v172, v179\n"
      "v_mov_b32 v173, v179\n"
      "v_mov_b32 v174, v179\n"
      "v_mov_b32 v175, v179\n"
      "v_sub_f32 v64, v64, v188\n"
      "v_sub_f32 v65, v65, v188\n"
      "v_sub_f32 v66, v66, v188\n"
      "v_sub_f32 v67, v67, v188\n"
      "v_sub_f32 v68, v68, v188\n"
      "v_sub_f32 v69, v69, v188\n"
      "v_sub_f32 v70, v70, v188\n"
      "v_sub_f32 v71, v71, v188\n"
      "v_sub_f32 v72, v72, v189\n"
      "v_sub_f32 v73, v73, v189\n"
      "v_sub_f32 v74, v74, v189\n"
      "v_sub_f32 v75, v75, v189\n"
      "v_sub_f32 v76, v76, v189\n"
      "v_sub_f32 v77, v77, v189\n"
      "v_sub_f32 v78, v78, v189\n"
      "v_sub_f32 v79, v79, v189\n"
      "v_sub_f32 v80, v80, v190\n"
      "v_sub_f32 v81, v81, v190\n"
      "v_sub_f32 v82, v82, v190\n"
      "v_sub_f32 v83, v83, v190\n"
      "v_sub_f32 v84, v84, v190\n"
      "v_sub_f32 v85, v85, v190\n"
      "v_sub_f32 v86, v86, v190\n"
      "v_sub_f32 v87, v87, v190\n"
      "v_sub_f32 v88, v88, v191\n"
      "v_sub_f32 v89, v89, v191\n"
      "v_sub_f32 v90, v90, v191\n"
      "v_sub_f32 v91, v91, v191\n"
      "v_sub_f32 v92, v92, v191\n"
      "v_sub_f32 v93, v93, v191\n"
      "v_sub_f32 v94, v94, v191\n"
      "v_sub_f32 v95, v95, v191\n"
      "v_sub_f32 v96, v96, v188\n"
      "v_sub_f32 v97, v97, v188\n"
      "v_sub_f32 v98, v98, v188\n"
      "v_sub_f32 v99, v99, v188\n"
      "v_sub_f32 v100, v100, v188\n"
      "v_sub_f32 v101, v101, v188\n"
      "v_sub_f32 v102, v102, v188\n"
      "v_sub_f32 v103, v103, v188\n"
      "v_sub_f32 v104, v104, v189\n"
      "v_sub_f32 v105, v105, v189\n"
      "v_sub_f32 v106, v106, v189\n"
      "v_sub_f32 v107, v107, v189\n"
      "v_sub_f32 v108, v108, v189\n"
      "v_sub_f32 v109, v109, v189\n"
      "v_sub_f32 v110, v110, v189\n"
      "v_sub_f32 v111, v111, v189\n"
      "v_sub_f32 v112, v112, v190\n"
      "v_sub_f32 v113, v113, v190\n"
      "v_sub_f32 v114, v114, v190\n"
      "v_sub_f32 v115, v115, v190\n"
      "v_sub_f32 v116, v116, v190\n"
      "v_sub_f32 v117, v117, v190\n"
      "v_sub_f32 v118, v118, v190\n"
      "v_sub_f32 v119, v119, v190\n"
      "v_sub_f32 v120, v120, v191\n"
      "v_sub_f32 v121, v121, v191\n"
      "v_sub_f32 v122, v122, v191\n"
      "v_sub_f32 v123, v123, v191\n"
      "v_sub_f32 v124, v124, v191\n"
      "v_sub_f32 v125, v125, v191\n"
      "v_sub_f32 v126, v126, v191\n"
      "v_sub_f32 v127, v127, v191\n"
      "v_mul_f32 v0, v0, v192\n"
      "v_mul_f32 v1, v1, v192\n"
      "v_mul_f32 v2, v2, v192\n"
      "v_mul_f32 v3, v3, v192\n"
      "v_mul_f32 v4, v4, v193\n"
      "v_mul_f32 v5, v5, v193\n"
      "v_mul_f32 v6, v6, v193\n"
      "v_mul_f32 v7, v7, v193\n"
      "v_mul_f32 v8, v8, v194\n"
      "v_mul_f32 v9, v9, v194\n"
      "v_mul_f32 v10, v10, v194\n"
      "v_mul_f32 v11, v11, v194\n"
      "v_mul_f32 v12, v12, v195\n"
      "v_mul_f32 v13, v13, v195\n"
      "v_mul_f32 v14, v14, v195\n"
      "v_mul_f32 v15, v15, v195\n"
      "v_mul_f32 v16, v16, v192\n"
      "v_mul_f32 v17, v17, v192\n"
      "v_mul_f32 v18, v18, v192\n"
      "v_mul_f32 v19, v19, v192\n"
      "v_mul_f32 v20, v20, v193\n"
      "v_mul_f32 v21, v21, v193\n"
      "v_mul_f32 v22, v22, v193\n"
      "v_mul_f32 v23, v23, v193\n"
      "v_mul_f32 v24, v24, v194\n"
      "v_mul_f32 v25, v25, v194\n"
      "v_mul_f32 v26, v26, v194\n"
      "v_mul_f32 v27, v27, v194\n"
      "v_mul_f32 v28, v28, v195\n"
      "v_mul_f32 v29, v29, v195\n"
      "v_mul_f32 v30, v30, v195\n"
      "v_mul_f32 v31, v31, v195\n"
      "v_mul_f32 v32, v32, v192\n"
      "v_mul_f32 v33, v33, v192\n"
      "v_mul_f32 v34, v34, v192\n"
      "v_mul_f32 v35, v35, v192\n"
      "v_mul_f32 v36, v36, v193\n"
      "v_mul_f32 v37, v37, v193\n"
      "v_mul_f32 v38, v38, v193\n"
      "v_mul_f32 v39, v39, v193\n"
      "v_mul_f32 v40, v40, v194\n"
      "v_mul_f32 v41, v41, v194\n"
      "v_mul_f32 v42, v42, v194\n"
      "v_mul_f32 v43, v43, v194\n"
      "v_mul_f32 v44, v44, v195\n"
      "v_mul_f32 v45, v45, v195\n"
      "v_mul_f32 v46, v46, v195\n"
      "v_mul_f32 v47, v47, v195\n"
      "v_mul_f32 v48, v48, v192\n"
      "v_mul_f32 v49, v49, v192\n"
      "v_mul_f32 v50, v50, v192\n"
      "v_mul_f32 v51, v51, v192\n"
      "v_mul_f32 v52, v52, v193\n"
      "v_mul_f32 v53, v53, v193\n"
      "v_mul_f32 v54, v54, v193\n"
      "v_mul_f32 v55, v55, v193\n"
      "v_mul_f32 v56, v56, v194\n"
      "v_mul_f32 v57, v57, v194\n"
      "v_mul_f32 v58, v58, v194\n"
      "v_mul_f32 v59, v59, v194\n"
      "v_mul_f32 v60, v60, v195\n"
      "v_mul_f32 v61, v61, v195\n"
      "v_mul_f32 v62, v62, v195\n"
      "v_mul_f32 v63, v63, v195\n"
      "v_accvgpr_read_b32 v196, a144\n"
      "v_accvgpr_read_b32 v197, a145\n"
      "v_accvgpr_read_b32 v198, a146\n"
      "v_accvgpr_read_b32 v199, a147\n"
      "v_mul_f32 v196, v196, v192\n"
      "v_mul_f32 v197, v197, v192\n"
      "v_mul_f32 v198, v198, v192\n"
      "v_mul_f32 v199, v199, v192\n"
      "v_accvgpr_write_b32 a144, v196\n"
      "v_accvgpr_write_b32 a145, v197\n"
      "v_accvgpr_write_b32 a146, v198\n"
      "v_accvgpr_write_b32 a147, v199\n"
      "v_accvgpr_read_b32 v196, a148\n"
      "v_accvgpr_read_b32 v197, a149\n"
      "v_accvgpr_read_b32 v198, a150\n"
      "v_accvgpr_read_b32 v199, a151\n"
      "v_mul_f32 v196, v196, v193\n"
      "v_mul_f32 v197, v197, v193\n"
      "v_mul_f32 v198, v198, v193\n"
      "v_mul_f32 v199, v199, v193\n"
      "v_accvgpr_write_b32 a148, v196\n"
      "v_accvgpr_write_b32 a149, v197\n"
      "v_accvgpr_write_b32 a150, v198\n"
      "v_accvgpr_write_b32 a151, v199\n"
      "v_accvgpr_read_b32 v196, a152\n"
      "v_accvgpr_read_b32 v197, a153\n"
      "v_accvgpr_read_b32 v198, a154\n"
      "v_accvgpr_read_b32 v199, a155\n"
      "v_mul_f32 v196, v196, v194\n"
      "v_mul_f32 v197, v197, v194\n"
      "v_mul_f32 v198, v198, v194\n"
      "v_mul_f32 v199, v199, v194\n"
      "v_accvgpr_write_b32 a152, v196\n"
      "v_accvgpr_write_b32 a153, v197\n"
      "v_accvgpr_write_b32 a154, v198\n"
      "v_accvgpr_write_b32 a155, v199\n"
      "v_accvgpr_read_b32 v196, a156\n"
      "v_accvgpr_read_b32 v197, a157\n"
      "v_accvgpr_read_b32 v198, a158\n"
      "v_accvgpr_read_b32 v199, a159\n"
      "v_mul_f32 v196, v196, v195\n"
      "v_mul_f32 v197, v197, v195\n"
      "v_mul_f32 v198, v198, v195\n"
      "v_mul_f32 v199, v199, v195\n"
      "v_accvgpr_write_b32 a156, v196\n"
      "v_accvgpr_write_b32 a157, v197\n"
      "v_accvgpr_write_b32 a158, v198\n"
      "v_accvgpr_write_b32 a159, v199\n"
      "v_accvgpr_read_b32 v196, a160\n"
      "v_accvgpr_read_b32 v197, a161\n"
      "v_accvgpr_read_b32 v198, a162\n"
      "v_accvgpr_read_b32 v199, a163\n"
      "v_mul_f32 v196, v196, v192\n"
      "v_mul_f32 v197, v197, v192\n"
      "v_mul_f32 v198, v198, v192\n"
      "v_mul_f32 v199, v199, v192\n"
      "v_accvgpr_write_b32 a160, v196\n"
      "v_accvgpr_write_b32 a161, v197\n"
      "v_accvgpr_write_b32 a162, v198\n"
      "v_accvgpr_write_b32 a163, v199\n"
      "v_accvgpr_read_b32 v196, a164\n"
      "v_accvgpr_read_b32 v197, a165\n"
      "v_accvgpr_read_b32 v198, a166\n"
      "v_accvgpr_read_b32 v199, a167\n"
      "v_mul_f32 v196, v196, v193\n"
      "v_mul_f32 v197, v197, v193\n"
      "v_mul_f32 v198, v198, v193\n"
      "v_mul_f32 v199, v199, v193\n"
      "v_accvgpr_write_b32 a164, v196\n"
      "v_accvgpr_write_b32 a165, v197\n"
      "v_accvgpr_write_b32 a166, v198\n"
      "v_accvgpr_write_b32 a167, v199\n"
      "v_accvgpr_read_b32 v196, a168\n"
      "v_accvgpr_read_b32 v197, a169\n"
      "v_accvgpr_read_b32 v198, a170\n"
      "v_accvgpr_read_b32 v199, a171\n"
      "v_mul_f32 v196, v196, v194\n"
      "v_mul_f32 v197, v197, v194\n"
      "v_mul_f32 v198, v198, v194\n"
      "v_mul_f32 v199, v199, v194\n"
      "v_accvgpr_write_b32 a168, v196\n"
      "v_accvgpr_write_b32 a169, v197\n"
      "v_accvgpr_write_b32 a170, v198\n"
      "v_accvgpr_write_b32 a171, v199\n"
      "v_accvgpr_read_b32 v196, a172\n"
      "v_accvgpr_read_b32 v197, a173\n"
      "v_accvgpr_read_b32 v198, a174\n"
      "v_accvgpr_read_b32 v199, a175\n"
      "v_mul_f32 v196, v196, v195\n"
      "v_mul_f32 v197, v197, v195\n"
      "v_mul_f32 v198, v198, v195\n"
      "v_mul_f32 v199, v199, v195\n"
      "v_accvgpr_write_b32 a172, v196\n"
      "v_accvgpr_write_b32 a173, v197\n"
      "v_accvgpr_write_b32 a174, v198\n"
      "v_accvgpr_write_b32 a175, v199\n"
      "v_accvgpr_read_b32 v196, a176\n"
      "v_accvgpr_read_b32 v197, a177\n"
      "v_accvgpr_read_b32 v198, a178\n"
      "v_accvgpr_read_b32 v199, a179\n"
      "v_mul_f32 v196, v196, v192\n"
      "v_mul_f32 v197, v197, v192\n"
      "v_mul_f32 v198, v198, v192\n"
      "v_mul_f32 v199, v199, v192\n"
      "v_accvgpr_write_b32 a176, v196\n"
      "v_accvgpr_write_b32 a177, v197\n"
      "v_accvgpr_write_b32 a178, v198\n"
      "v_accvgpr_write_b32 a179, v199\n"
      "v_accvgpr_read_b32 v196, a180\n"
      "v_accvgpr_read_b32 v197, a181\n"
      "v_accvgpr_read_b32 v198, a182\n"
      "v_accvgpr_read_b32 v199, a183\n"
      "v_mul_f32 v196, v196, v193\n"
      "v_mul_f32 v197, v197, v193\n"
      "v_mul_f32 v198, v198, v193\n"
      "v_mul_f32 v199, v199, v193\n"
      "v_accvgpr_write_b32 a180, v196\n"
      "v_accvgpr_write_b32 a181, v197\n"
      "v_accvgpr_write_b32 a182, v198\n"
      "v_accvgpr_write_b32 a183, v199\n"
      "v_accvgpr_read_b32 v196, a184\n"
      "v_accvgpr_read_b32 v197, a185\n"
      "v_accvgpr_read_b32 v198, a186\n"
      "v_accvgpr_read_b32 v199, a187\n"
      "v_mul_f32 v196, v196, v194\n"
      "v_mul_f32 v197, v197, v194\n"
      "v_mul_f32 v198, v198, v194\n"
      "v_mul_f32 v199, v199, v194\n"
      "v_accvgpr_write_b32 a184, v196\n"
      "v_accvgpr_write_b32 a185, v197\n"
      "v_accvgpr_write_b32 a186, v198\n"
      "v_accvgpr_write_b32 a187, v199\n"
      "v_accvgpr_read_b32 v196, a188\n"
      "v_accvgpr_read_b32 v197, a189\n"
      "v_accvgpr_read_b32 v198, a190\n"
      "v_accvgpr_read_b32 v199, a191\n"
      "v_mul_f32 v196, v196, v195\n"
      "v_mul_f32 v197, v197, v195\n"
      "v_mul_f32 v198, v198, v195\n"
      "v_mul_f32 v199, v199, v195\n"
      "v_accvgpr_write_b32 a188, v196\n"
      "v_accvgpr_write_b32 a189, v197\n"
      "v_accvgpr_write_b32 a190, v198\n"
      "v_accvgpr_write_b32 a191, v199\n"
      "v_accvgpr_read_b32 v196, a192\n"
      "v_accvgpr_read_b32 v197, a193\n"
      "v_accvgpr_read_b32 v198, a194\n"
      "v_accvgpr_read_b32 v199, a195\n"
      "v_mul_f32 v196, v196, v192\n"
      "v_mul_f32 v197, v197, v192\n"
      "v_mul_f32 v198, v198, v192\n"
      "v_mul_f32 v199, v199, v192\n"
      "v_accvgpr_write_b32 a192, v196\n"
      "v_accvgpr_write_b32 a193, v197\n"
      "v_accvgpr_write_b32 a194, v198\n"
      "v_accvgpr_write_b32 a195, v199\n"
      "v_accvgpr_read_b32 v196, a196\n"
      "v_accvgpr_read_b32 v197, a197\n"
      "v_accvgpr_read_b32 v198, a198\n"
      "v_accvgpr_read_b32 v199, a199\n"
      "v_mul_f32 v196, v196, v193\n"
      "v_mul_f32 v197, v197, v193\n"
      "v_mul_f32 v198, v198, v193\n"
      "v_mul_f32 v199, v199, v193\n"
      "v_accvgpr_write_b32 a196, v196\n"
      "v_accvgpr_write_b32 a197, v197\n"
      "v_accvgpr_write_b32 a198, v198\n"
      "v_accvgpr_write_b32 a199, v199\n"
      "v_accvgpr_read_b32 v196, a200\n"
      "v_accvgpr_read_b32 v197, a201\n"
      "v_accvgpr_read_b32 v198, a202\n"
      "v_accvgpr_read_b32 v199, a203\n"
      "v_mul_f32 v196, v196, v194\n"
      "v_mul_f32 v197, v197, v194\n"
      "v_mul_f32 v198, v198, v194\n"
      "v_mul_f32 v199, v199, v194\n"
      "v_accvgpr_write_b32 a200, v196\n"
      "v_accvgpr_write_b32 a201, v197\n"
      "v_accvgpr_write_b32 a202, v198\n"
      "v_accvgpr_write_b32 a203, v199\n"
      "v_accvgpr_read_b32 v196, a204\n"
      "v_accvgpr_read_b32 v197, a205\n"
      "v_accvgpr_read_b32 v198, a206\n"
      "v_accvgpr_read_b32 v199, a207\n"
      "v_mul_f32 v196, v196, v195\n"
      "v_mul_f32 v197, v197, v195\n"
      "v_mul_f32 v198, v198, v195\n"
      "v_mul_f32 v199, v199, v195\n"
      "v_accvgpr_write_b32 a204, v196\n"
      "v_accvgpr_write_b32 a205, v197\n"
      "v_accvgpr_write_b32 a206, v198\n"
      "v_accvgpr_write_b32 a207, v199\n"
      "v_mul_f32 v180, v180, v192\n"
      "v_mul_f32 v181, v181, v193\n"
      "v_mul_f32 v182, v182, v194\n"
      "v_mul_f32 v183, v183, v195\n"
      "v_exp_f32 v188, v64\n"
      "v_exp_f32 v189, v65\n"
      "v_exp_f32 v190, v66\n"
      "v_exp_f32 v191, v67\n"
      "v_cvt_pk_bf16_f32 v128, v188, v189\n"
      "v_cvt_pk_bf16_f32 v129, v190, v191\n"
      "v_exp_f32 v192, v68\n"
      "v_exp_f32 v193, v69\n"
      "v_exp_f32 v194, v70\n"
      "v_exp_f32 v195, v71\n"
      "v_cvt_pk_bf16_f32 v130, v192, v193\n"
      "v_cvt_pk_bf16_f32 v131, v194, v195\n"
      "v_exp_f32 v188, v72\n"
      "v_exp_f32 v189, v73\n"
      "v_exp_f32 v190, v74\n"
      "v_exp_f32 v191, v75\n"
      "v_cvt_pk_bf16_f32 v132, v188, v189\n"
      "v_cvt_pk_bf16_f32 v133, v190, v191\n"
      "v_exp_f32 v192, v76\n"
      "v_exp_f32 v193, v77\n"
      "v_exp_f32 v194, v78\n"
      "v_exp_f32 v195, v79\n"
      "v_cvt_pk_bf16_f32 v134, v192, v193\n"
      "v_cvt_pk_bf16_f32 v135, v194, v195\n"
      "v_exp_f32 v188, v80\n"
      "v_exp_f32 v189, v81\n"
      "v_exp_f32 v190, v82\n"
      "v_exp_f32 v191, v83\n"
      "v_cvt_pk_bf16_f32 v136, v188, v189\n"
      "v_cvt_pk_bf16_f32 v137, v190, v191\n"
      "v_exp_f32 v192, v84\n"
      "v_exp_f32 v193, v85\n"
      "v_exp_f32 v194, v86\n"
      "v_exp_f32 v195, v87\n"
      "v_cvt_pk_bf16_f32 v138, v192, v193\n"
      "v_cvt_pk_bf16_f32 v139, v194, v195\n"
      "v_exp_f32 v188, v88\n"
      "v_exp_f32 v189, v89\n"
      "v_exp_f32 v190, v90\n"
      "v_exp_f32 v191, v91\n"
      "v_cvt_pk_bf16_f32 v140, v188, v189\n"
      "v_cvt_pk_bf16_f32 v141, v190, v191\n"
      "v_exp_f32 v192, v92\n"
      "v_exp_f32 v193, v93\n"
      "v_exp_f32 v194, v94\n"
      "v_exp_f32 v195, v95\n"
      "v_cvt_pk_bf16_f32 v142, v192, v193\n"
      "v_cvt_pk_bf16_f32 v143, v194, v195\n"
      "s_nop 1\n"
      "v_mfma_f32_16x16x32_bf16 v[184:187], a[128:131], v[128:131], 0\n"
      "v_mfma_f32_16x16x32_bf16 v[184:187], a[132:135], v[132:135], v[184:187]\n"
      "v_mfma_f32_16x16x32_bf16 v[184:187], a[136:139], v[136:139], v[184:187]\n"
      "v_mfma_f32_16x16x32_bf16 v[184:187], a[140:143], v[140:143], v[184:187]\n"
      "s_nop 7\n"
      "s_setpc_b64 s[96:97]\n"
      "FB1_%=:\n"
      "v_mov_b32 v196, 139248\n"
      "s_nop 7\n"
      "ds_write_b32 v196, v196\n"
      "v_max3_f32 v188, v96, v97, 0\n"
      "v_max3_f32 v189, v104, v105, 0\n"
      "v_max3_f32 v190, v112, v113, 0\n"
      "v_max3_f32 v191, v120, v121, 0\n"
      "v_max3_f32 v188, v188, v98, v99\n"
      "v_max3_f32 v189, v189, v106, v107\n"
      "v_max3_f32 v190, v190, v114, v115\n"
      "v_max3_f32 v191, v191, v122, v123\n"
      "v_max3_f32 v188, v188, v100, v101\n"
      "v_max3_f32 v189, v189, v108, v109\n"
      "v_max3_f32 v190, v190, v116, v117\n"
      "v_max3_f32 v191, v191, v124, v125\n"
      "v_max3_f32 v188, v188, v102, v103\n"
      "v_max3_f32 v189, v189, v110, v111\n"
      "v_max3_f32 v190, v190, v118, v119\n"
      "v_max3_f32 v191, v191, v126, v127\n"
      "v_mov_b32 v192, v188\n"
      "v_mov_b32 v193, v189\n"
      "v_mov_b32 v194, v190\n"
      "v_mov_b32 v195, v191\n"
      "v_permlane16_swap_b32 v188, v192\n"
      "v_permlane16_swap_b32 v189, v193\n"
      "v_permlane16_swap_b32 v190, v194\n"
      "v_permlane16_swap_b32 v191, v195\n"
      "v_max_f32 v188, v188, v192\n"
      "v_max_f32 v189, v189, v193\n"
      "v_max_f32 v190, v190, v194\n"
      "v_max_f32 v191, v191, v195\n"
      "v_mov_b32 v192, v188\n"
      "v_mov_b32 v193, v189\n"
      "v_mov_b32 v194, v190\n"
      "v_mov_b32 v195, v191\n"
      "v_permlane32_swap_b32 v188, v192\n"
      "v_permlane32_swap_b32 v189, v193\n"
      "v_permlane32_swap_b32 v190, v194\n"
      "v_permlane32_swap_b32 v191, v195\n"
      "v_max_f32 v188, v188, v192\n"
      "v_max_f32 v189, v189, v193\n"
      "v_max_f32 v190, v190, v194\n"
      "v_max_f32 v191, v191, v195\n"
      "RS1_%=:\n"
      "v_exp_f32 v192, -v188\n"
      "v_exp_f32 v193, -v189\n"
      "v_exp_f32 v194, -v190\n"
      "v_exp_f32 v195, -v191\n"
      "v_sub_f32 v176, v176, v188\n"
      "v_sub_f32 v177, v177, v189\n"
      "v_sub_f32 v178, v178, v190\n"
      "v_sub_f32 v179, v179, v191\n"
      "v_mov_b32 v160, v176\n"
      "v_mov_b32 v161, v176\n"
      "v_mov_b32 v162, v176\n"
      "v_mov_b32 v163, v176\n"
      "v_mov_b32 v164, v177\n"
      "v_mov_b32 v165, v177\n"
      "v_mov_b32 v166, v177\n"
      "v_mov_b32 v167, v177\n"
      "v_mov_b32 v168, v178\n"
      "v_mov_b32 v169, v178\n"
      "v_mov_b32 v170, v178\n"
      "v_mov_b32 v171, v178\n"
      "v_mov_b32 v172, v179\n"
      "v_mov_b32 v173, v179\n"
      "v_mov_b32 v174, v179\n"
      "v_mov_b32 v175, v179\n"
      "v_sub_f32 v64, v64, v188\n"
      "v_sub_f32 v65, v65, v188\n"
      "v_sub_f32 v66, v66, v188\n"
      "v_sub_f32 v67, v67, v188\n"
      "v_sub_f32 v68, v68, v188\n"
      "v_sub_f32 v69, v69, v188\n"
      "v_sub_f32 v70, v70, v188\n"
      "v_sub_f32 v71, v71, v188\n"
      "v_sub_f32 v72, v72, v189\n"
      "v_sub_f32 v73, v73, v189\n"
      "v_sub_f32 v74, v74, v189\n"
      "v_sub_f32 v75, v75, v189\n"
      "v_sub_f32 v76, v76, v189\n"
      "v_sub_f32 v77, v77, v189\n"
      "v_sub_f32 v78, v78, v189\n"
      "v_sub_f32 v79, v79, v189\n"
      "v_sub_f32 v80, v80, v190\n"
      "v_sub_f32 v81, v81, v190\n"
      "v_sub_f32 v82, v82, v190\n"
      "v_sub_f32 v83, v83, v190\n"
      "v_sub_f32 v84, v84, v190\n"
      "v_sub_f32 v85, v85, v190\n"
      "v_sub_f32 v86, v86, v190\n"
      "v_sub_f32 v87, v87, v190\n"
      "v_sub_f32 v88, v88, v191\n"
      "v_sub_f32 v89, v89, v191\n"
      "v_sub_f32 v90, v90, v191\n"
      "v_sub_f32 v91, v91, v191\n"
      "v_sub_f32 v92, v92, v191\n"
      "v_sub_f32 v93, v93, v191\n"
      "v_sub_f32 v94, v94, v191\n"
      "v_sub_f32 v95, v95, v191\n"
      "v_sub_f32 v96, v96, v188\n"
      "v_sub_f32 v97, v97, v188\n"
      "v_sub_f32 v98, v98, v188\n"
      "v_sub_f32 v99, v99, v188\n"
      "v_sub_f32 v100, v100, v188\n"
      "v_sub_f32 v101, v101, v188\n"
      "v_sub_f32 v102, v102, v188\n"
      "v_sub_f32 v103, v103, v188\n"
      "v_sub_f32 v104, v104, v189\n"
      "v_sub_f32 v105, v105, v189\n"
      "v_sub_f32 v106, v106, v189\n"
      "v_sub_f32 v107, v107, v189\n"
      "v_sub_f32 v108, v108, v189\n"
      "v_sub_f32 v109, v109, v189\n"
      "v_sub_f32 v110, v110, v189\n"
      "v_sub_f32 v111, v111, v189\n"
      "v_sub_f32 v112, v112, v190\n"
      "v_sub_f32 v113, v113, v190\n"
      "v_sub_f32 v114, v114, v190\n"
      "v_sub_f32 v115, v115, v190\n"
      "v_sub_f32 v116, v116, v190\n"
      "v_sub_f32 v117, v117, v190\n"
      "v_sub_f32 v118, v118, v190\n"
      "v_sub_f32 v119, v119, v190\n"
      "v_sub_f32 v120, v120, v191\n"
      "v_sub_f32 v121, v121, v191\n"
      "v_sub_f32 v122, v122, v191\n"
      "v_sub_f32 v123, v123, v191\n"
      "v_sub_f32 v124, v124, v191\n"
      "v_sub_f32 v125, v125, v191\n"
      "v_sub_f32 v126, v126, v191\n"
      "v_sub_f32 v127, v127, v191\n"
      "v_mul_f32 v0, v0, v192\n"
      "v_mul_f32 v1, v1, v192\n"
      "v_mul_f32 v2, v2, v192\n"
      "v_mul_f32 v3, v3, v192\n"
      "v_mul_f32 v4, v4, v193\n"
      "v_mul_f32 v5, v5, v193\n"
      "v_mul_f32 v6, v6, v193\n"
      "v_mul_f32 v7, v7, v193\n"
      "v_mul_f32 v8, v8, v194\n"
      "v_mul_f32 v9, v9, v194\n"
      "v_mul_f32 v10, v10, v194\n"
      "v_mul_f32 v11, v11, v194\n"
      "v_mul_f32 v12, v12, v195\n"
      "v_mul_f32 v13, v13, v195\n"
      "v_mul_f32 v14, v14, v195\n"
      "v_mul_f32 v15, v15, v195\n"
      "v_mul_f32 v16, v16, v192\n"
      "v_mul_f32 v17, v17, v192\n"
      "v_mul_f32 v18, v18, v192\n"
      "v_mul_f32 v19, v19, v192\n"
      "v_mul_f32 v20, v20, v193\n"
      "v_mul_f32 v21, v21, v193\n"
      "v_mul_f32 v22, v22, v193\n"
      "v_mul_f32 v23, v23, v193\n"
      "v_mul_f32 v24, v24, v194\n"
      "v_mul_f32 v25, v25, v194\n"
      "v_mul_f32 v26, v26, v194\n"
      "v_mul_f32 v27, v27, v194\n"
      "v_mul_f32 v28, v28, v195\n"
      "v_mul_f32 v29, v29, v195\n"
      "v_mul_f32 v30, v30, v195\n"
      "v_mul_f32 v31, v31, v195\n"
      "v_mul_f32 v32, v32, v192\n"
      "v_mul_f32 v33, v33, v192\n"
      "v_mul_f32 v34, v34, v192\n"
      "v_mul_f32 v35, v35, v192\n"
      "v_mul_f32 v36, v36, v193\n"
      "v_mul_f32 v37, v37, v193\n"
      "v_mul_f32 v38, v38, v193\n"
      "v_mul_f32 v39, v39, v193\n"
      "v_mul_f32 v40, v40, v194\n"
      "v_mul_f32 v41, v41, v194\n"
      "v_mul_f32 v42, v42, v194\n"
      "v_mul_f32 v43, v43, v194\n"
      "v_mul_f32 v44, v44, v195\n"
      "v_mul_f32 v45, v45, v195\n"
      "v_mul_f32 v46, v46, v195\n"
      "v_mul_f32 v47, v47, v195\n"
      "v_mul_f32 v48, v48, v192\n"
      "v_mul_f32 v49, v49, v192\n"
      "v_mul_f32 v50, v50, v192\n"
      "v_mul_f32 v51, v51, v192\n"
      "v_mul_f32 v52, v52, v193\n"
      "v_mul_f32 v53, v53, v193\n"
      "v_mul_f32 v54, v54, v193\n"
      "v_mul_f32 v55, v55, v193\n"
      "v_mul_f32 v56, v56, v194\n"
      "v_mul_f32 v57, v57, v194\n"
      "v_mul_f32 v58, v58, v194\n"
      "v_mul_f32 v59, v59, v194\n"
      "v_mul_f32 v60, v60, v195\n"
      "v_mul_f32 v61, v61, v195\n"
      "v_mul_f32 v62, v62, v195\n"
      "v_mul_f32 v63, v63, v195\n"
      "v_accvgpr_read_b32 v196, a144\n"
      "v_accvgpr_read_b32 v197, a145\n"
      "v_accvgpr_read_b32 v198, a146\n"
      "v_accvgpr_read_b32 v199, a147\n"
      "v_mul_f32 v196, v196, v192\n"
      "v_mul_f32 v197, v197, v192\n"
      "v_mul_f32 v198, v198, v192\n"
      "v_mul_f32 v199, v199, v192\n"
      "v_accvgpr_write_b32 a144, v196\n"
      "v_accvgpr_write_b32 a145, v197\n"
      "v_accvgpr_write_b32 a146, v198\n"
      "v_accvgpr_write_b32 a147, v199\n"
      "v_accvgpr_read_b32 v196, a148\n"
      "v_accvgpr_read_b32 v197, a149\n"
      "v_accvgpr_read_b32 v198, a150\n"
      "v_accvgpr_read_b32 v199, a151\n"
      "v_mul_f32 v196, v196, v193\n"
      "v_mul_f32 v197, v197, v193\n"
      "v_mul_f32 v198, v198, v193\n"
      "v_mul_f32 v199, v199, v193\n"
      "v_accvgpr_write_b32 a148, v196\n"
      "v_accvgpr_write_b32 a149, v197\n"
      "v_accvgpr_write_b32 a150, v198\n"
      "v_accvgpr_write_b32 a151, v199\n"
      "v_accvgpr_read_b32 v196, a152\n"
      "v_accvgpr_read_b32 v197, a153\n"
      "v_accvgpr_read_b32 v198, a154\n"
      "v_accvgpr_read_b32 v199, a155\n"
      "v_mul_f32 v196, v196, v194\n"
      "v_mul_f32 v197, v197, v194\n"
      "v_mul_f32 v198, v198, v194\n"
      "v_mul_f32 v199, v199, v194\n"
      "v_accvgpr_write_b32 a152, v196\n"
      "v_accvgpr_write_b32 a153, v197\n"
      "v_accvgpr_write_b32 a154, v198\n"
      "v_accvgpr_write_b32 a155, v199\n"
      "v_accvgpr_read_b32 v196, a156\n"
      "v_accvgpr_read_b32 v197, a157\n"
      "v_accvgpr_read_b32 v198, a158\n"
      "v_accvgpr_read_b32 v199, a159\n"
      "v_mul_f32 v196, v196, v195\n"
      "v_mul_f32 v197, v197, v195\n"
      "v_mul_f32 v198, v198, v195\n"
      "v_mul_f32 v199, v199, v195\n"
      "v_accvgpr_write_b32 a156, v196\n"
      "v_accvgpr_write_b32 a157, v197\n"
      "v_accvgpr_write_b32 a158, v198\n"
      "v_accvgpr_write_b32 a159, v199\n"
      "v_accvgpr_read_b32 v196, a160\n"
      "v_accvgpr_read_b32 v197, a161\n"
      "v_accvgpr_read_b32 v198, a162\n"
      "v_accvgpr_read_b32 v199, a163\n"
      "v_mul_f32 v196, v196, v192\n"
      "v_mul_f32 v197, v197, v192\n"
      "v_mul_f32 v198, v198, v192\n"
      "v_mul_f32 v199, v199, v192\n"
      "v_accvgpr_write_b32 a160, v196\n"
      "v_accvgpr_write_b32 a161, v197\n"
      "v_accvgpr_write_b32 a162, v198\n"
      "v_accvgpr_write_b32 a163, v199\n"
      "v_accvgpr_read_b32 v196, a164\n"
      "v_accvgpr_read_b32 v197, a165\n"
      "v_accvgpr_read_b32 v198, a166\n"
      "v_accvgpr_read_b32 v199, a167\n"
      "v_mul_f32 v196, v196, v193\n"
      "v_mul_f32 v197, v197, v193\n"
      "v_mul_f32 v198, v198, v193\n"
      "v_mul_f32 v199, v199, v193\n"
      "v_accvgpr_write_b32 a164, v196\n"
      "v_accvgpr_write_b32 a165, v197\n"
      "v_accvgpr_write_b32 a166, v198\n"
      "v_accvgpr_write_b32 a167, v199\n"
      "v_accvgpr_read_b32 v196, a168\n"
      "v_accvgpr_read_b32 v197, a169\n"
      "v_accvgpr_read_b32 v198, a170\n"
      "v_accvgpr_read_b32 v199, a171\n"
      "v_mul_f32 v196, v196, v194\n"
      "v_mul_f32 v197, v197, v194\n"
      "v_mul_f32 v198, v198, v194\n"
      "v_mul_f32 v199, v199, v194\n"
      "v_accvgpr_write_b32 a168, v196\n"
      "v_accvgpr_write_b32 a169, v197\n"
      "v_accvgpr_write_b32 a170, v198\n"
      "v_accvgpr_write_b32 a171, v199\n"
      "v_accvgpr_read_b32 v196, a172\n"
      "v_accvgpr_read_b32 v197, a173\n"
      "v_accvgpr_read_b32 v198, a174\n"
      "v_accvgpr_read_b32 v199, a175\n"
      "v_mul_f32 v196, v196, v195\n"
      "v_mul_f32 v197, v197, v195\n"
      "v_mul_f32 v198, v198, v195\n"
      "v_mul_f32 v199, v199, v195\n"
      "v_accvgpr_write_b32 a172, v196\n"
      "v_accvgpr_write_b32 a173, v197\n"
      "v_accvgpr_write_b32 a174, v198\n"
      "v_accvgpr_write_b32 a175, v199\n"
      "v_accvgpr_read_b32 v196, a176\n"
      "v_accvgpr_read_b32 v197, a177\n"
      "v_accvgpr_read_b32 v198, a178\n"
      "v_accvgpr_read_b32 v199, a179\n"
      "v_mul_f32 v196, v196, v192\n"
      "v_mul_f32 v197, v197, v192\n"
      "v_mul_f32 v198, v198, v192\n"
      "v_mul_f32 v199, v199, v192\n"
      "v_accvgpr_write_b32 a176, v196\n"
      "v_accvgpr_write_b32 a177, v197\n"
      "v_accvgpr_write_b32 a178, v198\n"
      "v_accvgpr_write_b32 a179, v199\n"
      "v_accvgpr_read_b32 v196, a180\n"
      "v_accvgpr_read_b32 v197, a181\n"
      "v_accvgpr_read_b32 v198, a182\n"
      "v_accvgpr_read_b32 v199, a183\n"
      "v_mul_f32 v196, v196, v193\n"
      "v_mul_f32 v197, v197, v193\n"
      "v_mul_f32 v198, v198, v193\n"
      "v_mul_f32 v199, v199, v193\n"
      "v_accvgpr_write_b32 a180, v196\n"
      "v_accvgpr_write_b32 a181, v197\n"
      "v_accvgpr_write_b32 a182, v198\n"
      "v_accvgpr_write_b32 a183, v199\n"
      "v_accvgpr_read_b32 v196, a184\n"
      "v_accvgpr_read_b32 v197, a185\n"
      "v_accvgpr_read_b32 v198, a186\n"
      "v_accvgpr_read_b32 v199, a187\n"
      "v_mul_f32 v196, v196, v194\n"
      "v_mul_f32 v197, v197, v194\n"
      "v_mul_f32 v198, v198, v194\n"
      "v_mul_f32 v199, v199, v194\n"
      "v_accvgpr_write_b32 a184, v196\n"
      "v_accvgpr_write_b32 a185, v197\n"
      "v_accvgpr_write_b32 a186, v198\n"
      "v_accvgpr_write_b32 a187, v199\n"
      "v_accvgpr_read_b32 v196, a188\n"
      "v_accvgpr_read_b32 v197, a189\n"
      "v_accvgpr_read_b32 v198, a190\n"
      "v_accvgpr_read_b32 v199, a191\n"
      "v_mul_f32 v196, v196, v195\n"
      "v_mul_f32 v197, v197, v195\n"
      "v_mul_f32 v198, v198, v195\n"
      "v_mul_f32 v199, v199, v195\n"
      "v_accvgpr_write_b32 a188, v196\n"
      "v_accvgpr_write_b32 a189, v197\n"
      "v_accvgpr_write_b32 a190, v198\n"
      "v_accvgpr_write_b32 a191, v199\n"
      "v_accvgpr_read_b32 v196, a192\n"
      "v_accvgpr_read_b32 v197, a193\n"
      "v_accvgpr_read_b32 v198, a194\n"
      "v_accvgpr_read_b32 v199, a195\n"
      "v_mul_f32 v196, v196, v192\n"
      "v_mul_f32 v197, v197, v192\n"
      "v_mul_f32 v198, v198, v192\n"
      "v_mul_f32 v199, v199, v192\n"
      "v_accvgpr_write_b32 a192, v196\n"
      "v_accvgpr_write_b32 a193, v197\n"
      "v_accvgpr_write_b32 a194, v198\n"
      "v_accvgpr_write_b32 a195, v199\n"
      "v_accvgpr_read_b32 v196, a196\n"
      "v_accvgpr_read_b32 v197, a197\n"
      "v_accvgpr_read_b32 v198, a198\n"
      "v_accvgpr_read_b32 v199, a199\n"
      "v_mul_f32 v196, v196, v193\n"
      "v_mul_f32 v197, v197, v193\n"
      "v_mul_f32 v198, v198, v193\n"
      "v_mul_f32 v199, v199, v193\n"
      "v_accvgpr_write_b32 a196, v196\n"
      "v_accvgpr_write_b32 a197, v197\n"
      "v_accvgpr_write_b32 a198, v198\n"
      "v_accvgpr_write_b32 a199, v199\n"
      "v_accvgpr_read_b32 v196, a200\n"
      "v_accvgpr_read_b32 v197, a201\n"
      "v_accvgpr_read_b32 v198, a202\n"
      "v_accvgpr_read_b32 v199, a203\n"
      "v_mul_f32 v196, v196, v194\n"
      "v_mul_f32 v197, v197, v194\n"
      "v_mul_f32 v198, v198, v194\n"
      "v_mul_f32 v199, v199, v194\n"
      "v_accvgpr_write_b32 a200, v196\n"
      "v_accvgpr_write_b32 a201, v197\n"
      "v_accvgpr_write_b32 a202, v198\n"
      "v_accvgpr_write_b32 a203, v199\n"
      "v_accvgpr_read_b32 v196, a204\n"
      "v_accvgpr_read_b32 v197, a205\n"
      "v_accvgpr_read_b32 v198, a206\n"
      "v_accvgpr_read_b32 v199, a207\n"
      "v_mul_f32 v196, v196, v195\n"
      "v_mul_f32 v197, v197, v195\n"
      "v_mul_f32 v198, v198, v195\n"
      "v_mul_f32 v199, v199, v195\n"
      "v_accvgpr_write_b32 a204, v196\n"
      "v_accvgpr_write_b32 a205, v197\n"
      "v_accvgpr_write_b32 a206, v198\n"
      "v_accvgpr_write_b32 a207, v199\n"
      "v_mul_f32 v180, v180, v192\n"
      "v_mul_f32 v181, v181, v193\n"
      "v_mul_f32 v182, v182, v194\n"
      "v_mul_f32 v183, v183, v195\n"
      "v_lshlrev_b32 v196, 16, v128\n"
      "v_and_b32 v197, 0xffff0000, v128\n"
      "v_mul_f32 v196, v196, v192\n"
      "v_mul_f32 v197, v197, v192\n"
      "v_cvt_pk_bf16_f32 v128, v196, v197\n"
      "v_lshlrev_b32 v196, 16, v129\n"
      "v_and_b32 v197, 0xffff0000, v129\n"
      "v_mul_f32 v196, v196, v192\n"
      "v_mul_f32 v197, v197, v192\n"
      "v_cvt_pk_bf16_f32 v129, v196, v197\n"
      "v_lshlrev_b32 v196, 16, v130\n"
      "v_and_b32 v197, 0xffff0000, v130\n"
      "v_mul_f32 v196, v196, v192\n"
      "v_mul_f32 v197, v197, v192\n"
      "v_cvt_pk_bf16_f32 v130, v196, v197\n"
      "v_lshlrev_b32 v196, 16, v131\n"
      "v_and_b32 v197, 0xffff0000, v131\n"
      "v_mul_f32 v196, v196, v192\n"
      "v_mul_f32 v197, v197, v192\n"
      "v_cvt_pk_bf16_f32 v131, v196, v197\n"
      "v_lshlrev_b32 v196, 16, v132\n"
      "v_and_b32 v197, 0xffff0000, v132\n"
      "v_mul_f32 v196, v196, v193\n"
      "v_mul_f32 v197, v197, v193\n"
      "v_cvt_pk_bf16_f32 v132, v196, v197\n"
      "v_lshlrev_b32 v196, 16, v133\n"
      "v_and_b32 v197, 0xffff0000, v133\n"
      "v_mul_f32 v196, v196, v193\n"
      "v_mul_f32 v197, v197, v193\n"
      "v_cvt_pk_bf16_f32 v133, v196, v197\n"
      "v_lshlrev_b32 v196, 16, v134\n"
      "v_and_b32 v197, 0xffff0000, v134\n"
      "v_mul_f32 v196, v196, v193\n"
      "v_mul_f32 v197, v197, v193\n"
      "v_cvt_pk_bf16_f32 v134, v196, v197\n"
      "v_lshlrev_b32 v196, 16, v135\n"
      "v_and_b32 v197, 0xffff0000, v135\n"
      "v_mul_f32 v196, v196, v193\n"
      "v_mul_f32 v197, v197, v193\n"
      "v_cvt_pk_bf16_f32 v135, v196, v197\n"
      "v_lshlrev_b32 v196, 16, v136\n"
      "v_and_b32 v197, 0xffff0000, v136\n"
      "v_mul_f32 v196, v196, v194\n"
      "v_mul_f32 v197, v197, v194\n"
      "v_cvt_pk_bf16_f32 v136, v196, v197\n"
      "v_lshlrev_b32 v196, 16, v137\n"
      "v_and_b32 v197, 0xffff0000, v137\n"
      "v_mul_f32 v196, v196, v194\n"
      "v_mul_f32 v197, v197, v194\n"
      "v_cvt_pk_bf16_f32 v137, v196, v197\n"
      "v_lshlrev_b32 v196, 16, v138\n"
      "v_and_b32 v197, 0xffff0000, v138\n"
      "v_mul_f32 v196, v196, v194\n"
      "v_mul_f32 v197, v197, v194\n"
      "v_cvt_pk_bf16_f32 v138, v196, v197\n"
      "v_lshlrev_b32 v196, 16, v139\n"
      "v_and_b32 v197, 0xffff0000, v139\n"
      "v_mul_f32 v196, v196, v194\n"
      "v_mul_f32 v197, v197, v194\n"
      "v_cvt_pk_bf16_f32 v139, v196, v197\n"
      "v_lshlrev_b32 v196, 16, v140\n"
      "v_and_b32 v197, 0xffff0000, v140\n"
      "v_mul_f32 v196, v196, v195\n"
      "v_mul_f32 v197, v197, v195\n"
      "v_cvt_pk_bf16_f32 v140, v196, v197\n"
      "v_lshlrev_b32 v196, 16, v141\n"
      "v_and_b32 v197, 0xffff0000, v141\n"
      "v_mul_f32 v196, v196, v195\n"
      "v_mul_f32 v197, v197, v195\n"
      "v_cvt_pk_bf16_f32 v141, v196, v197\n"
      "v_lshlrev_b32 v196, 16, v142\n"
      "v_and_b32 v197, 0xffff0000, v142\n"
      "v_mul_f32 v196, v196, v195\n"
      "v_mul_f32 v197, v197, v195\n"
      "v_cvt_pk_bf16_f32 v142, v196, v197\n"
      "v_lshlrev_b32 v196, 16, v143\n"
      "v_and_b32 v197, 0xffff0000, v143\n"
      "v_mul_f32 v196, v196, v195\n"
      "v_mul_f32 v197, v197, v195\n"
      "v_cvt_pk_bf16_f32 v143, v196, v197\n"
      "v_exp_f32 v188, v96\n"
      "v_exp_f32 v189, v97\n"
      "v_exp_f32 v190, v98\n"
      "v_exp_f32 v191, v99\n"
      "v_cvt_pk_bf16_f32 v144, v188, v189\n"
      "v_cvt_pk_bf16_f32 v145, v190, v191\n"
      "v_exp_f32 v192, v100\n"
      "v_exp_f32 v193, v101\n"
      "v_exp_f32 v194, v102\n"
      "v_exp_f32 v195, v103\n"
      "v_cvt_pk_bf16_f32 v146, v192, v193\n"
      "v_cvt_pk_bf16_f32 v147, v194, v195\n"
      "v_exp_f32 v188, v104\n"
      "v_exp_f32 v189, v105\n"
      "v_exp_f32 v190, v106\n"
      "v_exp_f32 v191, v107\n"
      "v_cvt_pk_bf16_f32 v148, v188, v189\n"
      "v_cvt_pk_bf16_f32 v149, v190, v191\n"
      "v_exp_f32 v192, v108\n"
      "v_exp_f32 v193, v109\n"
      "v_exp_f32 v194, v110\n"
      "v_exp_f32 v195, v111\n"
      "v_cvt_pk_bf16_f32 v150, v192, v193\n"
      "v_cvt_pk_bf16_f32 v151, v194, v195\n"
      "v_exp_f32 v188, v112\n"
      "v_exp_f32 v189, v113\n"
      "v_exp_f32 v190, v114\n"
      "v_exp_f32 v191, v115\n"
      "v_cvt_pk_bf16_f32 v152, v188, v189\n"
      "v_cvt_pk_bf16_f32 v153, v190, v191\n"
      "v_exp_f32 v192, v116\n"
      "v_exp_f32 v193, v117\n"
      "v_exp_f32 v194, v118\n"
      "v_exp_f32 v195, v119\n"
      "v_cvt_pk_bf16_f32 v154, v192, v193\n"
      "v_cvt_pk_bf16_f32 v155, v194, v195\n"
      "v_exp_f32 v188, v120\n"
      "v_exp_f32 v189, v121\n"
      "v_exp_f32 v190, v122\n"
      "v_exp_f32 v191, v123\n"
      "v_cvt_pk_bf16_f32 v156, v188, v189\n"
      "v_cvt_pk_bf16_f32 v157, v190, v191\n"
      "v_exp_f32 v192, v124\n"
      "v_exp_f32 v193, v125\n"
      "v_exp_f32 v194, v126\n"
      "v_exp_f32 v195, v127\n"
      "v_cvt_pk_bf16_f32 v158, v192, v193\n"
      "v_cvt_pk_bf16_f32 v159, v194, v195\n"
      "s_nop 1\n"
      "v_mfma_f32_16x16x32_bf16 v[184:187], a[128:131], v[144:147], 0\n"
      "v_mfma_f32_16x16x32_bf16 v[184:187], a[132:135], v[148:151], v[184:187]\n"
      "v_mfma_f32_16x16x32_bf16 v[184:187], a[136:139], v[152:155], v[184:187]\n"
      "v_mfma_f32_16x16x32_bf16 v[184:187], a[140:143], v[156:159], v[184:187]\n"
      "s_nop 7\n"
      "s_setpc_b64 s[96:97]\n"
      "SLOW_%=:\n"
      "ds_write_b128 v234, v[176:179] offset:5120\n"
      "s_waitcnt lgkmcnt(0)\n"
      "s_barrier\n"
      "ds_read_b128 v[192:195], v235 offset:5120\n"
      "s_waitcnt lgkmcnt(0)\n"
      "v_sub_f32 v188, v176, v192\n"
      "v_sub_f32 v189, v177, v193\n"
      "v_sub_f32 v190, v178, v194\n"
      "v_sub_f32 v191, v179, v195\n"
      "v_max_f32 v188, 0, v188\n"
      "v_max_f32 v189, 0, v189\n"
      "v_max_f32 v190, 0, v190\n"
      "v_max_f32 v191, 0, v191\n"
      "s_call_b64 s[96:97], RS1_%=\n"
      "ds_write_b128 v232, a[176:179] offset:0\n"
      "ds_write_b128 v232, a[180:183] offset:1024\n"
      "ds_write_b128 v232, a[184:187] offset:2048\n"
      "ds_write_b128 v232, a[188:191] offset:3072\n"
      "ds_write_b128 v232, a[192:195] offset:4096\n"
      "ds_write_b128 v232, a[196:199] offset:5120\n"
      "ds_write_b128 v232, a[200:203] offset:6144\n"
      "ds_write_b128 v232, a[204:207] offset:7168\n"
      "ds_write_b128 v234, v[144:147] offset:0\n"
      "ds_write_b128 v234, v[148:151] offset:1024\n"
      "ds_write_b128 v234, v[152:155] offset:2048\n"
      "ds_write_b128 v234, v[156:159] offset:3072\n"
      "v_cndmask_b32 v200, v184, v185, s[74:75]\n"
      "v_cndmask_b32 v196, v186, v187, s[74:75]\n"
      "v_cndmask_b32 v200, v200, v196, s[94:95]\n"
      "v_cndmask_b32 v229, v180, v181, s[74:75]\n"
      "v_cndmask_b32 v196, v182, v183, s[74:75]\n"
      "v_cndmask_b32 v229, v229, v196, s[94:95]\n"
      "v_add_f32 v229, v229, v200\n"
      "ds_write_b32 v234, v229 offset:4096\n"
      "v_cndmask_b32 v228, v176, v177, s[74:75]\n"
      "v_cndmask_b32 v196, v178, v179, s[74:75]\n"
      "v_cndmask_b32 v228, v228, v196, s[94:95]\n"
      "s_waitcnt lgkmcnt(0)\n"
      "s_barrier\n"
      "ds_read_b32 v208, v209\n"
      "ds_read_b128 v[160:163], v235 offset:0\n"
      "ds_read_b128 v[164:167], v235 offset:1024\n"
      "ds_read_b128 v[168:171], v235 offset:2048\n"
      "ds_read_b128 v[172:175], v235 offset:3072\n"
      "ds_read_b128 v[64:67], v233 offset:0\n"
      "ds_read_b128 v[68:71], v233 offset:1024\n"
      "ds_read_b128 v[72:75], v233 offset:2048\n"
      "ds_read_b128 v[76:79], v233 offset:3072\n"
      "ds_read_b128 v[80:83], v233 offset:4096\n"
      "ds_read_b128 v[84:87], v233 offset:5120\n"
      "ds_read_b128 v[88:91], v233 offset:6144\n"
      "ds_read_b128 v[92:95], v233 offset:7168\n"
      "ds_read_b32 v204, v235 offset:4096\n"
      "s_branch AGAIN_%=\n"
      ".subsection 0\n"
      : "+{v0}"(tid)
      : [blk] "s"(blockIdx.x), [kp] "s"(__builtin_amdgcn_kernarg_segment_ptr()), "s"(lds0)
      : "v1", "v2", "v3", "v4", "v5", "v6", "v7", "v8", "v9", "v10", "v11", "v12", "v13", "v14", "v15", "v16", "v17", "v18", "v19", "v20", "v21", "v22", "v23", "v24", "v25", "v26", "v27", "v28", "v29", "v30", "v31", "v32", "v33", "v34", "v35", "v36", "v37", "v38", "v39", "v40", "v41", "v42", "v43", "v44", "v45", "v46", "v47", "v48", "v49", "v50", "v51", "v52", "v53", "v54", "v55", "v56", "v57", "v58", "v59", "v60", "v61", "v62", "v63", "v64", "v65", "v66", "v67", "v68", "v69", "v70", "v71", "v72", "v73", "v74", "v75", "v76", "v77", "v78", "v79", "v80", "v81", "v82", "v83", "v84", "v85", "v86", "v87", "v88", "v89", "v90", "v91", "v92", "v93", "v94", "v95", "v96", "v97", "v98", "v99", "v100", "v101", "v102", "v103", "v104", "v105", "v106", "v107", "v108", "v109", "v110", "v111", "v112", "v113", "v114", "v115", "v116", "v117", "v118", "v119", "v120", "v121", "v122", "v123", "v124", "v125", "v126", "v127", "v128", "v129", "v130", "v131", "v132", "v133", "v134", "v135", "v136", "v137", "v138", "v139", "v140", "v141", "v142", "v143", "v144", "v145", "v146", "v147", "v148", "v149", "v150", "v151", "v152", "v153", "v154", "v155", "v156", "v157", "v158", "v159", "v160", "v161", "v162", "v163", "v164", "v165", "v166", "v167", "v168", "v169", "v170", "v171", "v172", "v173", "v174", "v175", "v176", "v177", "v178", "v179", "v180", "v181", "v182", "v183", "v184", "v185", "v186", "v187", "v188", "v189", "v190", "v191", "v192", "v193", "v194", "v195", "v196", "v197", "v198", "v199", "v200", "v201", "v202", "v203", "v204", "v205", "v206", "v207", "v208", "v209", "v210", "v211", "v212", "v213", "v214", "v215", "v216", "v217", "v218", "v219", "v220", "v221", "v222", "v223", "v224", "v225", "v226", "v227", "v228", "v229", "v230", "v231", "v232", "v233", "v234", "v235", "v236", "v237", "v238", "v239", "v240", "v241", "v242", "v243", "v244", "v245", "v246", "v247", "v248", "v249", "v250", "v251", "a0", "a1", "a2", "a3", "a4", "a5", "a6", "a7", "a8", "a9", "a10", "a11", "a12", "a13", "a14", "a15", "a16", "a17", "a18", "a19", "a20", "a21", "a22", "a23", "a24", "a25", "a26", "a27", "a28", "a29", "a30", "a31", "a32", "a33", "a34", "a35", "a36", "a37", "a38", "a39", "a40", "a41", "a42", "a43", "a44", "a45", "a46", "a47", "a48", "a49", "a50", "a51", "a52", "a53", "a54", "a55", "a56", "a57", "a58", "a59", "a60", "a61", "a62", "a63", "a64", "a65", "a66", "a67", "a68", "a69", "a70", "a71", "a72", "a73", "a74", "a75", "a76", "a77", "a78", "a79", "a80", "a81", "a82", "a83", "a84", "a85", "a86", "a87", "a88", "a89", "a90", "a91", "a92", "a93", "a94", "a95", "a96", "a97", "a98", "a99", "a100", "a101", "a102", "a103", "a104", "a105", "a106", "a107", "a108", "a109", "a110", "a111", "a112", "a113", "a114", "a115", "a116", "a117", "a118", "a119", "a120", "a121", "a122", "a123", "a124", "a125", "a126", "a127", "a128", "a129", "a130", "a131", "a132", "a133", "a134", "a135", "a136", "a137", "a138", "a139", "a140", "a141", "a142", "a143", "a144", "a145", "a146", "a147", "a148", "a149", "a150", "a151", "a152", "a153", "a154", "a155", "a156", "a157", "a158", "a159", "a160", "a161", "a162", "a163", "a164", "a165", "a166", "a167", "a168", "a169", "a170", "a171", "a172", "a173", "a174", "a175", "a176", "a177", "a178", "a179", "a180", "a181", "a182", "a183", "a184", "a185", "a186", "a187", "a188", "a189", "a190", "a191", "a192", "a193", "a194", "a195", "a196", "a197", "a198", "a199", "a200", "a201", "a202", "a203", "a204", "a205", "a206", "a207", "s4", "s5", "s6", "s7", "s8", "s9", "s10", "s11", "s12", "s13", "s14", "s15", "s16", "s17", "s18", "s19", "s20", "s21", "s22", "s23", "s24", "s25", "s26", "s27", "s28", "s29", "s30", "s31", "s32", "s33", "s34", "s35", "s38", "s39", "s40", "s41", "s42", "s43", "s44", "s45", "s46", "s47", "s50", "s51", "s52", "s53", "s54", "s55", "s56", "s57", "s58", "s59", "s60", "s61", "s62", "s63", "s64", "s65", "s66", "s67", "s68", "s69", "s70", "s71", "s72", "s73", "s74", "s75", "s76", "s77", "s78", "s79", "s80", "s81", "s82", "s83", "s84", "s85", "s86", "s87", "s88", "s89", "s90", "s91", "s92", "s93", "s94", "s95", "s96", "s97", "vcc", "scc", "memory");
}

}

extern "C" void kernel_launch(void* const* d_in, const int* in_sizes, int n_in, void* d_out, int out_size,
                              void* d_ws, size_t ws_size, hipStream_t stream) {
  if (n_in != 6 || ws_size < WS_END) return;
  const __hip_bfloat16* q = (const __hip_bfloat16*)d_in[0];
  const __hip_bfloat16* kc = (const __hip_bfloat16*)d_in[1];
  const __hip_bfloat16* vc = (const __hip_bfloat16*)d_in[2];
  const __hip_bfloat16* kt = (const __hip_bfloat16*)d_in[3];
  const __hip_bfloat16* vt = (const __hip_bfloat16*)d_in[4];
  const __hip_bfloat16* tm = (const __hip_bfloat16*)d_in[5];
  hipLaunchKernelGGL(tree_attn_main, dim3(B_ * HKV_ * 2), dim3(256), 0, stream, q, kc, vc, kt, vt, tm,
                     (__hip_bfloat16*)d_out, (char*)d_ws);
}
```

```cpp
#include <hip/hip_runtime.h>
#include <hip/hip_bf16.h>
#include <stdint.h>

#pragma clang diagnostic ignored "-Wint-to-pointer-cast"
#pragma clang diagnostic ignored "-Winline-asm"

namespace {

constexpr int B_ = 16, HKV_ = 8, D_ = 128;
constexpr int SLOT = 64 * D_ * 2;
constexpr int LDS_BYTES = 10 * SLOT;

constexpr size_t WS_EPOCH = 0;
constexpr size_t WS_REF = WS_EPOCH + 128 * 4 * 128;
constexpr size_t WS_X = WS_REF + 16384;
constexpr size_t XW_BYTES = 4608;
constexpr size_t WS_END = WS_X + (size_t)128 * 2 * 4 * XW_BYTES;

__global__ __launch_bounds__(256, 1)
void tree_attn_main(const __hip_bfloat16*, const __hip_bfloat16*, const __hip_bfloat16*, const __hip_bfloat16*,
                    const __hip_bfloat16*, const __hip_bfloat16*, __hip_bfloat16*, char*) {
  __shared__ __attribute__((aligned(1024))) char smem[LDS_BYTES];
  const unsigned lds0 = (unsigned)(uintptr_t)smem;
  unsigned tid = threadIdx.x;
  asm volatile(
      "s_load_dwordx16 s[4:19], %[kp], 0x0\n"
      "s_getpc_b64 s[60:61]\n"
      "v_lshlrev_b32 v241, 4, v0\n"
      "s_lshl_b32 s62, %[blk], 9\n"
      "s_and_b32 s62, s62, 0x3000\n"
      "s_add_u32 s60, s60, s62\n"
      "s_addc_u32 s61, s61, 0\n"
      "global_load_dwordx4 v[236:239], v241, s[60:61]\n"
      "v_and_b32 v1, 0xc0, v0\n"
      "v_lshlrev_b32 v248, 1, v0\n"
      "v_readfirstlane_b32 s50, v1\n"
      "v_and_b32 v248, 0x60, v248\n"
      "v_and_b32 v249, 0x70, v241\n"
      "v_xor_b32 v248, v248, v249\n"
      "v_lshl_or_b32 v248, v1, 6, v248\n"
      "v_and_b32 v1, 56, v0\n"
      "v_lshl_or_b32 v248, v1, 5, v248\n"
      "s_lshl_b32 s62, %[blk], 4\n"
      "s_and_b32 s63, s62, 0x80\n"
      "v_or_b32 v248, s63, v248\n"
      "v_xor_b32 v249, 0x80, v248\n"
      "s_and_b32 s62, s62, 0x70\n"
      "s_lshr_b32 s64, %[blk], 4\n"
      "s_add_u32 s44, s62, s64\n"
      "s_lshl_b32 s51, s64, 14\n"
      "s_and_b32 s51, s51, 0x4000\n"
      "s_lshl_b32 s64, s44, 20\n"
      "s_lshl_b32 s65, s63, 8\n"
      "s_add_u32 s64, s64, s65\n"
      "s_lshl_b32 s62, s44, 15\n"
      "s_lshl_b32 s50, s50, 6\n"
      "s_add_u32 m0, s50, 131072\n"
      "s_mov_b32 s58, 0x120000\n"
      "s_mov_b32 s59, 0x20000\n"
      "s_movk_i32 s22, 0x4000\n"
      "s_waitcnt lgkmcnt(0)\n"
      "s_add_u32 s56, s4, s62\n"
      "s_addc_u32 s57, s5, 0\n"
      "buffer_load_dwordx4 v248, s[56:59], 0 offen nt lds\n"
      "buffer_load_dwordx4 v248, s[56:59], 0 offen offset:2048 nt lds\n"
      "s_bitset1_b32 m0, 10\n"
      "s_memrealtime s[82:83]\n"
      "s_add_u32 s20, s6, s64\n"
      "s_addc_u32 s21, s7, 0\n"
      "s_add_u32 s32, s8, s64\n"
      "s_addc_u32 s33, s9, 0\n"
      "s_load_dword s92, s[20:21], 0x0\n"
      "s_load_dword s92, s[10:11], 0x0\n"
      "s_load_dword s92, s[32:33], 0x0\n"
      "buffer_load_dwordx4 v249, s[56:59], 0 offen nt lds\n"
      "buffer_load_dwordx4 v249, s[56:59], 0 offen offset:2048 nt lds\n"
      "s_add_u32 m0, s50, 147456\n"
      "s_load_dword s92, s[18:19], 0x0\n"
      "buffer_load_dwordx4 v248, s[56:59], s22 offen nt lds\n"
      "buffer_load_dwordx4 v248, s[56:59], s22 offen offset:2048 nt lds\n"
      "s_bitset1_b32 m0, 10\n"
      "s_nop 0\n"
      "buffer_load_dwordx4 v249, s[56:59], s22 offen nt lds\n"
      "buffer_load_dwordx4 v249, s[56:59], s22 offen offset:2048 nt lds\n"
      "s_mov_b64 s[56:57], s[20:21]\n"
      "s_load_dword s92, s[14:15], 0x0\n"
      "s_load_dword s92, s[12:13], 0x0\n"
      "s_lshr_b32 s74, s50, 2\n"
      "s_lshr_b32 s75, s50, 5\n"
      "s_xor_b32 s75, s75, s63\n"
      "s_and_b32 s75, s75, 0x80\n"
      "s_and_b32 s76, s74, 0x800\n"
      "s_add_u32 s75, s75, s76\n"
      "s_add_u32 s70, s65, s63\n"
      "s_add_u32 s70, s70, s50\n"
      "s_sub_u32 s70, s70, s75\n"
      "s_lshr_b32 s75, s50, 1\n"
      "s_lshr_b32 s71, s62, 2\n"
      "s_sub_u32 s71, s71, s75\n"
      "s_ashr_i32 s72, s71, 31\n"
      "s_mov_b32 s23, s59\n"
      "s_mov_b64 s[26:27], s[22:23]\n"
      "s_sub_u32 s20, s56, s70\n"
      "s_subb_u32 s21, s57, 0\n"
      "s_add_u32 m0, s74, 24576\n"
      "s_add_u32 s24, s10, s71\n"
      "buffer_load_dwordx4 v248, s[20:23], 0 offen nt lds\n"
      "s_addc_u32 s25, s11, s72\n"
      "s_add_u32 m0, s75, 16384\n"
      "s_nop 0\n"
      "buffer_load_dwordx4 v248, s[24:27], 0 offen nt lds\n"
      "s_bitset1_b32 m0, 10\n"
      "s_nop 0\n"
      "buffer_load_dwordx4 v249, s[24:27], 0 offen nt lds\n"
      "s_lshr_b32 s43, s63, 7\n"
      "s_lshl_b32 s76, s44, 9\n"
      "s_lshr_b32 s69, s50, 5\n"
      "s_add_u32 s76, s76, s69\n"
      "s_add_u32 s46, s18, s76\n"
      "s_addc_u32 s47, s19, 0\n"
      "s_mul_i32 s76, s43, 0x3000\n"
      "s_lshl_b32 s69, s44, 8\n"
      "s_and_b32 s69, s69, 0x7800\n"
      "s_add_u32 s14, s14, s69\n"
      "s_addc_u32 s15, s15, 0\n"
      "v_mov_b64 v[192:193], 0\n"
      "v_mov_b64 v[194:195], 0\n"
      "v_lshlrev_b32 v3, 6, v0\n"
      "v_and_b32 v3, 0x3c0, v3\n"
      "v_lshrrev_b32 v4, 1, v0\n"
      "v_and_or_b32 v211, v4, 24, v3\n"
      "v_mov_b64 v[200:201], 0\n"
      "v_mov_b64 v[202:203], 0\n"
      "v_mov_b64 v[204:205], 0\n"
      "v_mov_b64 v[206:207], 0\n"
      "s_cmp_lg_u32 s76, s50\n"
      "s_cbranch_scc1 NOMASK_%=\n"
      "global_load_dwordx2 v[200:201], v211, s[14:15] offset:0\n"
      "global_load_dwordx2 v[202:203], v211, s[14:15] offset:32\n"
      "global_load_dwordx2 v[204:205], v211, s[14:15] offset:1024\n"
      "global_load_dwordx2 v[206:207], v211, s[14:15] offset:1056\n"
      "NOMASK_%=:\n"
      "global_load_dword v208, v192, s[46:47] sc1\n"
      "s_mov_b64 s[30:31], s[22:23]\n"
      "s_add_u32 s28, s12, s71\n"
      "s_addc_u32 s29, s13, s72\n"
      "s_sub_u32 s32, s32, 73728\n"
      "s_subb_u32 s33, s33, 0\n"
      "s_xor_b64 s[32:33], s[32:33], s[56:57]\n"
      "s_lshl_b32 s69, s43, 13\n"
      "s_xor_b32 s69, s69, 0x2000\n"
      "v_add_u32 v2, s69, v241\n"
      "ds_write_b128 v241, v[192:195] offset:28672\n"
      "ds_write_b128 v2, v[192:195] offset:49152\n"
      "ds_write_b128 v2, v[192:195] offset:53248\n"
      "v_and_b32 v241, 0x3f0, v241\n"
      "s_and_b32 s91, s50, 8192\n"
      "v_lshlrev_b32 v4, 8, v0\n"
      "v_and_b32 v4, 0x800, v4\n"
      "v_lshlrev_b32 v5, 7, v0\n"
      "v_and_b32 v6, 0x380, v5\n"
      "v_or3_b32 v4, v4, v6, s91\n"
      "v_and_b32 v6, 0x60, v241\n"
      "v_and_b32 v2, 48, v0\n"
      "v_xor_b32 v6, v6, v2\n"
      "v_or_b32 v242, v4, v6\n"
      "v_xor_b32 v243, 64, v242\n"
      "v_lshlrev_b32 v4, 6, v0\n"
      "v_and_b32 v6, 0x800, v4\n"
      "v_lshlrev_b32 v2, 5, v0\n"
      "v_and_b32 v2, 0x380, v2\n"
      "s_add_u32 s69, s91, 16384\n"
      "v_or3_b32 v6, v6, v2, s69\n"
      "v_lshlrev_b32 v2, 3, v0\n"
      "v_and_or_b32 v6, v2, 24, v6\n"
      "v_lshlrev_b32 v2, 2, v0\n"
      "v_and_b32 v2, 0x60, v2\n"
      "v_xor_b32 v244, v6, v2\n"
      "v_xor_b32 v245, 0x20, v244\n"
      "v_xor_b32 v246, 0x40, v244\n"
      "v_xor_b32 v247, 0x60, v244\n"
      "v_and_b32 v5, 0x1e00, v5\n"
      "v_and_or_b32 v250, v241, 48, v5\n"
      "s_lshl_b32 s69, %[blk], 9\n"
      "s_and_b32 s69, s69, 0x1e00\n"
      "v_and_b32 v4, 0x1c0, v4\n"
      "v_or_b32 v251, s69, v4\n"
      "s_and_b32 s69, s50, 4096\n"
      "s_lshl_b32 s69, s69, 2\n"
      "s_add_u32 s69, s69, 131072\n"
      "s_sub_u32 s91, 16384, s91\n"
      "v_add_u32 v196, s69, v242\n"
      "v_add_u32 v198, s91, v242\n"
      "v_xor_b32 v214, 0x2000, v196\n"
      "v_add_u32 v197, s69, v243\n"
      "v_add_u32 v199, s91, v243\n"
      "v_xor_b32 v215, 0x2000, v197\n"
      "s_add_u32 s69, s50, 131072\n"
      "v_add_u32 v210, s69, v241\n"
      "v_and_b32 v2, 3, v0\n"
      "v_mov_b32 v4, 0x3f803f80\n"
      "v_cmp_eq_u32 vcc, 0, v2\n"
      "s_nop 1\n"
      "v_cndmask_b32 v5, 0, v4, vcc\n"
      "v_accvgpr_write_b32 a128, v5\n"
      "v_accvgpr_write_b32 a129, v5\n"
      "v_accvgpr_write_b32 a130, v5\n"
      "v_accvgpr_write_b32 a131, v5\n"
      "v_cmp_eq_u32 vcc, 1, v2\n"
      "s_nop 1\n"
      "v_cndmask_b32 v5, 0, v4, vcc\n"
      "v_accvgpr_write_b32 a132, v5\n"
      "v_accvgpr_write_b32 a133, v5\n"
      "v_accvgpr_write_b32 a134, v5\n"
      "v_accvgpr_write_b32 a135, v5\n"
      "v_cmp_eq_u32 vcc, 2, v2\n"
      "s_nop 1\n"
      "v_cndmask_b32 v5, 0, v4, vcc\n"
      "v_accvgpr_write_b32 a136, v5\n"
      "v_accvgpr_write_b32 a137, v5\n"
      "v_accvgpr_write_b32 a138, v5\n"
      "v_accvgpr_write_b32 a139, v5\n"
      "v_cmp_eq_u32 vcc, 3, v2\n"
      "s_nop 1\n"
      "v_cndmask_b32 v5, 0, v4, vcc\n"
      "v_accvgpr_write_b32 a140, v5\n"
      "v_accvgpr_write_b32 a141, v5\n"
      "v_accvgpr_write_b32 a142, v5\n"
      "v_accvgpr_write_b32 a143, v5\n"
      "v_mfma_f32_32x32x16_bf16 a[144:159], v[192:195], v[192:195], 0\n"
      "v_mfma_f32_32x32x16_bf16 a[160:175], v[192:195], v[192:195], 0\n"
      "v_mfma_f32_32x32x16_bf16 a[176:191], v[192:195], v[192:195], 0\n"
      "v_mfma_f32_32x32x16_bf16 a[192:207], v[192:195], v[192:195], 0\n"
      "v_mfma_f32_32x32x16_bf16 v[0:15], v[192:195], v[192:195], 0\n"
      "v_mfma_f32_32x32x16_bf16 v[16:31], v[192:195], v[192:195], 0\n"
      "v_mfma_f32_32x32x16_bf16 v[32:47], v[192:195], v[192:195], 0\n"
      "v_mfma_f32_32x32x16_bf16 v[48:63], v[192:195], v[192:195], 0\n"
      "v_mfma_f32_32x32x16_bf16 v[128:143], v[192:195], v[192:195], 0\n"
      "v_mov_b64 v[180:181], 0\n"
      "v_mov_b64 v[182:183], 0\n"
      "v_mov_b64 v[184:185], 0\n"
      "v_mov_b64 v[186:187], 0\n"
      "s_mov_b32 s61, 0x4000\n"
      "s_xor_b32 s60, s61, s51\n"
      "s_add_u32 s62, s50, 32768\n"
      "s_mov_b32 s94, 0x3e0293ee\n"
      "s_mov_b32 s95, s94\n"
      "s_lshl_b32 s69, s43, 13\n"
      "s_add_u32 s45, s69, s75\n"
      "s_mov_b32 m0, s50\n"
      "s_nop 0\n"
      "buffer_load_dwordx4 v248, s[56:59], s51 offen nt lds\n"
      "buffer_load_dwordx4 v248, s[56:59], s51 offen offset:2048 nt lds\n"
      "s_bitset1_b32 m0, 10\n"
      "s_nop 0\n"
      "buffer_load_dwordx4 v249, s[56:59], s51 offen nt lds\n"
      "buffer_load_dwordx4 v249, s[56:59], s51 offen offset:2048 nt lds\n"
      "s_waitcnt vmcnt(8)\n"
      "s_memrealtime s[68:69]\n"
      "ds_read_b128 v[64:67], v210 offset:0\n"
      "ds_read_b128 v[68:71], v210 offset:1024\n"
      "ds_read_b128 v[72:75], v210 offset:2048\n"
      "ds_read_b128 v[76:79], v210 offset:3072\n"
      "ds_read_b128 v[80:83], v210 offset:16384\n"
      "ds_read_b128 v[84:87], v210 offset:17408\n"
      "ds_read_b128 v[88:91], v210 offset:18432\n"
      "ds_read_b128 v[92:95], v210 offset:19456\n"
      "s_add_u32 s20, s18, 65536\n"
      "s_addc_u32 s21, s19, 0\n"
      "s_lshl_b32 s75, %[blk], 6\n"
      "s_add_u32 s75, s75, s50\n"
      "s_waitcnt lgkmcnt(0)\n"
      "s_sub_u32 s67, s68, s82\n"
      "s_min_u32 s67, s67, 165\n"
      "s_add_u32 s93, s82, s67\n"
      "s_sub_u32 s71, s68, 400\n"
      "s_cmp_lt_u32 s75, 512\n"
      "s_cbranch_scc0 NOLEAD_%=\n"
      "v_mov_b32 v188, s68\n"
      "v_mov_b32 v189, s68\n"
      "v_mov_b32 v190, s68\n"
      "v_mov_b32 v191, s68\n"
      "buffer_store_dwordx4 v[188:191], v250, s[20:23], s75 offen sc1\n"
      "s_nop 1\n"
      "NOLEAD_%=:\n"
      "s_mov_b32 m0, s62\n"
      "s_nop 0\n"
      "buffer_load_dwordx4 v248, s[56:59], s60 offen nt lds\n"
      "buffer_load_dwordx4 v248, s[56:59], s60 offen offset:2048 nt lds\n"
      "s_bitset1_b32 m0, 10\n"
      "s_nop 0\n"
      "buffer_load_dwordx4 v249, s[56:59], s60 offen nt lds\n"
      "buffer_load_dwordx4 v249, s[56:59], s60 offen offset:2048 nt lds\n"
      "s_xor_b64 s[56:57], s[56:57], s[32:33]\n"
      "s_bitset1_b32 s61, 15\n"
      "s_add_u32 s61, s61, 0x2000\n"
      "s_bitset0_b32 s61, 15\n"
      "s_xor_b32 s60, s61, s51\n"
      "s_add_u32 s62, s62, 0x4000\n"
      "s_and_b32 s62, s62, 0x1ffff\n"
      "s_xor_b64 s[56:57], s[56:57], s[32:33]\n"
      "s_bitset1_b32 s61, 15\n"
      "s_add_u32 s61, s61, 0x2000\n"
      "s_bitset0_b32 s61, 15\n"
      "s_xor_b32 s60, s61, s51\n"
      "s_add_u32 s62, s62, 0x4000\n"
      "s_and_b32 s62, s62, 0x1ffff\n"
      "s_add_u32 m0, s45, 49152\n"
      "s_nop 0\n"
      "buffer_load_dwordx4 v248, s[28:31], 0 offen nt lds\n"
      "s_bitset1_b32 m0, 10\n"
      "s_nop 0\n"
      "buffer_load_dwordx4 v249, s[28:31], 0 offen nt lds\n"
      "v_lshlrev_b32 v188, 16, v64\n"
      "v_and_b32 v189, 0xffff0000, v64\n"
      "v_lshlrev_b32 v190, 16, v65\n"
      "v_and_b32 v191, 0xffff0000, v65\n"
      "v_lshlrev_b32 v192, 16, v66\n"
      "v_and_b32 v193, 0xffff0000, v66\n"
      "v_lshlrev_b32 v194, 16, v67\n"
      "v_and_b32 v195, 0xffff0000, v67\n"
      "v_pk_mul_f32 v[188:189], v[188:189], s[94:95]\n"
      "v_pk_mul_f32 v[190:191], v[190:191], s[94:95]\n"
      "v_pk_mul_f32 v[192:193], v[192:193], s[94:95]\n"
      "v_pk_mul_f32 v[194:195], v[194:195], s[94:95]\n"
      "v_cvt_pk_bf16_f32 v64, v188, v189\n"
      "v_cvt_pk_bf16_f32 v65, v190, v191\n"
      "v_cvt_pk_bf16_f32 v66, v192, v193\n"
      "v_cvt_pk_bf16_f32 v67, v194, v195\n"
      "ds_write_b128 v210, v[64:67] offset:0\n"
      "v_lshlrev_b32 v188, 16, v68\n"
      "v_and_b32 v189, 0xffff0000, v68\n"
      "v_lshlrev_b32 v190, 16, v69\n"
      "v_and_b32 v191, 0xffff0000, v69\n"
      "v_lshlrev_b32 v192, 16, v70\n"
      "v_and_b32 v193, 0xffff0000, v70\n"
      "v_lshlrev_b32 v194, 16, v71\n"
      "v_and_b32 v195, 0xffff0000, v71\n"
      "v_pk_mul_f32 v[188:189], v[188:189], s[94:95]\n"
      "v_pk_mul_f32 v[190:191], v[190:191], s[94:95]\n"
      "v_pk_mul_f32 v[192:193], v[192:193], s[94:95]\n"
      "v_pk_mul_f32 v[194:195], v[194:195], s[94:95]\n"
      "v_cvt_pk_bf16_f32 v68, v188, v189\n"
      "v_cvt_pk_bf16_f32 v69, v190, v191\n"
      "v_cvt_pk_bf16_f32 v70, v192, v193\n"
      "v_cvt_pk_bf16_f32 v71, v194, v195\n"
      "ds_write_b128 v210, v[68:71] offset:1024\n"
      "v_lshlrev_b32 v188, 16, v72\n"
      "v_and_b32 v189, 0xffff0000, v72\n"
      "v_lshlrev_b32 v190, 16, v73\n"
      "v_and_b32 v191, 0xffff0000, v73\n"
      "v_lshlrev_b32 v192, 16, v74\n"
      "v_and_b32 v193, 0xffff0000, v74\n"
      "v_lshlrev_b32 v194, 16, v75\n"
      "v_and_b32 v195, 0xffff0000, v75\n"
      "v_pk_mul_f32 v[188:189], v[188:189], s[94:95]\n"
      "v_pk_mul_f32 v[190:191], v[190:191], s[94:95]\n"
      "v_pk_mul_f32 v[192:193], v[192:193], s[94:95]\n"
      "v_pk_mul_f32 v[194:195], v[194:195], s[94:95]\n"
      "v_cvt_pk_bf16_f32 v72, v188, v189\n"
      "v_cvt_pk_bf16_f32 v73, v190, v191\n"
      "v_cvt_pk_bf16_f32 v74, v192, v193\n"
      "v_cvt_pk_bf16_f32 v75, v194, v195\n"
      "ds_write_b128 v210, v[72:75] offset:2048\n"
      "v_lshlrev_b32 v188, 16, v76\n"
      "v_and_b32 v189, 0xffff0000, v76\n"
      "v_lshlrev_b32 v190, 16, v77\n"
      "v_and_b32 v191, 0xffff0000, v77\n"
      "v_lshlrev_b32 v192, 16, v78\n"
      "v_and_b32 v193, 0xffff0000, v78\n"
      "v_lshlrev_b32 v194, 16, v79\n"
      "v_and_b32 v195, 0xffff0000, v79\n"
      "v_pk_mul_f32 v[188:189], v[188:189], s[94:95]\n"
      "v_pk_mul_f32 v[190:191], v[190:191], s[94:95]\n"
      "v_pk_mul_f32 v[192:193], v[192:193], s[94:95]\n"
      "v_pk_mul_f32 v[194:195], v[194:195], s[94:95]\n"
      "v_cvt_pk_bf16_f32 v76, v188, v189\n"
      "v_cvt_pk_bf16_f32 v77, v190, v191\n"
      "v_cvt_pk_bf16_f32 v78, v192, v193\n"
      "v_cvt_pk_bf16_f32 v79, v194, v195\n"
      "ds_write_b128 v210, v[76:79] offset:3072\n"
      "v_lshlrev_b32 v188, 16, v80\n"
      "v_and_b32 v189, 0xffff0000, v80\n"
      "v_lshlrev_b32 v190, 16, v81\n"
      "v_and_b32 v191, 0xffff0000, v81\n"
      "v_lshlrev_b32 v192, 16, v82\n"
      "v_and_b32 v193, 0xffff0000, v82\n"
      "v_lshlrev_b32 v194, 16, v83\n"
      "v_and_b32 v195, 0xffff0000, v83\n"
      "v_pk_mul_f32 v[188:189], v[188:189], s[94:95]\n"
      "v_pk_mul_f32 v[190:191], v[190:191], s[94:95]\n"
      "v_pk_mul_f32 v[192:193], v[192:193], s[94:95]\n"
      "v_pk_mul_f32 v[194:195], v[194:195], s[94:95]\n"
      "v_cvt_pk_bf16_f32 v80, v188, v189\n"
      "v_cvt_pk_bf16_f32 v81, v190, v191\n"
      "v_cvt_pk_bf16_f32 v82, v192, v193\n"
      "v_cvt_pk_bf16_f32 v83, v194, v195\n"
      "ds_write_b128 v210, v[80:83] offset:16384\n"
      "v_lshlrev_b32 v188, 16, v84\n"
      "v_and_b32 v189, 0xffff0000, v84\n"
      "v_lshlrev_b32 v190, 16, v85\n"
      "v_and_b32 v191, 0xffff0000, v85\n"
      "v_lshlrev_b32 v192, 16, v86\n"
      "v_and_b32 v193, 0xffff0000, v86\n"
      "v_lshlrev_b32 v194, 16, v87\n"
      "v_and_b32 v195, 0xffff0000, v87\n"
      "v_pk_mul_f32 v[188:189], v[188:189], s[94:95]\n"
      "v_pk_mul_f32 v[190:191], v[190:191], s[94:95]\n"
      "v_pk_mul_f32 v[192:193], v[192:193], s[94:95]\n"
      "v_pk_mul_f32 v[194:195], v[194:195], s[94:95]\n"
      "v_cvt_pk_bf16_f32 v84, v188, v189\n"
      "v_cvt_pk_bf16_f32 v85, v190, v191\n"
      "v_cvt_pk_bf16_f32 v86, v192, v193\n"
      "v_cvt_pk_bf16_f32 v87, v194, v195\n"
      "ds_write_b128 v210, v[84:87] offset:17408\n"
      "v_lshlrev_b32 v188, 16, v88\n"
      "v_and_b32 v189, 0xffff0000, v88\n"
      "v_lshlrev_b32 v190, 16, v89\n"
      "v_and_b32 v191, 0xffff0000, v89\n"
      "v_lshlrev_b32 v192, 16, v90\n"
      "v_and_b32 v193, 0xffff0000, v90\n"
      "v_lshlrev_b32 v194, 16, v91\n"
      "v_and_b32 v195, 0xffff0000, v91\n"
      "v_pk_mul_f32 v[188:189], v[188:189], s[94:95]\n"
      "v_pk_mul_f32 v[190:191], v[190:191], s[94:95]\n"
      "v_pk_mul_f32 v[192:193], v[192:193], s[94:95]\n"
      "v_pk_mul_f32 v[194:195], v[194:195], s[94:95]\n"
      "v_cvt_pk_bf16_f32 v88, v188, v189\n"
      "v_cvt_pk_bf16_f32 v89, v190, v191\n"
      "v_cvt_pk_bf16_f32 v90, v192, v193\n"
      "v_cvt_pk_bf16_f32 v91, v194, v195\n"
      "ds_write_b128 v210, v[88:91] offset:18432\n"
      "v_lshlrev_b32 v188, 16, v92\n"
      "v_and_b32 v189, 0xffff0000, v92\n"
      "v_lshlrev_b32 v190, 16, v93\n"
      "v_and_b32 v191, 0xffff0000, v93\n"
      "v_lshlrev_b32 v192, 16, v94\n"
      "v_and_b32 v193, 0xffff0000, v94\n"
      "v_lshlrev_b32 v194, 16, v95\n"
      "v_and_b32 v195, 0xffff0000, v95\n"
      "v_pk_mul_f32 v[188:189], v[188:189], s[94:95]\n"
      "v_pk_mul_f32 v[190:191], v[190:191], s[94:95]\n"
      "v_pk_mul_f32 v[192:193], v[192:193], s[94:95]\n"
      "v_pk_mul_f32 v[194:195], v[194:195], s[94:95]\n"
      "v_cvt_pk_bf16_f32 v92, v188, v189\n"
      "v_cvt_pk_bf16_f32 v93, v190, v191\n"
      "v_cvt_pk_bf16_f32 v94, v192, v193\n"
      "v_cvt_pk_bf16_f32 v95, v194, v195\n"
      "ds_write_b128 v210, v[92:95] offset:19456\n"
      "s_mov_b32 m0, s62\n"
      "s_nop 0\n"
      "buffer_load_dwordx4 v248, s[56:59], s60 offen nt lds\n"
      "buffer_load_dwordx4 v248, s[56:59], s60 offen offset:2048 nt lds\n"
      "s_bitset1_b32 m0, 10\n"
      "s_nop 0\n"
      "buffer_load_dwordx4 v249, s[56:59], s60 offen nt lds\n"
      "buffer_load_dwordx4 v249, s[56:59], s60 offen offset:2048 nt lds\n"
      "s_xor_b64 s[56:57], s[56:57], s[32:33]\n"
      "s_bitset1_b32 s61, 15\n"
      "s_add_u32 s61, s61, 0x2000\n"
      "s_bitset0_b32 s61, 15\n"
      "s_xor_b32 s60, s61, s51\n"
      "s_add_u32 s62, s62, 0x4000\n"
      "s_and_b32 s62, s62, 0x1ffff\n"
      "s_cmp_eq_u32 s50, 0\n"
      "s_cselect_b32 s38, 0x41554001, 1\n"
      "s_cselect_b32 s39, 0x3f440083, 3\n"
      "s_cselect_b32 s52, 0x3133aa90, 0\n"
      "s_cselect_b32 s53, 0x80011, 0\n"
      "s_mov_b64 s[34:35], 0\n"
      "s_and_b32 s69, %[blk], 7\n"
      "s_mul_i32 s69, s69, 5\n"
      "s_add_u32 s69, s69, s44\n"
      "s_and_b32 s69, s69, 15\n"
      "s_mul_i32 s74, s69, 20\n"
      "s_lshr_b32 s74, s74, 6\n"
      "s_lshl2_add_u32 s76, s74, 9\n"
      "s_bitset1_b64 s[34:35], s76\n"
      "s_lshl_b32 s74, s74, 1\n"
      "s_sub_u32 s74, 28, s74\n"
      "s_bitset1_b32 s39, s74\n"
      "s_mul_i32 s74, s69, 24\n"
      "s_lshr_b32 s74, s74, 6\n"
      "s_lshl2_add_u32 s76, s74, 29\n"
      "s_bitset1_b64 s[34:35], s76\n"
      "s_lshl_b32 s74, s74, 1\n"
      "s_sub_u32 s74, 18, s74\n"
      "s_bitset1_b32 s39, s74\n"
      "s_mul_i32 s74, s69, 12\n"
      "s_lshr_b32 s74, s74, 6\n"
      "s_lshl2_add_u32 s76, s74, 53\n"
      "s_bitset1_b64 s[34:35], s76\n"
      "s_lshl_b32 s74, s74, 1\n"
      "s_sub_u32 s74, 6, s74\n"
      "s_bitset1_b32 s39, s74\n"
      "s_mov_b32 s54, 3\n"
      "s_mov_b32 s70, 250\n"
      "s_mov_b32 s86, 59\n"
      "s_mov_b32 s77, 177\n"
      "s_lshl_b32 s91, s50, 16\n"
      "s_sub_u32 s80, 46, s91\n"
      "s_sub_u32 s90, 34, s91\n"
      "s_sub_u32 s72, s75, 14336\n"
      "s_and_b32 s79, %[blk], 31\n"
      "s_lshl_b32 s79, s79, 8\n"
      "s_add_u32 s79, s79, 8192\n"
      "s_and_b32 s69, %[blk], 7\n"
      "s_lshl_b32 s69, s69, 2\n"
      "s_and_b32 s74, %[blk], 0xe0\n"
      "s_add_u32 s69, s69, s74\n"
      "s_mul_i32 s69, s69, 42\n"
      "s_lshr_b32 s69, s69, 8\n"
      "s_add_u32 s66, s93, 135\n"
      "s_sub_u32 s81, s93, s71\n"
      "s_sub_u32 s81, s81, s69\n"
      "s_add_u32 s81, s81, 11\n"
      "s_sub_u32 s66, s66, s91\n"
      "s_mov_b32 s88, s66\n"
      "s_mov_b32 s65, s66\n"
      "s_getpc_b64 s[84:85]\n"
      "PC_%=:\n"
      "s_add_u32 s84, s84, HOOK_%=-PC_%=\n"
      "s_addc_u32 s85, s85, 0\n"
      "s_waitcnt vmcnt(14) lgkmcnt(0)\n"
      "s_barrier\n"
      "ds_read_b128 a[64:67], v198 offset:8192\n"
      "ds_read_b128 a[72:75], v199 offset:8192\n"
      "ds_read_b128 a[80:83], v198 offset:9216\n"
      "ds_read_b128 a[88:91], v199 offset:9216\n"
      "ds_read_b128 a[0:3], v196 offset:0\n"
      "ds_read_b128 a[16:19], v197 offset:0\n"
      "ds_read_b128 a[32:35], v196 offset:1024\n"
      "ds_read_b128 a[48:51], v197 offset:1024\n"
      "ds_read_b128 a[4:7], v196 offset:4096\n"
      "ds_read_b128 a[20:23], v197 offset:4096\n"
      "ds_read_b128 a[36:39], v196 offset:5120\n"
      "ds_read_b128 a[52:55], v197 offset:5120\n"
      "v_readfirstlane_b32 s40, v208\n"
      "s_mov_b32 s94, 0xceabfb8f\n"
      "v_lshlrev_b32 v144, 16, v200\n"
      "v_and_b32 v145, 0xffff0000, v200\n"
      "v_lshlrev_b32 v146, 16, v201\n"
      "v_and_b32 v147, 0xffff0000, v201\n"
      "v_lshlrev_b32 v148, 16, v202\n"
      "v_and_b32 v149, 0xffff0000, v202\n"
      "v_lshlrev_b32 v150, 16, v203\n"
      "v_and_b32 v151, 0xffff0000, v203\n"
      "v_lshlrev_b32 v152, 16, v204\n"
      "v_and_b32 v153, 0xffff0000, v204\n"
      "v_lshlrev_b32 v154, 16, v205\n"
      "v_and_b32 v155, 0xffff0000, v205\n"
      "v_lshlrev_b32 v156, 16, v206\n"
      "v_and_b32 v157, 0xffff0000, v206\n"
      "v_lshlrev_b32 v158, 16, v207\n"
      "v_and_b32 v159, 0xffff0000, v207\n"
      "s_waitcnt lgkmcnt(4)\n"
      "v_mfma_f32_16x16x32_bf16 v[64:67], a[64:67], a[0:3], 0\n"
      "ds_read_b128 a[8:11], v214 offset:0\n"
      "v_fma_f32 v144, -v144, s94, s94\n"
      "v_mfma_f32_16x16x32_bf16 v[64:67], a[72:75], a[16:19], v[64:67]\n"
      "ds_read_b128 a[24:27], v215 offset:0\n"
      "v_fma_f32 v145, -v145, s94, s94\n"
      "v_mfma_f32_16x16x32_bf16 v[64:67], a[80:83], a[32:35], v[64:67]\n"
      "ds_read_b128 a[40:43], v214 offset:1024\n"
      "v_fma_f32 v146, -v146, s94, s94\n"
      "v_mfma_f32_16x16x32_bf16 v[64:67], a[88:91], a[48:51], v[64:67]\n"
      "ds_read_b128 a[56:59], v215 offset:1024\n"
      "v_fma_f32 v147, -v147, s94, s94\n"
      "s_waitcnt lgkmcnt(4)\n"
      "v_mfma_f32_16x16x32_bf16 v[72:75], a[64:67], a[4:7], 0\n"
      "ds_read_b128 a[12:15], v214 offset:4096\n"
      "v_fma_f32 v148, -v148, s94, s94\n"
      "v_mfma_f32_16x16x32_bf16 v[72:75], a[72:75], a[20:23], v[72:75]\n"
      "ds_read_b128 a[28:31], v215 offset:4096\n"
      "v_fma_f32 v149, -v149, s94, s94\n"
      "v_mfma_f32_16x16x32_bf16 v[72:75], a[80:83], a[36:39], v[72:75]\n"
      "ds_read_b128 a[44:47], v214 offset:5120\n"
      "v_fma_f32 v150, -v150, s94, s94\n"
      "v_mfma_f32_16x16x32_bf16 v[72:75], a[88:91], a[52:55], v[72:75]\n"
      "ds_read_b128 a[60:63], v215 offset:5120\n"
      "v_fma_f32 v151, -v151, s94, s94\n"
      "s_waitcnt lgkmcnt(4)\n"
      "v_mfma_f32_16x16x32_bf16 v[80:83], a[64:67], a[8:11], 0\n"
      "ds_read_b128 a[96:99], v198 offset:0\n"
      "v_fma_f32 v152, -v152, s94, s94\n"
      "v_mfma_f32_16x16x32_bf16 v[80:83], a[72:75], a[24:27], v[80:83]\n"
      "ds_read_b128 a[100:103], v198 offset:4096\n"
      "v_fma_f32 v153, -v153, s94, s94\n"
      "v_mfma_f32_16x16x32_bf16 v[80:83], a[80:83], a[40:43], v[80:83]\n"
      "ds_read_b128 a[104:107], v199 offset:0\n"
      "v_fma_f32 v154, -v154, s94, s94\n"
      "v_mfma_f32_16x16x32_bf16 v[80:83], a[88:91], a[56:59], v[80:83]\n"
      "ds_read_b128 a[108:111], v199 offset:4096\n"
      "v_fma_f32 v155, -v155, s94, s94\n"
      "s_waitcnt lgkmcnt(4)\n"
      "v_mfma_f32_16x16x32_bf16 v[88:91], a[64:67], a[12:15], 0\n"
      "ds_read_b128 a[112:115], v198 offset:1024\n"
      "v_fma_f32 v156, -v156, s94, s94\n"
      "v_mfma_f32_16x16x32_bf16 v[88:91], a[72:75], a[28:31], v[88:91]\n"
      "ds_read_b128 a[116:119], v198 offset:5120\n"
      "v_fma_f32 v157, -v157, s94, s94\n"
      "v_mfma_f32_16x16x32_bf16 v[88:91], a[80:83], a[44:47], v[88:91]\n"
      "ds_read_b128 a[120:123], v199 offset:1024\n"
      "v_fma_f32 v158, -v158, s94, s94\n"
      "v_mfma_f32_16x16x32_bf16 v[88:91], a[88:91], a[60:63], v[88:91]\n"
      "ds_read_b128 a[124:127], v199 offset:5120\n"
      "v_fma_f32 v159, -v159, s94, s94\n"
      "s_mov_b32 m0, s62\n"
      "s_nop 0\n"
      "buffer_load_dwordx4 v248, s[56:59], s60 offen nt lds\n"
      "buffer_load_dwordx4 v248, s[56:59], s60 offen offset:2048 nt lds\n"
      "s_bitset1_b32 m0, 10\n"
      "s_nop 0\n"
      "buffer_load_dwordx4 v249, s[56:59], s60 offen nt lds\n"
      "buffer_load_dwordx4 v249, s[56:59], s60 offen offset:2048 nt lds\n"
      "s_waitcnt lgkmcnt(0)\n"
      "v_mfma_f32_16x16x32_bf16 v[96:99], a[96:99], a[0:3], v[144:147]\n"
      "s_add_u32 s40, s40, 0x7f4a7c15\n"
      "v_mfma_f32_16x16x32_bf16 v[96:99], a[104:107], a[16:19], v[96:99]\n"
      "s_lshl_b32 s69, s44, 3\n"
      "v_max3_f32 v188, v64, v65, v66\n"
      "v_max3_f32 v189, v72, v73, v74\n"
      "v_mfma_f32_16x16x32_bf16 v[96:99], a[112:115], a[32:35], v[96:99]\n"
      "s_lshr_b32 s74, s50, 12\n"
      "v_max3_f32 v190, v80, v81, v82\n"
      "v_max3_f32 v191, v88, v89, v90\n"
      "v_mfma_f32_16x16x32_bf16 v[96:99], a[120:123], a[48:51], v[96:99]\n"
      "s_add_u32 s69, s69, s74\n"
      "v_max_f32 v188, v188, v67\n"
      "v_max_f32 v189, v189, v75\n"
      "v_mfma_f32_16x16x32_bf16 v[104:107], a[96:99], a[4:7], v[152:155]\n"
      "s_mul_i32 s69, s69, 4608\n"
      "v_max_f32 v190, v190, v83\n"
      "v_max_f32 v191, v191, v91\n"
      "v_mfma_f32_16x16x32_bf16 v[104:107], a[104:107], a[20:23], v[104:107]\n"
      "s_add_u32 s69, s69, 81920\n"
      "v_mov_b32 v192, v188\n"
      "v_mov_b32 v193, v189\n"
      "v_mfma_f32_16x16x32_bf16 v[104:107], a[112:115], a[36:39], v[104:107]\n"
      "s_add_u32 s24, s18, s69\n"
      "v_mov_b32 v194, v190\n"
      "v_mov_b32 v195, v191\n"
      "v_mfma_f32_16x16x32_bf16 v[104:107], a[120:123], a[52:55], v[104:107]\n"
      "s_addc_u32 s25, s19, 0\n"
      "v_permlane16_swap_b32 v188, v192\n"
      "v_permlane16_swap_b32 v189, v193\n"
      "v_mfma_f32_16x16x32_bf16 v[112:115], a[96:99], a[8:11], v[144:147]\n"
      "s_movk_i32 s26, 23040\n"
      "v_permlane16_swap_b32 v190, v194\n"
      "v_permlane16_swap_b32 v191, v195\n"
      "v_mfma_f32_16x16x32_bf16 v[112:115], a[104:107], a[24:27], v[112:115]\n"
      "s_mul_i32 s78, s43, 18432\n"
      "v_max_f32 v188, v188, v192\n"
      "v_max_f32 v189, v189, v193\n"
      "v_mfma_f32_16x16x32_bf16 v[112:115], a[112:115], a[40:43], v[112:115]\n"
      "s_and_b32 s69, s50, 4096\n"
      "v_max_f32 v190, v190, v194\n"
      "v_max_f32 v191, v191, v195\n"
      "v_mfma_f32_16x16x32_bf16 v[112:115], a[120:123], a[56:59], v[112:115]\n"
      "s_lshl_b32 s69, s69, 2\n"
      "v_mov_b32 v192, v188\n"
      "v_mov_b32 v193, v189\n"
      "v_mfma_f32_16x16x32_bf16 v[120:123], a[96:99], a[12:15], v[152:155]\n"
      "s_and_b32 s74, s50, 8192\n"
      "v_mov_b32 v194, v190\n"
      "v_mov_b32 v195, v191\n"
      "v_mfma_f32_16x16x32_bf16 v[120:123], a[104:107], a[28:31], v[120:123]\n"
      "s_add_u32 s74, s74, 4096\n"
      "v_permlane32_swap_b32 v188, v192\n"
      "v_permlane32_swap_b32 v189, v193\n"
      "v_mfma_f32_16x16x32_bf16 v[120:123], a[112:115], a[44:47], v[120:123]\n"
      "s_add_u32 s69, s69, s74\n"
      "v_permlane32_swap_b32 v190, v194\n"
      "v_permlane32_swap_b32 v191, v195\n"
      "v_mfma_f32_16x16x32_bf16 v[120:123], a[120:123], a[60:63], v[120:123]\n"
      "s_lshl_b32 s74, s43, 7\n"
      "v_max_f32 v188, v188, v192\n"
      "v_max_f32 v189, v189, v193\n"
      "v_mfma_f32_16x16x32_bf16 v[100:103], a[100:103], a[0:3], v[148:151]\n"
      "s_add_u32 s69, s69, s74\n"
      "v_max_f32 v190, v190, v194\n"
      "v_max_f32 v191, v191, v195\n"
      "v_mfma_f32_16x16x32_bf16 v[100:103], a[108:111], a[16:19], v[100:103]\n"
      "s_lshl_b32 s74, s44, 15\n"
      "v_xor_b32 v176, 0x80000000, v188\n"
      "v_xor_b32 v177, 0x80000000, v189\n"
      "v_mfma_f32_16x16x32_bf16 v[100:103], a[116:119], a[32:35], v[100:103]\n"
      "s_add_u32 s69, s69, s74\n"
      "v_xor_b32 v178, 0x80000000, v190\n"
      "v_xor_b32 v179, 0x80000000, v191\n"
      "v_mfma_f32_16x16x32_bf16 v[100:103], a[124:127], a[48:51], v[100:103]\n"
      "s_add_u32 s30, s16, s69\n"
      "v_mov_b32 v160, v176\n"
      "v_mov_b32 v161, v176\n"
      "v_mfma_f32_16x16x32_bf16 v[108:111], a[100:103], a[4:7], v[156:159]\n"
      "s_addc_u32 s31, s17, 0\n"
      "v_mov_b32 v162, v176\n"
      "v_mov_b32 v163, v176\n"
      "v_mfma_f32_16x16x32_bf16 v[108:111], a[108:111], a[20:23], v[108:111]\n"
      "v_mov_b32 v164, v177\n"
      "v_mov_b32 v165, v177\n"
      "v_mfma_f32_16x16x32_bf16 v[108:111], a[116:119], a[36:39], v[108:111]\n"
      "v_mov_b32 v166, v177\n"
      "v_mov_b32 v167, v177\n"
      "v_mfma_f32_16x16x32_bf16 v[108:111], a[124:127], a[52:55], v[108:111]\n"
      "v_mov_b32 v168, v178\n"
      "v_mov_b32 v169, v178\n"
      "v_mfma_f32_16x16x32_bf16 v[116:119], a[100:103], a[8:11], v[148:151]\n"
      "v_mov_b32 v170, v178\n"
      "v_mov_b32 v171, v178\n"
      "v_mfma_f32_16x16x32_bf16 v[116:119], a[108:111], a[24:27], v[116:119]\n"
      "v_mov_b32 v172, v179\n"
      "v_mov_b32 v173, v179\n"
      "v_mfma_f32_16x16x32_bf16 v[116:119], a[116:119], a[40:43], v[116:119]\n"
      "v_mov_b32 v174, v179\n"
      "v_mov_b32 v175, v179\n"
      "v_mfma_f32_16x16x32_bf16 v[116:119], a[124:127], a[56:59], v[116:119]\n"
      "v_mov_b32 v209, 139248\n"
      "v_lshl_add_u32 v232, s50, 1, v241\n"
      "v_mfma_f32_16x16x32_bf16 v[124:127], a[100:103], a[12:15], v[156:159]\n"
      "v_add_u32 v232, 0x10000, v232\n"
      "v_xor_b32 v233, 0x4000, v232\n"
      "v_mfma_f32_16x16x32_bf16 v[124:127], a[108:111], a[28:31], v[124:127]\n"
      "v_add_u32 v234, 0x10000, v232\n"
      "v_xor_b32 v235, 0x4000, v234\n"
      "v_mfma_f32_16x16x32_bf16 v[124:127], a[116:119], a[44:47], v[124:127]\n"
      "v_lshrrev_b32 v240, 1, v241\n"
      "v_add_u32 v240, 0x1000, v240\n"
      "v_mfma_f32_16x16x32_bf16 v[124:127], a[124:127], a[60:63], v[124:127]\n"
      "v_mov_b32 v196, 0\n"
      "v_pk_add_f32 v[96:97], v[96:97], v[160:161]\n"
      "v_pk_add_f32 v[98:99], v[98:99], v[160:161]\n"
      "v_pk_add_f32 v[104:105], v[104:105], v[164:165]\n"
      "v_pk_add_f32 v[106:107], v[106:107], v[164:165]\n"
      "v_pk_add_f32 v[112:113], v[112:113], v[168:169]\n"
      "v_pk_add_f32 v[114:115], v[114:115], v[168:169]\n"
      "v_pk_add_f32 v[120:121], v[120:121], v[172:173]\n"
      "v_pk_add_f32 v[122:123], v[122:123], v[172:173]\n"
      "v_pk_add_f32 v[100:101], v[100:101], v[160:161]\n"
      "v_pk_add_f32 v[102:103], v[102:103], v[160:161]\n"
      "v_pk_add_f32 v[108:109], v[108:109], v[164:165]\n"
      "v_pk_add_f32 v[110:111], v[110:111], v[164:165]\n"
      "v_pk_add_f32 v[116:117], v[116:117], v[168:169]\n"
      "v_pk_add_f32 v[118:119], v[118:119], v[168:169]\n"
      "v_pk_add_f32 v[124:125], v[124:125], v[172:173]\n"
      "v_pk_add_f32 v[126:127], v[126:127], v[172:173]\n"
      "v_mov_b32 v224, v244\n"
      "v_mov_b32 v225, v245\n"
      "v_mov_b32 v226, v246\n"
      "v_mov_b32 v227, v247\n"
      "v_xor_b32 v220, 0x2000, v244\n"
      "v_xor_b32 v221, 0x2000, v245\n"
      "v_xor_b32 v222, 0x2000, v246\n"
      "v_xor_b32 v223, 0x2000, v247\n"
      "s_lshl_b32 s76, s43, 3\n"
      "v_mov_b32 v237, s40\n"
      "v_mov_b32 v238, s76\n"
      "v_xor_b32 v239, 8, v238\n"
      "s_memrealtime s[68:69]\n"
      "s_waitcnt vmcnt(14) lgkmcnt(0)\n"
      "s_sub_u32 s67, s68, s88\n"
      "s_cmp_lt_i32 s67, -8\n"
      "s_cbranch_scc1 GW0_%=\n"
      "GX0_%=:\n"
      "s_barrier\n"
      "ds_read_b128 a[64:67], v242 offset:0\n"
      "ds_read_b128 a[68:71], v242 offset:4096\n"
      "ds_read_b128 a[72:75], v243 offset:0\n"
      "ds_read_b128 a[76:79], v243 offset:4096\n"
      "ds_read_b128 a[80:83], v242 offset:1024\n"
      "ds_read_b128 a[84:87], v242 offset:5120\n"
      "ds_read_b128 a[88:91], v243 offset:1024\n"
      "ds_read_b128 a[92:95], v243 offset:5120\n"
      "ds_write_b32 v209, v196\n"
      "s_add_u32 s38, s38, s38\n"
      "s_memrealtime s[68:69]\n"
      "s_xor_b64 s[56:57], s[56:57], s[32:33]\n"
      "s_bitset1_b32 s61, 15\n"
      "s_add_u32 s61, s61, 0x2000\n"
      "s_bitset0_b32 s61, 15\n"
      "s_xor_b32 s60, s61, s51\n"
      "s_add_u32 s62, s62, 0x4000\n"
      "s_and_b32 s62, s62, 0x1ffff\n"
      "s_waitcnt lgkmcnt(0)\n"
      "s_sub_u32 s67, s68, s66\n"
      "s_cmp_gt_i32 s67, s70\n"
      "s_cselect_b32 s69, s77, s86\n"
      "s_cselect_b32 s70, 60, s70\n"
      "s_cselect_b32 s90, s80, s90\n"
      "s_add_u32 s66, s66, s69\n"
      "s_mov_b32 s82, s68\n"
      "LOOP_%=:\n"
      "v_mfma_f32_16x16x32_bf16 v[64:67], a[64:67], a[0:3], v[160:163]\n"
      "s_add_u32 s65, s68, s90\n"
      "v_exp_f32 v188, v96\n"
      "v_mfma_f32_16x16x32_bf16 v[64:67], a[72:75], a[16:19], v[64:67]\n"
      "s_max_u32 s88, s66, s65\n"
      "v_add_f32 v180, v180, v184\n"
      "v_add_f32 v181, v181, v185\n"
      "v_mfma_f32_16x16x32_bf16 v[64:67], a[80:83], a[32:35], v[64:67]\n"
      "v_exp_f32 v189, v97\n"
      "v_mfma_f32_16x16x32_bf16 v[64:67], a[88:91], a[48:51], v[64:67]\n"
      "v_add_f32 v182, v182, v186\n"
      "v_add_f32 v183, v183, v187\n"
      "v_mfma_f32_16x16x32_bf16 v[72:75], a[64:67], a[4:7], v[164:167]\n"
      "v_exp_f32 v190, v98\n"
      "v_mfma_f32_16x16x32_bf16 v[72:75], a[72:75], a[20:23], v[72:75]\n"
      "v_exp_f32 v191, v99\n"
      "v_mfma_f32_16x16x32_bf16 v[72:75], a[80:83], a[36:39], v[72:75]\n"
      "v_cvt_pk_bf16_f32 v144, v188, v189\n"
      "v_cvt_pk_bf16_f32 v145, v190, v191\n"
      "v_mfma_f32_16x16x32_bf16 v[72:75], a[88:91], a[52:55], v[72:75]\n"
      "v_exp_f32 v192, v100\n"
      "v_mfma_f32_16x16x32_bf16 v[80:83], a[64:67], a[8:11], v[168:171]\n"
      "s_add_u32 s39, s39, s39\n"
      "v_exp_f32 v193, v101\n"
      "v_mfma_f32_16x16x32_bf16 v[80:83], a[72:75], a[24:27], v[80:83]\n"
      "s_memrealtime s[68:69]\n"
      "v_exp_f32 v194, v102\n"
      "v_mfma_f32_16x16x32_bf16 v[80:83], a[80:83], a[40:43], v[80:83]\n"
      "s_mov_b32 m0, s62\n"
      "v_exp_f32 v195, v103\n"
      "v_mfma_f32_16x16x32_bf16 v[80:83], a[88:91], a[56:59], v[80:83]\n"
      "buffer_load_dwordx4 v248, s[56:59], s60 offen nt lds\n"
      "v_mfma_f32_16x16x32_bf16 v[88:91], a[64:67], a[12:15], v[172:175]\n"
      "buffer_load_dwordx4 v248, s[56:59], s60 offen offset:2048 nt lds\n"
      "v_mfma_f32_16x16x32_bf16 v[88:91], a[72:75], a[28:31], v[88:91]\n"
      "s_bitset1_b32 m0, 10\n"
      "v_exp_f32 v188, v104\n"
      "v_mfma_f32_16x16x32_bf16 v[88:91], a[80:83], a[44:47], v[88:91]\n"
      "buffer_load_dwordx4 v249, s[56:59], s60 offen nt lds\n"
      "v_mfma_f32_16x16x32_bf16 v[88:91], a[88:91], a[60:63], v[88:91]\n"
      "buffer_load_dwordx4 v249, s[56:59], s60 offen offset:2048 nt lds\n"
      "s_cbranch_scc1 SP1_%=\n"
      "HD1_%=:\n"
      "s_cmp_lg_u32 s54, 0\n"
      "s_waitcnt vmcnt(12) lgkmcnt(0)\n"
      "s_cbranch_scc1 W1_%=\n"
      "s_waitcnt vmcnt(8)\n"
      "W1_%=:\n"
      "s_sub_u32 s67, s68, s88\n"
      "s_cmp_lt_i32 s67, -8\n"
      "s_cbranch_scc1 GW4_%=\n"
      "GX4_%=:\n"
      "s_barrier\n"
      "v_mfma_f32_16x16x32_bf16 v[68:71], a[68:71], a[0:3], v[160:163]\n"
      "s_memrealtime s[68:69]\n"
      "ds_read_b64_tr_b16 a[96:97], v244 offset:0\n"
      "ds_read_b64_tr_b16 a[98:99], v244 offset:4096\n"
      "v_mfma_f32_16x16x32_bf16 v[68:71], a[76:79], a[16:19], v[68:71]\n"
      "s_xor_b64 s[56:57], s[56:57], s[32:33]\n"
      "ds_read_b64_tr_b16 a[100:101], v245 offset:0\n"
      "ds_read_b64_tr_b16 a[102:103], v245 offset:4096\n"
      "v_mfma_f32_16x16x32_bf16 v[68:71], a[84:87], a[32:35], v[68:71]\n"
      "s_bitset1_b32 s61, 15\n"
      "ds_read_b64_tr_b16 a[104:105], v246 offset:0\n"
      "ds_read_b64_tr_b16 a[106:107], v246 offset:4096\n"
      "v_mfma_f32_16x16x32_bf16 v[68:71], a[92:95], a[48:51], v[68:71]\n"
      "s_add_u32 s61, s61, 0x2000\n"
      "ds_read_b64_tr_b16 a[108:109], v247 offset:0\n"
      "ds_read_b64_tr_b16 a[110:111], v247 offset:4096\n"
      "v_mfma_f32_16x16x32_bf16 v[76:79], a[68:71], a[4:7], v[164:167]\n"
      "s_bitset0_b32 s61, 15\n"
      "v_exp_f32 v189, v105\n"
      "v_mfma_f32_16x16x32_bf16 v[76:79], a[76:79], a[20:23], v[76:79]\n"
      "s_xor_b32 s60, s61, s51\n"
      "v_cvt_pk_bf16_f32 v146, v192, v193\n"
      "v_cvt_pk_bf16_f32 v147, v194, v195\n"
      "v_mfma_f32_16x16x32_bf16 v[76:79], a[84:87], a[36:39], v[76:79]\n"
      "s_add_u32 s62, s62, 0x4000\n"
      "v_exp_f32 v190, v106\n"
      "v_mfma_f32_16x16x32_bf16 v[184:187], a[128:131], v[144:147], 0\n"
      "s_and_b32 s62, s62, 0x1ffff\n"
      "v_exp_f32 v191, v107\n"
      "v_mfma_f32_16x16x32_bf16 v[76:79], a[92:95], a[52:55], v[76:79]\n"
      "v_cvt_pk_bf16_f32 v148, v188, v189\n"
      "v_cvt_pk_bf16_f32 v149, v190, v191\n"
      "v_mfma_f32_16x16x32_bf16 v[84:87], a[68:71], a[8:11], v[168:171]\n"
      "v_exp_f32 v192, v108\n"
      "v_mfma_f32_16x16x32_bf16 v[84:87], a[76:79], a[24:27], v[84:87]\n"
      "v_exp_f32 v193, v109\n"
      "s_waitcnt lgkmcnt(0)\n"
      "v_mfma_f32_16x16x32_bf16 v[84:87], a[84:87], a[40:43], v[84:87]\n"
      "s_sub_u32 s67, s68, s66\n"
      "ds_read_b64_tr_b16 a[112:113], v244 offset:1024\n"
      "v_cvt_pk_bf16_f32 v150, v192, v193\n"
      "v_mfma_f32_16x16x32_bf16 v[84:87], a[92:95], a[56:59], v[84:87]\n"
      "s_cmp_gt_i32 s67, s70\n"
      "v_exp_f32 v194, v110\n"
      "v_mfma_f32_16x16x32_bf16 v[92:95], a[68:71], a[12:15], v[172:175]\n"
      "s_cselect_b32 s69, s77, s86\n"
      "ds_read_b64_tr_b16 a[114:115], v244 offset:5120\n"
      "ds_read_b64_tr_b16 a[116:117], v245 offset:1024\n"
      "v_mfma_f32_16x16x32_bf16 v[92:95], a[76:79], a[28:31], v[92:95]\n"
      "s_cselect_b32 s70, 60, s70\n"
      "v_exp_f32 v195, v111\n"
      "v_mfma_f32_16x16x32_bf16 v[92:95], a[84:87], a[44:47], v[92:95]\n"
      "s_cselect_b32 s90, s80, s90\n"
      "ds_read_b64_tr_b16 a[118:119], v245 offset:5120\n"
      "v_cvt_pk_bf16_f32 v151, v194, v195\n"
      "v_mfma_f32_16x16x32_bf16 v[92:95], a[92:95], a[60:63], v[92:95]\n"
      "s_add_u32 s66, s66, s69\n"
      "v_exp_f32 v188, v112\n"
      "v_mfma_f32_16x16x32_bf16 v[184:187], a[132:135], v[148:151], v[184:187]\n"
      "s_mov_b32 s82, s68\n"
      "ds_read_b64_tr_b16 a[120:121], v246 offset:1024\n"
      "ds_read_b64_tr_b16 a[122:123], v246 offset:5120\n"
      "v_mfma_f32_16x16x32_bf16 v[0:3], a[96:99], v[128:131], v[0:3]\n"
      "s_add_u32 s65, s68, s90\n"
      "v_exp_f32 v189, v113\n"
      "v_mfma_f32_16x16x32_bf16 v[4:7], a[96:99], v[132:135], v[4:7]\n"
      "s_max_u32 s88, s66, s65\n"
      "ds_read_b64_tr_b16 a[124:125], v247 offset:1024\n"
      "v_cvt_pk_bf16_f32 v152, v188, v189\n"
      "v_mfma_f32_16x16x32_bf16 a[144:147], a[96:99], v[136:139], a[144:147]\n"
      "v_exp_f32 v190, v114\n"
      "v_mfma_f32_16x16x32_bf16 a[148:151], a[96:99], v[140:143], a[148:151]\n"
      "v_exp_f32 v191, v115\n"
      "v_mfma_f32_16x16x32_bf16 a[156:159], a[100:103], v[140:143], a[156:159]\n"
      "ds_read_b64_tr_b16 a[126:127], v247 offset:5120\n"
      "v_cvt_pk_bf16_f32 v153, v190, v191\n"
      "v_mfma_f32_16x16x32_bf16 a[152:155], a[100:103], v[136:139], a[152:155]\n"
      "v_exp_f32 v192, v116\n"
      "v_mfma_f32_16x16x32_bf16 v[12:15], a[100:103], v[132:135], v[12:15]\n"
      "v_exp_f32 v193, v117\n"
      "v_mfma_f32_16x16x32_bf16 v[8:11], a[100:103], v[128:131], v[8:11]\n"
      "v_exp_f32 v194, v118\n"
      "v_mfma_f32_16x16x32_bf16 v[16:19], a[104:107], v[128:131], v[16:19]\n"
      "s_add_u32 s38, s38, s38\n"
      "v_exp_f32 v195, v119\n"
      "v_mfma_f32_16x16x32_bf16 v[20:23], a[104:107], v[132:135], v[20:23]\n"
      "s_memrealtime s[68:69]\n"
      "v_cvt_pk_bf16_f32 v154, v192, v193\n"
      "v_cvt_pk_bf16_f32 v155, v194, v195\n"
      "v_mfma_f32_16x16x32_bf16 a[160:163], a[104:107], v[136:139], a[160:163]\n"
      "s_mov_b32 m0, s62\n"
      "v_exp_f32 v188, v120\n"
      "v_mfma_f32_16x16x32_bf16 v[184:187], a[136:139], v[152:155], v[184:187]\n"
      "buffer_load_dwordx4 v248, s[56:59], s60 offen nt lds\n"
      "v_mfma_f32_16x16x32_bf16 a[164:167], a[104:107], v[140:143], a[164:167]\n"
      "buffer_load_dwordx4 v248, s[56:59], s60 offen offset:2048 nt lds\n"
      "v_mfma_f32_16x16x32_bf16 a[172:175], a[108:111], v[140:143], a[172:175]\n"
      "s_bitset1_b32 m0, 10\n"
      "v_exp_f32 v189, v121\n"
      "v_mfma_f32_16x16x32_bf16 a[168:171], a[108:111], v[136:139], a[168:171]\n"
      "buffer_load_dwordx4 v249, s[56:59], s60 offen nt lds\n"
      "v_mfma_f32_16x16x32_bf16 v[28:31], a[108:111], v[132:135], v[28:31]\n"
      "buffer_load_dwordx4 v249, s[56:59], s60 offen offset:2048 nt lds\n"
      "s_cbranch_scc1 SP2_%=\n"
      "HD2_%=:\n"
      "s_cmp_lg_u32 s54, 0\n"
      "s_waitcnt vmcnt(12) lgkmcnt(0)\n"
      "s_cbranch_scc1 W2_%=\n"
      "s_waitcnt vmcnt(8)\n"
      "W2_%=:\n"
      "s_sub_u32 s67, s68, s88\n"
      "s_cmp_lt_i32 s67, -8\n"
      "s_cbranch_scc1 GW5_%=\n"
      "GX5_%=:\n"
      "s_barrier\n"
      "v_mfma_f32_16x16x32_bf16 v[24:27], a[108:111], v[128:131], v[24:27]\n"
      "s_memrealtime s[68:69]\n"
      "ds_read_b128 a[64:67], v242 offset:32768\n"
      "ds_read_b128 a[72:75], v243 offset:32768\n"
      "v_mfma_f32_16x16x32_bf16 v[32:35], a[112:115], v[128:131], v[32:35]\n"
      "s_xor_b64 s[56:57], s[56:57], s[32:33]\n"
      "ds_read_b128 a[80:83], v242 offset:33792\n"
      "ds_read_b128 a[88:91], v243 offset:33792\n"
      "v_mfma_f32_16x16x32_bf16 v[36:39], a[112:115], v[132:135], v[36:39]\n"
      "s_bitset1_b32 s61, 15\n"
      "v_exp_f32 v190, v122\n"
      "v_mfma_f32_16x16x32_bf16 a[176:179], a[112:115], v[136:139], a[176:179]\n"
      "s_add_u32 s61, s61, 0x2000\n"
      "v_exp_f32 v191, v123\n"
      "v_mfma_f32_16x16x32_bf16 a[180:183], a[112:115], v[140:143], a[180:183]\n"
      "s_bitset0_b32 s61, 15\n"
      "v_cvt_pk_bf16_f32 v156, v188, v189\n"
      "v_cvt_pk_bf16_f32 v157, v190, v191\n"
      "v_mfma_f32_16x16x32_bf16 a[188:191], a[116:119], v[140:143], a[188:191]\n"
      "s_xor_b32 s60, s61, s51\n"
      "v_exp_f32 v192, v124\n"
      "v_mfma_f32_16x16x32_bf16 a[184:187], a[116:119], v[136:139], a[184:187]\n"
      "s_add_u32 s62, s62, 0x4000\n"
      "v_exp_f32 v193, v125\n"
      "v_mfma_f32_16x16x32_bf16 v[44:47], a[116:119], v[132:135], v[44:47]\n"
      "s_and_b32 s62, s62, 0x1ffff\n"
      "v_exp_f32 v194, v126\n"
      "v_mfma_f32_16x16x32_bf16 v[40:43], a[116:119], v[128:131], v[40:43]\n"
      "v_exp_f32 v195, v127\n"
      "v_mfma_f32_16x16x32_bf16 v[48:51], a[120:123], v[128:131], v[48:51]\n"
      "v_cvt_pk_bf16_f32 v158, v192, v193\n"
      "v_cvt_pk_bf16_f32 v159, v194, v195\n"
      "v_mfma_f32_16x16x32_bf16 v[52:55], a[120:123], v[132:135], v[52:55]\n"
      "s_waitcnt lgkmcnt(0)\n"
      "v_mfma_f32_16x16x32_bf16 v[184:187], a[140:143], v[156:159], v[184:187]\n"
      "s_sub_u32 s67, s68, s66\n"
      "ds_read_b128 a[68:71], v242 offset:36864\n"
      "ds_read_b128 a[76:79], v243 offset:36864\n"
      "v_mfma_f32_16x16x32_bf16 a[192:195], a[120:123], v[136:139], a[192:195]\n"
      "s_cmp_gt_i32 s67, s70\n"
      "ds_read_b128 a[84:87], v242 offset:37888\n"
      "ds_read_b128 a[92:95], v243 offset:37888\n"
      "v_mfma_f32_16x16x32_bf16 a[196:199], a[120:123], v[140:143], a[196:199]\n"
      "s_cselect_b32 s69, s77, s86\n"
      "v_max3_f32 v196, v184, v185, v186\n"
      "v_max_f32 v196, v196, v187\n"
      "v_mfma_f32_16x16x32_bf16 a[204:207], a[124:127], v[140:143], a[204:207]\n"
      "s_cselect_b32 s70, 60, s70\n"
      "v_cmp_nge_f32 vcc, 0x4b800000, v196\n"
      "v_mfma_f32_16x16x32_bf16 a[200:203], a[124:127], v[136:139], a[200:203]\n"
      "s_cselect_b32 s90, s80, s90\n"
      "v_mfma_f32_16x16x32_bf16 v[60:63], a[124:127], v[132:135], v[60:63]\n"
      "s_add_u32 s66, s66, s69\n"
      "v_mfma_f32_16x16x32_bf16 v[56:59], a[124:127], v[128:131], v[56:59]\n"
      "s_mov_b32 s82, s68\n"
      "s_cbranch_vccnz FS1_%=\n"
      "FR1_%=:\n"
      "v_mfma_f32_16x16x32_bf16 v[96:99], a[64:67], a[0:3], v[160:163]\n"
      "s_add_u32 s65, s68, s90\n"
      "v_exp_f32 v188, v64\n"
      "v_mfma_f32_16x16x32_bf16 v[96:99], a[72:75], a[16:19], v[96:99]\n"
      "s_max_u32 s88, s66, s65\n"
      "v_add_f32 v180, v180, v184\n"
      "v_add_f32 v181, v181, v185\n"
      "v_mfma_f32_16x16x32_bf16 v[96:99], a[80:83], a[32:35], v[96:99]\n"
      "v_exp_f32 v189, v65\n"
      "v_mfma_f32_16x16x32_bf16 v[96:99], a[88:91], a[48:51], v[96:99]\n"
      "v_add_f32 v182, v182, v186\n"
      "v_add_f32 v183, v183, v187\n"
      "v_mfma_f32_16x16x32_bf16 v[104:107], a[64:67], a[4:7], v[164:167]\n"
      "v_exp_f32 v190, v66\n"
      "v_mfma_f32_16x16x32_bf16 v[104:107], a[72:75], a[20:23], v[104:107]\n"
      "v_exp_f32 v191, v67\n"
      "v_mfma_f32_16x16x32_bf16 v[104:107], a[80:83], a[36:39], v[104:107]\n"
      "v_cvt_pk_bf16_f32 v128, v188, v189\n"
      "v_cvt_pk_bf16_f32 v129, v190, v191\n"
      "v_mfma_f32_16x16x32_bf16 v[104:107], a[88:91], a[52:55], v[104:107]\n"
      "v_exp_f32 v192, v68\n"
      "v_mfma_f32_16x16x32_bf16 v[112:115], a[64:67], a[8:11], v[168:171]\n"
      "s_add_u32 s39, s39, s39\n"
      "v_exp_f32 v193, v69\n"
      "v_mfma_f32_16x16x32_bf16 v[112:115], a[72:75], a[24:27], v[112:115]\n"
      "s_memrealtime s[68:69]\n"
      "v_exp_f32 v194, v70\n"
      "v_mfma_f32_16x16x32_bf16 v[112:115], a[80:83], a[40:43], v[112:115]\n"
      "s_mov_b32 m0, s62\n"
      "v_exp_f32 v195, v71\n"
      "v_mfma_f32_16x16x32_bf16 v[112:115], a[88:91], a[56:59], v[112:115]\n"
      "buffer_load_dwordx4 v248, s[56:59], s60 offen nt lds\n"
      "v_mfma_f32_16x16x32_bf16 v[120:123], a[64:67], a[12:15], v[172:175]\n"
      "buffer_load_dwordx4 v248, s[56:59], s60 offen offset:2048 nt lds\n"
      "v_mfma_f32_16x16x32_bf16 v[120:123], a[72:75], a[28:31], v[120:123]\n"
      "s_bitset1_b32 m0, 10\n"
      "v_exp_f32 v188, v72\n"
      "v_mfma_f32_16x16x32_bf16 v[120:123], a[80:83], a[44:47], v[120:123]\n"
      "buffer_load_dwordx4 v249, s[56:59], s60 offen nt lds\n"
      "v_mfma_f32_16x16x32_bf16 v[120:123], a[88:91], a[60:63], v[120:123]\n"
      "buffer_load_dwordx4 v249, s[56:59], s60 offen offset:2048 nt lds\n"
      "s_cbranch_scc1 SP3_%=\n"
      "HD3_%=:\n"
      "s_cmp_lg_u32 s54, 0\n"
      "s_waitcnt vmcnt(12) lgkmcnt(0)\n"
      "s_cbranch_scc1 W3_%=\n"
      "s_waitcnt vmcnt(8)\n"
      "W3_%=:\n"
      "s_sub_u32 s67, s68, s88\n"
      "s_cmp_lt_i32 s67, -8\n"
      "s_cbranch_scc1 GW6_%=\n"
      "GX6_%=:\n"
      "s_barrier\n"
      "v_mfma_f32_16x16x32_bf16 v[100:103], a[68:71], a[0:3], v[160:163]\n"
      "s_memrealtime s[68:69]\n"
      "ds_read_b64_tr_b16 a[96:97], v244 offset:32768\n"
      "ds_read_b64_tr_b16 a[98:99], v244 offset:36864\n"
      "v_mfma_f32_16x16x32_bf16 v[100:103], a[76:79], a[16:19], v[100:103]\n"
      "s_xor_b64 s[56:57], s[56:57], s[32:33]\n"
      "ds_read_b64_tr_b16 a[100:101], v245 offset:32768\n"
      "ds_read_b64_tr_b16 a[102:103], v245 offset:36864\n"
      "v_mfma_f32_16x16x32_bf16 v[100:103], a[84:87], a[32:35], v[100:103]\n"
      "s_bitset1_b32 s61, 15\n"
      "ds_read_b64_tr_b16 a[104:105], v246 offset:32768\n"
      "ds_read_b64_tr_b16 a[106:107], v246 offset:36864\n"
      "v_mfma_f32_16x16x32_bf16 v[100:103], a[92:95], a[48:51], v[100:103]\n"
      "s_add_u32 s61, s61, 0x2000\n"
      "ds_read_b64_tr_b16 a[108:109], v247 offset:32768\n"
      "ds_read_b64_tr_b16 a[110:111], v247 offset:36864\n"
      "v_mfma_f32_16x16x32_bf16 v[108:111], a[68:71], a[4:7], v[164:167]\n"
      "s_bitset0_b32 s61, 15\n"
      "v_exp_f32 v189, v73\n"
      "v_mfma_f32_16x16x32_bf16 v[108:111], a[76:79], a[20:23], v[108:111]\n"
      "s_xor_b32 s60, s61, s51\n"
      "v_cvt_pk_bf16_f32 v130, v192, v193\n"
      "v_cvt_pk_bf16_f32 v131, v194, v195\n"
      "v_mfma_f32_16x16x32_bf16 v[108:111], a[84:87], a[36:39], v[108:111]\n"
      "s_add_u32 s62, s62, 0x4000\n"
      "v_exp_f32 v190, v74\n"
      "v_mfma_f32_16x16x32_bf16 v[184:187], a[128:131], v[128:131], 0\n"
      "s_and_b32 s62, s62, 0x1ffff\n"
      "v_xor_b32 v242, 0x10000, v242\n"
      "v_xor_b32 v243, 0x10000, v243\n"
      "v_mfma_f32_16x16x32_bf16 v[108:111], a[92:95], a[52:55], v[108:111]\n"
      "v_exp_f32 v191, v75\n"
      "v_mfma_f32_16x16x32_bf16 v[116:119], a[68:71], a[8:11], v[168:171]\n"
      "v_cvt_pk_bf16_f32 v132, v188, v189\n"
      "v_cvt_pk_bf16_f32 v133, v190, v191\n"
      "v_mfma_f32_16x16x32_bf16 v[116:119], a[76:79], a[24:27], v[116:119]\n"
      "v_exp_f32 v192, v76\n"
      "s_waitcnt lgkmcnt(0)\n"
      "v_mfma_f32_16x16x32_bf16 v[116:119], a[84:87], a[40:43], v[116:119]\n"
      "s_sub_u32 s67, s68, s66\n"
      "ds_read_b64_tr_b16 a[112:113], v244 offset:33792\n"
      "ds_read_b64_tr_b16 a[114:115], v244 offset:37888\n"
      "v_mfma_f32_16x16x32_bf16 v[116:119], a[92:95], a[56:59], v[116:119]\n"
      "s_cmp_gt_i32 s67, s70\n"
      "v_exp_f32 v193, v77\n"
      "v_mfma_f32_16x16x32_bf16 v[124:127], a[68:71], a[12:15], v[172:175]\n"
      "s_cselect_b32 s69, s77, s86\n"
      "ds_read_b64_tr_b16 a[116:117], v245 offset:33792\n"
      "v_cvt_pk_bf16_f32 v134, v192, v193\n"
      "v_mfma_f32_16x16x32_bf16 v[124:127], a[76:79], a[28:31], v[124:127]\n"
      "s_cselect_b32 s70, 60, s70\n"
      "v_exp_f32 v194, v78\n"
      "v_mfma_f32_16x16x32_bf16 v[124:127], a[84:87], a[44:47], v[124:127]\n"
      "s_cselect_b32 s90, s80, s90\n"
      "ds_read_b64_tr_b16 a[118:119], v245 offset:37888\n"
      "ds_read_b64_tr_b16 a[120:121], v246 offset:33792\n"
      "v_mfma_f32_16x16x32_bf16 v[124:127], a[92:95], a[60:63], v[124:127]\n"
      "s_add_u32 s66, s66, s69\n"
      "v_exp_f32 v195, v79\n"
      "v_mfma_f32_16x16x32_bf16 v[0:3], a[96:99], v[144:147], v[0:3]\n"
      "s_mov_b32 s82, s68\n"
      "ds_read_b64_tr_b16 a[122:123], v246 offset:37888\n"
      "v_cvt_pk_bf16_f32 v135, v194, v195\n"
      "v_mfma_f32_16x16x32_bf16 v[4:7], a[96:99], v[148:151], v[4:7]\n"
      "s_add_u32 s65, s68, s90\n"
      "v_exp_f32 v188, v80\n"
      "v_mfma_f32_16x16x32_bf16 v[184:187], a[132:135], v[132:135], v[184:187]\n"
      "s_max_u32 s88, s66, s65\n"
      "ds_read_b64_tr_b16 a[124:125], v247 offset:33792\n"
      "ds_read_b64_tr_b16 a[126:127], v247 offset:37888\n"
      "v_mfma_f32_16x16x32_bf16 a[144:147], a[96:99], v[152:155], a[144:147]\n"
      "v_exp_f32 v189, v81\n"
      "v_mfma_f32_16x16x32_bf16 a[148:151], a[96:99], v[156:159], a[148:151]\n"
      "v_exp_f32 v190, v82\n"
      "v_mfma_f32_16x16x32_bf16 a[156:159], a[100:103], v[156:159], a[156:159]\n"
      "v_exp_f32 v191, v83\n"
      "v_mfma_f32_16x16x32_bf16 a[152:155], a[100:103], v[152:155], a[152:155]\n"
      "v_cvt_pk_bf16_f32 v136, v188, v189\n"
      "v_cvt_pk_bf16_f32 v137, v190, v191\n"
      "v_mfma_f32_16x16x32_bf16 v[12:15], a[100:103], v[148:151], v[12:15]\n"
      "v_exp_f32 v192, v84\n"
      "v_mfma_f32_16x16x32_bf16 v[8:11], a[100:103], v[144:147], v[8:11]\n"
      "v_exp_f32 v193, v85\n"
      "v_mfma_f32_16x16x32_bf16 v[16:19], a[104:107], v[144:147], v[16:19]\n"
      "s_add_u32 s38, s38, s38\n"
      "v_exp_f32 v194, v86\n"
      "v_mfma_f32_16x16x32_bf16 v[20:23], a[104:107], v[148:151], v[20:23]\n"
      "s_memrealtime s[68:69]\n"
      "v_exp_f32 v195, v87\n"
      "v_mfma_f32_16x16x32_bf16 a[160:163], a[104:107], v[152:155], a[160:163]\n"
      "s_mov_b32 m0, s62\n"
      "v_cvt_pk_bf16_f32 v138, v192, v193\n"
      "v_cvt_pk_bf16_f32 v139, v194, v195\n"
      "v_mfma_f32_16x16x32_bf16 a[164:167], a[104:107], v[156:159], a[164:167]\n"
      "buffer_load_dwordx4 v248, s[56:59], s60 offen nt lds\n"
      "v_mfma_f32_16x16x32_bf16 v[184:187], a[136:139], v[136:139], v[184:187]\n"
      "buffer_load_dwordx4 v248, s[56:59], s60 offen offset:2048 nt lds\n"
      "v_mfma_f32_16x16x32_bf16 a[172:175], a[108:111], v[156:159], a[172:175]\n"
      "s_bitset1_b32 m0, 10\n"
      "v_exp_f32 v188, v88\n"
      "v_mfma_f32_16x16x32_bf16 a[168:171], a[108:111], v[152:155], a[168:171]\n"
      "buffer_load_dwordx4 v249, s[56:59], s60 offen nt lds\n"
      "v_mfma_f32_16x16x32_bf16 v[28:31], a[108:111], v[148:151], v[28:31]\n"
      "buffer_load_dwordx4 v249, s[56:59], s60 offen offset:2048 nt lds\n"
      "HD0_%=:\n"
      "s_cmp_lg_u32 s54, 0\n"
      "s_waitcnt vmcnt(12) lgkmcnt(0)\n"
      "s_cbranch_scc1 W4_%=\n"
      "s_waitcnt vmcnt(8)\n"
      "W4_%=:\n"
      "s_sub_u32 s67, s68, s88\n"
      "s_cmp_lt_i32 s67, -8\n"
      "s_cbranch_scc1 GW7_%=\n"
      "GX7_%=:\n"
      "s_barrier\n"
      "v_mfma_f32_16x16x32_bf16 v[24:27], a[108:111], v[144:147], v[24:27]\n"
      "s_memrealtime s[68:69]\n"
      "ds_read_b128 a[64:67], v242 offset:0\n"
      "ds_read_b128 a[72:75], v243 offset:0\n"
      "v_mfma_f32_16x16x32_bf16 v[32:35], a[112:115], v[144:147], v[32:35]\n"
      "s_xor_b64 s[56:57], s[56:57], s[32:33]\n"
      "ds_read_b128 a[80:83], v242 offset:1024\n"
      "ds_read_b128 a[88:91], v243 offset:1024\n"
      "v_mfma_f32_16x16x32_bf16 v[36:39], a[112:115], v[148:151], v[36:39]\n"
      "s_bitset1_b32 s61, 15\n"
      "v_exp_f32 v189, v89\n"
      "v_mfma_f32_16x16x32_bf16 a[176:179], a[112:115], v[152:155], a[176:179]\n"
      "s_add_u32 s61, s61, 0x2000\n"
      "v_exp_f32 v190, v90\n"
      "v_mfma_f32_16x16x32_bf16 a[180:183], a[112:115], v[156:159], a[180:183]\n"
      "s_bitset0_b32 s61, 15\n"
      "v_exp_f32 v191, v91\n"
      "v_mfma_f32_16x16x32_bf16 a[188:191], a[116:119], v[156:159], a[188:191]\n"
      "s_xor_b32 s60, s61, s51\n"
      "v_cvt_pk_bf16_f32 v140, v188, v189\n"
      "v_cvt_pk_bf16_f32 v141, v190, v191\n"
      "v_mfma_f32_16x16x32_bf16 a[184:187], a[116:119], v[152:155], a[184:187]\n"
      "s_add_u32 s62, s62, 0x4000\n"
      "v_exp_f32 v192, v92\n"
      "v_mfma_f32_16x16x32_bf16 v[44:47], a[116:119], v[148:151], v[44:47]\n"
      "s_and_b32 s62, s62, 0x1ffff\n"
      "v_exp_f32 v193, v93\n"
      "v_mfma_f32_16x16x32_bf16 v[40:43], a[116:119], v[144:147], v[40:43]\n"
      "v_exp_f32 v194, v94\n"
      "v_mfma_f32_16x16x32_bf16 v[48:51], a[120:123], v[144:147], v[48:51]\n"
      "v_exp_f32 v195, v95\n"
      "v_mfma_f32_16x16x32_bf16 v[52:55], a[120:123], v[148:151], v[52:55]\n"
      "v_cvt_pk_bf16_f32 v142, v192, v193\n"
      "v_cvt_pk_bf16_f32 v143, v194, v195\n"
      "s_waitcnt lgkmcnt(0)\n"
      "v_mfma_f32_16x16x32_bf16 a[192:195], a[120:123], v[152:155], a[192:195]\n"
      "s_sub_u32 s67, s68, s66\n"
      "v_xor_b32 v244, 0x10000, v244\n"
      "v_xor_b32 v245, 0x10000, v245\n"
      "v_mfma_f32_16x16x32_bf16 v[184:187], a[140:143], v[140:143], v[184:187]\n"
      "s_cmp_gt_i32 s67, s70\n"
      "ds_read_b128 a[68:71], v242 offset:4096\n"
      "ds_read_b128 a[76:79], v243 offset:4096\n"
      "v_mfma_f32_16x16x32_bf16 a[196:199], a[120:123], v[156:159], a[196:199]\n"
      "s_cselect_b32 s69, s77, s86\n"
      "ds_read_b128 a[84:87], v242 offset:5120\n"
      "ds_read_b128 a[92:95], v243 offset:5120\n"
      "v_mfma_f32_16x16x32_bf16 a[204:207], a[124:127], v[156:159], a[204:207]\n"
      "s_cselect_b32 s70, 60, s70\n"
      "v_xor_b32 v246, 0x10000, v246\n"
      "v_xor_b32 v247, 0x10000, v247\n"
      "v_mfma_f32_16x16x32_bf16 a[200:203], a[124:127], v[152:155], a[200:203]\n"
      "s_cselect_b32 s90, s80, s90\n"
      "v_max3_f32 v196, v184, v185, v186\n"
      "v_max_f32 v196, v196, v187\n"
      "v_mfma_f32_16x16x32_bf16 v[60:63], a[124:127], v[148:151], v[60:63]\n"
      "s_add_u32 s66, s66, s69\n"
      "v_cmp_nge_f32 vcc, 0x4b800000, v196\n"
      "v_mfma_f32_16x16x32_bf16 v[56:59], a[124:127], v[144:147], v[56:59]\n"
      "s_mov_b32 s82, s68\n"
      "s_cbranch_vccnz FS2_%=\n"
      "FR2_%=:\n"
      "s_branch LOOP_%=\n"
      "SP1_%=:\n"
      "s_ff1_i32_b32 s64, s39\n"
      "s_lshl1_add_u32 s64, s64, 1\n"
      "s_bitcmp1_b64 s[52:53], s64\n"
      "s_cbranch_scc0 SH1_%=\n"
      "s_swappc_b64 s[84:85], s[84:85]\n"
      "SH1_%=:\n"
      "s_cmp_lt_u32 s64, 63\n"
      "s_cbranch_scc1 HD1_%=\n"
      "s_xor_b64 s[56:57], s[56:57], s[32:33]\n"
      "s_bitset1_b32 s61, 15\n"
      "s_add_u32 s61, s61, 0x2000\n"
      "s_bitset0_b32 s61, 15\n"
      "s_xor_b32 s60, s61, s51\n"
      "s_add_u32 s62, s62, 0x4000\n"
      "s_and_b32 s62, s62, 0x1ffff\n"
      "v_add_u32 v248, s51, v249\n"
      "s_mov_b32 s51, 0\n"
      "v_xor_b32 v249, 0x4000, v248\n"
      "s_branch HD1_%=\n"
      "SP2_%=:\n"
      "s_cmp_eq_u32 s38, 0\n"
      "s_cbranch_scc1 L62_%=\n"
      "s_ff1_i32_b32 s64, s38\n"
      "s_lshl1_add_u32 s64, s64, 0\n"
      "s_bitcmp1_b64 s[52:53], s64\n"
      "s_cbranch_scc0 SH2_%=\n"
      "s_swappc_b64 s[84:85], s[84:85]\n"
      "SH2_%=:\n"
      "s_branch HD2_%=\n"
      "L62_%=:\n"
      "s_xor_b64 s[56:57], s[56:57], s[32:33]\n"
      "s_sub_u32 s61, s61, 0x2000\n"
      "v_xor_b32 v248, 128, v248\n"
      "v_xor_b32 v249, 128, v249\n"
      "s_mov_b32 s38, 0x80000000\n"
      "s_branch HD2_%=\n"
      "SP3_%=:\n"
      "s_cmp_eq_u32 s39, 0\n"
      "s_cbranch_scc1 NH_%=\n"
      "s_ff1_i32_b32 s64, s39\n"
      "s_lshl1_add_u32 s64, s64, 1\n"
      "s_bitcmp1_b64 s[52:53], s64\n"
      "s_cbranch_scc0 SH3_%=\n"
      "s_swappc_b64 s[84:85], s[84:85]\n"
      "SH3_%=:\n"
      "s_bitcmp1_b64 s[34:35], s64\n"
      "s_cbranch_scc0 HD3_%=\n"
      "NH_%=:\n"
      "s_cmp_lg_u32 s54, 0\n"
      "s_waitcnt vmcnt(12) lgkmcnt(0)\n"
      "s_cbranch_scc1 W5_%=\n"
      "s_waitcnt vmcnt(8)\n"
      "W5_%=:\n"
      "s_barrier\n"
      "v_mfma_f32_16x16x32_bf16 v[100:103], a[68:71], a[0:3], v[160:163]\n"
      "s_sub_u32 s54, s54, 1\n"
      "ds_read_b64_tr_b16 a[96:97], v244 offset:32768\n"
      "ds_read_b64_tr_b16 a[98:99], v244 offset:36864\n"
      "v_mfma_f32_16x16x32_bf16 v[100:103], a[76:79], a[16:19], v[100:103]\n"
      "ds_read_b64_tr_b16 a[100:101], v245 offset:32768\n"
      "ds_read_b64_tr_b16 a[102:103], v245 offset:36864\n"
      "v_mfma_f32_16x16x32_bf16 v[100:103], a[84:87], a[32:35], v[100:103]\n"
      "ds_read_b64_tr_b16 a[104:105], v246 offset:32768\n"
      "ds_read_b64_tr_b16 a[106:107], v246 offset:36864\n"
      "v_mfma_f32_16x16x32_bf16 v[100:103], a[92:95], a[48:51], v[100:103]\n"
      "ds_read_b64_tr_b16 a[108:109], v247 offset:32768\n"
      "ds_read_b64_tr_b16 a[110:111], v247 offset:36864\n"
      "v_mfma_f32_16x16x32_bf16 v[108:111], a[68:71], a[4:7], v[164:167]\n"
      "v_exp_f32 v189, v73\n"
      "v_mfma_f32_16x16x32_bf16 v[108:111], a[76:79], a[20:23], v[108:111]\n"
      "v_cvt_pk_bf16_f32 v130, v192, v193\n"
      "v_cvt_pk_bf16_f32 v131, v194, v195\n"
      "v_mfma_f32_16x16x32_bf16 v[108:111], a[84:87], a[36:39], v[108:111]\n"
      "v_exp_f32 v190, v74\n"
      "v_mfma_f32_16x16x32_bf16 v[184:187], a[128:131], v[128:131], 0\n"
      "v_xor_b32 v242, 0x10000, v242\n"
      "v_xor_b32 v243, 0x10000, v243\n"
      "v_mfma_f32_16x16x32_bf16 v[108:111], a[92:95], a[52:55], v[108:111]\n"
      "v_exp_f32 v191, v75\n"
      "v_mfma_f32_16x16x32_bf16 v[116:119], a[68:71], a[8:11], v[168:171]\n"
      "v_cvt_pk_bf16_f32 v132, v188, v189\n"
      "v_cvt_pk_bf16_f32 v133, v190, v191\n"
      "v_mfma_f32_16x16x32_bf16 v[116:119], a[76:79], a[24:27], v[116:119]\n"
      "v_exp_f32 v192, v76\n"
      "s_waitcnt lgkmcnt(0)\n"
      "v_mfma_f32_16x16x32_bf16 v[116:119], a[84:87], a[40:43], v[116:119]\n"
      "ds_read_b64_tr_b16 a[112:113], v244 offset:33792\n"
      "ds_read_b64_tr_b16 a[114:115], v244 offset:37888\n"
      "v_mfma_f32_16x16x32_bf16 v[116:119], a[92:95], a[56:59], v[116:119]\n"
      "v_exp_f32 v193, v77\n"
      "v_mfma_f32_16x16x32_bf16 v[124:127], a[68:71], a[12:15], v[172:175]\n"
      "ds_read_b64_tr_b16 a[116:117], v245 offset:33792\n"
      "v_cvt_pk_bf16_f32 v134, v192, v193\n"
      "v_mfma_f32_16x16x32_bf16 v[124:127], a[76:79], a[28:31], v[124:127]\n"
      "v_exp_f32 v194, v78\n"
      "v_mfma_f32_16x16x32_bf16 v[124:127], a[84:87], a[44:47], v[124:127]\n"
      "ds_read_b64_tr_b16 a[118:119], v245 offset:37888\n"
      "ds_read_b64_tr_b16 a[120:121], v246 offset:33792\n"
      "v_mfma_f32_16x16x32_bf16 v[124:127], a[92:95], a[60:63], v[124:127]\n"
      "v_exp_f32 v195, v79\n"
      "v_mfma_f32_16x16x32_bf16 v[0:3], a[96:99], v[144:147], v[0:3]\n"
      "ds_read_b64_tr_b16 a[122:123], v246 offset:37888\n"
      "v_cvt_pk_bf16_f32 v135, v194, v195\n"
      "v_mfma_f32_16x16x32_bf16 v[4:7], a[96:99], v[148:151], v[4:7]\n"
      "v_exp_f32 v188, v80\n"
      "v_mfma_f32_16x16x32_bf16 v[184:187], a[132:135], v[132:135], v[184:187]\n"
      "ds_read_b64_tr_b16 a[124:125], v247 offset:33792\n"
      "ds_read_b64_tr_b16 a[126:127], v247 offset:37888\n"
      "v_mfma_f32_16x16x32_bf16 a[144:147], a[96:99], v[152:155], a[144:147]\n"
      "v_exp_f32 v189, v81\n"
      "v_mfma_f32_16x16x32_bf16 a[148:151], a[96:99], v[156:159], a[148:151]\n"
      "v_exp_f32 v190, v82\n"
      "v_mfma_f32_16x16x32_bf16 a[156:159], a[100:103], v[156:159], a[156:159]\n"
      "v_exp_f32 v191, v83\n"
      "v_mfma_f32_16x16x32_bf16 a[152:155], a[100:103], v[152:155], a[152:155]\n"
      "v_cvt_pk_bf16_f32 v136, v188, v189\n"
      "v_cvt_pk_bf16_f32 v137, v190, v191\n"
      "v_mfma_f32_16x16x32_bf16 v[12:15], a[100:103], v[148:151], v[12:15]\n"
      "v_exp_f32 v192, v84\n"
      "v_mfma_f32_16x16x32_bf16 v[8:11], a[100:103], v[144:147], v[8:11]\n"
      "v_exp_f32 v193, v85\n"
      "v_mfma_f32_16x16x32_bf16 v[16:19], a[104:107], v[144:147], v[16:19]\n"
      "s_add_u32 s38, s38, s38\n"
      "v_exp_f32 v194, v86\n"
      "v_mfma_f32_16x16x32_bf16 v[20:23], a[104:107], v[148:151], v[20:23]\n"
      "s_memrealtime s[68:69]\n"
      "v_exp_f32 v195, v87\n"
      "v_mfma_f32_16x16x32_bf16 a[160:163], a[104:107], v[152:155], a[160:163]\n"
      "v_cvt_pk_bf16_f32 v138, v192, v193\n"
      "v_cvt_pk_bf16_f32 v139, v194, v195\n"
      "v_mfma_f32_16x16x32_bf16 a[164:167], a[104:107], v[156:159], a[164:167]\n"
      "s_nop 0\n"
      "v_mfma_f32_16x16x32_bf16 v[184:187], a[136:139], v[136:139], v[184:187]\n"
      "s_nop 0\n"
      "v_mfma_f32_16x16x32_bf16 a[172:175], a[108:111], v[156:159], a[172:175]\n"
      "v_exp_f32 v188, v88\n"
      "v_mfma_f32_16x16x32_bf16 a[168:171], a[108:111], v[152:155], a[168:171]\n"
      "s_nop 0\n"
      "v_mfma_f32_16x16x32_bf16 v[28:31], a[108:111], v[148:151], v[28:31]\n"
      "s_nop 0\n"
      "s_cbranch_scc0 MH_%=\n"
      "END_%=:\n"
      "s_waitcnt lgkmcnt(0)\n"
      "v_mfma_f32_16x16x32_bf16 v[24:27], a[108:111], v[144:147], v[24:27]\n"
      "v_mov_b32 v236, s68\n"
      "v_exp_f32 v189, v89\n"
      "v_mfma_f32_16x16x32_bf16 v[32:35], a[112:115], v[144:147], v[32:35]\n"
      "s_mov_b64 exec, 1\n"
      "global_store_dwordx2 v238, v[236:237], s[46:47] offset:8 sc1\n"
      "s_mov_b64 exec, -1\n"
      "v_mfma_f32_16x16x32_bf16 v[36:39], a[112:115], v[148:151], v[36:39]\n"
      "s_mov_b32 s82, s68\n"
      "v_exp_f32 v190, v90\n"
      "v_mfma_f32_16x16x32_bf16 a[176:179], a[112:115], v[152:155], a[176:179]\n"
      "v_exp_f32 v191, v91\n"
      "v_mfma_f32_16x16x32_bf16 a[180:183], a[112:115], v[156:159], a[180:183]\n"
      "v_cvt_pk_bf16_f32 v140, v188, v189\n"
      "v_cvt_pk_bf16_f32 v141, v190, v191\n"
      "v_mfma_f32_16x16x32_bf16 a[188:191], a[116:119], v[156:159], a[188:191]\n"
      "v_exp_f32 v192, v92\n"
      "s_waitcnt vmcnt(5)\n"
      "s_barrier\n"
      "v_mfma_f32_16x16x32_bf16 a[184:187], a[116:119], v[152:155], a[184:187]\n"
      "ds_read_b64_tr_b16 a[80:81], v224 offset:0\n"
      "ds_read_b64_tr_b16 a[82:83], v224 offset:4096\n"
      "v_mfma_f32_16x16x32_bf16 v[44:47], a[116:119], v[148:151], v[44:47]\n"
      "ds_read_b64_tr_b16 a[84:85], v225 offset:0\n"
      "ds_read_b64_tr_b16 a[86:87], v225 offset:4096\n"
      "v_mfma_f32_16x16x32_bf16 v[40:43], a[116:119], v[144:147], v[40:43]\n"
      "s_mov_b32 s74, 0xffff0000\n"
      "ds_read_b64_tr_b16 a[88:89], v226 offset:0\n"
      "ds_read_b64_tr_b16 a[90:91], v226 offset:4096\n"
      "v_mfma_f32_16x16x32_bf16 v[48:51], a[120:123], v[144:147], v[48:51]\n"
      "s_mov_b32 s75, 0xffff0000\n"
      "ds_read_b64_tr_b16 a[92:93], v227 offset:0\n"
      "ds_read_b64_tr_b16 a[94:95], v227 offset:4096\n"
      "v_mfma_f32_16x16x32_bf16 v[52:55], a[120:123], v[148:151], v[52:55]\n"
      "s_mov_b32 s94, 0\n"
      "v_exp_f32 v193, v93\n"
      "v_mfma_f32_16x16x32_bf16 a[192:195], a[120:123], v[152:155], a[192:195]\n"
      "s_mov_b32 s95, -1\n"
      "ds_read_b64_tr_b16 a[16:17], v224 offset:1024\n"
      "v_cvt_pk_bf16_f32 v142, v192, v193\n"
      "v_mfma_f32_16x16x32_bf16 a[196:199], a[120:123], v[156:159], a[196:199]\n"
      "v_exp_f32 v194, v94\n"
      "v_mfma_f32_16x16x32_bf16 a[204:207], a[124:127], v[156:159], a[204:207]\n"
      "ds_read_b64_tr_b16 a[18:19], v224 offset:5120\n"
      "ds_read_b64_tr_b16 a[20:21], v225 offset:1024\n"
      "v_mfma_f32_16x16x32_bf16 a[200:203], a[124:127], v[152:155], a[200:203]\n"
      "v_exp_f32 v195, v95\n"
      "v_mfma_f32_16x16x32_bf16 v[60:63], a[124:127], v[148:151], v[60:63]\n"
      "ds_read_b64_tr_b16 a[22:23], v225 offset:5120\n"
      "v_cvt_pk_bf16_f32 v143, v194, v195\n"
      "v_mfma_f32_16x16x32_bf16 v[56:59], a[124:127], v[144:147], v[56:59]\n"
      "ds_read_b64_tr_b16 a[24:25], v226 offset:1024\n"
      "ds_read_b64_tr_b16 a[26:27], v226 offset:5120\n"
      "v_mfma_f32_16x16x32_bf16 v[184:187], a[140:143], v[140:143], v[184:187]\n"
      "ds_read_b64_tr_b16 a[28:29], v227 offset:1024\n"
      "ds_read_b64_tr_b16 a[30:31], v227 offset:5120\n"
      "ds_read_b64_tr_b16 a[0:1], v220 offset:1024\n"
      "ds_read_b64_tr_b16 a[2:3], v220 offset:5120\n"
      "ds_read_b64_tr_b16 a[4:5], v221 offset:1024\n"
      "ds_read_b64_tr_b16 a[6:7], v221 offset:5120\n"
      "ds_read_b64_tr_b16 a[8:9], v222 offset:1024\n"
      "ds_read_b64_tr_b16 a[10:11], v222 offset:5120\n"
      "ds_read_b64_tr_b16 a[12:13], v223 offset:1024\n"
      "ds_read_b64_tr_b16 a[14:15], v223 offset:5120\n"
      "v_max3_f32 v196, v184, v185, v186\n"
      "v_max_f32 v196, v196, v187\n"
      "v_cmp_nge_f32 vcc, 0x4b800000, v196\n"
      "s_cbranch_vccnz FS3_%=\n"
      "FR3_%=:\n"
      "s_waitcnt lgkmcnt(15)\n"
      "v_mfma_f32_16x16x32_bf16 a[176:179], a[80:83], v[136:139], a[176:179]\n"
      "v_add_f32 v180, v180, v184\n"
      "v_add_f32 v181, v181, v185\n"
      "v_mfma_f32_16x16x32_bf16 a[180:183], a[80:83], v[140:143], a[180:183]\n"
      "v_add_f32 v182, v182, v186\n"
      "v_add_f32 v183, v183, v187\n"
      "v_mfma_f32_16x16x32_bf16 a[184:187], a[84:87], v[136:139], a[184:187]\n"
      "v_exp_f32 v188, v112\n"
      "v_mfma_f32_16x16x32_bf16 a[188:191], a[84:87], v[140:143], a[188:191]\n"
      "ds_write_b128 v232, a[176:179] offset:0\n"
      "v_exp_f32 v189, v113\n"
      "v_mfma_f32_16x16x32_bf16 a[192:195], a[88:91], v[136:139], a[192:195]\n"
      "v_exp_f32 v190, v114\n"
      "v_mfma_f32_16x16x32_bf16 a[196:199], a[88:91], v[140:143], a[196:199]\n"
      "v_exp_f32 v191, v115\n"
      "v_mfma_f32_16x16x32_bf16 a[200:203], a[92:95], v[136:139], a[200:203]\n"
      "ds_write_b128 v232, a[180:183] offset:1024\n"
      "v_exp_f32 v192, v116\n"
      "v_mfma_f32_16x16x32_bf16 a[204:207], a[92:95], v[140:143], a[204:207]\n"
      "v_exp_f32 v193, v117\n"
      "v_exp_f32 v194, v118\n"
      "v_cvt_pk_bf16_f32 v152, v188, v189\n"
      "v_exp_f32 v195, v119\n"
      "v_cvt_pk_bf16_f32 v153, v190, v191\n"
      "ds_write_b128 v232, a[184:187] offset:2048\n"
      "v_exp_f32 v188, v120\n"
      "v_cvt_pk_bf16_f32 v154, v192, v193\n"
      "v_mfma_f32_16x16x32_bf16 v[32:35], a[80:83], v[128:131], v[32:35]\n"
      "v_exp_f32 v189, v121\n"
      "v_cvt_pk_bf16_f32 v155, v194, v195\n"
      "v_exp_f32 v190, v122\n"
      "v_cvt_pk_bf16_f32 v156, v188, v189\n"
      "v_mfma_f32_16x16x32_bf16 v[184:187], a[136:139], v[152:155], 0\n"
      "ds_write_b128 v234, v[152:155] offset:0\n"
      "v_exp_f32 v191, v123\n"
      "v_mfma_f32_16x16x32_bf16 v[36:39], a[80:83], v[132:135], v[36:39]\n"
      "v_exp_f32 v192, v124\n"
      "v_cvt_pk_bf16_f32 v157, v190, v191\n"
      "v_exp_f32 v193, v125\n"
      "v_exp_f32 v194, v126\n"
      "v_cvt_pk_bf16_f32 v158, v192, v193\n"
      "ds_write_b128 v232, a[188:191] offset:3072\n"
      "v_mfma_f32_16x16x32_bf16 v[40:43], a[84:87], v[128:131], v[40:43]\n"
      "v_exp_f32 v195, v127\n"
      "v_exp_f32 v188, v96\n"
      "v_cvt_pk_bf16_f32 v159, v194, v195\n"
      "v_exp_f32 v189, v97\n"
      "v_mfma_f32_16x16x32_bf16 v[44:47], a[84:87], v[132:135], v[44:47]\n"
      "v_exp_f32 v190, v98\n"
      "ds_write_b128 v234, v[156:159] offset:1024\n"
      "v_cvt_pk_bf16_f32 v144, v188, v189\n"
      "v_mfma_f32_16x16x32_bf16 v[184:187], a[140:143], v[156:159], v[184:187]\n"
      "v_exp_f32 v191, v99\n"
      "v_exp_f32 v192, v100\n"
      "v_cvt_pk_bf16_f32 v145, v190, v191\n"
      "v_mfma_f32_16x16x32_bf16 v[48:51], a[88:91], v[128:131], v[48:51]\n"
      "v_exp_f32 v193, v101\n"
      "ds_write_b128 v232, a[192:195] offset:4096\n"
      "v_exp_f32 v194, v102\n"
      "v_cvt_pk_bf16_f32 v146, v192, v193\n"
      "v_exp_f32 v195, v103\n"
      "v_mfma_f32_16x16x32_bf16 v[52:55], a[88:91], v[132:135], v[52:55]\n"
      "v_exp_f32 v188, v104\n"
      "v_cvt_pk_bf16_f32 v147, v194, v195\n"
      "v_exp_f32 v189, v105\n"
      "ds_write_b128 v232, a[196:199] offset:5120\n"
      "v_exp_f32 v190, v106\n"
      "v_cvt_pk_bf16_f32 v148, v188, v189\n"
      "v_mfma_f32_16x16x32_bf16 v[184:187], a[128:131], v[144:147], v[184:187]\n"
      "v_exp_f32 v191, v107\n"
      "v_exp_f32 v192, v108\n"
      "v_cvt_pk_bf16_f32 v149, v190, v191\n"
      "v_exp_f32 v193, v109\n"
      "ds_write_b128 v232, a[200:203] offset:6144\n"
      "v_mfma_f32_16x16x32_bf16 v[56:59], a[92:95], v[128:131], v[56:59]\n"
      "v_exp_f32 v194, v110\n"
      "v_cvt_pk_bf16_f32 v150, v192, v193\n"
      "v_exp_f32 v195, v111\n"
      "v_cndmask_b32 v229, v180, v181, s[74:75]\n"
      "v_cvt_pk_bf16_f32 v151, v194, v195\n"
      "v_mfma_f32_16x16x32_bf16 v[60:63], a[92:95], v[132:135], v[60:63]\n"
      "v_cndmask_b32 v200, v182, v183, s[74:75]\n"
      "ds_write_b128 v232, a[204:207] offset:7168\n"
      "v_cndmask_b32 v228, v176, v177, s[74:75]\n"
      "v_mfma_f32_16x16x32_bf16 v[184:187], a[132:135], v[148:151], v[184:187]\n"
      "s_nop 7\n"
      "v_max3_f32 v196, v184, v185, v186\n"
      "v_max_f32 v196, v196, v187\n"
      "v_cmp_nge_f32 vcc, 0x4b800000, v196\n"
      "s_cbranch_vccnz FS8_%=\n"
      "FR8_%=:\n"
      "v_cndmask_b32 v202, v186, v187, s[74:75]\n"
      "v_cndmask_b32 v201, v184, v185, s[74:75]\n"
      "v_add_f32 v200, v200, v202\n"
      "ds_write_b32 v234, v200 offset:4096\n"
      "v_add_f32 v229, v229, v201\n"
      "s_waitcnt lgkmcnt(8)\n"
      "v_mfma_f32_16x16x32_bf16 v[32:35], a[16:19], v[144:147], v[32:35]\n"
      "v_mfma_f32_16x16x32_bf16 v[36:39], a[16:19], v[148:151], v[36:39]\n"
      "v_mfma_f32_16x16x32_bf16 v[40:43], a[20:23], v[144:147], v[40:43]\n"
      "s_waitcnt lgkmcnt(0)\n"
      "s_barrier\n"
      "v_mfma_f32_16x16x32_bf16 v[44:47], a[20:23], v[148:151], v[44:47]\n"
      "ds_read_b32 v208, v209\n"
      "ds_read_b128 v[160:163], v235 offset:0\n"
      "v_mfma_f32_16x16x32_bf16 v[48:51], a[24:27], v[144:147], v[48:51]\n"
      "ds_read_b128 v[164:167], v235 offset:1024\n"
      "ds_read_b128 v[64:67], v233 offset:0\n"
      "v_mfma_f32_16x16x32_bf16 v[52:55], a[24:27], v[148:151], v[52:55]\n"
      "ds_read_b128 v[68:71], v233 offset:1024\n"
      "ds_read_b128 v[72:75], v233 offset:2048\n"
      "ds_read_b128 v[76:79], v233 offset:3072\n"
      "v_mfma_f32_16x16x32_bf16 v[56:59], a[28:31], v[144:147], v[56:59]\n"
      "ds_read_b128 v[80:83], v233 offset:4096\n"
      "ds_read_b128 v[84:87], v233 offset:5120\n"
      "v_mfma_f32_16x16x32_bf16 v[60:63], a[28:31], v[148:151], v[60:63]\n"
      "ds_read_b128 v[88:91], v233 offset:6144\n"
      "ds_read_b128 v[92:95], v233 offset:7168\n"
      "ds_read_b32 v204, v235 offset:4096\n"
      "s_waitcnt lgkmcnt(11)\n"
      "v_cmp_ne_u32 vcc, 0, v208\n"
      "s_cbranch_vccnz SLOW_%=\n"
      "AGAIN_%=:\n"
      "s_waitcnt lgkmcnt(9)\n"
      "v_mfma_f32_16x16x32_bf16 v[32:35], a[0:3], v[160:163], v[32:35]\n"
      "v_mfma_f32_16x16x32_bf16 v[36:39], a[0:3], v[164:167], v[36:39]\n"
      "v_mfma_f32_16x16x32_bf16 v[40:43], a[4:7], v[160:163], v[40:43]\n"
      "v_mfma_f32_16x16x32_bf16 v[44:47], a[4:7], v[164:167], v[44:47]\n"
      "v_mfma_f32_16x16x32_bf16 v[48:51], a[8:11], v[160:163], v[48:51]\n"
      "v_mfma_f32_16x16x32_bf16 v[52:55], a[8:11], v[164:167], v[52:55]\n"
      "v_mfma_f32_16x16x32_bf16 v[56:59], a[12:15], v[160:163], v[56:59]\n"
      "v_mfma_f32_16x16x32_bf16 v[60:63], a[12:15], v[164:167], v[60:63]\n"
      "global_load_dwordx2 v[206:207], v239, s[46:47] offset:8 sc1\n"
      "s_waitcnt lgkmcnt(7)\n"
      "v_pk_add_f32 v[32:33], v[32:33], v[64:65]\n"
      "v_pk_add_f32 v[34:35], v[34:35], v[66:67]\n"
      "v_pk_add_f32 v[36:37], v[36:37], v[68:69]\n"
      "v_pk_add_f32 v[38:39], v[38:39], v[70:71]\n"
      "v_cvt_pk_bf16_f32 v212, v32, v33\n"
      "v_cvt_pk_bf16_f32 v213, v34, v35\n"
      "v_cvt_pk_bf16_f32 v214, v36, v37\n"
      "v_cvt_pk_bf16_f32 v215, v38, v39\n"
      "buffer_store_dwordx4 v[212:215], v241, s[24:27], s78 offen offset:0 sc1\n"
      "s_waitcnt lgkmcnt(5)\n"
      "v_pk_add_f32 v[40:41], v[40:41], v[72:73]\n"
      "v_pk_add_f32 v[42:43], v[42:43], v[74:75]\n"
      "v_pk_add_f32 v[44:45], v[44:45], v[76:77]\n"
      "v_pk_add_f32 v[46:47], v[46:47], v[78:79]\n"
      "v_cvt_pk_bf16_f32 v216, v40, v41\n"
      "v_cvt_pk_bf16_f32 v217, v42, v43\n"
      "v_cvt_pk_bf16_f32 v218, v44, v45\n"
      "v_cvt_pk_bf16_f32 v219, v46, v47\n"
      "buffer_store_dwordx4 v[216:219], v241, s[24:27], s78 offen offset:1024 sc1\n"
      "s_waitcnt lgkmcnt(3)\n"
      "v_pk_add_f32 v[48:49], v[48:49], v[80:81]\n"
      "v_pk_add_f32 v[50:51], v[50:51], v[82:83]\n"
      "v_pk_add_f32 v[52:53], v[52:53], v[84:85]\n"
      "v_pk_add_f32 v[54:55], v[54:55], v[86:87]\n"
      "v_cvt_pk_bf16_f32 v220, v48, v49\n"
      "v_cvt_pk_bf16_f32 v221, v50, v51\n"
      "v_cvt_pk_bf16_f32 v222, v52, v53\n"
      "v_cvt_pk_bf16_f32 v223, v54, v55\n"
      "buffer_store_dwordx4 v[220:223], v241, s[24:27], s78 offen offset:2048 sc1\n"
      "s_waitcnt lgkmcnt(0)\n"
      "v_pk_add_f32 v[56:57], v[56:57], v[88:89]\n"
      "v_pk_add_f32 v[58:59], v[58:59], v[90:91]\n"
      "v_pk_add_f32 v[60:61], v[60:61], v[92:93]\n"
      "v_pk_add_f32 v[62:63], v[62:63], v[94:95]\n"
      "v_cvt_pk_bf16_f32 v224, v56, v57\n"
      "v_cvt_pk_bf16_f32 v225, v58, v59\n"
      "v_cvt_pk_bf16_f32 v226, v60, v61\n"
      "v_cvt_pk_bf16_f32 v227, v62, v63\n"
      "buffer_store_dwordx4 v[224:227], v241, s[24:27], s78 offen offset:3072 sc1\n"
      "v_add_f32 v229, v229, v204\n"
      "v_cndmask_b32 v230, v228, v229, s[94:95]\n"
      "v_add_u32 v231, s40, v230\n"
      "v_add3_u32 v231, v231, v212, v213\n"
      "v_add3_u32 v231, v231, v214, v215\n"
      "v_add3_u32 v231, v231, v216, v217\n"
      "v_add3_u32 v231, v231, v218, v219\n"
      "v_add3_u32 v231, v231, v220, v221\n"
      "v_add3_u32 v231, v231, v222, v223\n"
      "v_add3_u32 v231, v231, v224, v225\n"
      "v_add3_u32 v231, v231, v226, v227\n"
      "buffer_store_dwordx2 v[230:231], v240, s[24:27], s78 offen sc1\n"
      "v_xor_b32 v244, 0x10000, v244\n"
      "v_xor_b32 v245, 0x10000, v245\n"
      "v_xor_b32 v246, 0x10000, v246\n"
      "v_xor_b32 v247, 0x10000, v247\n"
      "s_waitcnt vmcnt(7)\n"
      "s_barrier\n"
      "ds_read_b64_tr_b16 a[64:65], v244 offset:16384\n"
      "ds_read_b64_tr_b16 a[66:67], v244 offset:20480\n"
      "ds_read_b64_tr_b16 a[68:69], v245 offset:16384\n"
      "ds_read_b64_tr_b16 a[70:71], v245 offset:20480\n"
      "ds_read_b64_tr_b16 a[72:73], v246 offset:16384\n"
      "ds_read_b64_tr_b16 a[74:75], v246 offset:20480\n"
      "ds_read_b64_tr_b16 a[76:77], v247 offset:16384\n"
      "ds_read_b64_tr_b16 a[78:79], v247 offset:20480\n"
      "s_waitcnt lgkmcnt(4)\n"
      "v_mfma_f32_16x16x32_bf16 a[144:147], a[64:67], v[136:139], a[144:147]\n"
      "ds_read_b64_tr_b16 a[96:97], v244 offset:17408\n"
      "ds_read_b64_tr_b16 a[98:99], v244 offset:21504\n"
      "v_mfma_f32_16x16x32_bf16 a[148:151], a[64:67], v[140:143], a[148:151]\n"
      "ds_read_b64_tr_b16 a[100:101], v245 offset:17408\n"
      "ds_read_b64_tr_b16 a[102:103], v245 offset:21504\n"
      "v_mfma_f32_16x16x32_bf16 a[152:155], a[68:71], v[136:139], a[152:155]\n"
      "ds_read_b64_tr_b16 a[104:105], v246 offset:17408\n"
      "ds_read_b64_tr_b16 a[106:107], v246 offset:21504\n"
      "v_mfma_f32_16x16x32_bf16 a[156:159], a[68:71], v[140:143], a[156:159]\n"
      "ds_read_b64_tr_b16 a[108:109], v247 offset:17408\n"
      "ds_read_b64_tr_b16 a[110:111], v247 offset:21504\n"
      "s_waitcnt lgkmcnt(8)\n"
      "v_mfma_f32_16x16x32_bf16 a[160:163], a[72:75], v[136:139], a[160:163]\n"
      "s_sub_u32 s76, 18432, s78\n"
      "v_mfma_f32_16x16x32_bf16 a[164:167], a[72:75], v[140:143], a[164:167]\n"
      "s_mov_b32 s63, 0x40000\n"
      "v_mfma_f32_16x16x32_bf16 a[168:171], a[76:79], v[136:139], a[168:171]\n"
      "v_mov_b32 v198, 0\n"
      "v_mfma_f32_16x16x32_bf16 a[172:175], a[76:79], v[140:143], a[172:175]\n"
      "v_and_b32 v197, 0xf0, v241\n"
      "s_waitcnt lgkmcnt(0)\n"
      "v_mfma_f32_16x16x32_bf16 a[144:147], a[96:99], v[152:155], a[144:147]\n"
      "v_lshrrev_b32 v196, 3, v241\n"
      "v_mfma_f32_16x16x32_bf16 a[148:151], a[96:99], v[156:159], a[148:151]\n"
      "v_and_b32 v196, 32, v196\n"
      "v_mfma_f32_16x16x32_bf16 a[152:155], a[100:103], v[152:155], a[152:155]\n"
      "v_lshl_or_b32 v196, v197, 4, v196\n"
      "v_mfma_f32_16x16x32_bf16 a[156:159], a[100:103], v[156:159], a[156:159]\n"
      "v_lshrrev_b32 v199, 5, v241\n"
      "v_mfma_f32_16x16x32_bf16 a[160:163], a[104:107], v[152:155], a[160:163]\n"
      "v_and_or_b32 v196, v199, 16, v196\n"
      "v_mfma_f32_16x16x32_bf16 a[164:167], a[104:107], v[156:159], a[164:167]\n"
      "v_mov_b32 v191, s40\n"
      "v_mfma_f32_16x16x32_bf16 a[168:171], a[108:111], v[152:155], a[168:171]\n"
      "v_mfma_f32_16x16x32_bf16 a[172:175], a[108:111], v[156:159], a[172:175]\n"
      "s_waitcnt vmcnt(0)\n"
      "v_subrev_u32 v205, s82, v206\n"
      "v_cmp_ne_u32 vcc, s40, v207\n"
      "s_cbranch_vccnz LAG_%=\n"
      "v_cmp_lt_i32 vcc, 16, v205\n"
      "s_cbranch_vccnz LAG_%=\n"
      "READ_%=:\n"
      "buffer_load_dwordx2 v[160:161], v240, s[24:27], s76 offen sc1\n"
      "buffer_load_dwordx4 v[96:99], v241, s[24:27], s76 offen offset:0 sc1\n"
      "buffer_load_dwordx4 v[100:103], v241, s[24:27], s76 offen offset:1024 sc1\n"
      "buffer_load_dwordx4 v[104:107], v241, s[24:27], s76 offen offset:2048 sc1\n"
      "buffer_load_dwordx4 v[108:111], v241, s[24:27], s76 offen offset:3072 sc1\n"
      "v_mfma_f32_16x16x32_bf16 v[0:3], a[64:67], v[128:131], v[0:3]\n"
      "v_mfma_f32_16x16x32_bf16 v[4:7], a[64:67], v[132:135], v[4:7]\n"
      "ds_write_b128 v232, a[144:147] offset:32768\n"
      "v_mfma_f32_16x16x32_bf16 v[8:11], a[68:71], v[128:131], v[8:11]\n"
      "v_mfma_f32_16x16x32_bf16 v[12:15], a[68:71], v[132:135], v[12:15]\n"
      "ds_write_b128 v232, a[148:151] offset:33792\n"
      "v_mfma_f32_16x16x32_bf16 v[16:19], a[72:75], v[128:131], v[16:19]\n"
      "v_mfma_f32_16x16x32_bf16 v[20:23], a[72:75], v[132:135], v[20:23]\n"
      "ds_write_b128 v232, a[152:155] offset:34816\n"
      "v_mfma_f32_16x16x32_bf16 v[24:27], a[76:79], v[128:131], v[24:27]\n"
      "v_mfma_f32_16x16x32_bf16 v[28:31], a[76:79], v[132:135], v[28:31]\n"
      "ds_write_b128 v232, a[156:159] offset:35840\n"
      "v_mfma_f32_16x16x32_bf16 v[0:3], a[96:99], v[144:147], v[0:3]\n"
      "v_mfma_f32_16x16x32_bf16 v[4:7], a[96:99], v[148:151], v[4:7]\n"
      "ds_write_b128 v232, a[160:163] offset:36864\n"
      "v_mfma_f32_16x16x32_bf16 v[8:11], a[100:103], v[144:147], v[8:11]\n"
      "v_mfma_f32_16x16x32_bf16 v[12:15], a[100:103], v[148:151], v[12:15]\n"
      "ds_write_b128 v232, a[164:167] offset:37888\n"
      "v_mfma_f32_16x16x32_bf16 v[16:19], a[104:107], v[144:147], v[16:19]\n"
      "v_mfma_f32_16x16x32_bf16 v[20:23], a[104:107], v[148:151], v[20:23]\n"
      "ds_write_b128 v232, a[168:171] offset:38912\n"
      "v_mfma_f32_16x16x32_bf16 v[24:27], a[108:111], v[144:147], v[24:27]\n"
      "v_mfma_f32_16x16x32_bf16 v[28:31], a[108:111], v[148:151], v[28:31]\n"
      "ds_write_b128 v232, a[172:175] offset:39936\n"
      "s_waitcnt lgkmcnt(0)\n"
      "s_barrier\n"
      "ds_read_b128 v[64:67], v233 offset:32768\n"
      "ds_read_b128 v[68:71], v233 offset:33792\n"
      "ds_read_b128 v[72:75], v233 offset:34816\n"
      "ds_read_b128 v[76:79], v233 offset:35840\n"
      "ds_read_b128 v[80:83], v233 offset:36864\n"
      "ds_read_b128 v[84:87], v233 offset:37888\n"
      "ds_read_b128 v[88:91], v233 offset:38912\n"
      "ds_read_b128 v[92:95], v233 offset:39936\n"
      "s_waitcnt lgkmcnt(6)\n"
      "v_pk_add_f32 v[0:1], v[0:1], v[64:65]\n"
      "v_pk_add_f32 v[2:3], v[2:3], v[66:67]\n"
      "v_pk_add_f32 v[4:5], v[4:5], v[68:69]\n"
      "v_pk_add_f32 v[6:7], v[6:7], v[70:71]\n"
      "s_waitcnt lgkmcnt(4)\n"
      "v_pk_add_f32 v[8:9], v[8:9], v[72:73]\n"
      "v_pk_add_f32 v[10:11], v[10:11], v[74:75]\n"
      "v_pk_add_f32 v[12:13], v[12:13], v[76:77]\n"
      "v_pk_add_f32 v[14:15], v[14:15], v[78:79]\n"
      "s_waitcnt lgkmcnt(2)\n"
      "v_pk_add_f32 v[16:17], v[16:17], v[80:81]\n"
      "v_pk_add_f32 v[18:19], v[18:19], v[82:83]\n"
      "v_pk_add_f32 v[20:21], v[20:21], v[84:85]\n"
      "v_pk_add_f32 v[22:23], v[22:23], v[86:87]\n"
      "s_waitcnt lgkmcnt(0)\n"
      "v_pk_add_f32 v[24:25], v[24:25], v[88:89]\n"
      "v_pk_add_f32 v[26:27], v[26:27], v[90:91]\n"
      "v_pk_add_f32 v[28:29], v[28:29], v[92:93]\n"
      "v_pk_add_f32 v[30:31], v[30:31], v[94:95]\n"
      "TRY_%=:\n"
      "s_waitcnt vmcnt(4)\n"
      "v_mov_b32 v162, v160\n"
      "v_add_u32 v231, s40, v160\n"
      "s_nop 0\n"
      "v_permlane32_swap_b32 v160, v162\n"
      "v_sub_f32 v168, v160, v228\n"
      "v_min_f32 v172, 0, -v168\n"
      "v_min_f32 v168, 0, v168\n"
      "v_exp_f32 v168, v168\n"
      "v_exp_f32 v172, v172\n"
      "v_mul_f32 v163, v229, v168\n"
      "v_fmac_f32 v163, v162, v172\n"
      "v_rcp_f32 v163, v163\n"
      "s_nop 0\n"
      "v_mul_f32 v168, v168, v163\n"
      "v_mul_f32 v172, v172, v163\n"
      "v_mov_b32 v169, v168\n"
      "v_mov_b32 v173, v172\n"
      "s_nop 0\n"
      "v_permlane16_swap_b32 v168, v169\n"
      "v_permlane16_swap_b32 v172, v173\n"
      "v_pk_mul_f32 v[32:33], v[0:1], v[168:169] op_sel:[0,0] op_sel_hi:[1,0]\n"
      "v_pk_mul_f32 v[34:35], v[2:3], v[168:169] op_sel:[0,0] op_sel_hi:[1,0]\n"
      "v_pk_mul_f32 v[36:37], v[4:5], v[168:169] op_sel:[0,1] op_sel_hi:[1,1]\n"
      "v_pk_mul_f32 v[38:39], v[6:7], v[168:169] op_sel:[0,1] op_sel_hi:[1,1]\n"
      "v_pk_mul_f32 v[40:41], v[8:9], v[168:169] op_sel:[0,0] op_sel_hi:[1,0]\n"
      "v_pk_mul_f32 v[42:43], v[10:11], v[168:169] op_sel:[0,0] op_sel_hi:[1,0]\n"
      "v_pk_mul_f32 v[44:45], v[12:13], v[168:169] op_sel:[0,1] op_sel_hi:[1,1]\n"
      "v_pk_mul_f32 v[46:47], v[14:15], v[168:169] op_sel:[0,1] op_sel_hi:[1,1]\n"
      "v_pk_mul_f32 v[48:49], v[16:17], v[168:169] op_sel:[0,0] op_sel_hi:[1,0]\n"
      "v_pk_mul_f32 v[50:51], v[18:19], v[168:169] op_sel:[0,0] op_sel_hi:[1,0]\n"
      "v_pk_mul_f32 v[52:53], v[20:21], v[168:169] op_sel:[0,1] op_sel_hi:[1,1]\n"
      "v_pk_mul_f32 v[54:55], v[22:23], v[168:169] op_sel:[0,1] op_sel_hi:[1,1]\n"
      "v_pk_mul_f32 v[56:57], v[24:25], v[168:169] op_sel:[0,0] op_sel_hi:[1,0]\n"
      "v_pk_mul_f32 v[58:59], v[26:27], v[168:169] op_sel:[0,0] op_sel_hi:[1,0]\n"
      "v_pk_mul_f32 v[60:61], v[28:29], v[168:169] op_sel:[0,1] op_sel_hi:[1,1]\n"
      "v_pk_mul_f32 v[62:63], v[30:31], v[168:169] op_sel:[0,1] op_sel_hi:[1,1]\n"
      "s_waitcnt vmcnt(3)\n"
      "v_add3_u32 v231, v231, v96, v97\n"
      "v_add3_u32 v231, v231, v98, v99\n"
      "s_waitcnt vmcnt(2)\n"
      "v_add3_u32 v231, v231, v100, v101\n"
      "v_add3_u32 v231, v231, v102, v103\n"
      "s_waitcnt vmcnt(1)\n"
      "v_add3_u32 v231, v231, v104, v105\n"
      "v_add3_u32 v231, v231, v106, v107\n"
      "s_waitcnt vmcnt(0)\n"
      "v_add3_u32 v231, v231, v108, v109\n"
      "v_add3_u32 v231, v231, v110, v111\n"
      "v_cmp_ne_u32 vcc, v231, v161\n"
      "s_cbranch_vccnz RETRY_%=\n"
      "GOT_%=:\n"
      "v_lshlrev_b32 v0, 16, v96\n"
      "v_and_b32 v1, 0xffff0000, v96\n"
      "v_lshlrev_b32 v2, 16, v97\n"
      "v_and_b32 v3, 0xffff0000, v97\n"
      "v_lshlrev_b32 v8, 16, v100\n"
      "v_and_b32 v9, 0xffff0000, v100\n"
      "v_lshlrev_b32 v10, 16, v101\n"
      "v_and_b32 v11, 0xffff0000, v101\n"
      "v_pk_fma_f32 v[32:33], v[0:1], v[172:173], v[32:33] op_sel:[0,0,0] op_sel_hi:[1,0,1]\n"
      "v_pk_fma_f32 v[34:35], v[2:3], v[172:173], v[34:35] op_sel:[0,0,0] op_sel_hi:[1,0,1]\n"
      "v_pk_fma_f32 v[40:41], v[8:9], v[172:173], v[40:41] op_sel:[0,0,0] op_sel_hi:[1,0,1]\n"
      "v_pk_fma_f32 v[42:43], v[10:11], v[172:173], v[42:43] op_sel:[0,0,0] op_sel_hi:[1,0,1]\n"
      "v_cvt_pk_bf16_f32 v112, v32, v33\n"
      "v_cvt_pk_bf16_f32 v114, v40, v41\n"
      "v_cvt_pk_bf16_f32 v113, v34, v35\n"
      "v_cvt_pk_bf16_f32 v115, v42, v43\n"
      "v_permlane16_swap_b32 v112, v114\n"
      "s_nop 0\n"
      "v_permlane16_swap_b32 v113, v115\n"
      "global_store_dwordx4 v196, v[112:115], s[30:31] offset:-4096 sc1 nt\n"
      "v_lshlrev_b32 v16, 16, v104\n"
      "v_and_b32 v17, 0xffff0000, v104\n"
      "v_lshlrev_b32 v18, 16, v105\n"
      "v_and_b32 v19, 0xffff0000, v105\n"
      "v_lshlrev_b32 v24, 16, v108\n"
      "v_and_b32 v25, 0xffff0000, v108\n"
      "v_lshlrev_b32 v26, 16, v109\n"
      "v_and_b32 v27, 0xffff0000, v109\n"
      "v_pk_fma_f32 v[48:49], v[16:17], v[172:173], v[48:49] op_sel:[0,0,0] op_sel_hi:[1,0,1]\n"
      "v_pk_fma_f32 v[50:51], v[18:19], v[172:173], v[50:51] op_sel:[0,0,0] op_sel_hi:[1,0,1]\n"
      "v_pk_fma_f32 v[56:57], v[24:25], v[172:173], v[56:57] op_sel:[0,0,0] op_sel_hi:[1,0,1]\n"
      "v_pk_fma_f32 v[58:59], v[26:27], v[172:173], v[58:59] op_sel:[0,0,0] op_sel_hi:[1,0,1]\n"
      "v_cvt_pk_bf16_f32 v116, v48, v49\n"
      "v_cvt_pk_bf16_f32 v118, v56, v57\n"
      "v_cvt_pk_bf16_f32 v117, v50, v51\n"
      "v_cvt_pk_bf16_f32 v119, v58, v59\n"
      "v_permlane16_swap_b32 v116, v118\n"
      "s_nop 0\n"
      "v_permlane16_swap_b32 v117, v119\n"
      "global_store_dwordx4 v196, v[116:119], s[30:31] offset:-4032 sc1 nt\n"
      "v_lshlrev_b32 v4, 16, v98\n"
      "v_and_b32 v5, 0xffff0000, v98\n"
      "v_lshlrev_b32 v6, 16, v99\n"
      "v_and_b32 v7, 0xffff0000, v99\n"
      "v_lshlrev_b32 v12, 16, v102\n"
      "v_and_b32 v13, 0xffff0000, v102\n"
      "v_lshlrev_b32 v14, 16, v103\n"
      "v_and_b32 v15, 0xffff0000, v103\n"
      "v_pk_fma_f32 v[36:37], v[4:5], v[172:173], v[36:37] op_sel:[0,1,0] op_sel_hi:[1,1,1]\n"
      "v_pk_fma_f32 v[38:39], v[6:7], v[172:173], v[38:39] op_sel:[0,1,0] op_sel_hi:[1,1,1]\n"
      "v_pk_fma_f32 v[44:45], v[12:13], v[172:173], v[44:45] op_sel:[0,1,0] op_sel_hi:[1,1,1]\n"
      "v_pk_fma_f32 v[46:47], v[14:15], v[172:173], v[46:47] op_sel:[0,1,0] op_sel_hi:[1,1,1]\n"
      "v_cvt_pk_bf16_f32 v120, v36, v37\n"
      "v_cvt_pk_bf16_f32 v122, v44, v45\n"
      "v_cvt_pk_bf16_f32 v121, v38, v39\n"
      "v_cvt_pk_bf16_f32 v123, v46, v47\n"
      "v_permlane16_swap_b32 v120, v122\n"
      "s_nop 0\n"
      "v_permlane16_swap_b32 v121, v123\n"
      "global_store_dwordx4 v196, v[120:123], s[30:31] offset:0 sc1 nt\n"
      "v_lshlrev_b32 v20, 16, v106\n"
      "v_and_b32 v21, 0xffff0000, v106\n"
      "v_lshlrev_b32 v22, 16, v107\n"
      "v_and_b32 v23, 0xffff0000, v107\n"
      "v_lshlrev_b32 v28, 16, v110\n"
      "v_and_b32 v29, 0xffff0000, v110\n"
      "v_lshlrev_b32 v30, 16, v111\n"
      "v_and_b32 v31, 0xffff0000, v111\n"
      "v_pk_fma_f32 v[52:53], v[20:21], v[172:173], v[52:53] op_sel:[0,1,0] op_sel_hi:[1,1,1]\n"
      "v_pk_fma_f32 v[54:55], v[22:23], v[172:173], v[54:55] op_sel:[0,1,0] op_sel_hi:[1,1,1]\n"
      "v_pk_fma_f32 v[60:61], v[28:29], v[172:173], v[60:61] op_sel:[0,1,0] op_sel_hi:[1,1,1]\n"
      "v_pk_fma_f32 v[62:63], v[30:31], v[172:173], v[62:63] op_sel:[0,1,0] op_sel_hi:[1,1,1]\n"
      "v_cvt_pk_bf16_f32 v124, v52, v53\n"
      "v_cvt_pk_bf16_f32 v126, v60, v61\n"
      "v_cvt_pk_bf16_f32 v125, v54, v55\n"
      "v_cvt_pk_bf16_f32 v127, v62, v63\n"
      "v_permlane16_swap_b32 v124, v126\n"
      "s_nop 0\n"
      "v_permlane16_swap_b32 v125, v127\n"
      "global_store_dwordx4 v196, v[124:127], s[30:31] offset:64 sc1 nt\n"
      "s_mov_b64 exec, 1\n"
      "global_store_dword v198, v191, s[46:47] sc1\n"
      "s_mov_b64 exec, -1\n"
      "ENDE_%=:\n"
      ".subsection 1\n"
      "MH_%=:\n"
      "s_cmp_lg_u32 s54, 0\n"
      "s_waitcnt vmcnt(12) lgkmcnt(0)\n"
      "s_cbranch_scc1 W6_%=\n"
      "s_waitcnt vmcnt(8)\n"
      "W6_%=:\n"
      "s_barrier\n"
      "v_mfma_f32_16x16x32_bf16 v[24:27], a[108:111], v[144:147], v[24:27]\n"
      "s_xor_b64 s[56:57], s[56:57], s[32:33]\n"
      "ds_read_b128 a[64:67], v242 offset:0\n"
      "ds_read_b128 a[72:75], v243 offset:0\n"
      "v_mfma_f32_16x16x32_bf16 v[32:35], a[112:115], v[144:147], v[32:35]\n"
      "s_bitset1_b32 s61, 15\n"
      "ds_read_b128 a[80:83], v242 offset:1024\n"
      "ds_read_b128 a[88:91], v243 offset:1024\n"
      "v_mfma_f32_16x16x32_bf16 v[36:39], a[112:115], v[148:151], v[36:39]\n"
      "s_add_u32 s61, s61, 0x2000\n"
      "v_exp_f32 v189, v89\n"
      "v_mfma_f32_16x16x32_bf16 a[176:179], a[112:115], v[152:155], a[176:179]\n"
      "s_bitset0_b32 s61, 15\n"
      "v_exp_f32 v190, v90\n"
      "v_mfma_f32_16x16x32_bf16 a[180:183], a[112:115], v[156:159], a[180:183]\n"
      "s_xor_b32 s60, s61, s51\n"
      "v_exp_f32 v191, v91\n"
      "v_mfma_f32_16x16x32_bf16 a[188:191], a[116:119], v[156:159], a[188:191]\n"
      "s_add_u32 s62, s62, 0x4000\n"
      "v_cvt_pk_bf16_f32 v140, v188, v189\n"
      "v_cvt_pk_bf16_f32 v141, v190, v191\n"
      "v_mfma_f32_16x16x32_bf16 a[184:187], a[116:119], v[152:155], a[184:187]\n"
      "s_and_b32 s62, s62, 0x1ffff\n"
      "v_exp_f32 v192, v92\n"
      "v_mfma_f32_16x16x32_bf16 v[44:47], a[116:119], v[148:151], v[44:47]\n"
      "s_add_u32 s66, s66, s86\n"
      "v_exp_f32 v193, v93\n"
      "v_mfma_f32_16x16x32_bf16 v[40:43], a[116:119], v[144:147], v[40:43]\n"
      "s_add_u32 s82, s82, s86\n"
      "v_exp_f32 v194, v94\n"
      "v_mfma_f32_16x16x32_bf16 v[48:51], a[120:123], v[144:147], v[48:51]\n"
      "s_add_u32 s65, s65, s86\n"
      "v_exp_f32 v195, v95\n"
      "v_mfma_f32_16x16x32_bf16 v[52:55], a[120:123], v[148:151], v[52:55]\n"
      "s_max_u32 s88, s66, s65\n"
      "v_cvt_pk_bf16_f32 v142, v192, v193\n"
      "v_cvt_pk_bf16_f32 v143, v194, v195\n"
      "s_waitcnt lgkmcnt(0)\n"
      "v_mfma_f32_16x16x32_bf16 a[192:195], a[120:123], v[152:155], a[192:195]\n"
      "v_xor_b32 v244, 0x10000, v244\n"
      "v_xor_b32 v245, 0x10000, v245\n"
      "v_mfma_f32_16x16x32_bf16 v[184:187], a[140:143], v[140:143], v[184:187]\n"
      "s_mov_b32 m0, s62\n"
      "ds_read_b128 a[68:71], v242 offset:4096\n"
      "ds_read_b128 a[76:79], v243 offset:4096\n"
      "v_mfma_f32_16x16x32_bf16 a[196:199], a[120:123], v[156:159], a[196:199]\n"
      "buffer_load_dwordx4 v248, s[56:59], s60 offen nt lds\n"
      "ds_read_b128 a[84:87], v242 offset:5120\n"
      "ds_read_b128 a[92:95], v243 offset:5120\n"
      "v_mfma_f32_16x16x32_bf16 a[204:207], a[124:127], v[156:159], a[204:207]\n"
      "buffer_load_dwordx4 v248, s[56:59], s60 offen offset:2048 nt lds\n"
      "v_xor_b32 v246, 0x10000, v246\n"
      "v_xor_b32 v247, 0x10000, v247\n"
      "v_mfma_f32_16x16x32_bf16 a[200:203], a[124:127], v[152:155], a[200:203]\n"
      "s_bitset1_b32 m0, 10\n"
      "v_max3_f32 v196, v184, v185, v186\n"
      "v_max_f32 v196, v196, v187\n"
      "v_mfma_f32_16x16x32_bf16 v[60:63], a[124:127], v[148:151], v[60:63]\n"
      "buffer_load_dwordx4 v249, s[56:59], s60 offen nt lds\n"
      "v_cmp_nge_f32 vcc, 0x4b800000, v196\n"
      "v_mfma_f32_16x16x32_bf16 v[56:59], a[124:127], v[144:147], v[56:59]\n"
      "buffer_load_dwordx4 v249, s[56:59], s60 offen offset:2048 nt lds\n"
      "s_cbranch_vccnz FS11_%=\n"
      "FR11_%=:\n"
      "v_mfma_f32_16x16x32_bf16 v[64:67], a[64:67], a[0:3], v[160:163]\n"
      "v_exp_f32 v188, v96\n"
      "v_mfma_f32_16x16x32_bf16 v[64:67], a[72:75], a[16:19], v[64:67]\n"
      "v_add_f32 v180, v180, v184\n"
      "v_add_f32 v181, v181, v185\n"
      "v_mfma_f32_16x16x32_bf16 v[64:67], a[80:83], a[32:35], v[64:67]\n"
      "v_exp_f32 v189, v97\n"
      "v_mfma_f32_16x16x32_bf16 v[64:67], a[88:91], a[48:51], v[64:67]\n"
      "v_add_f32 v182, v182, v186\n"
      "v_add_f32 v183, v183, v187\n"
      "v_mfma_f32_16x16x32_bf16 v[72:75], a[64:67], a[4:7], v[164:167]\n"
      "v_exp_f32 v190, v98\n"
      "v_mfma_f32_16x16x32_bf16 v[72:75], a[72:75], a[20:23], v[72:75]\n"
      "v_exp_f32 v191, v99\n"
      "v_mfma_f32_16x16x32_bf16 v[72:75], a[80:83], a[36:39], v[72:75]\n"
      "v_cvt_pk_bf16_f32 v144, v188, v189\n"
      "v_cvt_pk_bf16_f32 v145, v190, v191\n"
      "v_mfma_f32_16x16x32_bf16 v[72:75], a[88:91], a[52:55], v[72:75]\n"
      "v_exp_f32 v192, v100\n"
      "v_mfma_f32_16x16x32_bf16 v[80:83], a[64:67], a[8:11], v[168:171]\n"
      "s_add_u32 s39, s39, s39\n"
      "v_exp_f32 v193, v101\n"
      "v_mfma_f32_16x16x32_bf16 v[80:83], a[72:75], a[24:27], v[80:83]\n"
      "s_memrealtime s[68:69]\n"
      "v_exp_f32 v194, v102\n"
      "v_mfma_f32_16x16x32_bf16 v[80:83], a[80:83], a[40:43], v[80:83]\n"
      "v_exp_f32 v195, v103\n"
      "v_mfma_f32_16x16x32_bf16 v[80:83], a[88:91], a[56:59], v[80:83]\n"
      "s_nop 0\n"
      "v_mfma_f32_16x16x32_bf16 v[88:91], a[64:67], a[12:15], v[172:175]\n"
      "s_nop 0\n"
      "v_mfma_f32_16x16x32_bf16 v[88:91], a[72:75], a[28:31], v[88:91]\n"
      "v_exp_f32 v188, v104\n"
      "v_mfma_f32_16x16x32_bf16 v[88:91], a[80:83], a[44:47], v[88:91]\n"
      "s_nop 0\n"
      "v_mfma_f32_16x16x32_bf16 v[88:91], a[88:91], a[60:63], v[88:91]\n"
      "s_nop 0\n"
      "s_cbranch_scc1 SP1_%=\n"
      "s_branch HD1_%=\n"
      "HOOK_%=:\n"
      "buffer_load_dword v250, v251, s[20:23], 0 offen sc1\n"
      "s_swappc_b64 s[84:85], s[84:85]\n"
      "s_waitcnt vmcnt(12)\n"
      "v_subrev_u32 v250, s71, v250\n"
      "v_min_u32 v250, 0x258, v250\n"
      "s_nop 1\n"
      "v_add_u32_dpp v250, v250, v250 quad_perm:[1,0,3,2] row_mask:0xf bank_mask:0xf\n"
      "s_nop 1\n"
      "v_add_u32_dpp v250, v250, v250 quad_perm:[2,3,0,1] row_mask:0xf bank_mask:0xf\n"
      "s_nop 1\n"
      "v_add_u32_dpp v250, v250, v250 row_half_mirror row_mask:0xf bank_mask:0xf\n"
      "s_nop 0\n"
      "v_readfirstlane_b32 s67, v250\n"
      "s_sub_u32 s66, s66, s81\n"
      "s_lshr_b32 s67, s67, 3\n"
      "s_add_u32 s66, s66, s67\n"
      "s_max_u32 s88, s66, s65\n"
      "s_mov_b32 s86, 57\n"
      "s_mov_b32 s77, 171\n"
      "v_bfe_u32 v251, v241, 2, 7\n"
      "v_add_u32 v251, s79, v251\n"
      "buffer_load_dword v250, v251, s[20:23], 0 offen sc1\n"
      "s_swappc_b64 s[84:85], s[84:85]\n"
      "s_mov_b32 s89, 0\n"
      "s_swappc_b64 s[84:85], s[84:85]\n"
      "s_mov_b32 s86, 55\n"
      "s_mov_b32 s77, 165\n"
      "s_swappc_b64 s[84:85], s[84:85]\n"
      "s_cmp_lt_u32 s89, 2\n"
      "s_cselect_b32 s67, 2048, s72\n"
      "s_mov_b32 s89, 0\n"
      "s_cmp_lt_u32 s67, 2048\n"
      "s_cbranch_scc0 NV0_%=\n"
      "v_and_b32 v216, 0x1f0, v241\n"
      "v_lshlrev_b32 v216, 4, v216\n"
      "v_mov_b32 v215, s82\n"
      "s_lshr_b32 s67, s67, 4\n"
      "s_add_u32 s67, s67, 8192\n"
      "buffer_store_dword v215, v216, s[20:23], s67 offen sc1\n"
      "NV0_%=:\n"
      "s_swappc_b64 s[84:85], s[84:85]\n"
      "s_mov_b32 s86, 54\n"
      "s_mov_b32 s77, 162\n"
      "s_swappc_b64 s[84:85], s[84:85]\n"
      "buffer_load_dword v250, v251, s[20:23], 0 offen sc1\n"
      "s_swappc_b64 s[84:85], s[84:85]\n"
      "s_cmp_lt_u32 s89, 2\n"
      "s_cselect_b32 s67, 2048, s72\n"
      "s_mov_b32 s89, 0\n"
      "s_cmp_lt_u32 s67, 2048\n"
      "s_cbranch_scc0 NV1_%=\n"
      "v_and_b32 v216, 0x1f0, v241\n"
      "v_lshlrev_b32 v216, 4, v216\n"
      "v_mov_b32 v215, s82\n"
      "s_lshr_b32 s67, s67, 4\n"
      "s_add_u32 s67, s67, 8192\n"
      "buffer_store_dword v215, v216, s[20:23], s67 offen sc1\n"
      "NV1_%=:\n"
      "s_swappc_b64 s[84:85], s[84:85]\n"
      "s_waitcnt vmcnt(12)\n"
      "v_sub_u32 v250, s82, v250\n"
      "v_cmp_gt_u32 vcc, 1600, v250\n"
      "s_movk_i32 s76, 100\n"
      "s_nop 2\n"
      "s_bcnt1_i32_b32 s67, vcc_lo\n"
      "s_cmp_eq_u32 s70, 60\n"
      "s_cselect_b32 s67, 0, s67\n"
      "s_cmp_ge_u32 s67, 5\n"
      "s_cbranch_scc1 SM0_%=\n"
      "buffer_load_dword v250, v251, s[20:23], 0 offen sc1\n"
      "s_swappc_b64 s[84:85], s[84:85]\n"
      "s_cmp_lt_u32 s89, 2\n"
      "s_cselect_b32 s67, 2048, s72\n"
      "s_mov_b32 s89, 0\n"
      "s_cmp_lt_u32 s67, 2048\n"
      "s_cbranch_scc0 NV2_%=\n"
      "v_and_b32 v216, 0x1f0, v241\n"
      "v_lshlrev_b32 v216, 4, v216\n"
      "v_mov_b32 v215, s82\n"
      "s_lshr_b32 s67, s67, 4\n"
      "s_add_u32 s67, s67, 8192\n"
      "buffer_store_dword v215, v216, s[20:23], s67 offen sc1\n"
      "NV2_%=:\n"
      "s_swappc_b64 s[84:85], s[84:85]\n"
      "s_waitcnt vmcnt(12)\n"
      "v_sub_u32 v250, s82, v250\n"
      "v_cmp_gt_u32 vcc, 1600, v250\n"
      "s_movk_i32 s76, 146\n"
      "s_nop 2\n"
      "s_bcnt1_i32_b32 s67, vcc_lo\n"
      "s_cmp_eq_u32 s70, 60\n"
      "s_cselect_b32 s67, 0, s67\n"
      "s_cmp_ge_u32 s67, 5\n"
      "s_cbranch_scc1 SM1_%=\n"
      "buffer_load_dword v250, v251, s[20:23], 0 offen sc1\n"
      "s_swappc_b64 s[84:85], s[84:85]\n"
      "s_min_i32 s70, s70, 150\n"
      "s_waitcnt vmcnt(12)\n"
      "v_sub_u32 v250, s82, v250\n"
      "v_cmp_gt_u32 vcc, 1600, v250\n"
      "s_movk_i32 s76, 154\n"
      "s_nop 2\n"
      "s_bcnt1_i32_b32 s67, vcc_lo\n"
      "s_cmp_eq_u32 s70, 60\n"
      "s_cselect_b32 s67, 0, s67\n"
      "s_cmp_ge_u32 s67, 5\n"
      "s_cbranch_scc1 SM1_%=\n"
      "s_swappc_b64 s[84:85], s[84:85]\n"
      "s_cmp_lt_u32 s89, 2\n"
      "s_cselect_b32 s67, 2048, s72\n"
      "s_mov_b32 s89, 0\n"
      "s_cmp_lt_u32 s67, 2048\n"
      "s_cbranch_scc0 NV3_%=\n"
      "v_and_b32 v216, 0x1f0, v241\n"
      "v_lshlrev_b32 v216, 4, v216\n"
      "v_mov_b32 v215, s82\n"
      "s_lshr_b32 s67, s67, 4\n"
      "s_add_u32 s67, s67, 8192\n"
      "buffer_store_dword v215, v216, s[20:23], s67 offen sc1\n"
      "NV3_%=:\n"
      "s_swappc_b64 s[84:85], s[84:85]\n"
      "buffer_load_dword v250, v251, s[20:23], 0 offen sc1\n"
      "s_swappc_b64 s[84:85], s[84:85]\n"
      "s_waitcnt vmcnt(12)\n"
      "v_sub_u32 v250, s82, v250\n"
      "v_cmp_gt_u32 vcc, 1600, v250\n"
      "s_movk_i32 s76, 170\n"
      "s_nop 2\n"
      "s_bcnt1_i32_b32 s67, vcc_lo\n"
      "s_cmp_eq_u32 s70, 60\n"
      "s_cselect_b32 s67, 0, s67\n"
      "s_cmp_ge_u32 s67, 5\n"
      "s_cbranch_scc1 SM1_%=\n"
      "s_swappc_b64 s[84:85], s[84:85]\n"
      "s_mov_b32 s86, 53\n"
      "s_mov_b32 s77, 159\n"
      "s_getpc_b64 s[74:75]\n"
      "PF_%=:\n"
      "s_sub_u32 s74, s74, PF_%=-END_%=\n"
      "s_subb_u32 s75, s75, 0\n"
      "s_lshl_b32 s76, %[blk], 7\n"
      "s_and_b32 s76, s76, 0x1c00\n"
      "s_min_u32 s76, s76, ENDE_%=-END_%=-1024\n"
      "v_add_u32 v200, s76, v241\n"
      "global_load_dwordx4 v[200:203], v200, s[74:75]\n"
      "v_mov_b32 v212, 0\n"
      "global_load_dword v213, v212, s[30:31] nt\n"
      "buffer_load_dword v214, off, s[24:27], s78 nt\n"
      "s_swappc_b64 s[84:85], s[84:85]\n"
      "SM1_%=:\n"
      "s_add_u32 s66, s66, s76\n"
      "s_max_u32 s88, s66, s65\n"
      "s_min_i32 s70, s70, 150\n"
      "s_branch SM2_%=\n"
      "SM0_%=:\n"
      "s_or_b32 s39, s39, 0xe0000000\n"
      "s_mov_b32 s52, 0xaaaaaaaa\n"
      "s_mov_b32 s53, 0xaaaaaaaa\n"
      "s_add_u32 s66, s66, s76\n"
      "s_max_u32 s88, s66, s65\n"
      "s_min_i32 s70, s70, 150\n"
      "s_mov_b32 s86, 66\n"
      "s_mov_b32 s77, 198\n"
      "s_swappc_b64 s[84:85], s[84:85]\n"
      "s_mov_b32 s86, 62\n"
      "s_mov_b32 s77, 186\n"
      "s_swappc_b64 s[84:85], s[84:85]\n"
      "s_mov_b32 s86, 59\n"
      "s_mov_b32 s77, 177\n"
      "s_swappc_b64 s[84:85], s[84:85]\n"
      "SM2_%=:\n"
      "s_mov_b32 s86, 56\n"
      "s_mov_b32 s77, 168\n"
      "s_mov_b32 s52, 0x0\n"
      "s_mov_b32 s53, 0x80000\n"
      "s_swappc_b64 s[84:85], s[84:85]\n"
      "s_getpc_b64 s[74:75]\n"
      "PG_%=:\n"
      "s_sub_u32 s74, s74, PG_%=-END_%=\n"
      "s_subb_u32 s75, s75, 0\n"
      "s_lshl_b32 s76, %[blk], 7\n"
      "s_and_b32 s76, s76, 0x1c00\n"
      "s_min_u32 s76, s76, ENDE_%=-END_%=-1024\n"
      "v_add_u32 v200, s76, v241\n"
      "global_load_dwordx4 v[200:203], v200, s[74:75]\n"
      "v_mov_b32 v212, 0\n"
      "global_load_dword v213, v212, s[30:31] nt\n"
      "buffer_load_dword v214, off, s[24:27], s78 nt\n"
      "s_swappc_b64 s[84:85], s[84:85]\n"
      "GW0_%=:\n"
      "s_memrealtime s[68:69]\n"
      "s_waitcnt lgkmcnt(0)\n"
      "s_getreg_b32 s67, hwreg(HW_REG_IB_STS, 0, 4)\n"
      "s_cmp_gt_u32 s67, 9\n"
      "s_addc_u32 s89, s89, 0\n"
      "GS0_%=:\n"
      "s_sub_u32 s67, s68, s88\n"
      "s_cmp_ge_i32 s67, 0\n"
      "s_cbranch_scc1 GX0_%=\n"
      "s_memrealtime s[68:69]\n"
      "s_waitcnt lgkmcnt(0)\n"
      "s_branch GS0_%=\n"
      "FS1_%=:\n"
      "s_call_b64 s[96:97], FB1_%=\n"
      "s_branch FR1_%=\n"
      "FS2_%=:\n"
      "s_call_b64 s[96:97], FB0_%=\n"
      "s_branch FR2_%=\n"
      "FS3_%=:\n"
      "s_call_b64 s[96:97], FB0_%=\n"
      "s_branch FR3_%=\n"
      "GW4_%=:\n"
      "s_memrealtime s[68:69]\n"
      "s_waitcnt lgkmcnt(0)\n"
      "s_getreg_b32 s67, hwreg(HW_REG_IB_STS, 0, 4)\n"
      "s_cmp_gt_u32 s67, 9\n"
      "s_addc_u32 s89, s89, 0\n"
      "GS4_%=:\n"
      "s_sub_u32 s67, s68, s88\n"
      "s_cmp_ge_i32 s67, 0\n"
      "s_cbranch_scc1 GX4_%=\n"
      "s_memrealtime s[68:69]\n"
      "s_waitcnt lgkmcnt(0)\n"
      "s_branch GS4_%=\n"
      "GW5_%=:\n"
      "s_memrealtime s[68:69]\n"
      "s_waitcnt lgkmcnt(0)\n"
      "s_getreg_b32 s67, hwreg(HW_REG_IB_STS, 0, 4)\n"
      "s_cmp_gt_u32 s67, 9\n"
      "s_addc_u32 s89, s89, 0\n"
      "GS5_%=:\n"
      "s_sub_u32 s67, s68, s88\n"
      "s_cmp_ge_i32 s67, 0\n"
      "s_cbranch_scc1 GX5_%=\n"
      "s_memrealtime s[68:69]\n"
      "s_waitcnt lgkmcnt(0)\n"
      "s_branch GS5_%=\n"
      "GW6_%=:\n"
      "s_memrealtime s[68:69]\n"
      "s_waitcnt lgkmcnt(0)\n"
      "s_getreg_b32 s67, hwreg(HW_REG_IB_STS, 0, 4)\n"
      "s_cmp_gt_u32 s67, 9\n"
      "s_addc_u32 s89, s89, 0\n"
      "GS6_%=:\n"
      "s_sub_u32 s67, s68, s88\n"
      "s_cmp_ge_i32 s67, 0\n"
      "s_cbranch_scc1 GX6_%=\n"
      "s_memrealtime s[68:69]\n"
      "s_waitcnt lgkmcnt(0)\n"
      "s_branch GS6_%=\n"
      "GW7_%=:\n"
      "s_memrealtime s[68:69]\n"
      "s_waitcnt lgkmcnt(0)\n"
      "s_getreg_b32 s67, hwreg(HW_REG_IB_STS, 0, 4)\n"
      "s_cmp_gt_u32 s67, 9\n"
      "s_addc_u32 s89, s89, 0\n"
      "GS7_%=:\n"
      "s_sub_u32 s67, s68, s88\n"
      "s_cmp_ge_i32 s67, 0\n"
      "s_cbranch_scc1 GX7_%=\n"
      "s_memrealtime s[68:69]\n"
      "s_waitcnt lgkmcnt(0)\n"
      "s_branch GS7_%=\n"
      "FS8_%=:\n"
      "s_call_b64 s[96:97], FB1_%=\n"
      "s_branch FR8_%=\n"
      "LAG_%=:\n"
      "s_memrealtime s[68:69]\n"
      "s_waitcnt lgkmcnt(0)\n"
      "s_mov_b32 s88, s68\n"
      "LG1_%=:\n"
      "v_cmp_ne_u32 vcc, s40, v207\n"
      "s_cbranch_vccz LG2_%=\n"
      "s_sub_u32 s63, s63, 1\n"
      "s_cmp_eq_u32 s63, 0\n"
      "s_cbranch_scc1 READ_%=\n"
      "s_sleep 4\n"
      "global_load_dwordx2 v[206:207], v239, s[46:47] offset:8 sc1\n"
      "s_waitcnt vmcnt(0)\n"
      "s_branch LG1_%=\n"
      "LG2_%=:\n"
      "v_readfirstlane_b32 s67, v206\n"
      "s_sub_u32 s88, s88, s82\n"
      "s_min_u32 s88, s88, 400\n"
      "s_add_u32 s88, s88, s67\n"
      "LG3_%=:\n"
      "s_memrealtime s[68:69]\n"
      "s_waitcnt lgkmcnt(0)\n"
      "s_sub_u32 s67, s88, s68\n"
      "s_sub_u32 s67, s67, 17\n"
      "s_cmp_lt_u32 s67, 400\n"
      "s_cbranch_scc1 LG3_%=\n"
      "s_branch READ_%=\n"
      "RETRY_%=:\n"
      "s_sub_u32 s63, s63, 1\n"
      "s_cbranch_scc1 GOT_%=\n"
      "buffer_load_dwordx2 v[160:161], v240, s[24:27], s76 offen sc1\n"
      "buffer_load_dwordx4 v[96:99], v241, s[24:27], s76 offen offset:0 sc1\n"
      "buffer_load_dwordx4 v[100:103], v241, s[24:27], s76 offen offset:1024 sc1\n"
      "buffer_load_dwordx4 v[104:107], v241, s[24:27], s76 offen offset:2048 sc1\n"
      "buffer_load_dwordx4 v[108:111], v241, s[24:27], s76 offen offset:3072 sc1\n"
      "s_branch TRY_%=\n"
      "FS11_%=:\n"
      "s_call_b64 s[96:97], FB0_%=\n"
      "s_branch FR11_%=\n"
      "COLD_%=:\n"
      "FB0_%=:\n"
      "v_mov_b32 v196, 139248\n"
      "s_nop 7\n"
      "ds_write_b32 v196, v196\n"
      "v_max3_f32 v188, v64, v65, 0\n"
      "v_max3_f32 v189, v72, v73, 0\n"
      "v_max3_f32 v190, v80, v81, 0\n"
      "v_max3_f32 v191, v88, v89, 0\n"
      "v_max3_f32 v188, v188, v66, v67\n"
      "v_max3_f32 v189, v189, v74, v75\n"
      "v_max3_f32 v190, v190, v82, v83\n"
      "v_max3_f32 v191, v191, v90, v91\n"
      "v_max3_f32 v188, v188, v68, v69\n"
      "v_max3_f32 v189, v189, v76, v77\n"
      "v_max3_f32 v190, v190, v84, v85\n"
      "v_max3_f32 v191, v191, v92, v93\n"
      "v_max3_f32 v188, v188, v70, v71\n"
      "v_max3_f32 v189, v189, v78, v79\n"
      "v_max3_f32 v190, v190, v86, v87\n"
      "v_max3_f32 v191, v191, v94, v95\n"
      "v_mov_b32 v192, v188\n"
      "v_mov_b32 v193, v189\n"
      "v_mov_b32 v194, v190\n"
      "v_mov_b32 v195, v191\n"
      "v_permlane16_swap_b32 v188, v192\n"
      "v_permlane16_swap_b32 v189, v193\n"
      "v_permlane16_swap_b32 v190, v194\n"
      "v_permlane16_swap_b32 v191, v195\n"
      "v_max_f32 v188, v188, v192\n"
      "v_max_f32 v189, v189, v193\n"
      "v_max_f32 v190, v190, v194\n"
      "v_max_f32 v191, v191, v195\n"
      "v_mov_b32 v192, v188\n"
      "v_mov_b32 v193, v189\n"
      "v_mov_b32 v194, v190\n"
      "v_mov_b32 v195, v191\n"
      "v_permlane32_swap_b32 v188, v192\n"
      "v_permlane32_swap_b32 v189, v193\n"
      "v_permlane32_swap_b32 v190, v194\n"
      "v_permlane32_swap_b32 v191, v195\n"
      "v_max_f32 v188, v188, v192\n"
      "v_max_f32 v189, v189, v193\n"
      "v_max_f32 v190, v190, v194\n"
      "v_max_f32 v191, v191, v195\n"
      "RS0_%=:\n"
      "v_exp_f32 v192, -v188\n"
      "v_exp_f32 v193, -v189\n"
      "v_exp_f32 v194, -v190\n"
      "v_exp_f32 v195, -v191\n"
      "v_sub_f32 v176, v176, v188\n"
      "v_sub_f32 v177, v177, v189\n"
      "v_sub_f32 v178, v178, v190\n"
      "v_sub_f32 v179, v179, v191\n"
      "v_mov_b32 v160, v176\n"
      "v_mov_b32 v161, v176\n"
      "v_mov_b32 v162, v176\n"
      "v_mov_b32 v163, v176\n"
      "v_mov_b32 v164, v177\n"
      "v_mov_b32 v165, v177\n"
      "v_mov_b32 v166, v177\n"
      "v_mov_b32 v167, v177\n"
      "v_mov_b32 v168, v178\n"
      "v_mov_b32 v169, v178\n"
      "v_mov_b32 v170, v178\n"
      "v_mov_b32 v171, v178\n"
      "v_mov_b32 v172, v179\n"
      "v_mov_b32 v173, v179\n"
      "v_mov_b32 v174, v179\n"
      "v_mov_b32 v175, v179\n"
      "v_sub_f32 v64, v64, v188\n"
      "v_sub_f32 v65, v65, v188\n"
      "v_sub_f32 v66, v66, v188\n"
      "v_sub_f32 v67, v67, v188\n"
      "v_sub_f32 v68, v68, v188\n"
      "v_sub_f32 v69, v69, v188\n"
      "v_sub_f32 v70, v70, v188\n"
      "v_sub_f32 v71, v71, v188\n"
      "v_sub_f32 v72, v72, v189\n"
      "v_sub_f32 v73, v73, v189\n"
      "v_sub_f32 v74, v74, v189\n"
      "v_sub_f32 v75, v75, v189\n"
      "v_sub_f32 v76, v76, v189\n"
      "v_sub_f32 v77, v77, v189\n"
      "v_sub_f32 v78, v78, v189\n"
      "v_sub_f32 v79, v79, v189\n"
      "v_sub_f32 v80, v80, v190\n"
      "v_sub_f32 v81, v81, v190\n"
      "v_sub_f32 v82, v82, v190\n"
      "v_sub_f32 v83, v83, v190\n"
      "v_sub_f32 v84, v84, v190\n"
      "v_sub_f32 v85, v85, v190\n"
      "v_sub_f32 v86, v86, v190\n"
      "v_sub_f32 v87, v87, v190\n"
      "v_sub_f32 v88, v88, v191\n"
      "v_sub_f32 v89, v89, v191\n"
      "v_sub_f32 v90, v90, v191\n"
      "v_sub_f32 v91, v91, v191\n"
      "v_sub_f32 v92, v92, v191\n"
      "v_sub_f32 v93, v93, v191\n"
      "v_sub_f32 v94, v94, v191\n"
      "v_sub_f32 v95, v95, v191\n"
      "v_sub_f32 v96, v96, v188\n"
      "v_sub_f32 v97, v97, v188\n"
      "v_sub_f32 v98, v98, v188\n"
      "v_sub_f32 v99, v99, v188\n"
      "v_sub_f32 v100, v100, v188\n"
      "v_sub_f32 v101, v101, v188\n"
      "v_sub_f32 v102, v102, v188\n"
      "v_sub_f32 v103, v103, v188\n"
      "v_sub_f32 v104, v104, v189\n"
      "v_sub_f32 v105, v105, v189\n"
      "v_sub_f32 v106, v106, v189\n"
      "v_sub_f32 v107, v107, v189\n"
      "v_sub_f32 v108, v108, v189\n"
      "v_sub_f32 v109, v109, v189\n"
      "v_sub_f32 v110, v110, v189\n"
      "v_sub_f32 v111, v111, v189\n"
      "v_sub_f32 v112, v112, v190\n"
      "v_sub_f32 v113, v113, v190\n"
      "v_sub_f32 v114, v114, v190\n"
      "v_sub_f32 v115, v115, v190\n"
      "v_sub_f32 v116, v116, v190\n"
      "v_sub_f32 v117, v117, v190\n"
      "v_sub_f32 v118, v118, v190\n"
      "v_sub_f32 v119, v119, v190\n"
      "v_sub_f32 v120, v120, v191\n"
      "v_sub_f32 v121, v121, v191\n"
      "v_sub_f32 v122, v122, v191\n"
      "v_sub_f32 v123, v123, v191\n"
      "v_sub_f32 v124, v124, v191\n"
      "v_sub_f32 v125, v125, v191\n"
      "v_sub_f32 v126, v126, v191\n"
      "v_sub_f32 v127, v127, v191\n"
      "v_mul_f32 v0, v0, v192\n"
      "v_mul_f32 v1, v1, v192\n"
      "v_mul_f32 v2, v2, v192\n"
      "v_mul_f32 v3, v3, v192\n"
      "v_mul_f32 v4, v4, v193\n"
      "v_mul_f32 v5, v5, v193\n"
      "v_mul_f32 v6, v6, v193\n"
      "v_mul_f32 v7, v7, v193\n"
      "v_mul_f32 v8, v8, v192\n"
      "v_mul_f32 v9, v9, v192\n"
      "v_mul_f32 v10, v10, v192\n"
      "v_mul_f32 v11, v11, v192\n"
      "v_mul_f32 v12, v12, v193\n"
      "v_mul_f32 v13, v13, v193\n"
      "v_mul_f32 v14, v14, v193\n"
      "v_mul_f32 v15, v15, v193\n"
      "v_mul_f32 v16, v16, v192\n"
      "v_mul_f32 v17, v17, v192\n"
      "v_mul_f32 v18, v18, v192\n"
      "v_mul_f32 v19, v19, v192\n"
      "v_mul_f32 v20, v20, v193\n"
      "v_mul_f32 v21, v21, v193\n"
      "v_mul_f32 v22, v22, v193\n"
      "v_mul_f32 v23, v23, v193\n"
      "v_mul_f32 v24, v24, v192\n"
      "v_mul_f32 v25, v25, v192\n"
      "v_mul_f32 v26, v26, v192\n"
      "v_mul_f32 v27, v27, v192\n"
      "v_mul_f32 v28, v28, v193\n"
      "v_mul_f32 v29, v29, v193\n"
      "v_mul_f32 v30, v30, v193\n"
      "v_mul_f32 v31, v31, v193\n"
      "v_mul_f32 v32, v32, v192\n"
      "v_mul_f32 v33, v33, v192\n"
      "v_mul_f32 v34, v34, v192\n"
      "v_mul_f32 v35, v35, v192\n"
      "v_mul_f32 v36, v36, v193\n"
      "v_mul_f32 v37, v37, v193\n"
      "v_mul_f32 v38, v38, v193\n"
      "v_mul_f32 v39, v39, v193\n"
      "v_mul_f32 v40, v40, v192\n"
      "v_mul_f32 v41, v41, v192\n"
      "v_mul_f32 v42, v42, v192\n"
      "v_mul_f32 v43, v43, v192\n"
      "v_mul_f32 v44, v44, v193\n"
      "v_mul_f32 v45, v45, v193\n"
      "v_mul_f32 v46, v46, v193\n"
      "v_mul_f32 v47, v47, v193\n"
      "v_mul_f32 v48, v48, v192\n"
      "v_mul_f32 v49, v49, v192\n"
      "v_mul_f32 v50, v50, v192\n"
      "v_mul_f32 v51, v51, v192\n"
      "v_mul_f32 v52, v52, v193\n"
      "v_mul_f32 v53, v53, v193\n"
      "v_mul_f32 v54, v54, v193\n"
      "v_mul_f32 v55, v55, v193\n"
      "v_mul_f32 v56, v56, v192\n"
      "v_mul_f32 v57, v57, v192\n"
      "v_mul_f32 v58, v58, v192\n"
      "v_mul_f32 v59, v59, v192\n"
      "v_mul_f32 v60, v60, v193\n"
      "v_mul_f32 v61, v61, v193\n"
      "v_mul_f32 v62, v62, v193\n"
      "v_mul_f32 v63, v63, v193\n"
      "v_accvgpr_read_b32 v196, a144\n"
      "v_accvgpr_read_b32 v197, a145\n"
      "v_accvgpr_read_b32 v198, a146\n"
      "v_accvgpr_read_b32 v199, a147\n"
      "v_mul_f32 v196, v196, v194\n"
      "v_mul_f32 v197, v197, v194\n"
      "v_mul_f32 v198, v198, v194\n"
      "v_mul_f32 v199, v199, v194\n"
      "v_accvgpr_write_b32 a144, v196\n"
      "v_accvgpr_write_b32 a145, v197\n"
      "v_accvgpr_write_b32 a146, v198\n"
      "v_accvgpr_write_b32 a147, v199\n"
      "v_accvgpr_read_b32 v196, a148\n"
      "v_accvgpr_read_b32 v197, a149\n"
      "v_accvgpr_read_b32 v198, a150\n"
      "v_accvgpr_read_b32 v199, a151\n"
      "v_mul_f32 v196, v196, v195\n"
      "v_mul_f32 v197, v197, v195\n"
      "v_mul_f32 v198, v198, v195\n"
      "v_mul_f32 v199, v199, v195\n"
      "v_accvgpr_write_b32 a148, v196\n"
      "v_accvgpr_write_b32 a149, v197\n"
      "v_accvgpr_write_b32 a150, v198\n"
      "v_accvgpr_write_b32 a151, v199\n"
      "v_accvgpr_read_b32 v196, a152\n"
      "v_accvgpr_read_b32 v197, a153\n"
      "v_accvgpr_read_b32 v198, a154\n"
      "v_accvgpr_read_b32 v199, a155\n"
      "v_mul_f32 v196, v196, v194\n"
      "v_mul_f32 v197, v197, v194\n"
      "v_mul_f32 v198, v198, v194\n"
      "v_mul_f32 v199, v199, v194\n"
      "v_accvgpr_write_b32 a152, v196\n"
      "v_accvgpr_write_b32 a153, v197\n"
      "v_accvgpr_write_b32 a154, v198\n"
      "v_accvgpr_write_b32 a155, v199\n"
      "v_accvgpr_read_b32 v196, a156\n"
      "v_accvgpr_read_b32 v197, a157\n"
      "v_accvgpr_read_b32 v198, a158\n"
      "v_accvgpr_read_b32 v199, a159\n"
      "v_mul_f32 v196, v196, v195\n"
      "v_mul_f32 v197, v197, v195\n"
      "v_mul_f32 v198, v198, v195\n"
      "v_mul_f32 v199, v199, v195\n"
      "v_accvgpr_write_b32 a156, v196\n"
      "v_accvgpr_write_b32 a157, v197\n"
      "v_accvgpr_write_b32 a158, v198\n"
      "v_accvgpr_write_b32 a159, v199\n"
      "v_accvgpr_read_b32 v196, a160\n"
      "v_accvgpr_read_b32 v197, a161\n"
      "v_accvgpr_read_b32 v198, a162\n"
      "v_accvgpr_read_b32 v199, a163\n"
      "v_mul_f32 v196, v196, v194\n"
      "v_mul_f32 v197, v197, v194\n"
      "v_mul_f32 v198, v198, v194\n"
      "v_mul_f32 v199, v199, v194\n"
      "v_accvgpr_write_b32 a160, v196\n"
      "v_accvgpr_write_b32 a161, v197\n"
      "v_accvgpr_write_b32 a162, v198\n"
      "v_accvgpr_write_b32 a163, v199\n"
      "v_accvgpr_read_b32 v196, a164\n"
      "v_accvgpr_read_b32 v197, a165\n"
      "v_accvgpr_read_b32 v198, a166\n"
      "v_accvgpr_read_b32 v199, a167\n"
      "v_mul_f32 v196, v196, v195\n"
      "v_mul_f32 v197, v197, v195\n"
      "v_mul_f32 v198, v198, v195\n"
      "v_mul_f32 v199, v199, v195\n"
      "v_accvgpr_write_b32 a164, v196\n"
      "v_accvgpr_write_b32 a165, v197\n"
      "v_accvgpr_write_b32 a166, v198\n"
      "v_accvgpr_write_b32 a167, v199\n"
      "v_accvgpr_read_b32 v196, a168\n"
      "v_accvgpr_read_b32 v197, a169\n"
      "v_accvgpr_read_b32 v198, a170\n"
      "v_accvgpr_read_b32 v199, a171\n"
      "v_mul_f32 v196, v196, v194\n"
      "v_mul_f32 v197, v197, v194\n"
      "v_mul_f32 v198, v198, v194\n"
      "v_mul_f32 v199, v199, v194\n"
      "v_accvgpr_write_b32 a168, v196\n"
      "v_accvgpr_write_b32 a169, v197\n"
      "v_accvgpr_write_b32 a170, v198\n"
      "v_accvgpr_write_b32 a171, v199\n"
      "v_accvgpr_read_b32 v196, a172\n"
      "v_accvgpr_read_b32 v197, a173\n"
      "v_accvgpr_read_b32 v198, a174\n"
      "v_accvgpr_read_b32 v199, a175\n"
      "v_mul_f32 v196, v196, v195\n"
      "v_mul_f32 v197, v197, v195\n"
      "v_mul_f32 v198, v198, v195\n"
      "v_mul_f32 v199, v199, v195\n"
      "v_accvgpr_write_b32 a172, v196\n"
      "v_accvgpr_write_b32 a173, v197\n"
      "v_accvgpr_write_b32 a174, v198\n"
      "v_accvgpr_write_b32 a175, v199\n"
      "v_accvgpr_read_b32 v196, a176\n"
      "v_accvgpr_read_b32 v197, a177\n"
      "v_accvgpr_read_b32 v198, a178\n"
      "v_accvgpr_read_b32 v199, a179\n"
      "v_mul_f32 v196, v196, v194\n"
      "v_mul_f32 v197, v197, v194\n"
      "v_mul_f32 v198, v198, v194\n"
      "v_mul_f32 v199, v199, v194\n"
      "v_accvgpr_write_b32 a176, v196\n"
      "v_accvgpr_write_b32 a177, v197\n"
      "v_accvgpr_write_b32 a178, v198\n"
      "v_accvgpr_write_b32 a179, v199\n"
      "v_accvgpr_read_b32 v196, a180\n"
      "v_accvgpr_read_b32 v197, a181\n"
      "v_accvgpr_read_b32 v198, a182\n"
      "v_accvgpr_read_b32 v199, a183\n"
      "v_mul_f32 v196, v196, v195\n"
      "v_mul_f32 v197, v197, v195\n"
      "v_mul_f32 v198, v198, v195\n"
      "v_mul_f32 v199, v199, v195\n"
      "v_accvgpr_write_b32 a180, v196\n"
      "v_accvgpr_write_b32 a181, v197\n"
      "v_accvgpr_write_b32 a182, v198\n"
      "v_accvgpr_write_b32 a183, v199\n"
      "v_accvgpr_read_b32 v196, a184\n"
      "v_accvgpr_read_b32 v197, a185\n"
      "v_accvgpr_read_b32 v198, a186\n"
      "v_accvgpr_read_b32 v199, a187\n"
      "v_mul_f32 v196, v196, v194\n"
      "v_mul_f32 v197, v197, v194\n"
      "v_mul_f32 v198, v198, v194\n"
      "v_mul_f32 v199, v199, v194\n"
      "v_accvgpr_write_b32 a184, v196\n"
      "v_accvgpr_write_b32 a185, v197\n"
      "v_accvgpr_write_b32 a186, v198\n"
      "v_accvgpr_write_b32 a187, v199\n"
      "v_accvgpr_read_b32 v196, a188\n"
      "v_accvgpr_read_b32 v197, a189\n"
      "v_accvgpr_read_b32 v198, a190\n"
      "v_accvgpr_read_b32 v199, a191\n"
      "v_mul_f32 v196, v196, v195\n"
      "v_mul_f32 v197, v197, v195\n"
      "v_mul_f32 v198, v198, v195\n"
      "v_mul_f32 v199, v199, v195\n"
      "v_accvgpr_write_b32 a188, v196\n"
      "v_accvgpr_write_b32 a189, v197\n"
      "v_accvgpr_write_b32 a190, v198\n"
      "v_accvgpr_write_b32 a191, v199\n"
      "v_accvgpr_read_b32 v196, a192\n"
      "v_accvgpr_read_b32 v197, a193\n"
      "v_accvgpr_read_b32 v198, a194\n"
      "v_accvgpr_read_b32 v199, a195\n"
      "v_mul_f32 v196, v196, v194\n"
      "v_mul_f32 v197, v197, v194\n"
      "v_mul_f32 v198, v198, v194\n"
      "v_mul_f32 v199, v199, v194\n"
      "v_accvgpr_write_b32 a192, v196\n"
      "v_accvgpr_write_b32 a193, v197\n"
      "v_accvgpr_write_b32 a194, v198\n"
      "v_accvgpr_write_b32 a195, v199\n"
      "v_accvgpr_read_b32 v196, a196\n"
      "v_accvgpr_read_b32 v197, a197\n"
      "v_accvgpr_read_b32 v198, a198\n"
      "v_accvgpr_read_b32 v199, a199\n"
      "v_mul_f32 v196, v196, v195\n"
      "v_mul_f32 v197, v197, v195\n"
      "v_mul_f32 v198, v198, v195\n"
      "v_mul_f32 v199, v199, v195\n"
      "v_accvgpr_write_b32 a196, v196\n"
      "v_accvgpr_write_b32 a197, v197\n"
      "v_accvgpr_write_b32 a198, v198\n"
      "v_accvgpr_write_b32 a199, v199\n"
      "v_accvgpr_read_b32 v196, a200\n"
      "v_accvgpr_read_b32 v197, a201\n"
      "v_accvgpr_read_b32 v198, a202\n"
      "v_accvgpr_read_b32 v199, a203\n"
      "v_mul_f32 v196, v196, v194\n"
      "v_mul_f32 v197, v197, v194\n"
      "v_mul_f32 v198, v198, v194\n"
      "v_mul_f32 v199, v199, v194\n"
      "v_accvgpr_write_b32 a200, v196\n"
      "v_accvgpr_write_b32 a201, v197\n"
      "v_accvgpr_write_b32 a202, v198\n"
      "v_accvgpr_write_b32 a203, v199\n"
      "v_accvgpr_read_b32 v196, a204\n"
      "v_accvgpr_read_b32 v197, a205\n"
      "v_accvgpr_read_b32 v198, a206\n"
      "v_accvgpr_read_b32 v199, a207\n"
      "v_mul_f32 v196, v196, v195\n"
      "v_mul_f32 v197, v197, v195\n"
      "v_mul_f32 v198, v198, v195\n"
      "v_mul_f32 v199, v199, v195\n"
      "v_accvgpr_write_b32 a204, v196\n"
      "v_accvgpr_write_b32 a205, v197\n"
      "v_accvgpr_write_b32 a206, v198\n"
      "v_accvgpr_write_b32 a207, v199\n"
      "v_mul_f32 v180, v180, v192\n"
      "v_mul_f32 v181, v181, v193\n"
      "v_mul_f32 v182, v182, v194\n"
      "v_mul_f32 v183, v183, v195\n"
      "v_exp_f32 v188, v64\n"
      "v_exp_f32 v189, v65\n"
      "v_exp_f32 v190, v66\n"
      "v_exp_f32 v191, v67\n"
      "v_cvt_pk_bf16_f32 v128, v188, v189\n"
      "v_cvt_pk_bf16_f32 v129, v190, v191\n"
      "v_exp_f32 v192, v68\n"
      "v_exp_f32 v193, v69\n"
      "v_exp_f32 v194, v70\n"
      "v_exp_f32 v195, v71\n"
      "v_cvt_pk_bf16_f32 v130, v192, v193\n"
      "v_cvt_pk_bf16_f32 v131, v194, v195\n"
      "v_exp_f32 v188, v72\n"
      "v_exp_f32 v189, v73\n"
      "v_exp_f32 v190, v74\n"
      "v_exp_f32 v191, v75\n"
      "v_cvt_pk_bf16_f32 v132, v188, v189\n"
      "v_cvt_pk_bf16_f32 v133, v190, v191\n"
      "v_exp_f32 v192, v76\n"
      "v_exp_f32 v193, v77\n"
      "v_exp_f32 v194, v78\n"
      "v_exp_f32 v195, v79\n"
      "v_cvt_pk_bf16_f32 v134, v192, v193\n"
      "v_cvt_pk_bf16_f32 v135, v194, v195\n"
      "v_exp_f32 v188, v80\n"
      "v_exp_f32 v189, v81\n"
      "v_exp_f32 v190, v82\n"
      "v_exp_f32 v191, v83\n"
      "v_cvt_pk_bf16_f32 v136, v188, v189\n"
      "v_cvt_pk_bf16_f32 v137, v190, v191\n"
      "v_exp_f32 v192, v84\n"
      "v_exp_f32 v193, v85\n"
      "v_exp_f32 v194, v86\n"
      "v_exp_f32 v195, v87\n"
      "v_cvt_pk_bf16_f32 v138, v192, v193\n"
      "v_cvt_pk_bf16_f32 v139, v194, v195\n"
      "v_exp_f32 v188, v88\n"
      "v_exp_f32 v189, v89\n"
      "v_exp_f32 v190, v90\n"
      "v_exp_f32 v191, v91\n"
      "v_cvt_pk_bf16_f32 v140, v188, v189\n"
      "v_cvt_pk_bf16_f32 v141, v190, v191\n"
      "v_exp_f32 v192, v92\n"
      "v_exp_f32 v193, v93\n"
      "v_exp_f32 v194, v94\n"
      "v_exp_f32 v195, v95\n"
      "v_cvt_pk_bf16_f32 v142, v192, v193\n"
      "v_cvt_pk_bf16_f32 v143, v194, v195\n"
      "s_nop 1\n"
      "v_mfma_f32_16x16x32_bf16 v[184:187], a[128:131], v[128:131], 0\n"
      "v_mfma_f32_16x16x32_bf16 v[184:187], a[132:135], v[132:135], v[184:187]\n"
      "v_mfma_f32_16x16x32_bf16 v[184:187], a[136:139], v[136:139], v[184:187]\n"
      "v_mfma_f32_16x16x32_bf16 v[184:187], a[140:143], v[140:143], v[184:187]\n"
      "s_nop 7\n"
      "s_setpc_b64 s[96:97]\n"
      "FB1_%=:\n"
      "v_mov_b32 v196, 139248\n"
      "s_nop 7\n"
      "ds_write_b32 v196, v196\n"
      "v_max3_f32 v188, v96, v97, 0\n"
      "v_max3_f32 v189, v104, v105, 0\n"
      "v_max3_f32 v190, v112, v113, 0\n"
      "v_max3_f32 v191, v120, v121, 0\n"
      "v_max3_f32 v188, v188, v98, v99\n"
      "v_max3_f32 v189, v189, v106, v107\n"
      "v_max3_f32 v190, v190, v114, v115\n"
      "v_max3_f32 v191, v191, v122, v123\n"
      "v_max3_f32 v188, v188, v100, v101\n"
      "v_max3_f32 v189, v189, v108, v109\n"
      "v_max3_f32 v190, v190, v116, v117\n"
      "v_max3_f32 v191, v191, v124, v125\n"
      "v_max3_f32 v188, v188, v102, v103\n"
      "v_max3_f32 v189, v189, v110, v111\n"
      "v_max3_f32 v190, v190, v118, v119\n"
      "v_max3_f32 v191, v191, v126, v127\n"
      "v_mov_b32 v192, v188\n"
      "v_mov_b32 v193, v189\n"
      "v_mov_b32 v194, v190\n"
      "v_mov_b32 v195, v191\n"
      "v_permlane16_swap_b32 v188, v192\n"
      "v_permlane16_swap_b32 v189, v193\n"
      "v_permlane16_swap_b32 v190, v194\n"
      "v_permlane16_swap_b32 v191, v195\n"
      "v_max_f32 v188, v188, v192\n"
      "v_max_f32 v189, v189, v193\n"
      "v_max_f32 v190, v190, v194\n"
      "v_max_f32 v191, v191, v195\n"
      "v_mov_b32 v192, v188\n"
      "v_mov_b32 v193, v189\n"
      "v_mov_b32 v194, v190\n"
      "v_mov_b32 v195, v191\n"
      "v_permlane32_swap_b32 v188, v192\n"
      "v_permlane32_swap_b32 v189, v193\n"
      "v_permlane32_swap_b32 v190, v194\n"
      "v_permlane32_swap_b32 v191, v195\n"
      "v_max_f32 v188, v188, v192\n"
      "v_max_f32 v189, v189, v193\n"
      "v_max_f32 v190, v190, v194\n"
      "v_max_f32 v191, v191, v195\n"
      "RS1_%=:\n"
      "v_exp_f32 v192, -v188\n"
      "v_exp_f32 v193, -v189\n"
      "v_exp_f32 v194, -v190\n"
      "v_exp_f32 v195, -v191\n"
      "v_sub_f32 v176, v176, v188\n"
      "v_sub_f32 v177, v177, v189\n"
      "v_sub_f32 v178, v178, v190\n"
      "v_sub_f32 v179, v179, v191\n"
      "v_mov_b32 v160, v176\n"
      "v_mov_b32 v161, v176\n"
      "v_mov_b32 v162, v176\n"
      "v_mov_b32 v163, v176\n"
      "v_mov_b32 v164, v177\n"
      "v_mov_b32 v165, v177\n"
      "v_mov_b32 v166, v177\n"
      "v_mov_b32 v167, v177\n"
      "v_mov_b32 v168, v178\n"
      "v_mov_b32 v169, v178\n"
      "v_mov_b32 v170, v178\n"
      "v_mov_b32 v171, v178\n"
      "v_mov_b32 v172, v179\n"
      "v_mov_b32 v173, v179\n"
      "v_mov_b32 v174, v179\n"
      "v_mov_b32 v175, v179\n"
      "v_sub_f32 v64, v64, v188\n"
      "v_sub_f32 v65, v65, v188\n"
      "v_sub_f32 v66, v66, v188\n"
      "v_sub_f32 v67, v67, v188\n"
      "v_sub_f32 v68, v68, v188\n"
      "v_sub_f32 v69, v69, v188\n"
      "v_sub_f32 v70, v70, v188\n"
      "v_sub_f32 v71, v71, v188\n"
      "v_sub_f32 v72, v72, v189\n"
      "v_sub_f32 v73, v73, v189\n"
      "v_sub_f32 v74, v74, v189\n"
      "v_sub_f32 v75, v75, v189\n"
      "v_sub_f32 v76, v76, v189\n"
      "v_sub_f32 v77, v77, v189\n"
      "v_sub_f32 v78, v78, v189\n"
      "v_sub_f32 v79, v79, v189\n"
      "v_sub_f32 v80, v80, v190\n"
      "v_sub_f32 v81, v81, v190\n"
      "v_sub_f32 v82, v82, v190\n"
      "v_sub_f32 v83, v83, v190\n"
      "v_sub_f32 v84, v84, v190\n"
      "v_sub_f32 v85, v85, v190\n"
      "v_sub_f32 v86, v86, v190\n"
      "v_sub_f32 v87, v87, v190\n"
      "v_sub_f32 v88, v88, v191\n"
      "v_sub_f32 v89, v89, v191\n"
      "v_sub_f32 v90, v90, v191\n"
      "v_sub_f32 v91, v91, v191\n"
      "v_sub_f32 v92, v92, v191\n"
      "v_sub_f32 v93, v93, v191\n"
      "v_sub_f32 v94, v94, v191\n"
      "v_sub_f32 v95, v95, v191\n"
      "v_sub_f32 v96, v96, v188\n"
      "v_sub_f32 v97, v97, v188\n"
      "v_sub_f32 v98, v98, v188\n"
      "v_sub_f32 v99, v99, v188\n"
      "v_sub_f32 v100, v100, v188\n"
      "v_sub_f32 v101, v101, v188\n"
      "v_sub_f32 v102, v102, v188\n"
      "v_sub_f32 v103, v103, v188\n"
      "v_sub_f32 v104, v104, v189\n"
      "v_sub_f32 v105, v105, v189\n"
      "v_sub_f32 v106, v106, v189\n"
      "v_sub_f32 v107, v107, v189\n"
      "v_sub_f32 v108, v108, v189\n"
      "v_sub_f32 v109, v109, v189\n"
      "v_sub_f32 v110, v110, v189\n"
      "v_sub_f32 v111, v111, v189\n"
      "v_sub_f32 v112, v112, v190\n"
      "v_sub_f32 v113, v113, v190\n"
      "v_sub_f32 v114, v114, v190\n"
      "v_sub_f32 v115, v115, v190\n"
      "v_sub_f32 v116, v116, v190\n"
      "v_sub_f32 v117, v117, v190\n"
      "v_sub_f32 v118, v118, v190\n"
      "v_sub_f32 v119, v119, v190\n"
      "v_sub_f32 v120, v120, v191\n"
      "v_sub_f32 v121, v121, v191\n"
      "v_sub_f32 v122, v122, v191\n"
      "v_sub_f32 v123, v123, v191\n"
      "v_sub_f32 v124, v124, v191\n"
      "v_sub_f32 v125, v125, v191\n"
      "v_sub_f32 v126, v126, v191\n"
      "v_sub_f32 v127, v127, v191\n"
      "v_mul_f32 v0, v0, v192\n"
      "v_mul_f32 v1, v1, v192\n"
      "v_mul_f32 v2, v2, v192\n"
      "v_mul_f32 v3, v3, v192\n"
      "v_mul_f32 v4, v4, v193\n"
      "v_mul_f32 v5, v5, v193\n"
      "v_mul_f32 v6, v6, v193\n"
      "v_mul_f32 v7, v7, v193\n"
      "v_mul_f32 v8, v8, v192\n"
      "v_mul_f32 v9, v9, v192\n"
      "v_mul_f32 v10, v10, v192\n"
      "v_mul_f32 v11, v11, v192\n"
      "v_mul_f32 v12, v12, v193\n"
      "v_mul_f32 v13, v13, v193\n"
      "v_mul_f32 v14, v14, v193\n"
      "v_mul_f32 v15, v15, v193\n"
      "v_mul_f32 v16, v16, v192\n"
      "v_mul_f32 v17, v17, v192\n"
      "v_mul_f32 v18, v18, v192\n"
      "v_mul_f32 v19, v19, v192\n"
      "v_mul_f32 v20, v20, v193\n"
      "v_mul_f32 v21, v21, v193\n"
      "v_mul_f32 v22, v22, v193\n"
      "v_mul_f32 v23, v23, v193\n"
      "v_mul_f32 v24, v24, v192\n"
      "v_mul_f32 v25, v25, v192\n"
      "v_mul_f32 v26, v26, v192\n"
      "v_mul_f32 v27, v27, v192\n"
      "v_mul_f32 v28, v28, v193\n"
      "v_mul_f32 v29, v29, v193\n"
      "v_mul_f32 v30, v30, v193\n"
      "v_mul_f32 v31, v31, v193\n"
      "v_mul_f32 v32, v32, v192\n"
      "v_mul_f32 v33, v33, v192\n"
      "v_mul_f32 v34, v34, v192\n"
      "v_mul_f32 v35, v35, v192\n"
      "v_mul_f32 v36, v36, v193\n"
      "v_mul_f32 v37, v37, v193\n"
      "v_mul_f32 v38, v38, v193\n"
      "v_mul_f32 v39, v39, v193\n"
      "v_mul_f32 v40, v40, v192\n"
      "v_mul_f32 v41, v41, v192\n"
      "v_mul_f32 v42, v42, v192\n"
      "v_mul_f32 v43, v43, v192\n"
      "v_mul_f32 v44, v44, v193\n"
      "v_mul_f32 v45, v45, v193\n"
      "v_mul_f32 v46, v46, v193\n"
      "v_mul_f32 v47, v47, v193\n"
      "v_mul_f32 v48, v48, v192\n"
      "v_mul_f32 v49, v49, v192\n"
      "v_mul_f32 v50, v50, v192\n"
      "v_mul_f32 v51, v51, v192\n"
      "v_mul_f32 v52, v52, v193\n"
      "v_mul_f32 v53, v53, v193\n"
      "v_mul_f32 v54, v54, v193\n"
      "v_mul_f32 v55, v55, v193\n"
      "v_mul_f32 v56, v56, v192\n"
      "v_mul_f32 v57, v57, v192\n"
      "v_mul_f32 v58, v58, v192\n"
      "v_mul_f32 v59, v59, v192\n"
      "v_mul_f32 v60, v60, v193\n"
      "v_mul_f32 v61, v61, v193\n"
      "v_mul_f32 v62, v62, v193\n"
      "v_mul_f32 v63, v63, v193\n"
      "v_accvgpr_read_b32 v196, a144\n"
      "v_accvgpr_read_b32 v197, a145\n"
      "v_accvgpr_read_b32 v198, a146\n"
      "v_accvgpr_read_b32 v199, a147\n"
      "v_mul_f32 v196, v196, v194\n"
      "v_mul_f32 v197, v197, v194\n"
      "v_mul_f32 v198, v198, v194\n"
      "v_mul_f32 v199, v199, v194\n"
      "v_accvgpr_write_b32 a144, v196\n"
      "v_accvgpr_write_b32 a145, v197\n"
      "v_accvgpr_write_b32 a146, v198\n"
      "v_accvgpr_write_b32 a147, v199\n"
      "v_accvgpr_read_b32 v196, a148\n"
      "v_accvgpr_read_b32 v197, a149\n"
      "v_accvgpr_read_b32 v198, a150\n"
      "v_accvgpr_read_b32 v199, a151\n"
      "v_mul_f32 v196, v196, v195\n"
      "v_mul_f32 v197, v197, v195\n"
      "v_mul_f32 v198, v198, v195\n"
      "v_mul_f32 v199, v199, v195\n"
      "v_accvgpr_write_b32 a148, v196\n"
      "v_accvgpr_write_b32 a149, v197\n"
      "v_accvgpr_write_b32 a150, v198\n"
      "v_accvgpr_write_b32 a151, v199\n"
      "v_accvgpr_read_b32 v196, a152\n"
      "v_accvgpr_read_b32 v197, a153\n"
      "v_accvgpr_read_b32 v198, a154\n"
      "v_accvgpr_read_b32 v199, a155\n"
      "v_mul_f32 v196, v196, v194\n"
      "v_mul_f32 v197, v197, v194\n"
      "v_mul_f32 v198, v198, v194\n"
      "v_mul_f32 v199, v199, v194\n"
      "v_accvgpr_write_b32 a152, v196\n"
      "v_accvgpr_write_b32 a153, v197\n"
      "v_accvgpr_write_b32 a154, v198\n"
      "v_accvgpr_write_b32 a155, v199\n"
      "v_accvgpr_read_b32 v196, a156\n"
      "v_accvgpr_read_b32 v197, a157\n"
      "v_accvgpr_read_b32 v198, a158\n"
      "v_accvgpr_read_b32 v199, a159\n"
      "v_mul_f32 v196, v196, v195\n"
      "v_mul_f32 v197, v197, v195\n"
      "v_mul_f32 v198, v198, v195\n"
      "v_mul_f32 v199, v199, v195\n"
      "v_accvgpr_write_b32 a156, v196\n"
      "v_accvgpr_write_b32 a157, v197\n"
      "v_accvgpr_write_b32 a158, v198\n"
      "v_accvgpr_write_b32 a159, v199\n"
      "v_accvgpr_read_b32 v196, a160\n"
      "v_accvgpr_read_b32 v197, a161\n"
      "v_accvgpr_read_b32 v198, a162\n"
      "v_accvgpr_read_b32 v199, a163\n"
      "v_mul_f32 v196, v196, v194\n"
      "v_mul_f32 v197, v197, v194\n"
      "v_mul_f32 v198, v198, v194\n"
      "v_mul_f32 v199, v199, v194\n"
      "v_accvgpr_write_b32 a160, v196\n"
      "v_accvgpr_write_b32 a161, v197\n"
      "v_accvgpr_write_b32 a162, v198\n"
      "v_accvgpr_write_b32 a163, v199\n"
      "v_accvgpr_read_b32 v196, a164\n"
      "v_accvgpr_read_b32 v197, a165\n"
      "v_accvgpr_read_b32 v198, a166\n"
      "v_accvgpr_read_b32 v199, a167\n"
      "v_mul_f32 v196, v196, v195\n"
      "v_mul_f32 v197, v197, v195\n"
      "v_mul_f32 v198, v198, v195\n"
      "v_mul_f32 v199, v199, v195\n"
      "v_accvgpr_write_b32 a164, v196\n"
      "v_accvgpr_write_b32 a165, v197\n"
      "v_accvgpr_write_b32 a166, v198\n"
      "v_accvgpr_write_b32 a167, v199\n"
      "v_accvgpr_read_b32 v196, a168\n"
      "v_accvgpr_read_b32 v197, a169\n"
      "v_accvgpr_read_b32 v198, a170\n"
      "v_accvgpr_read_b32 v199, a171\n"
      "v_mul_f32 v196, v196, v194\n"
      "v_mul_f32 v197, v197, v194\n"
      "v_mul_f32 v198, v198, v194\n"
      "v_mul_f32 v199, v199, v194\n"
      "v_accvgpr_write_b32 a168, v196\n"
      "v_accvgpr_write_b32 a169, v197\n"
      "v_accvgpr_write_b32 a170, v198\n"
      "v_accvgpr_write_b32 a171, v199\n"
      "v_accvgpr_read_b32 v196, a172\n"
      "v_accvgpr_read_b32 v197, a173\n"
      "v_accvgpr_read_b32 v198, a174\n"
      "v_accvgpr_read_b32 v199, a175\n"
      "v_mul_f32 v196, v196, v195\n"
      "v_mul_f32 v197, v197, v195\n"
      "v_mul_f32 v198, v198, v195\n"
      "v_mul_f32 v199, v199, v195\n"
      "v_accvgpr_write_b32 a172, v196\n"
      "v_accvgpr_write_b32 a173, v197\n"
      "v_accvgpr_write_b32 a174, v198\n"
      "v_accvgpr_write_b32 a175, v199\n"
      "v_accvgpr_read_b32 v196, a176\n"
      "v_accvgpr_read_b32 v197, a177\n"
      "v_accvgpr_read_b32 v198, a178\n"
      "v_accvgpr_read_b32 v199, a179\n"
      "v_mul_f32 v196, v196, v194\n"
      "v_mul_f32 v197, v197, v194\n"
      "v_mul_f32 v198, v198, v194\n"
      "v_mul_f32 v199, v199, v194\n"
      "v_accvgpr_write_b32 a176, v196\n"
      "v_accvgpr_write_b32 a177, v197\n"
      "v_accvgpr_write_b32 a178, v198\n"
      "v_accvgpr_write_b32 a179, v199\n"
      "v_accvgpr_read_b32 v196, a180\n"
      "v_accvgpr_read_b32 v197, a181\n"
      "v_accvgpr_read_b32 v198, a182\n"
      "v_accvgpr_read_b32 v199, a183\n"
      "v_mul_f32 v196, v196, v195\n"
      "v_mul_f32 v197, v197, v195\n"
      "v_mul_f32 v198, v198, v195\n"
      "v_mul_f32 v199, v199, v195\n"
      "v_accvgpr_write_b32 a180, v196\n"
      "v_accvgpr_write_b32 a181, v197\n"
      "v_accvgpr_write_b32 a182, v198\n"
      "v_accvgpr_write_b32 a183, v199\n"
      "v_accvgpr_read_b32 v196, a184\n"
      "v_accvgpr_read_b32 v197, a185\n"
      "v_accvgpr_read_b32 v198, a186\n"
      "v_accvgpr_read_b32 v199, a187\n"
      "v_mul_f32 v196, v196, v194\n"
      "v_mul_f32 v197, v197, v194\n"
      "v_mul_f32 v198, v198, v194\n"
      "v_mul_f32 v199, v199, v194\n"
      "v_accvgpr_write_b32 a184, v196\n"
      "v_accvgpr_write_b32 a185, v197\n"
      "v_accvgpr_write_b32 a186, v198\n"
      "v_accvgpr_write_b32 a187, v199\n"
      "v_accvgpr_read_b32 v196, a188\n"
      "v_accvgpr_read_b32 v197, a189\n"
      "v_accvgpr_read_b32 v198, a190\n"
      "v_accvgpr_read_b32 v199, a191\n"
      "v_mul_f32 v196, v196, v195\n"
      "v_mul_f32 v197, v197, v195\n"
      "v_mul_f32 v198, v198, v195\n"
      "v_mul_f32 v199, v199, v195\n"
      "v_accvgpr_write_b32 a188, v196\n"
      "v_accvgpr_write_b32 a189, v197\n"
      "v_accvgpr_write_b32 a190, v198\n"
      "v_accvgpr_write_b32 a191, v199\n"
      "v_accvgpr_read_b32 v196, a192\n"
      "v_accvgpr_read_b32 v197, a193\n"
      "v_accvgpr_read_b32 v198, a194\n"
      "v_accvgpr_read_b32 v199, a195\n"
      "v_mul_f32 v196, v196, v194\n"
      "v_mul_f32 v197, v197, v194\n"
      "v_mul_f32 v198, v198, v194\n"
      "v_mul_f32 v199, v199, v194\n"
      "v_accvgpr_write_b32 a192, v196\n"
      "v_accvgpr_write_b32 a193, v197\n"
      "v_accvgpr_write_b32 a194, v198\n"
      "v_accvgpr_write_b32 a195, v199\n"
      "v_accvgpr_read_b32 v196, a196\n"
      "v_accvgpr_read_b32 v197, a197\n"
      "v_accvgpr_read_b32 v198, a198\n"
      "v_accvgpr_read_b32 v199, a199\n"
      "v_mul_f32 v196, v196, v195\n"
      "v_mul_f32 v197, v197, v195\n"
      "v_mul_f32 v198, v198, v195\n"
      "v_mul_f32 v199, v199, v195\n"
      "v_accvgpr_write_b32 a196, v196\n"
      "v_accvgpr_write_b32 a197, v197\n"
      "v_accvgpr_write_b32 a198, v198\n"
      "v_accvgpr_write_b32 a199, v199\n"
      "v_accvgpr_read_b32 v196, a200\n"
      "v_accvgpr_read_b32 v197, a201\n"
      "v_accvgpr_read_b32 v198, a202\n"
      "v_accvgpr_read_b32 v199, a203\n"
      "v_mul_f32 v196, v196, v194\n"
      "v_mul_f32 v197, v197, v194\n"
      "v_mul_f32 v198, v198, v194\n"
      "v_mul_f32 v199, v199, v194\n"
      "v_accvgpr_write_b32 a200, v196\n"
      "v_accvgpr_write_b32 a201, v197\n"
      "v_accvgpr_write_b32 a202, v198\n"
      "v_accvgpr_write_b32 a203, v199\n"
      "v_accvgpr_read_b32 v196, a204\n"
      "v_accvgpr_read_b32 v197, a205\n"
      "v_accvgpr_read_b32 v198, a206\n"
      "v_accvgpr_read_b32 v199, a207\n"
      "v_mul_f32 v196, v196, v195\n"
      "v_mul_f32 v197, v197, v195\n"
      "v_mul_f32 v198, v198, v195\n"
      "v_mul_f32 v199, v199, v195\n"
      "v_accvgpr_write_b32 a204, v196\n"
      "v_accvgpr_write_b32 a205, v197\n"
      "v_accvgpr_write_b32 a206, v198\n"
      "v_accvgpr_write_b32 a207, v199\n"
      "v_mul_f32 v180, v180, v192\n"
      "v_mul_f32 v181, v181, v193\n"
      "v_mul_f32 v182, v182, v194\n"
      "v_mul_f32 v183, v183, v195\n"
      "v_lshlrev_b32 v196, 16, v128\n"
      "v_and_b32 v197, 0xffff0000, v128\n"
      "v_mul_f32 v196, v196, v192\n"
      "v_mul_f32 v197, v197, v192\n"
      "v_cvt_pk_bf16_f32 v128, v196, v197\n"
      "v_lshlrev_b32 v196, 16, v129\n"
      "v_and_b32 v197, 0xffff0000, v129\n"
      "v_mul_f32 v196, v196, v192\n"
      "v_mul_f32 v197, v197, v192\n"
      "v_cvt_pk_bf16_f32 v129, v196, v197\n"
      "v_lshlrev_b32 v196, 16, v130\n"
      "v_and_b32 v197, 0xffff0000, v130\n"
      "v_mul_f32 v196, v196, v192\n"
      "v_mul_f32 v197, v197, v192\n"
      "v_cvt_pk_bf16_f32 v130, v196, v197\n"
      "v_lshlrev_b32 v196, 16, v131\n"
      "v_and_b32 v197, 0xffff0000, v131\n"
      "v_mul_f32 v196, v196, v192\n"
      "v_mul_f32 v197, v197, v192\n"
      "v_cvt_pk_bf16_f32 v131, v196, v197\n"
      "v_lshlrev_b32 v196, 16, v132\n"
      "v_and_b32 v197, 0xffff0000, v132\n"
      "v_mul_f32 v196, v196, v193\n"
      "v_mul_f32 v197, v197, v193\n"
      "v_cvt_pk_bf16_f32 v132, v196, v197\n"
      "v_lshlrev_b32 v196, 16, v133\n"
      "v_and_b32 v197, 0xffff0000, v133\n"
      "v_mul_f32 v196, v196, v193\n"
      "v_mul_f32 v197, v197, v193\n"
      "v_cvt_pk_bf16_f32 v133, v196, v197\n"
      "v_lshlrev_b32 v196, 16, v134\n"
      "v_and_b32 v197, 0xffff0000, v134\n"
      "v_mul_f32 v196, v196, v193\n"
      "v_mul_f32 v197, v197, v193\n"
      "v_cvt_pk_bf16_f32 v134, v196, v197\n"
      "v_lshlrev_b32 v196, 16, v135\n"
      "v_and_b32 v197, 0xffff0000, v135\n"
      "v_mul_f32 v196, v196, v193\n"
      "v_mul_f32 v197, v197, v193\n"
      "v_cvt_pk_bf16_f32 v135, v196, v197\n"
      "v_lshlrev_b32 v196, 16, v136\n"
      "v_and_b32 v197, 0xffff0000, v136\n"
      "v_mul_f32 v196, v196, v194\n"
      "v_mul_f32 v197, v197, v194\n"
      "v_cvt_pk_bf16_f32 v136, v196, v197\n"
      "v_lshlrev_b32 v196, 16, v137\n"
      "v_and_b32 v197, 0xffff0000, v137\n"
      "v_mul_f32 v196, v196, v194\n"
      "v_mul_f32 v197, v197, v194\n"
      "v_cvt_pk_bf16_f32 v137, v196, v197\n"
      "v_lshlrev_b32 v196, 16, v138\n"
      "v_and_b32 v197, 0xffff0000, v138\n"
      "v_mul_f32 v196, v196, v194\n"
      "v_mul_f32 v197, v197, v194\n"
      "v_cvt_pk_bf16_f32 v138, v196, v197\n"
      "v_lshlrev_b32 v196, 16, v139\n"
      "v_and_b32 v197, 0xffff0000, v139\n"
      "v_mul_f32 v196, v196, v194\n"
      "v_mul_f32 v197, v197, v194\n"
      "v_cvt_pk_bf16_f32 v139, v196, v197\n"
      "v_lshlrev_b32 v196, 16, v140\n"
      "v_and_b32 v197, 0xffff0000, v140\n"
      "v_mul_f32 v196, v196, v195\n"
      "v_mul_f32 v197, v197, v195\n"
      "v_cvt_pk_bf16_f32 v140, v196, v197\n"
      "v_lshlrev_b32 v196, 16, v141\n"
      "v_and_b32 v197, 0xffff0000, v141\n"
      "v_mul_f32 v196, v196, v195\n"
      "v_mul_f32 v197, v197, v195\n"
      "v_cvt_pk_bf16_f32 v141, v196, v197\n"
      "v_lshlrev_b32 v196, 16, v142\n"
      "v_and_b32 v197, 0xffff0000, v142\n"
      "v_mul_f32 v196, v196, v195\n"
      "v_mul_f32 v197, v197, v195\n"
      "v_cvt_pk_bf16_f32 v142, v196, v197\n"
      "v_lshlrev_b32 v196, 16, v143\n"
      "v_and_b32 v197, 0xffff0000, v143\n"
      "v_mul_f32 v196, v196, v195\n"
      "v_mul_f32 v197, v197, v195\n"
      "v_cvt_pk_bf16_f32 v143, v196, v197\n"
      "v_exp_f32 v188, v96\n"
      "v_exp_f32 v189, v97\n"
      "v_exp_f32 v190, v98\n"
      "v_exp_f32 v191, v99\n"
      "v_cvt_pk_bf16_f32 v144, v188, v189\n"
      "v_cvt_pk_bf16_f32 v145, v190, v191\n"
      "v_exp_f32 v192, v100\n"
      "v_exp_f32 v193, v101\n"
      "v_exp_f32 v194, v102\n"
      "v_exp_f32 v195, v103\n"
      "v_cvt_pk_bf16_f32 v146, v192, v193\n"
      "v_cvt_pk_bf16_f32 v147, v194, v195\n"
      "v_exp_f32 v188, v104\n"
      "v_exp_f32 v189, v105\n"
      "v_exp_f32 v190, v106\n"
      "v_exp_f32 v191, v107\n"
      "v_cvt_pk_bf16_f32 v148, v188, v189\n"
      "v_cvt_pk_bf16_f32 v149, v190, v191\n"
      "v_exp_f32 v192, v108\n"
      "v_exp_f32 v193, v109\n"
      "v_exp_f32 v194, v110\n"
      "v_exp_f32 v195, v111\n"
      "v_cvt_pk_bf16_f32 v150, v192, v193\n"
      "v_cvt_pk_bf16_f32 v151, v194, v195\n"
      "v_exp_f32 v188, v112\n"
      "v_exp_f32 v189, v113\n"
      "v_exp_f32 v190, v114\n"
      "v_exp_f32 v191, v115\n"
      "v_cvt_pk_bf16_f32 v152, v188, v189\n"
      "v_cvt_pk_bf16_f32 v153, v190, v191\n"
      "v_exp_f32 v192, v116\n"
      "v_exp_f32 v193, v117\n"
      "v_exp_f32 v194, v118\n"
      "v_exp_f32 v195, v119\n"
      "v_cvt_pk_bf16_f32 v154, v192, v193\n"
      "v_cvt_pk_bf16_f32 v155, v194, v195\n"
      "v_exp_f32 v188, v120\n"
      "v_exp_f32 v189, v121\n"
      "v_exp_f32 v190, v122\n"
      "v_exp_f32 v191, v123\n"
      "v_cvt_pk_bf16_f32 v156, v188, v189\n"
      "v_cvt_pk_bf16_f32 v157, v190, v191\n"
      "v_exp_f32 v192, v124\n"
      "v_exp_f32 v193, v125\n"
      "v_exp_f32 v194, v126\n"
      "v_exp_f32 v195, v127\n"
      "v_cvt_pk_bf16_f32 v158, v192, v193\n"
      "v_cvt_pk_bf16_f32 v159, v194, v195\n"
      "s_nop 1\n"
      "v_mfma_f32_16x16x32_bf16 v[184:187], a[128:131], v[144:147], 0\n"
      "v_mfma_f32_16x16x32_bf16 v[184:187], a[132:135], v[148:151], v[184:187]\n"
      "v_mfma_f32_16x16x32_bf16 v[184:187], a[136:139], v[152:155], v[184:187]\n"
      "v_mfma_f32_16x16x32_bf16 v[184:187], a[140:143], v[156:159], v[184:187]\n"
      "s_nop 7\n"
      "s_setpc_b64 s[96:97]\n"
      "SLOW_%=:\n"
      "ds_write_b128 v234, v[176:179] offset:5120\n"
      "s_waitcnt lgkmcnt(0)\n"
      "s_barrier\n"
      "ds_read_b128 v[192:195], v235 offset:5120\n"
      "s_waitcnt lgkmcnt(0)\n"
      "v_sub_f32 v188, v176, v194\n"
      "v_sub_f32 v189, v177, v195\n"
      "v_sub_f32 v190, v178, v192\n"
      "v_sub_f32 v191, v179, v193\n"
      "v_max_f32 v188, 0, v188\n"
      "v_max_f32 v189, 0, v189\n"
      "v_max_f32 v190, 0, v190\n"
      "v_max_f32 v191, 0, v191\n"
      "s_call_b64 s[96:97], RS1_%=\n"
      "ds_write_b128 v232, a[176:179] offset:0\n"
      "ds_write_b128 v232, a[180:183] offset:1024\n"
      "ds_write_b128 v232, a[184:187] offset:2048\n"
      "ds_write_b128 v232, a[188:191] offset:3072\n"
      "ds_write_b128 v232, a[192:195] offset:4096\n"
      "ds_write_b128 v232, a[196:199] offset:5120\n"
      "ds_write_b128 v232, a[200:203] offset:6144\n"
      "ds_write_b128 v232, a[204:207] offset:7168\n"
      "ds_write_b128 v234, v[152:155] offset:0\n"
      "ds_write_b128 v234, v[156:159] offset:1024\n"
      "v_cndmask_b32 v229, v180, v181, s[74:75]\n"
      "v_cndmask_b32 v200, v182, v183, s[74:75]\n"
      "v_cndmask_b32 v228, v176, v177, s[74:75]\n"
      "v_cndmask_b32 v202, v186, v187, s[74:75]\n"
      "v_cndmask_b32 v201, v184, v185, s[74:75]\n"
      "v_add_f32 v200, v200, v202\n"
      "ds_write_b32 v234, v200 offset:4096\n"
      "v_add_f32 v229, v229, v201\n"
      "s_waitcnt lgkmcnt(0)\n"
      "s_barrier\n"
      "ds_read_b32 v208, v209\n"
      "ds_read_b128 v[160:163], v235 offset:0\n"
      "ds_read_b128 v[164:167], v235 offset:1024\n"
      "ds_read_b128 v[64:67], v233 offset:0\n"
      "ds_read_b128 v[68:71], v233 offset:1024\n"
      "ds_read_b128 v[72:75], v233 offset:2048\n"
      "ds_read_b128 v[76:79], v233 offset:3072\n"
      "ds_read_b128 v[80:83], v233 offset:4096\n"
      "ds_read_b128 v[84:87], v233 offset:5120\n"
      "ds_read_b128 v[88:91], v233 offset:6144\n"
      "ds_read_b128 v[92:95], v233 offset:7168\n"
      "ds_read_b32 v204, v235 offset:4096\n"
      "s_branch AGAIN_%=\n"
      ".subsection 0\n"
      : "+{v0}"(tid)
      : [blk] "s"(blockIdx.x), [kp] "s"(__builtin_amdgcn_kernarg_segment_ptr()), "s"(lds0)
      : "v1", "v2", "v3", "v4", "v5", "v6", "v7", "v8", "v9", "v10", "v11", "v12", "v13", "v14", "v15", "v16", "v17", "v18", "v19", "v20", "v21", "v22", "v23", "v24", "v25", "v26", "v27", "v28", "v29", "v30", "v31", "v32", "v33", "v34", "v35", "v36", "v37", "v38", "v39", "v40", "v41", "v42", "v43", "v44", "v45", "v46", "v47", "v48", "v49", "v50", "v51", "v52", "v53", "v54", "v55", "v56", "v57", "v58", "v59", "v60", "v61", "v62", "v63", "v64", "v65", "v66", "v67", "v68", "v69", "v70", "v71", "v72", "v73", "v74", "v75", "v76", "v77", "v78", "v79", "v80", "v81", "v82", "v83", "v84", "v85", "v86", "v87", "v88", "v89", "v90", "v91", "v92", "v93", "v94", "v95", "v96", "v97", "v98", "v99", "v100", "v101", "v102", "v103", "v104", "v105", "v106", "v107", "v108", "v109", "v110", "v111", "v112", "v113", "v114", "v115", "v116", "v117", "v118", "v119", "v120", "v121", "v122", "v123", "v124", "v125", "v126", "v127", "v128", "v129", "v130", "v131", "v132", "v133", "v134", "v135", "v136", "v137", "v138", "v139", "v140", "v141", "v142", "v143", "v144", "v145", "v146", "v147", "v148", "v149", "v150", "v151", "v152", "v153", "v154", "v155", "v156", "v157", "v158", "v159", "v160", "v161", "v162", "v163", "v164", "v165", "v166", "v167", "v168", "v169", "v170", "v171", "v172", "v173", "v174", "v175", "v176", "v177", "v178", "v179", "v180", "v181", "v182", "v183", "v184", "v185", "v186", "v187", "v188", "v189", "v190", "v191", "v192", "v193", "v194", "v195", "v196", "v197", "v198", "v199", "v200", "v201", "v202", "v203", "v204", "v205", "v206", "v207", "v208", "v209", "v210", "v211", "v212", "v213", "v214", "v215", "v216", "v217", "v218", "v219", "v220", "v221", "v222", "v223", "v224", "v225", "v226", "v227", "v228", "v229", "v230", "v231", "v232", "v233", "v234", "v235", "v236", "v237", "v238", "v239", "v240", "v241", "v242", "v243", "v244", "v245", "v246", "v247", "v248", "v249", "v250", "v251", "a0", "a1", "a2", "a3", "a4", "a5", "a6", "a7", "a8", "a9", "a10", "a11", "a12", "a13", "a14", "a15", "a16", "a17", "a18", "a19", "a20", "a21", "a22", "a23", "a24", "a25", "a26", "a27", "a28", "a29", "a30", "a31", "a32", "a33", "a34", "a35", "a36", "a37", "a38", "a39", "a40", "a41", "a42", "a43", "a44", "a45", "a46", "a47", "a48", "a49", "a50", "a51", "a52", "a53", "a54", "a55", "a56", "a57", "a58", "a59", "a60", "a61", "a62", "a63", "a64", "a65", "a66", "a67", "a68", "a69", "a70", "a71", "a72", "a73", "a74", "a75", "a76", "a77", "a78", "a79", "a80", "a81", "a82", "a83", "a84", "a85", "a86", "a87", "a88", "a89", "a90", "a91", "a92", "a93", "a94", "a95", "a96", "a97", "a98", "a99", "a100", "a101", "a102", "a103", "a104", "a105", "a106", "a107", "a108", "a109", "a110", "a111", "a112", "a113", "a114", "a115", "a116", "a117", "a118", "a119", "a120", "a121", "a122", "a123", "a124", "a125", "a126", "a127", "a128", "a129", "a130", "a131", "a132", "a133", "a134", "a135", "a136", "a137", "a138", "a139", "a140", "a141", "a142", "a143", "a144", "a145", "a146", "a147", "a148", "a149", "a150", "a151", "a152", "a153", "a154", "a155", "a156", "a157", "a158", "a159", "a160", "a161", "a162", "a163", "a164", "a165", "a166", "a167", "a168", "a169", "a170", "a171", "a172", "a173", "a174", "a175", "a176", "a177", "a178", "a179", "a180", "a181", "a182", "a183", "a184", "a185", "a186", "a187", "a188", "a189", "a190", "a191", "a192", "a193", "a194", "a195", "a196", "a197", "a198", "a199", "a200", "a201", "a202", "a203", "a204", "a205", "a206", "a207", "s4", "s5", "s6", "s7", "s8", "s9", "s10", "s11", "s12", "s13", "s14", "s15", "s16", "s17", "s18", "s19", "s20", "s21", "s22", "s23", "s24", "s25", "s26", "s27", "s28", "s29", "s30", "s31", "s32", "s33", "s34", "s35", "s38", "s39", "s40", "s43", "s44", "s45", "s46", "s47", "s50", "s51", "s52", "s53", "s54", "s56", "s57", "s58", "s59", "s60", "s61", "s62", "s63", "s64", "s65", "s66", "s67", "s68", "s69", "s70", "s71", "s72", "s74", "s75", "s76", "s77", "s78", "s79", "s80", "s81", "s82", "s83", "s84", "s85", "s86", "s88", "s89", "s90", "s91", "s92", "s93", "s94", "s95", "s96", "s97", "vcc", "scc", "memory");
}

}

extern "C" void kernel_launch(void* const* d_in, const int* in_sizes, int n_in, void* d_out, int out_size,
                              void* d_ws, size_t ws_size, hipStream_t stream) {
  if (n_in != 6 || ws_size < WS_END) return;
  const __hip_bfloat16* q = (const __hip_bfloat16*)d_in[0];
  const __hip_bfloat16* kc = (const __hip_bfloat16*)d_in[1];
  const __hip_bfloat16* vc = (const __hip_bfloat16*)d_in[2];
  const __hip_bfloat16* kt = (const __hip_bfloat16*)d_in[3];
  const __hip_bfloat16* vt = (const __hip_bfloat16*)d_in[4];
  const __hip_bfloat16* tm = (const __hip_bfloat16*)d_in[5];
  hipLaunchKernelGGL(tree_attn_main, dim3(B_ * HKV_ * 2), dim3(256), 0, stream, q, kc, vc, kt, vt, tm,
                     (__hip_bfloat16*)d_out, (char*)d_ws);
}
```

```cpp
#include <hip/hip_runtime.h>
#include <hip/hip_bf16.h>
#include <stdint.h>

#pragma clang diagnostic ignored "-Wint-to-pointer-cast"
#pragma clang diagnostic ignored "-Winline-asm"

namespace {

constexpr int B_ = 16, HKV_ = 8, D_ = 128;
constexpr int SLOT = 64 * D_ * 2;
constexpr int LDS_BYTES = 10 * SLOT;

constexpr size_t WS_EPOCH = 0;
constexpr size_t WS_REF = WS_EPOCH + 128 * 4 * 128;
constexpr size_t WS_X = WS_REF + 16384;
constexpr size_t XW_BYTES = 4608;
constexpr size_t WS_END = WS_X + (size_t)128 * 2 * 4 * XW_BYTES;

__global__ __launch_bounds__(256, 1)
void tree_attn_main(const __hip_bfloat16*, const __hip_bfloat16*, const __hip_bfloat16*, const __hip_bfloat16*,
                    const __hip_bfloat16*, const __hip_bfloat16*, __hip_bfloat16*, char*) {
  __shared__ __attribute__((aligned(1024))) char smem[LDS_BYTES];
  const unsigned lds0 = (unsigned)(uintptr_t)smem;
  unsigned tid = threadIdx.x;
  asm volatile(
      "s_load_dwordx16 s[4:19], %[kp], 0x0\n"
      "s_getpc_b64 s[60:61]\n"
      "v_lshlrev_b32 v241, 4, v0\n"
      "s_lshl_b32 s62, %[blk], 9\n"
      "s_and_b32 s62, s62, 0x3000\n"
      "s_add_u32 s60, s60, s62\n"
      "s_addc_u32 s61, s61, 0\n"
      "global_load_dwordx4 v[236:239], v241, s[60:61]\n"
      "v_and_b32 v1, 0xc0, v0\n"
      "v_lshlrev_b32 v248, 1, v0\n"
      "v_readfirstlane_b32 s50, v1\n"
      "v_and_b32 v248, 0x60, v248\n"
      "v_and_b32 v249, 0x70, v241\n"
      "v_xor_b32 v248, v248, v249\n"
      "v_lshl_or_b32 v248, v1, 6, v248\n"
      "v_and_b32 v1, 56, v0\n"
      "v_lshl_or_b32 v248, v1, 5, v248\n"
      "s_lshl_b32 s62, %[blk], 4\n"
      "s_and_b32 s63, s62, 0x80\n"
      "v_or_b32 v248, s63, v248\n"
      "v_xor_b32 v249, 0x80, v248\n"
      "s_and_b32 s62, s62, 0x70\n"
      "s_lshr_b32 s64, %[blk], 4\n"
      "s_add_u32 s44, s62, s64\n"
      "s_lshl_b32 s51, s64, 14\n"
      "s_and_b32 s51, s51, 0x4000\n"
      "s_lshl_b32 s64, s44, 20\n"
      "s_lshl_b32 s65, s63, 8\n"
      "s_add_u32 s64, s64, s65\n"
      "s_lshl_b32 s62, s44, 15\n"
      "s_lshl_b32 s50, s50, 6\n"
      "s_add_u32 m0, s50, 131072\n"
      "s_mov_b32 s58, 0x120000\n"
      "s_mov_b32 s59, 0x20000\n"
      "s_movk_i32 s22, 0x4000\n"
      "s_waitcnt lgkmcnt(0)\n"
      "s_add_u32 s56, s4, s62\n"
      "s_addc_u32 s57, s5, 0\n"
      "buffer_load_dwordx4 v248, s[56:59], 0 offen nt lds\n"
      "buffer_load_dwordx4 v248, s[56:59], 0 offen offset:2048 nt lds\n"
      "s_bitset1_b32 m0, 10\n"
      "s_memrealtime s[82:83]\n"
      "s_add_u32 s20, s6, s64\n"
      "s_addc_u32 s21, s7, 0\n"
      "s_add_u32 s32, s8, s64\n"
      "s_addc_u32 s33, s9, 0\n"
      "s_load_dword s92, s[20:21], 0x0\n"
      "s_load_dword s92, s[10:11], 0x0\n"
      "s_load_dword s92, s[32:33], 0x0\n"
      "buffer_load_dwordx4 v249, s[56:59], 0 offen nt lds\n"
      "buffer_load_dwordx4 v249, s[56:59], 0 offen offset:2048 nt lds\n"
      "s_add_u32 m0, s50, 147456\n"
      "s_load_dword s92, s[18:19], 0x0\n"
      "buffer_load_dwordx4 v248, s[56:59], s22 offen nt lds\n"
      "buffer_load_dwordx4 v248, s[56:59], s22 offen offset:2048 nt lds\n"
      "s_bitset1_b32 m0, 10\n"
      "s_nop 0\n"
      "buffer_load_dwordx4 v249, s[56:59], s22 offen nt lds\n"
      "buffer_load_dwordx4 v249, s[56:59], s22 offen offset:2048 nt lds\n"
      "s_mov_b64 s[56:57], s[20:21]\n"
      "s_load_dword s92, s[14:15], 0x0\n"
      "s_load_dword s92, s[12:13], 0x0\n"
      "s_lshr_b32 s74, s50, 2\n"
      "s_lshr_b32 s75, s50, 5\n"
      "s_xor_b32 s75, s75, s63\n"
      "s_and_b32 s75, s75, 0x80\n"
      "s_and_b32 s76, s74, 0x800\n"
      "s_add_u32 s75, s75, s76\n"
      "s_add_u32 s70, s65, s63\n"
      "s_add_u32 s70, s70, s50\n"
      "s_sub_u32 s70, s70, s75\n"
      "s_lshr_b32 s75, s50, 1\n"
      "s_lshr_b32 s71, s62, 2\n"
      "s_sub_u32 s71, s71, s75\n"
      "s_ashr_i32 s72, s71, 31\n"
      "s_mov_b32 s23, s59\n"
      "s_mov_b64 s[26:27], s[22:23]\n"
      "s_sub_u32 s20, s56, s70\n"
      "s_subb_u32 s21, s57, 0\n"
      "s_add_u32 m0, s74, 24576\n"
      "s_add_u32 s24, s10, s71\n"
      "buffer_load_dwordx4 v248, s[20:23], 0 offen nt lds\n"
      "s_addc_u32 s25, s11, s72\n"
      "s_add_u32 m0, s75, 16384\n"
      "s_nop 0\n"
      "buffer_load_dwordx4 v248, s[24:27], 0 offen nt lds\n"
      "s_bitset1_b32 m0, 10\n"
      "s_nop 0\n"
      "buffer_load_dwordx4 v249, s[24:27], 0 offen nt lds\n"
      "s_lshr_b32 s43, s63, 7\n"
      "s_lshl_b32 s76, s44, 9\n"
      "s_lshr_b32 s69, s50, 5\n"
      "s_add_u32 s76, s76, s69\n"
      "s_add_u32 s46, s18, s76\n"
      "s_addc_u32 s47, s19, 0\n"
      "s_mul_i32 s76, s43, 0x3000\n"
      "s_lshl_b32 s69, s44, 8\n"
      "s_and_b32 s69, s69, 0x7800\n"
      "s_add_u32 s14, s14, s69\n"
      "s_addc_u32 s15, s15, 0\n"
      "v_mov_b64 v[192:193], 0\n"
      "v_mov_b64 v[194:195], 0\n"
      "v_lshlrev_b32 v3, 6, v0\n"
      "v_and_b32 v3, 0x3c0, v3\n"
      "v_lshrrev_b32 v4, 1, v0\n"
      "v_and_or_b32 v211, v4, 24, v3\n"
      "v_mov_b64 v[200:201], 0\n"
      "v_mov_b64 v[202:203], 0\n"
      "v_mov_b64 v[204:205], 0\n"
      "v_mov_b64 v[206:207], 0\n"
      "s_cmp_lg_u32 s76, s50\n"
      "s_cbranch_scc1 NOMASK_%=\n"
      "global_load_dwordx2 v[200:201], v211, s[14:15] offset:0\n"
      "global_load_dwordx2 v[202:203], v211, s[14:15] offset:32\n"
      "global_load_dwordx2 v[204:205], v211, s[14:15] offset:1024\n"
      "global_load_dwordx2 v[206:207], v211, s[14:15] offset:1056\n"
      "NOMASK_%=:\n"
      "global_load_dword v208, v192, s[46:47] sc1\n"
      "s_mov_b64 s[30:31], s[22:23]\n"
      "s_add_u32 s28, s12, s71\n"
      "s_addc_u32 s29, s13, s72\n"
      "s_sub_u32 s32, s32, 73728\n"
      "s_subb_u32 s33, s33, 0\n"
      "s_xor_b64 s[32:33], s[32:33], s[56:57]\n"
      "s_lshl_b32 s69, s43, 13\n"
      "s_xor_b32 s69, s69, 0x2000\n"
      "v_add_u32 v2, s69, v241\n"
      "ds_write_b128 v241, v[192:195] offset:28672\n"
      "ds_write_b128 v2, v[192:195] offset:49152\n"
      "ds_write_b128 v2, v[192:195] offset:53248\n"
      "v_and_b32 v241, 0x3f0, v241\n"
      "s_and_b32 s91, s50, 8192\n"
      "v_lshlrev_b32 v4, 8, v0\n"
      "v_and_b32 v4, 0x800, v4\n"
      "v_lshlrev_b32 v5, 7, v0\n"
      "v_and_b32 v6, 0x380, v5\n"
      "v_or3_b32 v4, v4, v6, s91\n"
      "v_and_b32 v6, 0x60, v241\n"
      "v_and_b32 v2, 48, v0\n"
      "v_xor_b32 v6, v6, v2\n"
      "v_or_b32 v242, v4, v6\n"
      "v_xor_b32 v243, 64, v242\n"
      "v_lshlrev_b32 v4, 6, v0\n"
      "v_and_b32 v6, 0x800, v4\n"
      "v_lshlrev_b32 v2, 5, v0\n"
      "v_and_b32 v2, 0x380, v2\n"
      "s_add_u32 s69, s91, 16384\n"
      "v_or3_b32 v6, v6, v2, s69\n"
      "v_lshlrev_b32 v2, 3, v0\n"
      "v_and_or_b32 v6, v2, 24, v6\n"
      "v_lshlrev_b32 v2, 2, v0\n"
      "v_and_b32 v2, 0x60, v2\n"
      "v_xor_b32 v244, v6, v2\n"
      "v_xor_b32 v245, 0x20, v244\n"
      "v_xor_b32 v246, 0x40, v244\n"
      "v_xor_b32 v247, 0x60, v244\n"
      "v_and_b32 v5, 0x1e00, v5\n"
      "v_and_or_b32 v250, v241, 48, v5\n"
      "s_lshl_b32 s69, %[blk], 9\n"
      "s_and_b32 s69, s69, 0x1e00\n"
      "v_and_b32 v4, 0x1c0, v4\n"
      "v_or_b32 v251, s69, v4\n"
      "s_and_b32 s69, s50, 4096\n"
      "s_lshl_b32 s69, s69, 2\n"
      "s_add_u32 s69, s69, 131072\n"
      "s_sub_u32 s91, 16384, s91\n"
      "v_add_u32 v196, s69, v242\n"
      "v_add_u32 v198, s91, v242\n"
      "v_xor_b32 v214, 0x2000, v196\n"
      "v_add_u32 v197, s69, v243\n"
      "v_add_u32 v199, s91, v243\n"
      "v_xor_b32 v215, 0x2000, v197\n"
      "s_add_u32 s69, s50, 131072\n"
      "v_add_u32 v210, s69, v241\n"
      "v_and_b32 v2, 3, v0\n"
      "v_mov_b32 v4, 0x3f803f80\n"
      "v_cmp_eq_u32 vcc, 0, v2\n"
      "s_nop 1\n"
      "v_cndmask_b32 v5, 0, v4, vcc\n"
      "v_accvgpr_write_b32 a128, v5\n"
      "v_accvgpr_write_b32 a129, v5\n"
      "v_accvgpr_write_b32 a130, v5\n"
      "v_accvgpr_write_b32 a131, v5\n"
      "v_cmp_eq_u32 vcc, 1, v2\n"
      "s_nop 1\n"
      "v_cndmask_b32 v5, 0, v4, vcc\n"
      "v_accvgpr_write_b32 a132, v5\n"
      "v_accvgpr_write_b32 a133, v5\n"
      "v_accvgpr_write_b32 a134, v5\n"
      "v_accvgpr_write_b32 a135, v5\n"
      "v_cmp_eq_u32 vcc, 2, v2\n"
      "s_nop 1\n"
      "v_cndmask_b32 v5, 0, v4, vcc\n"
      "v_accvgpr_write_b32 a136, v5\n"
      "v_accvgpr_write_b32 a137, v5\n"
      "v_accvgpr_write_b32 a138, v5\n"
      "v_accvgpr_write_b32 a139, v5\n"
      "v_cmp_eq_u32 vcc, 3, v2\n"
      "s_nop 1\n"
      "v_cndmask_b32 v5, 0, v4, vcc\n"
      "v_accvgpr_write_b32 a140, v5\n"
      "v_accvgpr_write_b32 a141, v5\n"
      "v_accvgpr_write_b32 a142, v5\n"
      "v_accvgpr_write_b32 a143, v5\n"
      "v_mfma_f32_32x32x16_bf16 a[144:159], v[192:195], v[192:195], 0\n"
      "v_mfma_f32_32x32x16_bf16 a[160:175], v[192:195], v[192:195], 0\n"
      "v_mfma_f32_32x32x16_bf16 a[176:191], v[192:195], v[192:195], 0\n"
      "v_mfma_f32_32x32x16_bf16 a[192:207], v[192:195], v[192:195], 0\n"
      "v_mfma_f32_32x32x16_bf16 v[0:15], v[192:195], v[192:195], 0\n"
      "v_mfma_f32_32x32x16_bf16 v[16:31], v[192:195], v[192:195], 0\n"
      "v_mfma_f32_32x32x16_bf16 v[32:47], v[192:195], v[192:195], 0\n"
      "v_mfma_f32_32x32x16_bf16 v[48:63], v[192:195], v[192:195], 0\n"
      "v_mfma_f32_32x32x16_bf16 v[128:143], v[192:195], v[192:195], 0\n"
      "v_mov_b64 v[180:181], 0\n"
      "v_mov_b64 v[182:183], 0\n"
      "v_mov_b64 v[184:185], 0\n"
      "v_mov_b64 v[186:187], 0\n"
      "s_mov_b32 s61, 0x4000\n"
      "s_xor_b32 s60, s61, s51\n"
      "s_add_u32 s62, s50, 32768\n"
      "s_mov_b32 s94, 0x3e0293ee\n"
      "s_mov_b32 s95, s94\n"
      "s_lshl_b32 s69, s43, 13\n"
      "s_add_u32 s45, s69, s75\n"
      "s_mov_b32 m0, s50\n"
      "s_nop 0\n"
      "buffer_load_dwordx4 v248, s[56:59], s51 offen nt lds\n"
      "buffer_load_dwordx4 v248, s[56:59], s51 offen offset:2048 nt lds\n"
      "s_bitset1_b32 m0, 10\n"
      "s_nop 0\n"
      "buffer_load_dwordx4 v249, s[56:59], s51 offen nt lds\n"
      "buffer_load_dwordx4 v249, s[56:59], s51 offen offset:2048 nt lds\n"
      "s_waitcnt vmcnt(8)\n"
      "s_memrealtime s[68:69]\n"
      "ds_read_b128 v[64:67], v210 offset:0\n"
      "ds_read_b128 v[68:71], v210 offset:1024\n"
      "ds_read_b128 v[72:75], v210 offset:2048\n"
      "ds_read_b128 v[76:79], v210 offset:3072\n"
      "ds_read_b128 v[80:83], v210 offset:16384\n"
      "ds_read_b128 v[84:87], v210 offset:17408\n"
      "ds_read_b128 v[88:91], v210 offset:18432\n"
      "ds_read_b128 v[92:95], v210 offset:19456\n"
      "s_add_u32 s20, s18, 65536\n"
      "s_addc_u32 s21, s19, 0\n"
      "s_lshl_b32 s75, %[blk], 6\n"
      "s_add_u32 s75, s75, s50\n"
      "s_waitcnt lgkmcnt(0)\n"
      "s_sub_u32 s67, s68, s82\n"
      "s_min_u32 s67, s67, 165\n"
      "s_add_u32 s93, s82, s67\n"
      "s_sub_u32 s71, s68, 400\n"
      "s_cmp_lt_u32 s75, 512\n"
      "s_cbranch_scc0 NOLEAD_%=\n"
      "v_mov_b32 v188, s68\n"
      "v_mov_b32 v189, s68\n"
      "v_mov_b32 v190, s68\n"
      "v_mov_b32 v191, s68\n"
      "buffer_store_dwordx4 v[188:191], v250, s[20:23], s75 offen sc1\n"
      "s_nop 1\n"
      "NOLEAD_%=:\n"
      "s_mov_b32 m0, s62\n"
      "s_nop 0\n"
      "buffer_load_dwordx4 v248, s[56:59], s60 offen nt lds\n"
      "buffer_load_dwordx4 v248, s[56:59], s60 offen offset:2048 nt lds\n"
      "s_bitset1_b32 m0, 10\n"
      "s_nop 0\n"
      "buffer_load_dwordx4 v249, s[56:59], s60 offen nt lds\n"
      "buffer_load_dwordx4 v249, s[56:59], s60 offen offset:2048 nt lds\n"
      "s_xor_b64 s[56:57], s[56:57], s[32:33]\n"
      "s_bitset1_b32 s61, 15\n"
      "s_add_u32 s61, s61, 0x2000\n"
      "s_bitset0_b32 s61, 15\n"
      "s_xor_b32 s60, s61, s51\n"
      "s_add_u32 s62, s62, 0x4000\n"
      "s_and_b32 s62, s62, 0x1ffff\n"
      "s_xor_b64 s[56:57], s[56:57], s[32:33]\n"
      "s_bitset1_b32 s61, 15\n"
      "s_add_u32 s61, s61, 0x2000\n"
      "s_bitset0_b32 s61, 15\n"
      "s_xor_b32 s60, s61, s51\n"
      "s_add_u32 s62, s62, 0x4000\n"
      "s_and_b32 s62, s62, 0x1ffff\n"
      "s_add_u32 m0, s45, 49152\n"
      "s_nop 0\n"
      "buffer_load_dwordx4 v248, s[28:31], 0 offen nt lds\n"
      "s_bitset1_b32 m0, 10\n"
      "s_nop 0\n"
      "buffer_load_dwordx4 v249, s[28:31], 0 offen nt lds\n"
      "v_lshlrev_b32 v188, 16, v64\n"
      "v_and_b32 v189, 0xffff0000, v64\n"
      "v_lshlrev_b32 v190, 16, v65\n"
      "v_and_b32 v191, 0xffff0000, v65\n"
      "v_lshlrev_b32 v192, 16, v66\n"
      "v_and_b32 v193, 0xffff0000, v66\n"
      "v_lshlrev_b32 v194, 16, v67\n"
      "v_and_b32 v195, 0xffff0000, v67\n"
      "v_pk_mul_f32 v[188:189], v[188:189], s[94:95]\n"
      "v_pk_mul_f32 v[190:191], v[190:191], s[94:95]\n"
      "v_pk_mul_f32 v[192:193], v[192:193], s[94:95]\n"
      "v_pk_mul_f32 v[194:195], v[194:195], s[94:95]\n"
      "v_cvt_pk_bf16_f32 v64, v188, v189\n"
      "v_cvt_pk_bf16_f32 v65, v190, v191\n"
      "v_cvt_pk_bf16_f32 v66, v192, v193\n"
      "v_cvt_pk_bf16_f32 v67, v194, v195\n"
      "ds_write_b128 v210, v[64:67] offset:0\n"
      "v_lshlrev_b32 v188, 16, v68\n"
      "v_and_b32 v189, 0xffff0000, v68\n"
      "v_lshlrev_b32 v190, 16, v69\n"
      "v_and_b32 v191, 0xffff0000, v69\n"
      "v_lshlrev_b32 v192, 16, v70\n"
      "v_and_b32 v193, 0xffff0000, v70\n"
      "v_lshlrev_b32 v194, 16, v71\n"
      "v_and_b32 v195, 0xffff0000, v71\n"
      "v_pk_mul_f32 v[188:189], v[188:189], s[94:95]\n"
      "v_pk_mul_f32 v[190:191], v[190:191], s[94:95]\n"
      "v_pk_mul_f32 v[192:193], v[192:193], s[94:95]\n"
      "v_pk_mul_f32 v[194:195], v[194:195], s[94:95]\n"
      "v_cvt_pk_bf16_f32 v68, v188, v189\n"
      "v_cvt_pk_bf16_f32 v69, v190, v191\n"
      "v_cvt_pk_bf16_f32 v70, v192, v193\n"
      "v_cvt_pk_bf16_f32 v71, v194, v195\n"
      "ds_write_b128 v210, v[68:71] offset:1024\n"
      "v_lshlrev_b32 v188, 16, v72\n"
      "v_and_b32 v189, 0xffff0000, v72\n"
      "v_lshlrev_b32 v190, 16, v73\n"
      "v_and_b32 v191, 0xffff0000, v73\n"
      "v_lshlrev_b32 v192, 16, v74\n"
      "v_and_b32 v193, 0xffff0000, v74\n"
      "v_lshlrev_b32 v194, 16, v75\n"
      "v_and_b32 v195, 0xffff0000, v75\n"
      "v_pk_mul_f32 v[188:189], v[188:189], s[94:95]\n"
      "v_pk_mul_f32 v[190:191], v[190:191], s[94:95]\n"
      "v_pk_mul_f32 v[192:193], v[192:193], s[94:95]\n"
      "v_pk_mul_f32 v[194:195], v[194:195], s[94:95]\n"
      "v_cvt_pk_bf16_f32 v72, v188, v189\n"
      "v_cvt_pk_bf16_f32 v73, v190, v191\n"
      "v_cvt_pk_bf16_f32 v74, v192, v193\n"
      "v_cvt_pk_bf16_f32 v75, v194, v195\n"
      "ds_write_b128 v210, v[72:75] offset:2048\n"
      "v_lshlrev_b32 v188, 16, v76\n"
      "v_and_b32 v189, 0xffff0000, v76\n"
      "v_lshlrev_b32 v190, 16, v77\n"
      "v_and_b32 v191, 0xffff0000, v77\n"
      "v_lshlrev_b32 v192, 16, v78\n"
      "v_and_b32 v193, 0xffff0000, v78\n"
      "v_lshlrev_b32 v194, 16, v79\n"
      "v_and_b32 v195, 0xffff0000, v79\n"
      "v_pk_mul_f32 v[188:189], v[188:189], s[94:95]\n"
      "v_pk_mul_f32 v[190:191], v[190:191], s[94:95]\n"
      "v_pk_mul_f32 v[192:193], v[192:193], s[94:95]\n"
      "v_pk_mul_f32 v[194:195], v[194:195], s[94:95]\n"
      "v_cvt_pk_bf16_f32 v76, v188, v189\n"
      "v_cvt_pk_bf16_f32 v77, v190, v191\n"
      "v_cvt_pk_bf16_f32 v78, v192, v193\n"
      "v_cvt_pk_bf16_f32 v79, v194, v195\n"
      "ds_write_b128 v210, v[76:79] offset:3072\n"
      "v_lshlrev_b32 v188, 16, v80\n"
      "v_and_b32 v189, 0xffff0000, v80\n"
      "v_lshlrev_b32 v190, 16, v81\n"
      "v_and_b32 v191, 0xffff0000, v81\n"
      "v_lshlrev_b32 v192, 16, v82\n"
      "v_and_b32 v193, 0xffff0000, v82\n"
      "v_lshlrev_b32 v194, 16, v83\n"
      "v_and_b32 v195, 0xffff0000, v83\n"
      "v_pk_mul_f32 v[188:189], v[188:189], s[94:95]\n"
      "v_pk_mul_f32 v[190:191], v[190:191], s[94:95]\n"
      "v_pk_mul_f32 v[192:193], v[192:193], s[94:95]\n"
      "v_pk_mul_f32 v[194:195], v[194:195], s[94:95]\n"
      "v_cvt_pk_bf16_f32 v80, v188, v189\n"
      "v_cvt_pk_bf16_f32 v81, v190, v191\n"
      "v_cvt_pk_bf16_f32 v82, v192, v193\n"
      "v_cvt_pk_bf16_f32 v83, v194, v195\n"
      "ds_write_b128 v210, v[80:83] offset:16384\n"
      "v_lshlrev_b32 v188, 16, v84\n"
      "v_and_b32 v189, 0xffff0000, v84\n"
      "v_lshlrev_b32 v190, 16, v85\n"
      "v_and_b32 v191, 0xffff0000, v85\n"
      "v_lshlrev_b32 v192, 16, v86\n"
      "v_and_b32 v193, 0xffff0000, v86\n"
      "v_lshlrev_b32 v194, 16, v87\n"
      "v_and_b32 v195, 0xffff0000, v87\n"
      "v_pk_mul_f32 v[188:189], v[188:189], s[94:95]\n"
      "v_pk_mul_f32 v[190:191], v[190:191], s[94:95]\n"
      "v_pk_mul_f32 v[192:193], v[192:193], s[94:95]\n"
      "v_pk_mul_f32 v[194:195], v[194:195], s[94:95]\n"
      "v_cvt_pk_bf16_f32 v84, v188, v189\n"
      "v_cvt_pk_bf16_f32 v85, v190, v191\n"
      "v_cvt_pk_bf16_f32 v86, v192, v193\n"
      "v_cvt_pk_bf16_f32 v87, v194, v195\n"
      "ds_write_b128 v210, v[84:87] offset:17408\n"
      "v_lshlrev_b32 v188, 16, v88\n"
      "v_and_b32 v189, 0xffff0000, v88\n"
      "v_lshlrev_b32 v190, 16, v89\n"
      "v_and_b32 v191, 0xffff0000, v89\n"
      "v_lshlrev_b32 v192, 16, v90\n"
      "v_and_b32 v193, 0xffff0000, v90\n"
      "v_lshlrev_b32 v194, 16, v91\n"
      "v_and_b32 v195, 0xffff0000, v91\n"
      "v_pk_mul_f32 v[188:189], v[188:189], s[94:95]\n"
      "v_pk_mul_f32 v[190:191], v[190:191], s[94:95]\n"
      "v_pk_mul_f32 v[192:193], v[192:193], s[94:95]\n"
      "v_pk_mul_f32 v[194:195], v[194:195], s[94:95]\n"
      "v_cvt_pk_bf16_f32 v88, v188, v189\n"
      "v_cvt_pk_bf16_f32 v89, v190, v191\n"
      "v_cvt_pk_bf16_f32 v90, v192, v193\n"
      "v_cvt_pk_bf16_f32 v91, v194, v195\n"
      "ds_write_b128 v210, v[88:91] offset:18432\n"
      "v_lshlrev_b32 v188, 16, v92\n"
      "v_and_b32 v189, 0xffff0000, v92\n"
      "v_lshlrev_b32 v190, 16, v93\n"
      "v_and_b32 v191, 0xffff0000, v93\n"
      "v_lshlrev_b32 v192, 16, v94\n"
      "v_and_b32 v193, 0xffff0000, v94\n"
      "v_lshlrev_b32 v194, 16, v95\n"
      "v_and_b32 v195, 0xffff0000, v95\n"
      "v_pk_mul_f32 v[188:189], v[188:189], s[94:95]\n"
      "v_pk_mul_f32 v[190:191], v[190:191], s[94:95]\n"
      "v_pk_mul_f32 v[192:193], v[192:193], s[94:95]\n"
      "v_pk_mul_f32 v[194:195], v[194:195], s[94:95]\n"
      "v_cvt_pk_bf16_f32 v92, v188, v189\n"
      "v_cvt_pk_bf16_f32 v93, v190, v191\n"
      "v_cvt_pk_bf16_f32 v94, v192, v193\n"
      "v_cvt_pk_bf16_f32 v95, v194, v195\n"
      "ds_write_b128 v210, v[92:95] offset:19456\n"
      "s_mov_b32 m0, s62\n"
      "s_nop 0\n"
      "buffer_load_dwordx4 v248, s[56:59], s60 offen nt lds\n"
      "buffer_load_dwordx4 v248, s[56:59], s60 offen offset:2048 nt lds\n"
      "s_bitset1_b32 m0, 10\n"
      "s_nop 0\n"
      "buffer_load_dwordx4 v249, s[56:59], s60 offen nt lds\n"
      "buffer_load_dwordx4 v249, s[56:59], s60 offen offset:2048 nt lds\n"
      "s_xor_b64 s[56:57], s[56:57], s[32:33]\n"
      "s_bitset1_b32 s61, 15\n"
      "s_add_u32 s61, s61, 0x2000\n"
      "s_bitset0_b32 s61, 15\n"
      "s_xor_b32 s60, s61, s51\n"
      "s_add_u32 s62, s62, 0x4000\n"
      "s_and_b32 s62, s62, 0x1ffff\n"
      "s_cmp_eq_u32 s50, 0\n"
      "s_cselect_b32 s38, 0x41554001, 1\n"
      "s_cselect_b32 s39, 0x3f440083, 3\n"
      "s_cselect_b32 s52, 0x3133aa90, 0\n"
      "s_cselect_b32 s53, 0x80011, 0\n"
      "s_mov_b64 s[34:35], 0\n"
      "s_and_b32 s69, %[blk], 7\n"
      "s_mul_i32 s69, s69, 5\n"
      "s_add_u32 s69, s69, s44\n"
      "s_and_b32 s69, s69, 15\n"
      "s_mul_i32 s74, s69, 20\n"
      "s_lshr_b32 s74, s74, 6\n"
      "s_lshl2_add_u32 s76, s74, 9\n"
      "s_bitset1_b64 s[34:35], s76\n"
      "s_lshl_b32 s74, s74, 1\n"
      "s_sub_u32 s74, 28, s74\n"
      "s_bitset1_b32 s39, s74\n"
      "s_mul_i32 s74, s69, 24\n"
      "s_lshr_b32 s74, s74, 6\n"
      "s_lshl2_add_u32 s76, s74, 29\n"
      "s_bitset1_b64 s[34:35], s76\n"
      "s_lshl_b32 s74, s74, 1\n"
      "s_sub_u32 s74, 18, s74\n"
      "s_bitset1_b32 s39, s74\n"
      "s_mul_i32 s74, s69, 12\n"
      "s_lshr_b32 s74, s74, 6\n"
      "s_lshl2_add_u32 s76, s74, 53\n"
      "s_bitset1_b64 s[34:35], s76\n"
      "s_lshl_b32 s74, s74, 1\n"
      "s_sub_u32 s74, 6, s74\n"
      "s_bitset1_b32 s39, s74\n"
      "s_mov_b32 s87, 0\n"
      "s_mov_b32 s55, 0\n"
      "s_mov_b32 s54, 3\n"
      "s_mov_b32 s70, 250\n"
      "s_mov_b32 s86, 59\n"
      "s_mov_b32 s77, 177\n"
      "s_lshl_b32 s91, s50, 16\n"
      "s_sub_u32 s80, 46, s91\n"
      "s_sub_u32 s90, 34, s91\n"
      "s_sub_u32 s72, s75, 14336\n"
      "s_and_b32 s79, %[blk], 31\n"
      "s_lshl_b32 s79, s79, 8\n"
      "s_add_u32 s79, s79, 8192\n"
      "s_and_b32 s69, %[blk], 7\n"
      "s_lshl_b32 s69, s69, 2\n"
      "s_and_b32 s74, %[blk], 0xe0\n"
      "s_add_u32 s69, s69, s74\n"
      "s_mul_i32 s69, s69, 53\n"
      "s_lshr_b32 s69, s69, 8\n"
      "s_add_u32 s66, s93, 150\n"
      "s_sub_u32 s81, s93, s71\n"
      "s_sub_u32 s81, s81, s69\n"
      "s_add_u32 s81, s81, 24\n"
      "s_sub_u32 s66, s66, s91\n"
      "s_mov_b32 s88, s66\n"
      "s_mov_b32 s65, s66\n"
      "s_getpc_b64 s[84:85]\n"
      "PC_%=:\n"
      "s_add_u32 s84, s84, HOOK_%=-PC_%=\n"
      "s_addc_u32 s85, s85, 0\n"
      "s_waitcnt vmcnt(14) lgkmcnt(0)\n"
      "s_barrier\n"
      "ds_read_b128 a[64:67], v198 offset:8192\n"
      "ds_read_b128 a[72:75], v199 offset:8192\n"
      "ds_read_b128 a[80:83], v198 offset:9216\n"
      "ds_read_b128 a[88:91], v199 offset:9216\n"
      "ds_read_b128 a[0:3], v196 offset:0\n"
      "ds_read_b128 a[16:19], v197 offset:0\n"
      "ds_read_b128 a[32:35], v196 offset:1024\n"
      "ds_read_b128 a[48:51], v197 offset:1024\n"
      "ds_read_b128 a[4:7], v196 offset:4096\n"
      "ds_read_b128 a[20:23], v197 offset:4096\n"
      "ds_read_b128 a[36:39], v196 offset:5120\n"
      "ds_read_b128 a[52:55], v197 offset:5120\n"
      "v_readfirstlane_b32 s40, v208\n"
      "s_mov_b32 s94, 0xceabfb8f\n"
      "v_lshlrev_b32 v144, 16, v200\n"
      "v_and_b32 v145, 0xffff0000, v200\n"
      "v_lshlrev_b32 v146, 16, v201\n"
      "v_and_b32 v147, 0xffff0000, v201\n"
      "v_lshlrev_b32 v148, 16, v202\n"
      "v_and_b32 v149, 0xffff0000, v202\n"
      "v_lshlrev_b32 v150, 16, v203\n"
      "v_and_b32 v151, 0xffff0000, v203\n"
      "v_lshlrev_b32 v152, 16, v204\n"
      "v_and_b32 v153, 0xffff0000, v204\n"
      "v_lshlrev_b32 v154, 16, v205\n"
      "v_and_b32 v155, 0xffff0000, v205\n"
      "v_lshlrev_b32 v156, 16, v206\n"
      "v_and_b32 v157, 0xffff0000, v206\n"
      "v_lshlrev_b32 v158, 16, v207\n"
      "v_and_b32 v159, 0xffff0000, v207\n"
      "s_waitcnt lgkmcnt(4)\n"
      "v_mfma_f32_16x16x32_bf16 v[64:67], a[64:67], a[0:3], 0\n"
      "ds_read_b128 a[8:11], v214 offset:0\n"
      "v_fma_f32 v144, -v144, s94, s94\n"
      "v_mfma_f32_16x16x32_bf16 v[64:67], a[72:75], a[16:19], v[64:67]\n"
      "ds_read_b128 a[24:27], v215 offset:0\n"
      "v_fma_f32 v145, -v145, s94, s94\n"
      "v_mfma_f32_16x16x32_bf16 v[64:67], a[80:83], a[32:35], v[64:67]\n"
      "ds_read_b128 a[40:43], v214 offset:1024\n"
      "v_fma_f32 v146, -v146, s94, s94\n"
      "v_mfma_f32_16x16x32_bf16 v[64:67], a[88:91], a[48:51], v[64:67]\n"
      "ds_read_b128 a[56:59], v215 offset:1024\n"
      "v_fma_f32 v147, -v147, s94, s94\n"
      "s_waitcnt lgkmcnt(4)\n"
      "v_mfma_f32_16x16x32_bf16 v[72:75], a[64:67], a[4:7], 0\n"
      "ds_read_b128 a[12:15], v214 offset:4096\n"
      "v_fma_f32 v148, -v148, s94, s94\n"
      "v_mfma_f32_16x16x32_bf16 v[72:75], a[72:75], a[20:23], v[72:75]\n"
      "ds_read_b128 a[28:31], v215 offset:4096\n"
      "v_fma_f32 v149, -v149, s94, s94\n"
      "v_mfma_f32_16x16x32_bf16 v[72:75], a[80:83], a[36:39], v[72:75]\n"
      "ds_read_b128 a[44:47], v214 offset:5120\n"
      "v_fma_f32 v150, -v150, s94, s94\n"
      "v_mfma_f32_16x16x32_bf16 v[72:75], a[88:91], a[52:55], v[72:75]\n"
      "ds_read_b128 a[60:63], v215 offset:5120\n"
      "v_fma_f32 v151, -v151, s94, s94\n"
      "s_waitcnt lgkmcnt(4)\n"
      "v_mfma_f32_16x16x32_bf16 v[80:83], a[64:67], a[8:11], 0\n"
      "ds_read_b128 a[96:99], v198 offset:0\n"
      "v_fma_f32 v152, -v152, s94, s94\n"
      "v_mfma_f32_16x16x32_bf16 v[80:83], a[72:75], a[24:27], v[80:83]\n"
      "ds_read_b128 a[100:103], v198 offset:4096\n"
      "v_fma_f32 v153, -v153, s94, s94\n"
      "v_mfma_f32_16x16x32_bf16 v[80:83], a[80:83], a[40:43], v[80:83]\n"
      "ds_read_b128 a[104:107], v199 offset:0\n"
      "v_fma_f32 v154, -v154, s94, s94\n"
      "v_mfma_f32_16x16x32_bf16 v[80:83], a[88:91], a[56:59], v[80:83]\n"
      "ds_read_b128 a[108:111], v199 offset:4096\n"
      "v_fma_f32 v155, -v155, s94, s94\n"
      "s_waitcnt lgkmcnt(4)\n"
      "v_mfma_f32_16x16x32_bf16 v[88:91], a[64:67], a[12:15], 0\n"
      "ds_read_b128 a[112:115], v198 offset:1024\n"
      "v_fma_f32 v156, -v156, s94, s94\n"
      "v_mfma_f32_16x16x32_bf16 v[88:91], a[72:75], a[28:31], v[88:91]\n"
      "ds_read_b128 a[116:119], v198 offset:5120\n"
      "v_fma_f32 v157, -v157, s94, s94\n"
      "v_mfma_f32_16x16x32_bf16 v[88:91], a[80:83], a[44:47], v[88:91]\n"
      "ds_read_b128 a[120:123], v199 offset:1024\n"
      "v_fma_f32 v158, -v158, s94, s94\n"
      "v_mfma_f32_16x16x32_bf16 v[88:91], a[88:91], a[60:63], v[88:91]\n"
      "ds_read_b128 a[124:127], v199 offset:5120\n"
      "v_fma_f32 v159, -v159, s94, s94\n"
      "s_mov_b32 m0, s62\n"
      "s_nop 0\n"
      "buffer_load_dwordx4 v248, s[56:59], s60 offen nt lds\n"
      "buffer_load_dwordx4 v248, s[56:59], s60 offen offset:2048 nt lds\n"
      "s_bitset1_b32 m0, 10\n"
      "s_nop 0\n"
      "buffer_load_dwordx4 v249, s[56:59], s60 offen nt lds\n"
      "buffer_load_dwordx4 v249, s[56:59], s60 offen offset:2048 nt lds\n"
      "s_waitcnt lgkmcnt(0)\n"
      "v_mfma_f32_16x16x32_bf16 v[96:99], a[96:99], a[0:3], v[144:147]\n"
      "s_add_u32 s40, s40, 0x7f4a7c15\n"
      "v_mfma_f32_16x16x32_bf16 v[96:99], a[104:107], a[16:19], v[96:99]\n"
      "s_lshl_b32 s69, s44, 3\n"
      "v_max3_f32 v188, v64, v65, v66\n"
      "v_max3_f32 v189, v72, v73, v74\n"
      "v_mfma_f32_16x16x32_bf16 v[96:99], a[112:115], a[32:35], v[96:99]\n"
      "s_lshr_b32 s74, s50, 12\n"
      "v_max3_f32 v190, v80, v81, v82\n"
      "v_max3_f32 v191, v88, v89, v90\n"
      "v_mfma_f32_16x16x32_bf16 v[96:99], a[120:123], a[48:51], v[96:99]\n"
      "s_add_u32 s69, s69, s74\n"
      "v_max_f32 v188, v188, v67\n"
      "v_max_f32 v189, v189, v75\n"
      "v_mfma_f32_16x16x32_bf16 v[104:107], a[96:99], a[4:7], v[152:155]\n"
      "s_mul_i32 s69, s69, 4608\n"
      "v_max_f32 v190, v190, v83\n"
      "v_max_f32 v191, v191, v91\n"
      "v_mfma_f32_16x16x32_bf16 v[104:107], a[104:107], a[20:23], v[104:107]\n"
      "s_add_u32 s69, s69, 81920\n"
      "v_mov_b32 v192, v188\n"
      "v_mov_b32 v193, v189\n"
      "v_mfma_f32_16x16x32_bf16 v[104:107], a[112:115], a[36:39], v[104:107]\n"
      "s_add_u32 s24, s18, s69\n"
      "v_mov_b32 v194, v190\n"
      "v_mov_b32 v195, v191\n"
      "v_mfma_f32_16x16x32_bf16 v[104:107], a[120:123], a[52:55], v[104:107]\n"
      "s_addc_u32 s25, s19, 0\n"
      "v_permlane16_swap_b32 v188, v192\n"
      "v_permlane16_swap_b32 v189, v193\n"
      "v_mfma_f32_16x16x32_bf16 v[112:115], a[96:99], a[8:11], v[144:147]\n"
      "s_movk_i32 s26, 23040\n"
      "v_permlane16_swap_b32 v190, v194\n"
      "v_permlane16_swap_b32 v191, v195\n"
      "v_mfma_f32_16x16x32_bf16 v[112:115], a[104:107], a[24:27], v[112:115]\n"
      "s_mul_i32 s78, s43, 18432\n"
      "v_max_f32 v188, v188, v192\n"
      "v_max_f32 v189, v189, v193\n"
      "v_mfma_f32_16x16x32_bf16 v[112:115], a[112:115], a[40:43], v[112:115]\n"
      "s_and_b32 s69, s50, 4096\n"
      "v_max_f32 v190, v190, v194\n"
      "v_max_f32 v191, v191, v195\n"
      "v_mfma_f32_16x16x32_bf16 v[112:115], a[120:123], a[56:59], v[112:115]\n"
      "s_lshl_b32 s69, s69, 2\n"
      "v_mov_b32 v192, v188\n"
      "v_mov_b32 v193, v189\n"
      "v_mfma_f32_16x16x32_bf16 v[120:123], a[96:99], a[12:15], v[152:155]\n"
      "s_and_b32 s74, s50, 8192\n"
      "v_mov_b32 v194, v190\n"
      "v_mov_b32 v195, v191\n"
      "v_mfma_f32_16x16x32_bf16 v[120:123], a[104:107], a[28:31], v[120:123]\n"
      "s_add_u32 s74, s74, 4096\n"
      "v_permlane32_swap_b32 v188, v192\n"
      "v_permlane32_swap_b32 v189, v193\n"
      "v_mfma_f32_16x16x32_bf16 v[120:123], a[112:115], a[44:47], v[120:123]\n"
      "s_add_u32 s69, s69, s74\n"
      "v_permlane32_swap_b32 v190, v194\n"
      "v_permlane32_swap_b32 v191, v195\n"
      "v_mfma_f32_16x16x32_bf16 v[120:123], a[120:123], a[60:63], v[120:123]\n"
      "s_lshl_b32 s74, s43, 7\n"
      "v_max_f32 v188, v188, v192\n"
      "v_max_f32 v189, v189, v193\n"
      "v_mfma_f32_16x16x32_bf16 v[100:103], a[100:103], a[0:3], v[148:151]\n"
      "s_add_u32 s69, s69, s74\n"
      "v_max_f32 v190, v190, v194\n"
      "v_max_f32 v191, v191, v195\n"
      "v_mfma_f32_16x16x32_bf16 v[100:103], a[108:111], a[16:19], v[100:103]\n"
      "s_lshl_b32 s74, s44, 15\n"
      "v_xor_b32 v176, 0x80000000, v188\n"
      "v_xor_b32 v177, 0x80000000, v189\n"
      "v_mfma_f32_16x16x32_bf16 v[100:103], a[116:119], a[32:35], v[100:103]\n"
      "s_add_u32 s69, s69, s74\n"
      "v_xor_b32 v178, 0x80000000, v190\n"
      "v_xor_b32 v179, 0x80000000, v191\n"
      "v_mfma_f32_16x16x32_bf16 v[100:103], a[124:127], a[48:51], v[100:103]\n"
      "s_add_u32 s30, s16, s69\n"
      "v_mov_b32 v160, v176\n"
      "v_mov_b32 v161, v176\n"
      "v_mfma_f32_16x16x32_bf16 v[108:111], a[100:103], a[4:7], v[156:159]\n"
      "s_addc_u32 s31, s17, 0\n"
      "v_mov_b32 v162, v176\n"
      "v_mov_b32 v163, v176\n"
      "v_mfma_f32_16x16x32_bf16 v[108:111], a[108:111], a[20:23], v[108:111]\n"
      "v_mov_b32 v164, v177\n"
      "v_mov_b32 v165, v177\n"
      "v_mfma_f32_16x16x32_bf16 v[108:111], a[116:119], a[36:39], v[108:111]\n"
      "v_mov_b32 v166, v177\n"
      "v_mov_b32 v167, v177\n"
      "v_mfma_f32_16x16x32_bf16 v[108:111], a[124:127], a[52:55], v[108:111]\n"
      "v_mov_b32 v168, v178\n"
      "v_mov_b32 v169, v178\n"
      "v_mfma_f32_16x16x32_bf16 v[116:119], a[100:103], a[8:11], v[148:151]\n"
      "v_mov_b32 v170, v178\n"
      "v_mov_b32 v171, v178\n"
      "v_mfma_f32_16x16x32_bf16 v[116:119], a[108:111], a[24:27], v[116:119]\n"
      "v_mov_b32 v172, v179\n"
      "v_mov_b32 v173, v179\n"
      "v_mfma_f32_16x16x32_bf16 v[116:119], a[116:119], a[40:43], v[116:119]\n"
      "v_mov_b32 v174, v179\n"
      "v_mov_b32 v175, v179\n"
      "v_mfma_f32_16x16x32_bf16 v[116:119], a[124:127], a[56:59], v[116:119]\n"
      "v_mov_b32 v209, 139248\n"
      "v_lshl_add_u32 v232, s50, 1, v241\n"
      "v_mfma_f32_16x16x32_bf16 v[124:127], a[100:103], a[12:15], v[156:159]\n"
      "v_add_u32 v232, 0x10000, v232\n"
      "v_xor_b32 v233, 0x4000, v232\n"
      "v_mfma_f32_16x16x32_bf16 v[124:127], a[108:111], a[28:31], v[124:127]\n"
      "v_add_u32 v234, 0x10000, v232\n"
      "v_xor_b32 v235, 0x4000, v234\n"
      "v_mfma_f32_16x16x32_bf16 v[124:127], a[116:119], a[44:47], v[124:127]\n"
      "v_lshrrev_b32 v240, 1, v241\n"
      "v_add_u32 v240, 0x1000, v240\n"
      "v_mfma_f32_16x16x32_bf16 v[124:127], a[124:127], a[60:63], v[124:127]\n"
      "v_mov_b32 v196, 0\n"
      "v_pk_add_f32 v[96:97], v[96:97], v[160:161]\n"
      "v_pk_add_f32 v[98:99], v[98:99], v[160:161]\n"
      "v_pk_add_f32 v[104:105], v[104:105], v[164:165]\n"
      "v_pk_add_f32 v[106:107], v[106:107], v[164:165]\n"
      "v_pk_add_f32 v[112:113], v[112:113], v[168:169]\n"
      "v_pk_add_f32 v[114:115], v[114:115], v[168:169]\n"
      "v_pk_add_f32 v[120:121], v[120:121], v[172:173]\n"
      "v_pk_add_f32 v[122:123], v[122:123], v[172:173]\n"
      "v_pk_add_f32 v[100:101], v[100:101], v[160:161]\n"
      "v_pk_add_f32 v[102:103], v[102:103], v[160:161]\n"
      "v_pk_add_f32 v[108:109], v[108:109], v[164:165]\n"
      "v_pk_add_f32 v[110:111], v[110:111], v[164:165]\n"
      "v_pk_add_f32 v[116:117], v[116:117], v[168:169]\n"
      "v_pk_add_f32 v[118:119], v[118:119], v[168:169]\n"
      "v_pk_add_f32 v[124:125], v[124:125], v[172:173]\n"
      "v_pk_add_f32 v[126:127], v[126:127], v[172:173]\n"
      "v_mov_b32 v224, v244\n"
      "v_mov_b32 v225, v245\n"
      "v_mov_b32 v226, v246\n"
      "v_mov_b32 v227, v247\n"
      "v_xor_b32 v220, 0x2000, v244\n"
      "v_xor_b32 v221, 0x2000, v245\n"
      "v_xor_b32 v222, 0x2000, v246\n"
      "v_xor_b32 v223, 0x2000, v247\n"
      "s_lshl_b32 s76, s43, 3\n"
      "v_mov_b32 v237, s40\n"
      "v_mov_b32 v238, s76\n"
      "v_xor_b32 v239, 8, v238\n"
      "s_memrealtime s[68:69]\n"
      "s_waitcnt vmcnt(14) lgkmcnt(0)\n"
      "s_sub_u32 s67, s68, s88\n"
      "s_cmp_lt_i32 s67, -8\n"
      "s_cbranch_scc1 GW0_%=\n"
      "GX0_%=:\n"
      "s_barrier\n"
      "ds_read_b128 a[64:67], v242 offset:0\n"
      "ds_read_b128 a[68:71], v242 offset:4096\n"
      "ds_read_b128 a[72:75], v243 offset:0\n"
      "ds_read_b128 a[76:79], v243 offset:4096\n"
      "ds_read_b128 a[80:83], v242 offset:1024\n"
      "ds_read_b128 a[84:87], v242 offset:5120\n"
      "ds_read_b128 a[88:91], v243 offset:1024\n"
      "ds_read_b128 a[92:95], v243 offset:5120\n"
      "ds_write_b32 v209, v196\n"
      "s_add_u32 s38, s38, s38\n"
      "s_memrealtime s[68:69]\n"
      "s_xor_b64 s[56:57], s[56:57], s[32:33]\n"
      "s_bitset1_b32 s61, 15\n"
      "s_add_u32 s61, s61, 0x2000\n"
      "s_bitset0_b32 s61, 15\n"
      "s_xor_b32 s60, s61, s51\n"
      "s_add_u32 s62, s62, 0x4000\n"
      "s_and_b32 s62, s62, 0x1ffff\n"
      "s_waitcnt lgkmcnt(0)\n"
      "s_sub_u32 s67, s68, s66\n"
      "s_cmp_gt_i32 s67, s70\n"
      "s_cselect_b32 s69, s77, s86\n"
      "s_cselect_b32 s70, 60, s70\n"
      "s_cselect_b32 s90, s80, s90\n"
      "s_add_u32 s66, s66, s69\n"
      "s_mov_b32 s82, s68\n"
      "LOOP_%=:\n"
      "v_mfma_f32_16x16x32_bf16 v[64:67], a[64:67], a[0:3], v[160:163]\n"
      "s_add_u32 s65, s68, s90\n"
      "v_exp_f32 v188, v96\n"
      "v_mfma_f32_16x16x32_bf16 v[64:67], a[72:75], a[16:19], v[64:67]\n"
      "s_getreg_b32 s67, hwreg(HW_REG_IB_STS, 0, 4)\n"
      "v_add_f32 v180, v180, v184\n"
      "v_add_f32 v181, v181, v185\n"
      "v_mfma_f32_16x16x32_bf16 v[64:67], a[80:83], a[32:35], v[64:67]\n"
      "s_cmp_gt_u32 s67, 6\n"
      "v_exp_f32 v189, v97\n"
      "v_mfma_f32_16x16x32_bf16 v[64:67], a[88:91], a[48:51], v[64:67]\n"
      "s_cselect_b32 s67, s87, 0\n"
      "v_add_f32 v182, v182, v186\n"
      "v_add_f32 v183, v183, v187\n"
      "v_mfma_f32_16x16x32_bf16 v[72:75], a[64:67], a[4:7], v[164:167]\n"
      "s_max_i32 s67, s67, s55\n"
      "v_exp_f32 v190, v98\n"
      "v_mfma_f32_16x16x32_bf16 v[72:75], a[72:75], a[20:23], v[72:75]\n"
      "s_sub_u32 s55, s55, s67\n"
      "v_exp_f32 v191, v99\n"
      "v_mfma_f32_16x16x32_bf16 v[72:75], a[80:83], a[36:39], v[72:75]\n"
      "s_add_u32 s66, s66, s67\n"
      "v_cvt_pk_bf16_f32 v144, v188, v189\n"
      "v_cvt_pk_bf16_f32 v145, v190, v191\n"
      "v_mfma_f32_16x16x32_bf16 v[72:75], a[88:91], a[52:55], v[72:75]\n"
      "s_max_u32 s88, s66, s65\n"
      "v_exp_f32 v192, v100\n"
      "v_mfma_f32_16x16x32_bf16 v[80:83], a[64:67], a[8:11], v[168:171]\n"
      "s_add_u32 s39, s39, s39\n"
      "v_exp_f32 v193, v101\n"
      "v_mfma_f32_16x16x32_bf16 v[80:83], a[72:75], a[24:27], v[80:83]\n"
      "s_memrealtime s[68:69]\n"
      "v_exp_f32 v194, v102\n"
      "v_mfma_f32_16x16x32_bf16 v[80:83], a[80:83], a[40:43], v[80:83]\n"
      "s_mov_b32 m0, s62\n"
      "v_exp_f32 v195, v103\n"
      "v_mfma_f32_16x16x32_bf16 v[80:83], a[88:91], a[56:59], v[80:83]\n"
      "buffer_load_dwordx4 v248, s[56:59], s60 offen nt lds\n"
      "v_mfma_f32_16x16x32_bf16 v[88:91], a[64:67], a[12:15], v[172:175]\n"
      "buffer_load_dwordx4 v248, s[56:59], s60 offen offset:2048 nt lds\n"
      "v_mfma_f32_16x16x32_bf16 v[88:91], a[72:75], a[28:31], v[88:91]\n"
      "s_bitset1_b32 m0, 10\n"
      "v_exp_f32 v188, v104\n"
      "v_mfma_f32_16x16x32_bf16 v[88:91], a[80:83], a[44:47], v[88:91]\n"
      "buffer_load_dwordx4 v249, s[56:59], s60 offen nt lds\n"
      "v_mfma_f32_16x16x32_bf16 v[88:91], a[88:91], a[60:63], v[88:91]\n"
      "buffer_load_dwordx4 v249, s[56:59], s60 offen offset:2048 nt lds\n"
      "s_cbranch_scc1 SP1_%=\n"
      "HD1_%=:\n"
      "s_cmp_lg_u32 s54, 0\n"
      "s_waitcnt vmcnt(12) lgkmcnt(0)\n"
      "s_cbranch_scc1 W1_%=\n"
      "s_waitcnt vmcnt(8)\n"
      "W1_%=:\n"
      "s_sub_u32 s67, s68, s88\n"
      "s_cmp_lt_i32 s67, -8\n"
      "s_cbranch_scc1 GW4_%=\n"
      "GX4_%=:\n"
      "s_barrier\n"
      "v_mfma_f32_16x16x32_bf16 v[68:71], a[68:71], a[0:3], v[160:163]\n"
      "s_memrealtime s[68:69]\n"
      "ds_read_b64_tr_b16 a[96:97], v244 offset:0\n"
      "ds_read_b64_tr_b16 a[98:99], v244 offset:4096\n"
      "v_mfma_f32_16x16x32_bf16 v[68:71], a[76:79], a[16:19], v[68:71]\n"
      "s_xor_b64 s[56:57], s[56:57], s[32:33]\n"
      "ds_read_b64_tr_b16 a[100:101], v245 offset:0\n"
      "ds_read_b64_tr_b16 a[102:103], v245 offset:4096\n"
      "v_mfma_f32_16x16x32_bf16 v[68:71], a[84:87], a[32:35], v[68:71]\n"
      "s_bitset1_b32 s61, 15\n"
      "ds_read_b64_tr_b16 a[104:105], v246 offset:0\n"
      "ds_read_b64_tr_b16 a[106:107], v246 offset:4096\n"
      "v_mfma_f32_16x16x32_bf16 v[68:71], a[92:95], a[48:51], v[68:71]\n"
      "s_add_u32 s61, s61, 0x2000\n"
      "ds_read_b64_tr_b16 a[108:109], v247 offset:0\n"
      "ds_read_b64_tr_b16 a[110:111], v247 offset:4096\n"
      "v_mfma_f32_16x16x32_bf16 v[76:79], a[68:71], a[4:7], v[164:167]\n"
      "s_bitset0_b32 s61, 15\n"
      "v_exp_f32 v189, v105\n"
      "v_mfma_f32_16x16x32_bf16 v[76:79], a[76:79], a[20:23], v[76:79]\n"
      "s_xor_b32 s60, s61, s51\n"
      "v_cvt_pk_bf16_f32 v146, v192, v193\n"
      "v_cvt_pk_bf16_f32 v147, v194, v195\n"
      "v_mfma_f32_16x16x32_bf16 v[76:79], a[84:87], a[36:39], v[76:79]\n"
      "s_add_u32 s62, s62, 0x4000\n"
      "v_exp_f32 v190, v106\n"
      "v_mfma_f32_16x16x32_bf16 v[184:187], a[128:131], v[144:147], 0\n"
      "s_and_b32 s62, s62, 0x1ffff\n"
      "v_exp_f32 v191, v107\n"
      "v_mfma_f32_16x16x32_bf16 v[76:79], a[92:95], a[52:55], v[76:79]\n"
      "v_cvt_pk_bf16_f32 v148, v188, v189\n"
      "v_cvt_pk_bf16_f32 v149, v190, v191\n"
      "v_mfma_f32_16x16x32_bf16 v[84:87], a[68:71], a[8:11], v[168:171]\n"
      "v_exp_f32 v192, v108\n"
      "v_mfma_f32_16x16x32_bf16 v[84:87], a[76:79], a[24:27], v[84:87]\n"
      "v_exp_f32 v193, v109\n"
      "s_waitcnt lgkmcnt(0)\n"
      "v_mfma_f32_16x16x32_bf16 v[84:87], a[84:87], a[40:43], v[84:87]\n"
      "s_sub_u32 s67, s68, s66\n"
      "ds_read_b64_tr_b16 a[112:113], v244 offset:1024\n"
      "v_cvt_pk_bf16_f32 v150, v192, v193\n"
      "v_mfma_f32_16x16x32_bf16 v[84:87], a[92:95], a[56:59], v[84:87]\n"
      "s_cmp_gt_i32 s67, s70\n"
      "v_exp_f32 v194, v110\n"
      "v_mfma_f32_16x16x32_bf16 v[92:95], a[68:71], a[12:15], v[172:175]\n"
      "s_cselect_b32 s69, s77, s86\n"
      "ds_read_b64_tr_b16 a[114:115], v244 offset:5120\n"
      "ds_read_b64_tr_b16 a[116:117], v245 offset:1024\n"
      "v_mfma_f32_16x16x32_bf16 v[92:95], a[76:79], a[28:31], v[92:95]\n"
      "s_cselect_b32 s70, 60, s70\n"
      "v_exp_f32 v195, v111\n"
      "v_mfma_f32_16x16x32_bf16 v[92:95], a[84:87], a[44:47], v[92:95]\n"
      "s_cselect_b32 s90, s80, s90\n"
      "ds_read_b64_tr_b16 a[118:119], v245 offset:5120\n"
      "v_cvt_pk_bf16_f32 v151, v194, v195\n"
      "v_mfma_f32_16x16x32_bf16 v[92:95], a[92:95], a[60:63], v[92:95]\n"
      "s_add_u32 s66, s66, s69\n"
      "v_exp_f32 v188, v112\n"
      "v_mfma_f32_16x16x32_bf16 v[184:187], a[132:135], v[148:151], v[184:187]\n"
      "s_mov_b32 s82, s68\n"
      "ds_read_b64_tr_b16 a[120:121], v246 offset:1024\n"
      "ds_read_b64_tr_b16 a[122:123], v246 offset:5120\n"
      "v_mfma_f32_16x16x32_bf16 v[0:3], a[96:99], v[128:131], v[0:3]\n"
      "s_add_u32 s65, s68, s90\n"
      "v_exp_f32 v189, v113\n"
      "v_mfma_f32_16x16x32_bf16 v[4:7], a[96:99], v[132:135], v[4:7]\n"
      "s_getreg_b32 s67, hwreg(HW_REG_IB_STS, 0, 4)\n"
      "ds_read_b64_tr_b16 a[124:125], v247 offset:1024\n"
      "v_cvt_pk_bf16_f32 v152, v188, v189\n"
      "v_mfma_f32_16x16x32_bf16 a[144:147], a[96:99], v[136:139], a[144:147]\n"
      "s_cmp_gt_u32 s67, 6\n"
      "v_exp_f32 v190, v114\n"
      "v_mfma_f32_16x16x32_bf16 a[148:151], a[96:99], v[140:143], a[148:151]\n"
      "s_cselect_b32 s67, s87, 0\n"
      "v_exp_f32 v191, v115\n"
      "v_mfma_f32_16x16x32_bf16 a[156:159], a[100:103], v[140:143], a[156:159]\n"
      "s_max_i32 s67, s67, s55\n"
      "ds_read_b64_tr_b16 a[126:127], v247 offset:5120\n"
      "v_cvt_pk_bf16_f32 v153, v190, v191\n"
      "v_mfma_f32_16x16x32_bf16 a[152:155], a[100:103], v[136:139], a[152:155]\n"
      "s_sub_u32 s55, s55, s67\n"
      "v_exp_f32 v192, v116\n"
      "v_mfma_f32_16x16x32_bf16 v[12:15], a[100:103], v[132:135], v[12:15]\n"
      "s_add_u32 s66, s66, s67\n"
      "v_exp_f32 v193, v117\n"
      "v_mfma_f32_16x16x32_bf16 v[8:11], a[100:103], v[128:131], v[8:11]\n"
      "s_max_u32 s88, s66, s65\n"
      "v_exp_f32 v194, v118\n"
      "v_mfma_f32_16x16x32_bf16 v[16:19], a[104:107], v[128:131], v[16:19]\n"
      "s_add_u32 s38, s38, s38\n"
      "v_exp_f32 v195, v119\n"
      "v_mfma_f32_16x16x32_bf16 v[20:23], a[104:107], v[132:135], v[20:23]\n"
      "s_memrealtime s[68:69]\n"
      "v_cvt_pk_bf16_f32 v154, v192, v193\n"
      "v_cvt_pk_bf16_f32 v155, v194, v195\n"
      "v_mfma_f32_16x16x32_bf16 a[160:163], a[104:107], v[136:139], a[160:163]\n"
      "s_mov_b32 m0, s62\n"
      "v_exp_f32 v188, v120\n"
      "v_mfma_f32_16x16x32_bf16 v[184:187], a[136:139], v[152:155], v[184:187]\n"
      "buffer_load_dwordx4 v248, s[56:59], s60 offen nt lds\n"
      "v_mfma_f32_16x16x32_bf16 a[164:167], a[104:107], v[140:143], a[164:167]\n"
      "buffer_load_dwordx4 v248, s[56:59], s60 offen offset:2048 nt lds\n"
      "v_mfma_f32_16x16x32_bf16 a[172:175], a[108:111], v[140:143], a[172:175]\n"
      "s_bitset1_b32 m0, 10\n"
      "v_exp_f32 v189, v121\n"
      "v_mfma_f32_16x16x32_bf16 a[168:171], a[108:111], v[136:139], a[168:171]\n"
      "buffer_load_dwordx4 v249, s[56:59], s60 offen nt lds\n"
      "v_mfma_f32_16x16x32_bf16 v[28:31], a[108:111], v[132:135], v[28:31]\n"
      "buffer_load_dwordx4 v249, s[56:59], s60 offen offset:2048 nt lds\n"
      "s_cbranch_scc1 SP2_%=\n"
      "HD2_%=:\n"
      "s_cmp_lg_u32 s54, 0\n"
      "s_waitcnt vmcnt(12) lgkmcnt(0)\n"
      "s_cbranch_scc1 W2_%=\n"
      "s_waitcnt vmcnt(8)\n"
      "W2_%=:\n"
      "s_sub_u32 s67, s68, s88\n"
      "s_cmp_lt_i32 s67, -8\n"
      "s_cbranch_scc1 GW5_%=\n"
      "GX5_%=:\n"
      "s_barrier\n"
      "v_mfma_f32_16x16x32_bf16 v[24:27], a[108:111], v[128:131], v[24:27]\n"
      "s_memrealtime s[68:69]\n"
      "ds_read_b128 a[64:67], v242 offset:32768\n"
      "ds_read_b128 a[72:75], v243 offset:32768\n"
      "v_mfma_f32_16x16x32_bf16 v[32:35], a[112:115], v[128:131], v[32:35]\n"
      "s_xor_b64 s[56:57], s[56:57], s[32:33]\n"
      "ds_read_b128 a[80:83], v242 offset:33792\n"
      "ds_read_b128 a[88:91], v243 offset:33792\n"
      "v_mfma_f32_16x16x32_bf16 v[36:39], a[112:115], v[132:135], v[36:39]\n"
      "s_bitset1_b32 s61, 15\n"
      "v_exp_f32 v190, v122\n"
      "v_mfma_f32_16x16x32_bf16 a[176:179], a[112:115], v[136:139], a[176:179]\n"
      "s_add_u32 s61, s61, 0x2000\n"
      "v_exp_f32 v191, v123\n"
      "v_mfma_f32_16x16x32_bf16 a[180:183], a[112:115], v[140:143], a[180:183]\n"
      "s_bitset0_b32 s61, 15\n"
      "v_cvt_pk_bf16_f32 v156, v188, v189\n"
      "v_cvt_pk_bf16_f32 v157, v190, v191\n"
      "v_mfma_f32_16x16x32_bf16 a[188:191], a[116:119], v[140:143], a[188:191]\n"
      "s_xor_b32 s60, s61, s51\n"
      "v_exp_f32 v192, v124\n"
      "v_mfma_f32_16x16x32_bf16 a[184:187], a[116:119], v[136:139], a[184:187]\n"
      "s_add_u32 s62, s62, 0x4000\n"
      "v_exp_f32 v193, v125\n"
      "v_mfma_f32_16x16x32_bf16 v[44:47], a[116:119], v[132:135], v[44:47]\n"
      "s_and_b32 s62, s62, 0x1ffff\n"
      "v_exp_f32 v194, v126\n"
      "v_mfma_f32_16x16x32_bf16 v[40:43], a[116:119], v[128:131], v[40:43]\n"
      "v_exp_f32 v195, v127\n"
      "v_mfma_f32_16x16x32_bf16 v[48:51], a[120:123], v[128:131], v[48:51]\n"
      "v_cvt_pk_bf16_f32 v158, v192, v193\n"
      "v_cvt_pk_bf16_f32 v159, v194, v195\n"
      "v_mfma_f32_16x16x32_bf16 v[52:55], a[120:123], v[132:135], v[52:55]\n"
      "s_waitcnt lgkmcnt(0)\n"
      "v_mfma_f32_16x16x32_bf16 v[184:187], a[140:143], v[156:159], v[184:187]\n"
      "s_sub_u32 s67, s68, s66\n"
      "ds_read_b128 a[68:71], v242 offset:36864\n"
      "ds_read_b128 a[76:79], v243 offset:36864\n"
      "v_mfma_f32_16x16x32_bf16 a[192:195], a[120:123], v[136:139], a[192:195]\n"
      "s_cmp_gt_i32 s67, s70\n"
      "ds_read_b128 a[84:87], v242 offset:37888\n"
      "ds_read_b128 a[92:95], v243 offset:37888\n"
      "v_mfma_f32_16x16x32_bf16 a[196:199], a[120:123], v[140:143], a[196:199]\n"
      "s_cselect_b32 s69, s77, s86\n"
      "v_max3_f32 v196, v184, v185, v186\n"
      "v_max_f32 v196, v196, v187\n"
      "v_mfma_f32_16x16x32_bf16 a[204:207], a[124:127], v[140:143], a[204:207]\n"
      "s_cselect_b32 s70, 60, s70\n"
      "v_cmp_nge_f32 vcc, 0x4b800000, v196\n"
      "v_mfma_f32_16x16x32_bf16 a[200:203], a[124:127], v[136:139], a[200:203]\n"
      "s_cselect_b32 s90, s80, s90\n"
      "v_mfma_f32_16x16x32_bf16 v[60:63], a[124:127], v[132:135], v[60:63]\n"
      "s_add_u32 s66, s66, s69\n"
      "v_mfma_f32_16x16x32_bf16 v[56:59], a[124:127], v[128:131], v[56:59]\n"
      "s_mov_b32 s82, s68\n"
      "s_cbranch_vccnz FS1_%=\n"
      "FR1_%=:\n"
      "v_mfma_f32_16x16x32_bf16 v[96:99], a[64:67], a[0:3], v[160:163]\n"
      "s_add_u32 s65, s68, s90\n"
      "v_exp_f32 v188, v64\n"
      "v_mfma_f32_16x16x32_bf16 v[96:99], a[72:75], a[16:19], v[96:99]\n"
      "s_getreg_b32 s67, hwreg(HW_REG_IB_STS, 0, 4)\n"
      "v_add_f32 v180, v180, v184\n"
      "v_add_f32 v181, v181, v185\n"
      "v_mfma_f32_16x16x32_bf16 v[96:99], a[80:83], a[32:35], v[96:99]\n"
      "s_cmp_gt_u32 s67, 6\n"
      "v_exp_f32 v189, v65\n"
      "v_mfma_f32_16x16x32_bf16 v[96:99], a[88:91], a[48:51], v[96:99]\n"
      "s_cselect_b32 s67, s87, 0\n"
      "v_add_f32 v182, v182, v186\n"
      "v_add_f32 v183, v183, v187\n"
      "v_mfma_f32_16x16x32_bf16 v[104:107], a[64:67], a[4:7], v[164:167]\n"
      "s_max_i32 s67, s67, s55\n"
      "v_exp_f32 v190, v66\n"
      "v_mfma_f32_16x16x32_bf16 v[104:107], a[72:75], a[20:23], v[104:107]\n"
      "s_sub_u32 s55, s55, s67\n"
      "v_exp_f32 v191, v67\n"
      "v_mfma_f32_16x16x32_bf16 v[104:107], a[80:83], a[36:39], v[104:107]\n"
      "s_add_u32 s66, s66, s67\n"
      "v_cvt_pk_bf16_f32 v128, v188, v189\n"
      "v_cvt_pk_bf16_f32 v129, v190, v191\n"
      "v_mfma_f32_16x16x32_bf16 v[104:107], a[88:91], a[52:55], v[104:107]\n"
      "s_max_u32 s88, s66, s65\n"
      "v_exp_f32 v192, v68\n"
      "v_mfma_f32_16x16x32_bf16 v[112:115], a[64:67], a[8:11], v[168:171]\n"
      "s_add_u32 s39, s39, s39\n"
      "v_exp_f32 v193, v69\n"
      "v_mfma_f32_16x16x32_bf16 v[112:115], a[72:75], a[24:27], v[112:115]\n"
      "s_memrealtime s[68:69]\n"
      "v_exp_f32 v194, v70\n"
      "v_mfma_f32_16x16x32_bf16 v[112:115], a[80:83], a[40:43], v[112:115]\n"
      "s_mov_b32 m0, s62\n"
      "v_exp_f32 v195, v71\n"
      "v_mfma_f32_16x16x32_bf16 v[112:115], a[88:91], a[56:59], v[112:115]\n"
      "buffer_load_dwordx4 v248, s[56:59], s60 offen nt lds\n"
      "v_mfma_f32_16x16x32_bf16 v[120:123], a[64:67], a[12:15], v[172:175]\n"
      "buffer_load_dwordx4 v248, s[56:59], s60 offen offset:2048 nt lds\n"
      "v_mfma_f32_16x16x32_bf16 v[120:123], a[72:75], a[28:31], v[120:123]\n"
      "s_bitset1_b32 m0, 10\n"
      "v_exp_f32 v188, v72\n"
      "v_mfma_f32_16x16x32_bf16 v[120:123], a[80:83], a[44:47], v[120:123]\n"
      "buffer_load_dwordx4 v249, s[56:59], s60 offen nt lds\n"
      "v_mfma_f32_16x16x32_bf16 v[120:123], a[88:91], a[60:63], v[120:123]\n"
      "buffer_load_dwordx4 v249, s[56:59], s60 offen offset:2048 nt lds\n"
      "s_cbranch_scc1 SP3_%=\n"
      "HD3_%=:\n"
      "s_cmp_lg_u32 s54, 0\n"
      "s_waitcnt vmcnt(12) lgkmcnt(0)\n"
      "s_cbranch_scc1 W3_%=\n"
      "s_waitcnt vmcnt(8)\n"
      "W3_%=:\n"
      "s_sub_u32 s67, s68, s88\n"
      "s_cmp_lt_i32 s67, -8\n"
      "s_cbranch_scc1 GW6_%=\n"
      "GX6_%=:\n"
      "s_barrier\n"
      "v_mfma_f32_16x16x32_bf16 v[100:103], a[68:71], a[0:3], v[160:163]\n"
      "s_memrealtime s[68:69]\n"
      "ds_read_b64_tr_b16 a[96:97], v244 offset:32768\n"
      "ds_read_b64_tr_b16 a[98:99], v244 offset:36864\n"
      "v_mfma_f32_16x16x32_bf16 v[100:103], a[76:79], a[16:19], v[100:103]\n"
      "s_xor_b64 s[56:57], s[56:57], s[32:33]\n"
      "ds_read_b64_tr_b16 a[100:101], v245 offset:32768\n"
      "ds_read_b64_tr_b16 a[102:103], v245 offset:36864\n"
      "v_mfma_f32_16x16x32_bf16 v[100:103], a[84:87], a[32:35], v[100:103]\n"
      "s_bitset1_b32 s61, 15\n"
      "ds_read_b64_tr_b16 a[104:105], v246 offset:32768\n"
      "ds_read_b64_tr_b16 a[106:107], v246 offset:36864\n"
      "v_mfma_f32_16x16x32_bf16 v[100:103], a[92:95], a[48:51], v[100:103]\n"
      "s_add_u32 s61, s61, 0x2000\n"
      "ds_read_b64_tr_b16 a[108:109], v247 offset:32768\n"
      "ds_read_b64_tr_b16 a[110:111], v247 offset:36864\n"
      "v_mfma_f32_16x16x32_bf16 v[108:111], a[68:71], a[4:7], v[164:167]\n"
      "s_bitset0_b32 s61, 15\n"
      "v_exp_f32 v189, v73\n"
      "v_mfma_f32_16x16x32_bf16 v[108:111], a[76:79], a[20:23], v[108:111]\n"
      "s_xor_b32 s60, s61, s51\n"
      "v_cvt_pk_bf16_f32 v130, v192, v193\n"
      "v_cvt_pk_bf16_f32 v131, v194, v195\n"
      "v_mfma_f32_16x16x32_bf16 v[108:111], a[84:87], a[36:39], v[108:111]\n"
      "s_add_u32 s62, s62, 0x4000\n"
      "v_exp_f32 v190, v74\n"
      "v_mfma_f32_16x16x32_bf16 v[184:187], a[128:131], v[128:131], 0\n"
      "s_and_b32 s62, s62, 0x1ffff\n"
      "v_xor_b32 v242, 0x10000, v242\n"
      "v_xor_b32 v243, 0x10000, v243\n"
      "v_mfma_f32_16x16x32_bf16 v[108:111], a[92:95], a[52:55], v[108:111]\n"
      "v_exp_f32 v191, v75\n"
      "v_mfma_f32_16x16x32_bf16 v[116:119], a[68:71], a[8:11], v[168:171]\n"
      "v_cvt_pk_bf16_f32 v132, v188, v189\n"
      "v_cvt_pk_bf16_f32 v133, v190, v191\n"
      "v_mfma_f32_16x16x32_bf16 v[116:119], a[76:79], a[24:27], v[116:119]\n"
      "v_exp_f32 v192, v76\n"
      "s_waitcnt lgkmcnt(0)\n"
      "v_mfma_f32_16x16x32_bf16 v[116:119], a[84:87], a[40:43], v[116:119]\n"
      "s_sub_u32 s67, s68, s66\n"
      "ds_read_b64_tr_b16 a[112:113], v244 offset:33792\n"
      "ds_read_b64_tr_b16 a[114:115], v244 offset:37888\n"
      "v_mfma_f32_16x16x32_bf16 v[116:119], a[92:95], a[56:59], v[116:119]\n"
      "s_cmp_gt_i32 s67, s70\n"
      "v_exp_f32 v193, v77\n"
      "v_mfma_f32_16x16x32_bf16 v[124:127], a[68:71], a[12:15], v[172:175]\n"
      "s_cselect_b32 s69, s77, s86\n"
      "ds_read_b64_tr_b16 a[116:117], v245 offset:33792\n"
      "v_cvt_pk_bf16_f32 v134, v192, v193\n"
      "v_mfma_f32_16x16x32_bf16 v[124:127], a[76:79], a[28:31], v[124:127]\n"
      "s_cselect_b32 s70, 60, s70\n"
      "v_exp_f32 v194, v78\n"
      "v_mfma_f32_16x16x32_bf16 v[124:127], a[84:87], a[44:47], v[124:127]\n"
      "s_cselect_b32 s90, s80, s90\n"
      "ds_read_b64_tr_b16 a[118:119], v245 offset:37888\n"
      "ds_read_b64_tr_b16 a[120:121], v246 offset:33792\n"
      "v_mfma_f32_16x16x32_bf16 v[124:127], a[92:95], a[60:63], v[124:127]\n"
      "s_add_u32 s66, s66, s69\n"
      "v_exp_f32 v195, v79\n"
      "v_mfma_f32_16x16x32_bf16 v[0:3], a[96:99], v[144:147], v[0:3]\n"
      "s_mov_b32 s82, s68\n"
      "ds_read_b64_tr_b16 a[122:123], v246 offset:37888\n"
      "v_cvt_pk_bf16_f32 v135, v194, v195\n"
      "v_mfma_f32_16x16x32_bf16 v[4:7], a[96:99], v[148:151], v[4:7]\n"
      "s_add_u32 s65, s68, s90\n"
      "v_exp_f32 v188, v80\n"
      "v_mfma_f32_16x16x32_bf16 v[184:187], a[132:135], v[132:135], v[184:187]\n"
      "s_getreg_b32 s67, hwreg(HW_REG_IB_STS, 0, 4)\n"
      "ds_read_b64_tr_b16 a[124:125], v247 offset:33792\n"
      "ds_read_b64_tr_b16 a[126:127], v247 offset:37888\n"
      "v_mfma_f32_16x16x32_bf16 a[144:147], a[96:99], v[152:155], a[144:147]\n"
      "s_cmp_gt_u32 s67, 6\n"
      "v_exp_f32 v189, v81\n"
      "v_mfma_f32_16x16x32_bf16 a[148:151], a[96:99], v[156:159], a[148:151]\n"
      "s_cselect_b32 s67, s87, 0\n"
      "v_exp_f32 v190, v82\n"
      "v_mfma_f32_16x16x32_bf16 a[156:159], a[100:103], v[156:159], a[156:159]\n"
      "s_max_i32 s67, s67, s55\n"
      "v_exp_f32 v191, v83\n"
      "v_mfma_f32_16x16x32_bf16 a[152:155], a[100:103], v[152:155], a[152:155]\n"
      "s_sub_u32 s55, s55, s67\n"
      "v_cvt_pk_bf16_f32 v136, v188, v189\n"
      "v_cvt_pk_bf16_f32 v137, v190, v191\n"
      "v_mfma_f32_16x16x32_bf16 v[12:15], a[100:103], v[148:151], v[12:15]\n"
      "s_add_u32 s66, s66, s67\n"
      "v_exp_f32 v192, v84\n"
      "v_mfma_f32_16x16x32_bf16 v[8:11], a[100:103], v[144:147], v[8:11]\n"
      "s_max_u32 s88, s66, s65\n"
      "v_exp_f32 v193, v85\n"
      "v_mfma_f32_16x16x32_bf16 v[16:19], a[104:107], v[144:147], v[16:19]\n"
      "s_add_u32 s38, s38, s38\n"
      "v_exp_f32 v194, v86\n"
      "v_mfma_f32_16x16x32_bf16 v[20:23], a[104:107], v[148:151], v[20:23]\n"
      "s_memrealtime s[68:69]\n"
      "v_exp_f32 v195, v87\n"
      "v_mfma_f32_16x16x32_bf16 a[160:163], a[104:107], v[152:155], a[160:163]\n"
      "s_mov_b32 m0, s62\n"
      "v_cvt_pk_bf16_f32 v138, v192, v193\n"
      "v_cvt_pk_bf16_f32 v139, v194, v195\n"
      "v_mfma_f32_16x16x32_bf16 a[164:167], a[104:107], v[156:159], a[164:167]\n"
      "buffer_load_dwordx4 v248, s[56:59], s60 offen nt lds\n"
      "v_mfma_f32_16x16x32_bf16 v[184:187], a[136:139], v[136:139], v[184:187]\n"
      "buffer_load_dwordx4 v248, s[56:59], s60 offen offset:2048 nt lds\n"
      "v_mfma_f32_16x16x32_bf16 a[172:175], a[108:111], v[156:159], a[172:175]\n"
      "s_bitset1_b32 m0, 10\n"
      "v_exp_f32 v188, v88\n"
      "v_mfma_f32_16x16x32_bf16 a[168:171], a[108:111], v[152:155], a[168:171]\n"
      "buffer_load_dwordx4 v249, s[56:59], s60 offen nt lds\n"
      "v_mfma_f32_16x16x32_bf16 v[28:31], a[108:111], v[148:151], v[28:31]\n"
      "buffer_load_dwordx4 v249, s[56:59], s60 offen offset:2048 nt lds\n"
      "HD0_%=:\n"
      "s_cmp_lg_u32 s54, 0\n"
      "s_waitcnt vmcnt(12) lgkmcnt(0)\n"
      "s_cbranch_scc1 W4_%=\n"
      "s_waitcnt vmcnt(8)\n"
      "W4_%=:\n"
      "s_sub_u32 s67, s68, s88\n"
      "s_cmp_lt_i32 s67, -8\n"
      "s_cbranch_scc1 GW7_%=\n"
      "GX7_%=:\n"
      "s_barrier\n"
      "v_mfma_f32_16x16x32_bf16 v[24:27], a[108:111], v[144:147], v[24:27]\n"
      "s_memrealtime s[68:69]\n"
      "ds_read_b128 a[64:67], v242 offset:0\n"
      "ds_read_b128 a[72:75], v243 offset:0\n"
      "v_mfma_f32_16x16x32_bf16 v[32:35], a[112:115], v[144:147], v[32:35]\n"
      "s_xor_b64 s[56:57], s[56:57], s[32:33]\n"
      "ds_read_b128 a[80:83], v242 offset:1024\n"
      "ds_read_b128 a[88:91], v243 offset:1024\n"
      "v_mfma_f32_16x16x32_bf16 v[36:39], a[112:115], v[148:151], v[36:39]\n"
      "s_bitset1_b32 s61, 15\n"
      "v_exp_f32 v189, v89\n"
      "v_mfma_f32_16x16x32_bf16 a[176:179], a[112:115], v[152:155], a[176:179]\n"
      "s_add_u32 s61, s61, 0x2000\n"
      "v_exp_f32 v190, v90\n"
      "v_mfma_f32_16x16x32_bf16 a[180:183], a[112:115], v[156:159], a[180:183]\n"
      "s_bitset0_b32 s61, 15\n"
      "v_exp_f32 v191, v91\n"
      "v_mfma_f32_16x16x32_bf16 a[188:191], a[116:119], v[156:159], a[188:191]\n"
      "s_xor_b32 s60, s61, s51\n"
      "v_cvt_pk_bf16_f32 v140, v188, v189\n"
      "v_cvt_pk_bf16_f32 v141, v190, v191\n"
      "v_mfma_f32_16x16x32_bf16 a[184:187], a[116:119], v[152:155], a[184:187]\n"
      "s_add_u32 s62, s62, 0x4000\n"
      "v_exp_f32 v192, v92\n"
      "v_mfma_f32_16x16x32_bf16 v[44:47], a[116:119], v[148:151], v[44:47]\n"
      "s_and_b32 s62, s62, 0x1ffff\n"
      "v_exp_f32 v193, v93\n"
      "v_mfma_f32_16x16x32_bf16 v[40:43], a[116:119], v[144:147], v[40:43]\n"
      "v_exp_f32 v194, v94\n"
      "v_mfma_f32_16x16x32_bf16 v[48:51], a[120:123], v[144:147], v[48:51]\n"
      "v_exp_f32 v195, v95\n"
      "v_mfma_f32_16x16x32_bf16 v[52:55], a[120:123], v[148:151], v[52:55]\n"
      "v_cvt_pk_bf16_f32 v142, v192, v193\n"
      "v_cvt_pk_bf16_f32 v143, v194, v195\n"
      "s_waitcnt lgkmcnt(0)\n"
      "v_mfma_f32_16x16x32_bf16 a[192:195], a[120:123], v[152:155], a[192:195]\n"
      "s_sub_u32 s67, s68, s66\n"
      "v_xor_b32 v244, 0x10000, v244\n"
      "v_xor_b32 v245, 0x10000, v245\n"
      "v_mfma_f32_16x16x32_bf16 v[184:187], a[140:143], v[140:143], v[184:187]\n"
      "s_cmp_gt_i32 s67, s70\n"
      "ds_read_b128 a[68:71], v242 offset:4096\n"
      "ds_read_b128 a[76:79], v243 offset:4096\n"
      "v_mfma_f32_16x16x32_bf16 a[196:199], a[120:123], v[156:159], a[196:199]\n"
      "s_cselect_b32 s69, s77, s86\n"
      "ds_read_b128 a[84:87], v242 offset:5120\n"
      "ds_read_b128 a[92:95], v243 offset:5120\n"
      "v_mfma_f32_16x16x32_bf16 a[204:207], a[124:127], v[156:159], a[204:207]\n"
      "s_cselect_b32 s70, 60, s70\n"
      "v_xor_b32 v246, 0x10000, v246\n"
      "v_xor_b32 v247, 0x10000, v247\n"
      "v_mfma_f32_16x16x32_bf16 a[200:203], a[124:127], v[152:155], a[200:203]\n"
      "s_cselect_b32 s90, s80, s90\n"
      "v_max3_f32 v196, v184, v185, v186\n"
      "v_max_f32 v196, v196, v187\n"
      "v_mfma_f32_16x16x32_bf16 v[60:63], a[124:127], v[148:151], v[60:63]\n"
      "s_add_u32 s66, s66, s69\n"
      "v_cmp_nge_f32 vcc, 0x4b800000, v196\n"
      "v_mfma_f32_16x16x32_bf16 v[56:59], a[124:127], v[144:147], v[56:59]\n"
      "s_mov_b32 s82, s68\n"
      "s_cbranch_vccnz FS2_%=\n"
      "FR2_%=:\n"
      "s_branch LOOP_%=\n"
      "SP1_%=:\n"
      "s_ff1_i32_b32 s64, s39\n"
      "s_lshl1_add_u32 s64, s64, 1\n"
      "s_bitcmp1_b64 s[52:53], s64\n"
      "s_cbranch_scc0 SH1_%=\n"
      "s_swappc_b64 s[84:85], s[84:85]\n"
      "SH1_%=:\n"
      "s_cmp_lt_u32 s64, 63\n"
      "s_cbranch_scc1 HD1_%=\n"
      "s_xor_b64 s[56:57], s[56:57], s[32:33]\n"
      "s_bitset1_b32 s61, 15\n"
      "s_add_u32 s61, s61, 0x2000\n"
      "s_bitset0_b32 s61, 15\n"
      "s_xor_b32 s60, s61, s51\n"
      "s_add_u32 s62, s62, 0x4000\n"
      "s_and_b32 s62, s62, 0x1ffff\n"
      "v_add_u32 v248, s51, v249\n"
      "s_mov_b32 s51, 0\n"
      "v_xor_b32 v249, 0x4000, v248\n"
      "s_branch HD1_%=\n"
      "SP2_%=:\n"
      "s_cmp_eq_u32 s38, 0\n"
      "s_cbranch_scc1 L62_%=\n"
      "s_ff1_i32_b32 s64, s38\n"
      "s_lshl1_add_u32 s64, s64, 0\n"
      "s_bitcmp1_b64 s[52:53], s64\n"
      "s_cbranch_scc0 SH2_%=\n"
      "s_swappc_b64 s[84:85], s[84:85]\n"
      "SH2_%=:\n"
      "s_branch HD2_%=\n"
      "L62_%=:\n"
      "s_xor_b64 s[56:57], s[56:57], s[32:33]\n"
      "s_sub_u32 s61, s61, 0x2000\n"
      "v_xor_b32 v248, 128, v248\n"
      "v_xor_b32 v249, 128, v249\n"
      "s_mov_b32 s38, 0x80000000\n"
      "s_branch HD2_%=\n"
      "SP3_%=:\n"
      "s_cmp_eq_u32 s39, 0\n"
      "s_cbranch_scc1 NH_%=\n"
      "s_ff1_i32_b32 s64, s39\n"
      "s_lshl1_add_u32 s64, s64, 1\n"
      "s_bitcmp1_b64 s[52:53], s64\n"
      "s_cbranch_scc0 SH3_%=\n"
      "s_swappc_b64 s[84:85], s[84:85]\n"
      "SH3_%=:\n"
      "s_bitcmp1_b64 s[34:35], s64\n"
      "s_cbranch_scc0 HD3_%=\n"
      "NH_%=:\n"
      "s_cmp_lg_u32 s54, 0\n"
      "s_waitcnt vmcnt(12) lgkmcnt(0)\n"
      "s_cbranch_scc1 W5_%=\n"
      "s_waitcnt vmcnt(8)\n"
      "W5_%=:\n"
      "s_barrier\n"
      "v_mfma_f32_16x16x32_bf16 v[100:103], a[68:71], a[0:3], v[160:163]\n"
      "s_sub_u32 s54, s54, 1\n"
      "ds_read_b64_tr_b16 a[96:97], v244 offset:32768\n"
      "ds_read_b64_tr_b16 a[98:99], v244 offset:36864\n"
      "v_mfma_f32_16x16x32_bf16 v[100:103], a[76:79], a[16:19], v[100:103]\n"
      "ds_read_b64_tr_b16 a[100:101], v245 offset:32768\n"
      "ds_read_b64_tr_b16 a[102:103], v245 offset:36864\n"
      "v_mfma_f32_16x16x32_bf16 v[100:103], a[84:87], a[32:35], v[100:103]\n"
      "ds_read_b64_tr_b16 a[104:105], v246 offset:32768\n"
      "ds_read_b64_tr_b16 a[106:107], v246 offset:36864\n"
      "v_mfma_f32_16x16x32_bf16 v[100:103], a[92:95], a[48:51], v[100:103]\n"
      "ds_read_b64_tr_b16 a[108:109], v247 offset:32768\n"
      "ds_read_b64_tr_b16 a[110:111], v247 offset:36864\n"
      "v_mfma_f32_16x16x32_bf16 v[108:111], a[68:71], a[4:7], v[164:167]\n"
      "v_exp_f32 v189, v73\n"
      "v_mfma_f32_16x16x32_bf16 v[108:111], a[76:79], a[20:23], v[108:111]\n"
      "v_cvt_pk_bf16_f32 v130, v192, v193\n"
      "v_cvt_pk_bf16_f32 v131, v194, v195\n"
      "v_mfma_f32_16x16x32_bf16 v[108:111], a[84:87], a[36:39], v[108:111]\n"
      "v_exp_f32 v190, v74\n"
      "v_mfma_f32_16x16x32_bf16 v[184:187], a[128:131], v[128:131], 0\n"
      "v_xor_b32 v242, 0x10000, v242\n"
      "v_xor_b32 v243, 0x10000, v243\n"
      "v_mfma_f32_16x16x32_bf16 v[108:111], a[92:95], a[52:55], v[108:111]\n"
      "v_exp_f32 v191, v75\n"
      "v_mfma_f32_16x16x32_bf16 v[116:119], a[68:71], a[8:11], v[168:171]\n"
      "v_cvt_pk_bf16_f32 v132, v188, v189\n"
      "v_cvt_pk_bf16_f32 v133, v190, v191\n"
      "v_mfma_f32_16x16x32_bf16 v[116:119], a[76:79], a[24:27], v[116:119]\n"
      "v_exp_f32 v192, v76\n"
      "s_waitcnt lgkmcnt(0)\n"
      "v_mfma_f32_16x16x32_bf16 v[116:119], a[84:87], a[40:43], v[116:119]\n"
      "ds_read_b64_tr_b16 a[112:113], v244 offset:33792\n"
      "ds_read_b64_tr_b16 a[114:115], v244 offset:37888\n"
      "v_mfma_f32_16x16x32_bf16 v[116:119], a[92:95], a[56:59], v[116:119]\n"
      "v_exp_f32 v193, v77\n"
      "v_mfma_f32_16x16x32_bf16 v[124:127], a[68:71], a[12:15], v[172:175]\n"
      "ds_read_b64_tr_b16 a[116:117], v245 offset:33792\n"
      "v_cvt_pk_bf16_f32 v134, v192, v193\n"
      "v_mfma_f32_16x16x32_bf16 v[124:127], a[76:79], a[28:31], v[124:127]\n"
      "v_exp_f32 v194, v78\n"
      "v_mfma_f32_16x16x32_bf16 v[124:127], a[84:87], a[44:47], v[124:127]\n"
      "ds_read_b64_tr_b16 a[118:119], v245 offset:37888\n"
      "ds_read_b64_tr_b16 a[120:121], v246 offset:33792\n"
      "v_mfma_f32_16x16x32_bf16 v[124:127], a[92:95], a[60:63], v[124:127]\n"
      "v_exp_f32 v195, v79\n"
      "v_mfma_f32_16x16x32_bf16 v[0:3], a[96:99], v[144:147], v[0:3]\n"
      "ds_read_b64_tr_b16 a[122:123], v246 offset:37888\n"
      "v_cvt_pk_bf16_f32 v135, v194, v195\n"
      "v_mfma_f32_16x16x32_bf16 v[4:7], a[96:99], v[148:151], v[4:7]\n"
      "v_exp_f32 v188, v80\n"
      "v_mfma_f32_16x16x32_bf16 v[184:187], a[132:135], v[132:135], v[184:187]\n"
      "ds_read_b64_tr_b16 a[124:125], v247 offset:33792\n"
      "ds_read_b64_tr_b16 a[126:127], v247 offset:37888\n"
      "v_mfma_f32_16x16x32_bf16 a[144:147], a[96:99], v[152:155], a[144:147]\n"
      "v_exp_f32 v189, v81\n"
      "v_mfma_f32_16x16x32_bf16 a[148:151], a[96:99], v[156:159], a[148:151]\n"
      "v_exp_f32 v190, v82\n"
      "v_mfma_f32_16x16x32_bf16 a[156:159], a[100:103], v[156:159], a[156:159]\n"
      "v_exp_f32 v191, v83\n"
      "v_mfma_f32_16x16x32_bf16 a[152:155], a[100:103], v[152:155], a[152:155]\n"
      "v_cvt_pk_bf16_f32 v136, v188, v189\n"
      "v_cvt_pk_bf16_f32 v137, v190, v191\n"
      "v_mfma_f32_16x16x32_bf16 v[12:15], a[100:103], v[148:151], v[12:15]\n"
      "v_exp_f32 v192, v84\n"
      "v_mfma_f32_16x16x32_bf16 v[8:11], a[100:103], v[144:147], v[8:11]\n"
      "v_exp_f32 v193, v85\n"
      "v_mfma_f32_16x16x32_bf16 v[16:19], a[104:107], v[144:147], v[16:19]\n"
      "s_add_u32 s38, s38, s38\n"
      "v_exp_f32 v194, v86\n"
      "v_mfma_f32_16x16x32_bf16 v[20:23], a[104:107], v[148:151], v[20:23]\n"
      "s_memrealtime s[68:69]\n"
      "v_exp_f32 v195, v87\n"
      "v_mfma_f32_16x16x32_bf16 a[160:163], a[104:107], v[152:155], a[160:163]\n"
      "v_cvt_pk_bf16_f32 v138, v192, v193\n"
      "v_cvt_pk_bf16_f32 v139, v194, v195\n"
      "v_mfma_f32_16x16x32_bf16 a[164:167], a[104:107], v[156:159], a[164:167]\n"
      "s_nop 0\n"
      "v_mfma_f32_16x16x32_bf16 v[184:187], a[136:139], v[136:139], v[184:187]\n"
      "s_nop 0\n"
      "v_mfma_f32_16x16x32_bf16 a[172:175], a[108:111], v[156:159], a[172:175]\n"
      "v_exp_f32 v188, v88\n"
      "v_mfma_f32_16x16x32_bf16 a[168:171], a[108:111], v[152:155], a[168:171]\n"
      "s_nop 0\n"
      "v_mfma_f32_16x16x32_bf16 v[28:31], a[108:111], v[148:151], v[28:31]\n"
      "s_nop 0\n"
      "s_cbranch_scc0 MH_%=\n"
      "END_%=:\n"
      "s_waitcnt lgkmcnt(0)\n"
      "v_mfma_f32_16x16x32_bf16 v[24:27], a[108:111], v[144:147], v[24:27]\n"
      "v_mov_b32 v236, s68\n"
      "v_exp_f32 v189, v89\n"
      "v_mfma_f32_16x16x32_bf16 v[32:35], a[112:115], v[144:147], v[32:35]\n"
      "s_mov_b64 exec, 1\n"
      "global_store_dwordx2 v238, v[236:237], s[46:47] offset:8 sc1\n"
      "s_mov_b64 exec, -1\n"
      "v_mfma_f32_16x16x32_bf16 v[36:39], a[112:115], v[148:151], v[36:39]\n"
      "s_mov_b32 s82, s68\n"
      "v_exp_f32 v190, v90\n"
      "v_mfma_f32_16x16x32_bf16 a[176:179], a[112:115], v[152:155], a[176:179]\n"
      "v_exp_f32 v191, v91\n"
      "v_mfma_f32_16x16x32_bf16 a[180:183], a[112:115], v[156:159], a[180:183]\n"
      "v_cvt_pk_bf16_f32 v140, v188, v189\n"
      "v_cvt_pk_bf16_f32 v141, v190, v191\n"
      "v_mfma_f32_16x16x32_bf16 a[188:191], a[116:119], v[156:159], a[188:191]\n"
      "v_exp_f32 v192, v92\n"
      "s_waitcnt vmcnt(5)\n"
      "s_barrier\n"
      "v_mfma_f32_16x16x32_bf16 a[184:187], a[116:119], v[152:155], a[184:187]\n"
      "ds_read_b64_tr_b16 a[80:81], v224 offset:0\n"
      "ds_read_b64_tr_b16 a[82:83], v224 offset:4096\n"
      "v_mfma_f32_16x16x32_bf16 v[44:47], a[116:119], v[148:151], v[44:47]\n"
      "ds_read_b64_tr_b16 a[84:85], v225 offset:0\n"
      "ds_read_b64_tr_b16 a[86:87], v225 offset:4096\n"
      "v_mfma_f32_16x16x32_bf16 v[40:43], a[116:119], v[144:147], v[40:43]\n"
      "s_mov_b32 s74, 0xffff0000\n"
      "ds_read_b64_tr_b16 a[88:89], v226 offset:0\n"
      "ds_read_b64_tr_b16 a[90:91], v226 offset:4096\n"
      "v_mfma_f32_16x16x32_bf16 v[48:51], a[120:123], v[144:147], v[48:51]\n"
      "s_mov_b32 s75, 0xffff0000\n"
      "ds_read_b64_tr_b16 a[92:93], v227 offset:0\n"
      "ds_read_b64_tr_b16 a[94:95], v227 offset:4096\n"
      "v_mfma_f32_16x16x32_bf16 v[52:55], a[120:123], v[148:151], v[52:55]\n"
      "s_mov_b32 s94, 0\n"
      "v_exp_f32 v193, v93\n"
      "v_mfma_f32_16x16x32_bf16 a[192:195], a[120:123], v[152:155], a[192:195]\n"
      "s_mov_b32 s95, -1\n"
      "ds_read_b64_tr_b16 a[16:17], v224 offset:1024\n"
      "v_cvt_pk_bf16_f32 v142, v192, v193\n"
      "v_mfma_f32_16x16x32_bf16 a[196:199], a[120:123], v[156:159], a[196:199]\n"
      "v_exp_f32 v194, v94\n"
      "v_mfma_f32_16x16x32_bf16 a[204:207], a[124:127], v[156:159], a[204:207]\n"
      "ds_read_b64_tr_b16 a[18:19], v224 offset:5120\n"
      "ds_read_b64_tr_b16 a[20:21], v225 offset:1024\n"
      "v_mfma_f32_16x16x32_bf16 a[200:203], a[124:127], v[152:155], a[200:203]\n"
      "v_exp_f32 v195, v95\n"
      "v_mfma_f32_16x16x32_bf16 v[60:63], a[124:127], v[148:151], v[60:63]\n"
      "ds_read_b64_tr_b16 a[22:23], v225 offset:5120\n"
      "v_cvt_pk_bf16_f32 v143, v194, v195\n"
      "v_mfma_f32_16x16x32_bf16 v[56:59], a[124:127], v[144:147], v[56:59]\n"
      "ds_read_b64_tr_b16 a[24:25], v226 offset:1024\n"
      "ds_read_b64_tr_b16 a[26:27], v226 offset:5120\n"
      "v_mfma_f32_16x16x32_bf16 v[184:187], a[140:143], v[140:143], v[184:187]\n"
      "ds_read_b64_tr_b16 a[28:29], v227 offset:1024\n"
      "ds_read_b64_tr_b16 a[30:31], v227 offset:5120\n"
      "ds_read_b64_tr_b16 a[0:1], v220 offset:1024\n"
      "ds_read_b64_tr_b16 a[2:3], v220 offset:5120\n"
      "ds_read_b64_tr_b16 a[4:5], v221 offset:1024\n"
      "ds_read_b64_tr_b16 a[6:7], v221 offset:5120\n"
      "ds_read_b64_tr_b16 a[8:9], v222 offset:1024\n"
      "ds_read_b64_tr_b16 a[10:11], v222 offset:5120\n"
      "ds_read_b64_tr_b16 a[12:13], v223 offset:1024\n"
      "ds_read_b64_tr_b16 a[14:15], v223 offset:5120\n"
      "v_max3_f32 v196, v184, v185, v186\n"
      "v_max_f32 v196, v196, v187\n"
      "v_cmp_nge_f32 vcc, 0x4b800000, v196\n"
      "s_cbranch_vccnz FS3_%=\n"
      "FR3_%=:\n"
      "s_waitcnt lgkmcnt(15)\n"
      "v_mfma_f32_16x16x32_bf16 a[176:179], a[80:83], v[136:139], a[176:179]\n"
      "v_add_f32 v180, v180, v184\n"
      "v_add_f32 v181, v181, v185\n"
      "v_mfma_f32_16x16x32_bf16 a[180:183], a[80:83], v[140:143], a[180:183]\n"
      "v_add_f32 v182, v182, v186\n"
      "v_add_f32 v183, v183, v187\n"
      "v_mfma_f32_16x16x32_bf16 a[184:187], a[84:87], v[136:139], a[184:187]\n"
      "v_exp_f32 v188, v112\n"
      "v_mfma_f32_16x16x32_bf16 a[188:191], a[84:87], v[140:143], a[188:191]\n"
      "ds_write_b128 v232, a[176:179] offset:0\n"
      "v_exp_f32 v189, v113\n"
      "v_mfma_f32_16x16x32_bf16 a[192:195], a[88:91], v[136:139], a[192:195]\n"
      "v_exp_f32 v190, v114\n"
      "v_mfma_f32_16x16x32_bf16 a[196:199], a[88:91], v[140:143], a[196:199]\n"
      "v_exp_f32 v191, v115\n"
      "v_mfma_f32_16x16x32_bf16 a[200:203], a[92:95], v[136:139], a[200:203]\n"
      "ds_write_b128 v232, a[180:183] offset:1024\n"
      "v_exp_f32 v192, v116\n"
      "v_mfma_f32_16x16x32_bf16 a[204:207], a[92:95], v[140:143], a[204:207]\n"
      "v_exp_f32 v193, v117\n"
      "v_exp_f32 v194, v118\n"
      "v_cvt_pk_bf16_f32 v152, v188, v189\n"
      "v_exp_f32 v195, v119\n"
      "v_cvt_pk_bf16_f32 v153, v190, v191\n"
      "ds_write_b128 v232, a[184:187] offset:2048\n"
      "v_exp_f32 v188, v120\n"
      "v_cvt_pk_bf16_f32 v154, v192, v193\n"
      "v_mfma_f32_16x16x32_bf16 v[32:35], a[80:83], v[128:131], v[32:35]\n"
      "v_exp_f32 v189, v121\n"
      "v_cvt_pk_bf16_f32 v155, v194, v195\n"
      "v_exp_f32 v190, v122\n"
      "v_cvt_pk_bf16_f32 v156, v188, v189\n"
      "v_mfma_f32_16x16x32_bf16 v[184:187], a[136:139], v[152:155], 0\n"
      "ds_write_b128 v234, v[152:155] offset:0\n"
      "v_exp_f32 v191, v123\n"
      "v_mfma_f32_16x16x32_bf16 v[36:39], a[80:83], v[132:135], v[36:39]\n"
      "v_exp_f32 v192, v124\n"
      "v_cvt_pk_bf16_f32 v157, v190, v191\n"
      "v_exp_f32 v193, v125\n"
      "v_exp_f32 v194, v126\n"
      "v_cvt_pk_bf16_f32 v158, v192, v193\n"
      "ds_write_b128 v232, a[188:191] offset:3072\n"
      "v_mfma_f32_16x16x32_bf16 v[40:43], a[84:87], v[128:131], v[40:43]\n"
      "v_exp_f32 v195, v127\n"
      "v_exp_f32 v188, v96\n"
      "v_cvt_pk_bf16_f32 v159, v194, v195\n"
      "v_exp_f32 v189, v97\n"
      "v_mfma_f32_16x16x32_bf16 v[44:47], a[84:87], v[132:135], v[44:47]\n"
      "v_exp_f32 v190, v98\n"
      "ds_write_b128 v234, v[156:159] offset:1024\n"
      "v_cvt_pk_bf16_f32 v144, v188, v189\n"
      "v_mfma_f32_16x16x32_bf16 v[184:187], a[140:143], v[156:159], v[184:187]\n"
      "v_exp_f32 v191, v99\n"
      "v_exp_f32 v192, v100\n"
      "v_cvt_pk_bf16_f32 v145, v190, v191\n"
      "v_mfma_f32_16x16x32_bf16 v[48:51], a[88:91], v[128:131], v[48:51]\n"
      "v_exp_f32 v193, v101\n"
      "ds_write_b128 v232, a[192:195] offset:4096\n"
      "v_exp_f32 v194, v102\n"
      "v_cvt_pk_bf16_f32 v146, v192, v193\n"
      "v_exp_f32 v195, v103\n"
      "v_mfma_f32_16x16x32_bf16 v[52:55], a[88:91], v[132:135], v[52:55]\n"
      "v_exp_f32 v188, v104\n"
      "v_cvt_pk_bf16_f32 v147, v194, v195\n"
      "v_exp_f32 v189, v105\n"
      "ds_write_b128 v232, a[196:199] offset:5120\n"
      "v_exp_f32 v190, v106\n"
      "v_cvt_pk_bf16_f32 v148, v188, v189\n"
      "v_mfma_f32_16x16x32_bf16 v[184:187], a[128:131], v[144:147], v[184:187]\n"
      "v_exp_f32 v191, v107\n"
      "v_exp_f32 v192, v108\n"
      "v_cvt_pk_bf16_f32 v149, v190, v191\n"
      "v_exp_f32 v193, v109\n"
      "ds_write_b128 v232, a[200:203] offset:6144\n"
      "v_mfma_f32_16x16x32_bf16 v[56:59], a[92:95], v[128:131], v[56:59]\n"
      "v_exp_f32 v194, v110\n"
      "v_cvt_pk_bf16_f32 v150, v192, v193\n"
      "v_exp_f32 v195, v111\n"
      "v_cndmask_b32 v229, v180, v181, s[74:75]\n"
      "v_cvt_pk_bf16_f32 v151, v194, v195\n"
      "v_mfma_f32_16x16x32_bf16 v[60:63], a[92:95], v[132:135], v[60:63]\n"
      "v_cndmask_b32 v200, v182, v183, s[74:75]\n"
      "ds_write_b128 v232, a[204:207] offset:7168\n"
      "v_cndmask_b32 v228, v176, v177, s[74:75]\n"
      "v_mfma_f32_16x16x32_bf16 v[184:187], a[132:135], v[148:151], v[184:187]\n"
      "s_nop 7\n"
      "v_max3_f32 v196, v184, v185, v186\n"
      "v_max_f32 v196, v196, v187\n"
      "v_cmp_nge_f32 vcc, 0x4b800000, v196\n"
      "s_cbranch_vccnz FS8_%=\n"
      "FR8_%=:\n"
      "v_cndmask_b32 v202, v186, v187, s[74:75]\n"
      "v_cndmask_b32 v201, v184, v185, s[74:75]\n"
      "v_add_f32 v200, v200, v202\n"
      "ds_write_b32 v234, v200 offset:4096\n"
      "v_add_f32 v229, v229, v201\n"
      "s_waitcnt lgkmcnt(8)\n"
      "v_mfma_f32_16x16x32_bf16 v[32:35], a[16:19], v[144:147], v[32:35]\n"
      "v_mfma_f32_16x16x32_bf16 v[36:39], a[16:19], v[148:151], v[36:39]\n"
      "v_mfma_f32_16x16x32_bf16 v[40:43], a[20:23], v[144:147], v[40:43]\n"
      "s_waitcnt lgkmcnt(0)\n"
      "s_barrier\n"
      "v_mfma_f32_16x16x32_bf16 v[44:47], a[20:23], v[148:151], v[44:47]\n"
      "ds_read_b32 v208, v209\n"
      "ds_read_b128 v[160:163], v235 offset:0\n"
      "v_mfma_f32_16x16x32_bf16 v[48:51], a[24:27], v[144:147], v[48:51]\n"
      "ds_read_b128 v[164:167], v235 offset:1024\n"
      "ds_read_b128 v[64:67], v233 offset:0\n"
      "v_mfma_f32_16x16x32_bf16 v[52:55], a[24:27], v[148:151], v[52:55]\n"
      "ds_read_b128 v[68:71], v233 offset:1024\n"
      "ds_read_b128 v[72:75], v233 offset:2048\n"
      "ds_read_b128 v[76:79], v233 offset:3072\n"
      "v_mfma_f32_16x16x32_bf16 v[56:59], a[28:31], v[144:147], v[56:59]\n"
      "ds_read_b128 v[80:83], v233 offset:4096\n"
      "ds_read_b128 v[84:87], v233 offset:5120\n"
      "v_mfma_f32_16x16x32_bf16 v[60:63], a[28:31], v[148:151], v[60:63]\n"
      "ds_read_b128 v[88:91], v233 offset:6144\n"
      "ds_read_b128 v[92:95], v233 offset:7168\n"
      "ds_read_b32 v204, v235 offset:4096\n"
      "s_waitcnt lgkmcnt(11)\n"
      "v_cmp_ne_u32 vcc, 0, v208\n"
      "s_cbranch_vccnz SLOW_%=\n"
      "AGAIN_%=:\n"
      "s_waitcnt lgkmcnt(9)\n"
      "v_mfma_f32_16x16x32_bf16 v[32:35], a[0:3], v[160:163], v[32:35]\n"
      "v_mfma_f32_16x16x32_bf16 v[36:39], a[0:3], v[164:167], v[36:39]\n"
      "v_mfma_f32_16x16x32_bf16 v[40:43], a[4:7], v[160:163], v[40:43]\n"
      "v_mfma_f32_16x16x32_bf16 v[44:47], a[4:7], v[164:167], v[44:47]\n"
      "v_mfma_f32_16x16x32_bf16 v[48:51], a[8:11], v[160:163], v[48:51]\n"
      "v_mfma_f32_16x16x32_bf16 v[52:55], a[8:11], v[164:167], v[52:55]\n"
      "v_mfma_f32_16x16x32_bf16 v[56:59], a[12:15], v[160:163], v[56:59]\n"
      "v_mfma_f32_16x16x32_bf16 v[60:63], a[12:15], v[164:167], v[60:63]\n"
      "global_load_dwordx2 v[206:207], v239, s[46:47] offset:8 sc1\n"
      "s_waitcnt lgkmcnt(7)\n"
      "v_pk_add_f32 v[32:33], v[32:33], v[64:65]\n"
      "v_pk_add_f32 v[34:35], v[34:35], v[66:67]\n"
      "v_pk_add_f32 v[36:37], v[36:37], v[68:69]\n"
      "v_pk_add_f32 v[38:39], v[38:39], v[70:71]\n"
      "v_cvt_pk_bf16_f32 v212, v32, v33\n"
      "v_cvt_pk_bf16_f32 v213, v34, v35\n"
      "v_cvt_pk_bf16_f32 v214, v36, v37\n"
      "v_cvt_pk_bf16_f32 v215, v38, v39\n"
      "buffer_store_dwordx4 v[212:215], v241, s[24:27], s78 offen offset:0 sc1\n"
      "s_waitcnt lgkmcnt(5)\n"
      "v_pk_add_f32 v[40:41], v[40:41], v[72:73]\n"
      "v_pk_add_f32 v[42:43], v[42:43], v[74:75]\n"
      "v_pk_add_f32 v[44:45], v[44:45], v[76:77]\n"
      "v_pk_add_f32 v[46:47], v[46:47], v[78:79]\n"
      "v_cvt_pk_bf16_f32 v216, v40, v41\n"
      "v_cvt_pk_bf16_f32 v217, v42, v43\n"
      "v_cvt_pk_bf16_f32 v218, v44, v45\n"
      "v_cvt_pk_bf16_f32 v219, v46, v47\n"
      "buffer_store_dwordx4 v[216:219], v241, s[24:27], s78 offen offset:1024 sc1\n"
      "s_waitcnt lgkmcnt(3)\n"
      "v_pk_add_f32 v[48:49], v[48:49], v[80:81]\n"
      "v_pk_add_f32 v[50:51], v[50:51], v[82:83]\n"
      "v_pk_add_f32 v[52:53], v[52:53], v[84:85]\n"
      "v_pk_add_f32 v[54:55], v[54:55], v[86:87]\n"
      "v_cvt_pk_bf16_f32 v220, v48, v49\n"
      "v_cvt_pk_bf16_f32 v221, v50, v51\n"
      "v_cvt_pk_bf16_f32 v222, v52, v53\n"
      "v_cvt_pk_bf16_f32 v223, v54, v55\n"
      "buffer_store_dwordx4 v[220:223], v241, s[24:27], s78 offen offset:2048 sc1\n"
      "s_waitcnt lgkmcnt(0)\n"
      "v_pk_add_f32 v[56:57], v[56:57], v[88:89]\n"
      "v_pk_add_f32 v[58:59], v[58:59], v[90:91]\n"
      "v_pk_add_f32 v[60:61], v[60:61], v[92:93]\n"
      "v_pk_add_f32 v[62:63], v[62:63], v[94:95]\n"
      "v_cvt_pk_bf16_f32 v224, v56, v57\n"
      "v_cvt_pk_bf16_f32 v225, v58, v59\n"
      "v_cvt_pk_bf16_f32 v226, v60, v61\n"
      "v_cvt_pk_bf16_f32 v227, v62, v63\n"
      "buffer_store_dwordx4 v[224:227], v241, s[24:27], s78 offen offset:3072 sc1\n"
      "v_add_f32 v229, v229, v204\n"
      "v_cndmask_b32 v230, v228, v229, s[94:95]\n"
      "v_add_u32 v231, s40, v230\n"
      "v_add3_u32 v231, v231, v212, v213\n"
      "v_add3_u32 v231, v231, v214, v215\n"
      "v_add3_u32 v231, v231, v216, v217\n"
      "v_add3_u32 v231, v231, v218, v219\n"
      "v_add3_u32 v231, v231, v220, v221\n"
      "v_add3_u32 v231, v231, v222, v223\n"
      "v_add3_u32 v231, v231, v224, v225\n"
      "v_add3_u32 v231, v231, v226, v227\n"
      "buffer_store_dwordx2 v[230:231], v240, s[24:27], s78 offen sc1\n"
      "v_xor_b32 v244, 0x10000, v244\n"
      "v_xor_b32 v245, 0x10000, v245\n"
      "v_xor_b32 v246, 0x10000, v246\n"
      "v_xor_b32 v247, 0x10000, v247\n"
      "s_waitcnt vmcnt(7)\n"
      "s_barrier\n"
      "ds_read_b64_tr_b16 a[64:65], v244 offset:16384\n"
      "ds_read_b64_tr_b16 a[66:67], v244 offset:20480\n"
      "ds_read_b64_tr_b16 a[68:69], v245 offset:16384\n"
      "ds_read_b64_tr_b16 a[70:71], v245 offset:20480\n"
      "ds_read_b64_tr_b16 a[72:73], v246 offset:16384\n"
      "ds_read_b64_tr_b16 a[74:75], v246 offset:20480\n"
      "ds_read_b64_tr_b16 a[76:77], v247 offset:16384\n"
      "ds_read_b64_tr_b16 a[78:79], v247 offset:20480\n"
      "s_waitcnt lgkmcnt(4)\n"
      "v_mfma_f32_16x16x32_bf16 a[144:147], a[64:67], v[136:139], a[144:147]\n"
      "ds_read_b64_tr_b16 a[96:97], v244 offset:17408\n"
      "ds_read_b64_tr_b16 a[98:99], v244 offset:21504\n"
      "v_mfma_f32_16x16x32_bf16 a[148:151], a[64:67], v[140:143], a[148:151]\n"
      "ds_read_b64_tr_b16 a[100:101], v245 offset:17408\n"
      "ds_read_b64_tr_b16 a[102:103], v245 offset:21504\n"
      "v_mfma_f32_16x16x32_bf16 a[152:155], a[68:71], v[136:139], a[152:155]\n"
      "ds_read_b64_tr_b16 a[104:105], v246 offset:17408\n"
      "ds_read_b64_tr_b16 a[106:107], v246 offset:21504\n"
      "v_mfma_f32_16x16x32_bf16 a[156:159], a[68:71], v[140:143], a[156:159]\n"
      "ds_read_b64_tr_b16 a[108:109], v247 offset:17408\n"
      "ds_read_b64_tr_b16 a[110:111], v247 offset:21504\n"
      "s_waitcnt lgkmcnt(8)\n"
      "v_mfma_f32_16x16x32_bf16 a[160:163], a[72:75], v[136:139], a[160:163]\n"
      "s_sub_u32 s76, 18432, s78\n"
      "v_mfma_f32_16x16x32_bf16 a[164:167], a[72:75], v[140:143], a[164:167]\n"
      "s_mov_b32 s63, 0x40000\n"
      "v_mfma_f32_16x16x32_bf16 a[168:171], a[76:79], v[136:139], a[168:171]\n"
      "v_mov_b32 v198, 0\n"
      "v_mfma_f32_16x16x32_bf16 a[172:175], a[76:79], v[140:143], a[172:175]\n"
      "v_and_b32 v197, 0xf0, v241\n"
      "s_waitcnt lgkmcnt(0)\n"
      "v_mfma_f32_16x16x32_bf16 a[144:147], a[96:99], v[152:155], a[144:147]\n"
      "v_lshrrev_b32 v196, 3, v241\n"
      "v_mfma_f32_16x16x32_bf16 a[148:151], a[96:99], v[156:159], a[148:151]\n"
      "v_and_b32 v196, 32, v196\n"
      "v_mfma_f32_16x16x32_bf16 a[152:155], a[100:103], v[152:155], a[152:155]\n"
      "v_lshl_or_b32 v196, v197, 4, v196\n"
      "v_mfma_f32_16x16x32_bf16 a[156:159], a[100:103], v[156:159], a[156:159]\n"
      "v_lshrrev_b32 v199, 5, v241\n"
      "v_mfma_f32_16x16x32_bf16 a[160:163], a[104:107], v[152:155], a[160:163]\n"
      "v_and_or_b32 v196, v199, 16, v196\n"
      "v_mfma_f32_16x16x32_bf16 a[164:167], a[104:107], v[156:159], a[164:167]\n"
      "v_mov_b32 v191, s40\n"
      "v_mfma_f32_16x16x32_bf16 a[168:171], a[108:111], v[152:155], a[168:171]\n"
      "v_mfma_f32_16x16x32_bf16 a[172:175], a[108:111], v[156:159], a[172:175]\n"
      "s_waitcnt vmcnt(0)\n"
      "v_subrev_u32 v205, s82, v206\n"
      "v_cmp_ne_u32 vcc, s40, v207\n"
      "s_cbranch_vccnz LAG_%=\n"
      "v_cmp_lt_i32 vcc, 16, v205\n"
      "s_cbranch_vccnz LAG_%=\n"
      "READ_%=:\n"
      "buffer_load_dwordx2 v[160:161], v240, s[24:27], s76 offen sc1\n"
      "buffer_load_dwordx4 v[96:99], v241, s[24:27], s76 offen offset:0 sc1\n"
      "buffer_load_dwordx4 v[100:103], v241, s[24:27], s76 offen offset:1024 sc1\n"
      "buffer_load_dwordx4 v[104:107], v241, s[24:27], s76 offen offset:2048 sc1\n"
      "buffer_load_dwordx4 v[108:111], v241, s[24:27], s76 offen offset:3072 sc1\n"
      "v_mfma_f32_16x16x32_bf16 v[0:3], a[64:67], v[128:131], v[0:3]\n"
      "v_mfma_f32_16x16x32_bf16 v[4:7], a[64:67], v[132:135], v[4:7]\n"
      "ds_write_b128 v232, a[144:147] offset:32768\n"
      "v_mfma_f32_16x16x32_bf16 v[8:11], a[68:71], v[128:131], v[8:11]\n"
      "v_mfma_f32_16x16x32_bf16 v[12:15], a[68:71], v[132:135], v[12:15]\n"
      "ds_write_b128 v232, a[148:151] offset:33792\n"
      "v_mfma_f32_16x16x32_bf16 v[16:19], a[72:75], v[128:131], v[16:19]\n"
      "v_mfma_f32_16x16x32_bf16 v[20:23], a[72:75], v[132:135], v[20:23]\n"
      "ds_write_b128 v232, a[152:155] offset:34816\n"
      "v_mfma_f32_16x16x32_bf16 v[24:27], a[76:79], v[128:131], v[24:27]\n"
      "v_mfma_f32_16x16x32_bf16 v[28:31], a[76:79], v[132:135], v[28:31]\n"
      "ds_write_b128 v232, a[156:159] offset:35840\n"
      "v_mfma_f32_16x16x32_bf16 v[0:3], a[96:99], v[144:147], v[0:3]\n"
      "v_mfma_f32_16x16x32_bf16 v[4:7], a[96:99], v[148:151], v[4:7]\n"
      "ds_write_b128 v232, a[160:163] offset:36864\n"
      "v_mfma_f32_16x16x32_bf16 v[8:11], a[100:103], v[144:147], v[8:11]\n"
      "v_mfma_f32_16x16x32_bf16 v[12:15], a[100:103], v[148:151], v[12:15]\n"
      "ds_write_b128 v232, a[164:167] offset:37888\n"
      "v_mfma_f32_16x16x32_bf16 v[16:19], a[104:107], v[144:147], v[16:19]\n"
      "v_mfma_f32_16x16x32_bf16 v[20:23], a[104:107], v[148:151], v[20:23]\n"
      "ds_write_b128 v232, a[168:171] offset:38912\n"
      "v_mfma_f32_16x16x32_bf16 v[24:27], a[108:111], v[144:147], v[24:27]\n"
      "v_mfma_f32_16x16x32_bf16 v[28:31], a[108:111], v[148:151], v[28:31]\n"
      "ds_write_b128 v232, a[172:175] offset:39936\n"
      "s_waitcnt lgkmcnt(0)\n"
      "s_barrier\n"
      "ds_read_b128 v[64:67], v233 offset:32768\n"
      "ds_read_b128 v[68:71], v233 offset:33792\n"
      "ds_read_b128 v[72:75], v233 offset:34816\n"
      "ds_read_b128 v[76:79], v233 offset:35840\n"
      "ds_read_b128 v[80:83], v233 offset:36864\n"
      "ds_read_b128 v[84:87], v233 offset:37888\n"
      "ds_read_b128 v[88:91], v233 offset:38912\n"
      "ds_read_b128 v[92:95], v233 offset:39936\n"
      "s_waitcnt lgkmcnt(6)\n"
      "v_pk_add_f32 v[0:1], v[0:1], v[64:65]\n"
      "v_pk_add_f32 v[2:3], v[2:3], v[66:67]\n"
      "v_pk_add_f32 v[4:5], v[4:5], v[68:69]\n"
      "v_pk_add_f32 v[6:7], v[6:7], v[70:71]\n"
      "s_waitcnt lgkmcnt(4)\n"
      "v_pk_add_f32 v[8:9], v[8:9], v[72:73]\n"
      "v_pk_add_f32 v[10:11], v[10:11], v[74:75]\n"
      "v_pk_add_f32 v[12:13], v[12:13], v[76:77]\n"
      "v_pk_add_f32 v[14:15], v[14:15], v[78:79]\n"
      "s_waitcnt lgkmcnt(2)\n"
      "v_pk_add_f32 v[16:17], v[16:17], v[80:81]\n"
      "v_pk_add_f32 v[18:19], v[18:19], v[82:83]\n"
      "v_pk_add_f32 v[20:21], v[20:21], v[84:85]\n"
      "v_pk_add_f32 v[22:23], v[22:23], v[86:87]\n"
      "s_waitcnt lgkmcnt(0)\n"
      "v_pk_add_f32 v[24:25], v[24:25], v[88:89]\n"
      "v_pk_add_f32 v[26:27], v[26:27], v[90:91]\n"
      "v_pk_add_f32 v[28:29], v[28:29], v[92:93]\n"
      "v_pk_add_f32 v[30:31], v[30:31], v[94:95]\n"
      "TRY_%=:\n"
      "s_waitcnt vmcnt(4)\n"
      "v_mov_b32 v162, v160\n"
      "v_add_u32 v231, s40, v160\n"
      "s_nop 0\n"
      "v_permlane32_swap_b32 v160, v162\n"
      "v_sub_f32 v168, v160, v228\n"
      "v_min_f32 v172, 0, -v168\n"
      "v_min_f32 v168, 0, v168\n"
      "v_exp_f32 v168, v168\n"
      "v_exp_f32 v172, v172\n"
      "v_mul_f32 v163, v229, v168\n"
      "v_fmac_f32 v163, v162, v172\n"
      "v_rcp_f32 v163, v163\n"
      "s_nop 0\n"
      "v_mul_f32 v168, v168, v163\n"
      "v_mul_f32 v172, v172, v163\n"
      "v_mov_b32 v169, v168\n"
      "v_mov_b32 v173, v172\n"
      "s_nop 0\n"
      "v_permlane16_swap_b32 v168, v169\n"
      "v_permlane16_swap_b32 v172, v173\n"
      "v_pk_mul_f32 v[32:33], v[0:1], v[168:169] op_sel:[0,0] op_sel_hi:[1,0]\n"
      "v_pk_mul_f32 v[34:35], v[2:3], v[168:169] op_sel:[0,0] op_sel_hi:[1,0]\n"
      "v_pk_mul_f32 v[36:37], v[4:5], v[168:169] op_sel:[0,1] op_sel_hi:[1,1]\n"
      "v_pk_mul_f32 v[38:39], v[6:7], v[168:169] op_sel:[0,1] op_sel_hi:[1,1]\n"
      "v_pk_mul_f32 v[40:41], v[8:9], v[168:169] op_sel:[0,0] op_sel_hi:[1,0]\n"
      "v_pk_mul_f32 v[42:43], v[10:11], v[168:169] op_sel:[0,0] op_sel_hi:[1,0]\n"
      "v_pk_mul_f32 v[44:45], v[12:13], v[168:169] op_sel:[0,1] op_sel_hi:[1,1]\n"
      "v_pk_mul_f32 v[46:47], v[14:15], v[168:169] op_sel:[0,1] op_sel_hi:[1,1]\n"
      "v_pk_mul_f32 v[48:49], v[16:17], v[168:169] op_sel:[0,0] op_sel_hi:[1,0]\n"
      "v_pk_mul_f32 v[50:51], v[18:19], v[168:169] op_sel:[0,0] op_sel_hi:[1,0]\n"
      "v_pk_mul_f32 v[52:53], v[20:21], v[168:169] op_sel:[0,1] op_sel_hi:[1,1]\n"
      "v_pk_mul_f32 v[54:55], v[22:23], v[168:169] op_sel:[0,1] op_sel_hi:[1,1]\n"
      "v_pk_mul_f32 v[56:57], v[24:25], v[168:169] op_sel:[0,0] op_sel_hi:[1,0]\n"
      "v_pk_mul_f32 v[58:59], v[26:27], v[168:169] op_sel:[0,0] op_sel_hi:[1,0]\n"
      "v_pk_mul_f32 v[60:61], v[28:29], v[168:169] op_sel:[0,1] op_sel_hi:[1,1]\n"
      "v_pk_mul_f32 v[62:63], v[30:31], v[168:169] op_sel:[0,1] op_sel_hi:[1,1]\n"
      "s_waitcnt vmcnt(3)\n"
      "v_add3_u32 v231, v231, v96, v97\n"
      "v_add3_u32 v231, v231, v98, v99\n"
      "s_waitcnt vmcnt(2)\n"
      "v_add3_u32 v231, v231, v100, v101\n"
      "v_add3_u32 v231, v231, v102, v103\n"
      "s_waitcnt vmcnt(1)\n"
      "v_add3_u32 v231, v231, v104, v105\n"
      "v_add3_u32 v231, v231, v106, v107\n"
      "s_waitcnt vmcnt(0)\n"
      "v_add3_u32 v231, v231, v108, v109\n"
      "v_add3_u32 v231, v231, v110, v111\n"
      "v_cmp_ne_u32 vcc, v231, v161\n"
      "s_cbranch_vccnz RETRY_%=\n"
      "GOT_%=:\n"
      "v_lshlrev_b32 v0, 16, v96\n"
      "v_and_b32 v1, 0xffff0000, v96\n"
      "v_lshlrev_b32 v2, 16, v97\n"
      "v_and_b32 v3, 0xffff0000, v97\n"
      "v_lshlrev_b32 v8, 16, v100\n"
      "v_and_b32 v9, 0xffff0000, v100\n"
      "v_lshlrev_b32 v10, 16, v101\n"
      "v_and_b32 v11, 0xffff0000, v101\n"
      "v_pk_fma_f32 v[32:33], v[0:1], v[172:173], v[32:33] op_sel:[0,0,0] op_sel_hi:[1,0,1]\n"
      "v_pk_fma_f32 v[34:35], v[2:3], v[172:173], v[34:35] op_sel:[0,0,0] op_sel_hi:[1,0,1]\n"
      "v_pk_fma_f32 v[40:41], v[8:9], v[172:173], v[40:41] op_sel:[0,0,0] op_sel_hi:[1,0,1]\n"
      "v_pk_fma_f32 v[42:43], v[10:11], v[172:173], v[42:43] op_sel:[0,0,0] op_sel_hi:[1,0,1]\n"
      "v_cvt_pk_bf16_f32 v112, v32, v33\n"
      "v_cvt_pk_bf16_f32 v114, v40, v41\n"
      "v_cvt_pk_bf16_f32 v113, v34, v35\n"
      "v_cvt_pk_bf16_f32 v115, v42, v43\n"
      "v_permlane16_swap_b32 v112, v114\n"
      "s_nop 0\n"
      "v_permlane16_swap_b32 v113, v115\n"
      "global_store_dwordx4 v196, v[112:115], s[30:31] offset:-4096 sc1 nt\n"
      "v_lshlrev_b32 v16, 16, v104\n"
      "v_and_b32 v17, 0xffff0000, v104\n"
      "v_lshlrev_b32 v18, 16, v105\n"
      "v_and_b32 v19, 0xffff0000, v105\n"
      "v_lshlrev_b32 v24, 16, v108\n"
      "v_and_b32 v25, 0xffff0000, v108\n"
      "v_lshlrev_b32 v26, 16, v109\n"
      "v_and_b32 v27, 0xffff0000, v109\n"
      "v_pk_fma_f32 v[48:49], v[16:17], v[172:173], v[48:49] op_sel:[0,0,0] op_sel_hi:[1,0,1]\n"
      "v_pk_fma_f32 v[50:51], v[18:19], v[172:173], v[50:51] op_sel:[0,0,0] op_sel_hi:[1,0,1]\n"
      "v_pk_fma_f32 v[56:57], v[24:25], v[172:173], v[56:57] op_sel:[0,0,0] op_sel_hi:[1,0,1]\n"
      "v_pk_fma_f32 v[58:59], v[26:27], v[172:173], v[58:59] op_sel:[0,0,0] op_sel_hi:[1,0,1]\n"
      "v_cvt_pk_bf16_f32 v116, v48, v49\n"
      "v_cvt_pk_bf16_f32 v118, v56, v57\n"
      "v_cvt_pk_bf16_f32 v117, v50, v51\n"
      "v_cvt_pk_bf16_f32 v119, v58, v59\n"
      "v_permlane16_swap_b32 v116, v118\n"
      "s_nop 0\n"
      "v_permlane16_swap_b32 v117, v119\n"
      "global_store_dwordx4 v196, v[116:119], s[30:31] offset:-4032 sc1 nt\n"
      "v_lshlrev_b32 v4, 16, v98\n"
      "v_and_b32 v5, 0xffff0000, v98\n"
      "v_lshlrev_b32 v6, 16, v99\n"
      "v_and_b32 v7, 0xffff0000, v99\n"
      "v_lshlrev_b32 v12, 16, v102\n"
      "v_and_b32 v13, 0xffff0000, v102\n"
      "v_lshlrev_b32 v14, 16, v103\n"
      "v_and_b32 v15, 0xffff0000, v103\n"
      "v_pk_fma_f32 v[36:37], v[4:5], v[172:173], v[36:37] op_sel:[0,1,0] op_sel_hi:[1,1,1]\n"
      "v_pk_fma_f32 v[38:39], v[6:7], v[172:173], v[38:39] op_sel:[0,1,0] op_sel_hi:[1,1,1]\n"
      "v_pk_fma_f32 v[44:45], v[12:13], v[172:173], v[44:45] op_sel:[0,1,0] op_sel_hi:[1,1,1]\n"
      "v_pk_fma_f32 v[46:47], v[14:15], v[172:173], v[46:47] op_sel:[0,1,0] op_sel_hi:[1,1,1]\n"
      "v_cvt_pk_bf16_f32 v120, v36, v37\n"
      "v_cvt_pk_bf16_f32 v122, v44, v45\n"
      "v_cvt_pk_bf16_f32 v121, v38, v39\n"
      "v_cvt_pk_bf16_f32 v123, v46, v47\n"
      "v_permlane16_swap_b32 v120, v122\n"
      "s_nop 0\n"
      "v_permlane16_swap_b32 v121, v123\n"
      "global_store_dwordx4 v196, v[120:123], s[30:31] offset:0 sc1 nt\n"
      "v_lshlrev_b32 v20, 16, v106\n"
      "v_and_b32 v21, 0xffff0000, v106\n"
      "v_lshlrev_b32 v22, 16, v107\n"
      "v_and_b32 v23, 0xffff0000, v107\n"
      "v_lshlrev_b32 v28, 16, v110\n"
      "v_and_b32 v29, 0xffff0000, v110\n"
      "v_lshlrev_b32 v30, 16, v111\n"
      "v_and_b32 v31, 0xffff0000, v111\n"
      "v_pk_fma_f32 v[52:53], v[20:21], v[172:173], v[52:53] op_sel:[0,1,0] op_sel_hi:[1,1,1]\n"
      "v_pk_fma_f32 v[54:55], v[22:23], v[172:173], v[54:55] op_sel:[0,1,0] op_sel_hi:[1,1,1]\n"
      "v_pk_fma_f32 v[60:61], v[28:29], v[172:173], v[60:61] op_sel:[0,1,0] op_sel_hi:[1,1,1]\n"
      "v_pk_fma_f32 v[62:63], v[30:31], v[172:173], v[62:63] op_sel:[0,1,0] op_sel_hi:[1,1,1]\n"
      "v_cvt_pk_bf16_f32 v124, v52, v53\n"
      "v_cvt_pk_bf16_f32 v126, v60, v61\n"
      "v_cvt_pk_bf16_f32 v125, v54, v55\n"
      "v_cvt_pk_bf16_f32 v127, v62, v63\n"
      "v_permlane16_swap_b32 v124, v126\n"
      "s_nop 0\n"
      "v_permlane16_swap_b32 v125, v127\n"
      "global_store_dwordx4 v196, v[124:127], s[30:31] offset:64 sc1 nt\n"
      "s_mov_b64 exec, 1\n"
      "global_store_dword v198, v191, s[46:47] sc1\n"
      "s_mov_b64 exec, -1\n"
      "ENDE_%=:\n"
      ".subsection 1\n"
      "MH_%=:\n"
      "s_cmp_lg_u32 s54, 0\n"
      "s_waitcnt vmcnt(12) lgkmcnt(0)\n"
      "s_cbranch_scc1 W6_%=\n"
      "s_waitcnt vmcnt(8)\n"
      "W6_%=:\n"
      "s_barrier\n"
      "v_mfma_f32_16x16x32_bf16 v[24:27], a[108:111], v[144:147], v[24:27]\n"
      "s_xor_b64 s[56:57], s[56:57], s[32:33]\n"
      "ds_read_b128 a[64:67], v242 offset:0\n"
      "ds_read_b128 a[72:75], v243 offset:0\n"
      "v_mfma_f32_16x16x32_bf16 v[32:35], a[112:115], v[144:147], v[32:35]\n"
      "s_bitset1_b32 s61, 15\n"
      "ds_read_b128 a[80:83], v242 offset:1024\n"
      "ds_read_b128 a[88:91], v243 offset:1024\n"
      "v_mfma_f32_16x16x32_bf16 v[36:39], a[112:115], v[148:151], v[36:39]\n"
      "s_add_u32 s61, s61, 0x2000\n"
      "v_exp_f32 v189, v89\n"
      "v_mfma_f32_16x16x32_bf16 a[176:179], a[112:115], v[152:155], a[176:179]\n"
      "s_bitset0_b32 s61, 15\n"
      "v_exp_f32 v190, v90\n"
      "v_mfma_f32_16x16x32_bf16 a[180:183], a[112:115], v[156:159], a[180:183]\n"
      "s_xor_b32 s60, s61, s51\n"
      "v_exp_f32 v191, v91\n"
      "v_mfma_f32_16x16x32_bf16 a[188:191], a[116:119], v[156:159], a[188:191]\n"
      "s_add_u32 s62, s62, 0x4000\n"
      "v_cvt_pk_bf16_f32 v140, v188, v189\n"
      "v_cvt_pk_bf16_f32 v141, v190, v191\n"
      "v_mfma_f32_16x16x32_bf16 a[184:187], a[116:119], v[152:155], a[184:187]\n"
      "s_and_b32 s62, s62, 0x1ffff\n"
      "v_exp_f32 v192, v92\n"
      "v_mfma_f32_16x16x32_bf16 v[44:47], a[116:119], v[148:151], v[44:47]\n"
      "s_add_u32 s66, s66, s86\n"
      "v_exp_f32 v193, v93\n"
      "v_mfma_f32_16x16x32_bf16 v[40:43], a[116:119], v[144:147], v[40:43]\n"
      "s_add_u32 s82, s82, s86\n"
      "v_exp_f32 v194, v94\n"
      "v_mfma_f32_16x16x32_bf16 v[48:51], a[120:123], v[144:147], v[48:51]\n"
      "s_add_u32 s65, s65, s86\n"
      "v_exp_f32 v195, v95\n"
      "v_mfma_f32_16x16x32_bf16 v[52:55], a[120:123], v[148:151], v[52:55]\n"
      "s_max_u32 s88, s66, s65\n"
      "v_cvt_pk_bf16_f32 v142, v192, v193\n"
      "v_cvt_pk_bf16_f32 v143, v194, v195\n"
      "s_waitcnt lgkmcnt(0)\n"
      "v_mfma_f32_16x16x32_bf16 a[192:195], a[120:123], v[152:155], a[192:195]\n"
      "v_xor_b32 v244, 0x10000, v244\n"
      "v_xor_b32 v245, 0x10000, v245\n"
      "v_mfma_f32_16x16x32_bf16 v[184:187], a[140:143], v[140:143], v[184:187]\n"
      "s_mov_b32 m0, s62\n"
      "ds_read_b128 a[68:71], v242 offset:4096\n"
      "ds_read_b128 a[76:79], v243 offset:4096\n"
      "v_mfma_f32_16x16x32_bf16 a[196:199], a[120:123], v[156:159], a[196:199]\n"
      "buffer_load_dwordx4 v248, s[56:59], s60 offen nt lds\n"
      "ds_read_b128 a[84:87], v242 offset:5120\n"
      "ds_read_b128 a[92:95], v243 offset:5120\n"
      "v_mfma_f32_16x16x32_bf16 a[204:207], a[124:127], v[156:159], a[204:207]\n"
      "buffer_load_dwordx4 v248, s[56:59], s60 offen offset:2048 nt lds\n"
      "v_xor_b32 v246, 0x10000, v246\n"
      "v_xor_b32 v247, 0x10000, v247\n"
      "v_mfma_f32_16x16x32_bf16 a[200:203], a[124:127], v[152:155], a[200:203]\n"
      "s_bitset1_b32 m0, 10\n"
      "v_max3_f32 v196, v184, v185, v186\n"
      "v_max_f32 v196, v196, v187\n"
      "v_mfma_f32_16x16x32_bf16 v[60:63], a[124:127], v[148:151], v[60:63]\n"
      "buffer_load_dwordx4 v249, s[56:59], s60 offen nt lds\n"
      "v_cmp_nge_f32 vcc, 0x4b800000, v196\n"
      "v_mfma_f32_16x16x32_bf16 v[56:59], a[124:127], v[144:147], v[56:59]\n"
      "buffer_load_dwordx4 v249, s[56:59], s60 offen offset:2048 nt lds\n"
      "s_cbranch_vccnz FS11_%=\n"
      "FR11_%=:\n"
      "v_mfma_f32_16x16x32_bf16 v[64:67], a[64:67], a[0:3], v[160:163]\n"
      "v_exp_f32 v188, v96\n"
      "v_mfma_f32_16x16x32_bf16 v[64:67], a[72:75], a[16:19], v[64:67]\n"
      "v_add_f32 v180, v180, v184\n"
      "v_add_f32 v181, v181, v185\n"
      "v_mfma_f32_16x16x32_bf16 v[64:67], a[80:83], a[32:35], v[64:67]\n"
      "v_exp_f32 v189, v97\n"
      "v_mfma_f32_16x16x32_bf16 v[64:67], a[88:91], a[48:51], v[64:67]\n"
      "v_add_f32 v182, v182, v186\n"
      "v_add_f32 v183, v183, v187\n"
      "v_mfma_f32_16x16x32_bf16 v[72:75], a[64:67], a[4:7], v[164:167]\n"
      "v_exp_f32 v190, v98\n"
      "v_mfma_f32_16x16x32_bf16 v[72:75], a[72:75], a[20:23], v[72:75]\n"
      "v_exp_f32 v191, v99\n"
      "v_mfma_f32_16x16x32_bf16 v[72:75], a[80:83], a[36:39], v[72:75]\n"
      "v_cvt_pk_bf16_f32 v144, v188, v189\n"
      "v_cvt_pk_bf16_f32 v145, v190, v191\n"
      "v_mfma_f32_16x16x32_bf16 v[72:75], a[88:91], a[52:55], v[72:75]\n"
      "v_exp_f32 v192, v100\n"
      "v_mfma_f32_16x16x32_bf16 v[80:83], a[64:67], a[8:11], v[168:171]\n"
      "s_add_u32 s39, s39, s39\n"
      "v_exp_f32 v193, v101\n"
      "v_mfma_f32_16x16x32_bf16 v[80:83], a[72:75], a[24:27], v[80:83]\n"
      "s_memrealtime s[68:69]\n"
      "v_exp_f32 v194, v102\n"
      "v_mfma_f32_16x16x32_bf16 v[80:83], a[80:83], a[40:43], v[80:83]\n"
      "v_exp_f32 v195, v103\n"
      "v_mfma_f32_16x16x32_bf16 v[80:83], a[88:91], a[56:59], v[80:83]\n"
      "s_nop 0\n"
      "v_mfma_f32_16x16x32_bf16 v[88:91], a[64:67], a[12:15], v[172:175]\n"
      "s_nop 0\n"
      "v_mfma_f32_16x16x32_bf16 v[88:91], a[72:75], a[28:31], v[88:91]\n"
      "v_exp_f32 v188, v104\n"
      "v_mfma_f32_16x16x32_bf16 v[88:91], a[80:83], a[44:47], v[88:91]\n"
      "s_nop 0\n"
      "v_mfma_f32_16x16x32_bf16 v[88:91], a[88:91], a[60:63], v[88:91]\n"
      "s_nop 0\n"
      "s_cbranch_scc1 SP1_%=\n"
      "s_branch HD1_%=\n"
      "HOOK_%=:\n"
      "buffer_load_dword v250, v251, s[20:23], 0 offen sc1\n"
      "s_swappc_b64 s[84:85], s[84:85]\n"
      "s_waitcnt vmcnt(12)\n"
      "v_subrev_u32 v250, s71, v250\n"
      "v_min_u32 v250, 0x258, v250\n"
      "s_nop 1\n"
      "v_add_u32_dpp v250, v250, v250 quad_perm:[1,0,3,2] row_mask:0xf bank_mask:0xf\n"
      "s_nop 1\n"
      "v_add_u32_dpp v250, v250, v250 quad_perm:[2,3,0,1] row_mask:0xf bank_mask:0xf\n"
      "s_nop 1\n"
      "v_add_u32_dpp v250, v250, v250 row_half_mirror row_mask:0xf bank_mask:0xf\n"
      "s_nop 0\n"
      "v_readfirstlane_b32 s67, v250\n"
      "s_sub_u32 s66, s66, s81\n"
      "s_lshr_b32 s67, s67, 3\n"
      "s_add_u32 s66, s66, s67\n"
      "s_max_u32 s88, s66, s65\n"
      "s_mov_b32 s86, 57\n"
      "s_mov_b32 s77, 171\n"
      "v_bfe_u32 v251, v241, 2, 7\n"
      "v_add_u32 v251, s79, v251\n"
      "buffer_load_dword v250, v251, s[20:23], 0 offen sc1\n"
      "s_swappc_b64 s[84:85], s[84:85]\n"
      "s_mov_b32 s89, 0\n"
      "s_swappc_b64 s[84:85], s[84:85]\n"
      "s_mov_b32 s86, 55\n"
      "s_mov_b32 s77, 165\n"
      "s_swappc_b64 s[84:85], s[84:85]\n"
      "s_cmp_lt_u32 s89, 2\n"
      "s_cselect_b32 s67, 2048, s72\n"
      "s_mov_b32 s89, 0\n"
      "s_cmp_lt_u32 s67, 2048\n"
      "s_cbranch_scc0 NV0_%=\n"
      "v_and_b32 v216, 0x1f0, v241\n"
      "v_lshlrev_b32 v216, 4, v216\n"
      "v_mov_b32 v215, s82\n"
      "s_lshr_b32 s67, s67, 4\n"
      "s_add_u32 s67, s67, 8192\n"
      "buffer_store_dword v215, v216, s[20:23], s67 offen sc1\n"
      "NV0_%=:\n"
      "s_swappc_b64 s[84:85], s[84:85]\n"
      "s_mov_b32 s86, 54\n"
      "s_mov_b32 s77, 162\n"
      "s_swappc_b64 s[84:85], s[84:85]\n"
      "buffer_load_dword v250, v251, s[20:23], 0 offen sc1\n"
      "s_swappc_b64 s[84:85], s[84:85]\n"
      "s_cmp_lt_u32 s89, 2\n"
      "s_cselect_b32 s67, 2048, s72\n"
      "s_mov_b32 s89, 0\n"
      "s_cmp_lt_u32 s67, 2048\n"
      "s_cbranch_scc0 NV1_%=\n"
      "v_and_b32 v216, 0x1f0, v241\n"
      "v_lshlrev_b32 v216, 4, v216\n"
      "v_mov_b32 v215, s82\n"
      "s_lshr_b32 s67, s67, 4\n"
      "s_add_u32 s67, s67, 8192\n"
      "buffer_store_dword v215, v216, s[20:23], s67 offen sc1\n"
      "NV1_%=:\n"
      "s_swappc_b64 s[84:85], s[84:85]\n"
      "s_waitcnt vmcnt(12)\n"
      "v_sub_u32 v250, s82, v250\n"
      "v_cmp_gt_u32 vcc, 1600, v250\n"
      "s_movk_i32 s76, 100\n"
      "s_nop 2\n"
      "s_bcnt1_i32_b32 s67, vcc_lo\n"
      "s_cmp_eq_u32 s70, 60\n"
      "s_cselect_b32 s67, 0, s67\n"
      "s_cmp_ge_u32 s67, 5\n"
      "s_cbranch_scc1 SM0_%=\n"
      "buffer_load_dword v250, v251, s[20:23], 0 offen sc1\n"
      "s_swappc_b64 s[84:85], s[84:85]\n"
      "s_cmp_lt_u32 s89, 2\n"
      "s_cselect_b32 s67, 2048, s72\n"
      "s_mov_b32 s89, 0\n"
      "s_cmp_lt_u32 s67, 2048\n"
      "s_cbranch_scc0 NV2_%=\n"
      "v_and_b32 v216, 0x1f0, v241\n"
      "v_lshlrev_b32 v216, 4, v216\n"
      "v_mov_b32 v215, s82\n"
      "s_lshr_b32 s67, s67, 4\n"
      "s_add_u32 s67, s67, 8192\n"
      "buffer_store_dword v215, v216, s[20:23], s67 offen sc1\n"
      "NV2_%=:\n"
      "s_swappc_b64 s[84:85], s[84:85]\n"
      "s_waitcnt vmcnt(12)\n"
      "v_sub_u32 v250, s82, v250\n"
      "v_cmp_gt_u32 vcc, 1600, v250\n"
      "s_movk_i32 s76, 146\n"
      "s_nop 2\n"
      "s_bcnt1_i32_b32 s67, vcc_lo\n"
      "s_cmp_eq_u32 s70, 60\n"
      "s_cselect_b32 s67, 0, s67\n"
      "s_cmp_ge_u32 s67, 5\n"
      "s_cbranch_scc1 SM1_%=\n"
      "buffer_load_dword v250, v251, s[20:23], 0 offen sc1\n"
      "s_swappc_b64 s[84:85], s[84:85]\n"
      "s_min_i32 s70, s70, 150\n"
      "s_waitcnt vmcnt(12)\n"
      "v_sub_u32 v250, s82, v250\n"
      "v_cmp_gt_u32 vcc, 1600, v250\n"
      "s_movk_i32 s76, 154\n"
      "s_nop 2\n"
      "s_bcnt1_i32_b32 s67, vcc_lo\n"
      "s_cmp_eq_u32 s70, 60\n"
      "s_cselect_b32 s67, 0, s67\n"
      "s_cmp_ge_u32 s67, 5\n"
      "s_cbranch_scc1 SM1_%=\n"
      "s_swappc_b64 s[84:85], s[84:85]\n"
      "s_cmp_lt_u32 s89, 2\n"
      "s_cselect_b32 s67, 2048, s72\n"
      "s_mov_b32 s89, 0\n"
      "s_cmp_lt_u32 s67, 2048\n"
      "s_cbranch_scc0 NV3_%=\n"
      "v_and_b32 v216, 0x1f0, v241\n"
      "v_lshlrev_b32 v216, 4, v216\n"
      "v_mov_b32 v215, s82\n"
      "s_lshr_b32 s67, s67, 4\n"
      "s_add_u32 s67, s67, 8192\n"
      "buffer_store_dword v215, v216, s[20:23], s67 offen sc1\n"
      "NV3_%=:\n"
      "s_swappc_b64 s[84:85], s[84:85]\n"
      "s_mov_b32 s86, 53\n"
      "s_mov_b32 s77, 159\n"
      "s_mov_b32 s87, 6\n"
      "buffer_load_dword v250, v251, s[20:23], 0 offen sc1\n"
      "s_swappc_b64 s[84:85], s[84:85]\n"
      "s_waitcnt vmcnt(12)\n"
      "v_sub_u32 v250, s82, v250\n"
      "v_cmp_gt_u32 vcc, 1600, v250\n"
      "s_movk_i32 s76, 170\n"
      "s_nop 2\n"
      "s_bcnt1_i32_b32 s67, vcc_lo\n"
      "s_cmp_eq_u32 s70, 60\n"
      "s_cselect_b32 s67, 0, s67\n"
      "s_cmp_ge_u32 s67, 5\n"
      "s_cbranch_scc1 SM1_%=\n"
      "s_swappc_b64 s[84:85], s[84:85]\n"
      "s_getpc_b64 s[74:75]\n"
      "PF_%=:\n"
      "s_sub_u32 s74, s74, PF_%=-END_%=\n"
      "s_subb_u32 s75, s75, 0\n"
      "s_lshl_b32 s76, %[blk], 7\n"
      "s_and_b32 s76, s76, 0x1c00\n"
      "s_min_u32 s76, s76, ENDE_%=-END_%=-1024\n"
      "v_add_u32 v200, s76, v241\n"
      "global_load_dwordx4 v[200:203], v200, s[74:75]\n"
      "v_mov_b32 v212, 0\n"
      "global_load_dword v213, v212, s[30:31] nt\n"
      "buffer_load_dword v214, off, s[24:27], s78 nt\n"
      "s_swappc_b64 s[84:85], s[84:85]\n"
      "SM1_%=:\n"
      "s_add_u32 s66, s66, s76\n"
      "s_max_u32 s88, s66, s65\n"
      "s_min_i32 s70, s70, 150\n"
      "s_mov_b32 s87, 0\n"
      "s_branch SM2_%=\n"
      "SM0_%=:\n"
      "s_or_b32 s39, s39, 0xe0000000\n"
      "s_mov_b32 s52, 0xaaaaaaaa\n"
      "s_mov_b32 s53, 0xaaaaaaaa\n"
      "s_add_u32 s66, s66, s76\n"
      "s_max_u32 s88, s66, s65\n"
      "s_min_i32 s70, s70, 150\n"
      "s_mov_b32 s87, 0\n"
      "s_mov_b32 s86, 66\n"
      "s_mov_b32 s77, 198\n"
      "s_swappc_b64 s[84:85], s[84:85]\n"
      "s_mov_b32 s86, 62\n"
      "s_mov_b32 s77, 186\n"
      "s_swappc_b64 s[84:85], s[84:85]\n"
      "s_mov_b32 s86, 59\n"
      "s_mov_b32 s77, 177\n"
      "s_swappc_b64 s[84:85], s[84:85]\n"
      "SM2_%=:\n"
      "s_mov_b32 s86, 56\n"
      "s_mov_b32 s77, 168\n"
      "s_mov_b32 s52, 0x0\n"
      "s_mov_b32 s53, 0x80000\n"
      "s_swappc_b64 s[84:85], s[84:85]\n"
      "s_getpc_b64 s[74:75]\n"
      "PG_%=:\n"
      "s_sub_u32 s74, s74, PG_%=-END_%=\n"
      "s_subb_u32 s75, s75, 0\n"
      "s_lshl_b32 s76, %[blk], 7\n"
      "s_and_b32 s76, s76, 0x1c00\n"
      "s_min_u32 s76, s76, ENDE_%=-END_%=-1024\n"
      "v_add_u32 v200, s76, v241\n"
      "global_load_dwordx4 v[200:203], v200, s[74:75]\n"
      "v_mov_b32 v212, 0\n"
      "global_load_dword v213, v212, s[30:31] nt\n"
      "buffer_load_dword v214, off, s[24:27], s78 nt\n"
      "s_swappc_b64 s[84:85], s[84:85]\n"
      "GW0_%=:\n"
      "s_memrealtime s[68:69]\n"
      "s_waitcnt lgkmcnt(0)\n"
      "s_getreg_b32 s67, hwreg(HW_REG_IB_STS, 0, 4)\n"
      "s_cmp_gt_u32 s67, 9\n"
      "s_cselect_b32 s67, 0, 0\n"
      "s_addc_u32 s89, s89, 0\n"
      "s_add_u32 s66, s66, s67\n"
      "s_add_u32 s88, s88, s67\n"
      "GS0_%=:\n"
      "s_sub_u32 s67, s68, s88\n"
      "s_cmp_ge_i32 s67, 0\n"
      "s_cbranch_scc1 GX0_%=\n"
      "s_memrealtime s[68:69]\n"
      "s_waitcnt lgkmcnt(0)\n"
      "s_branch GS0_%=\n"
      "FS1_%=:\n"
      "s_call_b64 s[96:97], FB1_%=\n"
      "s_branch FR1_%=\n"
      "FS2_%=:\n"
      "s_call_b64 s[96:97], FB0_%=\n"
      "s_branch FR2_%=\n"
      "FS3_%=:\n"
      "s_call_b64 s[96:97], FB0_%=\n"
      "s_branch FR3_%=\n"
      "GW4_%=:\n"
      "s_memrealtime s[68:69]\n"
      "s_waitcnt lgkmcnt(0)\n"
      "s_getreg_b32 s67, hwreg(HW_REG_IB_STS, 0, 4)\n"
      "s_cmp_gt_u32 s67, 9\n"
      "s_cselect_b32 s67, 0, 0\n"
      "s_addc_u32 s89, s89, 0\n"
      "s_add_u32 s66, s66, s67\n"
      "s_add_u32 s88, s88, s67\n"
      "GS4_%=:\n"
      "s_sub_u32 s67, s68, s88\n"
      "s_cmp_ge_i32 s67, 0\n"
      "s_cbranch_scc1 GX4_%=\n"
      "s_memrealtime s[68:69]\n"
      "s_waitcnt lgkmcnt(0)\n"
      "s_branch GS4_%=\n"
      "GW5_%=:\n"
      "s_memrealtime s[68:69]\n"
      "s_waitcnt lgkmcnt(0)\n"
      "s_getreg_b32 s67, hwreg(HW_REG_IB_STS, 0, 4)\n"
      "s_cmp_gt_u32 s67, 9\n"
      "s_cselect_b32 s67, 0, 0\n"
      "s_addc_u32 s89, s89, 0\n"
      "s_add_u32 s66, s66, s67\n"
      "s_add_u32 s88, s88, s67\n"
      "GS5_%=:\n"
      "s_sub_u32 s67, s68, s88\n"
      "s_cmp_ge_i32 s67, 0\n"
      "s_cbranch_scc1 GX5_%=\n"
      "s_memrealtime s[68:69]\n"
      "s_waitcnt lgkmcnt(0)\n"
      "s_branch GS5_%=\n"
      "GW6_%=:\n"
      "s_memrealtime s[68:69]\n"
      "s_waitcnt lgkmcnt(0)\n"
      "s_getreg_b32 s67, hwreg(HW_REG_IB_STS, 0, 4)\n"
      "s_cmp_gt_u32 s67, 9\n"
      "s_cselect_b32 s67, 0, 0\n"
      "s_addc_u32 s89, s89, 0\n"
      "s_add_u32 s66, s66, s67\n"
      "s_add_u32 s88, s88, s67\n"
      "GS6_%=:\n"
      "s_sub_u32 s67, s68, s88\n"
      "s_cmp_ge_i32 s67, 0\n"
      "s_cbranch_scc1 GX6_%=\n"
      "s_memrealtime s[68:69]\n"
      "s_waitcnt lgkmcnt(0)\n"
      "s_branch GS6_%=\n"
      "GW7_%=:\n"
      "s_memrealtime s[68:69]\n"
      "s_waitcnt lgkmcnt(0)\n"
      "s_getreg_b32 s67, hwreg(HW_REG_IB_STS, 0, 4)\n"
      "s_cmp_gt_u32 s67, 9\n"
      "s_cselect_b32 s67, 0, 0\n"
      "s_addc_u32 s89, s89, 0\n"
      "s_add_u32 s66, s66, s67\n"
      "s_add_u32 s88, s88, s67\n"
      "GS7_%=:\n"
      "s_sub_u32 s67, s68, s88\n"
      "s_cmp_ge_i32 s67, 0\n"
      "s_cbranch_scc1 GX7_%=\n"
      "s_memrealtime s[68:69]\n"
      "s_waitcnt lgkmcnt(0)\n"
      "s_branch GS7_%=\n"
      "FS8_%=:\n"
      "s_call_b64 s[96:97], FB1_%=\n"
      "s_branch FR8_%=\n"
      "LAG_%=:\n"
      "s_memrealtime s[68:69]\n"
      "s_waitcnt lgkmcnt(0)\n"
      "s_mov_b32 s88, s68\n"
      "LG1_%=:\n"
      "v_cmp_ne_u32 vcc, s40, v207\n"
      "s_cbranch_vccz LG2_%=\n"
      "s_sub_u32 s63, s63, 1\n"
      "s_cmp_eq_u32 s63, 0\n"
      "s_cbranch_scc1 READ_%=\n"
      "s_sleep 4\n"
      "global_load_dwordx2 v[206:207], v239, s[46:47] offset:8 sc1\n"
      "s_waitcnt vmcnt(0)\n"
      "s_branch LG1_%=\n"
      "LG2_%=:\n"
      "v_readfirstlane_b32 s67, v206\n"
      "s_sub_u32 s88, s88, s82\n"
      "s_min_u32 s88, s88, 400\n"
      "s_add_u32 s88, s88, s67\n"
      "LG3_%=:\n"
      "s_memrealtime s[68:69]\n"
      "s_waitcnt lgkmcnt(0)\n"
      "s_sub_u32 s67, s88, s68\n"
      "s_sub_u32 s67, s67, 17\n"
      "s_cmp_lt_u32 s67, 400\n"
      "s_cbranch_scc1 LG3_%=\n"
      "s_branch READ_%=\n"
      "RETRY_%=:\n"
      "s_sub_u32 s63, s63, 1\n"
      "s_cbranch_scc1 GOT_%=\n"
      "buffer_load_dwordx2 v[160:161], v240, s[24:27], s76 offen sc1\n"
      "buffer_load_dwordx4 v[96:99], v241, s[24:27], s76 offen offset:0 sc1\n"
      "buffer_load_dwordx4 v[100:103], v241, s[24:27], s76 offen offset:1024 sc1\n"
      "buffer_load_dwordx4 v[104:107], v241, s[24:27], s76 offen offset:2048 sc1\n"
      "buffer_load_dwordx4 v[108:111], v241, s[24:27], s76 offen offset:3072 sc1\n"
      "s_branch TRY_%=\n"
      "FS11_%=:\n"
      "s_call_b64 s[96:97], FB0_%=\n"
      "s_branch FR11_%=\n"
      "COLD_%=:\n"
      "FB0_%=:\n"
      "v_mov_b32 v196, 139248\n"
      "s_nop 7\n"
      "ds_write_b32 v196, v196\n"
      "v_max3_f32 v188, v64, v65, 0\n"
      "v_max3_f32 v189, v72, v73, 0\n"
      "v_max3_f32 v190, v80, v81, 0\n"
      "v_max3_f32 v191, v88, v89, 0\n"
      "v_max3_f32 v188, v188, v66, v67\n"
      "v_max3_f32 v189, v189, v74, v75\n"
      "v_max3_f32 v190, v190, v82, v83\n"
      "v_max3_f32 v191, v191, v90, v91\n"
      "v_max3_f32 v188, v188, v68, v69\n"
      "v_max3_f32 v189, v189, v76, v77\n"
      "v_max3_f32 v190, v190, v84, v85\n"
      "v_max3_f32 v191, v191, v92, v93\n"
      "v_max3_f32 v188, v188, v70, v71\n"
      "v_max3_f32 v189, v189, v78, v79\n"
      "v_max3_f32 v190, v190, v86, v87\n"
      "v_max3_f32 v191, v191, v94, v95\n"
      "v_mov_b32 v192, v188\n"
      "v_mov_b32 v193, v189\n"
      "v_mov_b32 v194, v190\n"
      "v_mov_b32 v195, v191\n"
      "v_permlane16_swap_b32 v188, v192\n"
      "v_permlane16_swap_b32 v189, v193\n"
      "v_permlane16_swap_b32 v190, v194\n"
      "v_permlane16_swap_b32 v191, v195\n"
      "v_max_f32 v188, v188, v192\n"
      "v_max_f32 v189, v189, v193\n"
      "v_max_f32 v190, v190, v194\n"
      "v_max_f32 v191, v191, v195\n"
      "v_mov_b32 v192, v188\n"
      "v_mov_b32 v193, v189\n"
      "v_mov_b32 v194, v190\n"
      "v_mov_b32 v195, v191\n"
      "v_permlane32_swap_b32 v188, v192\n"
      "v_permlane32_swap_b32 v189, v193\n"
      "v_permlane32_swap_b32 v190, v194\n"
      "v_permlane32_swap_b32 v191, v195\n"
      "v_max_f32 v188, v188, v192\n"
      "v_max_f32 v189, v189, v193\n"
      "v_max_f32 v190, v190, v194\n"
      "v_max_f32 v191, v191, v195\n"
      "RS0_%=:\n"
      "v_exp_f32 v192, -v188\n"
      "v_exp_f32 v193, -v189\n"
      "v_exp_f32 v194, -v190\n"
      "v_exp_f32 v195, -v191\n"
      "v_sub_f32 v176, v176, v188\n"
      "v_sub_f32 v177, v177, v189\n"
      "v_sub_f32 v178, v178, v190\n"
      "v_sub_f32 v179, v179, v191\n"
      "v_mov_b32 v160, v176\n"
      "v_mov_b32 v161, v176\n"
      "v_mov_b32 v162, v176\n"
      "v_mov_b32 v163, v176\n"
      "v_mov_b32 v164, v177\n"
      "v_mov_b32 v165, v177\n"
      "v_mov_b32 v166, v177\n"
      "v_mov_b32 v167, v177\n"
      "v_mov_b32 v168, v178\n"
      "v_mov_b32 v169, v178\n"
      "v_mov_b32 v170, v178\n"
      "v_mov_b32 v171, v178\n"
      "v_mov_b32 v172, v179\n"
      "v_mov_b32 v173, v179\n"
      "v_mov_b32 v174, v179\n"
      "v_mov_b32 v175, v179\n"
      "v_sub_f32 v64, v64, v188\n"
      "v_sub_f32 v65, v65, v188\n"
      "v_sub_f32 v66, v66, v188\n"
      "v_sub_f32 v67, v67, v188\n"
      "v_sub_f32 v68, v68, v188\n"
      "v_sub_f32 v69, v69, v188\n"
      "v_sub_f32 v70, v70, v188\n"
      "v_sub_f32 v71, v71, v188\n"
      "v_sub_f32 v72, v72, v189\n"
      "v_sub_f32 v73, v73, v189\n"
      "v_sub_f32 v74, v74, v189\n"
      "v_sub_f32 v75, v75, v189\n"
      "v_sub_f32 v76, v76, v189\n"
      "v_sub_f32 v77, v77, v189\n"
      "v_sub_f32 v78, v78, v189\n"
      "v_sub_f32 v79, v79, v189\n"
      "v_sub_f32 v80, v80, v190\n"
      "v_sub_f32 v81, v81, v190\n"
      "v_sub_f32 v82, v82, v190\n"
      "v_sub_f32 v83, v83, v190\n"
      "v_sub_f32 v84, v84, v190\n"
      "v_sub_f32 v85, v85, v190\n"
      "v_sub_f32 v86, v86, v190\n"
      "v_sub_f32 v87, v87, v190\n"
      "v_sub_f32 v88, v88, v191\n"
      "v_sub_f32 v89, v89, v191\n"
      "v_sub_f32 v90, v90, v191\n"
      "v_sub_f32 v91, v91, v191\n"
      "v_sub_f32 v92, v92, v191\n"
      "v_sub_f32 v93, v93, v191\n"
      "v_sub_f32 v94, v94, v191\n"
      "v_sub_f32 v95, v95, v191\n"
      "v_sub_f32 v96, v96, v188\n"
      "v_sub_f32 v97, v97, v188\n"
      "v_sub_f32 v98, v98, v188\n"
      "v_sub_f32 v99, v99, v188\n"
      "v_sub_f32 v100, v100, v188\n"
      "v_sub_f32 v101, v101, v188\n"
      "v_sub_f32 v102, v102, v188\n"
      "v_sub_f32 v103, v103, v188\n"
      "v_sub_f32 v104, v104, v189\n"
      "v_sub_f32 v105, v105, v189\n"
      "v_sub_f32 v106, v106, v189\n"
      "v_sub_f32 v107, v107, v189\n"
      "v_sub_f32 v108, v108, v189\n"
      "v_sub_f32 v109, v109, v189\n"
      "v_sub_f32 v110, v110, v189\n"
      "v_sub_f32 v111, v111, v189\n"
      "v_sub_f32 v112, v112, v190\n"
      "v_sub_f32 v113, v113, v190\n"
      "v_sub_f32 v114, v114, v190\n"
      "v_sub_f32 v115, v115, v190\n"
      "v_sub_f32 v116, v116, v190\n"
      "v_sub_f32 v117, v117, v190\n"
      "v_sub_f32 v118, v118, v190\n"
      "v_sub_f32 v119, v119, v190\n"
      "v_sub_f32 v120, v120, v191\n"
      "v_sub_f32 v121, v121, v191\n"
      "v_sub_f32 v122, v122, v191\n"
      "v_sub_f32 v123, v123, v191\n"
      "v_sub_f32 v124, v124, v191\n"
      "v_sub_f32 v125, v125, v191\n"
      "v_sub_f32 v126, v126, v191\n"
      "v_sub_f32 v127, v127, v191\n"
      "v_mul_f32 v0, v0, v192\n"
      "v_mul_f32 v1, v1, v192\n"
      "v_mul_f32 v2, v2, v192\n"
      "v_mul_f32 v3, v3, v192\n"
      "v_mul_f32 v4, v4, v193\n"
      "v_mul_f32 v5, v5, v193\n"
      "v_mul_f32 v6, v6, v193\n"
      "v_mul_f32 v7, v7, v193\n"
      "v_mul_f32 v8, v8, v192\n"
      "v_mul_f32 v9, v9, v192\n"
      "v_mul_f32 v10, v10, v192\n"
      "v_mul_f32 v11, v11, v192\n"
      "v_mul_f32 v12, v12, v193\n"
      "v_mul_f32 v13, v13, v193\n"
      "v_mul_f32 v14, v14, v193\n"
      "v_mul_f32 v15, v15, v193\n"
      "v_mul_f32 v16, v16, v192\n"
      "v_mul_f32 v17, v17, v192\n"
      "v_mul_f32 v18, v18, v192\n"
      "v_mul_f32 v19, v19, v192\n"
      "v_mul_f32 v20, v20, v193\n"
      "v_mul_f32 v21, v21, v193\n"
      "v_mul_f32 v22, v22, v193\n"
      "v_mul_f32 v23, v23, v193\n"
      "v_mul_f32 v24, v24, v192\n"
      "v_mul_f32 v25, v25, v192\n"
      "v_mul_f32 v26, v26, v192\n"
      "v_mul_f32 v27, v27, v192\n"
      "v_mul_f32 v28, v28, v193\n"
      "v_mul_f32 v29, v29, v193\n"
      "v_mul_f32 v30, v30, v193\n"
      "v_mul_f32 v31, v31, v193\n"
      "v_mul_f32 v32, v32, v192\n"
      "v_mul_f32 v33, v33, v192\n"
      "v_mul_f32 v34, v34, v192\n"
      "v_mul_f32 v35, v35, v192\n"
      "v_mul_f32 v36, v36, v193\n"
      "v_mul_f32 v37, v37, v193\n"
      "v_mul_f32 v38, v38, v193\n"
      "v_mul_f32 v39, v39, v193\n"
      "v_mul_f32 v40, v40, v192\n"
      "v_mul_f32 v41, v41, v192\n"
      "v_mul_f32 v42, v42, v192\n"
      "v_mul_f32 v43, v43, v192\n"
      "v_mul_f32 v44, v44, v193\n"
      "v_mul_f32 v45, v45, v193\n"
      "v_mul_f32 v46, v46, v193\n"
      "v_mul_f32 v47, v47, v193\n"
      "v_mul_f32 v48, v48, v192\n"
      "v_mul_f32 v49, v49, v192\n"
      "v_mul_f32 v50, v50, v192\n"
      "v_mul_f32 v51, v51, v192\n"
      "v_mul_f32 v52, v52, v193\n"
      "v_mul_f32 v53, v53, v193\n"
      "v_mul_f32 v54, v54, v193\n"
      "v_mul_f32 v55, v55, v193\n"
      "v_mul_f32 v56, v56, v192\n"
      "v_mul_f32 v57, v57, v192\n"
      "v_mul_f32 v58, v58, v192\n"
      "v_mul_f32 v59, v59, v192\n"
      "v_mul_f32 v60, v60, v193\n"
      "v_mul_f32 v61, v61, v193\n"
      "v_mul_f32 v62, v62, v193\n"
      "v_mul_f32 v63, v63, v193\n"
      "v_accvgpr_read_b32 v196, a144\n"
      "v_accvgpr_read_b32 v197, a145\n"
      "v_accvgpr_read_b32 v198, a146\n"
      "v_accvgpr_read_b32 v199, a147\n"
      "v_mul_f32 v196, v196, v194\n"
      "v_mul_f32 v197, v197, v194\n"
      "v_mul_f32 v198, v198, v194\n"
      "v_mul_f32 v199, v199, v194\n"
      "v_accvgpr_write_b32 a144, v196\n"
      "v_accvgpr_write_b32 a145, v197\n"
      "v_accvgpr_write_b32 a146, v198\n"
      "v_accvgpr_write_b32 a147, v199\n"
      "v_accvgpr_read_b32 v196, a148\n"
      "v_accvgpr_read_b32 v197, a149\n"
      "v_accvgpr_read_b32 v198, a150\n"
      "v_accvgpr_read_b32 v199, a151\n"
      "v_mul_f32 v196, v196, v195\n"
      "v_mul_f32 v197, v197, v195\n"
      "v_mul_f32 v198, v198, v195\n"
      "v_mul_f32 v199, v199, v195\n"
      "v_accvgpr_write_b32 a148, v196\n"
      "v_accvgpr_write_b32 a149, v197\n"
      "v_accvgpr_write_b32 a150, v198\n"
      "v_accvgpr_write_b32 a151, v199\n"
      "v_accvgpr_read_b32 v196, a152\n"
      "v_accvgpr_read_b32 v197, a153\n"
      "v_accvgpr_read_b32 v198, a154\n"
      "v_accvgpr_read_b32 v199, a155\n"
      "v_mul_f32 v196, v196, v194\n"
      "v_mul_f32 v197, v197, v194\n"
      "v_mul_f32 v198, v198, v194\n"
      "v_mul_f32 v199, v199, v194\n"
      "v_accvgpr_write_b32 a152, v196\n"
      "v_accvgpr_write_b32 a153, v197\n"
      "v_accvgpr_write_b32 a154, v198\n"
      "v_accvgpr_write_b32 a155, v199\n"
      "v_accvgpr_read_b32 v196, a156\n"
      "v_accvgpr_read_b32 v197, a157\n"
      "v_accvgpr_read_b32 v198, a158\n"
      "v_accvgpr_read_b32 v199, a159\n"
      "v_mul_f32 v196, v196, v195\n"
      "v_mul_f32 v197, v197, v195\n"
      "v_mul_f32 v198, v198, v195\n"
      "v_mul_f32 v199, v199, v195\n"
      "v_accvgpr_write_b32 a156, v196\n"
      "v_accvgpr_write_b32 a157, v197\n"
      "v_accvgpr_write_b32 a158, v198\n"
      "v_accvgpr_write_b32 a159, v199\n"
      "v_accvgpr_read_b32 v196, a160\n"
      "v_accvgpr_read_b32 v197, a161\n"
      "v_accvgpr_read_b32 v198, a162\n"
      "v_accvgpr_read_b32 v199, a163\n"
      "v_mul_f32 v196, v196, v194\n"
      "v_mul_f32 v197, v197, v194\n"
      "v_mul_f32 v198, v198, v194\n"
      "v_mul_f32 v199, v199, v194\n"
      "v_accvgpr_write_b32 a160, v196\n"
      "v_accvgpr_write_b32 a161, v197\n"
      "v_accvgpr_write_b32 a162, v198\n"
      "v_accvgpr_write_b32 a163, v199\n"
      "v_accvgpr_read_b32 v196, a164\n"
      "v_accvgpr_read_b32 v197, a165\n"
      "v_accvgpr_read_b32 v198, a166\n"
      "v_accvgpr_read_b32 v199, a167\n"
      "v_mul_f32 v196, v196, v195\n"
      "v_mul_f32 v197, v197, v195\n"
      "v_mul_f32 v198, v198, v195\n"
      "v_mul_f32 v199, v199, v195\n"
      "v_accvgpr_write_b32 a164, v196\n"
      "v_accvgpr_write_b32 a165, v197\n"
      "v_accvgpr_write_b32 a166, v198\n"
      "v_accvgpr_write_b32 a167, v199\n"
      "v_accvgpr_read_b32 v196, a168\n"
      "v_accvgpr_read_b32 v197, a169\n"
      "v_accvgpr_read_b32 v198, a170\n"
      "v_accvgpr_read_b32 v199, a171\n"
      "v_mul_f32 v196, v196, v194\n"
      "v_mul_f32 v197, v197, v194\n"
      "v_mul_f32 v198, v198, v194\n"
      "v_mul_f32 v199, v199, v194\n"
      "v_accvgpr_write_b32 a168, v196\n"
      "v_accvgpr_write_b32 a169, v197\n"
      "v_accvgpr_write_b32 a170, v198\n"
      "v_accvgpr_write_b32 a171, v199\n"
      "v_accvgpr_read_b32 v196, a172\n"
      "v_accvgpr_read_b32 v197, a173\n"
      "v_accvgpr_read_b32 v198, a174\n"
      "v_accvgpr_read_b32 v199, a175\n"
      "v_mul_f32 v196, v196, v195\n"
      "v_mul_f32 v197, v197, v195\n"
      "v_mul_f32 v198, v198, v195\n"
      "v_mul_f32 v199, v199, v195\n"
      "v_accvgpr_write_b32 a172, v196\n"
      "v_accvgpr_write_b32 a173, v197\n"
      "v_accvgpr_write_b32 a174, v198\n"
      "v_accvgpr_write_b32 a175, v199\n"
      "v_accvgpr_read_b32 v196, a176\n"
      "v_accvgpr_read_b32 v197, a177\n"
      "v_accvgpr_read_b32 v198, a178\n"
      "v_accvgpr_read_b32 v199, a179\n"
      "v_mul_f32 v196, v196, v194\n"
      "v_mul_f32 v197, v197, v194\n"
      "v_mul_f32 v198, v198, v194\n"
      "v_mul_f32 v199, v199, v194\n"
      "v_accvgpr_write_b32 a176, v196\n"
      "v_accvgpr_write_b32 a177, v197\n"
      "v_accvgpr_write_b32 a178, v198\n"
      "v_accvgpr_write_b32 a179, v199\n"
      "v_accvgpr_read_b32 v196, a180\n"
      "v_accvgpr_read_b32 v197, a181\n"
      "v_accvgpr_read_b32 v198, a182\n"
      "v_accvgpr_read_b32 v199, a183\n"
      "v_mul_f32 v196, v196, v195\n"
      "v_mul_f32 v197, v197, v195\n"
      "v_mul_f32 v198, v198, v195\n"
      "v_mul_f32 v199, v199, v195\n"
      "v_accvgpr_write_b32 a180, v196\n"
      "v_accvgpr_write_b32 a181, v197\n"
      "v_accvgpr_write_b32 a182, v198\n"
      "v_accvgpr_write_b32 a183, v199\n"
      "v_accvgpr_read_b32 v196, a184\n"
      "v_accvgpr_read_b32 v197, a185\n"
      "v_accvgpr_read_b32 v198, a186\n"
      "v_accvgpr_read_b32 v199, a187\n"
      "v_mul_f32 v196, v196, v194\n"
      "v_mul_f32 v197, v197, v194\n"
      "v_mul_f32 v198, v198, v194\n"
      "v_mul_f32 v199, v199, v194\n"
      "v_accvgpr_write_b32 a184, v196\n"
      "v_accvgpr_write_b32 a185, v197\n"
      "v_accvgpr_write_b32 a186, v198\n"
      "v_accvgpr_write_b32 a187, v199\n"
      "v_accvgpr_read_b32 v196, a188\n"
      "v_accvgpr_read_b32 v197, a189\n"
      "v_accvgpr_read_b32 v198, a190\n"
      "v_accvgpr_read_b32 v199, a191\n"
      "v_mul_f32 v196, v196, v195\n"
      "v_mul_f32 v197, v197, v195\n"
      "v_mul_f32 v198, v198, v195\n"
      "v_mul_f32 v199, v199, v195\n"
      "v_accvgpr_write_b32 a188, v196\n"
      "v_accvgpr_write_b32 a189, v197\n"
      "v_accvgpr_write_b32 a190, v198\n"
      "v_accvgpr_write_b32 a191, v199\n"
      "v_accvgpr_read_b32 v196, a192\n"
      "v_accvgpr_read_b32 v197, a193\n"
      "v_accvgpr_read_b32 v198, a194\n"
      "v_accvgpr_read_b32 v199, a195\n"
      "v_mul_f32 v196, v196, v194\n"
      "v_mul_f32 v197, v197, v194\n"
      "v_mul_f32 v198, v198, v194\n"
      "v_mul_f32 v199, v199, v194\n"
      "v_accvgpr_write_b32 a192, v196\n"
      "v_accvgpr_write_b32 a193, v197\n"
      "v_accvgpr_write_b32 a194, v198\n"
      "v_accvgpr_write_b32 a195, v199\n"
      "v_accvgpr_read_b32 v196, a196\n"
      "v_accvgpr_read_b32 v197, a197\n"
      "v_accvgpr_read_b32 v198, a198\n"
      "v_accvgpr_read_b32 v199, a199\n"
      "v_mul_f32 v196, v196, v195\n"
      "v_mul_f32 v197, v197, v195\n"
      "v_mul_f32 v198, v198, v195\n"
      "v_mul_f32 v199, v199, v195\n"
      "v_accvgpr_write_b32 a196, v196\n"
      "v_accvgpr_write_b32 a197, v197\n"
      "v_accvgpr_write_b32 a198, v198\n"
      "v_accvgpr_write_b32 a199, v199\n"
      "v_accvgpr_read_b32 v196, a200\n"
      "v_accvgpr_read_b32 v197, a201\n"
      "v_accvgpr_read_b32 v198, a202\n"
      "v_accvgpr_read_b32 v199, a203\n"
      "v_mul_f32 v196, v196, v194\n"
      "v_mul_f32 v197, v197, v194\n"
      "v_mul_f32 v198, v198, v194\n"
      "v_mul_f32 v199, v199, v194\n"
      "v_accvgpr_write_b32 a200, v196\n"
      "v_accvgpr_write_b32 a201, v197\n"
      "v_accvgpr_write_b32 a202, v198\n"
      "v_accvgpr_write_b32 a203, v199\n"
      "v_accvgpr_read_b32 v196, a204\n"
      "v_accvgpr_read_b32 v197, a205\n"
      "v_accvgpr_read_b32 v198, a206\n"
      "v_accvgpr_read_b32 v199, a207\n"
      "v_mul_f32 v196, v196, v195\n"
      "v_mul_f32 v197, v197, v195\n"
      "v_mul_f32 v198, v198, v195\n"
      "v_mul_f32 v199, v199, v195\n"
      "v_accvgpr_write_b32 a204, v196\n"
      "v_accvgpr_write_b32 a205, v197\n"
      "v_accvgpr_write_b32 a206, v198\n"
      "v_accvgpr_write_b32 a207, v199\n"
      "v_mul_f32 v180, v180, v192\n"
      "v_mul_f32 v181, v181, v193\n"
      "v_mul_f32 v182, v182, v194\n"
      "v_mul_f32 v183, v183, v195\n"
      "v_exp_f32 v188, v64\n"
      "v_exp_f32 v189, v65\n"
      "v_exp_f32 v190, v66\n"
      "v_exp_f32 v191, v67\n"
      "v_cvt_pk_bf16_f32 v128, v188, v189\n"
      "v_cvt_pk_bf16_f32 v129, v190, v191\n"
      "v_exp_f32 v192, v68\n"
      "v_exp_f32 v193, v69\n"
      "v_exp_f32 v194, v70\n"
      "v_exp_f32 v195, v71\n"
      "v_cvt_pk_bf16_f32 v130, v192, v193\n"
      "v_cvt_pk_bf16_f32 v131, v194, v195\n"
      "v_exp_f32 v188, v72\n"
      "v_exp_f32 v189, v73\n"
      "v_exp_f32 v190, v74\n"
      "v_exp_f32 v191, v75\n"
      "v_cvt_pk_bf16_f32 v132, v188, v189\n"
      "v_cvt_pk_bf16_f32 v133, v190, v191\n"
      "v_exp_f32 v192, v76\n"
      "v_exp_f32 v193, v77\n"
      "v_exp_f32 v194, v78\n"
      "v_exp_f32 v195, v79\n"
      "v_cvt_pk_bf16_f32 v134, v192, v193\n"
      "v_cvt_pk_bf16_f32 v135, v194, v195\n"
      "v_exp_f32 v188, v80\n"
      "v_exp_f32 v189, v81\n"
      "v_exp_f32 v190, v82\n"
      "v_exp_f32 v191, v83\n"
      "v_cvt_pk_bf16_f32 v136, v188, v189\n"
      "v_cvt_pk_bf16_f32 v137, v190, v191\n"
      "v_exp_f32 v192, v84\n"
      "v_exp_f32 v193, v85\n"
      "v_exp_f32 v194, v86\n"
      "v_exp_f32 v195, v87\n"
      "v_cvt_pk_bf16_f32 v138, v192, v193\n"
      "v_cvt_pk_bf16_f32 v139, v194, v195\n"
      "v_exp_f32 v188, v88\n"
      "v_exp_f32 v189, v89\n"
      "v_exp_f32 v190, v90\n"
      "v_exp_f32 v191, v91\n"
      "v_cvt_pk_bf16_f32 v140, v188, v189\n"
      "v_cvt_pk_bf16_f32 v141, v190, v191\n"
      "v_exp_f32 v192, v92\n"
      "v_exp_f32 v193, v93\n"
      "v_exp_f32 v194, v94\n"
      "v_exp_f32 v195, v95\n"
      "v_cvt_pk_bf16_f32 v142, v192, v193\n"
      "v_cvt_pk_bf16_f32 v143, v194, v195\n"
      "s_nop 1\n"
      "v_mfma_f32_16x16x32_bf16 v[184:187], a[128:131], v[128:131], 0\n"
      "v_mfma_f32_16x16x32_bf16 v[184:187], a[132:135], v[132:135], v[184:187]\n"
      "v_mfma_f32_16x16x32_bf16 v[184:187], a[136:139], v[136:139], v[184:187]\n"
      "v_mfma_f32_16x16x32_bf16 v[184:187], a[140:143], v[140:143], v[184:187]\n"
      "s_nop 7\n"
      "s_setpc_b64 s[96:97]\n"
      "FB1_%=:\n"
      "v_mov_b32 v196, 139248\n"
      "s_nop 7\n"
      "ds_write_b32 v196, v196\n"
      "v_max3_f32 v188, v96, v97, 0\n"
      "v_max3_f32 v189, v104, v105, 0\n"
      "v_max3_f32 v190, v112, v113, 0\n"
      "v_max3_f32 v191, v120, v121, 0\n"
      "v_max3_f32 v188, v188, v98, v99\n"
      "v_max3_f32 v189, v189, v106, v107\n"
      "v_max3_f32 v190, v190, v114, v115\n"
      "v_max3_f32 v191, v191, v122, v123\n"
      "v_max3_f32 v188, v188, v100, v101\n"
      "v_max3_f32 v189, v189, v108, v109\n"
      "v_max3_f32 v190, v190, v116, v117\n"
      "v_max3_f32 v191, v191, v124, v125\n"
      "v_max3_f32 v188, v188, v102, v103\n"
      "v_max3_f32 v189, v189, v110, v111\n"
      "v_max3_f32 v190, v190, v118, v119\n"
      "v_max3_f32 v191, v191, v126, v127\n"
      "v_mov_b32 v192, v188\n"
      "v_mov_b32 v193, v189\n"
      "v_mov_b32 v194, v190\n"
      "v_mov_b32 v195, v191\n"
      "v_permlane16_swap_b32 v188, v192\n"
      "v_permlane16_swap_b32 v189, v193\n"
      "v_permlane16_swap_b32 v190, v194\n"
      "v_permlane16_swap_b32 v191, v195\n"
      "v_max_f32 v188, v188, v192\n"
      "v_max_f32 v189, v189, v193\n"
      "v_max_f32 v190, v190, v194\n"
      "v_max_f32 v191, v191, v195\n"
      "v_mov_b32 v192, v188\n"
      "v_mov_b32 v193, v189\n"
      "v_mov_b32 v194, v190\n"
      "v_mov_b32 v195, v191\n"
      "v_permlane32_swap_b32 v188, v192\n"
      "v_permlane32_swap_b32 v189, v193\n"
      "v_permlane32_swap_b32 v190, v194\n"
      "v_permlane32_swap_b32 v191, v195\n"
      "v_max_f32 v188, v188, v192\n"
      "v_max_f32 v189, v189, v193\n"
      "v_max_f32 v190, v190, v194\n"
      "v_max_f32 v191, v191, v195\n"
      "RS1_%=:\n"
      "v_exp_f32 v192, -v188\n"
      "v_exp_f32 v193, -v189\n"
      "v_exp_f32 v194, -v190\n"
      "v_exp_f32 v195, -v191\n"
      "v_sub_f32 v176, v176, v188\n"
      "v_sub_f32 v177, v177, v189\n"
      "v_sub_f32 v178, v178, v190\n"
      "v_sub_f32 v179, v179, v191\n"
      "v_mov_b32 v160, v176\n"
      "v_mov_b32 v161, v176\n"
      "v_mov_b32 v162, v176\n"
      "v_mov_b32 v163, v176\n"
      "v_mov_b32 v164, v177\n"
      "v_mov_b32 v165, v177\n"
      "v_mov_b32 v166, v177\n"
      "v_mov_b32 v167, v177\n"
      "v_mov_b32 v168, v178\n"
      "v_mov_b32 v169, v178\n"
      "v_mov_b32 v170, v178\n"
      "v_mov_b32 v171, v178\n"
      "v_mov_b32 v172, v179\n"
      "v_mov_b32 v173, v179\n"
      "v_mov_b32 v174, v179\n"
      "v_mov_b32 v175, v179\n"
      "v_sub_f32 v64, v64, v188\n"
      "v_sub_f32 v65, v65, v188\n"
      "v_sub_f32 v66, v66, v188\n"
      "v_sub_f32 v67, v67, v188\n"
      "v_sub_f32 v68, v68, v188\n"
      "v_sub_f32 v69, v69, v188\n"
      "v_sub_f32 v70, v70, v188\n"
      "v_sub_f32 v71, v71, v188\n"
      "v_sub_f32 v72, v72, v189\n"
      "v_sub_f32 v73, v73, v189\n"
      "v_sub_f32 v74, v74, v189\n"
      "v_sub_f32 v75, v75, v189\n"
      "v_sub_f32 v76, v76, v189\n"
      "v_sub_f32 v77, v77, v189\n"
      "v_sub_f32 v78, v78, v189\n"
      "v_sub_f32 v79, v79, v189\n"
      "v_sub_f32 v80, v80, v190\n"
      "v_sub_f32 v81, v81, v190\n"
      "v_sub_f32 v82, v82, v190\n"
      "v_sub_f32 v83, v83, v190\n"
      "v_sub_f32 v84, v84, v190\n"
      "v_sub_f32 v85, v85, v190\n"
      "v_sub_f32 v86, v86, v190\n"
      "v_sub_f32 v87, v87, v190\n"
      "v_sub_f32 v88, v88, v191\n"
      "v_sub_f32 v89, v89, v191\n"
      "v_sub_f32 v90, v90, v191\n"
      "v_sub_f32 v91, v91, v191\n"
      "v_sub_f32 v92, v92, v191\n"
      "v_sub_f32 v93, v93, v191\n"
      "v_sub_f32 v94, v94, v191\n"
      "v_sub_f32 v95, v95, v191\n"
      "v_sub_f32 v96, v96, v188\n"
      "v_sub_f32 v97, v97, v188\n"
      "v_sub_f32 v98, v98, v188\n"
      "v_sub_f32 v99, v99, v188\n"
      "v_sub_f32 v100, v100, v188\n"
      "v_sub_f32 v101, v101, v188\n"
      "v_sub_f32 v102, v102, v188\n"
      "v_sub_f32 v103, v103, v188\n"
      "v_sub_f32 v104, v104, v189\n"
      "v_sub_f32 v105, v105, v189\n"
      "v_sub_f32 v106, v106, v189\n"
      "v_sub_f32 v107, v107, v189\n"
      "v_sub_f32 v108, v108, v189\n"
      "v_sub_f32 v109, v109, v189\n"
      "v_sub_f32 v110, v110, v189\n"
      "v_sub_f32 v111, v111, v189\n"
      "v_sub_f32 v112, v112, v190\n"
      "v_sub_f32 v113, v113, v190\n"
      "v_sub_f32 v114, v114, v190\n"
      "v_sub_f32 v115, v115, v190\n"
      "v_sub_f32 v116, v116, v190\n"
      "v_sub_f32 v117, v117, v190\n"
      "v_sub_f32 v118, v118, v190\n"
      "v_sub_f32 v119, v119, v190\n"
      "v_sub_f32 v120, v120, v191\n"
      "v_sub_f32 v121, v121, v191\n"
      "v_sub_f32 v122, v122, v191\n"
      "v_sub_f32 v123, v123, v191\n"
      "v_sub_f32 v124, v124, v191\n"
      "v_sub_f32 v125, v125, v191\n"
      "v_sub_f32 v126, v126, v191\n"
      "v_sub_f32 v127, v127, v191\n"
      "v_mul_f32 v0, v0, v192\n"
      "v_mul_f32 v1, v1, v192\n"
      "v_mul_f32 v2, v2, v192\n"
      "v_mul_f32 v3, v3, v192\n"
      "v_mul_f32 v4, v4, v193\n"
      "v_mul_f32 v5, v5, v193\n"
      "v_mul_f32 v6, v6, v193\n"
      "v_mul_f32 v7, v7, v193\n"
      "v_mul_f32 v8, v8, v192\n"
      "v_mul_f32 v9, v9, v192\n"
      "v_mul_f32 v10, v10, v192\n"
      "v_mul_f32 v11, v11, v192\n"
      "v_mul_f32 v12, v12, v193\n"
      "v_mul_f32 v13, v13, v193\n"
      "v_mul_f32 v14, v14, v193\n"
      "v_mul_f32 v15, v15, v193\n"
      "v_mul_f32 v16, v16, v192\n"
      "v_mul_f32 v17, v17, v192\n"
      "v_mul_f32 v18, v18, v192\n"
      "v_mul_f32 v19, v19, v192\n"
      "v_mul_f32 v20, v20, v193\n"
      "v_mul_f32 v21, v21, v193\n"
      "v_mul_f32 v22, v22, v193\n"
      "v_mul_f32 v23, v23, v193\n"
      "v_mul_f32 v24, v24, v192\n"
      "v_mul_f32 v25, v25, v192\n"
      "v_mul_f32 v26, v26, v192\n"
      "v_mul_f32 v27, v27, v192\n"
      "v_mul_f32 v28, v28, v193\n"
      "v_mul_f32 v29, v29, v193\n"
      "v_mul_f32 v30, v30, v193\n"
      "v_mul_f32 v31, v31, v193\n"
      "v_mul_f32 v32, v32, v192\n"
      "v_mul_f32 v33, v33, v192\n"
      "v_mul_f32 v34, v34, v192\n"
      "v_mul_f32 v35, v35, v192\n"
      "v_mul_f32 v36, v36, v193\n"
      "v_mul_f32 v37, v37, v193\n"
      "v_mul_f32 v38, v38, v193\n"
      "v_mul_f32 v39, v39, v193\n"
      "v_mul_f32 v40, v40, v192\n"
      "v_mul_f32 v41, v41, v192\n"
      "v_mul_f32 v42, v42, v192\n"
      "v_mul_f32 v43, v43, v192\n"
      "v_mul_f32 v44, v44, v193\n"
      "v_mul_f32 v45, v45, v193\n"
      "v_mul_f32 v46, v46, v193\n"
      "v_mul_f32 v47, v47, v193\n"
      "v_mul_f32 v48, v48, v192\n"
      "v_mul_f32 v49, v49, v192\n"
      "v_mul_f32 v50, v50, v192\n"
      "v_mul_f32 v51, v51, v192\n"
      "v_mul_f32 v52, v52, v193\n"
      "v_mul_f32 v53, v53, v193\n"
      "v_mul_f32 v54, v54, v193\n"
      "v_mul_f32 v55, v55, v193\n"
      "v_mul_f32 v56, v56, v192\n"
      "v_mul_f32 v57, v57, v192\n"
      "v_mul_f32 v58, v58, v192\n"
      "v_mul_f32 v59, v59, v192\n"
      "v_mul_f32 v60, v60, v193\n"
      "v_mul_f32 v61, v61, v193\n"
      "v_mul_f32 v62, v62, v193\n"
      "v_mul_f32 v63, v63, v193\n"
      "v_accvgpr_read_b32 v196, a144\n"
      "v_accvgpr_read_b32 v197, a145\n"
      "v_accvgpr_read_b32 v198, a146\n"
      "v_accvgpr_read_b32 v199, a147\n"
      "v_mul_f32 v196, v196, v194\n"
      "v_mul_f32 v197, v197, v194\n"
      "v_mul_f32 v198, v198, v194\n"
      "v_mul_f32 v199, v199, v194\n"
      "v_accvgpr_write_b32 a144, v196\n"
      "v_accvgpr_write_b32 a145, v197\n"
      "v_accvgpr_write_b32 a146, v198\n"
      "v_accvgpr_write_b32 a147, v199\n"
      "v_accvgpr_read_b32 v196, a148\n"
      "v_accvgpr_read_b32 v197, a149\n"
      "v_accvgpr_read_b32 v198, a150\n"
      "v_accvgpr_read_b32 v199, a151\n"
      "v_mul_f32 v196, v196, v195\n"
      "v_mul_f32 v197, v197, v195\n"
      "v_mul_f32 v198, v198, v195\n"
      "v_mul_f32 v199, v199, v195\n"
      "v_accvgpr_write_b32 a148, v196\n"
      "v_accvgpr_write_b32 a149, v197\n"
      "v_accvgpr_write_b32 a150, v198\n"
      "v_accvgpr_write_b32 a151, v199\n"
      "v_accvgpr_read_b32 v196, a152\n"
      "v_accvgpr_read_b32 v197, a153\n"
      "v_accvgpr_read_b32 v198, a154\n"
      "v_accvgpr_read_b32 v199, a155\n"
      "v_mul_f32 v196, v196, v194\n"
      "v_mul_f32 v197, v197, v194\n"
      "v_mul_f32 v198, v198, v194\n"
      "v_mul_f32 v199, v199, v194\n"
      "v_accvgpr_write_b32 a152, v196\n"
      "v_accvgpr_write_b32 a153, v197\n"
      "v_accvgpr_write_b32 a154, v198\n"
      "v_accvgpr_write_b32 a155, v199\n"
      "v_accvgpr_read_b32 v196, a156\n"
      "v_accvgpr_read_b32 v197, a157\n"
      "v_accvgpr_read_b32 v198, a158\n"
      "v_accvgpr_read_b32 v199, a159\n"
      "v_mul_f32 v196, v196, v195\n"
      "v_mul_f32 v197, v197, v195\n"
      "v_mul_f32 v198, v198, v195\n"
      "v_mul_f32 v199, v199, v195\n"
      "v_accvgpr_write_b32 a156, v196\n"
      "v_accvgpr_write_b32 a157, v197\n"
      "v_accvgpr_write_b32 a158, v198\n"
      "v_accvgpr_write_b32 a159, v199\n"
      "v_accvgpr_read_b32 v196, a160\n"
      "v_accvgpr_read_b32 v197, a161\n"
      "v_accvgpr_read_b32 v198, a162\n"
      "v_accvgpr_read_b32 v199, a163\n"
      "v_mul_f32 v196, v196, v194\n"
      "v_mul_f32 v197, v197, v194\n"
      "v_mul_f32 v198, v198, v194\n"
      "v_mul_f32 v199, v199, v194\n"
      "v_accvgpr_write_b32 a160, v196\n"
      "v_accvgpr_write_b32 a161, v197\n"
      "v_accvgpr_write_b32 a162, v198\n"
      "v_accvgpr_write_b32 a163, v199\n"
      "v_accvgpr_read_b32 v196, a164\n"
      "v_accvgpr_read_b32 v197, a165\n"
      "v_accvgpr_read_b32 v198, a166\n"
      "v_accvgpr_read_b32 v199, a167\n"
      "v_mul_f32 v196, v196, v195\n"
      "v_mul_f32 v197, v197, v195\n"
      "v_mul_f32 v198, v198, v195\n"
      "v_mul_f32 v199, v199, v195\n"
      "v_accvgpr_write_b32 a164, v196\n"
      "v_accvgpr_write_b32 a165, v197\n"
      "v_accvgpr_write_b32 a166, v198\n"
      "v_accvgpr_write_b32 a167, v199\n"
      "v_accvgpr_read_b32 v196, a168\n"
      "v_accvgpr_read_b32 v197, a169\n"
      "v_accvgpr_read_b32 v198, a170\n"
      "v_accvgpr_read_b32 v199, a171\n"
      "v_mul_f32 v196, v196, v194\n"
      "v_mul_f32 v197, v197, v194\n"
      "v_mul_f32 v198, v198, v194\n"
      "v_mul_f32 v199, v199, v194\n"
      "v_accvgpr_write_b32 a168, v196\n"
      "v_accvgpr_write_b32 a169, v197\n"
      "v_accvgpr_write_b32 a170, v198\n"
      "v_accvgpr_write_b32 a171, v199\n"
      "v_accvgpr_read_b32 v196, a172\n"
      "v_accvgpr_read_b32 v197, a173\n"
      "v_accvgpr_read_b32 v198, a174\n"
      "v_accvgpr_read_b32 v199, a175\n"
      "v_mul_f32 v196, v196, v195\n"
      "v_mul_f32 v197, v197, v195\n"
      "v_mul_f32 v198, v198, v195\n"
      "v_mul_f32 v199, v199, v195\n"
      "v_accvgpr_write_b32 a172, v196\n"
      "v_accvgpr_write_b32 a173, v197\n"
      "v_accvgpr_write_b32 a174, v198\n"
      "v_accvgpr_write_b32 a175, v199\n"
      "v_accvgpr_read_b32 v196, a176\n"
      "v_accvgpr_read_b32 v197, a177\n"
      "v_accvgpr_read_b32 v198, a178\n"
      "v_accvgpr_read_b32 v199, a179\n"
      "v_mul_f32 v196, v196, v194\n"
      "v_mul_f32 v197, v197, v194\n"
      "v_mul_f32 v198, v198, v194\n"
      "v_mul_f32 v199, v199, v194\n"
      "v_accvgpr_write_b32 a176, v196\n"
      "v_accvgpr_write_b32 a177, v197\n"
      "v_accvgpr_write_b32 a178, v198\n"
      "v_accvgpr_write_b32 a179, v199\n"
      "v_accvgpr_read_b32 v196, a180\n"
      "v_accvgpr_read_b32 v197, a181\n"
      "v_accvgpr_read_b32 v198, a182\n"
      "v_accvgpr_read_b32 v199, a183\n"
      "v_mul_f32 v196, v196, v195\n"
      "v_mul_f32 v197, v197, v195\n"
      "v_mul_f32 v198, v198, v195\n"
      "v_mul_f32 v199, v199, v195\n"
      "v_accvgpr_write_b32 a180, v196\n"
      "v_accvgpr_write_b32 a181, v197\n"
      "v_accvgpr_write_b32 a182, v198\n"
      "v_accvgpr_write_b32 a183, v199\n"
      "v_accvgpr_read_b32 v196, a184\n"
      "v_accvgpr_read_b32 v197, a185\n"
      "v_accvgpr_read_b32 v198, a186\n"
      "v_accvgpr_read_b32 v199, a187\n"
      "v_mul_f32 v196, v196, v194\n"
      "v_mul_f32 v197, v197, v194\n"
      "v_mul_f32 v198, v198, v194\n"
      "v_mul_f32 v199, v199, v194\n"
      "v_accvgpr_write_b32 a184, v196\n"
      "v_accvgpr_write_b32 a185, v197\n"
      "v_accvgpr_write_b32 a186, v198\n"
      "v_accvgpr_write_b32 a187, v199\n"
      "v_accvgpr_read_b32 v196, a188\n"
      "v_accvgpr_read_b32 v197, a189\n"
      "v_accvgpr_read_b32 v198, a190\n"
      "v_accvgpr_read_b32 v199, a191\n"
      "v_mul_f32 v196, v196, v195\n"
      "v_mul_f32 v197, v197, v195\n"
      "v_mul_f32 v198, v198, v195\n"
      "v_mul_f32 v199, v199, v195\n"
      "v_accvgpr_write_b32 a188, v196\n"
      "v_accvgpr_write_b32 a189, v197\n"
      "v_accvgpr_write_b32 a190, v198\n"
      "v_accvgpr_write_b32 a191, v199\n"
      "v_accvgpr_read_b32 v196, a192\n"
      "v_accvgpr_read_b32 v197, a193\n"
      "v_accvgpr_read_b32 v198, a194\n"
      "v_accvgpr_read_b32 v199, a195\n"
      "v_mul_f32 v196, v196, v194\n"
      "v_mul_f32 v197, v197, v194\n"
      "v_mul_f32 v198, v198, v194\n"
      "v_mul_f32 v199, v199, v194\n"
      "v_accvgpr_write_b32 a192, v196\n"
      "v_accvgpr_write_b32 a193, v197\n"
      "v_accvgpr_write_b32 a194, v198\n"
      "v_accvgpr_write_b32 a195, v199\n"
      "v_accvgpr_read_b32 v196, a196\n"
      "v_accvgpr_read_b32 v197, a197\n"
      "v_accvgpr_read_b32 v198, a198\n"
      "v_accvgpr_read_b32 v199, a199\n"
      "v_mul_f32 v196, v196, v195\n"
      "v_mul_f32 v197, v197, v195\n"
      "v_mul_f32 v198, v198, v195\n"
      "v_mul_f32 v199, v199, v195\n"
      "v_accvgpr_write_b32 a196, v196\n"
      "v_accvgpr_write_b32 a197, v197\n"
      "v_accvgpr_write_b32 a198, v198\n"
      "v_accvgpr_write_b32 a199, v199\n"
      "v_accvgpr_read_b32 v196, a200\n"
      "v_accvgpr_read_b32 v197, a201\n"
      "v_accvgpr_read_b32 v198, a202\n"
      "v_accvgpr_read_b32 v199, a203\n"
      "v_mul_f32 v196, v196, v194\n"
      "v_mul_f32 v197, v197, v194\n"
      "v_mul_f32 v198, v198, v194\n"
      "v_mul_f32 v199, v199, v194\n"
      "v_accvgpr_write_b32 a200, v196\n"
      "v_accvgpr_write_b32 a201, v197\n"
      "v_accvgpr_write_b32 a202, v198\n"
      "v_accvgpr_write_b32 a203, v199\n"
      "v_accvgpr_read_b32 v196, a204\n"
      "v_accvgpr_read_b32 v197, a205\n"
      "v_accvgpr_read_b32 v198, a206\n"
      "v_accvgpr_read_b32 v199, a207\n"
      "v_mul_f32 v196, v196, v195\n"
      "v_mul_f32 v197, v197, v195\n"
      "v_mul_f32 v198, v198, v195\n"
      "v_mul_f32 v199, v199, v195\n"
      "v_accvgpr_write_b32 a204, v196\n"
      "v_accvgpr_write_b32 a205, v197\n"
      "v_accvgpr_write_b32 a206, v198\n"
      "v_accvgpr_write_b32 a207, v199\n"
      "v_mul_f32 v180, v180, v192\n"
      "v_mul_f32 v181, v181, v193\n"
      "v_mul_f32 v182, v182, v194\n"
      "v_mul_f32 v183, v183, v195\n"
      "v_lshlrev_b32 v196, 16, v128\n"
      "v_and_b32 v197, 0xffff0000, v128\n"
      "v_mul_f32 v196, v196, v192\n"
      "v_mul_f32 v197, v197, v192\n"
      "v_cvt_pk_bf16_f32 v128, v196, v197\n"
      "v_lshlrev_b32 v196, 16, v129\n"
      "v_and_b32 v197, 0xffff0000, v129\n"
      "v_mul_f32 v196, v196, v192\n"
      "v_mul_f32 v197, v197, v192\n"
      "v_cvt_pk_bf16_f32 v129, v196, v197\n"
      "v_lshlrev_b32 v196, 16, v130\n"
      "v_and_b32 v197, 0xffff0000, v130\n"
      "v_mul_f32 v196, v196, v192\n"
      "v_mul_f32 v197, v197, v192\n"
      "v_cvt_pk_bf16_f32 v130, v196, v197\n"
      "v_lshlrev_b32 v196, 16, v131\n"
      "v_and_b32 v197, 0xffff0000, v131\n"
      "v_mul_f32 v196, v196, v192\n"
      "v_mul_f32 v197, v197, v192\n"
      "v_cvt_pk_bf16_f32 v131, v196, v197\n"
      "v_lshlrev_b32 v196, 16, v132\n"
      "v_and_b32 v197, 0xffff0000, v132\n"
      "v_mul_f32 v196, v196, v193\n"
      "v_mul_f32 v197, v197, v193\n"
      "v_cvt_pk_bf16_f32 v132, v196, v197\n"
      "v_lshlrev_b32 v196, 16, v133\n"
      "v_and_b32 v197, 0xffff0000, v133\n"
      "v_mul_f32 v196, v196, v193\n"
      "v_mul_f32 v197, v197, v193\n"
      "v_cvt_pk_bf16_f32 v133, v196, v197\n"
      "v_lshlrev_b32 v196, 16, v134\n"
      "v_and_b32 v197, 0xffff0000, v134\n"
      "v_mul_f32 v196, v196, v193\n"
      "v_mul_f32 v197, v197, v193\n"
      "v_cvt_pk_bf16_f32 v134, v196, v197\n"
      "v_lshlrev_b32 v196, 16, v135\n"
      "v_and_b32 v197, 0xffff0000, v135\n"
      "v_mul_f32 v196, v196, v193\n"
      "v_mul_f32 v197, v197, v193\n"
      "v_cvt_pk_bf16_f32 v135, v196, v197\n"
      "v_lshlrev_b32 v196, 16, v136\n"
      "v_and_b32 v197, 0xffff0000, v136\n"
      "v_mul_f32 v196, v196, v194\n"
      "v_mul_f32 v197, v197, v194\n"
      "v_cvt_pk_bf16_f32 v136, v196, v197\n"
      "v_lshlrev_b32 v196, 16, v137\n"
      "v_and_b32 v197, 0xffff0000, v137\n"
      "v_mul_f32 v196, v196, v194\n"
      "v_mul_f32 v197, v197, v194\n"
      "v_cvt_pk_bf16_f32 v137, v196, v197\n"
      "v_lshlrev_b32 v196, 16, v138\n"
      "v_and_b32 v197, 0xffff0000, v138\n"
      "v_mul_f32 v196, v196, v194\n"
      "v_mul_f32 v197, v197, v194\n"
      "v_cvt_pk_bf16_f32 v138, v196, v197\n"
      "v_lshlrev_b32 v196, 16, v139\n"
      "v_and_b32 v197, 0xffff0000, v139\n"
      "v_mul_f32 v196, v196, v194\n"
      "v_mul_f32 v197, v197, v194\n"
      "v_cvt_pk_bf16_f32 v139, v196, v197\n"
      "v_lshlrev_b32 v196, 16, v140\n"
      "v_and_b32 v197, 0xffff0000, v140\n"
      "v_mul_f32 v196, v196, v195\n"
      "v_mul_f32 v197, v197, v195\n"
      "v_cvt_pk_bf16_f32 v140, v196, v197\n"
      "v_lshlrev_b32 v196, 16, v141\n"
      "v_and_b32 v197, 0xffff0000, v141\n"
      "v_mul_f32 v196, v196, v195\n"
      "v_mul_f32 v197, v197, v195\n"
      "v_cvt_pk_bf16_f32 v141, v196, v197\n"
      "v_lshlrev_b32 v196, 16, v142\n"
      "v_and_b32 v197, 0xffff0000, v142\n"
      "v_mul_f32 v196, v196, v195\n"
      "v_mul_f32 v197, v197, v195\n"
      "v_cvt_pk_bf16_f32 v142, v196, v197\n"
      "v_lshlrev_b32 v196, 16, v143\n"
      "v_and_b32 v197, 0xffff0000, v143\n"
      "v_mul_f32 v196, v196, v195\n"
      "v_mul_f32 v197, v197, v195\n"
      "v_cvt_pk_bf16_f32 v143, v196, v197\n"
      "v_exp_f32 v188, v96\n"
      "v_exp_f32 v189, v97\n"
      "v_exp_f32 v190, v98\n"
      "v_exp_f32 v191, v99\n"
      "v_cvt_pk_bf16_f32 v144, v188, v189\n"
      "v_cvt_pk_bf16_f32 v145, v190, v191\n"
      "v_exp_f32 v192, v100\n"
      "v_exp_f32 v193, v101\n"
      "v_exp_f32 v194, v102\n"
      "v_exp_f32 v195, v103\n"
      "v_cvt_pk_bf16_f32 v146, v192, v193\n"
      "v_cvt_pk_bf16_f32 v147, v194, v195\n"
      "v_exp_f32 v188, v104\n"
      "v_exp_f32 v189, v105\n"
      "v_exp_f32 v190, v106\n"
      "v_exp_f32 v191, v107\n"
      "v_cvt_pk_bf16_f32 v148, v188, v189\n"
      "v_cvt_pk_bf16_f32 v149, v190, v191\n"
      "v_exp_f32 v192, v108\n"
      "v_exp_f32 v193, v109\n"
      "v_exp_f32 v194, v110\n"
      "v_exp_f32 v195, v111\n"
      "v_cvt_pk_bf16_f32 v150, v192, v193\n"
      "v_cvt_pk_bf16_f32 v151, v194, v195\n"
      "v_exp_f32 v188, v112\n"
      "v_exp_f32 v189, v113\n"
      "v_exp_f32 v190, v114\n"
      "v_exp_f32 v191, v115\n"
      "v_cvt_pk_bf16_f32 v152, v188, v189\n"
      "v_cvt_pk_bf16_f32 v153, v190, v191\n"
      "v_exp_f32 v192, v116\n"
      "v_exp_f32 v193, v117\n"
      "v_exp_f32 v194, v118\n"
      "v_exp_f32 v195, v119\n"
      "v_cvt_pk_bf16_f32 v154, v192, v193\n"
      "v_cvt_pk_bf16_f32 v155, v194, v195\n"
      "v_exp_f32 v188, v120\n"
      "v_exp_f32 v189, v121\n"
      "v_exp_f32 v190, v122\n"
      "v_exp_f32 v191, v123\n"
      "v_cvt_pk_bf16_f32 v156, v188, v189\n"
      "v_cvt_pk_bf16_f32 v157, v190, v191\n"
      "v_exp_f32 v192, v124\n"
      "v_exp_f32 v193, v125\n"
      "v_exp_f32 v194, v126\n"
      "v_exp_f32 v195, v127\n"
      "v_cvt_pk_bf16_f32 v158, v192, v193\n"
      "v_cvt_pk_bf16_f32 v159, v194, v195\n"
      "s_nop 1\n"
      "v_mfma_f32_16x16x32_bf16 v[184:187], a[128:131], v[144:147], 0\n"
      "v_mfma_f32_16x16x32_bf16 v[184:187], a[132:135], v[148:151], v[184:187]\n"
      "v_mfma_f32_16x16x32_bf16 v[184:187], a[136:139], v[152:155], v[184:187]\n"
      "v_mfma_f32_16x16x32_bf16 v[184:187], a[140:143], v[156:159], v[184:187]\n"
      "s_nop 7\n"
      "s_setpc_b64 s[96:97]\n"
      "SLOW_%=:\n"
      "ds_write_b128 v234, v[176:179] offset:5120\n"
      "s_waitcnt lgkmcnt(0)\n"
      "s_barrier\n"
      "ds_read_b128 v[192:195], v235 offset:5120\n"
      "s_waitcnt lgkmcnt(0)\n"
      "v_sub_f32 v188, v176, v194\n"
      "v_sub_f32 v189, v177, v195\n"
      "v_sub_f32 v190, v178, v192\n"
      "v_sub_f32 v191, v179, v193\n"
      "v_max_f32 v188, 0, v188\n"
      "v_max_f32 v189, 0, v189\n"
      "v_max_f32 v190, 0, v190\n"
      "v_max_f32 v191, 0, v191\n"
      "s_call_b64 s[96:97], RS1_%=\n"
      "ds_write_b128 v232, a[176:179] offset:0\n"
      "ds_write_b128 v232, a[180:183] offset:1024\n"
      "ds_write_b128 v232, a[184:187] offset:2048\n"
      "ds_write_b128 v232, a[188:191] offset:3072\n"
      "ds_write_b128 v232, a[192:195] offset:4096\n"
      "ds_write_b128 v232, a[196:199] offset:5120\n"
      "ds_write_b128 v232, a[200:203] offset:6144\n"
      "ds_write_b128 v232, a[204:207] offset:7168\n"
      "ds_write_b128 v234, v[152:155] offset:0\n"
      "ds_write_b128 v234, v[156:159] offset:1024\n"
      "v_cndmask_b32 v229, v180, v181, s[74:75]\n"
      "v_cndmask_b32 v200, v182, v183, s[74:75]\n"
      "v_cndmask_b32 v228, v176, v177, s[74:75]\n"
      "v_cndmask_b32 v202, v186, v187, s[74:75]\n"
      "v_cndmask_b32 v201, v184, v185, s[74:75]\n"
      "v_add_f32 v200, v200, v202\n"
      "ds_write_b32 v234, v200 offset:4096\n"
      "v_add_f32 v229, v229, v201\n"
      "s_waitcnt lgkmcnt(0)\n"
      "s_barrier\n"
      "ds_read_b32 v208, v209\n"
      "ds_read_b128 v[160:163], v235 offset:0\n"
      "ds_read_b128 v[164:167], v235 offset:1024\n"
      "ds_read_b128 v[64:67], v233 offset:0\n"
      "ds_read_b128 v[68:71], v233 offset:1024\n"
      "ds_read_b128 v[72:75], v233 offset:2048\n"
      "ds_read_b128 v[76:79], v233 offset:3072\n"
      "ds_read_b128 v[80:83], v233 offset:4096\n"
      "ds_read_b128 v[84:87], v233 offset:5120\n"
      "ds_read_b128 v[88:91], v233 offset:6144\n"
      "ds_read_b128 v[92:95], v233 offset:7168\n"
      "ds_read_b32 v204, v235 offset:4096\n"
      "s_branch AGAIN_%=\n"
      ".subsection 0\n"
      : "+{v0}"(tid)
      : [blk] "s"(blockIdx.x), [kp] "s"(__builtin_amdgcn_kernarg_segment_ptr()), "s"(lds0)
      : "v1", "v2", "v3", "v4", "v5", "v6", "v7", "v8", "v9", "v10", "v11", "v12", "v13", "v14", "v15", "v16", "v17", "v18", "v19", "v20", "v21", "v22", "v23", "v24", "v25", "v26", "v27", "v28", "v29", "v30", "v31", "v32", "v33", "v34", "v35", "v36", "v37", "v38", "v39", "v40", "v41", "v42", "v43", "v44", "v45", "v46", "v47", "v48", "v49", "v50", "v51", "v52", "v53", "v54", "v55", "v56", "v57", "v58", "v59", "v60", "v61", "v62", "v63", "v64", "v65", "v66", "v67", "v68", "v69", "v70", "v71", "v72", "v73", "v74", "v75", "v76", "v77", "v78", "v79", "v80", "v81", "v82", "v83", "v84", "v85", "v86", "v87", "v88", "v89", "v90", "v91", "v92", "v93", "v94", "v95", "v96", "v97", "v98", "v99", "v100", "v101", "v102", "v103", "v104", "v105", "v106", "v107", "v108", "v109", "v110", "v111", "v112", "v113", "v114", "v115", "v116", "v117", "v118", "v119", "v120", "v121", "v122", "v123", "v124", "v125", "v126", "v127", "v128", "v129", "v130", "v131", "v132", "v133", "v134", "v135", "v136", "v137", "v138", "v139", "v140", "v141", "v142", "v143", "v144", "v145", "v146", "v147", "v148", "v149", "v150", "v151", "v152", "v153", "v154", "v155", "v156", "v157", "v158", "v159", "v160", "v161", "v162", "v163", "v164", "v165", "v166", "v167", "v168", "v169", "v170", "v171", "v172", "v173", "v174", "v175", "v176", "v177", "v178", "v179", "v180", "v181", "v182", "v183", "v184", "v185", "v186", "v187", "v188", "v189", "v190", "v191", "v192", "v193", "v194", "v195", "v196", "v197", "v198", "v199", "v200", "v201", "v202", "v203", "v204", "v205", "v206", "v207", "v208", "v209", "v210", "v211", "v212", "v213", "v214", "v215", "v216", "v217", "v218", "v219", "v220", "v221", "v222", "v223", "v224", "v225", "v226", "v227", "v228", "v229", "v230", "v231", "v232", "v233", "v234", "v235", "v236", "v237", "v238", "v239", "v240", "v241", "v242", "v243", "v244", "v245", "v246", "v247", "v248", "v249", "v250", "v251", "a0", "a1", "a2", "a3", "a4", "a5", "a6", "a7", "a8", "a9", "a10", "a11", "a12", "a13", "a14", "a15", "a16", "a17", "a18", "a19", "a20", "a21", "a22", "a23", "a24", "a25", "a26", "a27", "a28", "a29", "a30", "a31", "a32", "a33", "a34", "a35", "a36", "a37", "a38", "a39", "a40", "a41", "a42", "a43", "a44", "a45", "a46", "a47", "a48", "a49", "a50", "a51", "a52", "a53", "a54", "a55", "a56", "a57", "a58", "a59", "a60", "a61", "a62", "a63", "a64", "a65", "a66", "a67", "a68", "a69", "a70", "a71", "a72", "a73", "a74", "a75", "a76", "a77", "a78", "a79", "a80", "a81", "a82", "a83", "a84", "a85", "a86", "a87", "a88", "a89", "a90", "a91", "a92", "a93", "a94", "a95", "a96", "a97", "a98", "a99", "a100", "a101", "a102", "a103", "a104", "a105", "a106", "a107", "a108", "a109", "a110", "a111", "a112", "a113", "a114", "a115", "a116", "a117", "a118", "a119", "a120", "a121", "a122", "a123", "a124", "a125", "a126", "a127", "a128", "a129", "a130", "a131", "a132", "a133", "a134", "a135", "a136", "a137", "a138", "a139", "a140", "a141", "a142", "a143", "a144", "a145", "a146", "a147", "a148", "a149", "a150", "a151", "a152", "a153", "a154", "a155", "a156", "a157", "a158", "a159", "a160", "a161", "a162", "a163", "a164", "a165", "a166", "a167", "a168", "a169", "a170", "a171", "a172", "a173", "a174", "a175", "a176", "a177", "a178", "a179", "a180", "a181", "a182", "a183", "a184", "a185", "a186", "a187", "a188", "a189", "a190", "a191", "a192", "a193", "a194", "a195", "a196", "a197", "a198", "a199", "a200", "a201", "a202", "a203", "a204", "a205", "a206", "a207", "s4", "s5", "s6", "s7", "s8", "s9", "s10", "s11", "s12", "s13", "s14", "s15", "s16", "s17", "s18", "s19", "s20", "s21", "s22", "s23", "s24", "s25", "s26", "s27", "s28", "s29", "s30", "s31", "s32", "s33", "s34", "s35", "s38", "s39", "s40", "s43", "s44", "s45", "s46", "s47", "s50", "s51", "s52", "s53", "s54", "s55", "s56", "s57", "s58", "s59", "s60", "s61", "s62", "s63", "s64", "s65", "s66", "s67", "s68", "s69", "s70", "s71", "s72", "s74", "s75", "s76", "s77", "s78", "s79", "s80", "s81", "s82", "s83", "s84", "s85", "s86", "s87", "s88", "s89", "s90", "s91", "s92", "s93", "s94", "s95", "s96", "s97", "vcc", "scc", "memory");
}

}

extern "C" void kernel_launch(void* const* d_in, const int* in_sizes, int n_in, void* d_out, int out_size,
                              void* d_ws, size_t ws_size, hipStream_t stream) {
  if (n_in != 6 || ws_size < WS_END) return;
  const __hip_bfloat16* q = (const __hip_bfloat16*)d_in[0];
  const __hip_bfloat16* kc = (const __hip_bfloat16*)d_in[1];
  const __hip_bfloat16* vc = (const __hip_bfloat16*)d_in[2];
  const __hip_bfloat16* kt = (const __hip_bfloat16*)d_in[3];
  const __hip_bfloat16* vt = (const __hip_bfloat16*)d_in[4];
  const __hip_bfloat16* tm = (const __hip_bfloat16*)d_in[5];
  hipLaunchKernelGGL(tree_attn_main, dim3(B_ * HKV_ * 2), dim3(256), 0, stream, q, kc, vc, kt, vt, tm,
                     (__hip_bfloat16*)d_out, (char*)d_ws);
}
```

```cpp
#include <hip/hip_runtime.h>
#include <hip/hip_bf16.h>
#include <stdint.h>

#pragma clang diagnostic ignored "-Wint-to-pointer-cast"
#pragma clang diagnostic ignored "-Winline-asm"

namespace {

constexpr int B_ = 16, HKV_ = 8, D_ = 128;
constexpr int SLOT = 64 * D_ * 2;
constexpr int LDS_BYTES = 10 * SLOT;

constexpr size_t WS_EPOCH = 0;
constexpr size_t WS_REF = WS_EPOCH + 128 * 4 * 128;
constexpr size_t WS_X = WS_REF + 16384;
constexpr size_t XW_BYTES = 4608;
constexpr size_t WS_END = WS_X + (size_t)128 * 2 * 4 * XW_BYTES;

__global__ __launch_bounds__(256, 1)
void tree_attn_main(const __hip_bfloat16*, const __hip_bfloat16*, const __hip_bfloat16*, const __hip_bfloat16*,
                    const __hip_bfloat16*, const __hip_bfloat16*, __hip_bfloat16*, char*) {
  __shared__ __attribute__((aligned(1024))) char smem[LDS_BYTES];
  const unsigned lds0 = (unsigned)(uintptr_t)smem;
  unsigned tid = threadIdx.x;
  asm volatile(
      "s_load_dwordx16 s[4:19], %[kp], 0x0\n"
      "s_getpc_b64 s[60:61]\n"
      "v_lshlrev_b32 v241, 4, v0\n"
      "s_lshl_b32 s62, %[blk], 9\n"
      "s_and_b32 s62, s62, 0x3000\n"
      "s_add_u32 s60, s60, s62\n"
      "s_addc_u32 s61, s61, 0\n"
      "global_load_dwordx4 v[236:239], v241, s[60:61]\n"
      "v_and_b32 v1, 0xc0, v0\n"
      "v_lshlrev_b32 v248, 1, v0\n"
      "v_readfirstlane_b32 s50, v1\n"
      "v_and_b32 v248, 0x60, v248\n"
      "v_and_b32 v249, 0x70, v241\n"
      "v_xor_b32 v248, v248, v249\n"
      "v_lshl_or_b32 v248, v1, 6, v248\n"
      "v_and_b32 v1, 56, v0\n"
      "v_lshl_or_b32 v248, v1, 5, v248\n"
      "s_mov_b32 s64, 0x11100100\n"
      "s_mov_b32 s65, 0x51504140\n"
      "s_lshl_b32 s66, %[blk], 3\n"
      "s_lshr_b64 s[64:65], s[64:65], s66\n"
      "s_mov_b32 s66, 0x6020400\n"
      "s_mov_b32 s67, 0x86828480\n"
      "s_and_b32 s65, %[blk], 56\n"
      "s_lshr_b64 s[66:67], s[66:67], s65\n"
      "s_or_b32 s64, s64, s66\n"
      "s_lshr_b32 s65, %[blk], 3\n"
      "s_and_b32 s65, s65, 24\n"
      "s_lshr_b32 s65, 0x28082000, s65\n"
      "s_or_b32 s64, s64, s65\n"
      "s_and_b32 s63, s64, 0x80\n"
      "s_and_b32 s44, s64, 127\n"
      "v_or_b32 v248, s63, v248\n"
      "v_xor_b32 v249, 0x80, v248\n"
      "s_lshl_b32 s64, s44, 20\n"
      "s_lshl_b32 s62, s44, 15\n"
      "s_lshl_b32 s50, s50, 6\n"
      "s_add_u32 m0, s50, 131072\n"
      "s_mov_b32 s58, 0x120000\n"
      "s_mov_b32 s59, 0x20000\n"
      "s_movk_i32 s22, 0x4000\n"
      "s_waitcnt lgkmcnt(0)\n"
      "s_add_u32 s56, s4, s62\n"
      "s_addc_u32 s57, s5, 0\n"
      "buffer_load_dwordx4 v248, s[56:59], 0 offen nt lds\n"
      "buffer_load_dwordx4 v248, s[56:59], 0 offen offset:2048 nt lds\n"
      "s_bitset1_b32 m0, 10\n"
      "s_memrealtime s[82:83]\n"
      "s_add_u32 s20, s6, s64\n"
      "s_addc_u32 s21, s7, 0\n"
      "s_add_u32 s32, s8, s64\n"
      "s_addc_u32 s33, s9, 0\n"
      "s_load_dword s92, s[20:21], 0x0\n"
      "s_load_dword s92, s[10:11], 0x0\n"
      "s_load_dword s92, s[32:33], 0x0\n"
      "buffer_load_dwordx4 v249, s[56:59], 0 offen nt lds\n"
      "buffer_load_dwordx4 v249, s[56:59], 0 offen offset:2048 nt lds\n"
      "s_add_u32 m0, s50, 147456\n"
      "s_load_dword s92, s[18:19], 0x0\n"
      "buffer_load_dwordx4 v248, s[56:59], s22 offen nt lds\n"
      "buffer_load_dwordx4 v248, s[56:59], s22 offen offset:2048 nt lds\n"
      "s_bitset1_b32 m0, 10\n"
      "s_nop 0\n"
      "buffer_load_dwordx4 v249, s[56:59], s22 offen nt lds\n"
      "buffer_load_dwordx4 v249, s[56:59], s22 offen offset:2048 nt lds\n"
      "s_mov_b64 s[56:57], s[20:21]\n"
      "s_load_dword s92, s[14:15], 0x0\n"
      "s_load_dword s92, s[12:13], 0x0\n"
      "s_lshr_b32 s74, s50, 2\n"
      "s_lshr_b32 s75, s50, 5\n"
      "s_xor_b32 s75, s75, s63\n"
      "s_and_b32 s75, s75, 0x80\n"
      "s_and_b32 s76, s74, 0x800\n"
      "s_add_u32 s75, s75, s76\n"
      "s_add_u32 s70, s63, s50\n"
      "s_sub_u32 s70, s70, s75\n"
      "s_lshr_b32 s75, s50, 1\n"
      "s_lshr_b32 s71, s62, 2\n"
      "s_sub_u32 s71, s71, s75\n"
      "s_ashr_i32 s72, s71, 31\n"
      "s_mov_b32 s23, s59\n"
      "s_mov_b64 s[26:27], s[22:23]\n"
      "s_sub_u32 s20, s56, s70\n"
      "s_subb_u32 s21, s57, 0\n"
      "s_add_u32 m0, s74, 24576\n"
      "s_add_u32 s24, s10, s71\n"
      "buffer_load_dwordx4 v248, s[20:23], 0 offen nt lds\n"
      "s_addc_u32 s25, s11, s72\n"
      "s_add_u32 m0, s75, 16384\n"
      "s_nop 0\n"
      "buffer_load_dwordx4 v248, s[24:27], 0 offen nt lds\n"
      "s_bitset1_b32 m0, 10\n"
      "s_nop 0\n"
      "buffer_load_dwordx4 v249, s[24:27], 0 offen nt lds\n"
      "s_lshr_b32 s43, s63, 7\n"
      "s_lshl_b32 s76, s44, 9\n"
      "s_lshr_b32 s69, s50, 5\n"
      "s_add_u32 s76, s76, s69\n"
      "s_add_u32 s46, s18, s76\n"
      "s_addc_u32 s47, s19, 0\n"
      "s_mul_i32 s76, s43, 0x3000\n"
      "s_lshl_b32 s69, s44, 8\n"
      "s_and_b32 s69, s69, 0x7800\n"
      "s_add_u32 s14, s14, s69\n"
      "s_addc_u32 s15, s15, 0\n"
      "v_mov_b64 v[192:193], 0\n"
      "v_mov_b64 v[194:195], 0\n"
      "v_lshlrev_b32 v3, 6, v0\n"
      "v_and_b32 v3, 0x3c0, v3\n"
      "v_lshrrev_b32 v4, 1, v0\n"
      "v_and_or_b32 v211, v4, 24, v3\n"
      "v_mov_b64 v[200:201], 0\n"
      "v_mov_b64 v[202:203], 0\n"
      "v_mov_b64 v[204:205], 0\n"
      "v_mov_b64 v[206:207], 0\n"
      "s_cmp_lg_u32 s76, s50\n"
      "s_cbranch_scc1 NOMASK_%=\n"
      "global_load_dwordx2 v[200:201], v211, s[14:15] offset:0\n"
      "global_load_dwordx2 v[202:203], v211, s[14:15] offset:32\n"
      "global_load_dwordx2 v[204:205], v211, s[14:15] offset:1024\n"
      "global_load_dwordx2 v[206:207], v211, s[14:15] offset:1056\n"
      "NOMASK_%=:\n"
      "global_load_dword v208, v192, s[46:47] sc1\n"
      "s_mul_i32 s69, s43, 0x8100\n"
      "v_and_b32 v228, 0xff, v248\n"
      "v_xor_b32 v228, s69, v228\n"
      "v_bfe_i32 v2, v0, 3, 1\n"
      "v_and_b32 v2, 0x1000, v2\n"
      "v_xor_b32 v228, v228, v2\n"
      "v_bfe_i32 v2, v0, 4, 1\n"
      "v_and_b32 v2, 0x20400, v2\n"
      "v_xor_b32 v228, v228, v2\n"
      "v_bfe_i32 v2, v0, 5, 1\n"
      "v_and_b32 v2, 0x100, v2\n"
      "v_xor_b32 v228, v228, v2\n"
      "v_bfe_i32 v2, v0, 6, 1\n"
      "v_and_b32 v2, 0x400, v2\n"
      "v_xor_b32 v228, v228, v2\n"
      "v_bfe_i32 v2, v0, 7, 1\n"
      "v_and_b32 v2, 0x2000, v2\n"
      "v_xor_b32 v228, v228, v2\n"
      "v_xor_b32 v231, 0x280, v228\n"
      "v_xor_b32 v230, 0x200, v228\n"
      "v_xor_b32 v229, 0x80, v228\n"
      "v_add_u32 v228, 0xc00, v228\n"
      "v_add_u32 v229, 0x800, v229\n"
      "v_add_u32 v230, 0x400, v230\n"
      "s_sub_u32 s56, s56, 0xc00\n"
      "s_subb_u32 s57, s57, 0\n"
      "s_sub_u32 s32, s32, 0xc00\n"
      "s_subb_u32 s33, s33, 0\n"
      "s_xor_b64 s[32:33], s[32:33], s[56:57]\n"
      "s_mov_b32 s69, 0\n"
      "s_bitcmp1_b32 s44, 4\n"
      "s_cselect_b32 s66, 0x10000, 0\n"
      "s_xor_b32 s69, s69, s66\n"
      "s_bitcmp1_b32 s44, 0\n"
      "s_cselect_b32 s66, 0x40800, 0\n"
      "s_xor_b32 s69, s69, s66\n"
      "s_bitcmp1_b32 s44, 2\n"
      "s_cselect_b32 s66, 0x800, 0\n"
      "s_xor_b32 s69, s69, s66\n"
      "s_bitcmp1_b32 s44, 4\n"
      "s_cselect_b32 s66, 0x4000, 0\n"
      "s_xor_b32 s69, s69, s66\n"
      "s_mov_b32 s60, s69\n"
      "s_mov_b32 s62, s50\n"
      "s_mov_b64 s[30:31], s[22:23]\n"
      "s_add_u32 s28, s12, s71\n"
      "s_addc_u32 s29, s13, s72\n"
      "s_lshl_b32 s69, s43, 13\n"
      "s_xor_b32 s69, s69, 0x2000\n"
      "v_add_u32 v2, s69, v241\n"
      "ds_write_b128 v241, v[192:195] offset:28672\n"
      "ds_write_b128 v2, v[192:195] offset:49152\n"
      "ds_write_b128 v2, v[192:195] offset:53248\n"
      "v_and_b32 v241, 0x3f0, v241\n"
      "s_and_b32 s91, s50, 8192\n"
      "v_lshlrev_b32 v4, 8, v0\n"
      "v_and_b32 v4, 0x800, v4\n"
      "v_lshlrev_b32 v5, 7, v0\n"
      "v_and_b32 v6, 0x380, v5\n"
      "v_or3_b32 v4, v4, v6, s91\n"
      "v_and_b32 v6, 0x60, v241\n"
      "v_and_b32 v2, 48, v0\n"
      "v_xor_b32 v6, v6, v2\n"
      "v_or_b32 v242, v4, v6\n"
      "v_xor_b32 v243, 64, v242\n"
      "v_lshlrev_b32 v4, 6, v0\n"
      "v_and_b32 v6, 0x800, v4\n"
      "v_lshlrev_b32 v2, 5, v0\n"
      "v_and_b32 v2, 0x380, v2\n"
      "s_add_u32 s69, s91, 16384\n"
      "v_or3_b32 v6, v6, v2, s69\n"
      "v_lshlrev_b32 v2, 3, v0\n"
      "v_and_or_b32 v6, v2, 24, v6\n"
      "v_lshlrev_b32 v2, 2, v0\n"
      "v_and_b32 v2, 0x60, v2\n"
      "v_xor_b32 v244, v6, v2\n"
      "v_xor_b32 v245, 0x20, v244\n"
      "v_xor_b32 v246, 0x40, v244\n"
      "v_xor_b32 v247, 0x60, v244\n"
      "v_and_b32 v5, 0x1e00, v5\n"
      "v_and_or_b32 v250, v241, 48, v5\n"
      "s_lshl_b32 s69, %[blk], 9\n"
      "s_and_b32 s69, s69, 0x1e00\n"
      "v_and_b32 v4, 0x1c0, v4\n"
      "v_or_b32 v251, s69, v4\n"
      "s_and_b32 s69, s50, 4096\n"
      "s_lshl_b32 s69, s69, 2\n"
      "s_add_u32 s69, s69, 131072\n"
      "s_sub_u32 s91, 16384, s91\n"
      "v_add_u32 v196, s69, v242\n"
      "v_add_u32 v198, s91, v242\n"
      "v_xor_b32 v214, 0x2000, v196\n"
      "v_add_u32 v197, s69, v243\n"
      "v_add_u32 v199, s91, v243\n"
      "v_xor_b32 v215, 0x2000, v197\n"
      "v_and_b32 v3, 1, v0\n"
      "v_bfe_u32 v4, v0, 1, 5\n"
      "v_lshlrev_b32 v3, 1, v3\n"
      "v_sub_u32 v4, v4, v3\n"
      "v_mov_b32 v219, s60\n"
      "v_bfe_i32 v2, v4, 0, 1\n"
      "v_and_b32 v2, 0x10000, v2\n"
      "v_xor_b32 v219, v219, v2\n"
      "v_bfe_i32 v2, v4, 1, 1\n"
      "v_and_b32 v2, 0x40800, v2\n"
      "v_xor_b32 v219, v219, v2\n"
      "v_bfe_i32 v2, v4, 2, 1\n"
      "v_and_b32 v2, 0x800, v2\n"
      "v_xor_b32 v219, v219, v2\n"
      "v_bfe_i32 v2, v4, 3, 1\n"
      "v_and_b32 v2, 0x80000, v2\n"
      "v_xor_b32 v219, v219, v2\n"
      "v_bfe_i32 v2, v4, 4, 1\n"
      "v_and_b32 v2, 0x4000, v2\n"
      "v_xor_b32 v219, v219, v2\n"
      "v_writelane_b32 v219, 0, 1\n"
      "s_mov_b32 s61, 0\n"
      "s_add_u32 s69, s50, 131072\n"
      "v_add_u32 v210, s69, v241\n"
      "v_and_b32 v2, 3, v0\n"
      "v_mov_b32 v4, 0x3f803f80\n"
      "v_cmp_eq_u32 vcc, 0, v2\n"
      "s_nop 1\n"
      "v_cndmask_b32 v5, 0, v4, vcc\n"
      "v_accvgpr_write_b32 a128, v5\n"
      "v_accvgpr_write_b32 a129, v5\n"
      "v_accvgpr_write_b32 a130, v5\n"
      "v_accvgpr_write_b32 a131, v5\n"
      "v_cmp_eq_u32 vcc, 1, v2\n"
      "s_nop 1\n"
      "v_cndmask_b32 v5, 0, v4, vcc\n"
      "v_accvgpr_write_b32 a132, v5\n"
      "v_accvgpr_write_b32 a133, v5\n"
      "v_accvgpr_write_b32 a134, v5\n"
      "v_accvgpr_write_b32 a135, v5\n"
      "v_cmp_eq_u32 vcc, 2, v2\n"
      "s_nop 1\n"
      "v_cndmask_b32 v5, 0, v4, vcc\n"
      "v_accvgpr_write_b32 a136, v5\n"
      "v_accvgpr_write_b32 a137, v5\n"
      "v_accvgpr_write_b32 a138, v5\n"
      "v_accvgpr_write_b32 a139, v5\n"
      "v_cmp_eq_u32 vcc, 3, v2\n"
      "s_nop 1\n"
      "v_cndmask_b32 v5, 0, v4, vcc\n"
      "v_accvgpr_write_b32 a140, v5\n"
      "v_accvgpr_write_b32 a141, v5\n"
      "v_accvgpr_write_b32 a142, v5\n"
      "v_accvgpr_write_b32 a143, v5\n"
      "v_mfma_f32_32x32x16_bf16 a[144:159], v[192:195], v[192:195], 0\n"
      "v_mfma_f32_32x32x16_bf16 a[160:175], v[192:195], v[192:195], 0\n"
      "v_mfma_f32_32x32x16_bf16 a[176:191], v[192:195], v[192:195], 0\n"
      "v_mfma_f32_32x32x16_bf16 a[192:207], v[192:195], v[192:195], 0\n"
      "v_mfma_f32_32x32x16_bf16 v[0:15], v[192:195], v[192:195], 0\n"
      "v_mfma_f32_32x32x16_bf16 v[16:31], v[192:195], v[192:195], 0\n"
      "v_mfma_f32_32x32x16_bf16 v[32:47], v[192:195], v[192:195], 0\n"
      "v_mfma_f32_32x32x16_bf16 v[48:63], v[192:195], v[192:195], 0\n"
      "v_mfma_f32_32x32x16_bf16 v[128:143], v[192:195], v[192:195], 0\n"
      "v_mov_b64 v[180:181], 0\n"
      "v_mov_b64 v[182:183], 0\n"
      "v_mov_b64 v[184:185], 0\n"
      "v_mov_b64 v[186:187], 0\n"
      "s_mov_b32 s94, 0x3e0293ee\n"
      "s_mov_b32 s95, s94\n"
      "s_lshl_b32 s69, s43, 13\n"
      "s_add_u32 s45, s69, s75\n"
      "s_mov_b32 m0, s62\n"
      "s_nop 0\n"
      "buffer_load_dwordx4 v228, s[56:59], s60 offen nt lds\n"
      "buffer_load_dwordx4 v229, s[56:59], s60 offen offset:1024 nt lds\n"
      "buffer_load_dwordx4 v230, s[56:59], s60 offen offset:2048 nt lds\n"
      "buffer_load_dwordx4 v231, s[56:59], s60 offen offset:3072 nt lds\n"
      "s_xor_b64 s[56:57], s[56:57], s[32:33]\n"
      "s_add_u32 s61, s61, 1\n"
      "s_add_u32 s62, s62, 0x4000\n"
      "s_and_b32 s62, s62, 0x1ffff\n"
      "v_readlane_b32 s60, v219, s61\n"
      "s_xor_b64 s[56:57], s[56:57], s[32:33]\n"
      "s_add_u32 s61, s61, 1\n"
      "s_add_u32 s62, s62, 0x4000\n"
      "s_and_b32 s62, s62, 0x1ffff\n"
      "v_readlane_b32 s60, v219, s61\n"
      "s_waitcnt vmcnt(8)\n"
      "s_memrealtime s[68:69]\n"
      "ds_read_b128 v[64:67], v210 offset:0\n"
      "ds_read_b128 v[68:71], v210 offset:1024\n"
      "ds_read_b128 v[72:75], v210 offset:2048\n"
      "ds_read_b128 v[76:79], v210 offset:3072\n"
      "ds_read_b128 v[80:83], v210 offset:16384\n"
      "ds_read_b128 v[84:87], v210 offset:17408\n"
      "ds_read_b128 v[88:91], v210 offset:18432\n"
      "ds_read_b128 v[92:95], v210 offset:19456\n"
      "s_add_u32 s20, s18, 65536\n"
      "s_addc_u32 s21, s19, 0\n"
      "s_lshl_b32 s75, %[blk], 6\n"
      "s_add_u32 s75, s75, s50\n"
      "s_waitcnt lgkmcnt(0)\n"
      "s_sub_u32 s67, s68, s82\n"
      "s_min_u32 s67, s67, 165\n"
      "s_add_u32 s93, s82, s67\n"
      "s_sub_u32 s71, s68, 400\n"
      "s_cmp_lt_u32 s75, 512\n"
      "s_cbranch_scc0 NOLEAD_%=\n"
      "v_mov_b32 v188, s68\n"
      "v_mov_b32 v189, s68\n"
      "v_mov_b32 v190, s68\n"
      "v_mov_b32 v191, s68\n"
      "buffer_store_dwordx4 v[188:191], v250, s[20:23], s75 offen sc1\n"
      "s_nop 1\n"
      "NOLEAD_%=:\n"
      "s_mov_b32 m0, s62\n"
      "s_nop 0\n"
      "buffer_load_dwordx4 v228, s[56:59], s60 offen nt lds\n"
      "buffer_load_dwordx4 v229, s[56:59], s60 offen offset:1024 nt lds\n"
      "buffer_load_dwordx4 v230, s[56:59], s60 offen offset:2048 nt lds\n"
      "buffer_load_dwordx4 v231, s[56:59], s60 offen offset:3072 nt lds\n"
      "s_xor_b64 s[56:57], s[56:57], s[32:33]\n"
      "s_add_u32 s61, s61, 1\n"
      "s_add_u32 s62, s62, 0x4000\n"
      "s_and_b32 s62, s62, 0x1ffff\n"
      "v_readlane_b32 s60, v219, s61\n"
      "s_xor_b64 s[56:57], s[56:57], s[32:33]\n"
      "s_add_u32 s61, s61, 1\n"
      "s_add_u32 s62, s62, 0x4000\n"
      "s_and_b32 s62, s62, 0x1ffff\n"
      "v_readlane_b32 s60, v219, s61\n"
      "s_add_u32 m0, s45, 49152\n"
      "s_nop 0\n"
      "buffer_load_dwordx4 v248, s[28:31], 0 offen nt lds\n"
      "s_bitset1_b32 m0, 10\n"
      "s_nop 0\n"
      "buffer_load_dwordx4 v249, s[28:31], 0 offen nt lds\n"
      "v_lshlrev_b32 v188, 16, v64\n"
      "v_and_b32 v189, 0xffff0000, v64\n"
      "v_lshlrev_b32 v190, 16, v65\n"
      "v_and_b32 v191, 0xffff0000, v65\n"
      "v_lshlrev_b32 v192, 16, v66\n"
      "v_and_b32 v193, 0xffff0000, v66\n"
      "v_lshlrev_b32 v194, 16, v67\n"
      "v_and_b32 v195, 0xffff0000, v67\n"
      "v_pk_mul_f32 v[188:189], v[188:189], s[94:95]\n"
      "v_pk_mul_f32 v[190:191], v[190:191], s[94:95]\n"
      "v_pk_mul_f32 v[192:193], v[192:193], s[94:95]\n"
      "v_pk_mul_f32 v[194:195], v[194:195], s[94:95]\n"
      "v_cvt_pk_bf16_f32 v64, v188, v189\n"
      "v_cvt_pk_bf16_f32 v65, v190, v191\n"
      "v_cvt_pk_bf16_f32 v66, v192, v193\n"
      "v_cvt_pk_bf16_f32 v67, v194, v195\n"
      "ds_write_b128 v210, v[64:67] offset:0\n"
      "v_lshlrev_b32 v188, 16, v68\n"
      "v_and_b32 v189, 0xffff0000, v68\n"
      "v_lshlrev_b32 v190, 16, v69\n"
      "v_and_b32 v191, 0xffff0000, v69\n"
      "v_lshlrev_b32 v192, 16, v70\n"
      "v_and_b32 v193, 0xffff0000, v70\n"
      "v_lshlrev_b32 v194, 16, v71\n"
      "v_and_b32 v195, 0xffff0000, v71\n"
      "v_pk_mul_f32 v[188:189], v[188:189], s[94:95]\n"
      "v_pk_mul_f32 v[190:191], v[190:191], s[94:95]\n"
      "v_pk_mul_f32 v[192:193], v[192:193], s[94:95]\n"
      "v_pk_mul_f32 v[194:195], v[194:195], s[94:95]\n"
      "v_cvt_pk_bf16_f32 v68, v188, v189\n"
      "v_cvt_pk_bf16_f32 v69, v190, v191\n"
      "v_cvt_pk_bf16_f32 v70, v192, v193\n"
      "v_cvt_pk_bf16_f32 v71, v194, v195\n"
      "ds_write_b128 v210, v[68:71] offset:1024\n"
      "v_lshlrev_b32 v188, 16, v72\n"
      "v_and_b32 v189, 0xffff0000, v72\n"
      "v_lshlrev_b32 v190, 16, v73\n"
      "v_and_b32 v191, 0xffff0000, v73\n"
      "v_lshlrev_b32 v192, 16, v74\n"
      "v_and_b32 v193, 0xffff0000, v74\n"
      "v_lshlrev_b32 v194, 16, v75\n"
      "v_and_b32 v195, 0xffff0000, v75\n"
      "v_pk_mul_f32 v[188:189], v[188:189], s[94:95]\n"
      "v_pk_mul_f32 v[190:191], v[190:191], s[94:95]\n"
      "v_pk_mul_f32 v[192:193], v[192:193], s[94:95]\n"
      "v_pk_mul_f32 v[194:195], v[194:195], s[94:95]\n"
      "v_cvt_pk_bf16_f32 v72, v188, v189\n"
      "v_cvt_pk_bf16_f32 v73, v190, v191\n"
      "v_cvt_pk_bf16_f32 v74, v192, v193\n"
      "v_cvt_pk_bf16_f32 v75, v194, v195\n"
      "ds_write_b128 v210, v[72:75] offset:2048\n"
      "v_lshlrev_b32 v188, 16, v76\n"
      "v_and_b32 v189, 0xffff0000, v76\n"
      "v_lshlrev_b32 v190, 16, v77\n"
      "v_and_b32 v191, 0xffff0000, v77\n"
      "v_lshlrev_b32 v192, 16, v78\n"
      "v_and_b32 v193, 0xffff0000, v78\n"
      "v_lshlrev_b32 v194, 16, v79\n"
      "v_and_b32 v195, 0xffff0000, v79\n"
      "v_pk_mul_f32 v[188:189], v[188:189], s[94:95]\n"
      "v_pk_mul_f32 v[190:191], v[190:191], s[94:95]\n"
      "v_pk_mul_f32 v[192:193], v[192:193], s[94:95]\n"
      "v_pk_mul_f32 v[194:195], v[194:195], s[94:95]\n"
      "v_cvt_pk_bf16_f32 v76, v188, v189\n"
      "v_cvt_pk_bf16_f32 v77, v190, v191\n"
      "v_cvt_pk_bf16_f32 v78, v192, v193\n"
      "v_cvt_pk_bf16_f32 v79, v194, v195\n"
      "ds_write_b128 v210, v[76:79] offset:3072\n"
      "v_lshlrev_b32 v188, 16, v80\n"
      "v_and_b32 v189, 0xffff0000, v80\n"
      "v_lshlrev_b32 v190, 16, v81\n"
      "v_and_b32 v191, 0xffff0000, v81\n"
      "v_lshlrev_b32 v192, 16, v82\n"
      "v_and_b32 v193, 0xffff0000, v82\n"
      "v_lshlrev_b32 v194, 16, v83\n"
      "v_and_b32 v195, 0xffff0000, v83\n"
      "v_pk_mul_f32 v[188:189], v[188:189], s[94:95]\n"
      "v_pk_mul_f32 v[190:191], v[190:191], s[94:95]\n"
      "v_pk_mul_f32 v[192:193], v[192:193], s[94:95]\n"
      "v_pk_mul_f32 v[194:195], v[194:195], s[94:95]\n"
      "v_cvt_pk_bf16_f32 v80, v188, v189\n"
      "v_cvt_pk_bf16_f32 v81, v190, v191\n"
      "v_cvt_pk_bf16_f32 v82, v192, v193\n"
      "v_cvt_pk_bf16_f32 v83, v194, v195\n"
      "ds_write_b128 v210, v[80:83] offset:16384\n"
      "v_lshlrev_b32 v188, 16, v84\n"
      "v_and_b32 v189, 0xffff0000, v84\n"
      "v_lshlrev_b32 v190, 16, v85\n"
      "v_and_b32 v191, 0xffff0000, v85\n"
      "v_lshlrev_b32 v192, 16, v86\n"
      "v_and_b32 v193, 0xffff0000, v86\n"
      "v_lshlrev_b32 v194, 16, v87\n"
      "v_and_b32 v195, 0xffff0000, v87\n"
      "v_pk_mul_f32 v[188:189], v[188:189], s[94:95]\n"
      "v_pk_mul_f32 v[190:191], v[190:191], s[94:95]\n"
      "v_pk_mul_f32 v[192:193], v[192:193], s[94:95]\n"
      "v_pk_mul_f32 v[194:195], v[194:195], s[94:95]\n"
      "v_cvt_pk_bf16_f32 v84, v188, v189\n"
      "v_cvt_pk_bf16_f32 v85, v190, v191\n"
      "v_cvt_pk_bf16_f32 v86, v192, v193\n"
      "v_cvt_pk_bf16_f32 v87, v194, v195\n"
      "ds_write_b128 v210, v[84:87] offset:17408\n"
      "v_lshlrev_b32 v188, 16, v88\n"
      "v_and_b32 v189, 0xffff0000, v88\n"
      "v_lshlrev_b32 v190, 16, v89\n"
      "v_and_b32 v191, 0xffff0000, v89\n"
      "v_lshlrev_b32 v192, 16, v90\n"
      "v_and_b32 v193, 0xffff0000, v90\n"
      "v_lshlrev_b32 v194, 16, v91\n"
      "v_and_b32 v195, 0xffff0000, v91\n"
      "v_pk_mul_f32 v[188:189], v[188:189], s[94:95]\n"
      "v_pk_mul_f32 v[190:191], v[190:191], s[94:95]\n"
      "v_pk_mul_f32 v[192:193], v[192:193], s[94:95]\n"
      "v_pk_mul_f32 v[194:195], v[194:195], s[94:95]\n"
      "v_cvt_pk_bf16_f32 v88, v188, v189\n"
      "v_cvt_pk_bf16_f32 v89, v190, v191\n"
      "v_cvt_pk_bf16_f32 v90, v192, v193\n"
      "v_cvt_pk_bf16_f32 v91, v194, v195\n"
      "ds_write_b128 v210, v[88:91] offset:18432\n"
      "v_lshlrev_b32 v188, 16, v92\n"
      "v_and_b32 v189, 0xffff0000, v92\n"
      "v_lshlrev_b32 v190, 16, v93\n"
      "v_and_b32 v191, 0xffff0000, v93\n"
      "v_lshlrev_b32 v192, 16, v94\n"
      "v_and_b32 v193, 0xffff0000, v94\n"
      "v_lshlrev_b32 v194, 16, v95\n"
      "v_and_b32 v195, 0xffff0000, v95\n"
      "v_pk_mul_f32 v[188:189], v[188:189], s[94:95]\n"
      "v_pk_mul_f32 v[190:191], v[190:191], s[94:95]\n"
      "v_pk_mul_f32 v[192:193], v[192:193], s[94:95]\n"
      "v_pk_mul_f32 v[194:195], v[194:195], s[94:95]\n"
      "v_cvt_pk_bf16_f32 v92, v188, v189\n"
      "v_cvt_pk_bf16_f32 v93, v190, v191\n"
      "v_cvt_pk_bf16_f32 v94, v192, v193\n"
      "v_cvt_pk_bf16_f32 v95, v194, v195\n"
      "ds_write_b128 v210, v[92:95] offset:19456\n"
      "s_mov_b32 m0, s62\n"
      "s_nop 0\n"
      "buffer_load_dwordx4 v228, s[56:59], s60 offen nt lds\n"
      "buffer_load_dwordx4 v229, s[56:59], s60 offen offset:1024 nt lds\n"
      "buffer_load_dwordx4 v230, s[56:59], s60 offen offset:2048 nt lds\n"
      "buffer_load_dwordx4 v231, s[56:59], s60 offen offset:3072 nt lds\n"
      "s_xor_b64 s[56:57], s[56:57], s[32:33]\n"
      "s_add_u32 s61, s61, 1\n"
      "s_add_u32 s62, s62, 0x4000\n"
      "s_and_b32 s62, s62, 0x1ffff\n"
      "v_readlane_b32 s60, v219, s61\n"
      "s_cmp_eq_u32 s50, 0\n"
      "s_cselect_b32 s38, 0x41554001, 1\n"
      "s_cselect_b32 s39, 0x3f440083, 3\n"
      "s_cselect_b32 s52, 0x3133aa90, 0\n"
      "s_cselect_b32 s53, 0x80011, 0\n"
      "s_mov_b64 s[34:35], 0\n"
      "s_and_b32 s69, %[blk], 7\n"
      "s_mul_i32 s69, s69, 5\n"
      "s_lshr_b32 s74, %[blk], 4\n"
      "s_add_u32 s69, s69, s74\n"
      "s_and_b32 s69, s69, 15\n"
      "s_mul_i32 s74, s69, 20\n"
      "s_lshr_b32 s74, s74, 6\n"
      "s_lshl2_add_u32 s76, s74, 9\n"
      "s_bitset1_b64 s[34:35], s76\n"
      "s_lshl_b32 s74, s74, 1\n"
      "s_sub_u32 s74, 28, s74\n"
      "s_bitset1_b32 s39, s74\n"
      "s_mul_i32 s74, s69, 24\n"
      "s_lshr_b32 s74, s74, 6\n"
      "s_lshl2_add_u32 s76, s74, 29\n"
      "s_bitset1_b64 s[34:35], s76\n"
      "s_lshl_b32 s74, s74, 1\n"
      "s_sub_u32 s74, 18, s74\n"
      "s_bitset1_b32 s39, s74\n"
      "s_mul_i32 s74, s69, 12\n"
      "s_lshr_b32 s74, s74, 6\n"
      "s_lshl2_add_u32 s76, s74, 53\n"
      "s_bitset1_b64 s[34:35], s76\n"
      "s_lshl_b32 s74, s74, 1\n"
      "s_sub_u32 s74, 6, s74\n"
      "s_bitset1_b32 s39, s74\n"
      "s_mov_b32 s87, 0\n"
      "s_mov_b32 s55, 0\n"
      "s_mov_b32 s54, 3\n"
      "s_mov_b32 s70, 250\n"
      "s_mov_b32 s86, 59\n"
      "s_mov_b32 s77, 177\n"
      "s_lshl_b32 s91, s50, 16\n"
      "s_sub_u32 s80, 46, s91\n"
      "s_sub_u32 s90, 34, s91\n"
      "s_sub_u32 s72, s75, 14336\n"
      "s_and_b32 s79, %[blk], 31\n"
      "s_lshl_b32 s79, s79, 8\n"
      "s_add_u32 s79, s79, 8192\n"
      "s_mul_i32 s69, %[blk], 53\n"
      "s_lshr_b32 s69, s69, 8\n"
      "s_add_u32 s66, s93, 150\n"
      "s_sub_u32 s81, s93, s71\n"
      "s_sub_u32 s81, s81, s69\n"
      "s_add_u32 s81, s81, 24\n"
      "s_sub_u32 s66, s66, s91\n"
      "s_mov_b32 s88, s66\n"
      "s_mov_b32 s65, s66\n"
      "s_getpc_b64 s[84:85]\n"
      "PC_%=:\n"
      "s_add_u32 s84, s84, HOOK_%=-PC_%=\n"
      "s_addc_u32 s85, s85, 0\n"
      "s_waitcnt vmcnt(14) lgkmcnt(0)\n"
      "s_barrier\n"
      "ds_read_b128 a[64:67], v198 offset:8192\n"
      "ds_read_b128 a[72:75], v199 offset:8192\n"
      "ds_read_b128 a[80:83], v198 offset:9216\n"
      "ds_read_b128 a[88:91], v199 offset:9216\n"
      "ds_read_b128 a[0:3], v196 offset:0\n"
      "ds_read_b128 a[16:19], v197 offset:0\n"
      "ds_read_b128 a[32:35], v196 offset:1024\n"
      "ds_read_b128 a[48:51], v197 offset:1024\n"
      "ds_read_b128 a[4:7], v196 offset:4096\n"
      "ds_read_b128 a[20:23], v197 offset:4096\n"
      "ds_read_b128 a[36:39], v196 offset:5120\n"
      "ds_read_b128 a[52:55], v197 offset:5120\n"
      "v_readfirstlane_b32 s40, v208\n"
      "s_mov_b32 s94, 0xceabfb8f\n"
      "v_lshlrev_b32 v144, 16, v200\n"
      "v_and_b32 v145, 0xffff0000, v200\n"
      "v_lshlrev_b32 v146, 16, v201\n"
      "v_and_b32 v147, 0xffff0000, v201\n"
      "v_lshlrev_b32 v148, 16, v202\n"
      "v_and_b32 v149, 0xffff0000, v202\n"
      "v_lshlrev_b32 v150, 16, v203\n"
      "v_and_b32 v151, 0xffff0000, v203\n"
      "v_lshlrev_b32 v152, 16, v204\n"
      "v_and_b32 v153, 0xffff0000, v204\n"
      "v_lshlrev_b32 v154, 16, v205\n"
      "v_and_b32 v155, 0xffff0000, v205\n"
      "v_lshlrev_b32 v156, 16, v206\n"
      "v_and_b32 v157, 0xffff0000, v206\n"
      "v_lshlrev_b32 v158, 16, v207\n"
      "v_and_b32 v159, 0xffff0000, v207\n"
      "s_waitcnt lgkmcnt(4)\n"
      "v_mfma_f32_16x16x32_bf16 v[64:67], a[64:67], a[0:3], 0\n"
      "ds_read_b128 a[8:11], v214 offset:0\n"
      "v_fma_f32 v144, -v144, s94, s94\n"
      "v_mfma_f32_16x16x32_bf16 v[64:67], a[72:75], a[16:19], v[64:67]\n"
      "ds_read_b128 a[24:27], v215 offset:0\n"
      "v_fma_f32 v145, -v145, s94, s94\n"
      "v_mfma_f32_16x16x32_bf16 v[64:67], a[80:83], a[32:35], v[64:67]\n"
      "ds_read_b128 a[40:43], v214 offset:1024\n"
      "v_fma_f32 v146, -v146, s94, s94\n"
      "v_mfma_f32_16x16x32_bf16 v[64:67], a[88:91], a[48:51], v[64:67]\n"
      "ds_read_b128 a[56:59], v215 offset:1024\n"
      "v_fma_f32 v147, -v147, s94, s94\n"
      "s_waitcnt lgkmcnt(4)\n"
      "v_mfma_f32_16x16x32_bf16 v[72:75], a[64:67], a[4:7], 0\n"
      "ds_read_b128 a[12:15], v214 offset:4096\n"
      "v_fma_f32 v148, -v148, s94, s94\n"
      "v_mfma_f32_16x16x32_bf16 v[72:75], a[72:75], a[20:23], v[72:75]\n"
      "ds_read_b128 a[28:31], v215 offset:4096\n"
      "v_fma_f32 v149, -v149, s94, s94\n"
      "v_mfma_f32_16x16x32_bf16 v[72:75], a[80:83], a[36:39], v[72:75]\n"
      "ds_read_b128 a[44:47], v214 offset:5120\n"
      "v_fma_f32 v150, -v150, s94, s94\n"
      "v_mfma_f32_16x16x32_bf16 v[72:75], a[88:91], a[52:55], v[72:75]\n"
      "ds_read_b128 a[60:63], v215 offset:5120\n"
      "v_fma_f32 v151, -v151, s94, s94\n"
      "s_waitcnt lgkmcnt(4)\n"
      "v_mfma_f32_16x16x32_bf16 v[80:83], a[64:67], a[8:11], 0\n"
      "ds_read_b128 a[96:99], v198 offset:0\n"
      "v_fma_f32 v152, -v152, s94, s94\n"
      "v_mfma_f32_16x16x32_bf16 v[80:83], a[72:75], a[24:27], v[80:83]\n"
      "ds_read_b128 a[100:103], v198 offset:4096\n"
      "v_fma_f32 v153, -v153, s94, s94\n"
      "v_mfma_f32_16x16x32_bf16 v[80:83], a[80:83], a[40:43], v[80:83]\n"
      "ds_read_b128 a[104:107], v199 offset:0\n"
      "v_fma_f32 v154, -v154, s94, s94\n"
      "v_mfma_f32_16x16x32_bf16 v[80:83], a[88:91], a[56:59], v[80:83]\n"
      "ds_read_b128 a[108:111], v199 offset:4096\n"
      "v_fma_f32 v155, -v155, s94, s94\n"
      "s_waitcnt lgkmcnt(4)\n"
      "v_mfma_f32_16x16x32_bf16 v[88:91], a[64:67], a[12:15], 0\n"
      "ds_read_b128 a[112:115], v198 offset:1024\n"
      "v_fma_f32 v156, -v156, s94, s94\n"
      "v_mfma_f32_16x16x32_bf16 v[88:91], a[72:75], a[28:31], v[88:91]\n"
      "ds_read_b128 a[116:119], v198 offset:5120\n"
      "v_fma_f32 v157, -v157, s94, s94\n"
      "v_mfma_f32_16x16x32_bf16 v[88:91], a[80:83], a[44:47], v[88:91]\n"
      "ds_read_b128 a[120:123], v199 offset:1024\n"
      "v_fma_f32 v158, -v158, s94, s94\n"
      "v_mfma_f32_16x16x32_bf16 v[88:91], a[88:91], a[60:63], v[88:91]\n"
      "ds_read_b128 a[124:127], v199 offset:5120\n"
      "v_fma_f32 v159, -v159, s94, s94\n"
      "s_mov_b32 m0, s62\n"
      "s_nop 0\n"
      "buffer_load_dwordx4 v228, s[56:59], s60 offen nt lds\n"
      "buffer_load_dwordx4 v229, s[56:59], s60 offen offset:1024 nt lds\n"
      "buffer_load_dwordx4 v230, s[56:59], s60 offen offset:2048 nt lds\n"
      "buffer_load_dwordx4 v231, s[56:59], s60 offen offset:3072 nt lds\n"
      "s_waitcnt lgkmcnt(0)\n"
      "v_mfma_f32_16x16x32_bf16 v[96:99], a[96:99], a[0:3], v[144:147]\n"
      "s_add_u32 s40, s40, 0x7f4a7c15\n"
      "v_mfma_f32_16x16x32_bf16 v[96:99], a[104:107], a[16:19], v[96:99]\n"
      "s_lshl_b32 s69, s44, 3\n"
      "v_max3_f32 v188, v64, v65, v66\n"
      "v_max3_f32 v189, v72, v73, v74\n"
      "v_mfma_f32_16x16x32_bf16 v[96:99], a[112:115], a[32:35], v[96:99]\n"
      "s_lshr_b32 s74, s50, 12\n"
      "v_max3_f32 v190, v80, v81, v82\n"
      "v_max3_f32 v191, v88, v89, v90\n"
      "v_mfma_f32_16x16x32_bf16 v[96:99], a[120:123], a[48:51], v[96:99]\n"
      "s_add_u32 s69, s69, s74\n"
      "v_max_f32 v188, v188, v67\n"
      "v_max_f32 v189, v189, v75\n"
      "v_mfma_f32_16x16x32_bf16 v[104:107], a[96:99], a[4:7], v[152:155]\n"
      "s_mul_i32 s69, s69, 4608\n"
      "v_max_f32 v190, v190, v83\n"
      "v_max_f32 v191, v191, v91\n"
      "v_mfma_f32_16x16x32_bf16 v[104:107], a[104:107], a[20:23], v[104:107]\n"
      "s_add_u32 s69, s69, 81920\n"
      "v_mov_b32 v192, v188\n"
      "v_mov_b32 v193, v189\n"
      "v_mfma_f32_16x16x32_bf16 v[104:107], a[112:115], a[36:39], v[104:107]\n"
      "s_add_u32 s24, s18, s69\n"
      "v_mov_b32 v194, v190\n"
      "v_mov_b32 v195, v191\n"
      "v_mfma_f32_16x16x32_bf16 v[104:107], a[120:123], a[52:55], v[104:107]\n"
      "s_addc_u32 s25, s19, 0\n"
      "v_permlane16_swap_b32 v188, v192\n"
      "v_permlane16_swap_b32 v189, v193\n"
      "v_mfma_f32_16x16x32_bf16 v[112:115], a[96:99], a[8:11], v[144:147]\n"
      "s_movk_i32 s26, 23040\n"
      "v_permlane16_swap_b32 v190, v194\n"
      "v_permlane16_swap_b32 v191, v195\n"
      "v_mfma_f32_16x16x32_bf16 v[112:115], a[104:107], a[24:27], v[112:115]\n"
      "s_mul_i32 s78, s43, 18432\n"
      "v_max_f32 v188, v188, v192\n"
      "v_max_f32 v189, v189, v193\n"
      "v_mfma_f32_16x16x32_bf16 v[112:115], a[112:115], a[40:43], v[112:115]\n"
      "s_and_b32 s69, s50, 4096\n"
      "v_max_f32 v190, v190, v194\n"
      "v_max_f32 v191, v191, v195\n"
      "v_mfma_f32_16x16x32_bf16 v[112:115], a[120:123], a[56:59], v[112:115]\n"
      "s_lshl_b32 s69, s69, 2\n"
      "v_mov_b32 v192, v188\n"
      "v_mov_b32 v193, v189\n"
      "v_mfma_f32_16x16x32_bf16 v[120:123], a[96:99], a[12:15], v[152:155]\n"
      "s_and_b32 s74, s50, 8192\n"
      "v_mov_b32 v194, v190\n"
      "v_mov_b32 v195, v191\n"
      "v_mfma_f32_16x16x32_bf16 v[120:123], a[104:107], a[28:31], v[120:123]\n"
      "s_add_u32 s74, s74, 4096\n"
      "v_permlane32_swap_b32 v188, v192\n"
      "v_permlane32_swap_b32 v189, v193\n"
      "v_mfma_f32_16x16x32_bf16 v[120:123], a[112:115], a[44:47], v[120:123]\n"
      "s_add_u32 s69, s69, s74\n"
      "v_permlane32_swap_b32 v190, v194\n"
      "v_permlane32_swap_b32 v191, v195\n"
      "v_mfma_f32_16x16x32_bf16 v[120:123], a[120:123], a[60:63], v[120:123]\n"
      "s_lshl_b32 s74, s43, 7\n"
      "v_max_f32 v188, v188, v192\n"
      "v_max_f32 v189, v189, v193\n"
      "v_mfma_f32_16x16x32_bf16 v[100:103], a[100:103], a[0:3], v[148:151]\n"
      "s_add_u32 s69, s69, s74\n"
      "v_max_f32 v190, v190, v194\n"
      "v_max_f32 v191, v191, v195\n"
      "v_mfma_f32_16x16x32_bf16 v[100:103], a[108:111], a[16:19], v[100:103]\n"
      "s_lshl_b32 s74, s44, 15\n"
      "v_xor_b32 v176, 0x80000000, v188\n"
      "v_xor_b32 v177, 0x80000000, v189\n"
      "v_mfma_f32_16x16x32_bf16 v[100:103], a[116:119], a[32:35], v[100:103]\n"
      "s_add_u32 s69, s69, s74\n"
      "v_xor_b32 v178, 0x80000000, v190\n"
      "v_xor_b32 v179, 0x80000000, v191\n"
      "v_mfma_f32_16x16x32_bf16 v[100:103], a[124:127], a[48:51], v[100:103]\n"
      "s_add_u32 s30, s16, s69\n"
      "v_mov_b32 v160, v176\n"
      "v_mov_b32 v161, v176\n"
      "v_mfma_f32_16x16x32_bf16 v[108:111], a[100:103], a[4:7], v[156:159]\n"
      "s_addc_u32 s31, s17, 0\n"
      "v_mov_b32 v162, v176\n"
      "v_mov_b32 v163, v176\n"
      "v_mfma_f32_16x16x32_bf16 v[108:111], a[108:111], a[20:23], v[108:111]\n"
      "v_mov_b32 v164, v177\n"
      "v_mov_b32 v165, v177\n"
      "v_mfma_f32_16x16x32_bf16 v[108:111], a[116:119], a[36:39], v[108:111]\n"
      "v_mov_b32 v166, v177\n"
      "v_mov_b32 v167, v177\n"
      "v_mfma_f32_16x16x32_bf16 v[108:111], a[124:127], a[52:55], v[108:111]\n"
      "v_mov_b32 v168, v178\n"
      "v_mov_b32 v169, v178\n"
      "v_mfma_f32_16x16x32_bf16 v[116:119], a[100:103], a[8:11], v[148:151]\n"
      "v_mov_b32 v170, v178\n"
      "v_mov_b32 v171, v178\n"
      "v_mfma_f32_16x16x32_bf16 v[116:119], a[108:111], a[24:27], v[116:119]\n"
      "v_mov_b32 v172, v179\n"
      "v_mov_b32 v173, v179\n"
      "v_mfma_f32_16x16x32_bf16 v[116:119], a[116:119], a[40:43], v[116:119]\n"
      "v_mov_b32 v174, v179\n"
      "v_mov_b32 v175, v179\n"
      "v_mfma_f32_16x16x32_bf16 v[116:119], a[124:127], a[56:59], v[116:119]\n"
      "v_mov_b32 v209, 139248\n"
      "v_lshl_add_u32 v232, s50, 1, v241\n"
      "v_mfma_f32_16x16x32_bf16 v[124:127], a[100:103], a[12:15], v[156:159]\n"
      "v_add_u32 v232, 0x10000, v232\n"
      "v_xor_b32 v233, 0x4000, v232\n"
      "v_mfma_f32_16x16x32_bf16 v[124:127], a[108:111], a[28:31], v[124:127]\n"
      "v_add_u32 v234, 0x10000, v232\n"
      "v_xor_b32 v235, 0x4000, v234\n"
      "v_mfma_f32_16x16x32_bf16 v[124:127], a[116:119], a[44:47], v[124:127]\n"
      "v_lshrrev_b32 v240, 1, v241\n"
      "v_add_u32 v240, 0x1000, v240\n"
      "v_mfma_f32_16x16x32_bf16 v[124:127], a[124:127], a[60:63], v[124:127]\n"
      "v_mov_b32 v196, 0\n"
      "v_pk_add_f32 v[96:97], v[96:97], v[160:161]\n"
      "v_pk_add_f32 v[98:99], v[98:99], v[160:161]\n"
      "v_pk_add_f32 v[104:105], v[104:105], v[164:165]\n"
      "v_pk_add_f32 v[106:107], v[106:107], v[164:165]\n"
      "v_pk_add_f32 v[112:113], v[112:113], v[168:169]\n"
      "v_pk_add_f32 v[114:115], v[114:115], v[168:169]\n"
      "v_pk_add_f32 v[120:121], v[120:121], v[172:173]\n"
      "v_pk_add_f32 v[122:123], v[122:123], v[172:173]\n"
      "v_pk_add_f32 v[100:101], v[100:101], v[160:161]\n"
      "v_pk_add_f32 v[102:103], v[102:103], v[160:161]\n"
      "v_pk_add_f32 v[108:109], v[108:109], v[164:165]\n"
      "v_pk_add_f32 v[110:111], v[110:111], v[164:165]\n"
      "v_pk_add_f32 v[116:117], v[116:117], v[168:169]\n"
      "v_pk_add_f32 v[118:119], v[118:119], v[168:169]\n"
      "v_pk_add_f32 v[124:125], v[124:125], v[172:173]\n"
      "v_pk_add_f32 v[126:127], v[126:127], v[172:173]\n"
      "v_mov_b32 v224, v244\n"
      "v_mov_b32 v225, v245\n"
      "v_mov_b32 v226, v246\n"
      "v_mov_b32 v227, v247\n"
      "v_xor_b32 v220, 0x2000, v244\n"
      "v_xor_b32 v221, 0x2000, v245\n"
      "v_xor_b32 v222, 0x2000, v246\n"
      "v_xor_b32 v223, 0x2000, v247\n"
      "s_lshl_b32 s76, s43, 3\n"
      "v_mov_b32 v237, s40\n"
      "v_mov_b32 v238, s76\n"
      "v_xor_b32 v239, 8, v238\n"
      "s_memrealtime s[68:69]\n"
      "s_waitcnt vmcnt(14) lgkmcnt(0)\n"
      "s_sub_u32 s67, s68, s88\n"
      "s_cmp_lt_i32 s67, -8\n"
      "s_cbranch_scc1 GW0_%=\n"
      "GX0_%=:\n"
      "s_barrier\n"
      "ds_read_b128 a[64:67], v242 offset:0\n"
      "ds_read_b128 a[68:71], v242 offset:4096\n"
      "ds_read_b128 a[72:75], v243 offset:0\n"
      "ds_read_b128 a[76:79], v243 offset:4096\n"
      "ds_read_b128 a[80:83], v242 offset:1024\n"
      "ds_read_b128 a[84:87], v242 offset:5120\n"
      "ds_read_b128 a[88:91], v243 offset:1024\n"
      "ds_read_b128 a[92:95], v243 offset:5120\n"
      "ds_write_b32 v209, v196\n"
      "s_add_u32 s38, s38, s38\n"
      "s_memrealtime s[68:69]\n"
      "s_xor_b64 s[56:57], s[56:57], s[32:33]\n"
      "s_add_u32 s61, s61, 1\n"
      "s_add_u32 s62, s62, 0x4000\n"
      "s_and_b32 s62, s62, 0x1ffff\n"
      "v_readlane_b32 s60, v219, s61\n"
      "s_waitcnt lgkmcnt(0)\n"
      "s_sub_u32 s67, s68, s66\n"
      "s_cmp_gt_i32 s67, s70\n"
      "s_cselect_b32 s69, s77, s86\n"
      "s_cselect_b32 s70, 60, s70\n"
      "s_cselect_b32 s90, s80, s90\n"
      "s_add_u32 s66, s66, s69\n"
      "s_mov_b32 s82, s68\n"
      "LOOP_%=:\n"
      "v_mfma_f32_16x16x32_bf16 v[64:67], a[64:67], a[0:3], v[160:163]\n"
      "s_add_u32 s65, s68, s90\n"
      "v_exp_f32 v188, v96\n"
      "v_mfma_f32_16x16x32_bf16 v[64:67], a[72:75], a[16:19], v[64:67]\n"
      "s_getreg_b32 s67, hwreg(HW_REG_IB_STS, 0, 4)\n"
      "v_add_f32 v180, v180, v184\n"
      "v_add_f32 v181, v181, v185\n"
      "v_mfma_f32_16x16x32_bf16 v[64:67], a[80:83], a[32:35], v[64:67]\n"
      "s_cmp_gt_u32 s67, 6\n"
      "v_exp_f32 v189, v97\n"
      "v_mfma_f32_16x16x32_bf16 v[64:67], a[88:91], a[48:51], v[64:67]\n"
      "s_cselect_b32 s67, s87, 0\n"
      "v_add_f32 v182, v182, v186\n"
      "v_add_f32 v183, v183, v187\n"
      "v_mfma_f32_16x16x32_bf16 v[72:75], a[64:67], a[4:7], v[164:167]\n"
      "s_max_i32 s67, s67, s55\n"
      "v_exp_f32 v190, v98\n"
      "v_mfma_f32_16x16x32_bf16 v[72:75], a[72:75], a[20:23], v[72:75]\n"
      "s_sub_u32 s55, s55, s67\n"
      "v_exp_f32 v191, v99\n"
      "v_mfma_f32_16x16x32_bf16 v[72:75], a[80:83], a[36:39], v[72:75]\n"
      "s_add_u32 s66, s66, s67\n"
      "v_cvt_pk_bf16_f32 v144, v188, v189\n"
      "v_cvt_pk_bf16_f32 v145, v190, v191\n"
      "v_mfma_f32_16x16x32_bf16 v[72:75], a[88:91], a[52:55], v[72:75]\n"
      "s_max_u32 s88, s66, s65\n"
      "v_exp_f32 v192, v100\n"
      "v_mfma_f32_16x16x32_bf16 v[80:83], a[64:67], a[8:11], v[168:171]\n"
      "s_add_u32 s39, s39, s39\n"
      "v_exp_f32 v193, v101\n"
      "v_mfma_f32_16x16x32_bf16 v[80:83], a[72:75], a[24:27], v[80:83]\n"
      "s_memrealtime s[68:69]\n"
      "v_exp_f32 v194, v102\n"
      "v_mfma_f32_16x16x32_bf16 v[80:83], a[80:83], a[40:43], v[80:83]\n"
      "v_exp_f32 v195, v103\n"
      "v_mfma_f32_16x16x32_bf16 v[80:83], a[88:91], a[56:59], v[80:83]\n"
      "s_mov_b32 m0, s62\n"
      "v_cvt_pk_bf16_f32 v146, v192, v193\n"
      "v_cvt_pk_bf16_f32 v147, v194, v195\n"
      "v_mfma_f32_16x16x32_bf16 v[88:91], a[64:67], a[12:15], v[172:175]\n"
      "buffer_load_dwordx4 v228, s[56:59], s60 offen nt lds\n"
      "v_mfma_f32_16x16x32_bf16 v[184:187], a[128:131], v[144:147], 0\n"
      "buffer_load_dwordx4 v229, s[56:59], s60 offen offset:1024 nt lds\n"
      "v_mfma_f32_16x16x32_bf16 v[88:91], a[72:75], a[28:31], v[88:91]\n"
      "buffer_load_dwordx4 v230, s[56:59], s60 offen offset:2048 nt lds\n"
      "v_mfma_f32_16x16x32_bf16 v[88:91], a[80:83], a[44:47], v[88:91]\n"
      "buffer_load_dwordx4 v231, s[56:59], s60 offen offset:3072 nt lds\n"
      "s_cbranch_scc1 SP1_%=\n"
      "HD1_%=:\n"
      "s_cmp_lg_u32 s54, 0\n"
      "s_waitcnt vmcnt(12) lgkmcnt(0)\n"
      "s_cbranch_scc1 W1_%=\n"
      "s_waitcnt vmcnt(8)\n"
      "W1_%=:\n"
      "s_sub_u32 s67, s68, s88\n"
      "s_cmp_lt_i32 s67, -8\n"
      "s_cbranch_scc1 GW4_%=\n"
      "GX4_%=:\n"
      "s_barrier\n"
      "v_mfma_f32_16x16x32_bf16 v[88:91], a[88:91], a[60:63], v[88:91]\n"
      "s_memrealtime s[68:69]\n"
      "ds_read_b64_tr_b16 a[96:97], v244 offset:0\n"
      "ds_read_b64_tr_b16 a[98:99], v244 offset:4096\n"
      "v_mfma_f32_16x16x32_bf16 v[68:71], a[68:71], a[0:3], v[160:163]\n"
      "s_xor_b64 s[56:57], s[56:57], s[32:33]\n"
      "ds_read_b64_tr_b16 a[100:101], v245 offset:0\n"
      "ds_read_b64_tr_b16 a[102:103], v245 offset:4096\n"
      "v_mfma_f32_16x16x32_bf16 v[68:71], a[76:79], a[16:19], v[68:71]\n"
      "s_add_u32 s61, s61, 1\n"
      "ds_read_b64_tr_b16 a[104:105], v246 offset:0\n"
      "ds_read_b64_tr_b16 a[106:107], v246 offset:4096\n"
      "v_mfma_f32_16x16x32_bf16 v[68:71], a[84:87], a[32:35], v[68:71]\n"
      "s_add_u32 s62, s62, 0x4000\n"
      "ds_read_b64_tr_b16 a[108:109], v247 offset:0\n"
      "ds_read_b64_tr_b16 a[110:111], v247 offset:4096\n"
      "v_mfma_f32_16x16x32_bf16 v[68:71], a[92:95], a[48:51], v[68:71]\n"
      "s_and_b32 s62, s62, 0x1ffff\n"
      "v_exp_f32 v188, v104\n"
      "v_mfma_f32_16x16x32_bf16 v[76:79], a[68:71], a[4:7], v[164:167]\n"
      "v_readlane_b32 s60, v219, s61\n"
      "v_exp_f32 v189, v105\n"
      "v_mfma_f32_16x16x32_bf16 v[76:79], a[76:79], a[20:23], v[76:79]\n"
      "v_exp_f32 v190, v106\n"
      "v_mfma_f32_16x16x32_bf16 v[76:79], a[84:87], a[36:39], v[76:79]\n"
      "v_exp_f32 v191, v107\n"
      "v_mfma_f32_16x16x32_bf16 v[76:79], a[92:95], a[52:55], v[76:79]\n"
      "v_cvt_pk_bf16_f32 v148, v188, v189\n"
      "v_cvt_pk_bf16_f32 v149, v190, v191\n"
      "v_mfma_f32_16x16x32_bf16 v[84:87], a[68:71], a[8:11], v[168:171]\n"
      "v_exp_f32 v192, v108\n"
      "v_mfma_f32_16x16x32_bf16 v[84:87], a[76:79], a[24:27], v[84:87]\n"
      "v_exp_f32 v193, v109\n"
      "s_waitcnt lgkmcnt(0)\n"
      "v_mfma_f32_16x16x32_bf16 v[84:87], a[84:87], a[40:43], v[84:87]\n"
      "s_sub_u32 s67, s68, s66\n"
      "ds_read_b64_tr_b16 a[112:113], v244 offset:1024\n"
      "v_cvt_pk_bf16_f32 v150, v192, v193\n"
      "v_mfma_f32_16x16x32_bf16 v[84:87], a[92:95], a[56:59], v[84:87]\n"
      "s_cmp_gt_i32 s67, s70\n"
      "v_exp_f32 v194, v110\n"
      "v_mfma_f32_16x16x32_bf16 v[92:95], a[68:71], a[12:15], v[172:175]\n"
      "s_cselect_b32 s69, s77, s86\n"
      "ds_read_b64_tr_b16 a[114:115], v244 offset:5120\n"
      "ds_read_b64_tr_b16 a[116:117], v245 offset:1024\n"
      "v_mfma_f32_16x16x32_bf16 v[92:95], a[76:79], a[28:31], v[92:95]\n"
      "s_cselect_b32 s70, 60, s70\n"
      "v_exp_f32 v195, v111\n"
      "v_mfma_f32_16x16x32_bf16 v[92:95], a[84:87], a[44:47], v[92:95]\n"
      "s_cselect_b32 s90, s80, s90\n"
      "ds_read_b64_tr_b16 a[118:119], v245 offset:5120\n"
      "v_cvt_pk_bf16_f32 v151, v194, v195\n"
      "v_mfma_f32_16x16x32_bf16 v[92:95], a[92:95], a[60:63], v[92:95]\n"
      "s_add_u32 s66, s66, s69\n"
      "v_exp_f32 v188, v112\n"
      "v_mfma_f32_16x16x32_bf16 v[184:187], a[132:135], v[148:151], v[184:187]\n"
      "s_mov_b32 s82, s68\n"
      "ds_read_b64_tr_b16 a[120:121], v246 offset:1024\n"
      "ds_read_b64_tr_b16 a[122:123], v246 offset:5120\n"
      "v_mfma_f32_16x16x32_bf16 v[0:3], a[96:99], v[128:131], v[0:3]\n"
      "s_add_u32 s65, s68, s90\n"
      "v_exp_f32 v189, v113\n"
      "v_mfma_f32_16x16x32_bf16 v[4:7], a[96:99], v[132:135], v[4:7]\n"
      "s_getreg_b32 s67, hwreg(HW_REG_IB_STS, 0, 4)\n"
      "ds_read_b64_tr_b16 a[124:125], v247 offset:1024\n"
      "v_cvt_pk_bf16_f32 v152, v188, v189\n"
      "v_mfma_f32_16x16x32_bf16 a[144:147], a[96:99], v[136:139], a[144:147]\n"
      "s_cmp_gt_u32 s67, 6\n"
      "v_exp_f32 v190, v114\n"
      "v_mfma_f32_16x16x32_bf16 a[148:151], a[96:99], v[140:143], a[148:151]\n"
      "s_cselect_b32 s67, s87, 0\n"
      "v_exp_f32 v191, v115\n"
      "v_mfma_f32_16x16x32_bf16 a[156:159], a[100:103], v[140:143], a[156:159]\n"
      "s_max_i32 s67, s67, s55\n"
      "ds_read_b64_tr_b16 a[126:127], v247 offset:5120\n"
      "v_cvt_pk_bf16_f32 v153, v190, v191\n"
      "v_mfma_f32_16x16x32_bf16 a[152:155], a[100:103], v[136:139], a[152:155]\n"
      "s_sub_u32 s55, s55, s67\n"
      "v_exp_f32 v192, v116\n"
      "v_mfma_f32_16x16x32_bf16 v[12:15], a[100:103], v[132:135], v[12:15]\n"
      "s_add_u32 s66, s66, s67\n"
      "v_exp_f32 v193, v117\n"
      "v_mfma_f32_16x16x32_bf16 v[8:11], a[100:103], v[128:131], v[8:11]\n"
      "s_max_u32 s88, s66, s65\n"
      "v_exp_f32 v194, v118\n"
      "v_mfma_f32_16x16x32_bf16 v[16:19], a[104:107], v[128:131], v[16:19]\n"
      "s_add_u32 s38, s38, s38\n"
      "v_exp_f32 v195, v119\n"
      "v_mfma_f32_16x16x32_bf16 v[20:23], a[104:107], v[132:135], v[20:23]\n"
      "s_memrealtime s[68:69]\n"
      "v_cvt_pk_bf16_f32 v154, v192, v193\n"
      "v_cvt_pk_bf16_f32 v155, v194, v195\n"
      "v_mfma_f32_16x16x32_bf16 a[160:163], a[104:107], v[136:139], a[160:163]\n"
      "v_exp_f32 v188, v120\n"
      "v_mfma_f32_16x16x32_bf16 v[184:187], a[136:139], v[152:155], v[184:187]\n"
      "s_mov_b32 m0, s62\n"
      "v_exp_f32 v189, v121\n"
      "v_mfma_f32_16x16x32_bf16 a[164:167], a[104:107], v[140:143], a[164:167]\n"
      "buffer_load_dwordx4 v228, s[56:59], s60 offen nt lds\n"
      "v_mfma_f32_16x16x32_bf16 a[172:175], a[108:111], v[140:143], a[172:175]\n"
      "buffer_load_dwordx4 v229, s[56:59], s60 offen offset:1024 nt lds\n"
      "v_mfma_f32_16x16x32_bf16 a[168:171], a[108:111], v[136:139], a[168:171]\n"
      "buffer_load_dwordx4 v230, s[56:59], s60 offen offset:2048 nt lds\n"
      "v_mfma_f32_16x16x32_bf16 v[28:31], a[108:111], v[132:135], v[28:31]\n"
      "buffer_load_dwordx4 v231, s[56:59], s60 offen offset:3072 nt lds\n"
      "s_cbranch_scc1 SP2_%=\n"
      "HD2_%=:\n"
      "s_cmp_lg_u32 s54, 0\n"
      "s_waitcnt vmcnt(12) lgkmcnt(0)\n"
      "s_cbranch_scc1 W2_%=\n"
      "s_waitcnt vmcnt(8)\n"
      "W2_%=:\n"
      "s_sub_u32 s67, s68, s88\n"
      "s_cmp_lt_i32 s67, -8\n"
      "s_cbranch_scc1 GW5_%=\n"
      "GX5_%=:\n"
      "s_barrier\n"
      "v_mfma_f32_16x16x32_bf16 v[24:27], a[108:111], v[128:131], v[24:27]\n"
      "s_memrealtime s[68:69]\n"
      "ds_read_b128 a[64:67], v242 offset:32768\n"
      "ds_read_b128 a[72:75], v243 offset:32768\n"
      "v_mfma_f32_16x16x32_bf16 v[32:35], a[112:115], v[128:131], v[32:35]\n"
      "s_xor_b64 s[56:57], s[56:57], s[32:33]\n"
      "ds_read_b128 a[80:83], v242 offset:33792\n"
      "ds_read_b128 a[88:91], v243 offset:33792\n"
      "v_mfma_f32_16x16x32_bf16 v[36:39], a[112:115], v[132:135], v[36:39]\n"
      "s_add_u32 s61, s61, 1\n"
      "v_exp_f32 v190, v122\n"
      "v_mfma_f32_16x16x32_bf16 a[176:179], a[112:115], v[136:139], a[176:179]\n"
      "s_add_u32 s62, s62, 0x4000\n"
      "v_exp_f32 v191, v123\n"
      "v_mfma_f32_16x16x32_bf16 a[180:183], a[112:115], v[140:143], a[180:183]\n"
      "s_and_b32 s62, s62, 0x1ffff\n"
      "v_cvt_pk_bf16_f32 v156, v188, v189\n"
      "v_cvt_pk_bf16_f32 v157, v190, v191\n"
      "v_mfma_f32_16x16x32_bf16 a[188:191], a[116:119], v[140:143], a[188:191]\n"
      "v_readlane_b32 s60, v219, s61\n"
      "v_exp_f32 v192, v124\n"
      "v_mfma_f32_16x16x32_bf16 a[184:187], a[116:119], v[136:139], a[184:187]\n"
      "v_exp_f32 v193, v125\n"
      "v_mfma_f32_16x16x32_bf16 v[44:47], a[116:119], v[132:135], v[44:47]\n"
      "v_exp_f32 v194, v126\n"
      "v_mfma_f32_16x16x32_bf16 v[40:43], a[116:119], v[128:131], v[40:43]\n"
      "v_exp_f32 v195, v127\n"
      "v_mfma_f32_16x16x32_bf16 v[48:51], a[120:123], v[128:131], v[48:51]\n"
      "v_cvt_pk_bf16_f32 v158, v192, v193\n"
      "v_cvt_pk_bf16_f32 v159, v194, v195\n"
      "v_mfma_f32_16x16x32_bf16 v[52:55], a[120:123], v[132:135], v[52:55]\n"
      "s_waitcnt lgkmcnt(0)\n"
      "v_mfma_f32_16x16x32_bf16 v[184:187], a[140:143], v[156:159], v[184:187]\n"
      "s_sub_u32 s67, s68, s66\n"
      "ds_read_b128 a[68:71], v242 offset:36864\n"
      "ds_read_b128 a[76:79], v243 offset:36864\n"
      "v_mfma_f32_16x16x32_bf16 a[192:195], a[120:123], v[136:139], a[192:195]\n"
      "s_cmp_gt_i32 s67, s70\n"
      "ds_read_b128 a[84:87], v242 offset:37888\n"
      "ds_read_b128 a[92:95], v243 offset:37888\n"
      "v_mfma_f32_16x16x32_bf16 a[196:199], a[120:123], v[140:143], a[196:199]\n"
      "s_cselect_b32 s69, s77, s86\n"
      "v_max3_f32 v196, v184, v185, v186\n"
      "v_max_f32 v196, v196, v187\n"
      "v_mfma_f32_16x16x32_bf16 a[204:207], a[124:127], v[140:143], a[204:207]\n"
      "s_cselect_b32 s70, 60, s70\n"
      "v_cmp_nge_f32 vcc, 0x4b800000, v196\n"
      "v_mfma_f32_16x16x32_bf16 a[200:203], a[124:127], v[136:139], a[200:203]\n"
      "s_cselect_b32 s90, s80, s90\n"
      "v_mfma_f32_16x16x32_bf16 v[60:63], a[124:127], v[132:135], v[60:63]\n"
      "s_add_u32 s66, s66, s69\n"
      "v_mfma_f32_16x16x32_bf16 v[56:59], a[124:127], v[128:131], v[56:59]\n"
      "s_mov_b32 s82, s68\n"
      "s_cbranch_vccnz FS1_%=\n"
      "FR1_%=:\n"
      "v_mfma_f32_16x16x32_bf16 v[96:99], a[64:67], a[0:3], v[160:163]\n"
      "s_add_u32 s65, s68, s90\n"
      "v_exp_f32 v188, v64\n"
      "v_mfma_f32_16x16x32_bf16 v[96:99], a[72:75], a[16:19], v[96:99]\n"
      "s_getreg_b32 s67, hwreg(HW_REG_IB_STS, 0, 4)\n"
      "v_add_f32 v180, v180, v184\n"
      "v_add_f32 v181, v181, v185\n"
      "v_mfma_f32_16x16x32_bf16 v[96:99], a[80:83], a[32:35], v[96:99]\n"
      "s_cmp_gt_u32 s67, 6\n"
      "v_exp_f32 v189, v65\n"
      "v_mfma_f32_16x16x32_bf16 v[96:99], a[88:91], a[48:51], v[96:99]\n"
      "s_cselect_b32 s67, s87, 0\n"
      "v_add_f32 v182, v182, v186\n"
      "v_add_f32 v183, v183, v187\n"
      "v_mfma_f32_16x16x32_bf16 v[104:107], a[64:67], a[4:7], v[164:167]\n"
      "s_max_i32 s67, s67, s55\n"
      "v_exp_f32 v190, v66\n"
      "v_mfma_f32_16x16x32_bf16 v[104:107], a[72:75], a[20:23], v[104:107]\n"
      "s_sub_u32 s55, s55, s67\n"
      "v_exp_f32 v191, v67\n"
      "v_mfma_f32_16x16x32_bf16 v[104:107], a[80:83], a[36:39], v[104:107]\n"
      "s_add_u32 s66, s66, s67\n"
      "v_cvt_pk_bf16_f32 v128, v188, v189\n"
      "v_cvt_pk_bf16_f32 v129, v190, v191\n"
      "v_mfma_f32_16x16x32_bf16 v[104:107], a[88:91], a[52:55], v[104:107]\n"
      "s_max_u32 s88, s66, s65\n"
      "v_exp_f32 v192, v68\n"
      "v_mfma_f32_16x16x32_bf16 v[112:115], a[64:67], a[8:11], v[168:171]\n"
      "s_add_u32 s39, s39, s39\n"
      "v_exp_f32 v193, v69\n"
      "v_mfma_f32_16x16x32_bf16 v[112:115], a[72:75], a[24:27], v[112:115]\n"
      "s_memrealtime s[68:69]\n"
      "v_exp_f32 v194, v70\n"
      "v_mfma_f32_16x16x32_bf16 v[112:115], a[80:83], a[40:43], v[112:115]\n"
      "v_exp_f32 v195, v71\n"
      "v_mfma_f32_16x16x32_bf16 v[112:115], a[88:91], a[56:59], v[112:115]\n"
      "s_mov_b32 m0, s62\n"
      "v_cvt_pk_bf16_f32 v130, v192, v193\n"
      "v_cvt_pk_bf16_f32 v131, v194, v195\n"
      "v_mfma_f32_16x16x32_bf16 v[120:123], a[64:67], a[12:15], v[172:175]\n"
      "buffer_load_dwordx4 v228, s[56:59], s60 offen nt lds\n"
      "v_mfma_f32_16x16x32_bf16 v[184:187], a[128:131], v[128:131], 0\n"
      "buffer_load_dwordx4 v229, s[56:59], s60 offen offset:1024 nt lds\n"
      "v_mfma_f32_16x16x32_bf16 v[120:123], a[72:75], a[28:31], v[120:123]\n"
      "buffer_load_dwordx4 v230, s[56:59], s60 offen offset:2048 nt lds\n"
      "v_mfma_f32_16x16x32_bf16 v[120:123], a[80:83], a[44:47], v[120:123]\n"
      "buffer_load_dwordx4 v231, s[56:59], s60 offen offset:3072 nt lds\n"
      "s_cbranch_scc1 SP3_%=\n"
      "HD3_%=:\n"
      "s_cmp_lg_u32 s54, 0\n"
      "s_waitcnt vmcnt(12) lgkmcnt(0)\n"
      "s_cbranch_scc1 W3_%=\n"
      "s_waitcnt vmcnt(8)\n"
      "W3_%=:\n"
      "s_sub_u32 s67, s68, s88\n"
      "s_cmp_lt_i32 s67, -8\n"
      "s_cbranch_scc1 GW6_%=\n"
      "GX6_%=:\n"
      "s_barrier\n"
      "v_mfma_f32_16x16x32_bf16 v[120:123], a[88:91], a[60:63], v[120:123]\n"
      "s_memrealtime s[68:69]\n"
      "ds_read_b64_tr_b16 a[96:97], v244 offset:32768\n"
      "ds_read_b64_tr_b16 a[98:99], v244 offset:36864\n"
      "v_mfma_f32_16x16x32_bf16 v[100:103], a[68:71], a[0:3], v[160:163]\n"
      "s_xor_b64 s[56:57], s[56:57], s[32:33]\n"
      "ds_read_b64_tr_b16 a[100:101], v245 offset:32768\n"
      "ds_read_b64_tr_b16 a[102:103], v245 offset:36864\n"
      "v_mfma_f32_16x16x32_bf16 v[100:103], a[76:79], a[16:19], v[100:103]\n"
      "s_add_u32 s61, s61, 1\n"
      "ds_read_b64_tr_b16 a[104:105], v246 offset:32768\n"
      "ds_read_b64_tr_b16 a[106:107], v246 offset:36864\n"
      "v_mfma_f32_16x16x32_bf16 v[100:103], a[84:87], a[32:35], v[100:103]\n"
      "s_add_u32 s62, s62, 0x4000\n"
      "ds_read_b64_tr_b16 a[108:109], v247 offset:32768\n"
      "ds_read_b64_tr_b16 a[110:111], v247 offset:36864\n"
      "v_mfma_f32_16x16x32_bf16 v[100:103], a[92:95], a[48:51], v[100:103]\n"
      "s_and_b32 s62, s62, 0x1ffff\n"
      "v_exp_f32 v188, v72\n"
      "v_mfma_f32_16x16x32_bf16 v[108:111], a[68:71], a[4:7], v[164:167]\n"
      "v_readlane_b32 s60, v219, s61\n"
      "v_exp_f32 v189, v73\n"
      "v_mfma_f32_16x16x32_bf16 v[108:111], a[76:79], a[20:23], v[108:111]\n"
      "v_xor_b32 v242, 0x10000, v242\n"
      "v_xor_b32 v243, 0x10000, v243\n"
      "v_mfma_f32_16x16x32_bf16 v[108:111], a[84:87], a[36:39], v[108:111]\n"
      "v_exp_f32 v190, v74\n"
      "v_mfma_f32_16x16x32_bf16 v[108:111], a[92:95], a[52:55], v[108:111]\n"
      "v_exp_f32 v191, v75\n"
      "v_mfma_f32_16x16x32_bf16 v[116:119], a[68:71], a[8:11], v[168:171]\n"
      "v_cvt_pk_bf16_f32 v132, v188, v189\n"
      "v_cvt_pk_bf16_f32 v133, v190, v191\n"
      "v_mfma_f32_16x16x32_bf16 v[116:119], a[76:79], a[24:27], v[116:119]\n"
      "v_exp_f32 v192, v76\n"
      "s_waitcnt lgkmcnt(0)\n"
      "v_mfma_f32_16x16x32_bf16 v[116:119], a[84:87], a[40:43], v[116:119]\n"
      "s_sub_u32 s67, s68, s66\n"
      "ds_read_b64_tr_b16 a[112:113], v244 offset:33792\n"
      "ds_read_b64_tr_b16 a[114:115], v244 offset:37888\n"
      "v_mfma_f32_16x16x32_bf16 v[116:119], a[92:95], a[56:59], v[116:119]\n"
      "s_cmp_gt_i32 s67, s70\n"
      "v_exp_f32 v193, v77\n"
      "v_mfma_f32_16x16x32_bf16 v[124:127], a[68:71], a[12:15], v[172:175]\n"
      "s_cselect_b32 s69, s77, s86\n"
      "ds_read_b64_tr_b16 a[116:117], v245 offset:33792\n"
      "v_cvt_pk_bf16_f32 v134, v192, v193\n"
      "v_mfma_f32_16x16x32_bf16 v[124:127], a[76:79], a[28:31], v[124:127]\n"
      "s_cselect_b32 s70, 60, s70\n"
      "v_exp_f32 v194, v78\n"
      "v_mfma_f32_16x16x32_bf16 v[124:127], a[84:87], a[44:47], v[124:127]\n"
      "s_cselect_b32 s90, s80, s90\n"
      "ds_read_b64_tr_b16 a[118:119], v245 offset:37888\n"
      "ds_read_b64_tr_b16 a[120:121], v246 offset:33792\n"
      "v_mfma_f32_16x16x32_bf16 v[124:127], a[92:95], a[60:63], v[124:127]\n"
      "s_add_u32 s66, s66, s69\n"
      "v_exp_f32 v195, v79\n"
      "v_mfma_f32_16x16x32_bf16 v[0:3], a[96:99], v[144:147], v[0:3]\n"
      "s_mov_b32 s82, s68\n"
      "ds_read_b64_tr_b16 a[122:123], v246 offset:37888\n"
      "v_cvt_pk_bf16_f32 v135, v194, v195\n"
      "v_mfma_f32_16x16x32_bf16 v[4:7], a[96:99], v[148:151], v[4:7]\n"
      "s_add_u32 s65, s68, s90\n"
      "v_exp_f32 v188, v80\n"
      "v_mfma_f32_16x16x32_bf16 v[184:187], a[132:135], v[132:135], v[184:187]\n"
      "s_getreg_b32 s67, hwreg(HW_REG_IB_STS, 0, 4)\n"
      "ds_read_b64_tr_b16 a[124:125], v247 offset:33792\n"
      "ds_read_b64_tr_b16 a[126:127], v247 offset:37888\n"
      "v_mfma_f32_16x16x32_bf16 a[144:147], a[96:99], v[152:155], a[144:147]\n"
      "s_cmp_gt_u32 s67, 6\n"
      "v_exp_f32 v189, v81\n"
      "v_mfma_f32_16x16x32_bf16 a[148:151], a[96:99], v[156:159], a[148:151]\n"
      "s_cselect_b32 s67, s87, 0\n"
      "v_exp_f32 v190, v82\n"
      "v_mfma_f32_16x16x32_bf16 a[156:159], a[100:103], v[156:159], a[156:159]\n"
      "s_max_i32 s67, s67, s55\n"
      "v_exp_f32 v191, v83\n"
      "v_mfma_f32_16x16x32_bf16 a[152:155], a[100:103], v[152:155], a[152:155]\n"
      "s_sub_u32 s55, s55, s67\n"
      "v_cvt_pk_bf16_f32 v136, v188, v189\n"
      "v_cvt_pk_bf16_f32 v137, v190, v191\n"
      "v_mfma_f32_16x16x32_bf16 v[12:15], a[100:103], v[148:151], v[12:15]\n"
      "s_add_u32 s66, s66, s67\n"
      "v_exp_f32 v192, v84\n"
      "v_mfma_f32_16x16x32_bf16 v[8:11], a[100:103], v[144:147], v[8:11]\n"
      "s_max_u32 s88, s66, s65\n"
      "v_exp_f32 v193, v85\n"
      "v_mfma_f32_16x16x32_bf16 v[16:19], a[104:107], v[144:147], v[16:19]\n"
      "s_add_u32 s38, s38, s38\n"
      "v_exp_f32 v194, v86\n"
      "v_mfma_f32_16x16x32_bf16 v[20:23], a[104:107], v[148:151], v[20:23]\n"
      "s_memrealtime s[68:69]\n"
      "v_exp_f32 v195, v87\n"
      "v_mfma_f32_16x16x32_bf16 a[160:163], a[104:107], v[152:155], a[160:163]\n"
      "v_cvt_pk_bf16_f32 v138, v192, v193\n"
      "v_cvt_pk_bf16_f32 v139, v194, v195\n"
      "v_mfma_f32_16x16x32_bf16 a[164:167], a[104:107], v[156:159], a[164:167]\n"
      "s_mov_b32 m0, s62\n"
      "v_exp_f32 v188, v88\n"
      "v_mfma_f32_16x16x32_bf16 v[184:187], a[136:139], v[136:139], v[184:187]\n"
      "buffer_load_dwordx4 v228, s[56:59], s60 offen nt lds\n"
      "v_mfma_f32_16x16x32_bf16 a[172:175], a[108:111], v[156:159], a[172:175]\n"
      "buffer_load_dwordx4 v229, s[56:59], s60 offen offset:1024 nt lds\n"
      "v_mfma_f32_16x16x32_bf16 a[168:171], a[108:111], v[152:155], a[168:171]\n"
      "buffer_load_dwordx4 v230, s[56:59], s60 offen offset:2048 nt lds\n"
      "v_mfma_f32_16x16x32_bf16 v[28:31], a[108:111], v[148:151], v[28:31]\n"
      "buffer_load_dwordx4 v231, s[56:59], s60 offen offset:3072 nt lds\n"
      "HD0_%=:\n"
      "s_cmp_lg_u32 s54, 0\n"
      "s_waitcnt vmcnt(12) lgkmcnt(0)\n"
      "s_cbranch_scc1 W4_%=\n"
      "s_waitcnt vmcnt(8)\n"
      "W4_%=:\n"
      "s_sub_u32 s67, s68, s88\n"
      "s_cmp_lt_i32 s67, -8\n"
      "s_cbranch_scc1 GW7_%=\n"
      "GX7_%=:\n"
      "s_barrier\n"
      "v_mfma_f32_16x16x32_bf16 v[24:27], a[108:111], v[144:147], v[24:27]\n"
      "s_memrealtime s[68:69]\n"
      "ds_read_b128 a[64:67], v242 offset:0\n"
      "ds_read_b128 a[72:75], v243 offset:0\n"
      "v_mfma_f32_16x16x32_bf16 v[32:35], a[112:115], v[144:147], v[32:35]\n"
      "s_xor_b64 s[56:57], s[56:57], s[32:33]\n"
      "ds_read_b128 a[80:83], v242 offset:1024\n"
      "ds_read_b128 a[88:91], v243 offset:1024\n"
      "v_mfma_f32_16x16x32_bf16 v[36:39], a[112:115], v[148:151], v[36:39]\n"
      "s_add_u32 s61, s61, 1\n"
      "v_exp_f32 v189, v89\n"
      "v_mfma_f32_16x16x32_bf16 a[176:179], a[112:115], v[152:155], a[176:179]\n"
      "s_add_u32 s62, s62, 0x4000\n"
      "v_exp_f32 v190, v90\n"
      "v_mfma_f32_16x16x32_bf16 a[180:183], a[112:115], v[156:159], a[180:183]\n"
      "s_and_b32 s62, s62, 0x1ffff\n"
      "v_exp_f32 v191, v91\n"
      "v_mfma_f32_16x16x32_bf16 a[188:191], a[116:119], v[156:159], a[188:191]\n"
      "v_cvt_pk_bf16_f32 v140, v188, v189\n"
      "v_readlane_b32 s60, v219, s61\n"
      "v_mfma_f32_16x16x32_bf16 a[184:187], a[116:119], v[152:155], a[184:187]\n"
      "v_exp_f32 v192, v92\n"
      "v_mfma_f32_16x16x32_bf16 v[44:47], a[116:119], v[148:151], v[44:47]\n"
      "v_exp_f32 v193, v93\n"
      "v_mfma_f32_16x16x32_bf16 v[40:43], a[116:119], v[144:147], v[40:43]\n"
      "v_cvt_pk_bf16_f32 v141, v190, v191\n"
      "v_cvt_pk_bf16_f32 v142, v192, v193\n"
      "v_mfma_f32_16x16x32_bf16 v[48:51], a[120:123], v[144:147], v[48:51]\n"
      "v_exp_f32 v194, v94\n"
      "v_mfma_f32_16x16x32_bf16 v[52:55], a[120:123], v[148:151], v[52:55]\n"
      "v_exp_f32 v195, v95\n"
      "s_waitcnt lgkmcnt(0)\n"
      "v_mfma_f32_16x16x32_bf16 a[192:195], a[120:123], v[152:155], a[192:195]\n"
      "s_sub_u32 s67, s68, s66\n"
      "v_cvt_pk_bf16_f32 v143, v194, v195\n"
      "v_xor_b32 v244, 0x10000, v244\n"
      "v_mfma_f32_16x16x32_bf16 a[196:199], a[120:123], v[156:159], a[196:199]\n"
      "s_cmp_gt_i32 s67, s70\n"
      "ds_read_b128 a[68:71], v242 offset:4096\n"
      "ds_read_b128 a[76:79], v243 offset:4096\n"
      "v_mfma_f32_16x16x32_bf16 v[184:187], a[140:143], v[140:143], v[184:187]\n"
      "s_cselect_b32 s69, s77, s86\n"
      "ds_read_b128 a[84:87], v242 offset:5120\n"
      "ds_read_b128 a[92:95], v243 offset:5120\n"
      "v_mfma_f32_16x16x32_bf16 a[204:207], a[124:127], v[156:159], a[204:207]\n"
      "s_cselect_b32 s70, 60, s70\n"
      "v_xor_b32 v245, 0x10000, v245\n"
      "v_xor_b32 v246, 0x10000, v246\n"
      "v_mfma_f32_16x16x32_bf16 a[200:203], a[124:127], v[152:155], a[200:203]\n"
      "s_cselect_b32 s90, s80, s90\n"
      "v_xor_b32 v247, 0x10000, v247\n"
      "v_max3_f32 v196, v184, v185, v186\n"
      "v_mfma_f32_16x16x32_bf16 v[60:63], a[124:127], v[148:151], v[60:63]\n"
      "s_add_u32 s66, s66, s69\n"
      "v_max_f32 v196, v196, v187\n"
      "v_cmp_nge_f32 vcc, 0x4b800000, v196\n"
      "v_mfma_f32_16x16x32_bf16 v[56:59], a[124:127], v[144:147], v[56:59]\n"
      "s_mov_b32 s82, s68\n"
      "s_cbranch_vccnz FS2_%=\n"
      "FR2_%=:\n"
      "s_branch LOOP_%=\n"
      "SP1_%=:\n"
      "s_ff1_i32_b32 s64, s39\n"
      "s_lshl1_add_u32 s64, s64, 1\n"
      "s_bitcmp1_b64 s[52:53], s64\n"
      "s_cbranch_scc0 SH1_%=\n"
      "s_swappc_b64 s[84:85], s[84:85]\n"
      "SH1_%=:\n"
      "s_cmp_lt_u32 s64, 63\n"
      "s_cbranch_scc1 HD1_%=\n"
      "s_xor_b64 s[56:57], s[56:57], s[32:33]\n"
      "s_add_u32 s61, s61, 1\n"
      "s_add_u32 s62, s62, 0x4000\n"
      "s_and_b32 s62, s62, 0x1ffff\n"
      "v_readlane_b32 s74, v219, 60\n"
      "v_readlane_b32 s76, v219, 62\n"
      "s_mov_b32 s61, 0\n"
      "s_add_u32 s74, s74, 0x400\n"
      "v_add_u32 v228, s74, v229\n"
      "v_add_u32 v230, s74, v231\n"
      "v_add_u32 v229, s76, v229\n"
      "v_add_u32 v231, s76, v231\n"
      "s_branch HD1_%=\n"
      "SP2_%=:\n"
      "s_cmp_eq_u32 s38, 0\n"
      "s_cbranch_scc1 L62_%=\n"
      "s_ff1_i32_b32 s64, s38\n"
      "s_lshl1_add_u32 s64, s64, 0\n"
      "s_bitcmp1_b64 s[52:53], s64\n"
      "s_cbranch_scc0 SH2_%=\n"
      "s_swappc_b64 s[84:85], s[84:85]\n"
      "SH2_%=:\n"
      "s_branch HD2_%=\n"
      "L62_%=:\n"
      "s_xor_b64 s[56:57], s[56:57], s[32:33]\n"
      "s_mov_b32 s61, 0\n"
      "v_xor_b32 v228, 0x80, v228\n"
      "v_xor_b32 v229, 0x80, v229\n"
      "v_xor_b32 v230, 0x80, v230\n"
      "v_xor_b32 v231, 0x80, v231\n"
      "s_mov_b32 s38, 0x80000000\n"
      "s_branch HD2_%=\n"
      "SP3_%=:\n"
      "s_cmp_eq_u32 s39, 0\n"
      "s_cbranch_scc1 NH_%=\n"
      "s_ff1_i32_b32 s64, s39\n"
      "s_lshl1_add_u32 s64, s64, 1\n"
      "s_bitcmp1_b64 s[52:53], s64\n"
      "s_cbranch_scc0 SH3_%=\n"
      "s_swappc_b64 s[84:85], s[84:85]\n"
      "SH3_%=:\n"
      "s_bitcmp1_b64 s[34:35], s64\n"
      "s_cbranch_scc0 HD3_%=\n"
      "NH_%=:\n"
      "s_cmp_lg_u32 s54, 0\n"
      "s_waitcnt vmcnt(12) lgkmcnt(0)\n"
      "s_cbranch_scc1 W5_%=\n"
      "s_waitcnt vmcnt(8)\n"
      "W5_%=:\n"
      "s_barrier\n"
      "v_mfma_f32_16x16x32_bf16 v[120:123], a[88:91], a[60:63], v[120:123]\n"
      "s_sub_u32 s54, s54, 1\n"
      "ds_read_b64_tr_b16 a[96:97], v244 offset:32768\n"
      "ds_read_b64_tr_b16 a[98:99], v244 offset:36864\n"
      "v_mfma_f32_16x16x32_bf16 v[100:103], a[68:71], a[0:3], v[160:163]\n"
      "ds_read_b64_tr_b16 a[100:101], v245 offset:32768\n"
      "ds_read_b64_tr_b16 a[102:103], v245 offset:36864\n"
      "v_mfma_f32_16x16x32_bf16 v[100:103], a[76:79], a[16:19], v[100:103]\n"
      "ds_read_b64_tr_b16 a[104:105], v246 offset:32768\n"
      "ds_read_b64_tr_b16 a[106:107], v246 offset:36864\n"
      "v_mfma_f32_16x16x32_bf16 v[100:103], a[84:87], a[32:35], v[100:103]\n"
      "ds_read_b64_tr_b16 a[108:109], v247 offset:32768\n"
      "ds_read_b64_tr_b16 a[110:111], v247 offset:36864\n"
      "v_mfma_f32_16x16x32_bf16 v[100:103], a[92:95], a[48:51], v[100:103]\n"
      "v_exp_f32 v188, v72\n"
      "v_mfma_f32_16x16x32_bf16 v[108:111], a[68:71], a[4:7], v[164:167]\n"
      "v_exp_f32 v189, v73\n"
      "v_mfma_f32_16x16x32_bf16 v[108:111], a[76:79], a[20:23], v[108:111]\n"
      "v_xor_b32 v242, 0x10000, v242\n"
      "v_xor_b32 v243, 0x10000, v243\n"
      "v_mfma_f32_16x16x32_bf16 v[108:111], a[84:87], a[36:39], v[108:111]\n"
      "v_exp_f32 v190, v74\n"
      "v_mfma_f32_16x16x32_bf16 v[108:111], a[92:95], a[52:55], v[108:111]\n"
      "v_exp_f32 v191, v75\n"
      "v_mfma_f32_16x16x32_bf16 v[116:119], a[68:71], a[8:11], v[168:171]\n"
      "v_cvt_pk_bf16_f32 v132, v188, v189\n"
      "v_cvt_pk_bf16_f32 v133, v190, v191\n"
      "v_mfma_f32_16x16x32_bf16 v[116:119], a[76:79], a[24:27], v[116:119]\n"
      "v_exp_f32 v192, v76\n"
      "s_waitcnt lgkmcnt(0)\n"
      "v_mfma_f32_16x16x32_bf16 v[116:119], a[84:87], a[40:43], v[116:119]\n"
      "ds_read_b64_tr_b16 a[112:113], v244 offset:33792\n"
      "ds_read_b64_tr_b16 a[114:115], v244 offset:37888\n"
      "v_mfma_f32_16x16x32_bf16 v[116:119], a[92:95], a[56:59], v[116:119]\n"
      "v_exp_f32 v193, v77\n"
      "v_mfma_f32_16x16x32_bf16 v[124:127], a[68:71], a[12:15], v[172:175]\n"
      "ds_read_b64_tr_b16 a[116:117], v245 offset:33792\n"
      "v_cvt_pk_bf16_f32 v134, v192, v193\n"
      "v_mfma_f32_16x16x32_bf16 v[124:127], a[76:79], a[28:31], v[124:127]\n"
      "v_exp_f32 v194, v78\n"
      "v_mfma_f32_16x16x32_bf16 v[124:127], a[84:87], a[44:47], v[124:127]\n"
      "ds_read_b64_tr_b16 a[118:119], v245 offset:37888\n"
      "ds_read_b64_tr_b16 a[120:121], v246 offset:33792\n"
      "v_mfma_f32_16x16x32_bf16 v[124:127], a[92:95], a[60:63], v[124:127]\n"
      "v_exp_f32 v195, v79\n"
      "v_mfma_f32_16x16x32_bf16 v[0:3], a[96:99], v[144:147], v[0:3]\n"
      "ds_read_b64_tr_b16 a[122:123], v246 offset:37888\n"
      "v_cvt_pk_bf16_f32 v135, v194, v195\n"
      "v_mfma_f32_16x16x32_bf16 v[4:7], a[96:99], v[148:151], v[4:7]\n"
      "v_exp_f32 v188, v80\n"
      "v_mfma_f32_16x16x32_bf16 v[184:187], a[132:135], v[132:135], v[184:187]\n"
      "ds_read_b64_tr_b16 a[124:125], v247 offset:33792\n"
      "ds_read_b64_tr_b16 a[126:127], v247 offset:37888\n"
      "v_mfma_f32_16x16x32_bf16 a[144:147], a[96:99], v[152:155], a[144:147]\n"
      "v_exp_f32 v189, v81\n"
      "v_mfma_f32_16x16x32_bf16 a[148:151], a[96:99], v[156:159], a[148:151]\n"
      "v_exp_f32 v190, v82\n"
      "v_mfma_f32_16x16x32_bf16 a[156:159], a[100:103], v[156:159], a[156:159]\n"
      "v_exp_f32 v191, v83\n"
      "v_mfma_f32_16x16x32_bf16 a[152:155], a[100:103], v[152:155], a[152:155]\n"
      "v_cvt_pk_bf16_f32 v136, v188, v189\n"
      "v_cvt_pk_bf16_f32 v137, v190, v191\n"
      "v_mfma_f32_16x16x32_bf16 v[12:15], a[100:103], v[148:151], v[12:15]\n"
      "v_exp_f32 v192, v84\n"
      "v_mfma_f32_16x16x32_bf16 v[8:11], a[100:103], v[144:147], v[8:11]\n"
      "v_exp_f32 v193, v85\n"
      "v_mfma_f32_16x16x32_bf16 v[16:19], a[104:107], v[144:147], v[16:19]\n"
      "s_add_u32 s38, s38, s38\n"
      "v_exp_f32 v194, v86\n"
      "v_mfma_f32_16x16x32_bf16 v[20:23], a[104:107], v[148:151], v[20:23]\n"
      "s_memrealtime s[68:69]\n"
      "v_exp_f32 v195, v87\n"
      "v_mfma_f32_16x16x32_bf16 a[160:163], a[104:107], v[152:155], a[160:163]\n"
      "v_cvt_pk_bf16_f32 v138, v192, v193\n"
      "v_cvt_pk_bf16_f32 v139, v194, v195\n"
      "v_mfma_f32_16x16x32_bf16 a[164:167], a[104:107], v[156:159], a[164:167]\n"
      "v_exp_f32 v188, v88\n"
      "v_mfma_f32_16x16x32_bf16 v[184:187], a[136:139], v[136:139], v[184:187]\n"
      "s_nop 0\n"
      "v_mfma_f32_16x16x32_bf16 a[172:175], a[108:111], v[156:159], a[172:175]\n"
      "s_nop 0\n"
      "v_mfma_f32_16x16x32_bf16 a[168:171], a[108:111], v[152:155], a[168:171]\n"
      "s_nop 0\n"
      "v_mfma_f32_16x16x32_bf16 v[28:31], a[108:111], v[148:151], v[28:31]\n"
      "s_nop 0\n"
      "s_cbranch_scc0 MH_%=\n"
      "END_%=:\n"
      "s_waitcnt lgkmcnt(0)\n"
      "v_mfma_f32_16x16x32_bf16 v[24:27], a[108:111], v[144:147], v[24:27]\n"
      "v_mov_b32 v236, s68\n"
      "v_exp_f32 v189, v89\n"
      "v_mfma_f32_16x16x32_bf16 v[32:35], a[112:115], v[144:147], v[32:35]\n"
      "s_mov_b64 exec, 1\n"
      "global_store_dwordx2 v238, v[236:237], s[46:47] offset:8 sc1\n"
      "s_mov_b64 exec, -1\n"
      "v_mfma_f32_16x16x32_bf16 v[36:39], a[112:115], v[148:151], v[36:39]\n"
      "s_mov_b32 s82, s68\n"
      "v_exp_f32 v190, v90\n"
      "v_mfma_f32_16x16x32_bf16 a[176:179], a[112:115], v[152:155], a[176:179]\n"
      "v_exp_f32 v191, v91\n"
      "v_mfma_f32_16x16x32_bf16 a[180:183], a[112:115], v[156:159], a[180:183]\n"
      "v_cvt_pk_bf16_f32 v140, v188, v189\n"
      "v_cvt_pk_bf16_f32 v141, v190, v191\n"
      "v_mfma_f32_16x16x32_bf16 a[188:191], a[116:119], v[156:159], a[188:191]\n"
      "v_exp_f32 v192, v92\n"
      "s_waitcnt vmcnt(5)\n"
      "s_barrier\n"
      "v_mfma_f32_16x16x32_bf16 a[184:187], a[116:119], v[152:155], a[184:187]\n"
      "ds_read_b64_tr_b16 a[80:81], v224 offset:0\n"
      "ds_read_b64_tr_b16 a[82:83], v224 offset:4096\n"
      "v_mfma_f32_16x16x32_bf16 v[44:47], a[116:119], v[148:151], v[44:47]\n"
      "ds_read_b64_tr_b16 a[84:85], v225 offset:0\n"
      "ds_read_b64_tr_b16 a[86:87], v225 offset:4096\n"
      "v_mfma_f32_16x16x32_bf16 v[40:43], a[116:119], v[144:147], v[40:43]\n"
      "s_mov_b32 s74, 0xffff0000\n"
      "ds_read_b64_tr_b16 a[88:89], v226 offset:0\n"
      "ds_read_b64_tr_b16 a[90:91], v226 offset:4096\n"
      "v_mfma_f32_16x16x32_bf16 v[48:51], a[120:123], v[144:147], v[48:51]\n"
      "s_mov_b32 s75, 0xffff0000\n"
      "ds_read_b64_tr_b16 a[92:93], v227 offset:0\n"
      "ds_read_b64_tr_b16 a[94:95], v227 offset:4096\n"
      "v_mfma_f32_16x16x32_bf16 v[52:55], a[120:123], v[148:151], v[52:55]\n"
      "s_mov_b32 s94, 0\n"
      "v_exp_f32 v193, v93\n"
      "v_mfma_f32_16x16x32_bf16 a[192:195], a[120:123], v[152:155], a[192:195]\n"
      "s_mov_b32 s95, -1\n"
      "ds_read_b64_tr_b16 a[16:17], v224 offset:1024\n"
      "v_cvt_pk_bf16_f32 v142, v192, v193\n"
      "v_mfma_f32_16x16x32_bf16 a[196:199], a[120:123], v[156:159], a[196:199]\n"
      "v_exp_f32 v194, v94\n"
      "v_mfma_f32_16x16x32_bf16 a[204:207], a[124:127], v[156:159], a[204:207]\n"
      "ds_read_b64_tr_b16 a[18:19], v224 offset:5120\n"
      "ds_read_b64_tr_b16 a[20:21], v225 offset:1024\n"
      "v_mfma_f32_16x16x32_bf16 a[200:203], a[124:127], v[152:155], a[200:203]\n"
      "v_exp_f32 v195, v95\n"
      "v_mfma_f32_16x16x32_bf16 v[60:63], a[124:127], v[148:151], v[60:63]\n"
      "ds_read_b64_tr_b16 a[22:23], v225 offset:5120\n"
      "v_cvt_pk_bf16_f32 v143, v194, v195\n"
      "v_mfma_f32_16x16x32_bf16 v[56:59], a[124:127], v[144:147], v[56:59]\n"
      "ds_read_b64_tr_b16 a[24:25], v226 offset:1024\n"
      "ds_read_b64_tr_b16 a[26:27], v226 offset:5120\n"
      "v_mfma_f32_16x16x32_bf16 v[184:187], a[140:143], v[140:143], v[184:187]\n"
      "ds_read_b64_tr_b16 a[28:29], v227 offset:1024\n"
      "ds_read_b64_tr_b16 a[30:31], v227 offset:5120\n"
      "ds_read_b64_tr_b16 a[0:1], v220 offset:1024\n"
      "ds_read_b64_tr_b16 a[2:3], v220 offset:5120\n"
      "ds_read_b64_tr_b16 a[4:5], v221 offset:1024\n"
      "ds_read_b64_tr_b16 a[6:7], v221 offset:5120\n"
      "ds_read_b64_tr_b16 a[8:9], v222 offset:1024\n"
      "ds_read_b64_tr_b16 a[10:11], v222 offset:5120\n"
      "ds_read_b64_tr_b16 a[12:13], v223 offset:1024\n"
      "ds_read_b64_tr_b16 a[14:15], v223 offset:5120\n"
      "v_max3_f32 v196, v184, v185, v186\n"
      "v_max_f32 v196, v196, v187\n"
      "v_cmp_nge_f32 vcc, 0x4b800000, v196\n"
      "s_cbranch_vccnz FS3_%=\n"
      "FR3_%=:\n"
      "s_waitcnt lgkmcnt(15)\n"
      "v_mfma_f32_16x16x32_bf16 a[176:179], a[80:83], v[136:139], a[176:179]\n"
      "v_add_f32 v180, v180, v184\n"
      "v_add_f32 v181, v181, v185\n"
      "v_mfma_f32_16x16x32_bf16 a[180:183], a[80:83], v[140:143], a[180:183]\n"
      "v_add_f32 v182, v182, v186\n"
      "v_add_f32 v183, v183, v187\n"
      "v_mfma_f32_16x16x32_bf16 a[184:187], a[84:87], v[136:139], a[184:187]\n"
      "v_exp_f32 v188, v112\n"
      "v_mfma_f32_16x16x32_bf16 a[188:191], a[84:87], v[140:143], a[188:191]\n"
      "ds_write_b128 v232, a[176:179] offset:0\n"
      "v_exp_f32 v189, v113\n"
      "v_mfma_f32_16x16x32_bf16 a[192:195], a[88:91], v[136:139], a[192:195]\n"
      "v_exp_f32 v190, v114\n"
      "v_mfma_f32_16x16x32_bf16 a[196:199], a[88:91], v[140:143], a[196:199]\n"
      "v_exp_f32 v191, v115\n"
      "v_mfma_f32_16x16x32_bf16 a[200:203], a[92:95], v[136:139], a[200:203]\n"
      "ds_write_b128 v232, a[180:183] offset:1024\n"
      "v_exp_f32 v192, v116\n"
      "v_mfma_f32_16x16x32_bf16 a[204:207], a[92:95], v[140:143], a[204:207]\n"
      "v_exp_f32 v193, v117\n"
      "v_exp_f32 v194, v118\n"
      "v_cvt_pk_bf16_f32 v152, v188, v189\n"
      "v_exp_f32 v195, v119\n"
      "v_cvt_pk_bf16_f32 v153, v190, v191\n"
      "ds_write_b128 v232, a[184:187] offset:2048\n"
      "v_exp_f32 v188, v120\n"
      "v_cvt_pk_bf16_f32 v154, v192, v193\n"
      "v_mfma_f32_16x16x32_bf16 v[32:35], a[80:83], v[128:131], v[32:35]\n"
      "v_exp_f32 v189, v121\n"
      "v_cvt_pk_bf16_f32 v155, v194, v195\n"
      "v_exp_f32 v190, v122\n"
      "v_cvt_pk_bf16_f32 v156, v188, v189\n"
      "v_mfma_f32_16x16x32_bf16 v[184:187], a[136:139], v[152:155], 0\n"
      "ds_write_b128 v234, v[152:155] offset:0\n"
      "v_exp_f32 v191, v123\n"
      "v_mfma_f32_16x16x32_bf16 v[36:39], a[80:83], v[132:135], v[36:39]\n"
      "v_exp_f32 v192, v124\n"
      "v_cvt_pk_bf16_f32 v157, v190, v191\n"
      "v_exp_f32 v193, v125\n"
      "v_exp_f32 v194, v126\n"
      "v_cvt_pk_bf16_f32 v158, v192, v193\n"
      "ds_write_b128 v232, a[188:191] offset:3072\n"
      "v_mfma_f32_16x16x32_bf16 v[40:43], a[84:87], v[128:131], v[40:43]\n"
      "v_exp_f32 v195, v127\n"
      "v_exp_f32 v188, v96\n"
      "v_cvt_pk_bf16_f32 v159, v194, v195\n"
      "v_exp_f32 v189, v97\n"
      "v_mfma_f32_16x16x32_bf16 v[44:47], a[84:87], v[132:135], v[44:47]\n"
      "v_exp_f32 v190, v98\n"
      "ds_write_b128 v234, v[156:159] offset:1024\n"
      "v_cvt_pk_bf16_f32 v144, v188, v189\n"
      "v_mfma_f32_16x16x32_bf16 v[184:187], a[140:143], v[156:159], v[184:187]\n"
      "v_exp_f32 v191, v99\n"
      "v_exp_f32 v192, v100\n"
      "v_cvt_pk_bf16_f32 v145, v190, v191\n"
      "v_mfma_f32_16x16x32_bf16 v[48:51], a[88:91], v[128:131], v[48:51]\n"
      "v_exp_f32 v193, v101\n"
      "ds_write_b128 v232, a[192:195] offset:4096\n"
      "v_exp_f32 v194, v102\n"
      "v_cvt_pk_bf16_f32 v146, v192, v193\n"
      "v_exp_f32 v195, v103\n"
      "v_mfma_f32_16x16x32_bf16 v[52:55], a[88:91], v[132:135], v[52:55]\n"
      "v_exp_f32 v188, v104\n"
      "v_cvt_pk_bf16_f32 v147, v194, v195\n"
      "v_exp_f32 v189, v105\n"
      "ds_write_b128 v232, a[196:199] offset:5120\n"
      "v_exp_f32 v190, v106\n"
      "v_cvt_pk_bf16_f32 v148, v188, v189\n"
      "v_mfma_f32_16x16x32_bf16 v[184:187], a[128:131], v[144:147], v[184:187]\n"
      "v_exp_f32 v191, v107\n"
      "v_exp_f32 v192, v108\n"
      "v_cvt_pk_bf16_f32 v149, v190, v191\n"
      "v_exp_f32 v193, v109\n"
      "ds_write_b128 v232, a[200:203] offset:6144\n"
      "v_mfma_f32_16x16x32_bf16 v[56:59], a[92:95], v[128:131], v[56:59]\n"
      "v_exp_f32 v194, v110\n"
      "v_cvt_pk_bf16_f32 v150, v192, v193\n"
      "v_exp_f32 v195, v111\n"
      "v_cndmask_b32 v229, v180, v181, s[74:75]\n"
      "v_cvt_pk_bf16_f32 v151, v194, v195\n"
      "v_mfma_f32_16x16x32_bf16 v[60:63], a[92:95], v[132:135], v[60:63]\n"
      "v_cndmask_b32 v200, v182, v183, s[74:75]\n"
      "ds_write_b128 v232, a[204:207] offset:7168\n"
      "v_cndmask_b32 v228, v176, v177, s[74:75]\n"
      "v_mfma_f32_16x16x32_bf16 v[184:187], a[132:135], v[148:151], v[184:187]\n"
      "s_nop 7\n"
      "v_max3_f32 v196, v184, v185, v186\n"
      "v_max_f32 v196, v196, v187\n"
      "v_cmp_nge_f32 vcc, 0x4b800000, v196\n"
      "s_cbranch_vccnz FS8_%=\n"
      "FR8_%=:\n"
      "v_cndmask_b32 v202, v186, v187, s[74:75]\n"
      "v_cndmask_b32 v201, v184, v185, s[74:75]\n"
      "v_add_f32 v200, v200, v202\n"
      "ds_write_b32 v234, v200 offset:4096\n"
      "v_add_f32 v229, v229, v201\n"
      "s_waitcnt lgkmcnt(8)\n"
      "v_mfma_f32_16x16x32_bf16 v[32:35], a[16:19], v[144:147], v[32:35]\n"
      "v_mfma_f32_16x16x32_bf16 v[36:39], a[16:19], v[148:151], v[36:39]\n"
      "v_mfma_f32_16x16x32_bf16 v[40:43], a[20:23], v[144:147], v[40:43]\n"
      "s_waitcnt lgkmcnt(0)\n"
      "s_barrier\n"
      "v_mfma_f32_16x16x32_bf16 v[44:47], a[20:23], v[148:151], v[44:47]\n"
      "ds_read_b32 v208, v209\n"
      "ds_read_b128 v[160:163], v235 offset:0\n"
      "v_mfma_f32_16x16x32_bf16 v[48:51], a[24:27], v[144:147], v[48:51]\n"
      "ds_read_b128 v[164:167], v235 offset:1024\n"
      "ds_read_b128 v[64:67], v233 offset:0\n"
      "v_mfma_f32_16x16x32_bf16 v[52:55], a[24:27], v[148:151], v[52:55]\n"
      "ds_read_b128 v[68:71], v233 offset:1024\n"
      "ds_read_b128 v[72:75], v233 offset:2048\n"
      "ds_read_b128 v[76:79], v233 offset:3072\n"
      "v_mfma_f32_16x16x32_bf16 v[56:59], a[28:31], v[144:147], v[56:59]\n"
      "ds_read_b128 v[80:83], v233 offset:4096\n"
      "ds_read_b128 v[84:87], v233 offset:5120\n"
      "v_mfma_f32_16x16x32_bf16 v[60:63], a[28:31], v[148:151], v[60:63]\n"
      "ds_read_b128 v[88:91], v233 offset:6144\n"
      "ds_read_b128 v[92:95], v233 offset:7168\n"
      "ds_read_b32 v204, v235 offset:4096\n"
      "s_waitcnt lgkmcnt(11)\n"
      "v_cmp_ne_u32 vcc, 0, v208\n"
      "s_cbranch_vccnz SLOW_%=\n"
      "AGAIN_%=:\n"
      "s_waitcnt lgkmcnt(9)\n"
      "v_mfma_f32_16x16x32_bf16 v[32:35], a[0:3], v[160:163], v[32:35]\n"
      "v_mfma_f32_16x16x32_bf16 v[36:39], a[0:3], v[164:167], v[36:39]\n"
      "v_mfma_f32_16x16x32_bf16 v[40:43], a[4:7], v[160:163], v[40:43]\n"
      "v_mfma_f32_16x16x32_bf16 v[44:47], a[4:7], v[164:167], v[44:47]\n"
      "v_mfma_f32_16x16x32_bf16 v[48:51], a[8:11], v[160:163], v[48:51]\n"
      "v_mfma_f32_16x16x32_bf16 v[52:55], a[8:11], v[164:167], v[52:55]\n"
      "v_mfma_f32_16x16x32_bf16 v[56:59], a[12:15], v[160:163], v[56:59]\n"
      "v_mfma_f32_16x16x32_bf16 v[60:63], a[12:15], v[164:167], v[60:63]\n"
      "global_load_dwordx2 v[206:207], v239, s[46:47] offset:8 sc1\n"
      "s_waitcnt lgkmcnt(7)\n"
      "v_pk_add_f32 v[32:33], v[32:33], v[64:65]\n"
      "v_pk_add_f32 v[34:35], v[34:35], v[66:67]\n"
      "v_pk_add_f32 v[36:37], v[36:37], v[68:69]\n"
      "v_pk_add_f32 v[38:39], v[38:39], v[70:71]\n"
      "v_cvt_pk_bf16_f32 v212, v32, v33\n"
      "v_cvt_pk_bf16_f32 v213, v34, v35\n"
      "v_cvt_pk_bf16_f32 v214, v36, v37\n"
      "v_cvt_pk_bf16_f32 v215, v38, v39\n"
      "buffer_store_dwordx4 v[212:215], v241, s[24:27], s78 offen offset:0 sc1\n"
      "s_waitcnt lgkmcnt(5)\n"
      "v_pk_add_f32 v[40:41], v[40:41], v[72:73]\n"
      "v_pk_add_f32 v[42:43], v[42:43], v[74:75]\n"
      "v_pk_add_f32 v[44:45], v[44:45], v[76:77]\n"
      "v_pk_add_f32 v[46:47], v[46:47], v[78:79]\n"
      "v_cvt_pk_bf16_f32 v216, v40, v41\n"
      "v_cvt_pk_bf16_f32 v217, v42, v43\n"
      "v_cvt_pk_bf16_f32 v218, v44, v45\n"
      "v_cvt_pk_bf16_f32 v219, v46, v47\n"
      "buffer_store_dwordx4 v[216:219], v241, s[24:27], s78 offen offset:1024 sc1\n"
      "s_waitcnt lgkmcnt(3)\n"
      "v_pk_add_f32 v[48:49], v[48:49], v[80:81]\n"
      "v_pk_add_f32 v[50:51], v[50:51], v[82:83]\n"
      "v_pk_add_f32 v[52:53], v[52:53], v[84:85]\n"
      "v_pk_add_f32 v[54:55], v[54:55], v[86:87]\n"
      "v_cvt_pk_bf16_f32 v220, v48, v49\n"
      "v_cvt_pk_bf16_f32 v221, v50, v51\n"
      "v_cvt_pk_bf16_f32 v222, v52, v53\n"
      "v_cvt_pk_bf16_f32 v223, v54, v55\n"
      "buffer_store_dwordx4 v[220:223], v241, s[24:27], s78 offen offset:2048 sc1\n"
      "s_waitcnt lgkmcnt(0)\n"
      "v_pk_add_f32 v[56:57], v[56:57], v[88:89]\n"
      "v_pk_add_f32 v[58:59], v[58:59], v[90:91]\n"
      "v_pk_add_f32 v[60:61], v[60:61], v[92:93]\n"
      "v_pk_add_f32 v[62:63], v[62:63], v[94:95]\n"
      "v_cvt_pk_bf16_f32 v224, v56, v57\n"
      "v_cvt_pk_bf16_f32 v225, v58, v59\n"
      "v_cvt_pk_bf16_f32 v226, v60, v61\n"
      "v_cvt_pk_bf16_f32 v227, v62, v63\n"
      "buffer_store_dwordx4 v[224:227], v241, s[24:27], s78 offen offset:3072 sc1\n"
      "v_add_f32 v229, v229, v204\n"
      "v_cndmask_b32 v230, v228, v229, s[94:95]\n"
      "v_add_u32 v231, s40, v230\n"
      "v_add3_u32 v231, v231, v212, v213\n"
      "v_add3_u32 v231, v231, v214, v215\n"
      "v_add3_u32 v231, v231, v216, v217\n"
      "v_add3_u32 v231, v231, v218, v219\n"
      "v_add3_u32 v231, v231, v220, v221\n"
      "v_add3_u32 v231, v231, v222, v223\n"
      "v_add3_u32 v231, v231, v224, v225\n"
      "v_add3_u32 v231, v231, v226, v227\n"
      "buffer_store_dwordx2 v[230:231], v240, s[24:27], s78 offen sc1\n"
      "v_xor_b32 v244, 0x10000, v244\n"
      "v_xor_b32 v245, 0x10000, v245\n"
      "v_xor_b32 v246, 0x10000, v246\n"
      "v_xor_b32 v247, 0x10000, v247\n"
      "s_waitcnt vmcnt(7)\n"
      "s_barrier\n"
      "ds_read_b64_tr_b16 a[64:65], v244 offset:16384\n"
      "ds_read_b64_tr_b16 a[66:67], v244 offset:20480\n"
      "ds_read_b64_tr_b16 a[68:69], v245 offset:16384\n"
      "ds_read_b64_tr_b16 a[70:71], v245 offset:20480\n"
      "ds_read_b64_tr_b16 a[72:73], v246 offset:16384\n"
      "ds_read_b64_tr_b16 a[74:75], v246 offset:20480\n"
      "ds_read_b64_tr_b16 a[76:77], v247 offset:16384\n"
      "ds_read_b64_tr_b16 a[78:79], v247 offset:20480\n"
      "s_waitcnt lgkmcnt(4)\n"
      "v_mfma_f32_16x16x32_bf16 a[144:147], a[64:67], v[136:139], a[144:147]\n"
      "ds_read_b64_tr_b16 a[96:97], v244 offset:17408\n"
      "ds_read_b64_tr_b16 a[98:99], v244 offset:21504\n"
      "v_mfma_f32_16x16x32_bf16 a[148:151], a[64:67], v[140:143], a[148:151]\n"
      "ds_read_b64_tr_b16 a[100:101], v245 offset:17408\n"
      "ds_read_b64_tr_b16 a[102:103], v245 offset:21504\n"
      "v_mfma_f32_16x16x32_bf16 a[152:155], a[68:71], v[136:139], a[152:155]\n"
      "ds_read_b64_tr_b16 a[104:105], v246 offset:17408\n"
      "ds_read_b64_tr_b16 a[106:107], v246 offset:21504\n"
      "v_mfma_f32_16x16x32_bf16 a[156:159], a[68:71], v[140:143], a[156:159]\n"
      "ds_read_b64_tr_b16 a[108:109], v247 offset:17408\n"
      "ds_read_b64_tr_b16 a[110:111], v247 offset:21504\n"
      "s_waitcnt lgkmcnt(8)\n"
      "v_mfma_f32_16x16x32_bf16 a[160:163], a[72:75], v[136:139], a[160:163]\n"
      "s_sub_u32 s76, 18432, s78\n"
      "v_mfma_f32_16x16x32_bf16 a[164:167], a[72:75], v[140:143], a[164:167]\n"
      "s_mov_b32 s63, 0x40000\n"
      "v_mfma_f32_16x16x32_bf16 a[168:171], a[76:79], v[136:139], a[168:171]\n"
      "v_mov_b32 v198, 0\n"
      "v_mfma_f32_16x16x32_bf16 a[172:175], a[76:79], v[140:143], a[172:175]\n"
      "v_and_b32 v197, 0xf0, v241\n"
      "s_waitcnt lgkmcnt(0)\n"
      "v_mfma_f32_16x16x32_bf16 a[144:147], a[96:99], v[152:155], a[144:147]\n"
      "v_lshrrev_b32 v196, 3, v241\n"
      "v_mfma_f32_16x16x32_bf16 a[148:151], a[96:99], v[156:159], a[148:151]\n"
      "v_and_b32 v196, 32, v196\n"
      "v_mfma_f32_16x16x32_bf16 a[152:155], a[100:103], v[152:155], a[152:155]\n"
      "v_lshl_or_b32 v196, v197, 4, v196\n"
      "v_mfma_f32_16x16x32_bf16 a[156:159], a[100:103], v[156:159], a[156:159]\n"
      "v_lshrrev_b32 v199, 5, v241\n"
      "v_mfma_f32_16x16x32_bf16 a[160:163], a[104:107], v[152:155], a[160:163]\n"
      "v_and_or_b32 v196, v199, 16, v196\n"
      "v_mfma_f32_16x16x32_bf16 a[164:167], a[104:107], v[156:159], a[164:167]\n"
      "v_mov_b32 v191, s40\n"
      "v_mfma_f32_16x16x32_bf16 a[168:171], a[108:111], v[152:155], a[168:171]\n"
      "v_mfma_f32_16x16x32_bf16 a[172:175], a[108:111], v[156:159], a[172:175]\n"
      "s_waitcnt vmcnt(0)\n"
      "v_subrev_u32 v205, s82, v206\n"
      "v_cmp_ne_u32 vcc, s40, v207\n"
      "s_cbranch_vccnz LAG_%=\n"
      "v_cmp_lt_i32 vcc, 16, v205\n"
      "s_cbranch_vccnz LAG_%=\n"
      "READ_%=:\n"
      "buffer_load_dwordx2 v[160:161], v240, s[24:27], s76 offen sc1\n"
      "buffer_load_dwordx4 v[96:99], v241, s[24:27], s76 offen offset:0 sc1\n"
      "buffer_load_dwordx4 v[100:103], v241, s[24:27], s76 offen offset:1024 sc1\n"
      "buffer_load_dwordx4 v[104:107], v241, s[24:27], s76 offen offset:2048 sc1\n"
      "buffer_load_dwordx4 v[108:111], v241, s[24:27], s76 offen offset:3072 sc1\n"
      "v_mfma_f32_16x16x32_bf16 v[0:3], a[64:67], v[128:131], v[0:3]\n"
      "v_mfma_f32_16x16x32_bf16 v[4:7], a[64:67], v[132:135], v[4:7]\n"
      "ds_write_b128 v232, a[144:147] offset:32768\n"
      "v_mfma_f32_16x16x32_bf16 v[8:11], a[68:71], v[128:131], v[8:11]\n"
      "v_mfma_f32_16x16x32_bf16 v[12:15], a[68:71], v[132:135], v[12:15]\n"
      "ds_write_b128 v232, a[148:151] offset:33792\n"
      "v_mfma_f32_16x16x32_bf16 v[16:19], a[72:75], v[128:131], v[16:19]\n"
      "v_mfma_f32_16x16x32_bf16 v[20:23], a[72:75], v[132:135], v[20:23]\n"
      "ds_write_b128 v232, a[152:155] offset:34816\n"
      "v_mfma_f32_16x16x32_bf16 v[24:27], a[76:79], v[128:131], v[24:27]\n"
      "v_mfma_f32_16x16x32_bf16 v[28:31], a[76:79], v[132:135], v[28:31]\n"
      "ds_write_b128 v232, a[156:159] offset:35840\n"
      "v_mfma_f32_16x16x32_bf16 v[0:3], a[96:99], v[144:147], v[0:3]\n"
      "v_mfma_f32_16x16x32_bf16 v[4:7], a[96:99], v[148:151], v[4:7]\n"
      "ds_write_b128 v232, a[160:163] offset:36864\n"
      "v_mfma_f32_16x16x32_bf16 v[8:11], a[100:103], v[144:147], v[8:11]\n"
      "v_mfma_f32_16x16x32_bf16 v[12:15], a[100:103], v[148:151], v[12:15]\n"
      "ds_write_b128 v232, a[164:167] offset:37888\n"
      "v_mfma_f32_16x16x32_bf16 v[16:19], a[104:107], v[144:147], v[16:19]\n"
      "v_mfma_f32_16x16x32_bf16 v[20:23], a[104:107], v[148:151], v[20:23]\n"
      "ds_write_b128 v232, a[168:171] offset:38912\n"
      "v_mfma_f32_16x16x32_bf16 v[24:27], a[108:111], v[144:147], v[24:27]\n"
      "v_mfma_f32_16x16x32_bf16 v[28:31], a[108:111], v[148:151], v[28:31]\n"
      "ds_write_b128 v232, a[172:175] offset:39936\n"
      "s_waitcnt lgkmcnt(0)\n"
      "s_barrier\n"
      "ds_read_b128 v[64:67], v233 offset:32768\n"
      "ds_read_b128 v[68:71], v233 offset:33792\n"
      "ds_read_b128 v[72:75], v233 offset:34816\n"
      "ds_read_b128 v[76:79], v233 offset:35840\n"
      "ds_read_b128 v[80:83], v233 offset:36864\n"
      "ds_read_b128 v[84:87], v233 offset:37888\n"
      "ds_read_b128 v[88:91], v233 offset:38912\n"
      "ds_read_b128 v[92:95], v233 offset:39936\n"
      "s_waitcnt lgkmcnt(6)\n"
      "v_pk_add_f32 v[0:1], v[0:1], v[64:65]\n"
      "v_pk_add_f32 v[2:3], v[2:3], v[66:67]\n"
      "v_pk_add_f32 v[4:5], v[4:5], v[68:69]\n"
      "v_pk_add_f32 v[6:7], v[6:7], v[70:71]\n"
      "s_waitcnt lgkmcnt(4)\n"
      "v_pk_add_f32 v[8:9], v[8:9], v[72:73]\n"
      "v_pk_add_f32 v[10:11], v[10:11], v[74:75]\n"
      "v_pk_add_f32 v[12:13], v[12:13], v[76:77]\n"
      "v_pk_add_f32 v[14:15], v[14:15], v[78:79]\n"
      "s_waitcnt lgkmcnt(2)\n"
      "v_pk_add_f32 v[16:17], v[16:17], v[80:81]\n"
      "v_pk_add_f32 v[18:19], v[18:19], v[82:83]\n"
      "v_pk_add_f32 v[20:21], v[20:21], v[84:85]\n"
      "v_pk_add_f32 v[22:23], v[22:23], v[86:87]\n"
      "s_waitcnt lgkmcnt(0)\n"
      "v_pk_add_f32 v[24:25], v[24:25], v[88:89]\n"
      "v_pk_add_f32 v[26:27], v[26:27], v[90:91]\n"
      "v_pk_add_f32 v[28:29], v[28:29], v[92:93]\n"
      "v_pk_add_f32 v[30:31], v[30:31], v[94:95]\n"
      "TRY_%=:\n"
      "s_waitcnt vmcnt(4)\n"
      "v_mov_b32 v162, v160\n"
      "v_add_u32 v231, s40, v160\n"
      "s_nop 0\n"
      "v_permlane32_swap_b32 v160, v162\n"
      "v_sub_f32 v168, v160, v228\n"
      "v_min_f32 v172, 0, -v168\n"
      "v_min_f32 v168, 0, v168\n"
      "v_exp_f32 v168, v168\n"
      "v_exp_f32 v172, v172\n"
      "v_mul_f32 v163, v229, v168\n"
      "v_fmac_f32 v163, v162, v172\n"
      "v_rcp_f32 v163, v163\n"
      "s_nop 0\n"
      "v_mul_f32 v168, v168, v163\n"
      "v_mul_f32 v172, v172, v163\n"
      "v_mov_b32 v169, v168\n"
      "v_mov_b32 v173, v172\n"
      "s_nop 0\n"
      "v_permlane16_swap_b32 v168, v169\n"
      "v_permlane16_swap_b32 v172, v173\n"
      "v_pk_mul_f32 v[32:33], v[0:1], v[168:169] op_sel:[0,0] op_sel_hi:[1,0]\n"
      "v_pk_mul_f32 v[34:35], v[2:3], v[168:169] op_sel:[0,0] op_sel_hi:[1,0]\n"
      "v_pk_mul_f32 v[36:37], v[4:5], v[168:169] op_sel:[0,1] op_sel_hi:[1,1]\n"
      "v_pk_mul_f32 v[38:39], v[6:7], v[168:169] op_sel:[0,1] op_sel_hi:[1,1]\n"
      "v_pk_mul_f32 v[40:41], v[8:9], v[168:169] op_sel:[0,0] op_sel_hi:[1,0]\n"
      "v_pk_mul_f32 v[42:43], v[10:11], v[168:169] op_sel:[0,0] op_sel_hi:[1,0]\n"
      "v_pk_mul_f32 v[44:45], v[12:13], v[168:169] op_sel:[0,1] op_sel_hi:[1,1]\n"
      "v_pk_mul_f32 v[46:47], v[14:15], v[168:169] op_sel:[0,1] op_sel_hi:[1,1]\n"
      "v_pk_mul_f32 v[48:49], v[16:17], v[168:169] op_sel:[0,0] op_sel_hi:[1,0]\n"
      "v_pk_mul_f32 v[50:51], v[18:19], v[168:169] op_sel:[0,0] op_sel_hi:[1,0]\n"
      "v_pk_mul_f32 v[52:53], v[20:21], v[168:169] op_sel:[0,1] op_sel_hi:[1,1]\n"
      "v_pk_mul_f32 v[54:55], v[22:23], v[168:169] op_sel:[0,1] op_sel_hi:[1,1]\n"
      "v_pk_mul_f32 v[56:57], v[24:25], v[168:169] op_sel:[0,0] op_sel_hi:[1,0]\n"
      "v_pk_mul_f32 v[58:59], v[26:27], v[168:169] op_sel:[0,0] op_sel_hi:[1,0]\n"
      "v_pk_mul_f32 v[60:61], v[28:29], v[168:169] op_sel:[0,1] op_sel_hi:[1,1]\n"
      "v_pk_mul_f32 v[62:63], v[30:31], v[168:169] op_sel:[0,1] op_sel_hi:[1,1]\n"
      "s_waitcnt vmcnt(3)\n"
      "v_add3_u32 v231, v231, v96, v97\n"
      "v_add3_u32 v231, v231, v98, v99\n"
      "s_waitcnt vmcnt(2)\n"
      "v_add3_u32 v231, v231, v100, v101\n"
      "v_add3_u32 v231, v231, v102, v103\n"
      "s_waitcnt vmcnt(1)\n"
      "v_add3_u32 v231, v231, v104, v105\n"
      "v_add3_u32 v231, v231, v106, v107\n"
      "s_waitcnt vmcnt(0)\n"
      "v_add3_u32 v231, v231, v108, v109\n"
      "v_add3_u32 v231, v231, v110, v111\n"
      "v_cmp_ne_u32 vcc, v231, v161\n"
      "s_cbranch_vccnz RETRY_%=\n"
      "GOT_%=:\n"
      "v_lshlrev_b32 v0, 16, v96\n"
      "v_and_b32 v1, 0xffff0000, v96\n"
      "v_lshlrev_b32 v2, 16, v97\n"
      "v_and_b32 v3, 0xffff0000, v97\n"
      "v_lshlrev_b32 v8, 16, v100\n"
      "v_and_b32 v9, 0xffff0000, v100\n"
      "v_lshlrev_b32 v10, 16, v101\n"
      "v_and_b32 v11, 0xffff0000, v101\n"
      "v_pk_fma_f32 v[32:33], v[0:1], v[172:173], v[32:33] op_sel:[0,0,0] op_sel_hi:[1,0,1]\n"
      "v_pk_fma_f32 v[34:35], v[2:3], v[172:173], v[34:35] op_sel:[0,0,0] op_sel_hi:[1,0,1]\n"
      "v_pk_fma_f32 v[40:41], v[8:9], v[172:173], v[40:41] op_sel:[0,0,0] op_sel_hi:[1,0,1]\n"
      "v_pk_fma_f32 v[42:43], v[10:11], v[172:173], v[42:43] op_sel:[0,0,0] op_sel_hi:[1,0,1]\n"
      "v_cvt_pk_bf16_f32 v112, v32, v33\n"
      "v_cvt_pk_bf16_f32 v114, v40, v41\n"
      "v_cvt_pk_bf16_f32 v113, v34, v35\n"
      "v_cvt_pk_bf16_f32 v115, v42, v43\n"
      "v_permlane16_swap_b32 v112, v114\n"
      "s_nop 0\n"
      "v_permlane16_swap_b32 v113, v115\n"
      "global_store_dwordx4 v196, v[112:115], s[30:31] offset:-4096 sc1 nt\n"
      "v_lshlrev_b32 v16, 16, v104\n"
      "v_and_b32 v17, 0xffff0000, v104\n"
      "v_lshlrev_b32 v18, 16, v105\n"
      "v_and_b32 v19, 0xffff0000, v105\n"
      "v_lshlrev_b32 v24, 16, v108\n"
      "v_and_b32 v25, 0xffff0000, v108\n"
      "v_lshlrev_b32 v26, 16, v109\n"
      "v_and_b32 v27, 0xffff0000, v109\n"
      "v_pk_fma_f32 v[48:49], v[16:17], v[172:173], v[48:49] op_sel:[0,0,0] op_sel_hi:[1,0,1]\n"
      "v_pk_fma_f32 v[50:51], v[18:19], v[172:173], v[50:51] op_sel:[0,0,0] op_sel_hi:[1,0,1]\n"
      "v_pk_fma_f32 v[56:57], v[24:25], v[172:173], v[56:57] op_sel:[0,0,0] op_sel_hi:[1,0,1]\n"
      "v_pk_fma_f32 v[58:59], v[26:27], v[172:173], v[58:59] op_sel:[0,0,0] op_sel_hi:[1,0,1]\n"
      "v_cvt_pk_bf16_f32 v116, v48, v49\n"
      "v_cvt_pk_bf16_f32 v118, v56, v57\n"
      "v_cvt_pk_bf16_f32 v117, v50, v51\n"
      "v_cvt_pk_bf16_f32 v119, v58, v59\n"
      "v_permlane16_swap_b32 v116, v118\n"
      "s_nop 0\n"
      "v_permlane16_swap_b32 v117, v119\n"
      "global_store_dwordx4 v196, v[116:119], s[30:31] offset:-4032 sc1 nt\n"
      "v_lshlrev_b32 v4, 16, v98\n"
      "v_and_b32 v5, 0xffff0000, v98\n"
      "v_lshlrev_b32 v6, 16, v99\n"
      "v_and_b32 v7, 0xffff0000, v99\n"
      "v_lshlrev_b32 v12, 16, v102\n"
      "v_and_b32 v13, 0xffff0000, v102\n"
      "v_lshlrev_b32 v14, 16, v103\n"
      "v_and_b32 v15, 0xffff0000, v103\n"
      "v_pk_fma_f32 v[36:37], v[4:5], v[172:173], v[36:37] op_sel:[0,1,0] op_sel_hi:[1,1,1]\n"
      "v_pk_fma_f32 v[38:39], v[6:7], v[172:173], v[38:39] op_sel:[0,1,0] op_sel_hi:[1,1,1]\n"
      "v_pk_fma_f32 v[44:45], v[12:13], v[172:173], v[44:45] op_sel:[0,1,0] op_sel_hi:[1,1,1]\n"
      "v_pk_fma_f32 v[46:47], v[14:15], v[172:173], v[46:47] op_sel:[0,1,0] op_sel_hi:[1,1,1]\n"
      "v_cvt_pk_bf16_f32 v120, v36, v37\n"
      "v_cvt_pk_bf16_f32 v122, v44, v45\n"
      "v_cvt_pk_bf16_f32 v121, v38, v39\n"
      "v_cvt_pk_bf16_f32 v123, v46, v47\n"
      "v_permlane16_swap_b32 v120, v122\n"
      "s_nop 0\n"
      "v_permlane16_swap_b32 v121, v123\n"
      "global_store_dwordx4 v196, v[120:123], s[30:31] offset:0 sc1 nt\n"
      "v_lshlrev_b32 v20, 16, v106\n"
      "v_and_b32 v21, 0xffff0000, v106\n"
      "v_lshlrev_b32 v22, 16, v107\n"
      "v_and_b32 v23, 0xffff0000, v107\n"
      "v_lshlrev_b32 v28, 16, v110\n"
      "v_and_b32 v29, 0xffff0000, v110\n"
      "v_lshlrev_b32 v30, 16, v111\n"
      "v_and_b32 v31, 0xffff0000, v111\n"
      "v_pk_fma_f32 v[52:53], v[20:21], v[172:173], v[52:53] op_sel:[0,1,0] op_sel_hi:[1,1,1]\n"
      "v_pk_fma_f32 v[54:55], v[22:23], v[172:173], v[54:55] op_sel:[0,1,0] op_sel_hi:[1,1,1]\n"
      "v_pk_fma_f32 v[60:61], v[28:29], v[172:173], v[60:61] op_sel:[0,1,0] op_sel_hi:[1,1,1]\n"
      "v_pk_fma_f32 v[62:63], v[30:31], v[172:173], v[62:63] op_sel:[0,1,0] op_sel_hi:[1,1,1]\n"
      "v_cvt_pk_bf16_f32 v124, v52, v53\n"
      "v_cvt_pk_bf16_f32 v126, v60, v61\n"
      "v_cvt_pk_bf16_f32 v125, v54, v55\n"
      "v_cvt_pk_bf16_f32 v127, v62, v63\n"
      "v_permlane16_swap_b32 v124, v126\n"
      "s_nop 0\n"
      "v_permlane16_swap_b32 v125, v127\n"
      "global_store_dwordx4 v196, v[124:127], s[30:31] offset:64 sc1 nt\n"
      "s_mov_b64 exec, 1\n"
      "global_store_dword v198, v191, s[46:47] sc1\n"
      "s_mov_b64 exec, -1\n"
      "ENDE_%=:\n"
      ".subsection 1\n"
      "MH_%=:\n"
      "s_cmp_lg_u32 s54, 0\n"
      "s_waitcnt vmcnt(12) lgkmcnt(0)\n"
      "s_cbranch_scc1 W6_%=\n"
      "s_waitcnt vmcnt(8)\n"
      "W6_%=:\n"
      "s_barrier\n"
      "v_mfma_f32_16x16x32_bf16 v[24:27], a[108:111], v[144:147], v[24:27]\n"
      "s_xor_b64 s[56:57], s[56:57], s[32:33]\n"
      "ds_read_b128 a[64:67], v242 offset:0\n"
      "ds_read_b128 a[72:75], v243 offset:0\n"
      "v_mfma_f32_16x16x32_bf16 v[32:35], a[112:115], v[144:147], v[32:35]\n"
      "s_add_u32 s61, s61, 1\n"
      "ds_read_b128 a[80:83], v242 offset:1024\n"
      "ds_read_b128 a[88:91], v243 offset:1024\n"
      "v_mfma_f32_16x16x32_bf16 v[36:39], a[112:115], v[148:151], v[36:39]\n"
      "s_add_u32 s62, s62, 0x4000\n"
      "v_exp_f32 v189, v89\n"
      "v_mfma_f32_16x16x32_bf16 a[176:179], a[112:115], v[152:155], a[176:179]\n"
      "s_and_b32 s62, s62, 0x1ffff\n"
      "v_exp_f32 v190, v90\n"
      "v_mfma_f32_16x16x32_bf16 a[180:183], a[112:115], v[156:159], a[180:183]\n"
      "v_readlane_b32 s60, v219, s61\n"
      "v_exp_f32 v191, v91\n"
      "v_mfma_f32_16x16x32_bf16 a[188:191], a[116:119], v[156:159], a[188:191]\n"
      "s_add_u32 s66, s66, s86\n"
      "v_cvt_pk_bf16_f32 v140, v188, v189\n"
      "v_mfma_f32_16x16x32_bf16 a[184:187], a[116:119], v[152:155], a[184:187]\n"
      "v_exp_f32 v192, v92\n"
      "v_mfma_f32_16x16x32_bf16 v[44:47], a[116:119], v[148:151], v[44:47]\n"
      "s_add_u32 s82, s82, s86\n"
      "v_exp_f32 v193, v93\n"
      "v_mfma_f32_16x16x32_bf16 v[40:43], a[116:119], v[144:147], v[40:43]\n"
      "s_add_u32 s65, s65, s86\n"
      "v_cvt_pk_bf16_f32 v141, v190, v191\n"
      "v_cvt_pk_bf16_f32 v142, v192, v193\n"
      "v_mfma_f32_16x16x32_bf16 v[48:51], a[120:123], v[144:147], v[48:51]\n"
      "s_max_u32 s88, s66, s65\n"
      "v_exp_f32 v194, v94\n"
      "v_mfma_f32_16x16x32_bf16 v[52:55], a[120:123], v[148:151], v[52:55]\n"
      "v_exp_f32 v195, v95\n"
      "s_waitcnt lgkmcnt(0)\n"
      "v_mfma_f32_16x16x32_bf16 a[192:195], a[120:123], v[152:155], a[192:195]\n"
      "v_cvt_pk_bf16_f32 v143, v194, v195\n"
      "v_xor_b32 v244, 0x10000, v244\n"
      "v_mfma_f32_16x16x32_bf16 a[196:199], a[120:123], v[156:159], a[196:199]\n"
      "ds_read_b128 a[68:71], v242 offset:4096\n"
      "ds_read_b128 a[76:79], v243 offset:4096\n"
      "v_mfma_f32_16x16x32_bf16 v[184:187], a[140:143], v[140:143], v[184:187]\n"
      "s_mov_b32 m0, s62\n"
      "ds_read_b128 a[84:87], v242 offset:5120\n"
      "ds_read_b128 a[92:95], v243 offset:5120\n"
      "v_mfma_f32_16x16x32_bf16 a[204:207], a[124:127], v[156:159], a[204:207]\n"
      "buffer_load_dwordx4 v228, s[56:59], s60 offen nt lds\n"
      "v_xor_b32 v245, 0x10000, v245\n"
      "v_xor_b32 v246, 0x10000, v246\n"
      "v_mfma_f32_16x16x32_bf16 a[200:203], a[124:127], v[152:155], a[200:203]\n"
      "buffer_load_dwordx4 v229, s[56:59], s60 offen offset:1024 nt lds\n"
      "v_xor_b32 v247, 0x10000, v247\n"
      "v_max3_f32 v196, v184, v185, v186\n"
      "v_mfma_f32_16x16x32_bf16 v[60:63], a[124:127], v[148:151], v[60:63]\n"
      "buffer_load_dwordx4 v230, s[56:59], s60 offen offset:2048 nt lds\n"
      "v_max_f32 v196, v196, v187\n"
      "v_cmp_nge_f32 vcc, 0x4b800000, v196\n"
      "v_mfma_f32_16x16x32_bf16 v[56:59], a[124:127], v[144:147], v[56:59]\n"
      "buffer_load_dwordx4 v231, s[56:59], s60 offen offset:3072 nt lds\n"
      "s_cbranch_vccnz FS11_%=\n"
      "FR11_%=:\n"
      "v_mfma_f32_16x16x32_bf16 v[64:67], a[64:67], a[0:3], v[160:163]\n"
      "v_exp_f32 v188, v96\n"
      "v_mfma_f32_16x16x32_bf16 v[64:67], a[72:75], a[16:19], v[64:67]\n"
      "v_add_f32 v180, v180, v184\n"
      "v_add_f32 v181, v181, v185\n"
      "v_mfma_f32_16x16x32_bf16 v[64:67], a[80:83], a[32:35], v[64:67]\n"
      "v_exp_f32 v189, v97\n"
      "v_mfma_f32_16x16x32_bf16 v[64:67], a[88:91], a[48:51], v[64:67]\n"
      "v_add_f32 v182, v182, v186\n"
      "v_add_f32 v183, v183, v187\n"
      "v_mfma_f32_16x16x32_bf16 v[72:75], a[64:67], a[4:7], v[164:167]\n"
      "v_exp_f32 v190, v98\n"
      "v_mfma_f32_16x16x32_bf16 v[72:75], a[72:75], a[20:23], v[72:75]\n"
      "v_exp_f32 v191, v99\n"
      "v_mfma_f32_16x16x32_bf16 v[72:75], a[80:83], a[36:39], v[72:75]\n"
      "v_cvt_pk_bf16_f32 v144, v188, v189\n"
      "v_cvt_pk_bf16_f32 v145, v190, v191\n"
      "v_mfma_f32_16x16x32_bf16 v[72:75], a[88:91], a[52:55], v[72:75]\n"
      "v_exp_f32 v192, v100\n"
      "v_mfma_f32_16x16x32_bf16 v[80:83], a[64:67], a[8:11], v[168:171]\n"
      "s_add_u32 s39, s39, s39\n"
      "v_exp_f32 v193, v101\n"
      "v_mfma_f32_16x16x32_bf16 v[80:83], a[72:75], a[24:27], v[80:83]\n"
      "s_memrealtime s[68:69]\n"
      "v_exp_f32 v194, v102\n"
      "v_mfma_f32_16x16x32_bf16 v[80:83], a[80:83], a[40:43], v[80:83]\n"
      "v_exp_f32 v195, v103\n"
      "v_mfma_f32_16x16x32_bf16 v[80:83], a[88:91], a[56:59], v[80:83]\n"
      "v_cvt_pk_bf16_f32 v146, v192, v193\n"
      "v_cvt_pk_bf16_f32 v147, v194, v195\n"
      "v_mfma_f32_16x16x32_bf16 v[88:91], a[64:67], a[12:15], v[172:175]\n"
      "s_nop 0\n"
      "v_mfma_f32_16x16x32_bf16 v[184:187], a[128:131], v[144:147], 0\n"
      "s_nop 0\n"
      "v_mfma_f32_16x16x32_bf16 v[88:91], a[72:75], a[28:31], v[88:91]\n"
      "s_nop 0\n"
      "v_mfma_f32_16x16x32_bf16 v[88:91], a[80:83], a[44:47], v[88:91]\n"
      "s_nop 0\n"
      "s_cbranch_scc1 SP1_%=\n"
      "s_branch HD1_%=\n"
      "HOOK_%=:\n"
      "buffer_load_dword v250, v251, s[20:23], 0 offen sc1\n"
      "s_swappc_b64 s[84:85], s[84:85]\n"
      "s_waitcnt vmcnt(12)\n"
      "v_subrev_u32 v250, s71, v250\n"
      "v_min_u32 v250, 0x258, v250\n"
      "s_nop 1\n"
      "v_add_u32_dpp v250, v250, v250 quad_perm:[1,0,3,2] row_mask:0xf bank_mask:0xf\n"
      "s_nop 1\n"
      "v_add_u32_dpp v250, v250, v250 quad_perm:[2,3,0,1] row_mask:0xf bank_mask:0xf\n"
      "s_nop 1\n"
      "v_add_u32_dpp v250, v250, v250 row_half_mirror row_mask:0xf bank_mask:0xf\n"
      "s_nop 0\n"
      "v_readfirstlane_b32 s67, v250\n"
      "s_sub_u32 s66, s66, s81\n"
      "s_lshr_b32 s67, s67, 3\n"
      "s_add_u32 s66, s66, s67\n"
      "s_max_u32 s88, s66, s65\n"
      "s_mov_b32 s86, 57\n"
      "s_mov_b32 s77, 171\n"
      "v_bfe_u32 v251, v241, 2, 7\n"
      "v_add_u32 v251, s79, v251\n"
      "buffer_load_dword v250, v251, s[20:23], 0 offen sc1\n"
      "s_swappc_b64 s[84:85], s[84:85]\n"
      "s_mov_b32 s89, 0\n"
      "s_swappc_b64 s[84:85], s[84:85]\n"
      "s_mov_b32 s86, 55\n"
      "s_mov_b32 s77, 165\n"
      "s_swappc_b64 s[84:85], s[84:85]\n"
      "s_cmp_lt_u32 s89, 1\n"
      "s_cselect_b32 s67, 2048, s72\n"
      "s_mov_b32 s89, 0\n"
      "s_cmp_lt_u32 s67, 2048\n"
      "s_cbranch_scc0 NV0_%=\n"
      "v_and_b32 v216, 0x1f0, v241\n"
      "v_lshlrev_b32 v216, 4, v216\n"
      "v_mov_b32 v215, s82\n"
      "s_lshr_b32 s67, s67, 4\n"
      "s_add_u32 s67, s67, 8192\n"
      "buffer_store_dword v215, v216, s[20:23], s67 offen sc1\n"
      "NV0_%=:\n"
      "s_swappc_b64 s[84:85], s[84:85]\n"
      "s_mov_b32 s86, 53\n"
      "s_mov_b32 s77, 159\n"
      "s_swappc_b64 s[84:85], s[84:85]\n"
      "buffer_load_dword v250, v251, s[20:23], 0 offen sc1\n"
      "s_swappc_b64 s[84:85], s[84:85]\n"
      "s_cmp_lt_u32 s89, 2\n"
      "s_cselect_b32 s67, 2048, s72\n"
      "s_mov_b32 s89, 0\n"
      "s_cmp_lt_u32 s67, 2048\n"
      "s_cbranch_scc0 NV1_%=\n"
      "v_and_b32 v216, 0x1f0, v241\n"
      "v_lshlrev_b32 v216, 4, v216\n"
      "v_mov_b32 v215, s82\n"
      "s_lshr_b32 s67, s67, 4\n"
      "s_add_u32 s67, s67, 8192\n"
      "buffer_store_dword v215, v216, s[20:23], s67 offen sc1\n"
      "NV1_%=:\n"
      "s_swappc_b64 s[84:85], s[84:85]\n"
      "s_waitcnt vmcnt(12)\n"
      "v_sub_u32 v250, s82, v250\n"
      "v_cmp_gt_u32 vcc, 1600, v250\n"
      "s_movk_i32 s76, 50\n"
      "s_nop 2\n"
      "s_bcnt1_i32_b32 s67, vcc_lo\n"
      "s_cmp_eq_u32 s70, 60\n"
      "s_cselect_b32 s67, 0, s67\n"
      "s_cmp_ge_u32 s67, 7\n"
      "s_cbranch_scc1 SM0_%=\n"
      "buffer_load_dword v250, v251, s[20:23], 0 offen sc1\n"
      "s_swappc_b64 s[84:85], s[84:85]\n"
      "s_cmp_lt_u32 s89, 2\n"
      "s_cselect_b32 s67, 2048, s72\n"
      "s_mov_b32 s89, 0\n"
      "s_cmp_lt_u32 s67, 2048\n"
      "s_cbranch_scc0 NV2_%=\n"
      "v_and_b32 v216, 0x1f0, v241\n"
      "v_lshlrev_b32 v216, 4, v216\n"
      "v_mov_b32 v215, s82\n"
      "s_lshr_b32 s67, s67, 4\n"
      "s_add_u32 s67, s67, 8192\n"
      "buffer_store_dword v215, v216, s[20:23], s67 offen sc1\n"
      "NV2_%=:\n"
      "s_swappc_b64 s[84:85], s[84:85]\n"
      "s_waitcnt vmcnt(12)\n"
      "v_sub_u32 v250, s82, v250\n"
      "v_cmp_gt_u32 vcc, 1600, v250\n"
      "s_movk_i32 s76, 66\n"
      "s_nop 2\n"
      "s_bcnt1_i32_b32 s67, vcc_lo\n"
      "s_cmp_eq_u32 s70, 60\n"
      "s_cselect_b32 s67, 0, s67\n"
      "s_cmp_ge_u32 s67, 7\n"
      "s_cbranch_scc1 SM1_%=\n"
      "buffer_load_dword v250, v251, s[20:23], 0 offen sc1\n"
      "s_swappc_b64 s[84:85], s[84:85]\n"
      "s_min_i32 s70, s70, 150\n"
      "s_waitcnt vmcnt(12)\n"
      "v_sub_u32 v250, s82, v250\n"
      "v_cmp_gt_u32 vcc, 1600, v250\n"
      "s_movk_i32 s76, 70\n"
      "s_nop 2\n"
      "s_bcnt1_i32_b32 s67, vcc_lo\n"
      "s_cmp_eq_u32 s70, 60\n"
      "s_cselect_b32 s67, 0, s67\n"
      "s_cmp_ge_u32 s67, 7\n"
      "s_cbranch_scc1 SM1_%=\n"
      "s_swappc_b64 s[84:85], s[84:85]\n"
      "s_cmp_lt_u32 s89, 2\n"
      "s_cselect_b32 s67, 2048, s72\n"
      "s_mov_b32 s89, 0\n"
      "s_cmp_lt_u32 s67, 2048\n"
      "s_cbranch_scc0 NV3_%=\n"
      "v_and_b32 v216, 0x1f0, v241\n"
      "v_lshlrev_b32 v216, 4, v216\n"
      "v_mov_b32 v215, s82\n"
      "s_lshr_b32 s67, s67, 4\n"
      "s_add_u32 s67, s67, 8192\n"
      "buffer_store_dword v215, v216, s[20:23], s67 offen sc1\n"
      "NV3_%=:\n"
      "s_swappc_b64 s[84:85], s[84:85]\n"
      "buffer_load_dword v250, v251, s[20:23], 0 offen sc1\n"
      "s_swappc_b64 s[84:85], s[84:85]\n"
      "s_waitcnt vmcnt(12)\n"
      "v_sub_u32 v250, s82, v250\n"
      "v_cmp_gt_u32 vcc, 1600, v250\n"
      "s_movk_i32 s76, 78\n"
      "s_nop 2\n"
      "s_bcnt1_i32_b32 s67, vcc_lo\n"
      "s_cmp_eq_u32 s70, 60\n"
      "s_cselect_b32 s67, 0, s67\n"
      "s_cmp_ge_u32 s67, 7\n"
      "s_cbranch_scc1 SM1_%=\n"
      "s_swappc_b64 s[84:85], s[84:85]\n"
      "s_getpc_b64 s[74:75]\n"
      "PF_%=:\n"
      "s_sub_u32 s74, s74, PF_%=-END_%=\n"
      "s_subb_u32 s75, s75, 0\n"
      "s_lshl_b32 s76, %[blk], 7\n"
      "s_and_b32 s76, s76, 0x1c00\n"
      "s_min_u32 s76, s76, ENDE_%=-END_%=-1024\n"
      "v_add_u32 v200, s76, v241\n"
      "global_load_dwordx4 v[200:203], v200, s[74:75]\n"
      "v_mov_b32 v212, 0\n"
      "global_load_dword v213, v212, s[30:31] nt\n"
      "buffer_load_dword v214, off, s[24:27], s78 nt\n"
      "s_swappc_b64 s[84:85], s[84:85]\n"
      "SM1_%=:\n"
      "s_add_u32 s66, s66, s76\n"
      "s_max_u32 s88, s66, s65\n"
      "s_min_i32 s70, s70, 150\n"
      "s_mov_b32 s87, 0\n"
      "s_branch SM2_%=\n"
      "SM0_%=:\n"
      "s_or_b32 s39, s39, 0xc0000000\n"
      "s_mov_b32 s52, 0xaaaaaaaa\n"
      "s_mov_b32 s53, 0xaaaaaaaa\n"
      "s_add_u32 s66, s66, s76\n"
      "s_max_u32 s88, s66, s65\n"
      "s_min_i32 s70, s70, 150\n"
      "s_mov_b32 s87, 0\n"
      "s_mov_b32 s86, 59\n"
      "s_mov_b32 s77, 177\n"
      "s_swappc_b64 s[84:85], s[84:85]\n"
      "s_mov_b32 s86, 57\n"
      "s_mov_b32 s77, 171\n"
      "s_swappc_b64 s[84:85], s[84:85]\n"
      "SM2_%=:\n"
      "s_mov_b32 s86, 55\n"
      "s_mov_b32 s77, 165\n"
      "s_mov_b32 s52, 0x0\n"
      "s_mov_b32 s53, 0x80000\n"
      "s_swappc_b64 s[84:85], s[84:85]\n"
      "s_getpc_b64 s[74:75]\n"
      "PG_%=:\n"
      "s_sub_u32 s74, s74, PG_%=-END_%=\n"
      "s_subb_u32 s75, s75, 0\n"
      "s_lshl_b32 s76, %[blk], 7\n"
      "s_and_b32 s76, s76, 0x1c00\n"
      "s_min_u32 s76, s76, ENDE_%=-END_%=-1024\n"
      "v_add_u32 v200, s76, v241\n"
      "global_load_dwordx4 v[200:203], v200, s[74:75]\n"
      "v_mov_b32 v212, 0\n"
      "global_load_dword v213, v212, s[30:31] nt\n"
      "buffer_load_dword v214, off, s[24:27], s78 nt\n"
      "s_swappc_b64 s[84:85], s[84:85]\n"
      "GW0_%=:\n"
      "s_memrealtime s[68:69]\n"
      "s_waitcnt lgkmcnt(0)\n"
      "s_getreg_b32 s67, hwreg(HW_REG_IB_STS, 0, 4)\n"
      "s_cmp_gt_u32 s67, 9\n"
      "s_cselect_b32 s67, 0, 0\n"
      "s_addc_u32 s89, s89, 0\n"
      "s_add_u32 s66, s66, s67\n"
      "s_add_u32 s88, s88, s67\n"
      "GS0_%=:\n"
      "s_sub_u32 s67, s68, s88\n"
      "s_cmp_ge_i32 s67, 0\n"
      "s_cbranch_scc1 GX0_%=\n"
      "s_memrealtime s[68:69]\n"
      "s_waitcnt lgkmcnt(0)\n"
      "s_branch GS0_%=\n"
      "FS1_%=:\n"
      "s_call_b64 s[96:97], FB1_%=\n"
      "s_branch FR1_%=\n"
      "FS2_%=:\n"
      "s_call_b64 s[96:97], FB0_%=\n"
      "s_branch FR2_%=\n"
      "FS3_%=:\n"
      "s_call_b64 s[96:97], FB0_%=\n"
      "s_branch FR3_%=\n"
      "GW4_%=:\n"
      "s_memrealtime s[68:69]\n"
      "s_waitcnt lgkmcnt(0)\n"
      "s_getreg_b32 s67, hwreg(HW_REG_IB_STS, 0, 4)\n"
      "s_cmp_gt_u32 s67, 9\n"
      "s_cselect_b32 s67, 0, 0\n"
      "s_addc_u32 s89, s89, 0\n"
      "s_add_u32 s66, s66, s67\n"
      "s_add_u32 s88, s88, s67\n"
      "GS4_%=:\n"
      "s_sub_u32 s67, s68, s88\n"
      "s_cmp_ge_i32 s67, 0\n"
      "s_cbranch_scc1 GX4_%=\n"
      "s_memrealtime s[68:69]\n"
      "s_waitcnt lgkmcnt(0)\n"
      "s_branch GS4_%=\n"
      "GW5_%=:\n"
      "s_memrealtime s[68:69]\n"
      "s_waitcnt lgkmcnt(0)\n"
      "s_getreg_b32 s67, hwreg(HW_REG_IB_STS, 0, 4)\n"
      "s_cmp_gt_u32 s67, 9\n"
      "s_cselect_b32 s67, 0, 0\n"
      "s_addc_u32 s89, s89, 0\n"
      "s_add_u32 s66, s66, s67\n"
      "s_add_u32 s88, s88, s67\n"
      "GS5_%=:\n"
      "s_sub_u32 s67, s68, s88\n"
      "s_cmp_ge_i32 s67, 0\n"
      "s_cbranch_scc1 GX5_%=\n"
      "s_memrealtime s[68:69]\n"
      "s_waitcnt lgkmcnt(0)\n"
      "s_branch GS5_%=\n"
      "GW6_%=:\n"
      "s_memrealtime s[68:69]\n"
      "s_waitcnt lgkmcnt(0)\n"
      "s_getreg_b32 s67, hwreg(HW_REG_IB_STS, 0, 4)\n"
      "s_cmp_gt_u32 s67, 9\n"
      "s_cselect_b32 s67, 0, 0\n"
      "s_addc_u32 s89, s89, 0\n"
      "s_add_u32 s66, s66, s67\n"
      "s_add_u32 s88, s88, s67\n"
      "GS6_%=:\n"
      "s_sub_u32 s67, s68, s88\n"
      "s_cmp_ge_i32 s67, 0\n"
      "s_cbranch_scc1 GX6_%=\n"
      "s_memrealtime s[68:69]\n"
      "s_waitcnt lgkmcnt(0)\n"
      "s_branch GS6_%=\n"
      "GW7_%=:\n"
      "s_memrealtime s[68:69]\n"
      "s_waitcnt lgkmcnt(0)\n"
      "s_getreg_b32 s67, hwreg(HW_REG_IB_STS, 0, 4)\n"
      "s_cmp_gt_u32 s67, 9\n"
      "s_cselect_b32 s67, 0, 0\n"
      "s_addc_u32 s89, s89, 0\n"
      "s_add_u32 s66, s66, s67\n"
      "s_add_u32 s88, s88, s67\n"
      "GS7_%=:\n"
      "s_sub_u32 s67, s68, s88\n"
      "s_cmp_ge_i32 s67, 0\n"
      "s_cbranch_scc1 GX7_%=\n"
      "s_memrealtime s[68:69]\n"
      "s_waitcnt lgkmcnt(0)\n"
      "s_branch GS7_%=\n"
      "FS8_%=:\n"
      "s_call_b64 s[96:97], FB1_%=\n"
      "s_branch FR8_%=\n"
      "LAG_%=:\n"
      "s_memrealtime s[68:69]\n"
      "s_waitcnt lgkmcnt(0)\n"
      "s_mov_b32 s88, s68\n"
      "LG1_%=:\n"
      "v_cmp_ne_u32 vcc, s40, v207\n"
      "s_cbranch_vccz LG2_%=\n"
      "s_sub_u32 s63, s63, 1\n"
      "s_cmp_eq_u32 s63, 0\n"
      "s_cbranch_scc1 READ_%=\n"
      "s_sleep 4\n"
      "global_load_dwordx2 v[206:207], v239, s[46:47] offset:8 sc1\n"
      "s_waitcnt vmcnt(0)\n"
      "s_branch LG1_%=\n"
      "LG2_%=:\n"
      "v_readfirstlane_b32 s67, v206\n"
      "s_sub_u32 s88, s88, s82\n"
      "s_min_u32 s88, s88, 400\n"
      "s_add_u32 s88, s88, s67\n"
      "LG3_%=:\n"
      "s_memrealtime s[68:69]\n"
      "s_waitcnt lgkmcnt(0)\n"
      "s_sub_u32 s67, s88, s68\n"
      "s_sub_u32 s67, s67, 17\n"
      "s_cmp_lt_u32 s67, 400\n"
      "s_cbranch_scc1 LG3_%=\n"
      "s_branch READ_%=\n"
      "RETRY_%=:\n"
      "s_sub_u32 s63, s63, 1\n"
      "s_cbranch_scc1 GOT_%=\n"
      "buffer_load_dwordx2 v[160:161], v240, s[24:27], s76 offen sc1\n"
      "buffer_load_dwordx4 v[96:99], v241, s[24:27], s76 offen offset:0 sc1\n"
      "buffer_load_dwordx4 v[100:103], v241, s[24:27], s76 offen offset:1024 sc1\n"
      "buffer_load_dwordx4 v[104:107], v241, s[24:27], s76 offen offset:2048 sc1\n"
      "buffer_load_dwordx4 v[108:111], v241, s[24:27], s76 offen offset:3072 sc1\n"
      "s_branch TRY_%=\n"
      "FS11_%=:\n"
      "s_call_b64 s[96:97], FB0_%=\n"
      "s_branch FR11_%=\n"
      "COLD_%=:\n"
      "FB0_%=:\n"
      "v_mov_b32 v196, 139248\n"
      "s_nop 7\n"
      "ds_write_b32 v196, v196\n"
      "v_max3_f32 v188, v64, v65, 0\n"
      "v_max3_f32 v189, v72, v73, 0\n"
      "v_max3_f32 v190, v80, v81, 0\n"
      "v_max3_f32 v191, v88, v89, 0\n"
      "v_max3_f32 v188, v188, v66, v67\n"
      "v_max3_f32 v189, v189, v74, v75\n"
      "v_max3_f32 v190, v190, v82, v83\n"
      "v_max3_f32 v191, v191, v90, v91\n"
      "v_max3_f32 v188, v188, v68, v69\n"
      "v_max3_f32 v189, v189, v76, v77\n"
      "v_max3_f32 v190, v190, v84, v85\n"
      "v_max3_f32 v191, v191, v92, v93\n"
      "v_max3_f32 v188, v188, v70, v71\n"
      "v_max3_f32 v189, v189, v78, v79\n"
      "v_max3_f32 v190, v190, v86, v87\n"
      "v_max3_f32 v191, v191, v94, v95\n"
      "v_mov_b32 v192, v188\n"
      "v_mov_b32 v193, v189\n"
      "v_mov_b32 v194, v190\n"
      "v_mov_b32 v195, v191\n"
      "v_permlane16_swap_b32 v188, v192\n"
      "v_permlane16_swap_b32 v189, v193\n"
      "v_permlane16_swap_b32 v190, v194\n"
      "v_permlane16_swap_b32 v191, v195\n"
      "v_max_f32 v188, v188, v192\n"
      "v_max_f32 v189, v189, v193\n"
      "v_max_f32 v190, v190, v194\n"
      "v_max_f32 v191, v191, v195\n"
      "v_mov_b32 v192, v188\n"
      "v_mov_b32 v193, v189\n"
      "v_mov_b32 v194, v190\n"
      "v_mov_b32 v195, v191\n"
      "v_permlane32_swap_b32 v188, v192\n"
      "v_permlane32_swap_b32 v189, v193\n"
      "v_permlane32_swap_b32 v190, v194\n"
      "v_permlane32_swap_b32 v191, v195\n"
      "v_max_f32 v188, v188, v192\n"
      "v_max_f32 v189, v189, v193\n"
      "v_max_f32 v190, v190, v194\n"
      "v_max_f32 v191, v191, v195\n"
      "RS0_%=:\n"
      "v_exp_f32 v192, -v188\n"
      "v_exp_f32 v193, -v189\n"
      "v_exp_f32 v194, -v190\n"
      "v_exp_f32 v195, -v191\n"
      "v_sub_f32 v176, v176, v188\n"
      "v_sub_f32 v177, v177, v189\n"
      "v_sub_f32 v178, v178, v190\n"
      "v_sub_f32 v179, v179, v191\n"
      "v_mov_b32 v160, v176\n"
      "v_mov_b32 v161, v176\n"
      "v_mov_b32 v162, v176\n"
      "v_mov_b32 v163, v176\n"
      "v_mov_b32 v164, v177\n"
      "v_mov_b32 v165, v177\n"
      "v_mov_b32 v166, v177\n"
      "v_mov_b32 v167, v177\n"
      "v_mov_b32 v168, v178\n"
      "v_mov_b32 v169, v178\n"
      "v_mov_b32 v170, v178\n"
      "v_mov_b32 v171, v178\n"
      "v_mov_b32 v172, v179\n"
      "v_mov_b32 v173, v179\n"
      "v_mov_b32 v174, v179\n"
      "v_mov_b32 v175, v179\n"
      "v_sub_f32 v64, v64, v188\n"
      "v_sub_f32 v65, v65, v188\n"
      "v_sub_f32 v66, v66, v188\n"
      "v_sub_f32 v67, v67, v188\n"
      "v_sub_f32 v68, v68, v188\n"
      "v_sub_f32 v69, v69, v188\n"
      "v_sub_f32 v70, v70, v188\n"
      "v_sub_f32 v71, v71, v188\n"
      "v_sub_f32 v72, v72, v189\n"
      "v_sub_f32 v73, v73, v189\n"
      "v_sub_f32 v74, v74, v189\n"
      "v_sub_f32 v75, v75, v189\n"
      "v_sub_f32 v76, v76, v189\n"
      "v_sub_f32 v77, v77, v189\n"
      "v_sub_f32 v78, v78, v189\n"
      "v_sub_f32 v79, v79, v189\n"
      "v_sub_f32 v80, v80, v190\n"
      "v_sub_f32 v81, v81, v190\n"
      "v_sub_f32 v82, v82, v190\n"
      "v_sub_f32 v83, v83, v190\n"
      "v_sub_f32 v84, v84, v190\n"
      "v_sub_f32 v85, v85, v190\n"
      "v_sub_f32 v86, v86, v190\n"
      "v_sub_f32 v87, v87, v190\n"
      "v_sub_f32 v88, v88, v191\n"
      "v_sub_f32 v89, v89, v191\n"
      "v_sub_f32 v90, v90, v191\n"
      "v_sub_f32 v91, v91, v191\n"
      "v_sub_f32 v92, v92, v191\n"
      "v_sub_f32 v93, v93, v191\n"
      "v_sub_f32 v94, v94, v191\n"
      "v_sub_f32 v95, v95, v191\n"
      "v_sub_f32 v96, v96, v188\n"
      "v_sub_f32 v97, v97, v188\n"
      "v_sub_f32 v98, v98, v188\n"
      "v_sub_f32 v99, v99, v188\n"
      "v_sub_f32 v100, v100, v188\n"
      "v_sub_f32 v101, v101, v188\n"
      "v_sub_f32 v102, v102, v188\n"
      "v_sub_f32 v103, v103, v188\n"
      "v_sub_f32 v104, v104, v189\n"
      "v_sub_f32 v105, v105, v189\n"
      "v_sub_f32 v106, v106, v189\n"
      "v_sub_f32 v107, v107, v189\n"
      "v_sub_f32 v108, v108, v189\n"
      "v_sub_f32 v109, v109, v189\n"
      "v_sub_f32 v110, v110, v189\n"
      "v_sub_f32 v111, v111, v189\n"
      "v_sub_f32 v112, v112, v190\n"
      "v_sub_f32 v113, v113, v190\n"
      "v_sub_f32 v114, v114, v190\n"
      "v_sub_f32 v115, v115, v190\n"
      "v_sub_f32 v116, v116, v190\n"
      "v_sub_f32 v117, v117, v190\n"
      "v_sub_f32 v118, v118, v190\n"
      "v_sub_f32 v119, v119, v190\n"
      "v_sub_f32 v120, v120, v191\n"
      "v_sub_f32 v121, v121, v191\n"
      "v_sub_f32 v122, v122, v191\n"
      "v_sub_f32 v123, v123, v191\n"
      "v_sub_f32 v124, v124, v191\n"
      "v_sub_f32 v125, v125, v191\n"
      "v_sub_f32 v126, v126, v191\n"
      "v_sub_f32 v127, v127, v191\n"
      "v_mul_f32 v0, v0, v192\n"
      "v_mul_f32 v1, v1, v192\n"
      "v_mul_f32 v2, v2, v192\n"
      "v_mul_f32 v3, v3, v192\n"
      "v_mul_f32 v4, v4, v193\n"
      "v_mul_f32 v5, v5, v193\n"
      "v_mul_f32 v6, v6, v193\n"
      "v_mul_f32 v7, v7, v193\n"
      "v_mul_f32 v8, v8, v192\n"
      "v_mul_f32 v9, v9, v192\n"
      "v_mul_f32 v10, v10, v192\n"
      "v_mul_f32 v11, v11, v192\n"
      "v_mul_f32 v12, v12, v193\n"
      "v_mul_f32 v13, v13, v193\n"
      "v_mul_f32 v14, v14, v193\n"
      "v_mul_f32 v15, v15, v193\n"
      "v_mul_f32 v16, v16, v192\n"
      "v_mul_f32 v17, v17, v192\n"
      "v_mul_f32 v18, v18, v192\n"
      "v_mul_f32 v19, v19, v192\n"
      "v_mul_f32 v20, v20, v193\n"
      "v_mul_f32 v21, v21, v193\n"
      "v_mul_f32 v22, v22, v193\n"
      "v_mul_f32 v23, v23, v193\n"
      "v_mul_f32 v24, v24, v192\n"
      "v_mul_f32 v25, v25, v192\n"
      "v_mul_f32 v26, v26, v192\n"
      "v_mul_f32 v27, v27, v192\n"
      "v_mul_f32 v28, v28, v193\n"
      "v_mul_f32 v29, v29, v193\n"
      "v_mul_f32 v30, v30, v193\n"
      "v_mul_f32 v31, v31, v193\n"
      "v_mul_f32 v32, v32, v192\n"
      "v_mul_f32 v33, v33, v192\n"
      "v_mul_f32 v34, v34, v192\n"
      "v_mul_f32 v35, v35, v192\n"
      "v_mul_f32 v36, v36, v193\n"
      "v_mul_f32 v37, v37, v193\n"
      "v_mul_f32 v38, v38, v193\n"
      "v_mul_f32 v39, v39, v193\n"
      "v_mul_f32 v40, v40, v192\n"
      "v_mul_f32 v41, v41, v192\n"
      "v_mul_f32 v42, v42, v192\n"
      "v_mul_f32 v43, v43, v192\n"
      "v_mul_f32 v44, v44, v193\n"
      "v_mul_f32 v45, v45, v193\n"
      "v_mul_f32 v46, v46, v193\n"
      "v_mul_f32 v47, v47, v193\n"
      "v_mul_f32 v48, v48, v192\n"
      "v_mul_f32 v49, v49, v192\n"
      "v_mul_f32 v50, v50, v192\n"
      "v_mul_f32 v51, v51, v192\n"
      "v_mul_f32 v52, v52, v193\n"
      "v_mul_f32 v53, v53, v193\n"
      "v_mul_f32 v54, v54, v193\n"
      "v_mul_f32 v55, v55, v193\n"
      "v_mul_f32 v56, v56, v192\n"
      "v_mul_f32 v57, v57, v192\n"
      "v_mul_f32 v58, v58, v192\n"
      "v_mul_f32 v59, v59, v192\n"
      "v_mul_f32 v60, v60, v193\n"
      "v_mul_f32 v61, v61, v193\n"
      "v_mul_f32 v62, v62, v193\n"
      "v_mul_f32 v63, v63, v193\n"
      "v_accvgpr_read_b32 v196, a144\n"
      "v_accvgpr_read_b32 v197, a145\n"
      "v_accvgpr_read_b32 v198, a146\n"
      "v_accvgpr_read_b32 v199, a147\n"
      "v_mul_f32 v196, v196, v194\n"
      "v_mul_f32 v197, v197, v194\n"
      "v_mul_f32 v198, v198, v194\n"
      "v_mul_f32 v199, v199, v194\n"
      "v_accvgpr_write_b32 a144, v196\n"
      "v_accvgpr_write_b32 a145, v197\n"
      "v_accvgpr_write_b32 a146, v198\n"
      "v_accvgpr_write_b32 a147, v199\n"
      "v_accvgpr_read_b32 v196, a148\n"
      "v_accvgpr_read_b32 v197, a149\n"
      "v_accvgpr_read_b32 v198, a150\n"
      "v_accvgpr_read_b32 v199, a151\n"
      "v_mul_f32 v196, v196, v195\n"
      "v_mul_f32 v197, v197, v195\n"
      "v_mul_f32 v198, v198, v195\n"
      "v_mul_f32 v199, v199, v195\n"
      "v_accvgpr_write_b32 a148, v196\n"
      "v_accvgpr_write_b32 a149, v197\n"
      "v_accvgpr_write_b32 a150, v198\n"
      "v_accvgpr_write_b32 a151, v199\n"
      "v_accvgpr_read_b32 v196, a152\n"
      "v_accvgpr_read_b32 v197, a153\n"
      "v_accvgpr_read_b32 v198, a154\n"
      "v_accvgpr_read_b32 v199, a155\n"
      "v_mul_f32 v196, v196, v194\n"
      "v_mul_f32 v197, v197, v194\n"
      "v_mul_f32 v198, v198, v194\n"
      "v_mul_f32 v199, v199, v194\n"
      "v_accvgpr_write_b32 a152, v196\n"
      "v_accvgpr_write_b32 a153, v197\n"
      "v_accvgpr_write_b32 a154, v198\n"
      "v_accvgpr_write_b32 a155, v199\n"
      "v_accvgpr_read_b32 v196, a156\n"
      "v_accvgpr_read_b32 v197, a157\n"
      "v_accvgpr_read_b32 v198, a158\n"
      "v_accvgpr_read_b32 v199, a159\n"
      "v_mul_f32 v196, v196, v195\n"
      "v_mul_f32 v197, v197, v195\n"
      "v_mul_f32 v198, v198, v195\n"
      "v_mul_f32 v199, v199, v195\n"
      "v_accvgpr_write_b32 a156, v196\n"
      "v_accvgpr_write_b32 a157, v197\n"
      "v_accvgpr_write_b32 a158, v198\n"
      "v_accvgpr_write_b32 a159, v199\n"
      "v_accvgpr_read_b32 v196, a160\n"
      "v_accvgpr_read_b32 v197, a161\n"
      "v_accvgpr_read_b32 v198, a162\n"
      "v_accvgpr_read_b32 v199, a163\n"
      "v_mul_f32 v196, v196, v194\n"
      "v_mul_f32 v197, v197, v194\n"
      "v_mul_f32 v198, v198, v194\n"
      "v_mul_f32 v199, v199, v194\n"
      "v_accvgpr_write_b32 a160, v196\n"
      "v_accvgpr_write_b32 a161, v197\n"
      "v_accvgpr_write_b32 a162, v198\n"
      "v_accvgpr_write_b32 a163, v199\n"
      "v_accvgpr_read_b32 v196, a164\n"
      "v_accvgpr_read_b32 v197, a165\n"
      "v_accvgpr_read_b32 v198, a166\n"
      "v_accvgpr_read_b32 v199, a167\n"
      "v_mul_f32 v196, v196, v195\n"
      "v_mul_f32 v197, v197, v195\n"
      "v_mul_f32 v198, v198, v195\n"
      "v_mul_f32 v199, v199, v195\n"
      "v_accvgpr_write_b32 a164, v196\n"
      "v_accvgpr_write_b32 a165, v197\n"
      "v_accvgpr_write_b32 a166, v198\n"
      "v_accvgpr_write_b32 a167, v199\n"
      "v_accvgpr_read_b32 v196, a168\n"
      "v_accvgpr_read_b32 v197, a169\n"
      "v_accvgpr_read_b32 v198, a170\n"
      "v_accvgpr_read_b32 v199, a171\n"
      "v_mul_f32 v196, v196, v194\n"
      "v_mul_f32 v197, v197, v194\n"
      "v_mul_f32 v198, v198, v194\n"
      "v_mul_f32 v199, v199, v194\n"
      "v_accvgpr_write_b32 a168, v196\n"
      "v_accvgpr_write_b32 a169, v197\n"
      "v_accvgpr_write_b32 a170, v198\n"
      "v_accvgpr_write_b32 a171, v199\n"
      "v_accvgpr_read_b32 v196, a172\n"
      "v_accvgpr_read_b32 v197, a173\n"
      "v_accvgpr_read_b32 v198, a174\n"
      "v_accvgpr_read_b32 v199, a175\n"
      "v_mul_f32 v196, v196, v195\n"
      "v_mul_f32 v197, v197, v195\n"
      "v_mul_f32 v198, v198, v195\n"
      "v_mul_f32 v199, v199, v195\n"
      "v_accvgpr_write_b32 a172, v196\n"
      "v_accvgpr_write_b32 a173, v197\n"
      "v_accvgpr_write_b32 a174, v198\n"
      "v_accvgpr_write_b32 a175, v199\n"
      "v_accvgpr_read_b32 v196, a176\n"
      "v_accvgpr_read_b32 v197, a177\n"
      "v_accvgpr_read_b32 v198, a178\n"
      "v_accvgpr_read_b32 v199, a179\n"
      "v_mul_f32 v196, v196, v194\n"
      "v_mul_f32 v197, v197, v194\n"
      "v_mul_f32 v198, v198, v194\n"
      "v_mul_f32 v199, v199, v194\n"
      "v_accvgpr_write_b32 a176, v196\n"
      "v_accvgpr_write_b32 a177, v197\n"
      "v_accvgpr_write_b32 a178, v198\n"
      "v_accvgpr_write_b32 a179, v199\n"
      "v_accvgpr_read_b32 v196, a180\n"
      "v_accvgpr_read_b32 v197, a181\n"
      "v_accvgpr_read_b32 v198, a182\n"
      "v_accvgpr_read_b32 v199, a183\n"
      "v_mul_f32 v196, v196, v195\n"
      "v_mul_f32 v197, v197, v195\n"
      "v_mul_f32 v198, v198, v195\n"
      "v_mul_f32 v199, v199, v195\n"
      "v_accvgpr_write_b32 a180, v196\n"
      "v_accvgpr_write_b32 a181, v197\n"
      "v_accvgpr_write_b32 a182, v198\n"
      "v_accvgpr_write_b32 a183, v199\n"
      "v_accvgpr_read_b32 v196, a184\n"
      "v_accvgpr_read_b32 v197, a185\n"
      "v_accvgpr_read_b32 v198, a186\n"
      "v_accvgpr_read_b32 v199, a187\n"
      "v_mul_f32 v196, v196, v194\n"
      "v_mul_f32 v197, v197, v194\n"
      "v_mul_f32 v198, v198, v194\n"
      "v_mul_f32 v199, v199, v194\n"
      "v_accvgpr_write_b32 a184, v196\n"
      "v_accvgpr_write_b32 a185, v197\n"
      "v_accvgpr_write_b32 a186, v198\n"
      "v_accvgpr_write_b32 a187, v199\n"
      "v_accvgpr_read_b32 v196, a188\n"
      "v_accvgpr_read_b32 v197, a189\n"
      "v_accvgpr_read_b32 v198, a190\n"
      "v_accvgpr_read_b32 v199, a191\n"
      "v_mul_f32 v196, v196, v195\n"
      "v_mul_f32 v197, v197, v195\n"
      "v_mul_f32 v198, v198, v195\n"
      "v_mul_f32 v199, v199, v195\n"
      "v_accvgpr_write_b32 a188, v196\n"
      "v_accvgpr_write_b32 a189, v197\n"
      "v_accvgpr_write_b32 a190, v198\n"
      "v_accvgpr_write_b32 a191, v199\n"
      "v_accvgpr_read_b32 v196, a192\n"
      "v_accvgpr_read_b32 v197, a193\n"
      "v_accvgpr_read_b32 v198, a194\n"
      "v_accvgpr_read_b32 v199, a195\n"
      "v_mul_f32 v196, v196, v194\n"
      "v_mul_f32 v197, v197, v194\n"
      "v_mul_f32 v198, v198, v194\n"
      "v_mul_f32 v199, v199, v194\n"
      "v_accvgpr_write_b32 a192, v196\n"
      "v_accvgpr_write_b32 a193, v197\n"
      "v_accvgpr_write_b32 a194, v198\n"
      "v_accvgpr_write_b32 a195, v199\n"
      "v_accvgpr_read_b32 v196, a196\n"
      "v_accvgpr_read_b32 v197, a197\n"
      "v_accvgpr_read_b32 v198, a198\n"
      "v_accvgpr_read_b32 v199, a199\n"
      "v_mul_f32 v196, v196, v195\n"
      "v_mul_f32 v197, v197, v195\n"
      "v_mul_f32 v198, v198, v195\n"
      "v_mul_f32 v199, v199, v195\n"
      "v_accvgpr_write_b32 a196, v196\n"
      "v_accvgpr_write_b32 a197, v197\n"
      "v_accvgpr_write_b32 a198, v198\n"
      "v_accvgpr_write_b32 a199, v199\n"
      "v_accvgpr_read_b32 v196, a200\n"
      "v_accvgpr_read_b32 v197, a201\n"
      "v_accvgpr_read_b32 v198, a202\n"
      "v_accvgpr_read_b32 v199, a203\n"
      "v_mul_f32 v196, v196, v194\n"
      "v_mul_f32 v197, v197, v194\n"
      "v_mul_f32 v198, v198, v194\n"
      "v_mul_f32 v199, v199, v194\n"
      "v_accvgpr_write_b32 a200, v196\n"
      "v_accvgpr_write_b32 a201, v197\n"
      "v_accvgpr_write_b32 a202, v198\n"
      "v_accvgpr_write_b32 a203, v199\n"
      "v_accvgpr_read_b32 v196, a204\n"
      "v_accvgpr_read_b32 v197, a205\n"
      "v_accvgpr_read_b32 v198, a206\n"
      "v_accvgpr_read_b32 v199, a207\n"
      "v_mul_f32 v196, v196, v195\n"
      "v_mul_f32 v197, v197, v195\n"
      "v_mul_f32 v198, v198, v195\n"
      "v_mul_f32 v199, v199, v195\n"
      "v_accvgpr_write_b32 a204, v196\n"
      "v_accvgpr_write_b32 a205, v197\n"
      "v_accvgpr_write_b32 a206, v198\n"
      "v_accvgpr_write_b32 a207, v199\n"
      "v_mul_f32 v180, v180, v192\n"
      "v_mul_f32 v181, v181, v193\n"
      "v_mul_f32 v182, v182, v194\n"
      "v_mul_f32 v183, v183, v195\n"
      "v_exp_f32 v188, v64\n"
      "v_exp_f32 v189, v65\n"
      "v_exp_f32 v190, v66\n"
      "v_exp_f32 v191, v67\n"
      "v_cvt_pk_bf16_f32 v128, v188, v189\n"
      "v_cvt_pk_bf16_f32 v129, v190, v191\n"
      "v_exp_f32 v192, v68\n"
      "v_exp_f32 v193, v69\n"
      "v_exp_f32 v194, v70\n"
      "v_exp_f32 v195, v71\n"
      "v_cvt_pk_bf16_f32 v130, v192, v193\n"
      "v_cvt_pk_bf16_f32 v131, v194, v195\n"
      "v_exp_f32 v188, v72\n"
      "v_exp_f32 v189, v73\n"
      "v_exp_f32 v190, v74\n"
      "v_exp_f32 v191, v75\n"
      "v_cvt_pk_bf16_f32 v132, v188, v189\n"
      "v_cvt_pk_bf16_f32 v133, v190, v191\n"
      "v_exp_f32 v192, v76\n"
      "v_exp_f32 v193, v77\n"
      "v_exp_f32 v194, v78\n"
      "v_exp_f32 v195, v79\n"
      "v_cvt_pk_bf16_f32 v134, v192, v193\n"
      "v_cvt_pk_bf16_f32 v135, v194, v195\n"
      "v_exp_f32 v188, v80\n"
      "v_exp_f32 v189, v81\n"
      "v_exp_f32 v190, v82\n"
      "v_exp_f32 v191, v83\n"
      "v_cvt_pk_bf16_f32 v136, v188, v189\n"
      "v_cvt_pk_bf16_f32 v137, v190, v191\n"
      "v_exp_f32 v192, v84\n"
      "v_exp_f32 v193, v85\n"
      "v_exp_f32 v194, v86\n"
      "v_exp_f32 v195, v87\n"
      "v_cvt_pk_bf16_f32 v138, v192, v193\n"
      "v_cvt_pk_bf16_f32 v139, v194, v195\n"
      "v_exp_f32 v188, v88\n"
      "v_exp_f32 v189, v89\n"
      "v_exp_f32 v190, v90\n"
      "v_exp_f32 v191, v91\n"
      "v_cvt_pk_bf16_f32 v140, v188, v189\n"
      "v_cvt_pk_bf16_f32 v141, v190, v191\n"
      "v_exp_f32 v192, v92\n"
      "v_exp_f32 v193, v93\n"
      "v_exp_f32 v194, v94\n"
      "v_exp_f32 v195, v95\n"
      "v_cvt_pk_bf16_f32 v142, v192, v193\n"
      "v_cvt_pk_bf16_f32 v143, v194, v195\n"
      "s_nop 1\n"
      "v_mfma_f32_16x16x32_bf16 v[184:187], a[128:131], v[128:131], 0\n"
      "v_mfma_f32_16x16x32_bf16 v[184:187], a[132:135], v[132:135], v[184:187]\n"
      "v_mfma_f32_16x16x32_bf16 v[184:187], a[136:139], v[136:139], v[184:187]\n"
      "v_mfma_f32_16x16x32_bf16 v[184:187], a[140:143], v[140:143], v[184:187]\n"
      "s_nop 7\n"
      "s_setpc_b64 s[96:97]\n"
      "FB1_%=:\n"
      "v_mov_b32 v196, 139248\n"
      "s_nop 7\n"
      "ds_write_b32 v196, v196\n"
      "v_max3_f32 v188, v96, v97, 0\n"
      "v_max3_f32 v189, v104, v105, 0\n"
      "v_max3_f32 v190, v112, v113, 0\n"
      "v_max3_f32 v191, v120, v121, 0\n"
      "v_max3_f32 v188, v188, v98, v99\n"
      "v_max3_f32 v189, v189, v106, v107\n"
      "v_max3_f32 v190, v190, v114, v115\n"
      "v_max3_f32 v191, v191, v122, v123\n"
      "v_max3_f32 v188, v188, v100, v101\n"
      "v_max3_f32 v189, v189, v108, v109\n"
      "v_max3_f32 v190, v190, v116, v117\n"
      "v_max3_f32 v191, v191, v124, v125\n"
      "v_max3_f32 v188, v188, v102, v103\n"
      "v_max3_f32 v189, v189, v110, v111\n"
      "v_max3_f32 v190, v190, v118, v119\n"
      "v_max3_f32 v191, v191, v126, v127\n"
      "v_mov_b32 v192, v188\n"
      "v_mov_b32 v193, v189\n"
      "v_mov_b32 v194, v190\n"
      "v_mov_b32 v195, v191\n"
      "v_permlane16_swap_b32 v188, v192\n"
      "v_permlane16_swap_b32 v189, v193\n"
      "v_permlane16_swap_b32 v190, v194\n"
      "v_permlane16_swap_b32 v191, v195\n"
      "v_max_f32 v188, v188, v192\n"
      "v_max_f32 v189, v189, v193\n"
      "v_max_f32 v190, v190, v194\n"
      "v_max_f32 v191, v191, v195\n"
      "v_mov_b32 v192, v188\n"
      "v_mov_b32 v193, v189\n"
      "v_mov_b32 v194, v190\n"
      "v_mov_b32 v195, v191\n"
      "v_permlane32_swap_b32 v188, v192\n"
      "v_permlane32_swap_b32 v189, v193\n"
      "v_permlane32_swap_b32 v190, v194\n"
      "v_permlane32_swap_b32 v191, v195\n"
      "v_max_f32 v188, v188, v192\n"
      "v_max_f32 v189, v189, v193\n"
      "v_max_f32 v190, v190, v194\n"
      "v_max_f32 v191, v191, v195\n"
      "RS1_%=:\n"
      "v_exp_f32 v192, -v188\n"
      "v_exp_f32 v193, -v189\n"
      "v_exp_f32 v194, -v190\n"
      "v_exp_f32 v195, -v191\n"
      "v_sub_f32 v176, v176, v188\n"
      "v_sub_f32 v177, v177, v189\n"
      "v_sub_f32 v178, v178, v190\n"
      "v_sub_f32 v179, v179, v191\n"
      "v_mov_b32 v160, v176\n"
      "v_mov_b32 v161, v176\n"
      "v_mov_b32 v162, v176\n"
      "v_mov_b32 v163, v176\n"
      "v_mov_b32 v164, v177\n"
      "v_mov_b32 v165, v177\n"
      "v_mov_b32 v166, v177\n"
      "v_mov_b32 v167, v177\n"
      "v_mov_b32 v168, v178\n"
      "v_mov_b32 v169, v178\n"
      "v_mov_b32 v170, v178\n"
      "v_mov_b32 v171, v178\n"
      "v_mov_b32 v172, v179\n"
      "v_mov_b32 v173, v179\n"
      "v_mov_b32 v174, v179\n"
      "v_mov_b32 v175, v179\n"
      "v_sub_f32 v64, v64, v188\n"
      "v_sub_f32 v65, v65, v188\n"
      "v_sub_f32 v66, v66, v188\n"
      "v_sub_f32 v67, v67, v188\n"
      "v_sub_f32 v68, v68, v188\n"
      "v_sub_f32 v69, v69, v188\n"
      "v_sub_f32 v70, v70, v188\n"
      "v_sub_f32 v71, v71, v188\n"
      "v_sub_f32 v72, v72, v189\n"
      "v_sub_f32 v73, v73, v189\n"
      "v_sub_f32 v74, v74, v189\n"
      "v_sub_f32 v75, v75, v189\n"
      "v_sub_f32 v76, v76, v189\n"
      "v_sub_f32 v77, v77, v189\n"
      "v_sub_f32 v78, v78, v189\n"
      "v_sub_f32 v79, v79, v189\n"
      "v_sub_f32 v80, v80, v190\n"
      "v_sub_f32 v81, v81, v190\n"
      "v_sub_f32 v82, v82, v190\n"
      "v_sub_f32 v83, v83, v190\n"
      "v_sub_f32 v84, v84, v190\n"
      "v_sub_f32 v85, v85, v190\n"
      "v_sub_f32 v86, v86, v190\n"
      "v_sub_f32 v87, v87, v190\n"
      "v_sub_f32 v88, v88, v191\n"
      "v_sub_f32 v89, v89, v191\n"
      "v_sub_f32 v90, v90, v191\n"
      "v_sub_f32 v91, v91, v191\n"
      "v_sub_f32 v92, v92, v191\n"
      "v_sub_f32 v93, v93, v191\n"
      "v_sub_f32 v94, v94, v191\n"
      "v_sub_f32 v95, v95, v191\n"
      "v_sub_f32 v96, v96, v188\n"
      "v_sub_f32 v97, v97, v188\n"
      "v_sub_f32 v98, v98, v188\n"
      "v_sub_f32 v99, v99, v188\n"
      "v_sub_f32 v100, v100, v188\n"
      "v_sub_f32 v101, v101, v188\n"
      "v_sub_f32 v102, v102, v188\n"
      "v_sub_f32 v103, v103, v188\n"
      "v_sub_f32 v104, v104, v189\n"
      "v_sub_f32 v105, v105, v189\n"
      "v_sub_f32 v106, v106, v189\n"
      "v_sub_f32 v107, v107, v189\n"
      "v_sub_f32 v108, v108, v189\n"
      "v_sub_f32 v109, v109, v189\n"
      "v_sub_f32 v110, v110, v189\n"
      "v_sub_f32 v111, v111, v189\n"
      "v_sub_f32 v112, v112, v190\n"
      "v_sub_f32 v113, v113, v190\n"
      "v_sub_f32 v114, v114, v190\n"
      "v_sub_f32 v115, v115, v190\n"
      "v_sub_f32 v116, v116, v190\n"
      "v_sub_f32 v117, v117, v190\n"
      "v_sub_f32 v118, v118, v190\n"
      "v_sub_f32 v119, v119, v190\n"
      "v_sub_f32 v120, v120, v191\n"
      "v_sub_f32 v121, v121, v191\n"
      "v_sub_f32 v122, v122, v191\n"
      "v_sub_f32 v123, v123, v191\n"
      "v_sub_f32 v124, v124, v191\n"
      "v_sub_f32 v125, v125, v191\n"
      "v_sub_f32 v126, v126, v191\n"
      "v_sub_f32 v127, v127, v191\n"
      "v_mul_f32 v0, v0, v192\n"
      "v_mul_f32 v1, v1, v192\n"
      "v_mul_f32 v2, v2, v192\n"
      "v_mul_f32 v3, v3, v192\n"
      "v_mul_f32 v4, v4, v193\n"
      "v_mul_f32 v5, v5, v193\n"
      "v_mul_f32 v6, v6, v193\n"
      "v_mul_f32 v7, v7, v193\n"
      "v_mul_f32 v8, v8, v192\n"
      "v_mul_f32 v9, v9, v192\n"
      "v_mul_f32 v10, v10, v192\n"
      "v_mul_f32 v11, v11, v192\n"
      "v_mul_f32 v12, v12, v193\n"
      "v_mul_f32 v13, v13, v193\n"
      "v_mul_f32 v14, v14, v193\n"
      "v_mul_f32 v15, v15, v193\n"
      "v_mul_f32 v16, v16, v192\n"
      "v_mul_f32 v17, v17, v192\n"
      "v_mul_f32 v18, v18, v192\n"
      "v_mul_f32 v19, v19, v192\n"
      "v_mul_f32 v20, v20, v193\n"
      "v_mul_f32 v21, v21, v193\n"
      "v_mul_f32 v22, v22, v193\n"
      "v_mul_f32 v23, v23, v193\n"
      "v_mul_f32 v24, v24, v192\n"
      "v_mul_f32 v25, v25, v192\n"
      "v_mul_f32 v26, v26, v192\n"
      "v_mul_f32 v27, v27, v192\n"
      "v_mul_f32 v28, v28, v193\n"
      "v_mul_f32 v29, v29, v193\n"
      "v_mul_f32 v30, v30, v193\n"
      "v_mul_f32 v31, v31, v193\n"
      "v_mul_f32 v32, v32, v192\n"
      "v_mul_f32 v33, v33, v192\n"
      "v_mul_f32 v34, v34, v192\n"
      "v_mul_f32 v35, v35, v192\n"
      "v_mul_f32 v36, v36, v193\n"
      "v_mul_f32 v37, v37, v193\n"
      "v_mul_f32 v38, v38, v193\n"
      "v_mul_f32 v39, v39, v193\n"
      "v_mul_f32 v40, v40, v192\n"
      "v_mul_f32 v41, v41, v192\n"
      "v_mul_f32 v42, v42, v192\n"
      "v_mul_f32 v43, v43, v192\n"
      "v_mul_f32 v44, v44, v193\n"
      "v_mul_f32 v45, v45, v193\n"
      "v_mul_f32 v46, v46, v193\n"
      "v_mul_f32 v47, v47, v193\n"
      "v_mul_f32 v48, v48, v192\n"
      "v_mul_f32 v49, v49, v192\n"
      "v_mul_f32 v50, v50, v192\n"
      "v_mul_f32 v51, v51, v192\n"
      "v_mul_f32 v52, v52, v193\n"
      "v_mul_f32 v53, v53, v193\n"
      "v_mul_f32 v54, v54, v193\n"
      "v_mul_f32 v55, v55, v193\n"
      "v_mul_f32 v56, v56, v192\n"
      "v_mul_f32 v57, v57, v192\n"
      "v_mul_f32 v58, v58, v192\n"
      "v_mul_f32 v59, v59, v192\n"
      "v_mul_f32 v60, v60, v193\n"
      "v_mul_f32 v61, v61, v193\n"
      "v_mul_f32 v62, v62, v193\n"
      "v_mul_f32 v63, v63, v193\n"
      "v_accvgpr_read_b32 v196, a144\n"
      "v_accvgpr_read_b32 v197, a145\n"
      "v_accvgpr_read_b32 v198, a146\n"
      "v_accvgpr_read_b32 v199, a147\n"
      "v_mul_f32 v196, v196, v194\n"
      "v_mul_f32 v197, v197, v194\n"
      "v_mul_f32 v198, v198, v194\n"
      "v_mul_f32 v199, v199, v194\n"
      "v_accvgpr_write_b32 a144, v196\n"
      "v_accvgpr_write_b32 a145, v197\n"
      "v_accvgpr_write_b32 a146, v198\n"
      "v_accvgpr_write_b32 a147, v199\n"
      "v_accvgpr_read_b32 v196, a148\n"
      "v_accvgpr_read_b32 v197, a149\n"
      "v_accvgpr_read_b32 v198, a150\n"
      "v_accvgpr_read_b32 v199, a151\n"
      "v_mul_f32 v196, v196, v195\n"
      "v_mul_f32 v197, v197, v195\n"
      "v_mul_f32 v198, v198, v195\n"
      "v_mul_f32 v199, v199, v195\n"
      "v_accvgpr_write_b32 a148, v196\n"
      "v_accvgpr_write_b32 a149, v197\n"
      "v_accvgpr_write_b32 a150, v198\n"
      "v_accvgpr_write_b32 a151, v199\n"
      "v_accvgpr_read_b32 v196, a152\n"
      "v_accvgpr_read_b32 v197, a153\n"
      "v_accvgpr_read_b32 v198, a154\n"
      "v_accvgpr_read_b32 v199, a155\n"
      "v_mul_f32 v196, v196, v194\n"
      "v_mul_f32 v197, v197, v194\n"
      "v_mul_f32 v198, v198, v194\n"
      "v_mul_f32 v199, v199, v194\n"
      "v_accvgpr_write_b32 a152, v196\n"
      "v_accvgpr_write_b32 a153, v197\n"
      "v_accvgpr_write_b32 a154, v198\n"
      "v_accvgpr_write_b32 a155, v199\n"
      "v_accvgpr_read_b32 v196, a156\n"
      "v_accvgpr_read_b32 v197, a157\n"
      "v_accvgpr_read_b32 v198, a158\n"
      "v_accvgpr_read_b32 v199, a159\n"
      "v_mul_f32 v196, v196, v195\n"
      "v_mul_f32 v197, v197, v195\n"
      "v_mul_f32 v198, v198, v195\n"
      "v_mul_f32 v199, v199, v195\n"
      "v_accvgpr_write_b32 a156, v196\n"
      "v_accvgpr_write_b32 a157, v197\n"
      "v_accvgpr_write_b32 a158, v198\n"
      "v_accvgpr_write_b32 a159, v199\n"
      "v_accvgpr_read_b32 v196, a160\n"
      "v_accvgpr_read_b32 v197, a161\n"
      "v_accvgpr_read_b32 v198, a162\n"
      "v_accvgpr_read_b32 v199, a163\n"
      "v_mul_f32 v196, v196, v194\n"
      "v_mul_f32 v197, v197, v194\n"
      "v_mul_f32 v198, v198, v194\n"
      "v_mul_f32 v199, v199, v194\n"
      "v_accvgpr_write_b32 a160, v196\n"
      "v_accvgpr_write_b32 a161, v197\n"
      "v_accvgpr_write_b32 a162, v198\n"
      "v_accvgpr_write_b32 a163, v199\n"
      "v_accvgpr_read_b32 v196, a164\n"
      "v_accvgpr_read_b32 v197, a165\n"
      "v_accvgpr_read_b32 v198, a166\n"
      "v_accvgpr_read_b32 v199, a167\n"
      "v_mul_f32 v196, v196, v195\n"
      "v_mul_f32 v197, v197, v195\n"
      "v_mul_f32 v198, v198, v195\n"
      "v_mul_f32 v199, v199, v195\n"
      "v_accvgpr_write_b32 a164, v196\n"
      "v_accvgpr_write_b32 a165, v197\n"
      "v_accvgpr_write_b32 a166, v198\n"
      "v_accvgpr_write_b32 a167, v199\n"
      "v_accvgpr_read_b32 v196, a168\n"
      "v_accvgpr_read_b32 v197, a169\n"
      "v_accvgpr_read_b32 v198, a170\n"
      "v_accvgpr_read_b32 v199, a171\n"
      "v_mul_f32 v196, v196, v194\n"
      "v_mul_f32 v197, v197, v194\n"
      "v_mul_f32 v198, v198, v194\n"
      "v_mul_f32 v199, v199, v194\n"
      "v_accvgpr_write_b32 a168, v196\n"
      "v_accvgpr_write_b32 a169, v197\n"
      "v_accvgpr_write_b32 a170, v198\n"
      "v_accvgpr_write_b32 a171, v199\n"
      "v_accvgpr_read_b32 v196, a172\n"
      "v_accvgpr_read_b32 v197, a173\n"
      "v_accvgpr_read_b32 v198, a174\n"
      "v_accvgpr_read_b32 v199, a175\n"
      "v_mul_f32 v196, v196, v195\n"
      "v_mul_f32 v197, v197, v195\n"
      "v_mul_f32 v198, v198, v195\n"
      "v_mul_f32 v199, v199, v195\n"
      "v_accvgpr_write_b32 a172, v196\n"
      "v_accvgpr_write_b32 a173, v197\n"
      "v_accvgpr_write_b32 a174, v198\n"
      "v_accvgpr_write_b32 a175, v199\n"
      "v_accvgpr_read_b32 v196, a176\n"
      "v_accvgpr_read_b32 v197, a177\n"
      "v_accvgpr_read_b32 v198, a178\n"
      "v_accvgpr_read_b32 v199, a179\n"
      "v_mul_f32 v196, v196, v194\n"
      "v_mul_f32 v197, v197, v194\n"
      "v_mul_f32 v198, v198, v194\n"
      "v_mul_f32 v199, v199, v194\n"
      "v_accvgpr_write_b32 a176, v196\n"
      "v_accvgpr_write_b32 a177, v197\n"
      "v_accvgpr_write_b32 a178, v198\n"
      "v_accvgpr_write_b32 a179, v199\n"
      "v_accvgpr_read_b32 v196, a180\n"
      "v_accvgpr_read_b32 v197, a181\n"
      "v_accvgpr_read_b32 v198, a182\n"
      "v_accvgpr_read_b32 v199, a183\n"
      "v_mul_f32 v196, v196, v195\n"
      "v_mul_f32 v197, v197, v195\n"
      "v_mul_f32 v198, v198, v195\n"
      "v_mul_f32 v199, v199, v195\n"
      "v_accvgpr_write_b32 a180, v196\n"
      "v_accvgpr_write_b32 a181, v197\n"
      "v_accvgpr_write_b32 a182, v198\n"
      "v_accvgpr_write_b32 a183, v199\n"
      "v_accvgpr_read_b32 v196, a184\n"
      "v_accvgpr_read_b32 v197, a185\n"
      "v_accvgpr_read_b32 v198, a186\n"
      "v_accvgpr_read_b32 v199, a187\n"
      "v_mul_f32 v196, v196, v194\n"
      "v_mul_f32 v197, v197, v194\n"
      "v_mul_f32 v198, v198, v194\n"
      "v_mul_f32 v199, v199, v194\n"
      "v_accvgpr_write_b32 a184, v196\n"
      "v_accvgpr_write_b32 a185, v197\n"
      "v_accvgpr_write_b32 a186, v198\n"
      "v_accvgpr_write_b32 a187, v199\n"
      "v_accvgpr_read_b32 v196, a188\n"
      "v_accvgpr_read_b32 v197, a189\n"
      "v_accvgpr_read_b32 v198, a190\n"
      "v_accvgpr_read_b32 v199, a191\n"
      "v_mul_f32 v196, v196, v195\n"
      "v_mul_f32 v197, v197, v195\n"
      "v_mul_f32 v198, v198, v195\n"
      "v_mul_f32 v199, v199, v195\n"
      "v_accvgpr_write_b32 a188, v196\n"
      "v_accvgpr_write_b32 a189, v197\n"
      "v_accvgpr_write_b32 a190, v198\n"
      "v_accvgpr_write_b32 a191, v199\n"
      "v_accvgpr_read_b32 v196, a192\n"
      "v_accvgpr_read_b32 v197, a193\n"
      "v_accvgpr_read_b32 v198, a194\n"
      "v_accvgpr_read_b32 v199, a195\n"
      "v_mul_f32 v196, v196, v194\n"
      "v_mul_f32 v197, v197, v194\n"
      "v_mul_f32 v198, v198, v194\n"
      "v_mul_f32 v199, v199, v194\n"
      "v_accvgpr_write_b32 a192, v196\n"
      "v_accvgpr_write_b32 a193, v197\n"
      "v_accvgpr_write_b32 a194, v198\n"
      "v_accvgpr_write_b32 a195, v199\n"
      "v_accvgpr_read_b32 v196, a196\n"
      "v_accvgpr_read_b32 v197, a197\n"
      "v_accvgpr_read_b32 v198, a198\n"
      "v_accvgpr_read_b32 v199, a199\n"
      "v_mul_f32 v196, v196, v195\n"
      "v_mul_f32 v197, v197, v195\n"
      "v_mul_f32 v198, v198, v195\n"
      "v_mul_f32 v199, v199, v195\n"
      "v_accvgpr_write_b32 a196, v196\n"
      "v_accvgpr_write_b32 a197, v197\n"
      "v_accvgpr_write_b32 a198, v198\n"
      "v_accvgpr_write_b32 a199, v199\n"
      "v_accvgpr_read_b32 v196, a200\n"
      "v_accvgpr_read_b32 v197, a201\n"
      "v_accvgpr_read_b32 v198, a202\n"
      "v_accvgpr_read_b32 v199, a203\n"
      "v_mul_f32 v196, v196, v194\n"
      "v_mul_f32 v197, v197, v194\n"
      "v_mul_f32 v198, v198, v194\n"
      "v_mul_f32 v199, v199, v194\n"
      "v_accvgpr_write_b32 a200, v196\n"
      "v_accvgpr_write_b32 a201, v197\n"
      "v_accvgpr_write_b32 a202, v198\n"
      "v_accvgpr_write_b32 a203, v199\n"
      "v_accvgpr_read_b32 v196, a204\n"
      "v_accvgpr_read_b32 v197, a205\n"
      "v_accvgpr_read_b32 v198, a206\n"
      "v_accvgpr_read_b32 v199, a207\n"
      "v_mul_f32 v196, v196, v195\n"
      "v_mul_f32 v197, v197, v195\n"
      "v_mul_f32 v198, v198, v195\n"
      "v_mul_f32 v199, v199, v195\n"
      "v_accvgpr_write_b32 a204, v196\n"
      "v_accvgpr_write_b32 a205, v197\n"
      "v_accvgpr_write_b32 a206, v198\n"
      "v_accvgpr_write_b32 a207, v199\n"
      "v_mul_f32 v180, v180, v192\n"
      "v_mul_f32 v181, v181, v193\n"
      "v_mul_f32 v182, v182, v194\n"
      "v_mul_f32 v183, v183, v195\n"
      "v_lshlrev_b32 v196, 16, v128\n"
      "v_and_b32 v197, 0xffff0000, v128\n"
      "v_mul_f32 v196, v196, v192\n"
      "v_mul_f32 v197, v197, v192\n"
      "v_cvt_pk_bf16_f32 v128, v196, v197\n"
      "v_lshlrev_b32 v196, 16, v129\n"
      "v_and_b32 v197, 0xffff0000, v129\n"
      "v_mul_f32 v196, v196, v192\n"
      "v_mul_f32 v197, v197, v192\n"
      "v_cvt_pk_bf16_f32 v129, v196, v197\n"
      "v_lshlrev_b32 v196, 16, v130\n"
      "v_and_b32 v197, 0xffff0000, v130\n"
      "v_mul_f32 v196, v196, v192\n"
      "v_mul_f32 v197, v197, v192\n"
      "v_cvt_pk_bf16_f32 v130, v196, v197\n"
      "v_lshlrev_b32 v196, 16, v131\n"
      "v_and_b32 v197, 0xffff0000, v131\n"
      "v_mul_f32 v196, v196, v192\n"
      "v_mul_f32 v197, v197, v192\n"
      "v_cvt_pk_bf16_f32 v131, v196, v197\n"
      "v_lshlrev_b32 v196, 16, v132\n"
      "v_and_b32 v197, 0xffff0000, v132\n"
      "v_mul_f32 v196, v196, v193\n"
      "v_mul_f32 v197, v197, v193\n"
      "v_cvt_pk_bf16_f32 v132, v196, v197\n"
      "v_lshlrev_b32 v196, 16, v133\n"
      "v_and_b32 v197, 0xffff0000, v133\n"
      "v_mul_f32 v196, v196, v193\n"
      "v_mul_f32 v197, v197, v193\n"
      "v_cvt_pk_bf16_f32 v133, v196, v197\n"
      "v_lshlrev_b32 v196, 16, v134\n"
      "v_and_b32 v197, 0xffff0000, v134\n"
      "v_mul_f32 v196, v196, v193\n"
      "v_mul_f32 v197, v197, v193\n"
      "v_cvt_pk_bf16_f32 v134, v196, v197\n"
      "v_lshlrev_b32 v196, 16, v135\n"
      "v_and_b32 v197, 0xffff0000, v135\n"
      "v_mul_f32 v196, v196, v193\n"
      "v_mul_f32 v197, v197, v193\n"
      "v_cvt_pk_bf16_f32 v135, v196, v197\n"
      "v_lshlrev_b32 v196, 16, v136\n"
      "v_and_b32 v197, 0xffff0000, v136\n"
      "v_mul_f32 v196, v196, v194\n"
      "v_mul_f32 v197, v197, v194\n"
      "v_cvt_pk_bf16_f32 v136, v196, v197\n"
      "v_lshlrev_b32 v196, 16, v137\n"
      "v_and_b32 v197, 0xffff0000, v137\n"
      "v_mul_f32 v196, v196, v194\n"
      "v_mul_f32 v197, v197, v194\n"
      "v_cvt_pk_bf16_f32 v137, v196, v197\n"
      "v_lshlrev_b32 v196, 16, v138\n"
      "v_and_b32 v197, 0xffff0000, v138\n"
      "v_mul_f32 v196, v196, v194\n"
      "v_mul_f32 v197, v197, v194\n"
      "v_cvt_pk_bf16_f32 v138, v196, v197\n"
      "v_lshlrev_b32 v196, 16, v139\n"
      "v_and_b32 v197, 0xffff0000, v139\n"
      "v_mul_f32 v196, v196, v194\n"
      "v_mul_f32 v197, v197, v194\n"
      "v_cvt_pk_bf16_f32 v139, v196, v197\n"
      "v_lshlrev_b32 v196, 16, v140\n"
      "v_and_b32 v197, 0xffff0000, v140\n"
      "v_mul_f32 v196, v196, v195\n"
      "v_mul_f32 v197, v197, v195\n"
      "v_cvt_pk_bf16_f32 v140, v196, v197\n"
      "v_lshlrev_b32 v196, 16, v141\n"
      "v_and_b32 v197, 0xffff0000, v141\n"
      "v_mul_f32 v196, v196, v195\n"
      "v_mul_f32 v197, v197, v195\n"
      "v_cvt_pk_bf16_f32 v141, v196, v197\n"
      "v_lshlrev_b32 v196, 16, v142\n"
      "v_and_b32 v197, 0xffff0000, v142\n"
      "v_mul_f32 v196, v196, v195\n"
      "v_mul_f32 v197, v197, v195\n"
      "v_cvt_pk_bf16_f32 v142, v196, v197\n"
      "v_lshlrev_b32 v196, 16, v143\n"
      "v_and_b32 v197, 0xffff0000, v143\n"
      "v_mul_f32 v196, v196, v195\n"
      "v_mul_f32 v197, v197, v195\n"
      "v_cvt_pk_bf16_f32 v143, v196, v197\n"
      "v_exp_f32 v188, v96\n"
      "v_exp_f32 v189, v97\n"
      "v_exp_f32 v190, v98\n"
      "v_exp_f32 v191, v99\n"
      "v_cvt_pk_bf16_f32 v144, v188, v189\n"
      "v_cvt_pk_bf16_f32 v145, v190, v191\n"
      "v_exp_f32 v192, v100\n"
      "v_exp_f32 v193, v101\n"
      "v_exp_f32 v194, v102\n"
      "v_exp_f32 v195, v103\n"
      "v_cvt_pk_bf16_f32 v146, v192, v193\n"
      "v_cvt_pk_bf16_f32 v147, v194, v195\n"
      "v_exp_f32 v188, v104\n"
      "v_exp_f32 v189, v105\n"
      "v_exp_f32 v190, v106\n"
      "v_exp_f32 v191, v107\n"
      "v_cvt_pk_bf16_f32 v148, v188, v189\n"
      "v_cvt_pk_bf16_f32 v149, v190, v191\n"
      "v_exp_f32 v192, v108\n"
      "v_exp_f32 v193, v109\n"
      "v_exp_f32 v194, v110\n"
      "v_exp_f32 v195, v111\n"
      "v_cvt_pk_bf16_f32 v150, v192, v193\n"
      "v_cvt_pk_bf16_f32 v151, v194, v195\n"
      "v_exp_f32 v188, v112\n"
      "v_exp_f32 v189, v113\n"
      "v_exp_f32 v190, v114\n"
      "v_exp_f32 v191, v115\n"
      "v_cvt_pk_bf16_f32 v152, v188, v189\n"
      "v_cvt_pk_bf16_f32 v153, v190, v191\n"
      "v_exp_f32 v192, v116\n"
      "v_exp_f32 v193, v117\n"
      "v_exp_f32 v194, v118\n"
      "v_exp_f32 v195, v119\n"
      "v_cvt_pk_bf16_f32 v154, v192, v193\n"
      "v_cvt_pk_bf16_f32 v155, v194, v195\n"
      "v_exp_f32 v188, v120\n"
      "v_exp_f32 v189, v121\n"
      "v_exp_f32 v190, v122\n"
      "v_exp_f32 v191, v123\n"
      "v_cvt_pk_bf16_f32 v156, v188, v189\n"
      "v_cvt_pk_bf16_f32 v157, v190, v191\n"
      "v_exp_f32 v192, v124\n"
      "v_exp_f32 v193, v125\n"
      "v_exp_f32 v194, v126\n"
      "v_exp_f32 v195, v127\n"
      "v_cvt_pk_bf16_f32 v158, v192, v193\n"
      "v_cvt_pk_bf16_f32 v159, v194, v195\n"
      "s_nop 1\n"
      "v_mfma_f32_16x16x32_bf16 v[184:187], a[128:131], v[144:147], 0\n"
      "v_mfma_f32_16x16x32_bf16 v[184:187], a[132:135], v[148:151], v[184:187]\n"
      "v_mfma_f32_16x16x32_bf16 v[184:187], a[136:139], v[152:155], v[184:187]\n"
      "v_mfma_f32_16x16x32_bf16 v[184:187], a[140:143], v[156:159], v[184:187]\n"
      "s_nop 7\n"
      "s_setpc_b64 s[96:97]\n"
      "SLOW_%=:\n"
      "ds_write_b128 v234, v[176:179] offset:5120\n"
      "s_waitcnt lgkmcnt(0)\n"
      "s_barrier\n"
      "ds_read_b128 v[192:195], v235 offset:5120\n"
      "s_waitcnt lgkmcnt(0)\n"
      "v_sub_f32 v188, v176, v194\n"
      "v_sub_f32 v189, v177, v195\n"
      "v_sub_f32 v190, v178, v192\n"
      "v_sub_f32 v191, v179, v193\n"
      "v_max_f32 v188, 0, v188\n"
      "v_max_f32 v189, 0, v189\n"
      "v_max_f32 v190, 0, v190\n"
      "v_max_f32 v191, 0, v191\n"
      "s_call_b64 s[96:97], RS1_%=\n"
      "ds_write_b128 v232, a[176:179] offset:0\n"
      "ds_write_b128 v232, a[180:183] offset:1024\n"
      "ds_write_b128 v232, a[184:187] offset:2048\n"
      "ds_write_b128 v232, a[188:191] offset:3072\n"
      "ds_write_b128 v232, a[192:195] offset:4096\n"
      "ds_write_b128 v232, a[196:199] offset:5120\n"
      "ds_write_b128 v232, a[200:203] offset:6144\n"
      "ds_write_b128 v232, a[204:207] offset:7168\n"
      "ds_write_b128 v234, v[152:155] offset:0\n"
      "ds_write_b128 v234, v[156:159] offset:1024\n"
      "v_cndmask_b32 v229, v180, v181, s[74:75]\n"
      "v_cndmask_b32 v200, v182, v183, s[74:75]\n"
      "v_cndmask_b32 v228, v176, v177, s[74:75]\n"
      "v_cndmask_b32 v202, v186, v187, s[74:75]\n"
      "v_cndmask_b32 v201, v184, v185, s[74:75]\n"
      "v_add_f32 v200, v200, v202\n"
      "ds_write_b32 v234, v200 offset:4096\n"
      "v_add_f32 v229, v229, v201\n"
      "s_waitcnt lgkmcnt(0)\n"
      "s_barrier\n"
      "ds_read_b32 v208, v209\n"
      "ds_read_b128 v[160:163], v235 offset:0\n"
      "ds_read_b128 v[164:167], v235 offset:1024\n"
      "ds_read_b128 v[64:67], v233 offset:0\n"
      "ds_read_b128 v[68:71], v233 offset:1024\n"
      "ds_read_b128 v[72:75], v233 offset:2048\n"
      "ds_read_b128 v[76:79], v233 offset:3072\n"
      "ds_read_b128 v[80:83], v233 offset:4096\n"
      "ds_read_b128 v[84:87], v233 offset:5120\n"
      "ds_read_b128 v[88:91], v233 offset:6144\n"
      "ds_read_b128 v[92:95], v233 offset:7168\n"
      "ds_read_b32 v204, v235 offset:4096\n"
      "s_branch AGAIN_%=\n"
      ".subsection 0\n"
      : "+{v0}"(tid)
      : [blk] "s"(blockIdx.x), [kp] "s"(__builtin_amdgcn_kernarg_segment_ptr()), "s"(lds0)
      : "v1", "v2", "v3", "v4", "v5", "v6", "v7", "v8", "v9", "v10", "v11", "v12", "v13", "v14", "v15", "v16", "v17", "v18", "v19", "v20", "v21", "v22", "v23", "v24", "v25", "v26", "v27", "v28", "v29", "v30", "v31", "v32", "v33", "v34", "v35", "v36", "v37", "v38", "v39", "v40", "v41", "v42", "v43", "v44", "v45", "v46", "v47", "v48", "v49", "v50", "v51", "v52", "v53", "v54", "v55", "v56", "v57", "v58", "v59", "v60", "v61", "v62", "v63", "v64", "v65", "v66", "v67", "v68", "v69", "v70", "v71", "v72", "v73", "v74", "v75", "v76", "v77", "v78", "v79", "v80", "v81", "v82", "v83", "v84", "v85", "v86", "v87", "v88", "v89", "v90", "v91", "v92", "v93", "v94", "v95", "v96", "v97", "v98", "v99", "v100", "v101", "v102", "v103", "v104", "v105", "v106", "v107", "v108", "v109", "v110", "v111", "v112", "v113", "v114", "v115", "v116", "v117", "v118", "v119", "v120", "v121", "v122", "v123", "v124", "v125", "v126", "v127", "v128", "v129", "v130", "v131", "v132", "v133", "v134", "v135", "v136", "v137", "v138", "v139", "v140", "v141", "v142", "v143", "v144", "v145", "v146", "v147", "v148", "v149", "v150", "v151", "v152", "v153", "v154", "v155", "v156", "v157", "v158", "v159", "v160", "v161", "v162", "v163", "v164", "v165", "v166", "v167", "v168", "v169", "v170", "v171", "v172", "v173", "v174", "v175", "v176", "v177", "v178", "v179", "v180", "v181", "v182", "v183", "v184", "v185", "v186", "v187", "v188", "v189", "v190", "v191", "v192", "v193", "v194", "v195", "v196", "v197", "v198", "v199", "v200", "v201", "v202", "v203", "v204", "v205", "v206", "v207", "v208", "v209", "v210", "v211", "v212", "v213", "v214", "v215", "v216", "v217", "v218", "v219", "v220", "v221", "v222", "v223", "v224", "v225", "v226", "v227", "v228", "v229", "v230", "v231", "v232", "v233", "v234", "v235", "v236", "v237", "v238", "v239", "v240", "v241", "v242", "v243", "v244", "v245", "v246", "v247", "v248", "v249", "v250", "v251", "a0", "a1", "a2", "a3", "a4", "a5", "a6", "a7", "a8", "a9", "a10", "a11", "a12", "a13", "a14", "a15", "a16", "a17", "a18", "a19", "a20", "a21", "a22", "a23", "a24", "a25", "a26", "a27", "a28", "a29", "a30", "a31", "a32", "a33", "a34", "a35", "a36", "a37", "a38", "a39", "a40", "a41", "a42", "a43", "a44", "a45", "a46", "a47", "a48", "a49", "a50", "a51", "a52", "a53", "a54", "a55", "a56", "a57", "a58", "a59", "a60", "a61", "a62", "a63", "a64", "a65", "a66", "a67", "a68", "a69", "a70", "a71", "a72", "a73", "a74", "a75", "a76", "a77", "a78", "a79", "a80", "a81", "a82", "a83", "a84", "a85", "a86", "a87", "a88", "a89", "a90", "a91", "a92", "a93", "a94", "a95", "a96", "a97", "a98", "a99", "a100", "a101", "a102", "a103", "a104", "a105", "a106", "a107", "a108", "a109", "a110", "a111", "a112", "a113", "a114", "a115", "a116", "a117", "a118", "a119", "a120", "a121", "a122", "a123", "a124", "a125", "a126", "a127", "a128", "a129", "a130", "a131", "a132", "a133", "a134", "a135", "a136", "a137", "a138", "a139", "a140", "a141", "a142", "a143", "a144", "a145", "a146", "a147", "a148", "a149", "a150", "a151", "a152", "a153", "a154", "a155", "a156", "a157", "a158", "a159", "a160", "a161", "a162", "a163", "a164", "a165", "a166", "a167", "a168", "a169", "a170", "a171", "a172", "a173", "a174", "a175", "a176", "a177", "a178", "a179", "a180", "a181", "a182", "a183", "a184", "a185", "a186", "a187", "a188", "a189", "a190", "a191", "a192", "a193", "a194", "a195", "a196", "a197", "a198", "a199", "a200", "a201", "a202", "a203", "a204", "a205", "a206", "a207", "s4", "s5", "s6", "s7", "s8", "s9", "s10", "s11", "s12", "s13", "s14", "s15", "s16", "s17", "s18", "s19", "s20", "s21", "s22", "s23", "s24", "s25", "s26", "s27", "s28", "s29", "s30", "s31", "s32", "s33", "s34", "s35", "s38", "s39", "s40", "s43", "s44", "s45", "s46", "s47", "s50", "s52", "s53", "s54", "s55", "s56", "s57", "s58", "s59", "s60", "s61", "s62", "s63", "s64", "s65", "s66", "s67", "s68", "s69", "s70", "s71", "s72", "s74", "s75", "s76", "s77", "s78", "s79", "s80", "s81", "s82", "s83", "s84", "s85", "s86", "s87", "s88", "s89", "s90", "s91", "s92", "s93", "s94", "s95", "s96", "s97", "vcc", "scc", "memory");
}

}

extern "C" void kernel_launch(void* const* d_in, const int* in_sizes, int n_in, void* d_out, int out_size,
                              void* d_ws, size_t ws_size, hipStream_t stream) {
  if (n_in != 6 || ws_size < WS_END) return;
  const __hip_bfloat16* q = (const __hip_bfloat16*)d_in[0];
  const __hip_bfloat16* kc = (const __hip_bfloat16*)d_in[1];
  const __hip_bfloat16* vc = (const __hip_bfloat16*)d_in[2];
  const __hip_bfloat16* kt = (const __hip_bfloat16*)d_in[3];
  const __hip_bfloat16* vt = (const __hip_bfloat16*)d_in[4];
  const __hip_bfloat16* tm = (const __hip_bfloat16*)d_in[5];
  hipLaunchKernelGGL(tree_attn_main, dim3(B_ * HKV_ * 2), dim3(256), 0, stream, q, kc, vc, kt, vt, tm,
                     (__hip_bfloat16*)d_out, (char*)d_ws);
}
```

```cpp
#include <hip/hip_runtime.h>
#include <hip/hip_bf16.h>
#include <stdint.h>

#pragma clang diagnostic ignored "-Wint-to-pointer-cast"
#pragma clang diagnostic ignored "-Winline-asm"

namespace {

constexpr int B_ = 16, HKV_ = 8, D_ = 128;
constexpr int SLOT = 64 * D_ * 2;
constexpr int LDS_BYTES = 10 * SLOT;

constexpr size_t WS_EPOCH = 0;
constexpr size_t WS_REF = WS_EPOCH + 128 * 4 * 128;
constexpr size_t WS_X = WS_REF + 16384;
constexpr size_t XW_BYTES = 4608;
constexpr size_t WS_END = WS_X + (size_t)128 * 2 * 4 * XW_BYTES;

__global__ __launch_bounds__(256, 1)
void tree_attn_main(const __hip_bfloat16*, const __hip_bfloat16*, const __hip_bfloat16*, const __hip_bfloat16*,
                    const __hip_bfloat16*, const __hip_bfloat16*, __hip_bfloat16*, char*) {
  __shared__ __attribute__((aligned(1024))) char smem[LDS_BYTES];
  const unsigned lds0 = (unsigned)(uintptr_t)smem;
  unsigned tid = threadIdx.x;
  asm volatile(
      "s_load_dwordx16 s[4:19], %[kp], 0x0\n"
      "s_getpc_b64 s[60:61]\n"
      "v_lshlrev_b32 v241, 4, v0\n"
      "s_lshl_b32 s62, %[blk], 9\n"
      "s_and_b32 s62, s62, 0x3000\n"
      "s_add_u32 s60, s60, s62\n"
      "s_addc_u32 s61, s61, 0\n"
      "global_load_dwordx4 v[236:239], v241, s[60:61]\n"
      "v_and_b32 v1, 0xc0, v0\n"
      "v_lshlrev_b32 v248, 1, v0\n"
      "v_readfirstlane_b32 s50, v1\n"
      "v_and_b32 v248, 0x60, v248\n"
      "v_and_b32 v249, 0x70, v241\n"
      "v_xor_b32 v248, v248, v249\n"
      "v_lshl_or_b32 v248, v1, 6, v248\n"
      "v_and_b32 v1, 56, v0\n"
      "v_lshl_or_b32 v248, v1, 5, v248\n"
      "s_mov_b32 s64, 0x14100400\n"
      "s_mov_b32 s65, 0x54504440\n"
      "s_lshl_b32 s66, %[blk], 3\n"
      "s_lshr_b64 s[64:65], s[64:65], s66\n"
      "s_mov_b32 s66, 0x3020100\n"
      "s_mov_b32 s67, 0x83828180\n"
      "s_and_b32 s65, %[blk], 56\n"
      "s_lshr_b64 s[66:67], s[66:67], s65\n"
      "s_or_b32 s64, s64, s66\n"
      "s_lshr_b32 s65, %[blk], 3\n"
      "s_and_b32 s65, s65, 24\n"
      "s_lshr_b32 s65, 0x28082000, s65\n"
      "s_or_b32 s64, s64, s65\n"
      "s_and_b32 s63, s64, 0x80\n"
      "s_and_b32 s44, s64, 127\n"
      "v_or_b32 v248, s63, v248\n"
      "v_xor_b32 v249, 0x80, v248\n"
      "s_lshl_b32 s64, s44, 20\n"
      "s_lshl_b32 s62, s44, 15\n"
      "s_lshl_b32 s50, s50, 6\n"
      "s_add_u32 m0, s50, 131072\n"
      "s_mov_b32 s58, 0x120000\n"
      "s_mov_b32 s59, 0x20000\n"
      "s_movk_i32 s22, 0x4000\n"
      "s_waitcnt lgkmcnt(0)\n"
      "s_add_u32 s56, s4, s62\n"
      "s_addc_u32 s57, s5, 0\n"
      "buffer_load_dwordx4 v248, s[56:59], 0 offen nt lds\n"
      "buffer_load_dwordx4 v248, s[56:59], 0 offen offset:2048 nt lds\n"
      "s_bitset1_b32 m0, 10\n"
      "s_memrealtime s[82:83]\n"
      "s_add_u32 s20, s6, s64\n"
      "s_addc_u32 s21, s7, 0\n"
      "s_add_u32 s32, s8, s64\n"
      "s_addc_u32 s33, s9, 0\n"
      "s_load_dword s92, s[20:21], 0x0\n"
      "s_load_dword s92, s[10:11], 0x0\n"
      "s_load_dword s92, s[32:33], 0x0\n"
      "buffer_load_dwordx4 v249, s[56:59], 0 offen nt lds\n"
      "buffer_load_dwordx4 v249, s[56:59], 0 offen offset:2048 nt lds\n"
      "s_add_u32 m0, s50, 147456\n"
      "s_load_dword s92, s[18:19], 0x0\n"
      "buffer_load_dwordx4 v248, s[56:59], s22 offen nt lds\n"
      "buffer_load_dwordx4 v248, s[56:59], s22 offen offset:2048 nt lds\n"
      "s_bitset1_b32 m0, 10\n"
      "s_nop 0\n"
      "buffer_load_dwordx4 v249, s[56:59], s22 offen nt lds\n"
      "buffer_load_dwordx4 v249, s[56:59], s22 offen offset:2048 nt lds\n"
      "s_mov_b64 s[56:57], s[20:21]\n"
      "s_load_dword s92, s[14:15], 0x0\n"
      "s_load_dword s92, s[12:13], 0x0\n"
      "s_lshr_b32 s74, s50, 2\n"
      "s_lshr_b32 s75, s50, 5\n"
      "s_xor_b32 s75, s75, s63\n"
      "s_and_b32 s75, s75, 0x80\n"
      "s_and_b32 s76, s74, 0x800\n"
      "s_add_u32 s75, s75, s76\n"
      "s_add_u32 s70, s63, s50\n"
      "s_sub_u32 s70, s70, s75\n"
      "s_lshr_b32 s75, s50, 1\n"
      "s_lshr_b32 s71, s62, 2\n"
      "s_sub_u32 s71, s71, s75\n"
      "s_ashr_i32 s72, s71, 31\n"
      "s_mov_b32 s23, s59\n"
      "s_mov_b64 s[26:27], s[22:23]\n"
      "s_sub_u32 s20, s56, s70\n"
      "s_subb_u32 s21, s57, 0\n"
      "s_add_u32 m0, s74, 24576\n"
      "s_add_u32 s24, s10, s71\n"
      "buffer_load_dwordx4 v248, s[20:23], 0 offen nt lds\n"
      "s_addc_u32 s25, s11, s72\n"
      "s_add_u32 m0, s75, 16384\n"
      "s_nop 0\n"
      "buffer_load_dwordx4 v248, s[24:27], 0 offen nt lds\n"
      "s_bitset1_b32 m0, 10\n"
      "s_nop 0\n"
      "buffer_load_dwordx4 v249, s[24:27], 0 offen nt lds\n"
      "s_lshr_b32 s43, s63, 7\n"
      "s_lshl_b32 s76, s44, 9\n"
      "s_lshr_b32 s69, s50, 5\n"
      "s_add_u32 s76, s76, s69\n"
      "s_add_u32 s46, s18, s76\n"
      "s_addc_u32 s47, s19, 0\n"
      "s_mul_i32 s76, s43, 0x3000\n"
      "s_lshl_b32 s69, s44, 8\n"
      "s_and_b32 s69, s69, 0x7800\n"
      "s_add_u32 s14, s14, s69\n"
      "s_addc_u32 s15, s15, 0\n"
      "v_mov_b64 v[192:193], 0\n"
      "v_mov_b64 v[194:195], 0\n"
      "v_lshlrev_b32 v3, 6, v0\n"
      "v_and_b32 v3, 0x3c0, v3\n"
      "v_lshrrev_b32 v4, 1, v0\n"
      "v_and_or_b32 v211, v4, 24, v3\n"
      "v_mov_b64 v[200:201], 0\n"
      "v_mov_b64 v[202:203], 0\n"
      "v_mov_b64 v[204:205], 0\n"
      "v_mov_b64 v[206:207], 0\n"
      "s_cmp_lg_u32 s76, s50\n"
      "s_cbranch_scc1 NOMASK_%=\n"
      "global_load_dwordx2 v[200:201], v211, s[14:15] offset:0\n"
      "global_load_dwordx2 v[202:203], v211, s[14:15] offset:32\n"
      "global_load_dwordx2 v[204:205], v211, s[14:15] offset:1024\n"
      "global_load_dwordx2 v[206:207], v211, s[14:15] offset:1056\n"
      "NOMASK_%=:\n"
      "global_load_dword v208, v192, s[46:47] sc1\n"
      "s_mul_i32 s69, s43, 0x8000\n"
      "s_bitcmp1_b32 s44, 0\n"
      "s_cselect_b32 s66, 0x8000, 0\n"
      "s_xor_b32 s69, s69, s66\n"
      "v_and_b32 v228, 0xff, v248\n"
      "v_xor_b32 v228, s69, v228\n"
      "v_bfe_i32 v2, v0, 3, 1\n"
      "v_and_b32 v2, 0x1000, v2\n"
      "v_xor_b32 v228, v228, v2\n"
      "v_bfe_i32 v2, v0, 4, 1\n"
      "v_and_b32 v2, 0x20400, v2\n"
      "v_xor_b32 v228, v228, v2\n"
      "v_bfe_i32 v2, v0, 5, 1\n"
      "v_and_b32 v2, 0x100, v2\n"
      "v_xor_b32 v228, v228, v2\n"
      "v_bfe_i32 v2, v0, 6, 1\n"
      "v_and_b32 v2, 0x400, v2\n"
      "v_xor_b32 v228, v228, v2\n"
      "v_bfe_i32 v2, v0, 7, 1\n"
      "v_and_b32 v2, 0x2000, v2\n"
      "v_xor_b32 v228, v228, v2\n"
      "v_xor_b32 v231, 0x280, v228\n"
      "v_xor_b32 v230, 0x200, v228\n"
      "v_xor_b32 v229, 0x80, v228\n"
      "v_add_u32 v228, 0xc00, v228\n"
      "v_add_u32 v229, 0x800, v229\n"
      "v_add_u32 v230, 0x400, v230\n"
      "s_sub_u32 s56, s56, 0xc00\n"
      "s_subb_u32 s57, s57, 0\n"
      "s_sub_u32 s32, s32, 0xc00\n"
      "s_subb_u32 s33, s33, 0\n"
      "s_xor_b64 s[32:33], s[32:33], s[56:57]\n"
      "s_mov_b32 s69, 0\n"
      "s_bitcmp1_b32 s44, 4\n"
      "s_cselect_b32 s66, 0x10000, 0\n"
      "s_xor_b32 s69, s69, s66\n"
      "s_bitcmp1_b32 s44, 0\n"
      "s_cselect_b32 s66, 0x800, 0\n"
      "s_xor_b32 s69, s69, s66\n"
      "s_bitcmp1_b32 s44, 2\n"
      "s_cselect_b32 s66, 0x40800, 0\n"
      "s_xor_b32 s69, s69, s66\n"
      "s_bitcmp1_b32 s44, 4\n"
      "s_cselect_b32 s66, 0x4000, 0\n"
      "s_xor_b32 s69, s69, s66\n"
      "s_mov_b32 s60, s69\n"
      "s_mov_b32 s62, s50\n"
      "s_mov_b64 s[30:31], s[22:23]\n"
      "s_add_u32 s28, s12, s71\n"
      "s_addc_u32 s29, s13, s72\n"
      "s_lshl_b32 s69, s43, 13\n"
      "s_xor_b32 s69, s69, 0x2000\n"
      "v_add_u32 v2, s69, v241\n"
      "ds_write_b128 v241, v[192:195] offset:28672\n"
      "ds_write_b128 v2, v[192:195] offset:49152\n"
      "ds_write_b128 v2, v[192:195] offset:53248\n"
      "v_and_b32 v241, 0x3f0, v241\n"
      "s_and_b32 s91, s50, 8192\n"
      "v_lshlrev_b32 v4, 8, v0\n"
      "v_and_b32 v4, 0x800, v4\n"
      "v_lshlrev_b32 v5, 7, v0\n"
      "v_and_b32 v6, 0x380, v5\n"
      "v_or3_b32 v4, v4, v6, s91\n"
      "v_and_b32 v6, 0x60, v241\n"
      "v_and_b32 v2, 48, v0\n"
      "v_xor_b32 v6, v6, v2\n"
      "v_or_b32 v242, v4, v6\n"
      "v_xor_b32 v243, 64, v242\n"
      "v_lshlrev_b32 v4, 6, v0\n"
      "v_and_b32 v6, 0x800, v4\n"
      "v_lshlrev_b32 v2, 5, v0\n"
      "v_and_b32 v2, 0x380, v2\n"
      "s_add_u32 s69, s91, 16384\n"
      "v_or3_b32 v6, v6, v2, s69\n"
      "v_lshlrev_b32 v2, 3, v0\n"
      "v_and_or_b32 v6, v2, 24, v6\n"
      "v_lshlrev_b32 v2, 2, v0\n"
      "v_and_b32 v2, 0x60, v2\n"
      "v_xor_b32 v244, v6, v2\n"
      "v_xor_b32 v245, 0x20, v244\n"
      "v_xor_b32 v246, 0x40, v244\n"
      "v_xor_b32 v247, 0x60, v244\n"
      "v_and_b32 v5, 0x1e00, v5\n"
      "v_and_or_b32 v250, v241, 48, v5\n"
      "s_lshl_b32 s69, %[blk], 9\n"
      "s_and_b32 s69, s69, 0x1e00\n"
      "v_and_b32 v4, 0x1c0, v4\n"
      "v_or_b32 v251, s69, v4\n"
      "s_and_b32 s69, s50, 4096\n"
      "s_lshl_b32 s69, s69, 2\n"
      "s_add_u32 s69, s69, 131072\n"
      "s_sub_u32 s91, 16384, s91\n"
      "v_add_u32 v196, s69, v242\n"
      "v_add_u32 v198, s91, v242\n"
      "v_xor_b32 v214, 0x2000, v196\n"
      "v_add_u32 v197, s69, v243\n"
      "v_add_u32 v199, s91, v243\n"
      "v_xor_b32 v215, 0x2000, v197\n"
      "v_and_b32 v3, 1, v0\n"
      "v_bfe_u32 v4, v0, 1, 5\n"
      "v_lshlrev_b32 v3, 1, v3\n"
      "v_sub_u32 v4, v4, v3\n"
      "v_mov_b32 v219, s60\n"
      "v_bfe_i32 v2, v4, 0, 1\n"
      "v_and_b32 v2, 0x10000, v2\n"
      "v_xor_b32 v219, v219, v2\n"
      "v_bfe_i32 v2, v4, 1, 1\n"
      "v_and_b32 v2, 0x800, v2\n"
      "v_xor_b32 v219, v219, v2\n"
      "v_bfe_i32 v2, v4, 2, 1\n"
      "v_and_b32 v2, 0x40800, v2\n"
      "v_xor_b32 v219, v219, v2\n"
      "v_bfe_i32 v2, v4, 3, 1\n"
      "v_and_b32 v2, 0x80000, v2\n"
      "v_xor_b32 v219, v219, v2\n"
      "v_bfe_i32 v2, v4, 4, 1\n"
      "v_and_b32 v2, 0x4000, v2\n"
      "v_xor_b32 v219, v219, v2\n"
      "v_writelane_b32 v219, 0, 1\n"
      "s_mov_b32 s61, 0\n"
      "s_add_u32 s69, s50, 131072\n"
      "v_add_u32 v210, s69, v241\n"
      "v_and_b32 v2, 3, v0\n"
      "v_mov_b32 v4, 0x3f803f80\n"
      "v_cmp_eq_u32 vcc, 0, v2\n"
      "s_nop 1\n"
      "v_cndmask_b32 v5, 0, v4, vcc\n"
      "v_accvgpr_write_b32 a128, v5\n"
      "v_accvgpr_write_b32 a129, v5\n"
      "v_accvgpr_write_b32 a130, v5\n"
      "v_accvgpr_write_b32 a131, v5\n"
      "v_cmp_eq_u32 vcc, 1, v2\n"
      "s_nop 1\n"
      "v_cndmask_b32 v5, 0, v4, vcc\n"
      "v_accvgpr_write_b32 a132, v5\n"
      "v_accvgpr_write_b32 a133, v5\n"
      "v_accvgpr_write_b32 a134, v5\n"
      "v_accvgpr_write_b32 a135, v5\n"
      "v_cmp_eq_u32 vcc, 2, v2\n"
      "s_nop 1\n"
      "v_cndmask_b32 v5, 0, v4, vcc\n"
      "v_accvgpr_write_b32 a136, v5\n"
      "v_accvgpr_write_b32 a137, v5\n"
      "v_accvgpr_write_b32 a138, v5\n"
      "v_accvgpr_write_b32 a139, v5\n"
      "v_cmp_eq_u32 vcc, 3, v2\n"
      "s_nop 1\n"
      "v_cndmask_b32 v5, 0, v4, vcc\n"
      "v_accvgpr_write_b32 a140, v5\n"
      "v_accvgpr_write_b32 a141, v5\n"
      "v_accvgpr_write_b32 a142, v5\n"
      "v_accvgpr_write_b32 a143, v5\n"
      "v_mfma_f32_32x32x16_bf16 a[144:159], v[192:195], v[192:195], 0\n"
      "v_mfma_f32_32x32x16_bf16 a[160:175], v[192:195], v[192:195], 0\n"
      "v_mfma_f32_32x32x16_bf16 a[176:191], v[192:195], v[192:195], 0\n"
      "v_mfma_f32_32x32x16_bf16 a[192:207], v[192:195], v[192:195], 0\n"
      "v_mfma_f32_32x32x16_bf16 v[0:15], v[192:195], v[192:195], 0\n"
      "v_mfma_f32_32x32x16_bf16 v[16:31], v[192:195], v[192:195], 0\n"
      "v_mfma_f32_32x32x16_bf16 v[32:47], v[192:195], v[192:195], 0\n"
      "v_mfma_f32_32x32x16_bf16 v[48:63], v[192:195], v[192:195], 0\n"
      "v_mfma_f32_32x32x16_bf16 v[128:143], v[192:195], v[192:195], 0\n"
      "v_mov_b64 v[180:181], 0\n"
      "v_mov_b64 v[182:183], 0\n"
      "v_mov_b64 v[184:185], 0\n"
      "v_mov_b64 v[186:187], 0\n"
      "s_mov_b32 s94, 0x3e0293ee\n"
      "s_mov_b32 s95, s94\n"
      "s_lshl_b32 s69, s43, 13\n"
      "s_add_u32 s45, s69, s75\n"
      "s_mov_b32 m0, s62\n"
      "s_nop 0\n"
      "buffer_load_dwordx4 v228, s[56:59], s60 offen nt lds\n"
      "buffer_load_dwordx4 v229, s[56:59], s60 offen offset:1024 nt lds\n"
      "buffer_load_dwordx4 v230, s[56:59], s60 offen offset:2048 nt lds\n"
      "buffer_load_dwordx4 v231, s[56:59], s60 offen offset:3072 nt lds\n"
      "s_xor_b64 s[56:57], s[56:57], s[32:33]\n"
      "s_add_u32 s61, s61, 1\n"
      "s_add_u32 s62, s62, 0x4000\n"
      "s_and_b32 s62, s62, 0x1ffff\n"
      "v_readlane_b32 s60, v219, s61\n"
      "s_xor_b64 s[56:57], s[56:57], s[32:33]\n"
      "s_add_u32 s61, s61, 1\n"
      "s_add_u32 s62, s62, 0x4000\n"
      "s_and_b32 s62, s62, 0x1ffff\n"
      "v_readlane_b32 s60, v219, s61\n"
      "s_waitcnt vmcnt(8)\n"
      "s_memrealtime s[68:69]\n"
      "ds_read_b128 v[64:67], v210 offset:0\n"
      "ds_read_b128 v[68:71], v210 offset:1024\n"
      "ds_read_b128 v[72:75], v210 offset:2048\n"
      "ds_read_b128 v[76:79], v210 offset:3072\n"
      "ds_read_b128 v[80:83], v210 offset:16384\n"
      "ds_read_b128 v[84:87], v210 offset:17408\n"
      "ds_read_b128 v[88:91], v210 offset:18432\n"
      "ds_read_b128 v[92:95], v210 offset:19456\n"
      "s_add_u32 s20, s18, 65536\n"
      "s_addc_u32 s21, s19, 0\n"
      "s_lshl_b32 s75, %[blk], 6\n"
      "s_add_u32 s75, s75, s50\n"
      "s_waitcnt lgkmcnt(0)\n"
      "s_sub_u32 s67, s68, s82\n"
      "s_min_u32 s67, s67, 165\n"
      "s_add_u32 s93, s82, s67\n"
      "s_sub_u32 s71, s68, 400\n"
      "s_cmp_lt_u32 s75, 512\n"
      "s_cbranch_scc0 NOLEAD_%=\n"
      "v_mov_b32 v188, s68\n"
      "v_mov_b32 v189, s68\n"
      "v_mov_b32 v190, s68\n"
      "v_mov_b32 v191, s68\n"
      "buffer_store_dwordx4 v[188:191], v250, s[20:23], s75 offen sc1\n"
      "s_nop 1\n"
      "NOLEAD_%=:\n"
      "s_mov_b32 m0, s62\n"
      "s_nop 0\n"
      "buffer_load_dwordx4 v228, s[56:59], s60 offen nt lds\n"
      "buffer_load_dwordx4 v229, s[56:59], s60 offen offset:1024 nt lds\n"
      "buffer_load_dwordx4 v230, s[56:59], s60 offen offset:2048 nt lds\n"
      "buffer_load_dwordx4 v231, s[56:59], s60 offen offset:3072 nt lds\n"
      "s_xor_b64 s[56:57], s[56:57], s[32:33]\n"
      "s_add_u32 s61, s61, 1\n"
      "s_add_u32 s62, s62, 0x4000\n"
      "s_and_b32 s62, s62, 0x1ffff\n"
      "v_readlane_b32 s60, v219, s61\n"
      "s_xor_b64 s[56:57], s[56:57], s[32:33]\n"
      "s_add_u32 s61, s61, 1\n"
      "s_add_u32 s62, s62, 0x4000\n"
      "s_and_b32 s62, s62, 0x1ffff\n"
      "v_readlane_b32 s60, v219, s61\n"
      "s_add_u32 m0, s45, 49152\n"
      "s_nop 0\n"
      "buffer_load_dwordx4 v248, s[28:31], 0 offen nt lds\n"
      "s_bitset1_b32 m0, 10\n"
      "s_nop 0\n"
      "buffer_load_dwordx4 v249, s[28:31], 0 offen nt lds\n"
      "v_lshlrev_b32 v188, 16, v64\n"
      "v_and_b32 v189, 0xffff0000, v64\n"
      "v_lshlrev_b32 v190, 16, v65\n"
      "v_and_b32 v191, 0xffff0000, v65\n"
      "v_lshlrev_b32 v192, 16, v66\n"
      "v_and_b32 v193, 0xffff0000, v66\n"
      "v_lshlrev_b32 v194, 16, v67\n"
      "v_and_b32 v195, 0xffff0000, v67\n"
      "v_pk_mul_f32 v[188:189], v[188:189], s[94:95]\n"
      "v_pk_mul_f32 v[190:191], v[190:191], s[94:95]\n"
      "v_pk_mul_f32 v[192:193], v[192:193], s[94:95]\n"
      "v_pk_mul_f32 v[194:195], v[194:195], s[94:95]\n"
      "v_cvt_pk_bf16_f32 v64, v188, v189\n"
      "v_cvt_pk_bf16_f32 v65, v190, v191\n"
      "v_cvt_pk_bf16_f32 v66, v192, v193\n"
      "v_cvt_pk_bf16_f32 v67, v194, v195\n"
      "ds_write_b128 v210, v[64:67] offset:0\n"
      "v_lshlrev_b32 v188, 16, v68\n"
      "v_and_b32 v189, 0xffff0000, v68\n"
      "v_lshlrev_b32 v190, 16, v69\n"
      "v_and_b32 v191, 0xffff0000, v69\n"
      "v_lshlrev_b32 v192, 16, v70\n"
      "v_and_b32 v193, 0xffff0000, v70\n"
      "v_lshlrev_b32 v194, 16, v71\n"
      "v_and_b32 v195, 0xffff0000, v71\n"
      "v_pk_mul_f32 v[188:189], v[188:189], s[94:95]\n"
      "v_pk_mul_f32 v[190:191], v[190:191], s[94:95]\n"
      "v_pk_mul_f32 v[192:193], v[192:193], s[94:95]\n"
      "v_pk_mul_f32 v[194:195], v[194:195], s[94:95]\n"
      "v_cvt_pk_bf16_f32 v68, v188, v189\n"
      "v_cvt_pk_bf16_f32 v69, v190, v191\n"
      "v_cvt_pk_bf16_f32 v70, v192, v193\n"
      "v_cvt_pk_bf16_f32 v71, v194, v195\n"
      "ds_write_b128 v210, v[68:71] offset:1024\n"
      "v_lshlrev_b32 v188, 16, v72\n"
      "v_and_b32 v189, 0xffff0000, v72\n"
      "v_lshlrev_b32 v190, 16, v73\n"
      "v_and_b32 v191, 0xffff0000, v73\n"
      "v_lshlrev_b32 v192, 16, v74\n"
      "v_and_b32 v193, 0xffff0000, v74\n"
      "v_lshlrev_b32 v194, 16, v75\n"
      "v_and_b32 v195, 0xffff0000, v75\n"
      "v_pk_mul_f32 v[188:189], v[188:189], s[94:95]\n"
      "v_pk_mul_f32 v[190:191], v[190:191], s[94:95]\n"
      "v_pk_mul_f32 v[192:193], v[192:193], s[94:95]\n"
      "v_pk_mul_f32 v[194:195], v[194:195], s[94:95]\n"
      "v_cvt_pk_bf16_f32 v72, v188, v189\n"
      "v_cvt_pk_bf16_f32 v73, v190, v191\n"
      "v_cvt_pk_bf16_f32 v74, v192, v193\n"
      "v_cvt_pk_bf16_f32 v75, v194, v195\n"
      "ds_write_b128 v210, v[72:75] offset:2048\n"
      "v_lshlrev_b32 v188, 16, v76\n"
      "v_and_b32 v189, 0xffff0000, v76\n"
      "v_lshlrev_b32 v190, 16, v77\n"
      "v_and_b32 v191, 0xffff0000, v77\n"
      "v_lshlrev_b32 v192, 16, v78\n"
      "v_and_b32 v193, 0xffff0000, v78\n"
      "v_lshlrev_b32 v194, 16, v79\n"
      "v_and_b32 v195, 0xffff0000, v79\n"
      "v_pk_mul_f32 v[188:189], v[188:189], s[94:95]\n"
      "v_pk_mul_f32 v[190:191], v[190:191], s[94:95]\n"
      "v_pk_mul_f32 v[192:193], v[192:193], s[94:95]\n"
      "v_pk_mul_f32 v[194:195], v[194:195], s[94:95]\n"
      "v_cvt_pk_bf16_f32 v76, v188, v189\n"
      "v_cvt_pk_bf16_f32 v77, v190, v191\n"
      "v_cvt_pk_bf16_f32 v78, v192, v193\n"
      "v_cvt_pk_bf16_f32 v79, v194, v195\n"
      "ds_write_b128 v210, v[76:79] offset:3072\n"
      "v_lshlrev_b32 v188, 16, v80\n"
      "v_and_b32 v189, 0xffff0000, v80\n"
      "v_lshlrev_b32 v190, 16, v81\n"
      "v_and_b32 v191, 0xffff0000, v81\n"
      "v_lshlrev_b32 v192, 16, v82\n"
      "v_and_b32 v193, 0xffff0000, v82\n"
      "v_lshlrev_b32 v194, 16, v83\n"
      "v_and_b32 v195, 0xffff0000, v83\n"
      "v_pk_mul_f32 v[188:189], v[188:189], s[94:95]\n"
      "v_pk_mul_f32 v[190:191], v[190:191], s[94:95]\n"
      "v_pk_mul_f32 v[192:193], v[192:193], s[94:95]\n"
      "v_pk_mul_f32 v[194:195], v[194:195], s[94:95]\n"
      "v_cvt_pk_bf16_f32 v80, v188, v189\n"
      "v_cvt_pk_bf16_f32 v81, v190, v191\n"
      "v_cvt_pk_bf16_f32 v82, v192, v193\n"
      "v_cvt_pk_bf16_f32 v83, v194, v195\n"
      "ds_write_b128 v210, v[80:83] offset:16384\n"
      "v_lshlrev_b32 v188, 16, v84\n"
      "v_and_b32 v189, 0xffff0000, v84\n"
      "v_lshlrev_b32 v190, 16, v85\n"
      "v_and_b32 v191, 0xffff0000, v85\n"
      "v_lshlrev_b32 v192, 16, v86\n"
      "v_and_b32 v193, 0xffff0000, v86\n"
      "v_lshlrev_b32 v194, 16, v87\n"
      "v_and_b32 v195, 0xffff0000, v87\n"
      "v_pk_mul_f32 v[188:189], v[188:189], s[94:95]\n"
      "v_pk_mul_f32 v[190:191], v[190:191], s[94:95]\n"
      "v_pk_mul_f32 v[192:193], v[192:193], s[94:95]\n"
      "v_pk_mul_f32 v[194:195], v[194:195], s[94:95]\n"
      "v_cvt_pk_bf16_f32 v84, v188, v189\n"
      "v_cvt_pk_bf16_f32 v85, v190, v191\n"
      "v_cvt_pk_bf16_f32 v86, v192, v193\n"
      "v_cvt_pk_bf16_f32 v87, v194, v195\n"
      "ds_write_b128 v210, v[84:87] offset:17408\n"
      "v_lshlrev_b32 v188, 16, v88\n"
      "v_and_b32 v189, 0xffff0000, v88\n"
      "v_lshlrev_b32 v190, 16, v89\n"
      "v_and_b32 v191, 0xffff0000, v89\n"
      "v_lshlrev_b32 v192, 16, v90\n"
      "v_and_b32 v193, 0xffff0000, v90\n"
      "v_lshlrev_b32 v194, 16, v91\n"
      "v_and_b32 v195, 0xffff0000, v91\n"
      "v_pk_mul_f32 v[188:189], v[188:189], s[94:95]\n"
      "v_pk_mul_f32 v[190:191], v[190:191], s[94:95]\n"
      "v_pk_mul_f32 v[192:193], v[192:193], s[94:95]\n"
      "v_pk_mul_f32 v[194:195], v[194:195], s[94:95]\n"
      "v_cvt_pk_bf16_f32 v88, v188, v189\n"
      "v_cvt_pk_bf16_f32 v89, v190, v191\n"
      "v_cvt_pk_bf16_f32 v90, v192, v193\n"
      "v_cvt_pk_bf16_f32 v91, v194, v195\n"
      "ds_write_b128 v210, v[88:91] offset:18432\n"
      "v_lshlrev_b32 v188, 16, v92\n"
      "v_and_b32 v189, 0xffff0000, v92\n"
      "v_lshlrev_b32 v190, 16, v93\n"
      "v_and_b32 v191, 0xffff0000, v93\n"
      "v_lshlrev_b32 v192, 16, v94\n"
      "v_and_b32 v193, 0xffff0000, v94\n"
      "v_lshlrev_b32 v194, 16, v95\n"
      "v_and_b32 v195, 0xffff0000, v95\n"
      "v_pk_mul_f32 v[188:189], v[188:189], s[94:95]\n"
      "v_pk_mul_f32 v[190:191], v[190:191], s[94:95]\n"
      "v_pk_mul_f32 v[192:193], v[192:193], s[94:95]\n"
      "v_pk_mul_f32 v[194:195], v[194:195], s[94:95]\n"
      "v_cvt_pk_bf16_f32 v92, v188, v189\n"
      "v_cvt_pk_bf16_f32 v93, v190, v191\n"
      "v_cvt_pk_bf16_f32 v94, v192, v193\n"
      "v_cvt_pk_bf16_f32 v95, v194, v195\n"
      "ds_write_b128 v210, v[92:95] offset:19456\n"
      "s_mov_b32 m0, s62\n"
      "s_nop 0\n"
      "buffer_load_dwordx4 v228, s[56:59], s60 offen nt lds\n"
      "buffer_load_dwordx4 v229, s[56:59], s60 offen offset:1024 nt lds\n"
      "buffer_load_dwordx4 v230, s[56:59], s60 offen offset:2048 nt lds\n"
      "buffer_load_dwordx4 v231, s[56:59], s60 offen offset:3072 nt lds\n"
      "s_xor_b64 s[56:57], s[56:57], s[32:33]\n"
      "s_add_u32 s61, s61, 1\n"
      "s_add_u32 s62, s62, 0x4000\n"
      "s_and_b32 s62, s62, 0x1ffff\n"
      "v_readlane_b32 s60, v219, s61\n"
      "s_cmp_eq_u32 s50, 0\n"
      "s_cselect_b32 s38, 0x41554001, 1\n"
      "s_cselect_b32 s39, 0x3f440083, 3\n"
      "s_cselect_b32 s52, 0x3133aa90, 0\n"
      "s_cselect_b32 s53, 0x80011, 0\n"
      "s_mov_b64 s[34:35], 0\n"
      "s_and_b32 s69, %[blk], 7\n"
      "s_mul_i32 s69, s69, 5\n"
      "s_lshr_b32 s74, %[blk], 4\n"
      "s_add_u32 s69, s69, s74\n"
      "s_and_b32 s69, s69, 15\n"
      "s_mul_i32 s74, s69, 16\n"
      "s_lshr_b32 s74, s74, 6\n"
      "s_lshl2_add_u32 s76, s74, 9\n"
      "s_bitset1_b64 s[34:35], s76\n"
      "s_lshl_b32 s74, s74, 1\n"
      "s_sub_u32 s74, 28, s74\n"
      "s_bitset1_b32 s39, s74\n"
      "s_mul_i32 s74, s69, 20\n"
      "s_lshr_b32 s74, s74, 6\n"
      "s_lshl2_add_u32 s76, s74, 25\n"
      "s_bitset1_b64 s[34:35], s76\n"
      "s_lshl_b32 s74, s74, 1\n"
      "s_sub_u32 s74, 20, s74\n"
      "s_bitset1_b32 s39, s74\n"
      "s_mul_i32 s74, s69, 20\n"
      "s_lshr_b32 s74, s74, 6\n"
      "s_lshl2_add_u32 s76, s74, 45\n"
      "s_bitset1_b64 s[34:35], s76\n"
      "s_lshl_b32 s74, s74, 1\n"
      "s_sub_u32 s74, 10, s74\n"
      "s_bitset1_b32 s39, s74\n"
      "s_mov_b32 s87, 0\n"
      "s_mov_b32 s55, 0\n"
      "s_mov_b32 s54, 3\n"
      "s_mov_b32 s70, 250\n"
      "s_mov_b32 s86, 59\n"
      "s_mov_b32 s77, 177\n"
      "s_lshl_b32 s91, s50, 16\n"
      "s_sub_u32 s80, 46, s91\n"
      "s_sub_u32 s90, 34, s91\n"
      "s_sub_u32 s72, s75, 14336\n"
      "s_and_b32 s79, %[blk], 31\n"
      "s_lshl_b32 s79, s79, 8\n"
      "s_add_u32 s79, s79, 8192\n"
      "s_mul_i32 s69, %[blk], 53\n"
      "s_lshr_b32 s69, s69, 8\n"
      "s_add_u32 s66, s93, 150\n"
      "s_sub_u32 s81, s93, s71\n"
      "s_sub_u32 s81, s81, s69\n"
      "s_add_u32 s81, s81, 24\n"
      "s_sub_u32 s66, s66, s91\n"
      "s_mov_b32 s88, s66\n"
      "s_mov_b32 s65, s66\n"
      "s_getpc_b64 s[84:85]\n"
      "PC_%=:\n"
      "s_add_u32 s84, s84, HOOK_%=-PC_%=\n"
      "s_addc_u32 s85, s85, 0\n"
      "s_waitcnt vmcnt(14) lgkmcnt(0)\n"
      "s_barrier\n"
      "ds_read_b128 a[64:67], v198 offset:8192\n"
      "ds_read_b128 a[72:75], v199 offset:8192\n"
      "ds_read_b128 a[80:83], v198 offset:9216\n"
      "ds_read_b128 a[88:91], v199 offset:9216\n"
      "ds_read_b128 a[0:3], v196 offset:0\n"
      "ds_read_b128 a[16:19], v197 offset:0\n"
      "ds_read_b128 a[32:35], v196 offset:1024\n"
      "ds_read_b128 a[48:51], v197 offset:1024\n"
      "ds_read_b128 a[4:7], v196 offset:4096\n"
      "ds_read_b128 a[20:23], v197 offset:4096\n"
      "ds_read_b128 a[36:39], v196 offset:5120\n"
      "ds_read_b128 a[52:55], v197 offset:5120\n"
      "v_readfirstlane_b32 s40, v208\n"
      "s_mov_b32 s94, 0xceabfb8f\n"
      "v_lshlrev_b32 v144, 16, v200\n"
      "v_and_b32 v145, 0xffff0000, v200\n"
      "v_lshlrev_b32 v146, 16, v201\n"
      "v_and_b32 v147, 0xffff0000, v201\n"
      "v_lshlrev_b32 v148, 16, v202\n"
      "v_and_b32 v149, 0xffff0000, v202\n"
      "v_lshlrev_b32 v150, 16, v203\n"
      "v_and_b32 v151, 0xffff0000, v203\n"
      "v_lshlrev_b32 v152, 16, v204\n"
      "v_and_b32 v153, 0xffff0000, v204\n"
      "v_lshlrev_b32 v154, 16, v205\n"
      "v_and_b32 v155, 0xffff0000, v205\n"
      "v_lshlrev_b32 v156, 16, v206\n"
      "v_and_b32 v157, 0xffff0000, v206\n"
      "v_lshlrev_b32 v158, 16, v207\n"
      "v_and_b32 v159, 0xffff0000, v207\n"
      "s_waitcnt lgkmcnt(4)\n"
      "v_mfma_f32_16x16x32_bf16 v[64:67], a[64:67], a[0:3], 0\n"
      "ds_read_b128 a[8:11], v214 offset:0\n"
      "v_fma_f32 v144, -v144, s94, s94\n"
      "v_mfma_f32_16x16x32_bf16 v[64:67], a[72:75], a[16:19], v[64:67]\n"
      "ds_read_b128 a[24:27], v215 offset:0\n"
      "v_fma_f32 v145, -v145, s94, s94\n"
      "v_mfma_f32_16x16x32_bf16 v[64:67], a[80:83], a[32:35], v[64:67]\n"
      "ds_read_b128 a[40:43], v214 offset:1024\n"
      "v_fma_f32 v146, -v146, s94, s94\n"
      "v_mfma_f32_16x16x32_bf16 v[64:67], a[88:91], a[48:51], v[64:67]\n"
      "ds_read_b128 a[56:59], v215 offset:1024\n"
      "v_fma_f32 v147, -v147, s94, s94\n"
      "s_waitcnt lgkmcnt(4)\n"
      "v_mfma_f32_16x16x32_bf16 v[72:75], a[64:67], a[4:7], 0\n"
      "ds_read_b128 a[12:15], v214 offset:4096\n"
      "v_fma_f32 v148, -v148, s94, s94\n"
      "v_mfma_f32_16x16x32_bf16 v[72:75], a[72:75], a[20:23], v[72:75]\n"
      "ds_read_b128 a[28:31], v215 offset:4096\n"
      "v_fma_f32 v149, -v149, s94, s94\n"
      "v_mfma_f32_16x16x32_bf16 v[72:75], a[80:83], a[36:39], v[72:75]\n"
      "ds_read_b128 a[44:47], v214 offset:5120\n"
      "v_fma_f32 v150, -v150, s94, s94\n"
      "v_mfma_f32_16x16x32_bf16 v[72:75], a[88:91], a[52:55], v[72:75]\n"
      "ds_read_b128 a[60:63], v215 offset:5120\n"
      "v_fma_f32 v151, -v151, s94, s94\n"
      "s_waitcnt lgkmcnt(4)\n"
      "v_mfma_f32_16x16x32_bf16 v[80:83], a[64:67], a[8:11], 0\n"
      "ds_read_b128 a[96:99], v198 offset:0\n"
      "v_fma_f32 v152, -v152, s94, s94\n"
      "v_mfma_f32_16x16x32_bf16 v[80:83], a[72:75], a[24:27], v[80:83]\n"
      "ds_read_b128 a[100:103], v198 offset:4096\n"
      "v_fma_f32 v153, -v153, s94, s94\n"
      "v_mfma_f32_16x16x32_bf16 v[80:83], a[80:83], a[40:43], v[80:83]\n"
      "ds_read_b128 a[104:107], v199 offset:0\n"
      "v_fma_f32 v154, -v154, s94, s94\n"
      "v_mfma_f32_16x16x32_bf16 v[80:83], a[88:91], a[56:59], v[80:83]\n"
      "ds_read_b128 a[108:111], v199 offset:4096\n"
      "v_fma_f32 v155, -v155, s94, s94\n"
      "s_waitcnt lgkmcnt(4)\n"
      "v_mfma_f32_16x16x32_bf16 v[88:91], a[64:67], a[12:15], 0\n"
      "ds_read_b128 a[112:115], v198 offset:1024\n"
      "v_fma_f32 v156, -v156, s94, s94\n"
      "v_mfma_f32_16x16x32_bf16 v[88:91], a[72:75], a[28:31], v[88:91]\n"
      "ds_read_b128 a[116:119], v198 offset:5120\n"
      "v_fma_f32 v157, -v157, s94, s94\n"
      "v_mfma_f32_16x16x32_bf16 v[88:91], a[80:83], a[44:47], v[88:91]\n"
      "ds_read_b128 a[120:123], v199 offset:1024\n"
      "v_fma_f32 v158, -v158, s94, s94\n"
      "v_mfma_f32_16x16x32_bf16 v[88:91], a[88:91], a[60:63], v[88:91]\n"
      "ds_read_b128 a[124:127], v199 offset:5120\n"
      "v_fma_f32 v159, -v159, s94, s94\n"
      "s_mov_b32 m0, s62\n"
      "s_nop 0\n"
      "buffer_load_dwordx4 v228, s[56:59], s60 offen nt lds\n"
      "buffer_load_dwordx4 v229, s[56:59], s60 offen offset:1024 nt lds\n"
      "buffer_load_dwordx4 v230, s[56:59], s60 offen offset:2048 nt lds\n"
      "buffer_load_dwordx4 v231, s[56:59], s60 offen offset:3072 nt lds\n"
      "s_waitcnt lgkmcnt(0)\n"
      "v_mfma_f32_16x16x32_bf16 v[96:99], a[96:99], a[0:3], v[144:147]\n"
      "s_add_u32 s40, s40, 0x7f4a7c15\n"
      "v_mfma_f32_16x16x32_bf16 v[96:99], a[104:107], a[16:19], v[96:99]\n"
      "s_lshl_b32 s69, s44, 3\n"
      "v_max3_f32 v188, v64, v65, v66\n"
      "v_max3_f32 v189, v72, v73, v74\n"
      "v_mfma_f32_16x16x32_bf16 v[96:99], a[112:115], a[32:35], v[96:99]\n"
      "s_lshr_b32 s74, s50, 12\n"
      "v_max3_f32 v190, v80, v81, v82\n"
      "v_max3_f32 v191, v88, v89, v90\n"
      "v_mfma_f32_16x16x32_bf16 v[96:99], a[120:123], a[48:51], v[96:99]\n"
      "s_add_u32 s69, s69, s74\n"
      "v_max_f32 v188, v188, v67\n"
      "v_max_f32 v189, v189, v75\n"
      "v_mfma_f32_16x16x32_bf16 v[104:107], a[96:99], a[4:7], v[152:155]\n"
      "s_mul_i32 s69, s69, 4608\n"
      "v_max_f32 v190, v190, v83\n"
      "v_max_f32 v191, v191, v91\n"
      "v_mfma_f32_16x16x32_bf16 v[104:107], a[104:107], a[20:23], v[104:107]\n"
      "s_add_u32 s69, s69, 81920\n"
      "v_mov_b32 v192, v188\n"
      "v_mov_b32 v193, v189\n"
      "v_mfma_f32_16x16x32_bf16 v[104:107], a[112:115], a[36:39], v[104:107]\n"
      "s_add_u32 s24, s18, s69\n"
      "v_mov_b32 v194, v190\n"
      "v_mov_b32 v195, v191\n"
      "v_mfma_f32_16x16x32_bf16 v[104:107], a[120:123], a[52:55], v[104:107]\n"
      "s_addc_u32 s25, s19, 0\n"
      "v_permlane16_swap_b32 v188, v192\n"
      "v_permlane16_swap_b32 v189, v193\n"
      "v_mfma_f32_16x16x32_bf16 v[112:115], a[96:99], a[8:11], v[144:147]\n"
      "s_movk_i32 s26, 23040\n"
      "v_permlane16_swap_b32 v190, v194\n"
      "v_permlane16_swap_b32 v191, v195\n"
      "v_mfma_f32_16x16x32_bf16 v[112:115], a[104:107], a[24:27], v[112:115]\n"
      "s_mul_i32 s78, s43, 18432\n"
      "v_max_f32 v188, v188, v192\n"
      "v_max_f32 v189, v189, v193\n"
      "v_mfma_f32_16x16x32_bf16 v[112:115], a[112:115], a[40:43], v[112:115]\n"
      "s_and_b32 s69, s50, 4096\n"
      "v_max_f32 v190, v190, v194\n"
      "v_max_f32 v191, v191, v195\n"
      "v_mfma_f32_16x16x32_bf16 v[112:115], a[120:123], a[56:59], v[112:115]\n"
      "s_lshl_b32 s69, s69, 2\n"
      "v_mov_b32 v192, v188\n"
      "v_mov_b32 v193, v189\n"
      "v_mfma_f32_16x16x32_bf16 v[120:123], a[96:99], a[12:15], v[152:155]\n"
      "s_and_b32 s74, s50, 8192\n"
      "v_mov_b32 v194, v190\n"
      "v_mov_b32 v195, v191\n"
      "v_mfma_f32_16x16x32_bf16 v[120:123], a[104:107], a[28:31], v[120:123]\n"
      "s_add_u32 s74, s74, 4096\n"
      "v_permlane32_swap_b32 v188, v192\n"
      "v_permlane32_swap_b32 v189, v193\n"
      "v_mfma_f32_16x16x32_bf16 v[120:123], a[112:115], a[44:47], v[120:123]\n"
      "s_add_u32 s69, s69, s74\n"
      "v_permlane32_swap_b32 v190, v194\n"
      "v_permlane32_swap_b32 v191, v195\n"
      "v_mfma_f32_16x16x32_bf16 v[120:123], a[120:123], a[60:63], v[120:123]\n"
      "s_lshl_b32 s74, s43, 7\n"
      "v_max_f32 v188, v188, v192\n"
      "v_max_f32 v189, v189, v193\n"
      "v_mfma_f32_16x16x32_bf16 v[100:103], a[100:103], a[0:3], v[148:151]\n"
      "s_add_u32 s69, s69, s74\n"
      "v_max_f32 v190, v190, v194\n"
      "v_max_f32 v191, v191, v195\n"
      "v_mfma_f32_16x16x32_bf16 v[100:103], a[108:111], a[16:19], v[100:103]\n"
      "s_lshl_b32 s74, s44, 15\n"
      "v_xor_b32 v176, 0x80000000, v188\n"
      "v_xor_b32 v177, 0x80000000, v189\n"
      "v_mfma_f32_16x16x32_bf16 v[100:103], a[116:119], a[32:35], v[100:103]\n"
      "s_add_u32 s69, s69, s74\n"
      "v_xor_b32 v178, 0x80000000, v190\n"
      "v_xor_b32 v179, 0x80000000, v191\n"
      "v_mfma_f32_16x16x32_bf16 v[100:103], a[124:127], a[48:51], v[100:103]\n"
      "s_add_u32 s30, s16, s69\n"
      "v_mov_b32 v160, v176\n"
      "v_mov_b32 v161, v176\n"
      "v_mfma_f32_16x16x32_bf16 v[108:111], a[100:103], a[4:7], v[156:159]\n"
      "s_addc_u32 s31, s17, 0\n"
      "v_mov_b32 v162, v176\n"
      "v_mov_b32 v163, v176\n"
      "v_mfma_f32_16x16x32_bf16 v[108:111], a[108:111], a[20:23], v[108:111]\n"
      "v_mov_b32 v164, v177\n"
      "v_mov_b32 v165, v177\n"
      "v_mfma_f32_16x16x32_bf16 v[108:111], a[116:119], a[36:39], v[108:111]\n"
      "v_mov_b32 v166, v177\n"
      "v_mov_b32 v167, v177\n"
      "v_mfma_f32_16x16x32_bf16 v[108:111], a[124:127], a[52:55], v[108:111]\n"
      "v_mov_b32 v168, v178\n"
      "v_mov_b32 v169, v178\n"
      "v_mfma_f32_16x16x32_bf16 v[116:119], a[100:103], a[8:11], v[148:151]\n"
      "v_mov_b32 v170, v178\n"
      "v_mov_b32 v171, v178\n"
      "v_mfma_f32_16x16x32_bf16 v[116:119], a[108:111], a[24:27], v[116:119]\n"
      "v_mov_b32 v172, v179\n"
      "v_mov_b32 v173, v179\n"
      "v_mfma_f32_16x16x32_bf16 v[116:119], a[116:119], a[40:43], v[116:119]\n"
      "v_mov_b32 v174, v179\n"
      "v_mov_b32 v175, v179\n"
      "v_mfma_f32_16x16x32_bf16 v[116:119], a[124:127], a[56:59], v[116:119]\n"
      "v_mov_b32 v209, 139248\n"
      "v_lshl_add_u32 v232, s50, 1, v241\n"
      "v_mfma_f32_16x16x32_bf16 v[124:127], a[100:103], a[12:15], v[156:159]\n"
      "v_add_u32 v232, 0x10000, v232\n"
      "v_xor_b32 v233, 0x4000, v232\n"
      "v_mfma_f32_16x16x32_bf16 v[124:127], a[108:111], a[28:31], v[124:127]\n"
      "v_add_u32 v234, 0x10000, v232\n"
      "v_xor_b32 v235, 0x4000, v234\n"
      "v_mfma_f32_16x16x32_bf16 v[124:127], a[116:119], a[44:47], v[124:127]\n"
      "v_lshrrev_b32 v240, 1, v241\n"
      "v_add_u32 v240, 0x1000, v240\n"
      "v_mfma_f32_16x16x32_bf16 v[124:127], a[124:127], a[60:63], v[124:127]\n"
      "v_mov_b32 v196, 0\n"
      "v_pk_add_f32 v[96:97], v[96:97], v[160:161]\n"
      "v_pk_add_f32 v[98:99], v[98:99], v[160:161]\n"
      "v_pk_add_f32 v[104:105], v[104:105], v[164:165]\n"
      "v_pk_add_f32 v[106:107], v[106:107], v[164:165]\n"
      "v_pk_add_f32 v[112:113], v[112:113], v[168:169]\n"
      "v_pk_add_f32 v[114:115], v[114:115], v[168:169]\n"
      "v_pk_add_f32 v[120:121], v[120:121], v[172:173]\n"
      "v_pk_add_f32 v[122:123], v[122:123], v[172:173]\n"
      "v_pk_add_f32 v[100:101], v[100:101], v[160:161]\n"
      "v_pk_add_f32 v[102:103], v[102:103], v[160:161]\n"
      "v_pk_add_f32 v[108:109], v[108:109], v[164:165]\n"
      "v_pk_add_f32 v[110:111], v[110:111], v[164:165]\n"
      "v_pk_add_f32 v[116:117], v[116:117], v[168:169]\n"
      "v_pk_add_f32 v[118:119], v[118:119], v[168:169]\n"
      "v_pk_add_f32 v[124:125], v[124:125], v[172:173]\n"
      "v_pk_add_f32 v[126:127], v[126:127], v[172:173]\n"
      "v_mov_b32 v224, v244\n"
      "v_mov_b32 v225, v245\n"
      "v_mov_b32 v226, v246\n"
      "v_mov_b32 v227, v247\n"
      "v_xor_b32 v220, 0x2000, v244\n"
      "v_xor_b32 v221, 0x2000, v245\n"
      "v_xor_b32 v222, 0x2000, v246\n"
      "v_xor_b32 v223, 0x2000, v247\n"
      "s_lshl_b32 s76, s43, 3\n"
      "v_mov_b32 v237, s40\n"
      "v_mov_b32 v238, s76\n"
      "v_xor_b32 v239, 8, v238\n"
      "s_memrealtime s[68:69]\n"
      "s_waitcnt vmcnt(14) lgkmcnt(0)\n"
      "s_sub_u32 s67, s68, s88\n"
      "s_cmp_lt_i32 s67, -8\n"
      "s_cbranch_scc1 GW0_%=\n"
      "GX0_%=:\n"
      "s_barrier\n"
      "ds_read_b128 a[64:67], v242 offset:0\n"
      "ds_read_b128 a[68:71], v242 offset:4096\n"
      "ds_read_b128 a[72:75], v243 offset:0\n"
      "ds_read_b128 a[76:79], v243 offset:4096\n"
      "ds_read_b128 a[80:83], v242 offset:1024\n"
      "ds_read_b128 a[84:87], v242 offset:5120\n"
      "ds_read_b128 a[88:91], v243 offset:1024\n"
      "ds_read_b128 a[92:95], v243 offset:5120\n"
      "ds_write_b32 v209, v196\n"
      "s_add_u32 s38, s38, s38\n"
      "s_memrealtime s[68:69]\n"
      "s_xor_b64 s[56:57], s[56:57], s[32:33]\n"
      "s_add_u32 s61, s61, 1\n"
      "s_add_u32 s62, s62, 0x4000\n"
      "s_and_b32 s62, s62, 0x1ffff\n"
      "v_readlane_b32 s60, v219, s61\n"
      "s_waitcnt lgkmcnt(0)\n"
      "s_sub_u32 s67, s68, s66\n"
      "s_cmp_gt_i32 s67, s70\n"
      "s_cselect_b32 s69, s77, s86\n"
      "s_cselect_b32 s70, 60, s70\n"
      "s_cselect_b32 s90, s80, s90\n"
      "s_add_u32 s66, s66, s69\n"
      "s_mov_b32 s82, s68\n"
      "LOOP_%=:\n"
      "v_mfma_f32_16x16x32_bf16 v[64:67], a[64:67], a[0:3], v[160:163]\n"
      "s_add_u32 s65, s68, s90\n"
      "v_exp_f32 v188, v96\n"
      "v_mfma_f32_16x16x32_bf16 v[64:67], a[72:75], a[16:19], v[64:67]\n"
      "s_max_u32 s88, s66, s65\n"
      "v_add_f32 v180, v180, v184\n"
      "v_add_f32 v181, v181, v185\n"
      "v_mfma_f32_16x16x32_bf16 v[64:67], a[80:83], a[32:35], v[64:67]\n"
      "v_exp_f32 v189, v97\n"
      "v_mfma_f32_16x16x32_bf16 v[64:67], a[88:91], a[48:51], v[64:67]\n"
      "v_add_f32 v182, v182, v186\n"
      "v_add_f32 v183, v183, v187\n"
      "v_mfma_f32_16x16x32_bf16 v[72:75], a[64:67], a[4:7], v[164:167]\n"
      "v_exp_f32 v190, v98\n"
      "v_mfma_f32_16x16x32_bf16 v[72:75], a[72:75], a[20:23], v[72:75]\n"
      "v_exp_f32 v191, v99\n"
      "v_mfma_f32_16x16x32_bf16 v[72:75], a[80:83], a[36:39], v[72:75]\n"
      "v_cvt_pk_bf16_f32 v144, v188, v189\n"
      "v_cvt_pk_bf16_f32 v145, v190, v191\n"
      "v_mfma_f32_16x16x32_bf16 v[72:75], a[88:91], a[52:55], v[72:75]\n"
      "v_exp_f32 v192, v100\n"
      "v_mfma_f32_16x16x32_bf16 v[80:83], a[64:67], a[8:11], v[168:171]\n"
      "s_add_u32 s39, s39, s39\n"
      "v_exp_f32 v193, v101\n"
      "v_mfma_f32_16x16x32_bf16 v[80:83], a[72:75], a[24:27], v[80:83]\n"
      "s_memrealtime s[68:69]\n"
      "v_exp_f32 v194, v102\n"
      "v_mfma_f32_16x16x32_bf16 v[80:83], a[80:83], a[40:43], v[80:83]\n"
      "v_exp_f32 v195, v103\n"
      "v_mfma_f32_16x16x32_bf16 v[80:83], a[88:91], a[56:59], v[80:83]\n"
      "s_mov_b32 m0, s62\n"
      "v_cvt_pk_bf16_f32 v146, v192, v193\n"
      "v_cvt_pk_bf16_f32 v147, v194, v195\n"
      "v_mfma_f32_16x16x32_bf16 v[88:91], a[64:67], a[12:15], v[172:175]\n"
      "buffer_load_dwordx4 v228, s[56:59], s60 offen nt lds\n"
      "v_mfma_f32_16x16x32_bf16 v[184:187], a[128:131], v[144:147], 0\n"
      "buffer_load_dwordx4 v229, s[56:59], s60 offen offset:1024 nt lds\n"
      "v_mfma_f32_16x16x32_bf16 v[88:91], a[72:75], a[28:31], v[88:91]\n"
      "buffer_load_dwordx4 v230, s[56:59], s60 offen offset:2048 nt lds\n"
      "v_mfma_f32_16x16x32_bf16 v[88:91], a[80:83], a[44:47], v[88:91]\n"
      "buffer_load_dwordx4 v231, s[56:59], s60 offen offset:3072 nt lds\n"
      "s_cbranch_scc1 SP1_%=\n"
      "HD1_%=:\n"
      "s_cmp_lg_u32 s54, 0\n"
      "s_waitcnt vmcnt(12) lgkmcnt(0)\n"
      "s_cbranch_scc1 W1_%=\n"
      "s_waitcnt vmcnt(8)\n"
      "W1_%=:\n"
      "s_sub_u32 s67, s68, s88\n"
      "s_cmp_lt_i32 s67, -8\n"
      "s_cbranch_scc1 GW4_%=\n"
      "GX4_%=:\n"
      "s_barrier\n"
      "v_mfma_f32_16x16x32_bf16 v[88:91], a[88:91], a[60:63], v[88:91]\n"
      "s_memrealtime s[68:69]\n"
      "ds_read_b64_tr_b16 a[96:97], v244 offset:0\n"
      "ds_read_b64_tr_b16 a[98:99], v244 offset:4096\n"
      "v_mfma_f32_16x16x32_bf16 v[68:71], a[68:71], a[0:3], v[160:163]\n"
      "s_xor_b64 s[56:57], s[56:57], s[32:33]\n"
      "ds_read_b64_tr_b16 a[100:101], v245 offset:0\n"
      "ds_read_b64_tr_b16 a[102:103], v245 offset:4096\n"
      "v_mfma_f32_16x16x32_bf16 v[68:71], a[76:79], a[16:19], v[68:71]\n"
      "s_add_u32 s61, s61, 1\n"
      "ds_read_b64_tr_b16 a[104:105], v246 offset:0\n"
      "ds_read_b64_tr_b16 a[106:107], v246 offset:4096\n"
      "v_mfma_f32_16x16x32_bf16 v[68:71], a[84:87], a[32:35], v[68:71]\n"
      "s_add_u32 s62, s62, 0x4000\n"
      "ds_read_b64_tr_b16 a[108:109], v247 offset:0\n"
      "ds_read_b64_tr_b16 a[110:111], v247 offset:4096\n"
      "v_mfma_f32_16x16x32_bf16 v[68:71], a[92:95], a[48:51], v[68:71]\n"
      "s_and_b32 s62, s62, 0x1ffff\n"
      "v_exp_f32 v188, v104\n"
      "v_mfma_f32_16x16x32_bf16 v[76:79], a[68:71], a[4:7], v[164:167]\n"
      "v_readlane_b32 s60, v219, s61\n"
      "v_exp_f32 v189, v105\n"
      "v_mfma_f32_16x16x32_bf16 v[76:79], a[76:79], a[20:23], v[76:79]\n"
      "v_exp_f32 v190, v106\n"
      "v_mfma_f32_16x16x32_bf16 v[76:79], a[84:87], a[36:39], v[76:79]\n"
      "v_exp_f32 v191, v107\n"
      "v_mfma_f32_16x16x32_bf16 v[76:79], a[92:95], a[52:55], v[76:79]\n"
      "v_cvt_pk_bf16_f32 v148, v188, v189\n"
      "v_cvt_pk_bf16_f32 v149, v190, v191\n"
      "v_mfma_f32_16x16x32_bf16 v[84:87], a[68:71], a[8:11], v[168:171]\n"
      "v_exp_f32 v192, v108\n"
      "v_mfma_f32_16x16x32_bf16 v[84:87], a[76:79], a[24:27], v[84:87]\n"
      "v_exp_f32 v193, v109\n"
      "s_waitcnt lgkmcnt(0)\n"
      "v_mfma_f32_16x16x32_bf16 v[84:87], a[84:87], a[40:43], v[84:87]\n"
      "s_sub_u32 s67, s68, s66\n"
      "ds_read_b64_tr_b16 a[112:113], v244 offset:1024\n"
      "v_cvt_pk_bf16_f32 v150, v192, v193\n"
      "v_mfma_f32_16x16x32_bf16 v[84:87], a[92:95], a[56:59], v[84:87]\n"
      "s_cmp_gt_i32 s67, s70\n"
      "v_exp_f32 v194, v110\n"
      "v_mfma_f32_16x16x32_bf16 v[92:95], a[68:71], a[12:15], v[172:175]\n"
      "s_cselect_b32 s69, s77, s86\n"
      "ds_read_b64_tr_b16 a[114:115], v244 offset:5120\n"
      "ds_read_b64_tr_b16 a[116:117], v245 offset:1024\n"
      "v_mfma_f32_16x16x32_bf16 v[92:95], a[76:79], a[28:31], v[92:95]\n"
      "s_cselect_b32 s70, 60, s70\n"
      "v_exp_f32 v195, v111\n"
      "v_mfma_f32_16x16x32_bf16 v[92:95], a[84:87], a[44:47], v[92:95]\n"
      "s_cselect_b32 s90, s80, s90\n"
      "ds_read_b64_tr_b16 a[118:119], v245 offset:5120\n"
      "v_cvt_pk_bf16_f32 v151, v194, v195\n"
      "v_mfma_f32_16x16x32_bf16 v[92:95], a[92:95], a[60:63], v[92:95]\n"
      "s_add_u32 s66, s66, s69\n"
      "v_exp_f32 v188, v112\n"
      "v_mfma_f32_16x16x32_bf16 v[184:187], a[132:135], v[148:151], v[184:187]\n"
      "s_mov_b32 s82, s68\n"
      "ds_read_b64_tr_b16 a[120:121], v246 offset:1024\n"
      "ds_read_b64_tr_b16 a[122:123], v246 offset:5120\n"
      "v_mfma_f32_16x16x32_bf16 v[0:3], a[96:99], v[128:131], v[0:3]\n"
      "s_add_u32 s65, s68, s90\n"
      "v_exp_f32 v189, v113\n"
      "v_mfma_f32_16x16x32_bf16 v[4:7], a[96:99], v[132:135], v[4:7]\n"
      "s_max_u32 s88, s66, s65\n"
      "ds_read_b64_tr_b16 a[124:125], v247 offset:1024\n"
      "v_cvt_pk_bf16_f32 v152, v188, v189\n"
      "v_mfma_f32_16x16x32_bf16 a[144:147], a[96:99], v[136:139], a[144:147]\n"
      "v_exp_f32 v190, v114\n"
      "v_mfma_f32_16x16x32_bf16 a[148:151], a[96:99], v[140:143], a[148:151]\n"
      "v_exp_f32 v191, v115\n"
      "v_mfma_f32_16x16x32_bf16 a[156:159], a[100:103], v[140:143], a[156:159]\n"
      "ds_read_b64_tr_b16 a[126:127], v247 offset:5120\n"
      "v_cvt_pk_bf16_f32 v153, v190, v191\n"
      "v_mfma_f32_16x16x32_bf16 a[152:155], a[100:103], v[136:139], a[152:155]\n"
      "v_exp_f32 v192, v116\n"
      "v_mfma_f32_16x16x32_bf16 v[12:15], a[100:103], v[132:135], v[12:15]\n"
      "v_exp_f32 v193, v117\n"
      "v_mfma_f32_16x16x32_bf16 v[8:11], a[100:103], v[128:131], v[8:11]\n"
      "v_exp_f32 v194, v118\n"
      "v_mfma_f32_16x16x32_bf16 v[16:19], a[104:107], v[128:131], v[16:19]\n"
      "s_add_u32 s38, s38, s38\n"
      "v_exp_f32 v195, v119\n"
      "v_mfma_f32_16x16x32_bf16 v[20:23], a[104:107], v[132:135], v[20:23]\n"
      "s_memrealtime s[68:69]\n"
      "v_cvt_pk_bf16_f32 v154, v192, v193\n"
      "v_cvt_pk_bf16_f32 v155, v194, v195\n"
      "v_mfma_f32_16x16x32_bf16 a[160:163], a[104:107], v[136:139], a[160:163]\n"
      "v_exp_f32 v188, v120\n"
      "v_mfma_f32_16x16x32_bf16 v[184:187], a[136:139], v[152:155], v[184:187]\n"
      "s_mov_b32 m0, s62\n"
      "v_exp_f32 v189, v121\n"
      "v_mfma_f32_16x16x32_bf16 a[164:167], a[104:107], v[140:143], a[164:167]\n"
      "buffer_load_dwordx4 v228, s[56:59], s60 offen nt lds\n"
      "v_mfma_f32_16x16x32_bf16 a[172:175], a[108:111], v[140:143], a[172:175]\n"
      "buffer_load_dwordx4 v229, s[56:59], s60 offen offset:1024 nt lds\n"
      "v_mfma_f32_16x16x32_bf16 a[168:171], a[108:111], v[136:139], a[168:171]\n"
      "buffer_load_dwordx4 v230, s[56:59], s60 offen offset:2048 nt lds\n"
      "v_mfma_f32_16x16x32_bf16 v[28:31], a[108:111], v[132:135], v[28:31]\n"
      "buffer_load_dwordx4 v231, s[56:59], s60 offen offset:3072 nt lds\n"
      "s_cbranch_scc1 SP2_%=\n"
      "HD2_%=:\n"
      "s_cmp_lg_u32 s54, 0\n"
      "s_waitcnt vmcnt(12) lgkmcnt(0)\n"
      "s_cbranch_scc1 W2_%=\n"
      "s_waitcnt vmcnt(8)\n"
      "W2_%=:\n"
      "s_sub_u32 s67, s68, s88\n"
      "s_cmp_lt_i32 s67, -8\n"
      "s_cbranch_scc1 GW5_%=\n"
      "GX5_%=:\n"
      "s_barrier\n"
      "v_mfma_f32_16x16x32_bf16 v[24:27], a[108:111], v[128:131], v[24:27]\n"
      "s_memrealtime s[68:69]\n"
      "ds_read_b128 a[64:67], v242 offset:32768\n"
      "ds_read_b128 a[72:75], v243 offset:32768\n"
      "v_mfma_f32_16x16x32_bf16 v[32:35], a[112:115], v[128:131], v[32:35]\n"
      "s_xor_b64 s[56:57], s[56:57], s[32:33]\n"
      "ds_read_b128 a[80:83], v242 offset:33792\n"
      "ds_read_b128 a[88:91], v243 offset:33792\n"
      "v_mfma_f32_16x16x32_bf16 v[36:39], a[112:115], v[132:135], v[36:39]\n"
      "s_add_u32 s61, s61, 1\n"
      "v_exp_f32 v190, v122\n"
      "v_mfma_f32_16x16x32_bf16 a[176:179], a[112:115], v[136:139], a[176:179]\n"
      "s_add_u32 s62, s62, 0x4000\n"
      "v_exp_f32 v191, v123\n"
      "v_mfma_f32_16x16x32_bf16 a[180:183], a[112:115], v[140:143], a[180:183]\n"
      "s_and_b32 s62, s62, 0x1ffff\n"
      "v_cvt_pk_bf16_f32 v156, v188, v189\n"
      "v_cvt_pk_bf16_f32 v157, v190, v191\n"
      "v_mfma_f32_16x16x32_bf16 a[188:191], a[116:119], v[140:143], a[188:191]\n"
      "v_readlane_b32 s60, v219, s61\n"
      "v_exp_f32 v192, v124\n"
      "v_mfma_f32_16x16x32_bf16 a[184:187], a[116:119], v[136:139], a[184:187]\n"
      "v_exp_f32 v193, v125\n"
      "v_mfma_f32_16x16x32_bf16 v[44:47], a[116:119], v[132:135], v[44:47]\n"
      "v_exp_f32 v194, v126\n"
      "v_mfma_f32_16x16x32_bf16 v[40:43], a[116:119], v[128:131], v[40:43]\n"
      "v_exp_f32 v195, v127\n"
      "v_mfma_f32_16x16x32_bf16 v[48:51], a[120:123], v[128:131], v[48:51]\n"
      "v_cvt_pk_bf16_f32 v158, v192, v193\n"
      "v_cvt_pk_bf16_f32 v159, v194, v195\n"
      "v_mfma_f32_16x16x32_bf16 v[52:55], a[120:123], v[132:135], v[52:55]\n"
      "s_waitcnt lgkmcnt(0)\n"
      "v_mfma_f32_16x16x32_bf16 v[184:187], a[140:143], v[156:159], v[184:187]\n"
      "s_sub_u32 s67, s68, s66\n"
      "ds_read_b128 a[68:71], v242 offset:36864\n"
      "ds_read_b128 a[76:79], v243 offset:36864\n"
      "v_mfma_f32_16x16x32_bf16 a[192:195], a[120:123], v[136:139], a[192:195]\n"
      "s_cmp_gt_i32 s67, s70\n"
      "ds_read_b128 a[84:87], v242 offset:37888\n"
      "ds_read_b128 a[92:95], v243 offset:37888\n"
      "v_mfma_f32_16x16x32_bf16 a[196:199], a[120:123], v[140:143], a[196:199]\n"
      "s_cselect_b32 s69, s77, s86\n"
      "v_max3_f32 v196, v184, v185, v186\n"
      "v_max_f32 v196, v196, v187\n"
      "v_mfma_f32_16x16x32_bf16 a[204:207], a[124:127], v[140:143], a[204:207]\n"
      "s_cselect_b32 s70, 60, s70\n"
      "v_cmp_nge_f32 vcc, 0x4b800000, v196\n"
      "v_mfma_f32_16x16x32_bf16 a[200:203], a[124:127], v[136:139], a[200:203]\n"
      "s_cselect_b32 s90, s80, s90\n"
      "v_mfma_f32_16x16x32_bf16 v[60:63], a[124:127], v[132:135], v[60:63]\n"
      "s_add_u32 s66, s66, s69\n"
      "v_mfma_f32_16x16x32_bf16 v[56:59], a[124:127], v[128:131], v[56:59]\n"
      "s_mov_b32 s82, s68\n"
      "s_cbranch_vccnz FS1_%=\n"
      "FR1_%=:\n"
      "v_mfma_f32_16x16x32_bf16 v[96:99], a[64:67], a[0:3], v[160:163]\n"
      "s_add_u32 s65, s68, s90\n"
      "v_exp_f32 v188, v64\n"
      "v_mfma_f32_16x16x32_bf16 v[96:99], a[72:75], a[16:19], v[96:99]\n"
      "s_max_u32 s88, s66, s65\n"
      "v_add_f32 v180, v180, v184\n"
      "v_add_f32 v181, v181, v185\n"
      "v_mfma_f32_16x16x32_bf16 v[96:99], a[80:83], a[32:35], v[96:99]\n"
      "v_exp_f32 v189, v65\n"
      "v_mfma_f32_16x16x32_bf16 v[96:99], a[88:91], a[48:51], v[96:99]\n"
      "v_add_f32 v182, v182, v186\n"
      "v_add_f32 v183, v183, v187\n"
      "v_mfma_f32_16x16x32_bf16 v[104:107], a[64:67], a[4:7], v[164:167]\n"
      "v_exp_f32 v190, v66\n"
      "v_mfma_f32_16x16x32_bf16 v[104:107], a[72:75], a[20:23], v[104:107]\n"
      "v_exp_f32 v191, v67\n"
      "v_mfma_f32_16x16x32_bf16 v[104:107], a[80:83], a[36:39], v[104:107]\n"
      "v_cvt_pk_bf16_f32 v128, v188, v189\n"
      "v_cvt_pk_bf16_f32 v129, v190, v191\n"
      "v_mfma_f32_16x16x32_bf16 v[104:107], a[88:91], a[52:55], v[104:107]\n"
      "v_exp_f32 v192, v68\n"
      "v_mfma_f32_16x16x32_bf16 v[112:115], a[64:67], a[8:11], v[168:171]\n"
      "s_add_u32 s39, s39, s39\n"
      "v_exp_f32 v193, v69\n"
      "v_mfma_f32_16x16x32_bf16 v[112:115], a[72:75], a[24:27], v[112:115]\n"
      "s_memrealtime s[68:69]\n"
      "v_exp_f32 v194, v70\n"
      "v_mfma_f32_16x16x32_bf16 v[112:115], a[80:83], a[40:43], v[112:115]\n"
      "v_exp_f32 v195, v71\n"
      "v_mfma_f32_16x16x32_bf16 v[112:115], a[88:91], a[56:59], v[112:115]\n"
      "s_mov_b32 m0, s62\n"
      "v_cvt_pk_bf16_f32 v130, v192, v193\n"
      "v_cvt_pk_bf16_f32 v131, v194, v195\n"
      "v_mfma_f32_16x16x32_bf16 v[120:123], a[64:67], a[12:15], v[172:175]\n"
      "buffer_load_dwordx4 v228, s[56:59], s60 offen nt lds\n"
      "v_mfma_f32_16x16x32_bf16 v[184:187], a[128:131], v[128:131], 0\n"
      "buffer_load_dwordx4 v229, s[56:59], s60 offen offset:1024 nt lds\n"
      "v_mfma_f32_16x16x32_bf16 v[120:123], a[72:75], a[28:31], v[120:123]\n"
      "buffer_load_dwordx4 v230, s[56:59], s60 offen offset:2048 nt lds\n"
      "v_mfma_f32_16x16x32_bf16 v[120:123], a[80:83], a[44:47], v[120:123]\n"
      "buffer_load_dwordx4 v231, s[56:59], s60 offen offset:3072 nt lds\n"
      "s_cbranch_scc1 SP3_%=\n"
      "HD3_%=:\n"
      "s_cmp_lg_u32 s54, 0\n"
      "s_waitcnt vmcnt(12) lgkmcnt(0)\n"
      "s_cbranch_scc1 W3_%=\n"
      "s_waitcnt vmcnt(8)\n"
      "W3_%=:\n"
      "s_sub_u32 s67, s68, s88\n"
      "s_cmp_lt_i32 s67, -8\n"
      "s_cbranch_scc1 GW6_%=\n"
      "GX6_%=:\n"
      "s_barrier\n"
      "v_mfma_f32_16x16x32_bf16 v[120:123], a[88:91], a[60:63], v[120:123]\n"
      "s_memrealtime s[68:69]\n"
      "ds_read_b64_tr_b16 a[96:97], v244 offset:32768\n"
      "ds_read_b64_tr_b16 a[98:99], v244 offset:36864\n"
      "v_mfma_f32_16x16x32_bf16 v[100:103], a[68:71], a[0:3], v[160:163]\n"
      "s_xor_b64 s[56:57], s[56:57], s[32:33]\n"
      "ds_read_b64_tr_b16 a[100:101], v245 offset:32768\n"
      "ds_read_b64_tr_b16 a[102:103], v245 offset:36864\n"
      "v_mfma_f32_16x16x32_bf16 v[100:103], a[76:79], a[16:19], v[100:103]\n"
      "s_add_u32 s61, s61, 1\n"
      "ds_read_b64_tr_b16 a[104:105], v246 offset:32768\n"
      "ds_read_b64_tr_b16 a[106:107], v246 offset:36864\n"
      "v_mfma_f32_16x16x32_bf16 v[100:103], a[84:87], a[32:35], v[100:103]\n"
      "s_add_u32 s62, s62, 0x4000\n"
      "ds_read_b64_tr_b16 a[108:109], v247 offset:32768\n"
      "ds_read_b64_tr_b16 a[110:111], v247 offset:36864\n"
      "v_mfma_f32_16x16x32_bf16 v[100:103], a[92:95], a[48:51], v[100:103]\n"
      "s_and_b32 s62, s62, 0x1ffff\n"
      "v_exp_f32 v188, v72\n"
      "v_mfma_f32_16x16x32_bf16 v[108:111], a[68:71], a[4:7], v[164:167]\n"
      "v_readlane_b32 s60, v219, s61\n"
      "v_exp_f32 v189, v73\n"
      "v_mfma_f32_16x16x32_bf16 v[108:111], a[76:79], a[20:23], v[108:111]\n"
      "v_xor_b32 v242, 0x10000, v242\n"
      "v_xor_b32 v243, 0x10000, v243\n"
      "v_mfma_f32_16x16x32_bf16 v[108:111], a[84:87], a[36:39], v[108:111]\n"
      "v_exp_f32 v190, v74\n"
      "v_mfma_f32_16x16x32_bf16 v[108:111], a[92:95], a[52:55], v[108:111]\n"
      "v_exp_f32 v191, v75\n"
      "v_mfma_f32_16x16x32_bf16 v[116:119], a[68:71], a[8:11], v[168:171]\n"
      "v_cvt_pk_bf16_f32 v132, v188, v189\n"
      "v_cvt_pk_bf16_f32 v133, v190, v191\n"
      "v_mfma_f32_16x16x32_bf16 v[116:119], a[76:79], a[24:27], v[116:119]\n"
      "v_exp_f32 v192, v76\n"
      "s_waitcnt lgkmcnt(0)\n"
      "v_mfma_f32_16x16x32_bf16 v[116:119], a[84:87], a[40:43], v[116:119]\n"
      "s_sub_u32 s67, s68, s66\n"
      "ds_read_b64_tr_b16 a[112:113], v244 offset:33792\n"
      "ds_read_b64_tr_b16 a[114:115], v244 offset:37888\n"
      "v_mfma_f32_16x16x32_bf16 v[116:119], a[92:95], a[56:59], v[116:119]\n"
      "s_cmp_gt_i32 s67, s70\n"
      "v_exp_f32 v193, v77\n"
      "v_mfma_f32_16x16x32_bf16 v[124:127], a[68:71], a[12:15], v[172:175]\n"
      "s_cselect_b32 s69, s77, s86\n"
      "ds_read_b64_tr_b16 a[116:117], v245 offset:33792\n"
      "v_cvt_pk_bf16_f32 v134, v192, v193\n"
      "v_mfma_f32_16x16x32_bf16 v[124:127], a[76:79], a[28:31], v[124:127]\n"
      "s_cselect_b32 s70, 60, s70\n"
      "v_exp_f32 v194, v78\n"
      "v_mfma_f32_16x16x32_bf16 v[124:127], a[84:87], a[44:47], v[124:127]\n"
      "s_cselect_b32 s90, s80, s90\n"
      "ds_read_b64_tr_b16 a[118:119], v245 offset:37888\n"
      "ds_read_b64_tr_b16 a[120:121], v246 offset:33792\n"
      "v_mfma_f32_16x16x32_bf16 v[124:127], a[92:95], a[60:63], v[124:127]\n"
      "s_add_u32 s66, s66, s69\n"
      "v_exp_f32 v195, v79\n"
      "v_mfma_f32_16x16x32_bf16 v[0:3], a[96:99], v[144:147], v[0:3]\n"
      "s_mov_b32 s82, s68\n"
      "ds_read_b64_tr_b16 a[122:123], v246 offset:37888\n"
      "v_cvt_pk_bf16_f32 v135, v194, v195\n"
      "v_mfma_f32_16x16x32_bf16 v[4:7], a[96:99], v[148:151], v[4:7]\n"
      "s_add_u32 s65, s68, s90\n"
      "v_exp_f32 v188, v80\n"
      "v_mfma_f32_16x16x32_bf16 v[184:187], a[132:135], v[132:135], v[184:187]\n"
      "s_max_u32 s88, s66, s65\n"
      "ds_read_b64_tr_b16 a[124:125], v247 offset:33792\n"
      "ds_read_b64_tr_b16 a[126:127], v247 offset:37888\n"
      "v_mfma_f32_16x16x32_bf16 a[144:147], a[96:99], v[152:155], a[144:147]\n"
      "v_exp_f32 v189, v81\n"
      "v_mfma_f32_16x16x32_bf16 a[148:151], a[96:99], v[156:159], a[148:151]\n"
      "v_exp_f32 v190, v82\n"
      "v_mfma_f32_16x16x32_bf16 a[156:159], a[100:103], v[156:159], a[156:159]\n"
      "v_exp_f32 v191, v83\n"
      "v_mfma_f32_16x16x32_bf16 a[152:155], a[100:103], v[152:155], a[152:155]\n"
      "v_cvt_pk_bf16_f32 v136, v188, v189\n"
      "v_cvt_pk_bf16_f32 v137, v190, v191\n"
      "v_mfma_f32_16x16x32_bf16 v[12:15], a[100:103], v[148:151], v[12:15]\n"
      "v_exp_f32 v192, v84\n"
      "v_mfma_f32_16x16x32_bf16 v[8:11], a[100:103], v[144:147], v[8:11]\n"
      "v_exp_f32 v193, v85\n"
      "v_mfma_f32_16x16x32_bf16 v[16:19], a[104:107], v[144:147], v[16:19]\n"
      "s_add_u32 s38, s38, s38\n"
      "v_exp_f32 v194, v86\n"
      "v_mfma_f32_16x16x32_bf16 v[20:23], a[104:107], v[148:151], v[20:23]\n"
      "s_memrealtime s[68:69]\n"
      "v_exp_f32 v195, v87\n"
      "v_mfma_f32_16x16x32_bf16 a[160:163], a[104:107], v[152:155], a[160:163]\n"
      "v_cvt_pk_bf16_f32 v138, v192, v193\n"
      "v_cvt_pk_bf16_f32 v139, v194, v195\n"
      "v_mfma_f32_16x16x32_bf16 a[164:167], a[104:107], v[156:159], a[164:167]\n"
      "s_mov_b32 m0, s62\n"
      "v_exp_f32 v188, v88\n"
      "v_mfma_f32_16x16x32_bf16 v[184:187], a[136:139], v[136:139], v[184:187]\n"
      "buffer_load_dwordx4 v228, s[56:59], s60 offen nt lds\n"
      "v_mfma_f32_16x16x32_bf16 a[172:175], a[108:111], v[156:159], a[172:175]\n"
      "buffer_load_dwordx4 v229, s[56:59], s60 offen offset:1024 nt lds\n"
      "v_mfma_f32_16x16x32_bf16 a[168:171], a[108:111], v[152:155], a[168:171]\n"
      "buffer_load_dwordx4 v230, s[56:59], s60 offen offset:2048 nt lds\n"
      "v_mfma_f32_16x16x32_bf16 v[28:31], a[108:111], v[148:151], v[28:31]\n"
      "buffer_load_dwordx4 v231, s[56:59], s60 offen offset:3072 nt lds\n"
      "HD0_%=:\n"
      "s_cmp_lg_u32 s54, 0\n"
      "s_waitcnt vmcnt(12) lgkmcnt(0)\n"
      "s_cbranch_scc1 W4_%=\n"
      "s_waitcnt vmcnt(8)\n"
      "W4_%=:\n"
      "s_sub_u32 s67, s68, s88\n"
      "s_cmp_lt_i32 s67, -8\n"
      "s_cbranch_scc1 GW7_%=\n"
      "GX7_%=:\n"
      "s_barrier\n"
      "v_mfma_f32_16x16x32_bf16 v[24:27], a[108:111], v[144:147], v[24:27]\n"
      "s_memrealtime s[68:69]\n"
      "ds_read_b128 a[64:67], v242 offset:0\n"
      "ds_read_b128 a[72:75], v243 offset:0\n"
      "v_mfma_f32_16x16x32_bf16 v[32:35], a[112:115], v[144:147], v[32:35]\n"
      "s_xor_b64 s[56:57], s[56:57], s[32:33]\n"
      "ds_read_b128 a[80:83], v242 offset:1024\n"
      "ds_read_b128 a[88:91], v243 offset:1024\n"
      "v_mfma_f32_16x16x32_bf16 v[36:39], a[112:115], v[148:151], v[36:39]\n"
      "s_add_u32 s61, s61, 1\n"
      "v_exp_f32 v189, v89\n"
      "v_mfma_f32_16x16x32_bf16 a[176:179], a[112:115], v[152:155], a[176:179]\n"
      "s_add_u32 s62, s62, 0x4000\n"
      "v_exp_f32 v190, v90\n"
      "v_mfma_f32_16x16x32_bf16 a[180:183], a[112:115], v[156:159], a[180:183]\n"
      "s_and_b32 s62, s62, 0x1ffff\n"
      "v_exp_f32 v191, v91\n"
      "v_mfma_f32_16x16x32_bf16 a[188:191], a[116:119], v[156:159], a[188:191]\n"
      "v_cvt_pk_bf16_f32 v140, v188, v189\n"
      "v_readlane_b32 s60, v219, s61\n"
      "v_mfma_f32_16x16x32_bf16 a[184:187], a[116:119], v[152:155], a[184:187]\n"
      "v_exp_f32 v192, v92\n"
      "v_mfma_f32_16x16x32_bf16 v[44:47], a[116:119], v[148:151], v[44:47]\n"
      "v_exp_f32 v193, v93\n"
      "v_mfma_f32_16x16x32_bf16 v[40:43], a[116:119], v[144:147], v[40:43]\n"
      "v_cvt_pk_bf16_f32 v141, v190, v191\n"
      "v_cvt_pk_bf16_f32 v142, v192, v193\n"
      "v_mfma_f32_16x16x32_bf16 v[48:51], a[120:123], v[144:147], v[48:51]\n"
      "v_exp_f32 v194, v94\n"
      "v_mfma_f32_16x16x32_bf16 v[52:55], a[120:123], v[148:151], v[52:55]\n"
      "v_exp_f32 v195, v95\n"
      "s_waitcnt lgkmcnt(0)\n"
      "v_mfma_f32_16x16x32_bf16 a[192:195], a[120:123], v[152:155], a[192:195]\n"
      "s_sub_u32 s67, s68, s66\n"
      "v_cvt_pk_bf16_f32 v143, v194, v195\n"
      "v_xor_b32 v244, 0x10000, v244\n"
      "v_mfma_f32_16x16x32_bf16 a[196:199], a[120:123], v[156:159], a[196:199]\n"
      "s_cmp_gt_i32 s67, s70\n"
      "ds_read_b128 a[68:71], v242 offset:4096\n"
      "ds_read_b128 a[76:79], v243 offset:4096\n"
      "v_mfma_f32_16x16x32_bf16 v[184:187], a[140:143], v[140:143], v[184:187]\n"
      "s_cselect_b32 s69, s77, s86\n"
      "ds_read_b128 a[84:87], v242 offset:5120\n"
      "ds_read_b128 a[92:95], v243 offset:5120\n"
      "v_mfma_f32_16x16x32_bf16 a[204:207], a[124:127], v[156:159], a[204:207]\n"
      "s_cselect_b32 s70, 60, s70\n"
      "v_xor_b32 v245, 0x10000, v245\n"
      "v_xor_b32 v246, 0x10000, v246\n"
      "v_mfma_f32_16x16x32_bf16 a[200:203], a[124:127], v[152:155], a[200:203]\n"
      "s_cselect_b32 s90, s80, s90\n"
      "v_xor_b32 v247, 0x10000, v247\n"
      "v_max3_f32 v196, v184, v185, v186\n"
      "v_mfma_f32_16x16x32_bf16 v[60:63], a[124:127], v[148:151], v[60:63]\n"
      "s_add_u32 s66, s66, s69\n"
      "v_max_f32 v196, v196, v187\n"
      "v_cmp_nge_f32 vcc, 0x4b800000, v196\n"
      "v_mfma_f32_16x16x32_bf16 v[56:59], a[124:127], v[144:147], v[56:59]\n"
      "s_mov_b32 s82, s68\n"
      "s_cbranch_vccnz FS2_%=\n"
      "FR2_%=:\n"
      "s_branch LOOP_%=\n"
      "SP1_%=:\n"
      "s_ff1_i32_b32 s64, s39\n"
      "s_lshl1_add_u32 s64, s64, 1\n"
      "s_bitcmp1_b64 s[52:53], s64\n"
      "s_cbranch_scc0 SH1_%=\n"
      "s_swappc_b64 s[84:85], s[84:85]\n"
      "SH1_%=:\n"
      "s_cmp_lt_u32 s64, 63\n"
      "s_cbranch_scc1 HD1_%=\n"
      "s_xor_b64 s[56:57], s[56:57], s[32:33]\n"
      "s_add_u32 s61, s61, 1\n"
      "s_add_u32 s62, s62, 0x4000\n"
      "s_and_b32 s62, s62, 0x1ffff\n"
      "v_readlane_b32 s74, v219, 60\n"
      "v_readlane_b32 s76, v219, 62\n"
      "s_mov_b32 s61, 0\n"
      "s_add_u32 s74, s74, 0x400\n"
      "v_add_u32 v228, s74, v229\n"
      "v_add_u32 v230, s74, v231\n"
      "v_add_u32 v229, s76, v229\n"
      "v_add_u32 v231, s76, v231\n"
      "s_branch HD1_%=\n"
      "SP2_%=:\n"
      "s_cmp_eq_u32 s38, 0\n"
      "s_cbranch_scc1 L62_%=\n"
      "s_ff1_i32_b32 s64, s38\n"
      "s_lshl1_add_u32 s64, s64, 0\n"
      "s_bitcmp1_b64 s[52:53], s64\n"
      "s_cbranch_scc0 SH2_%=\n"
      "s_swappc_b64 s[84:85], s[84:85]\n"
      "SH2_%=:\n"
      "s_branch HD2_%=\n"
      "L62_%=:\n"
      "s_xor_b64 s[56:57], s[56:57], s[32:33]\n"
      "s_mov_b32 s61, 0\n"
      "v_xor_b32 v228, 0x80, v228\n"
      "v_xor_b32 v229, 0x80, v229\n"
      "v_xor_b32 v230, 0x80, v230\n"
      "v_xor_b32 v231, 0x80, v231\n"
      "s_mov_b32 s38, 0x80000000\n"
      "s_branch HD2_%=\n"
      "SP3_%=:\n"
      "s_cmp_eq_u32 s39, 0\n"
      "s_cbranch_scc1 NH_%=\n"
      "s_ff1_i32_b32 s64, s39\n"
      "s_lshl1_add_u32 s64, s64, 1\n"
      "s_bitcmp1_b64 s[52:53], s64\n"
      "s_cbranch_scc0 SH3_%=\n"
      "s_swappc_b64 s[84:85], s[84:85]\n"
      "SH3_%=:\n"
      "s_bitcmp1_b64 s[34:35], s64\n"
      "s_cbranch_scc0 HD3_%=\n"
      "NH_%=:\n"
      "s_cmp_lg_u32 s54, 0\n"
      "s_waitcnt vmcnt(12) lgkmcnt(0)\n"
      "s_cbranch_scc1 W5_%=\n"
      "s_waitcnt vmcnt(8)\n"
      "W5_%=:\n"
      "s_barrier\n"
      "v_mfma_f32_16x16x32_bf16 v[120:123], a[88:91], a[60:63], v[120:123]\n"
      "s_sub_u32 s54, s54, 1\n"
      "ds_read_b64_tr_b16 a[96:97], v244 offset:32768\n"
      "ds_read_b64_tr_b16 a[98:99], v244 offset:36864\n"
      "v_mfma_f32_16x16x32_bf16 v[100:103], a[68:71], a[0:3], v[160:163]\n"
      "ds_read_b64_tr_b16 a[100:101], v245 offset:32768\n"
      "ds_read_b64_tr_b16 a[102:103], v245 offset:36864\n"
      "v_mfma_f32_16x16x32_bf16 v[100:103], a[76:79], a[16:19], v[100:103]\n"
      "ds_read_b64_tr_b16 a[104:105], v246 offset:32768\n"
      "ds_read_b64_tr_b16 a[106:107], v246 offset:36864\n"
      "v_mfma_f32_16x16x32_bf16 v[100:103], a[84:87], a[32:35], v[100:103]\n"
      "ds_read_b64_tr_b16 a[108:109], v247 offset:32768\n"
      "ds_read_b64_tr_b16 a[110:111], v247 offset:36864\n"
      "v_mfma_f32_16x16x32_bf16 v[100:103], a[92:95], a[48:51], v[100:103]\n"
      "v_exp_f32 v188, v72\n"
      "v_mfma_f32_16x16x32_bf16 v[108:111], a[68:71], a[4:7], v[164:167]\n"
      "v_exp_f32 v189, v73\n"
      "v_mfma_f32_16x16x32_bf16 v[108:111], a[76:79], a[20:23], v[108:111]\n"
      "v_xor_b32 v242, 0x10000, v242\n"
      "v_xor_b32 v243, 0x10000, v243\n"
      "v_mfma_f32_16x16x32_bf16 v[108:111], a[84:87], a[36:39], v[108:111]\n"
      "v_exp_f32 v190, v74\n"
      "v_mfma_f32_16x16x32_bf16 v[108:111], a[92:95], a[52:55], v[108:111]\n"
      "v_exp_f32 v191, v75\n"
      "v_mfma_f32_16x16x32_bf16 v[116:119], a[68:71], a[8:11], v[168:171]\n"
      "v_cvt_pk_bf16_f32 v132, v188, v189\n"
      "v_cvt_pk_bf16_f32 v133, v190, v191\n"
      "v_mfma_f32_16x16x32_bf16 v[116:119], a[76:79], a[24:27], v[116:119]\n"
      "v_exp_f32 v192, v76\n"
      "s_waitcnt lgkmcnt(0)\n"
      "v_mfma_f32_16x16x32_bf16 v[116:119], a[84:87], a[40:43], v[116:119]\n"
      "ds_read_b64_tr_b16 a[112:113], v244 offset:33792\n"
      "ds_read_b64_tr_b16 a[114:115], v244 offset:37888\n"
      "v_mfma_f32_16x16x32_bf16 v[116:119], a[92:95], a[56:59], v[116:119]\n"
      "v_exp_f32 v193, v77\n"
      "v_mfma_f32_16x16x32_bf16 v[124:127], a[68:71], a[12:15], v[172:175]\n"
      "ds_read_b64_tr_b16 a[116:117], v245 offset:33792\n"
      "v_cvt_pk_bf16_f32 v134, v192, v193\n"
      "v_mfma_f32_16x16x32_bf16 v[124:127], a[76:79], a[28:31], v[124:127]\n"
      "v_exp_f32 v194, v78\n"
      "v_mfma_f32_16x16x32_bf16 v[124:127], a[84:87], a[44:47], v[124:127]\n"
      "ds_read_b64_tr_b16 a[118:119], v245 offset:37888\n"
      "ds_read_b64_tr_b16 a[120:121], v246 offset:33792\n"
      "v_mfma_f32_16x16x32_bf16 v[124:127], a[92:95], a[60:63], v[124:127]\n"
      "v_exp_f32 v195, v79\n"
      "v_mfma_f32_16x16x32_bf16 v[0:3], a[96:99], v[144:147], v[0:3]\n"
      "ds_read_b64_tr_b16 a[122:123], v246 offset:37888\n"
      "v_cvt_pk_bf16_f32 v135, v194, v195\n"
      "v_mfma_f32_16x16x32_bf16 v[4:7], a[96:99], v[148:151], v[4:7]\n"
      "v_exp_f32 v188, v80\n"
      "v_mfma_f32_16x16x32_bf16 v[184:187], a[132:135], v[132:135], v[184:187]\n"
      "ds_read_b64_tr_b16 a[124:125], v247 offset:33792\n"
      "ds_read_b64_tr_b16 a[126:127], v247 offset:37888\n"
      "v_mfma_f32_16x16x32_bf16 a[144:147], a[96:99], v[152:155], a[144:147]\n"
      "v_exp_f32 v189, v81\n"
      "v_mfma_f32_16x16x32_bf16 a[148:151], a[96:99], v[156:159], a[148:151]\n"
      "v_exp_f32 v190, v82\n"
      "v_mfma_f32_16x16x32_bf16 a[156:159], a[100:103], v[156:159], a[156:159]\n"
      "v_exp_f32 v191, v83\n"
      "v_mfma_f32_16x16x32_bf16 a[152:155], a[100:103], v[152:155], a[152:155]\n"
      "v_cvt_pk_bf16_f32 v136, v188, v189\n"
      "v_cvt_pk_bf16_f32 v137, v190, v191\n"
      "v_mfma_f32_16x16x32_bf16 v[12:15], a[100:103], v[148:151], v[12:15]\n"
      "v_exp_f32 v192, v84\n"
      "v_mfma_f32_16x16x32_bf16 v[8:11], a[100:103], v[144:147], v[8:11]\n"
      "v_exp_f32 v193, v85\n"
      "v_mfma_f32_16x16x32_bf16 v[16:19], a[104:107], v[144:147], v[16:19]\n"
      "s_add_u32 s38, s38, s38\n"
      "v_exp_f32 v194, v86\n"
      "v_mfma_f32_16x16x32_bf16 v[20:23], a[104:107], v[148:151], v[20:23]\n"
      "s_memrealtime s[68:69]\n"
      "v_exp_f32 v195, v87\n"
      "v_mfma_f32_16x16x32_bf16 a[160:163], a[104:107], v[152:155], a[160:163]\n"
      "v_cvt_pk_bf16_f32 v138, v192, v193\n"
      "v_cvt_pk_bf16_f32 v139, v194, v195\n"
      "v_mfma_f32_16x16x32_bf16 a[164:167], a[104:107], v[156:159], a[164:167]\n"
      "v_exp_f32 v188, v88\n"
      "v_mfma_f32_16x16x32_bf16 v[184:187], a[136:139], v[136:139], v[184:187]\n"
      "s_nop 0\n"
      "v_mfma_f32_16x16x32_bf16 a[172:175], a[108:111], v[156:159], a[172:175]\n"
      "s_nop 0\n"
      "v_mfma_f32_16x16x32_bf16 a[168:171], a[108:111], v[152:155], a[168:171]\n"
      "s_nop 0\n"
      "v_mfma_f32_16x16x32_bf16 v[28:31], a[108:111], v[148:151], v[28:31]\n"
      "s_nop 0\n"
      "s_cbranch_scc0 MH_%=\n"
      "END_%=:\n"
      "s_waitcnt lgkmcnt(0)\n"
      "v_mfma_f32_16x16x32_bf16 v[24:27], a[108:111], v[144:147], v[24:27]\n"
      "v_mov_b32 v236, s68\n"
      "v_exp_f32 v189, v89\n"
      "v_mfma_f32_16x16x32_bf16 v[32:35], a[112:115], v[144:147], v[32:35]\n"
      "s_mov_b64 exec, 1\n"
      "global_store_dwordx2 v238, v[236:237], s[46:47] offset:8 sc1\n"
      "s_mov_b64 exec, -1\n"
      "v_mfma_f32_16x16x32_bf16 v[36:39], a[112:115], v[148:151], v[36:39]\n"
      "s_mov_b32 s82, s68\n"
      "v_exp_f32 v190, v90\n"
      "v_mfma_f32_16x16x32_bf16 a[176:179], a[112:115], v[152:155], a[176:179]\n"
      "v_exp_f32 v191, v91\n"
      "v_mfma_f32_16x16x32_bf16 a[180:183], a[112:115], v[156:159], a[180:183]\n"
      "v_cvt_pk_bf16_f32 v140, v188, v189\n"
      "v_cvt_pk_bf16_f32 v141, v190, v191\n"
      "v_mfma_f32_16x16x32_bf16 a[188:191], a[116:119], v[156:159], a[188:191]\n"
      "v_exp_f32 v192, v92\n"
      "s_waitcnt vmcnt(5)\n"
      "s_barrier\n"
      "v_mfma_f32_16x16x32_bf16 a[184:187], a[116:119], v[152:155], a[184:187]\n"
      "ds_read_b64_tr_b16 a[80:81], v224 offset:0\n"
      "ds_read_b64_tr_b16 a[82:83], v224 offset:4096\n"
      "v_mfma_f32_16x16x32_bf16 v[44:47], a[116:119], v[148:151], v[44:47]\n"
      "ds_read_b64_tr_b16 a[84:85], v225 offset:0\n"
      "ds_read_b64_tr_b16 a[86:87], v225 offset:4096\n"
      "v_mfma_f32_16x16x32_bf16 v[40:43], a[116:119], v[144:147], v[40:43]\n"
      "s_mov_b32 s74, 0xffff0000\n"
      "ds_read_b64_tr_b16 a[88:89], v226 offset:0\n"
      "ds_read_b64_tr_b16 a[90:91], v226 offset:4096\n"
      "v_mfma_f32_16x16x32_bf16 v[48:51], a[120:123], v[144:147], v[48:51]\n"
      "s_mov_b32 s75, 0xffff0000\n"
      "ds_read_b64_tr_b16 a[92:93], v227 offset:0\n"
      "ds_read_b64_tr_b16 a[94:95], v227 offset:4096\n"
      "v_mfma_f32_16x16x32_bf16 v[52:55], a[120:123], v[148:151], v[52:55]\n"
      "s_mov_b32 s94, 0\n"
      "v_exp_f32 v193, v93\n"
      "v_mfma_f32_16x16x32_bf16 a[192:195], a[120:123], v[152:155], a[192:195]\n"
      "s_mov_b32 s95, -1\n"
      "ds_read_b64_tr_b16 a[16:17], v224 offset:1024\n"
      "v_cvt_pk_bf16_f32 v142, v192, v193\n"
      "v_mfma_f32_16x16x32_bf16 a[196:199], a[120:123], v[156:159], a[196:199]\n"
      "v_exp_f32 v194, v94\n"
      "v_mfma_f32_16x16x32_bf16 a[204:207], a[124:127], v[156:159], a[204:207]\n"
      "ds_read_b64_tr_b16 a[18:19], v224 offset:5120\n"
      "ds_read_b64_tr_b16 a[20:21], v225 offset:1024\n"
      "v_mfma_f32_16x16x32_bf16 a[200:203], a[124:127], v[152:155], a[200:203]\n"
      "v_exp_f32 v195, v95\n"
      "v_mfma_f32_16x16x32_bf16 v[60:63], a[124:127], v[148:151], v[60:63]\n"
      "ds_read_b64_tr_b16 a[22:23], v225 offset:5120\n"
      "v_cvt_pk_bf16_f32 v143, v194, v195\n"
      "v_mfma_f32_16x16x32_bf16 v[56:59], a[124:127], v[144:147], v[56:59]\n"
      "ds_read_b64_tr_b16 a[24:25], v226 offset:1024\n"
      "ds_read_b64_tr_b16 a[26:27], v226 offset:5120\n"
      "v_mfma_f32_16x16x32_bf16 v[184:187], a[140:143], v[140:143], v[184:187]\n"
      "ds_read_b64_tr_b16 a[28:29], v227 offset:1024\n"
      "ds_read_b64_tr_b16 a[30:31], v227 offset:5120\n"
      "ds_read_b64_tr_b16 a[0:1], v220 offset:1024\n"
      "ds_read_b64_tr_b16 a[2:3], v220 offset:5120\n"
      "ds_read_b64_tr_b16 a[4:5], v221 offset:1024\n"
      "ds_read_b64_tr_b16 a[6:7], v221 offset:5120\n"
      "ds_read_b64_tr_b16 a[8:9], v222 offset:1024\n"
      "ds_read_b64_tr_b16 a[10:11], v222 offset:5120\n"
      "ds_read_b64_tr_b16 a[12:13], v223 offset:1024\n"
      "ds_read_b64_tr_b16 a[14:15], v223 offset:5120\n"
      "v_max3_f32 v196, v184, v185, v186\n"
      "v_max_f32 v196, v196, v187\n"
      "v_cmp_nge_f32 vcc, 0x4b800000, v196\n"
      "s_cbranch_vccnz FS3_%=\n"
      "FR3_%=:\n"
      "s_waitcnt lgkmcnt(15)\n"
      "v_mfma_f32_16x16x32_bf16 a[176:179], a[80:83], v[136:139], a[176:179]\n"
      "v_add_f32 v180, v180, v184\n"
      "v_add_f32 v181, v181, v185\n"
      "v_mfma_f32_16x16x32_bf16 a[180:183], a[80:83], v[140:143], a[180:183]\n"
      "v_add_f32 v182, v182, v186\n"
      "v_add_f32 v183, v183, v187\n"
      "v_mfma_f32_16x16x32_bf16 a[184:187], a[84:87], v[136:139], a[184:187]\n"
      "v_exp_f32 v188, v112\n"
      "v_mfma_f32_16x16x32_bf16 a[188:191], a[84:87], v[140:143], a[188:191]\n"
      "ds_write_b128 v232, a[176:179] offset:0\n"
      "v_exp_f32 v189, v113\n"
      "v_mfma_f32_16x16x32_bf16 a[192:195], a[88:91], v[136:139], a[192:195]\n"
      "v_exp_f32 v190, v114\n"
      "v_mfma_f32_16x16x32_bf16 a[196:199], a[88:91], v[140:143], a[196:199]\n"
      "v_exp_f32 v191, v115\n"
      "v_mfma_f32_16x16x32_bf16 a[200:203], a[92:95], v[136:139], a[200:203]\n"
      "ds_write_b128 v232, a[180:183] offset:1024\n"
      "v_exp_f32 v192, v116\n"
      "v_mfma_f32_16x16x32_bf16 a[204:207], a[92:95], v[140:143], a[204:207]\n"
      "v_exp_f32 v193, v117\n"
      "v_exp_f32 v194, v118\n"
      "v_cvt_pk_bf16_f32 v152, v188, v189\n"
      "v_exp_f32 v195, v119\n"
      "v_cvt_pk_bf16_f32 v153, v190, v191\n"
      "ds_write_b128 v232, a[184:187] offset:2048\n"
      "v_exp_f32 v188, v120\n"
      "v_cvt_pk_bf16_f32 v154, v192, v193\n"
      "v_mfma_f32_16x16x32_bf16 v[32:35], a[80:83], v[128:131], v[32:35]\n"
      "v_exp_f32 v189, v121\n"
      "v_cvt_pk_bf16_f32 v155, v194, v195\n"
      "v_exp_f32 v190, v122\n"
      "v_cvt_pk_bf16_f32 v156, v188, v189\n"
      "v_mfma_f32_16x16x32_bf16 v[184:187], a[136:139], v[152:155], 0\n"
      "ds_write_b128 v234, v[152:155] offset:0\n"
      "v_exp_f32 v191, v123\n"
      "v_mfma_f32_16x16x32_bf16 v[36:39], a[80:83], v[132:135], v[36:39]\n"
      "v_exp_f32 v192, v124\n"
      "v_cvt_pk_bf16_f32 v157, v190, v191\n"
      "v_exp_f32 v193, v125\n"
      "v_exp_f32 v194, v126\n"
      "v_cvt_pk_bf16_f32 v158, v192, v193\n"
      "ds_write_b128 v232, a[188:191] offset:3072\n"
      "v_mfma_f32_16x16x32_bf16 v[40:43], a[84:87], v[128:131], v[40:43]\n"
      "v_exp_f32 v195, v127\n"
      "v_exp_f32 v188, v96\n"
      "v_cvt_pk_bf16_f32 v159, v194, v195\n"
      "v_exp_f32 v189, v97\n"
      "v_mfma_f32_16x16x32_bf16 v[44:47], a[84:87], v[132:135], v[44:47]\n"
      "v_exp_f32 v190, v98\n"
      "ds_write_b128 v234, v[156:159] offset:1024\n"
      "v_cvt_pk_bf16_f32 v144, v188, v189\n"
      "v_mfma_f32_16x16x32_bf16 v[184:187], a[140:143], v[156:159], v[184:187]\n"
      "v_exp_f32 v191, v99\n"
      "v_exp_f32 v192, v100\n"
      "v_cvt_pk_bf16_f32 v145, v190, v191\n"
      "v_mfma_f32_16x16x32_bf16 v[48:51], a[88:91], v[128:131], v[48:51]\n"
      "v_exp_f32 v193, v101\n"
      "ds_write_b128 v232, a[192:195] offset:4096\n"
      "v_exp_f32 v194, v102\n"
      "v_cvt_pk_bf16_f32 v146, v192, v193\n"
      "v_exp_f32 v195, v103\n"
      "v_mfma_f32_16x16x32_bf16 v[52:55], a[88:91], v[132:135], v[52:55]\n"
      "v_exp_f32 v188, v104\n"
      "v_cvt_pk_bf16_f32 v147, v194, v195\n"
      "v_exp_f32 v189, v105\n"
      "ds_write_b128 v232, a[196:199] offset:5120\n"
      "v_exp_f32 v190, v106\n"
      "v_cvt_pk_bf16_f32 v148, v188, v189\n"
      "v_mfma_f32_16x16x32_bf16 v[184:187], a[128:131], v[144:147], v[184:187]\n"
      "v_exp_f32 v191, v107\n"
      "v_exp_f32 v192, v108\n"
      "v_cvt_pk_bf16_f32 v149, v190, v191\n"
      "v_exp_f32 v193, v109\n"
      "ds_write_b128 v232, a[200:203] offset:6144\n"
      "v_mfma_f32_16x16x32_bf16 v[56:59], a[92:95], v[128:131], v[56:59]\n"
      "v_exp_f32 v194, v110\n"
      "v_cvt_pk_bf16_f32 v150, v192, v193\n"
      "v_exp_f32 v195, v111\n"
      "v_cndmask_b32 v229, v180, v181, s[74:75]\n"
      "v_cvt_pk_bf16_f32 v151, v194, v195\n"
      "v_mfma_f32_16x16x32_bf16 v[60:63], a[92:95], v[132:135], v[60:63]\n"
      "v_cndmask_b32 v200, v182, v183, s[74:75]\n"
      "ds_write_b128 v232, a[204:207] offset:7168\n"
      "v_cndmask_b32 v228, v176, v177, s[74:75]\n"
      "v_mfma_f32_16x16x32_bf16 v[184:187], a[132:135], v[148:151], v[184:187]\n"
      "s_nop 7\n"
      "v_max3_f32 v196, v184, v185, v186\n"
      "v_max_f32 v196, v196, v187\n"
      "v_cmp_nge_f32 vcc, 0x4b800000, v196\n"
      "s_cbranch_vccnz FS8_%=\n"
      "FR8_%=:\n"
      "v_cndmask_b32 v202, v186, v187, s[74:75]\n"
      "v_cndmask_b32 v201, v184, v185, s[74:75]\n"
      "v_add_f32 v200, v200, v202\n"
      "ds_write_b32 v234, v200 offset:4096\n"
      "v_add_f32 v229, v229, v201\n"
      "s_waitcnt lgkmcnt(8)\n"
      "v_mfma_f32_16x16x32_bf16 v[32:35], a[16:19], v[144:147], v[32:35]\n"
      "v_mfma_f32_16x16x32_bf16 v[36:39], a[16:19], v[148:151], v[36:39]\n"
      "v_mfma_f32_16x16x32_bf16 v[40:43], a[20:23], v[144:147], v[40:43]\n"
      "s_waitcnt lgkmcnt(0)\n"
      "s_barrier\n"
      "v_mfma_f32_16x16x32_bf16 v[44:47], a[20:23], v[148:151], v[44:47]\n"
      "ds_read_b32 v208, v209\n"
      "ds_read_b128 v[160:163], v235 offset:0\n"
      "v_mfma_f32_16x16x32_bf16 v[48:51], a[24:27], v[144:147], v[48:51]\n"
      "ds_read_b128 v[164:167], v235 offset:1024\n"
      "ds_read_b128 v[64:67], v233 offset:0\n"
      "v_mfma_f32_16x16x32_bf16 v[52:55], a[24:27], v[148:151], v[52:55]\n"
      "ds_read_b128 v[68:71], v233 offset:1024\n"
      "ds_read_b128 v[72:75], v233 offset:2048\n"
      "ds_read_b128 v[76:79], v233 offset:3072\n"
      "v_mfma_f32_16x16x32_bf16 v[56:59], a[28:31], v[144:147], v[56:59]\n"
      "ds_read_b128 v[80:83], v233 offset:4096\n"
      "ds_read_b128 v[84:87], v233 offset:5120\n"
      "v_mfma_f32_16x16x32_bf16 v[60:63], a[28:31], v[148:151], v[60:63]\n"
      "ds_read_b128 v[88:91], v233 offset:6144\n"
      "ds_read_b128 v[92:95], v233 offset:7168\n"
      "ds_read_b32 v204, v235 offset:4096\n"
      "s_waitcnt lgkmcnt(11)\n"
      "v_cmp_ne_u32 vcc, 0, v208\n"
      "s_cbranch_vccnz SLOW_%=\n"
      "AGAIN_%=:\n"
      "s_waitcnt lgkmcnt(9)\n"
      "v_mfma_f32_16x16x32_bf16 v[32:35], a[0:3], v[160:163], v[32:35]\n"
      "v_mfma_f32_16x16x32_bf16 v[36:39], a[0:3], v[164:167], v[36:39]\n"
      "v_mfma_f32_16x16x32_bf16 v[40:43], a[4:7], v[160:163], v[40:43]\n"
      "v_mfma_f32_16x16x32_bf16 v[44:47], a[4:7], v[164:167], v[44:47]\n"
      "v_mfma_f32_16x16x32_bf16 v[48:51], a[8:11], v[160:163], v[48:51]\n"
      "v_mfma_f32_16x16x32_bf16 v[52:55], a[8:11], v[164:167], v[52:55]\n"
      "v_mfma_f32_16x16x32_bf16 v[56:59], a[12:15], v[160:163], v[56:59]\n"
      "v_mfma_f32_16x16x32_bf16 v[60:63], a[12:15], v[164:167], v[60:63]\n"
      "global_load_dwordx2 v[206:207], v239, s[46:47] offset:8 sc1\n"
      "s_waitcnt lgkmcnt(7)\n"
      "v_pk_add_f32 v[32:33], v[32:33], v[64:65]\n"
      "v_pk_add_f32 v[34:35], v[34:35], v[66:67]\n"
      "v_pk_add_f32 v[36:37], v[36:37], v[68:69]\n"
      "v_pk_add_f32 v[38:39], v[38:39], v[70:71]\n"
      "v_cvt_pk_bf16_f32 v212, v32, v33\n"
      "v_cvt_pk_bf16_f32 v213, v34, v35\n"
      "v_cvt_pk_bf16_f32 v214, v36, v37\n"
      "v_cvt_pk_bf16_f32 v215, v38, v39\n"
      "buffer_store_dwordx4 v[212:215], v241, s[24:27], s78 offen offset:0 sc1\n"
      "s_waitcnt lgkmcnt(5)\n"
      "v_pk_add_f32 v[40:41], v[40:41], v[72:73]\n"
      "v_pk_add_f32 v[42:43], v[42:43], v[74:75]\n"
      "v_pk_add_f32 v[44:45], v[44:45], v[76:77]\n"
      "v_pk_add_f32 v[46:47], v[46:47], v[78:79]\n"
      "v_cvt_pk_bf16_f32 v216, v40, v41\n"
      "v_cvt_pk_bf16_f32 v217, v42, v43\n"
      "v_cvt_pk_bf16_f32 v218, v44, v45\n"
      "v_cvt_pk_bf16_f32 v219, v46, v47\n"
      "buffer_store_dwordx4 v[216:219], v241, s[24:27], s78 offen offset:1024 sc1\n"
      "s_waitcnt lgkmcnt(3)\n"
      "v_pk_add_f32 v[48:49], v[48:49], v[80:81]\n"
      "v_pk_add_f32 v[50:51], v[50:51], v[82:83]\n"
      "v_pk_add_f32 v[52:53], v[52:53], v[84:85]\n"
      "v_pk_add_f32 v[54:55], v[54:55], v[86:87]\n"
      "v_cvt_pk_bf16_f32 v220, v48, v49\n"
      "v_cvt_pk_bf16_f32 v221, v50, v51\n"
      "v_cvt_pk_bf16_f32 v222, v52, v53\n"
      "v_cvt_pk_bf16_f32 v223, v54, v55\n"
      "buffer_store_dwordx4 v[220:223], v241, s[24:27], s78 offen offset:2048 sc1\n"
      "s_waitcnt lgkmcnt(0)\n"
      "v_pk_add_f32 v[56:57], v[56:57], v[88:89]\n"
      "v_pk_add_f32 v[58:59], v[58:59], v[90:91]\n"
      "v_pk_add_f32 v[60:61], v[60:61], v[92:93]\n"
      "v_pk_add_f32 v[62:63], v[62:63], v[94:95]\n"
      "v_cvt_pk_bf16_f32 v224, v56, v57\n"
      "v_cvt_pk_bf16_f32 v225, v58, v59\n"
      "v_cvt_pk_bf16_f32 v226, v60, v61\n"
      "v_cvt_pk_bf16_f32 v227, v62, v63\n"
      "buffer_store_dwordx4 v[224:227], v241, s[24:27], s78 offen offset:3072 sc1\n"
      "v_add_f32 v229, v229, v204\n"
      "v_cndmask_b32 v230, v228, v229, s[94:95]\n"
      "v_add_u32 v231, s40, v230\n"
      "v_add3_u32 v231, v231, v212, v213\n"
      "v_add3_u32 v231, v231, v214, v215\n"
      "v_add3_u32 v231, v231, v216, v217\n"
      "v_add3_u32 v231, v231, v218, v219\n"
      "v_add3_u32 v231, v231, v220, v221\n"
      "v_add3_u32 v231, v231, v222, v223\n"
      "v_add3_u32 v231, v231, v224, v225\n"
      "v_add3_u32 v231, v231, v226, v227\n"
      "buffer_store_dwordx2 v[230:231], v240, s[24:27], s78 offen sc1\n"
      "v_xor_b32 v244, 0x10000, v244\n"
      "v_xor_b32 v245, 0x10000, v245\n"
      "v_xor_b32 v246, 0x10000, v246\n"
      "v_xor_b32 v247, 0x10000, v247\n"
      "s_waitcnt vmcnt(7)\n"
      "s_barrier\n"
      "ds_read_b64_tr_b16 a[64:65], v244 offset:16384\n"
      "ds_read_b64_tr_b16 a[66:67], v244 offset:20480\n"
      "ds_read_b64_tr_b16 a[68:69], v245 offset:16384\n"
      "ds_read_b64_tr_b16 a[70:71], v245 offset:20480\n"
      "ds_read_b64_tr_b16 a[72:73], v246 offset:16384\n"
      "ds_read_b64_tr_b16 a[74:75], v246 offset:20480\n"
      "ds_read_b64_tr_b16 a[76:77], v247 offset:16384\n"
      "ds_read_b64_tr_b16 a[78:79], v247 offset:20480\n"
      "s_waitcnt lgkmcnt(4)\n"
      "v_mfma_f32_16x16x32_bf16 a[144:147], a[64:67], v[136:139], a[144:147]\n"
      "ds_read_b64_tr_b16 a[96:97], v244 offset:17408\n"
      "ds_read_b64_tr_b16 a[98:99], v244 offset:21504\n"
      "v_mfma_f32_16x16x32_bf16 a[148:151], a[64:67], v[140:143], a[148:151]\n"
      "ds_read_b64_tr_b16 a[100:101], v245 offset:17408\n"
      "ds_read_b64_tr_b16 a[102:103], v245 offset:21504\n"
      "v_mfma_f32_16x16x32_bf16 a[152:155], a[68:71], v[136:139], a[152:155]\n"
      "ds_read_b64_tr_b16 a[104:105], v246 offset:17408\n"
      "ds_read_b64_tr_b16 a[106:107], v246 offset:21504\n"
      "v_mfma_f32_16x16x32_bf16 a[156:159], a[68:71], v[140:143], a[156:159]\n"
      "ds_read_b64_tr_b16 a[108:109], v247 offset:17408\n"
      "ds_read_b64_tr_b16 a[110:111], v247 offset:21504\n"
      "s_waitcnt lgkmcnt(8)\n"
      "v_mfma_f32_16x16x32_bf16 a[160:163], a[72:75], v[136:139], a[160:163]\n"
      "s_sub_u32 s76, 18432, s78\n"
      "v_mfma_f32_16x16x32_bf16 a[164:167], a[72:75], v[140:143], a[164:167]\n"
      "s_mov_b32 s63, 0x40000\n"
      "v_mfma_f32_16x16x32_bf16 a[168:171], a[76:79], v[136:139], a[168:171]\n"
      "v_mov_b32 v198, 0\n"
      "v_mfma_f32_16x16x32_bf16 a[172:175], a[76:79], v[140:143], a[172:175]\n"
      "v_and_b32 v197, 0xf0, v241\n"
      "s_waitcnt lgkmcnt(0)\n"
      "v_mfma_f32_16x16x32_bf16 a[144:147], a[96:99], v[152:155], a[144:147]\n"
      "v_lshrrev_b32 v196, 3, v241\n"
      "v_mfma_f32_16x16x32_bf16 a[148:151], a[96:99], v[156:159], a[148:151]\n"
      "v_and_b32 v196, 32, v196\n"
      "v_mfma_f32_16x16x32_bf16 a[152:155], a[100:103], v[152:155], a[152:155]\n"
      "v_lshl_or_b32 v196, v197, 4, v196\n"
      "v_mfma_f32_16x16x32_bf16 a[156:159], a[100:103], v[156:159], a[156:159]\n"
      "v_lshrrev_b32 v199, 5, v241\n"
      "v_mfma_f32_16x16x32_bf16 a[160:163], a[104:107], v[152:155], a[160:163]\n"
      "v_and_or_b32 v196, v199, 16, v196\n"
      "v_mfma_f32_16x16x32_bf16 a[164:167], a[104:107], v[156:159], a[164:167]\n"
      "v_mov_b32 v191, s40\n"
      "v_mfma_f32_16x16x32_bf16 a[168:171], a[108:111], v[152:155], a[168:171]\n"
      "v_mfma_f32_16x16x32_bf16 a[172:175], a[108:111], v[156:159], a[172:175]\n"
      "s_waitcnt vmcnt(0)\n"
      "v_subrev_u32 v205, s82, v206\n"
      "v_cmp_ne_u32 vcc, s40, v207\n"
      "s_cbranch_vccnz LAG_%=\n"
      "v_cmp_lt_i32 vcc, 16, v205\n"
      "s_cbranch_vccnz LAG_%=\n"
      "READ_%=:\n"
      "buffer_load_dwordx2 v[160:161], v240, s[24:27], s76 offen sc1\n"
      "buffer_load_dwordx4 v[96:99], v241, s[24:27], s76 offen offset:0 sc1\n"
      "buffer_load_dwordx4 v[100:103], v241, s[24:27], s76 offen offset:1024 sc1\n"
      "buffer_load_dwordx4 v[104:107], v241, s[24:27], s76 offen offset:2048 sc1\n"
      "buffer_load_dwordx4 v[108:111], v241, s[24:27], s76 offen offset:3072 sc1\n"
      "v_mfma_f32_16x16x32_bf16 v[0:3], a[64:67], v[128:131], v[0:3]\n"
      "v_mfma_f32_16x16x32_bf16 v[4:7], a[64:67], v[132:135], v[4:7]\n"
      "ds_write_b128 v232, a[144:147] offset:32768\n"
      "v_mfma_f32_16x16x32_bf16 v[8:11], a[68:71], v[128:131], v[8:11]\n"
      "v_mfma_f32_16x16x32_bf16 v[12:15], a[68:71], v[132:135], v[12:15]\n"
      "ds_write_b128 v232, a[148:151] offset:33792\n"
      "v_mfma_f32_16x16x32_bf16 v[16:19], a[72:75], v[128:131], v[16:19]\n"
      "v_mfma_f32_16x16x32_bf16 v[20:23], a[72:75], v[132:135], v[20:23]\n"
      "ds_write_b128 v232, a[152:155] offset:34816\n"
      "v_mfma_f32_16x16x32_bf16 v[24:27], a[76:79], v[128:131], v[24:27]\n"
      "v_mfma_f32_16x16x32_bf16 v[28:31], a[76:79], v[132:135], v[28:31]\n"
      "ds_write_b128 v232, a[156:159] offset:35840\n"
      "v_mfma_f32_16x16x32_bf16 v[0:3], a[96:99], v[144:147], v[0:3]\n"
      "v_mfma_f32_16x16x32_bf16 v[4:7], a[96:99], v[148:151], v[4:7]\n"
      "ds_write_b128 v232, a[160:163] offset:36864\n"
      "v_mfma_f32_16x16x32_bf16 v[8:11], a[100:103], v[144:147], v[8:11]\n"
      "v_mfma_f32_16x16x32_bf16 v[12:15], a[100:103], v[148:151], v[12:15]\n"
      "ds_write_b128 v232, a[164:167] offset:37888\n"
      "v_mfma_f32_16x16x32_bf16 v[16:19], a[104:107], v[144:147], v[16:19]\n"
      "v_mfma_f32_16x16x32_bf16 v[20:23], a[104:107], v[148:151], v[20:23]\n"
      "ds_write_b128 v232, a[168:171] offset:38912\n"
      "v_mfma_f32_16x16x32_bf16 v[24:27], a[108:111], v[144:147], v[24:27]\n"
      "v_mfma_f32_16x16x32_bf16 v[28:31], a[108:111], v[148:151], v[28:31]\n"
      "ds_write_b128 v232, a[172:175] offset:39936\n"
      "s_waitcnt lgkmcnt(0)\n"
      "s_barrier\n"
      "ds_read_b128 v[64:67], v233 offset:32768\n"
      "ds_read_b128 v[68:71], v233 offset:33792\n"
      "ds_read_b128 v[72:75], v233 offset:34816\n"
      "ds_read_b128 v[76:79], v233 offset:35840\n"
      "ds_read_b128 v[80:83], v233 offset:36864\n"
      "ds_read_b128 v[84:87], v233 offset:37888\n"
      "ds_read_b128 v[88:91], v233 offset:38912\n"
      "ds_read_b128 v[92:95], v233 offset:39936\n"
      "s_waitcnt lgkmcnt(6)\n"
      "v_pk_add_f32 v[0:1], v[0:1], v[64:65]\n"
      "v_pk_add_f32 v[2:3], v[2:3], v[66:67]\n"
      "v_pk_add_f32 v[4:5], v[4:5], v[68:69]\n"
      "v_pk_add_f32 v[6:7], v[6:7], v[70:71]\n"
      "s_waitcnt lgkmcnt(4)\n"
      "v_pk_add_f32 v[8:9], v[8:9], v[72:73]\n"
      "v_pk_add_f32 v[10:11], v[10:11], v[74:75]\n"
      "v_pk_add_f32 v[12:13], v[12:13], v[76:77]\n"
      "v_pk_add_f32 v[14:15], v[14:15], v[78:79]\n"
      "s_waitcnt lgkmcnt(2)\n"
      "v_pk_add_f32 v[16:17], v[16:17], v[80:81]\n"
      "v_pk_add_f32 v[18:19], v[18:19], v[82:83]\n"
      "v_pk_add_f32 v[20:21], v[20:21], v[84:85]\n"
      "v_pk_add_f32 v[22:23], v[22:23], v[86:87]\n"
      "s_waitcnt lgkmcnt(0)\n"
      "v_pk_add_f32 v[24:25], v[24:25], v[88:89]\n"
      "v_pk_add_f32 v[26:27], v[26:27], v[90:91]\n"
      "v_pk_add_f32 v[28:29], v[28:29], v[92:93]\n"
      "v_pk_add_f32 v[30:31], v[30:31], v[94:95]\n"
      "TRY_%=:\n"
      "s_waitcnt vmcnt(4)\n"
      "v_mov_b32 v162, v160\n"
      "v_add_u32 v231, s40, v160\n"
      "s_nop 0\n"
      "v_permlane32_swap_b32 v160, v162\n"
      "v_sub_f32 v168, v160, v228\n"
      "v_min_f32 v172, 0, -v168\n"
      "v_min_f32 v168, 0, v168\n"
      "v_exp_f32 v168, v168\n"
      "v_exp_f32 v172, v172\n"
      "v_mul_f32 v163, v229, v168\n"
      "v_fmac_f32 v163, v162, v172\n"
      "v_rcp_f32 v163, v163\n"
      "s_nop 0\n"
      "v_mul_f32 v168, v168, v163\n"
      "v_mul_f32 v172, v172, v163\n"
      "v_mov_b32 v169, v168\n"
      "v_mov_b32 v173, v172\n"
      "s_nop 0\n"
      "v_permlane16_swap_b32 v168, v169\n"
      "v_permlane16_swap_b32 v172, v173\n"
      "v_pk_mul_f32 v[32:33], v[0:1], v[168:169] op_sel:[0,0] op_sel_hi:[1,0]\n"
      "v_pk_mul_f32 v[34:35], v[2:3], v[168:169] op_sel:[0,0] op_sel_hi:[1,0]\n"
      "v_pk_mul_f32 v[36:37], v[4:5], v[168:169] op_sel:[0,1] op_sel_hi:[1,1]\n"
      "v_pk_mul_f32 v[38:39], v[6:7], v[168:169] op_sel:[0,1] op_sel_hi:[1,1]\n"
      "v_pk_mul_f32 v[40:41], v[8:9], v[168:169] op_sel:[0,0] op_sel_hi:[1,0]\n"
      "v_pk_mul_f32 v[42:43], v[10:11], v[168:169] op_sel:[0,0] op_sel_hi:[1,0]\n"
      "v_pk_mul_f32 v[44:45], v[12:13], v[168:169] op_sel:[0,1] op_sel_hi:[1,1]\n"
      "v_pk_mul_f32 v[46:47], v[14:15], v[168:169] op_sel:[0,1] op_sel_hi:[1,1]\n"
      "v_pk_mul_f32 v[48:49], v[16:17], v[168:169] op_sel:[0,0] op_sel_hi:[1,0]\n"
      "v_pk_mul_f32 v[50:51], v[18:19], v[168:169] op_sel:[0,0] op_sel_hi:[1,0]\n"
      "v_pk_mul_f32 v[52:53], v[20:21], v[168:169] op_sel:[0,1] op_sel_hi:[1,1]\n"
      "v_pk_mul_f32 v[54:55], v[22:23], v[168:169] op_sel:[0,1] op_sel_hi:[1,1]\n"
      "v_pk_mul_f32 v[56:57], v[24:25], v[168:169] op_sel:[0,0] op_sel_hi:[1,0]\n"
      "v_pk_mul_f32 v[58:59], v[26:27], v[168:169] op_sel:[0,0] op_sel_hi:[1,0]\n"
      "v_pk_mul_f32 v[60:61], v[28:29], v[168:169] op_sel:[0,1] op_sel_hi:[1,1]\n"
      "v_pk_mul_f32 v[62:63], v[30:31], v[168:169] op_sel:[0,1] op_sel_hi:[1,1]\n"
      "s_waitcnt vmcnt(3)\n"
      "v_add3_u32 v231, v231, v96, v97\n"
      "v_add3_u32 v231, v231, v98, v99\n"
      "s_waitcnt vmcnt(2)\n"
      "v_add3_u32 v231, v231, v100, v101\n"
      "v_add3_u32 v231, v231, v102, v103\n"
      "s_waitcnt vmcnt(1)\n"
      "v_add3_u32 v231, v231, v104, v105\n"
      "v_add3_u32 v231, v231, v106, v107\n"
      "s_waitcnt vmcnt(0)\n"
      "v_add3_u32 v231, v231, v108, v109\n"
      "v_add3_u32 v231, v231, v110, v111\n"
      "v_cmp_ne_u32 vcc, v231, v161\n"
      "s_cbranch_vccnz RETRY_%=\n"
      "GOT_%=:\n"
      "v_lshlrev_b32 v0, 16, v96\n"
      "v_and_b32 v1, 0xffff0000, v96\n"
      "v_lshlrev_b32 v2, 16, v97\n"
      "v_and_b32 v3, 0xffff0000, v97\n"
      "v_lshlrev_b32 v8, 16, v100\n"
      "v_and_b32 v9, 0xffff0000, v100\n"
      "v_lshlrev_b32 v10, 16, v101\n"
      "v_and_b32 v11, 0xffff0000, v101\n"
      "v_pk_fma_f32 v[32:33], v[0:1], v[172:173], v[32:33] op_sel:[0,0,0] op_sel_hi:[1,0,1]\n"
      "v_pk_fma_f32 v[34:35], v[2:3], v[172:173], v[34:35] op_sel:[0,0,0] op_sel_hi:[1,0,1]\n"
      "v_pk_fma_f32 v[40:41], v[8:9], v[172:173], v[40:41] op_sel:[0,0,0] op_sel_hi:[1,0,1]\n"
      "v_pk_fma_f32 v[42:43], v[10:11], v[172:173], v[42:43] op_sel:[0,0,0] op_sel_hi:[1,0,1]\n"
      "v_cvt_pk_bf16_f32 v112, v32, v33\n"
      "v_cvt_pk_bf16_f32 v114, v40, v41\n"
      "v_cvt_pk_bf16_f32 v113, v34, v35\n"
      "v_cvt_pk_bf16_f32 v115, v42, v43\n"
      "v_permlane16_swap_b32 v112, v114\n"
      "s_nop 0\n"
      "v_permlane16_swap_b32 v113, v115\n"
      "global_store_dwordx4 v196, v[112:115], s[30:31] offset:-4096 sc1 nt\n"
      "v_lshlrev_b32 v16, 16, v104\n"
      "v_and_b32 v17, 0xffff0000, v104\n"
      "v_lshlrev_b32 v18, 16, v105\n"
      "v_and_b32 v19, 0xffff0000, v105\n"
      "v_lshlrev_b32 v24, 16, v108\n"
      "v_and_b32 v25, 0xffff0000, v108\n"
      "v_lshlrev_b32 v26, 16, v109\n"
      "v_and_b32 v27, 0xffff0000, v109\n"
      "v_pk_fma_f32 v[48:49], v[16:17], v[172:173], v[48:49] op_sel:[0,0,0] op_sel_hi:[1,0,1]\n"
      "v_pk_fma_f32 v[50:51], v[18:19], v[172:173], v[50:51] op_sel:[0,0,0] op_sel_hi:[1,0,1]\n"
      "v_pk_fma_f32 v[56:57], v[24:25], v[172:173], v[56:57] op_sel:[0,0,0] op_sel_hi:[1,0,1]\n"
      "v_pk_fma_f32 v[58:59], v[26:27], v[172:173], v[58:59] op_sel:[0,0,0] op_sel_hi:[1,0,1]\n"
      "v_cvt_pk_bf16_f32 v116, v48, v49\n"
      "v_cvt_pk_bf16_f32 v118, v56, v57\n"
      "v_cvt_pk_bf16_f32 v117, v50, v51\n"
      "v_cvt_pk_bf16_f32 v119, v58, v59\n"
      "v_permlane16_swap_b32 v116, v118\n"
      "s_nop 0\n"
      "v_permlane16_swap_b32 v117, v119\n"
      "global_store_dwordx4 v196, v[116:119], s[30:31] offset:-4032 sc1 nt\n"
      "v_lshlrev_b32 v4, 16, v98\n"
      "v_and_b32 v5, 0xffff0000, v98\n"
      "v_lshlrev_b32 v6, 16, v99\n"
      "v_and_b32 v7, 0xffff0000, v99\n"
      "v_lshlrev_b32 v12, 16, v102\n"
      "v_and_b32 v13, 0xffff0000, v102\n"
      "v_lshlrev_b32 v14, 16, v103\n"
      "v_and_b32 v15, 0xffff0000, v103\n"
      "v_pk_fma_f32 v[36:37], v[4:5], v[172:173], v[36:37] op_sel:[0,1,0] op_sel_hi:[1,1,1]\n"
      "v_pk_fma_f32 v[38:39], v[6:7], v[172:173], v[38:39] op_sel:[0,1,0] op_sel_hi:[1,1,1]\n"
      "v_pk_fma_f32 v[44:45], v[12:13], v[172:173], v[44:45] op_sel:[0,1,0] op_sel_hi:[1,1,1]\n"
      "v_pk_fma_f32 v[46:47], v[14:15], v[172:173], v[46:47] op_sel:[0,1,0] op_sel_hi:[1,1,1]\n"
      "v_cvt_pk_bf16_f32 v120, v36, v37\n"
      "v_cvt_pk_bf16_f32 v122, v44, v45\n"
      "v_cvt_pk_bf16_f32 v121, v38, v39\n"
      "v_cvt_pk_bf16_f32 v123, v46, v47\n"
      "v_permlane16_swap_b32 v120, v122\n"
      "s_nop 0\n"
      "v_permlane16_swap_b32 v121, v123\n"
      "global_store_dwordx4 v196, v[120:123], s[30:31] offset:0 sc1 nt\n"
      "v_lshlrev_b32 v20, 16, v106\n"
      "v_and_b32 v21, 0xffff0000, v106\n"
      "v_lshlrev_b32 v22, 16, v107\n"
      "v_and_b32 v23, 0xffff0000, v107\n"
      "v_lshlrev_b32 v28, 16, v110\n"
      "v_and_b32 v29, 0xffff0000, v110\n"
      "v_lshlrev_b32 v30, 16, v111\n"
      "v_and_b32 v31, 0xffff0000, v111\n"
      "v_pk_fma_f32 v[52:53], v[20:21], v[172:173], v[52:53] op_sel:[0,1,0] op_sel_hi:[1,1,1]\n"
      "v_pk_fma_f32 v[54:55], v[22:23], v[172:173], v[54:55] op_sel:[0,1,0] op_sel_hi:[1,1,1]\n"
      "v_pk_fma_f32 v[60:61], v[28:29], v[172:173], v[60:61] op_sel:[0,1,0] op_sel_hi:[1,1,1]\n"
      "v_pk_fma_f32 v[62:63], v[30:31], v[172:173], v[62:63] op_sel:[0,1,0] op_sel_hi:[1,1,1]\n"
      "v_cvt_pk_bf16_f32 v124, v52, v53\n"
      "v_cvt_pk_bf16_f32 v126, v60, v61\n"
      "v_cvt_pk_bf16_f32 v125, v54, v55\n"
      "v_cvt_pk_bf16_f32 v127, v62, v63\n"
      "v_permlane16_swap_b32 v124, v126\n"
      "s_nop 0\n"
      "v_permlane16_swap_b32 v125, v127\n"
      "global_store_dwordx4 v196, v[124:127], s[30:31] offset:64 sc1 nt\n"
      "s_mov_b64 exec, 1\n"
      "global_store_dword v198, v191, s[46:47] sc1\n"
      "s_mov_b64 exec, -1\n"
      "ENDE_%=:\n"
      ".subsection 1\n"
      "MH_%=:\n"
      "s_cmp_lg_u32 s54, 0\n"
      "s_waitcnt vmcnt(12) lgkmcnt(0)\n"
      "s_cbranch_scc1 W6_%=\n"
      "s_waitcnt vmcnt(8)\n"
      "W6_%=:\n"
      "s_barrier\n"
      "v_mfma_f32_16x16x32_bf16 v[24:27], a[108:111], v[144:147], v[24:27]\n"
      "s_xor_b64 s[56:57], s[56:57], s[32:33]\n"
      "ds_read_b128 a[64:67], v242 offset:0\n"
      "ds_read_b128 a[72:75], v243 offset:0\n"
      "v_mfma_f32_16x16x32_bf16 v[32:35], a[112:115], v[144:147], v[32:35]\n"
      "s_add_u32 s61, s61, 1\n"
      "ds_read_b128 a[80:83], v242 offset:1024\n"
      "ds_read_b128 a[88:91], v243 offset:1024\n"
      "v_mfma_f32_16x16x32_bf16 v[36:39], a[112:115], v[148:151], v[36:39]\n"
      "s_add_u32 s62, s62, 0x4000\n"
      "v_exp_f32 v189, v89\n"
      "v_mfma_f32_16x16x32_bf16 a[176:179], a[112:115], v[152:155], a[176:179]\n"
      "s_and_b32 s62, s62, 0x1ffff\n"
      "v_exp_f32 v190, v90\n"
      "v_mfma_f32_16x16x32_bf16 a[180:183], a[112:115], v[156:159], a[180:183]\n"
      "v_readlane_b32 s60, v219, s61\n"
      "v_exp_f32 v191, v91\n"
      "v_mfma_f32_16x16x32_bf16 a[188:191], a[116:119], v[156:159], a[188:191]\n"
      "s_add_u32 s66, s66, s86\n"
      "v_cvt_pk_bf16_f32 v140, v188, v189\n"
      "v_mfma_f32_16x16x32_bf16 a[184:187], a[116:119], v[152:155], a[184:187]\n"
      "v_exp_f32 v192, v92\n"
      "v_mfma_f32_16x16x32_bf16 v[44:47], a[116:119], v[148:151], v[44:47]\n"
      "s_add_u32 s82, s82, s86\n"
      "v_exp_f32 v193, v93\n"
      "v_mfma_f32_16x16x32_bf16 v[40:43], a[116:119], v[144:147], v[40:43]\n"
      "s_add_u32 s65, s65, s86\n"
      "v_cvt_pk_bf16_f32 v141, v190, v191\n"
      "v_cvt_pk_bf16_f32 v142, v192, v193\n"
      "v_mfma_f32_16x16x32_bf16 v[48:51], a[120:123], v[144:147], v[48:51]\n"
      "s_max_u32 s88, s66, s65\n"
      "v_exp_f32 v194, v94\n"
      "v_mfma_f32_16x16x32_bf16 v[52:55], a[120:123], v[148:151], v[52:55]\n"
      "v_exp_f32 v195, v95\n"
      "s_waitcnt lgkmcnt(0)\n"
      "v_mfma_f32_16x16x32_bf16 a[192:195], a[120:123], v[152:155], a[192:195]\n"
      "v_cvt_pk_bf16_f32 v143, v194, v195\n"
      "v_xor_b32 v244, 0x10000, v244\n"
      "v_mfma_f32_16x16x32_bf16 a[196:199], a[120:123], v[156:159], a[196:199]\n"
      "ds_read_b128 a[68:71], v242 offset:4096\n"
      "ds_read_b128 a[76:79], v243 offset:4096\n"
      "v_mfma_f32_16x16x32_bf16 v[184:187], a[140:143], v[140:143], v[184:187]\n"
      "s_mov_b32 m0, s62\n"
      "ds_read_b128 a[84:87], v242 offset:5120\n"
      "ds_read_b128 a[92:95], v243 offset:5120\n"
      "v_mfma_f32_16x16x32_bf16 a[204:207], a[124:127], v[156:159], a[204:207]\n"
      "buffer_load_dwordx4 v228, s[56:59], s60 offen nt lds\n"
      "v_xor_b32 v245, 0x10000, v245\n"
      "v_xor_b32 v246, 0x10000, v246\n"
      "v_mfma_f32_16x16x32_bf16 a[200:203], a[124:127], v[152:155], a[200:203]\n"
      "buffer_load_dwordx4 v229, s[56:59], s60 offen offset:1024 nt lds\n"
      "v_xor_b32 v247, 0x10000, v247\n"
      "v_max3_f32 v196, v184, v185, v186\n"
      "v_mfma_f32_16x16x32_bf16 v[60:63], a[124:127], v[148:151], v[60:63]\n"
      "buffer_load_dwordx4 v230, s[56:59], s60 offen offset:2048 nt lds\n"
      "v_max_f32 v196, v196, v187\n"
      "v_cmp_nge_f32 vcc, 0x4b800000, v196\n"
      "v_mfma_f32_16x16x32_bf16 v[56:59], a[124:127], v[144:147], v[56:59]\n"
      "buffer_load_dwordx4 v231, s[56:59], s60 offen offset:3072 nt lds\n"
      "s_cbranch_vccnz FS11_%=\n"
      "FR11_%=:\n"
      "v_mfma_f32_16x16x32_bf16 v[64:67], a[64:67], a[0:3], v[160:163]\n"
      "v_exp_f32 v188, v96\n"
      "v_mfma_f32_16x16x32_bf16 v[64:67], a[72:75], a[16:19], v[64:67]\n"
      "v_add_f32 v180, v180, v184\n"
      "v_add_f32 v181, v181, v185\n"
      "v_mfma_f32_16x16x32_bf16 v[64:67], a[80:83], a[32:35], v[64:67]\n"
      "v_exp_f32 v189, v97\n"
      "v_mfma_f32_16x16x32_bf16 v[64:67], a[88:91], a[48:51], v[64:67]\n"
      "v_add_f32 v182, v182, v186\n"
      "v_add_f32 v183, v183, v187\n"
      "v_mfma_f32_16x16x32_bf16 v[72:75], a[64:67], a[4:7], v[164:167]\n"
      "v_exp_f32 v190, v98\n"
      "v_mfma_f32_16x16x32_bf16 v[72:75], a[72:75], a[20:23], v[72:75]\n"
      "v_exp_f32 v191, v99\n"
      "v_mfma_f32_16x16x32_bf16 v[72:75], a[80:83], a[36:39], v[72:75]\n"
      "v_cvt_pk_bf16_f32 v144, v188, v189\n"
      "v_cvt_pk_bf16_f32 v145, v190, v191\n"
      "v_mfma_f32_16x16x32_bf16 v[72:75], a[88:91], a[52:55], v[72:75]\n"
      "v_exp_f32 v192, v100\n"
      "v_mfma_f32_16x16x32_bf16 v[80:83], a[64:67], a[8:11], v[168:171]\n"
      "s_add_u32 s39, s39, s39\n"
      "v_exp_f32 v193, v101\n"
      "v_mfma_f32_16x16x32_bf16 v[80:83], a[72:75], a[24:27], v[80:83]\n"
      "s_memrealtime s[68:69]\n"
      "v_exp_f32 v194, v102\n"
      "v_mfma_f32_16x16x32_bf16 v[80:83], a[80:83], a[40:43], v[80:83]\n"
      "v_exp_f32 v195, v103\n"
      "v_mfma_f32_16x16x32_bf16 v[80:83], a[88:91], a[56:59], v[80:83]\n"
      "v_cvt_pk_bf16_f32 v146, v192, v193\n"
      "v_cvt_pk_bf16_f32 v147, v194, v195\n"
      "v_mfma_f32_16x16x32_bf16 v[88:91], a[64:67], a[12:15], v[172:175]\n"
      "s_nop 0\n"
      "v_mfma_f32_16x16x32_bf16 v[184:187], a[128:131], v[144:147], 0\n"
      "s_nop 0\n"
      "v_mfma_f32_16x16x32_bf16 v[88:91], a[72:75], a[28:31], v[88:91]\n"
      "s_nop 0\n"
      "v_mfma_f32_16x16x32_bf16 v[88:91], a[80:83], a[44:47], v[88:91]\n"
      "s_nop 0\n"
      "s_cbranch_scc1 SP1_%=\n"
      "s_branch HD1_%=\n"
      "HOOK_%=:\n"
      "buffer_load_dword v250, v251, s[20:23], 0 offen sc1\n"
      "s_swappc_b64 s[84:85], s[84:85]\n"
      "s_waitcnt vmcnt(12)\n"
      "v_subrev_u32 v250, s71, v250\n"
      "v_min_u32 v250, 0x258, v250\n"
      "s_nop 1\n"
      "v_add_u32_dpp v250, v250, v250 quad_perm:[1,0,3,2] row_mask:0xf bank_mask:0xf\n"
      "s_nop 1\n"
      "v_add_u32_dpp v250, v250, v250 quad_perm:[2,3,0,1] row_mask:0xf bank_mask:0xf\n"
      "s_nop 1\n"
      "v_add_u32_dpp v250, v250, v250 row_half_mirror row_mask:0xf bank_mask:0xf\n"
      "s_nop 0\n"
      "v_readfirstlane_b32 s67, v250\n"
      "s_sub_u32 s66, s66, s81\n"
      "s_lshr_b32 s67, s67, 3\n"
      "s_add_u32 s66, s66, s67\n"
      "s_max_u32 s88, s66, s65\n"
      "s_mov_b32 s86, 57\n"
      "s_mov_b32 s77, 171\n"
      "v_bfe_u32 v251, v241, 2, 7\n"
      "v_add_u32 v251, s79, v251\n"
      "buffer_load_dword v250, v251, s[20:23], 0 offen sc1\n"
      "s_swappc_b64 s[84:85], s[84:85]\n"
      "s_mov_b32 s89, 0\n"
      "s_swappc_b64 s[84:85], s[84:85]\n"
      "s_mov_b32 s86, 55\n"
      "s_mov_b32 s77, 165\n"
      "s_swappc_b64 s[84:85], s[84:85]\n"
      "s_cmp_lt_u32 s89, 1\n"
      "s_cselect_b32 s67, 2048, s72\n"
      "s_mov_b32 s89, 0\n"
      "s_cmp_lt_u32 s67, 2048\n"
      "s_cbranch_scc0 NV0_%=\n"
      "v_and_b32 v216, 0x1f0, v241\n"
      "v_lshlrev_b32 v216, 4, v216\n"
      "v_mov_b32 v215, s82\n"
      "s_lshr_b32 s67, s67, 4\n"
      "s_add_u32 s67, s67, 8192\n"
      "buffer_store_dword v215, v216, s[20:23], s67 offen sc1\n"
      "NV0_%=:\n"
      "s_swappc_b64 s[84:85], s[84:85]\n"
      "s_mov_b32 s86, 53\n"
      "s_mov_b32 s77, 159\n"
      "s_swappc_b64 s[84:85], s[84:85]\n"
      "buffer_load_dword v250, v251, s[20:23], 0 offen sc1\n"
      "s_swappc_b64 s[84:85], s[84:85]\n"
      "s_cmp_lt_u32 s89, 2\n"
      "s_cselect_b32 s67, 2048, s72\n"
      "s_mov_b32 s89, 0\n"
      "s_cmp_lt_u32 s67, 2048\n"
      "s_cbranch_scc0 NV1_%=\n"
      "v_and_b32 v216, 0x1f0, v241\n"
      "v_lshlrev_b32 v216, 4, v216\n"
      "v_mov_b32 v215, s82\n"
      "s_lshr_b32 s67, s67, 4\n"
      "s_add_u32 s67, s67, 8192\n"
      "buffer_store_dword v215, v216, s[20:23], s67 offen sc1\n"
      "NV1_%=:\n"
      "s_swappc_b64 s[84:85], s[84:85]\n"
      "s_waitcnt vmcnt(12)\n"
      "v_sub_u32 v250, s82, v250\n"
      "v_cmp_gt_u32 vcc, 1600, v250\n"
      "s_movk_i32 s76, 50\n"
      "s_nop 2\n"
      "s_bcnt1_i32_b32 s67, vcc_lo\n"
      "s_cmp_eq_u32 s70, 60\n"
      "s_cselect_b32 s67, 0, s67\n"
      "s_cmp_ge_u32 s67, 7\n"
      "s_cbranch_scc1 SM0_%=\n"
      "buffer_load_dword v250, v251, s[20:23], 0 offen sc1\n"
      "s_swappc_b64 s[84:85], s[84:85]\n"
      "s_cmp_lt_u32 s89, 2\n"
      "s_cselect_b32 s67, 2048, s72\n"
      "s_mov_b32 s89, 0\n"
      "s_cmp_lt_u32 s67, 2048\n"
      "s_cbranch_scc0 NV2_%=\n"
      "v_and_b32 v216, 0x1f0, v241\n"
      "v_lshlrev_b32 v216, 4, v216\n"
      "v_mov_b32 v215, s82\n"
      "s_lshr_b32 s67, s67, 4\n"
      "s_add_u32 s67, s67, 8192\n"
      "buffer_store_dword v215, v216, s[20:23], s67 offen sc1\n"
      "NV2_%=:\n"
      "s_swappc_b64 s[84:85], s[84:85]\n"
      "s_waitcnt vmcnt(12)\n"
      "v_sub_u32 v250, s82, v250\n"
      "v_cmp_gt_u32 vcc, 1600, v250\n"
      "s_movk_i32 s76, 66\n"
      "s_nop 2\n"
      "s_bcnt1_i32_b32 s67, vcc_lo\n"
      "s_cmp_eq_u32 s70, 60\n"
      "s_cselect_b32 s67, 0, s67\n"
      "s_cmp_ge_u32 s67, 7\n"
      "s_cbranch_scc1 SM1_%=\n"
      "buffer_load_dword v250, v251, s[20:23], 0 offen sc1\n"
      "s_swappc_b64 s[84:85], s[84:85]\n"
      "s_min_i32 s70, s70, 150\n"
      "s_waitcnt vmcnt(12)\n"
      "v_sub_u32 v250, s82, v250\n"
      "v_cmp_gt_u32 vcc, 1600, v250\n"
      "s_movk_i32 s76, 70\n"
      "s_nop 2\n"
      "s_bcnt1_i32_b32 s67, vcc_lo\n"
      "s_cmp_eq_u32 s70, 60\n"
      "s_cselect_b32 s67, 0, s67\n"
      "s_cmp_ge_u32 s67, 7\n"
      "s_cbranch_scc1 SM1_%=\n"
      "s_swappc_b64 s[84:85], s[84:85]\n"
      "s_cmp_lt_u32 s89, 2\n"
      "s_cselect_b32 s67, 2048, s72\n"
      "s_mov_b32 s89, 0\n"
      "s_cmp_lt_u32 s67, 2048\n"
      "s_cbranch_scc0 NV3_%=\n"
      "v_and_b32 v216, 0x1f0, v241\n"
      "v_lshlrev_b32 v216, 4, v216\n"
      "v_mov_b32 v215, s82\n"
      "s_lshr_b32 s67, s67, 4\n"
      "s_add_u32 s67, s67, 8192\n"
      "buffer_store_dword v215, v216, s[20:23], s67 offen sc1\n"
      "NV3_%=:\n"
      "s_swappc_b64 s[84:85], s[84:85]\n"
      "buffer_load_dword v250, v251, s[20:23], 0 offen sc1\n"
      "s_swappc_b64 s[84:85], s[84:85]\n"
      "s_waitcnt vmcnt(12)\n"
      "v_sub_u32 v250, s82, v250\n"
      "v_cmp_gt_u32 vcc, 1600, v250\n"
      "s_movk_i32 s76, 78\n"
      "s_nop 2\n"
      "s_bcnt1_i32_b32 s67, vcc_lo\n"
      "s_cmp_eq_u32 s70, 60\n"
      "s_cselect_b32 s67, 0, s67\n"
      "s_cmp_ge_u32 s67, 7\n"
      "s_cbranch_scc1 SM1_%=\n"
      "s_swappc_b64 s[84:85], s[84:85]\n"
      "s_getpc_b64 s[74:75]\n"
      "PF_%=:\n"
      "s_sub_u32 s74, s74, PF_%=-END_%=\n"
      "s_subb_u32 s75, s75, 0\n"
      "s_lshl_b32 s76, %[blk], 7\n"
      "s_and_b32 s76, s76, 0x1c00\n"
      "s_min_u32 s76, s76, ENDE_%=-END_%=-1024\n"
      "v_add_u32 v200, s76, v241\n"
      "global_load_dwordx4 v[200:203], v200, s[74:75]\n"
      "v_mov_b32 v212, 0\n"
      "global_load_dword v213, v212, s[30:31] nt\n"
      "buffer_load_dword v214, off, s[24:27], s78 nt\n"
      "s_swappc_b64 s[84:85], s[84:85]\n"
      "SM1_%=:\n"
      "s_add_u32 s66, s66, s76\n"
      "s_max_u32 s88, s66, s65\n"
      "s_min_i32 s70, s70, 150\n"
      "s_mov_b32 s87, 0\n"
      "s_branch SM2_%=\n"
      "SM0_%=:\n"
      "s_or_b32 s39, s39, 0xc0000000\n"
      "s_mov_b32 s52, 0xaaaaaaaa\n"
      "s_mov_b32 s53, 0xaaaaaaaa\n"
      "s_add_u32 s66, s66, s76\n"
      "s_max_u32 s88, s66, s65\n"
      "s_min_i32 s70, s70, 150\n"
      "s_mov_b32 s87, 0\n"
      "s_mov_b32 s86, 59\n"
      "s_mov_b32 s77, 177\n"
      "s_swappc_b64 s[84:85], s[84:85]\n"
      "s_mov_b32 s86, 57\n"
      "s_mov_b32 s77, 171\n"
      "s_swappc_b64 s[84:85], s[84:85]\n"
      "SM2_%=:\n"
      "s_mov_b32 s86, 55\n"
      "s_mov_b32 s77, 165\n"
      "s_mov_b32 s52, 0x0\n"
      "s_mov_b32 s53, 0x80000\n"
      "s_swappc_b64 s[84:85], s[84:85]\n"
      "s_getpc_b64 s[74:75]\n"
      "PG_%=:\n"
      "s_sub_u32 s74, s74, PG_%=-END_%=\n"
      "s_subb_u32 s75, s75, 0\n"
      "s_lshl_b32 s76, %[blk], 7\n"
      "s_and_b32 s76, s76, 0x1c00\n"
      "s_min_u32 s76, s76, ENDE_%=-END_%=-1024\n"
      "v_add_u32 v200, s76, v241\n"
      "global_load_dwordx4 v[200:203], v200, s[74:75]\n"
      "v_mov_b32 v212, 0\n"
      "global_load_dword v213, v212, s[30:31] nt\n"
      "buffer_load_dword v214, off, s[24:27], s78 nt\n"
      "s_swappc_b64 s[84:85], s[84:85]\n"
      "GW0_%=:\n"
      "s_memrealtime s[68:69]\n"
      "s_waitcnt lgkmcnt(0)\n"
      "s_getreg_b32 s67, hwreg(HW_REG_IB_STS, 0, 4)\n"
      "s_cmp_gt_u32 s67, 9\n"
      "s_cselect_b32 s67, s87, 0\n"
      "s_addc_u32 s89, s89, 0\n"
      "s_add_u32 s66, s66, s67\n"
      "s_add_u32 s88, s88, s67\n"
      "GS0_%=:\n"
      "s_sub_u32 s67, s68, s88\n"
      "s_cmp_ge_i32 s67, 0\n"
      "s_cbranch_scc1 GX0_%=\n"
      "s_memrealtime s[68:69]\n"
      "s_waitcnt lgkmcnt(0)\n"
      "s_branch GS0_%=\n"
      "FS1_%=:\n"
      "s_call_b64 s[96:97], FB1_%=\n"
      "s_branch FR1_%=\n"
      "FS2_%=:\n"
      "s_call_b64 s[96:97], FB0_%=\n"
      "s_branch FR2_%=\n"
      "FS3_%=:\n"
      "s_call_b64 s[96:97], FB0_%=\n"
      "s_branch FR3_%=\n"
      "GW4_%=:\n"
      "s_memrealtime s[68:69]\n"
      "s_waitcnt lgkmcnt(0)\n"
      "s_getreg_b32 s67, hwreg(HW_REG_IB_STS, 0, 4)\n"
      "s_cmp_gt_u32 s67, 9\n"
      "s_cselect_b32 s67, s87, 0\n"
      "s_addc_u32 s89, s89, 0\n"
      "s_add_u32 s66, s66, s67\n"
      "s_add_u32 s88, s88, s67\n"
      "GS4_%=:\n"
      "s_sub_u32 s67, s68, s88\n"
      "s_cmp_ge_i32 s67, 0\n"
      "s_cbranch_scc1 GX4_%=\n"
      "s_memrealtime s[68:69]\n"
      "s_waitcnt lgkmcnt(0)\n"
      "s_branch GS4_%=\n"
      "GW5_%=:\n"
      "s_memrealtime s[68:69]\n"
      "s_waitcnt lgkmcnt(0)\n"
      "s_getreg_b32 s67, hwreg(HW_REG_IB_STS, 0, 4)\n"
      "s_cmp_gt_u32 s67, 9\n"
      "s_cselect_b32 s67, s87, 0\n"
      "s_addc_u32 s89, s89, 0\n"
      "s_add_u32 s66, s66, s67\n"
      "s_add_u32 s88, s88, s67\n"
      "GS5_%=:\n"
      "s_sub_u32 s67, s68, s88\n"
      "s_cmp_ge_i32 s67, 0\n"
      "s_cbranch_scc1 GX5_%=\n"
      "s_memrealtime s[68:69]\n"
      "s_waitcnt lgkmcnt(0)\n"
      "s_branch GS5_%=\n"
      "GW6_%=:\n"
      "s_memrealtime s[68:69]\n"
      "s_waitcnt lgkmcnt(0)\n"
      "s_getreg_b32 s67, hwreg(HW_REG_IB_STS, 0, 4)\n"
      "s_cmp_gt_u32 s67, 9\n"
      "s_cselect_b32 s67, s87, 0\n"
      "s_addc_u32 s89, s89, 0\n"
      "s_add_u32 s66, s66, s67\n"
      "s_add_u32 s88, s88, s67\n"
      "GS6_%=:\n"
      "s_sub_u32 s67, s68, s88\n"
      "s_cmp_ge_i32 s67, 0\n"
      "s_cbranch_scc1 GX6_%=\n"
      "s_memrealtime s[68:69]\n"
      "s_waitcnt lgkmcnt(0)\n"
      "s_branch GS6_%=\n"
      "GW7_%=:\n"
      "s_memrealtime s[68:69]\n"
      "s_waitcnt lgkmcnt(0)\n"
      "s_getreg_b32 s67, hwreg(HW_REG_IB_STS, 0, 4)\n"
      "s_cmp_gt_u32 s67, 9\n"
      "s_cselect_b32 s67, s87, 0\n"
      "s_addc_u32 s89, s89, 0\n"
      "s_add_u32 s66, s66, s67\n"
      "s_add_u32 s88, s88, s67\n"
      "GS7_%=:\n"
      "s_sub_u32 s67, s68, s88\n"
      "s_cmp_ge_i32 s67, 0\n"
      "s_cbranch_scc1 GX7_%=\n"
      "s_memrealtime s[68:69]\n"
      "s_waitcnt lgkmcnt(0)\n"
      "s_branch GS7_%=\n"
      "FS8_%=:\n"
      "s_call_b64 s[96:97], FB1_%=\n"
      "s_branch FR8_%=\n"
      "LAG_%=:\n"
      "s_memrealtime s[68:69]\n"
      "s_waitcnt lgkmcnt(0)\n"
      "s_mov_b32 s88, s68\n"
      "LG1_%=:\n"
      "v_cmp_ne_u32 vcc, s40, v207\n"
      "s_cbranch_vccz LG2_%=\n"
      "s_sub_u32 s63, s63, 1\n"
      "s_cmp_eq_u32 s63, 0\n"
      "s_cbranch_scc1 READ_%=\n"
      "s_sleep 4\n"
      "global_load_dwordx2 v[206:207], v239, s[46:47] offset:8 sc1\n"
      "s_waitcnt vmcnt(0)\n"
      "s_branch LG1_%=\n"
      "LG2_%=:\n"
      "v_readfirstlane_b32 s67, v206\n"
      "s_sub_u32 s88, s88, s82\n"
      "s_min_u32 s88, s88, 400\n"
      "s_add_u32 s88, s88, s67\n"
      "LG3_%=:\n"
      "s_memrealtime s[68:69]\n"
      "s_waitcnt lgkmcnt(0)\n"
      "s_sub_u32 s67, s88, s68\n"
      "s_sub_u32 s67, s67, 17\n"
      "s_cmp_lt_u32 s67, 400\n"
      "s_cbranch_scc1 LG3_%=\n"
      "s_branch READ_%=\n"
      "RETRY_%=:\n"
      "s_sub_u32 s63, s63, 1\n"
      "s_cbranch_scc1 GOT_%=\n"
      "buffer_load_dwordx2 v[160:161], v240, s[24:27], s76 offen sc1\n"
      "buffer_load_dwordx4 v[96:99], v241, s[24:27], s76 offen offset:0 sc1\n"
      "buffer_load_dwordx4 v[100:103], v241, s[24:27], s76 offen offset:1024 sc1\n"
      "buffer_load_dwordx4 v[104:107], v241, s[24:27], s76 offen offset:2048 sc1\n"
      "buffer_load_dwordx4 v[108:111], v241, s[24:27], s76 offen offset:3072 sc1\n"
      "s_branch TRY_%=\n"
      "FS11_%=:\n"
      "s_call_b64 s[96:97], FB0_%=\n"
      "s_branch FR11_%=\n"
      "COLD_%=:\n"
      "FB0_%=:\n"
      "v_mov_b32 v196, 139248\n"
      "s_nop 7\n"
      "ds_write_b32 v196, v196\n"
      "v_max3_f32 v188, v64, v65, 0\n"
      "v_max3_f32 v189, v72, v73, 0\n"
      "v_max3_f32 v190, v80, v81, 0\n"
      "v_max3_f32 v191, v88, v89, 0\n"
      "v_max3_f32 v188, v188, v66, v67\n"
      "v_max3_f32 v189, v189, v74, v75\n"
      "v_max3_f32 v190, v190, v82, v83\n"
      "v_max3_f32 v191, v191, v90, v91\n"
      "v_max3_f32 v188, v188, v68, v69\n"
      "v_max3_f32 v189, v189, v76, v77\n"
      "v_max3_f32 v190, v190, v84, v85\n"
      "v_max3_f32 v191, v191, v92, v93\n"
      "v_max3_f32 v188, v188, v70, v71\n"
      "v_max3_f32 v189, v189, v78, v79\n"
      "v_max3_f32 v190, v190, v86, v87\n"
      "v_max3_f32 v191, v191, v94, v95\n"
      "v_mov_b32 v192, v188\n"
      "v_mov_b32 v193, v189\n"
      "v_mov_b32 v194, v190\n"
      "v_mov_b32 v195, v191\n"
      "v_permlane16_swap_b32 v188, v192\n"
      "v_permlane16_swap_b32 v189, v193\n"
      "v_permlane16_swap_b32 v190, v194\n"
      "v_permlane16_swap_b32 v191, v195\n"
      "v_max_f32 v188, v188, v192\n"
      "v_max_f32 v189, v189, v193\n"
      "v_max_f32 v190, v190, v194\n"
      "v_max_f32 v191, v191, v195\n"
      "v_mov_b32 v192, v188\n"
      "v_mov_b32 v193, v189\n"
      "v_mov_b32 v194, v190\n"
      "v_mov_b32 v195, v191\n"
      "v_permlane32_swap_b32 v188, v192\n"
      "v_permlane32_swap_b32 v189, v193\n"
      "v_permlane32_swap_b32 v190, v194\n"
      "v_permlane32_swap_b32 v191, v195\n"
      "v_max_f32 v188, v188, v192\n"
      "v_max_f32 v189, v189, v193\n"
      "v_max_f32 v190, v190, v194\n"
      "v_max_f32 v191, v191, v195\n"
      "RS0_%=:\n"
      "v_exp_f32 v192, -v188\n"
      "v_exp_f32 v193, -v189\n"
      "v_exp_f32 v194, -v190\n"
      "v_exp_f32 v195, -v191\n"
      "v_sub_f32 v176, v176, v188\n"
      "v_sub_f32 v177, v177, v189\n"
      "v_sub_f32 v178, v178, v190\n"
      "v_sub_f32 v179, v179, v191\n"
      "v_mov_b32 v160, v176\n"
      "v_mov_b32 v161, v176\n"
      "v_mov_b32 v162, v176\n"
      "v_mov_b32 v163, v176\n"
      "v_mov_b32 v164, v177\n"
      "v_mov_b32 v165, v177\n"
      "v_mov_b32 v166, v177\n"
      "v_mov_b32 v167, v177\n"
      "v_mov_b32 v168, v178\n"
      "v_mov_b32 v169, v178\n"
      "v_mov_b32 v170, v178\n"
      "v_mov_b32 v171, v178\n"
      "v_mov_b32 v172, v179\n"
      "v_mov_b32 v173, v179\n"
      "v_mov_b32 v174, v179\n"
      "v_mov_b32 v175, v179\n"
      "v_sub_f32 v64, v64, v188\n"
      "v_sub_f32 v65, v65, v188\n"
      "v_sub_f32 v66, v66, v188\n"
      "v_sub_f32 v67, v67, v188\n"
      "v_sub_f32 v68, v68, v188\n"
      "v_sub_f32 v69, v69, v188\n"
      "v_sub_f32 v70, v70, v188\n"
      "v_sub_f32 v71, v71, v188\n"
      "v_sub_f32 v72, v72, v189\n"
      "v_sub_f32 v73, v73, v189\n"
      "v_sub_f32 v74, v74, v189\n"
      "v_sub_f32 v75, v75, v189\n"
      "v_sub_f32 v76, v76, v189\n"
      "v_sub_f32 v77, v77, v189\n"
      "v_sub_f32 v78, v78, v189\n"
      "v_sub_f32 v79, v79, v189\n"
      "v_sub_f32 v80, v80, v190\n"
      "v_sub_f32 v81, v81, v190\n"
      "v_sub_f32 v82, v82, v190\n"
      "v_sub_f32 v83, v83, v190\n"
      "v_sub_f32 v84, v84, v190\n"
      "v_sub_f32 v85, v85, v190\n"
      "v_sub_f32 v86, v86, v190\n"
      "v_sub_f32 v87, v87, v190\n"
      "v_sub_f32 v88, v88, v191\n"
      "v_sub_f32 v89, v89, v191\n"
      "v_sub_f32 v90, v90, v191\n"
      "v_sub_f32 v91, v91, v191\n"
      "v_sub_f32 v92, v92, v191\n"
      "v_sub_f32 v93, v93, v191\n"
      "v_sub_f32 v94, v94, v191\n"
      "v_sub_f32 v95, v95, v191\n"
      "v_sub_f32 v96, v96, v188\n"
      "v_sub_f32 v97, v97, v188\n"
      "v_sub_f32 v98, v98, v188\n"
      "v_sub_f32 v99, v99, v188\n"
      "v_sub_f32 v100, v100, v188\n"
      "v_sub_f32 v101, v101, v188\n"
      "v_sub_f32 v102, v102, v188\n"
      "v_sub_f32 v103, v103, v188\n"
      "v_sub_f32 v104, v104, v189\n"
      "v_sub_f32 v105, v105, v189\n"
      "v_sub_f32 v106, v106, v189\n"
      "v_sub_f32 v107, v107, v189\n"
      "v_sub_f32 v108, v108, v189\n"
      "v_sub_f32 v109, v109, v189\n"
      "v_sub_f32 v110, v110, v189\n"
      "v_sub_f32 v111, v111, v189\n"
      "v_sub_f32 v112, v112, v190\n"
      "v_sub_f32 v113, v113, v190\n"
      "v_sub_f32 v114, v114, v190\n"
      "v_sub_f32 v115, v115, v190\n"
      "v_sub_f32 v116, v116, v190\n"
      "v_sub_f32 v117, v117, v190\n"
      "v_sub_f32 v118, v118, v190\n"
      "v_sub_f32 v119, v119, v190\n"
      "v_sub_f32 v120, v120, v191\n"
      "v_sub_f32 v121, v121, v191\n"
      "v_sub_f32 v122, v122, v191\n"
      "v_sub_f32 v123, v123, v191\n"
      "v_sub_f32 v124, v124, v191\n"
      "v_sub_f32 v125, v125, v191\n"
      "v_sub_f32 v126, v126, v191\n"
      "v_sub_f32 v127, v127, v191\n"
      "v_mul_f32 v0, v0, v192\n"
      "v_mul_f32 v1, v1, v192\n"
      "v_mul_f32 v2, v2, v192\n"
      "v_mul_f32 v3, v3, v192\n"
      "v_mul_f32 v4, v4, v193\n"
      "v_mul_f32 v5, v5, v193\n"
      "v_mul_f32 v6, v6, v193\n"
      "v_mul_f32 v7, v7, v193\n"
      "v_mul_f32 v8, v8, v192\n"
      "v_mul_f32 v9, v9, v192\n"
      "v_mul_f32 v10, v10, v192\n"
      "v_mul_f32 v11, v11, v192\n"
      "v_mul_f32 v12, v12, v193\n"
      "v_mul_f32 v13, v13, v193\n"
      "v_mul_f32 v14, v14, v193\n"
      "v_mul_f32 v15, v15, v193\n"
      "v_mul_f32 v16, v16, v192\n"
      "v_mul_f32 v17, v17, v192\n"
      "v_mul_f32 v18, v18, v192\n"
      "v_mul_f32 v19, v19, v192\n"
      "v_mul_f32 v20, v20, v193\n"
      "v_mul_f32 v21, v21, v193\n"
      "v_mul_f32 v22, v22, v193\n"
      "v_mul_f32 v23, v23, v193\n"
      "v_mul_f32 v24, v24, v192\n"
      "v_mul_f32 v25, v25, v192\n"
      "v_mul_f32 v26, v26, v192\n"
      "v_mul_f32 v27, v27, v192\n"
      "v_mul_f32 v28, v28, v193\n"
      "v_mul_f32 v29, v29, v193\n"
      "v_mul_f32 v30, v30, v193\n"
      "v_mul_f32 v31, v31, v193\n"
      "v_mul_f32 v32, v32, v192\n"
      "v_mul_f32 v33, v33, v192\n"
      "v_mul_f32 v34, v34, v192\n"
      "v_mul_f32 v35, v35, v192\n"
      "v_mul_f32 v36, v36, v193\n"
      "v_mul_f32 v37, v37, v193\n"
      "v_mul_f32 v38, v38, v193\n"
      "v_mul_f32 v39, v39, v193\n"
      "v_mul_f32 v40, v40, v192\n"
      "v_mul_f32 v41, v41, v192\n"
      "v_mul_f32 v42, v42, v192\n"
      "v_mul_f32 v43, v43, v192\n"
      "v_mul_f32 v44, v44, v193\n"
      "v_mul_f32 v45, v45, v193\n"
      "v_mul_f32 v46, v46, v193\n"
      "v_mul_f32 v47, v47, v193\n"
      "v_mul_f32 v48, v48, v192\n"
      "v_mul_f32 v49, v49, v192\n"
      "v_mul_f32 v50, v50, v192\n"
      "v_mul_f32 v51, v51, v192\n"
      "v_mul_f32 v52, v52, v193\n"
      "v_mul_f32 v53, v53, v193\n"
      "v_mul_f32 v54, v54, v193\n"
      "v_mul_f32 v55, v55, v193\n"
      "v_mul_f32 v56, v56, v192\n"
      "v_mul_f32 v57, v57, v192\n"
      "v_mul_f32 v58, v58, v192\n"
      "v_mul_f32 v59, v59, v192\n"
      "v_mul_f32 v60, v60, v193\n"
      "v_mul_f32 v61, v61, v193\n"
      "v_mul_f32 v62, v62, v193\n"
      "v_mul_f32 v63, v63, v193\n"
      "v_accvgpr_read_b32 v196, a144\n"
      "v_accvgpr_read_b32 v197, a145\n"
      "v_accvgpr_read_b32 v198, a146\n"
      "v_accvgpr_read_b32 v199, a147\n"
      "v_mul_f32 v196, v196, v194\n"
      "v_mul_f32 v197, v197, v194\n"
      "v_mul_f32 v198, v198, v194\n"
      "v_mul_f32 v199, v199, v194\n"
      "v_accvgpr_write_b32 a144, v196\n"
      "v_accvgpr_write_b32 a145, v197\n"
      "v_accvgpr_write_b32 a146, v198\n"
      "v_accvgpr_write_b32 a147, v199\n"
      "v_accvgpr_read_b32 v196, a148\n"
      "v_accvgpr_read_b32 v197, a149\n"
      "v_accvgpr_read_b32 v198, a150\n"
      "v_accvgpr_read_b32 v199, a151\n"
      "v_mul_f32 v196, v196, v195\n"
      "v_mul_f32 v197, v197, v195\n"
      "v_mul_f32 v198, v198, v195\n"
      "v_mul_f32 v199, v199, v195\n"
      "v_accvgpr_write_b32 a148, v196\n"
      "v_accvgpr_write_b32 a149, v197\n"
      "v_accvgpr_write_b32 a150, v198\n"
      "v_accvgpr_write_b32 a151, v199\n"
      "v_accvgpr_read_b32 v196, a152\n"
      "v_accvgpr_read_b32 v197, a153\n"
      "v_accvgpr_read_b32 v198, a154\n"
      "v_accvgpr_read_b32 v199, a155\n"
      "v_mul_f32 v196, v196, v194\n"
      "v_mul_f32 v197, v197, v194\n"
      "v_mul_f32 v198, v198, v194\n"
      "v_mul_f32 v199, v199, v194\n"
      "v_accvgpr_write_b32 a152, v196\n"
      "v_accvgpr_write_b32 a153, v197\n"
      "v_accvgpr_write_b32 a154, v198\n"
      "v_accvgpr_write_b32 a155, v199\n"
      "v_accvgpr_read_b32 v196, a156\n"
      "v_accvgpr_read_b32 v197, a157\n"
      "v_accvgpr_read_b32 v198, a158\n"
      "v_accvgpr_read_b32 v199, a159\n"
      "v_mul_f32 v196, v196, v195\n"
      "v_mul_f32 v197, v197, v195\n"
      "v_mul_f32 v198, v198, v195\n"
      "v_mul_f32 v199, v199, v195\n"
      "v_accvgpr_write_b32 a156, v196\n"
      "v_accvgpr_write_b32 a157, v197\n"
      "v_accvgpr_write_b32 a158, v198\n"
      "v_accvgpr_write_b32 a159, v199\n"
      "v_accvgpr_read_b32 v196, a160\n"
      "v_accvgpr_read_b32 v197, a161\n"
      "v_accvgpr_read_b32 v198, a162\n"
      "v_accvgpr_read_b32 v199, a163\n"
      "v_mul_f32 v196, v196, v194\n"
      "v_mul_f32 v197, v197, v194\n"
      "v_mul_f32 v198, v198, v194\n"
      "v_mul_f32 v199, v199, v194\n"
      "v_accvgpr_write_b32 a160, v196\n"
      "v_accvgpr_write_b32 a161, v197\n"
      "v_accvgpr_write_b32 a162, v198\n"
      "v_accvgpr_write_b32 a163, v199\n"
      "v_accvgpr_read_b32 v196, a164\n"
      "v_accvgpr_read_b32 v197, a165\n"
      "v_accvgpr_read_b32 v198, a166\n"
      "v_accvgpr_read_b32 v199, a167\n"
      "v_mul_f32 v196, v196, v195\n"
      "v_mul_f32 v197, v197, v195\n"
      "v_mul_f32 v198, v198, v195\n"
      "v_mul_f32 v199, v199, v195\n"
      "v_accvgpr_write_b32 a164, v196\n"
      "v_accvgpr_write_b32 a165, v197\n"
      "v_accvgpr_write_b32 a166, v198\n"
      "v_accvgpr_write_b32 a167, v199\n"
      "v_accvgpr_read_b32 v196, a168\n"
      "v_accvgpr_read_b32 v197, a169\n"
      "v_accvgpr_read_b32 v198, a170\n"
      "v_accvgpr_read_b32 v199, a171\n"
      "v_mul_f32 v196, v196, v194\n"
      "v_mul_f32 v197, v197, v194\n"
      "v_mul_f32 v198, v198, v194\n"
      "v_mul_f32 v199, v199, v194\n"
      "v_accvgpr_write_b32 a168, v196\n"
      "v_accvgpr_write_b32 a169, v197\n"
      "v_accvgpr_write_b32 a170, v198\n"
      "v_accvgpr_write_b32 a171, v199\n"
      "v_accvgpr_read_b32 v196, a172\n"
      "v_accvgpr_read_b32 v197, a173\n"
      "v_accvgpr_read_b32 v198, a174\n"
      "v_accvgpr_read_b32 v199, a175\n"
      "v_mul_f32 v196, v196, v195\n"
      "v_mul_f32 v197, v197, v195\n"
      "v_mul_f32 v198, v198, v195\n"
      "v_mul_f32 v199, v199, v195\n"
      "v_accvgpr_write_b32 a172, v196\n"
      "v_accvgpr_write_b32 a173, v197\n"
      "v_accvgpr_write_b32 a174, v198\n"
      "v_accvgpr_write_b32 a175, v199\n"
      "v_accvgpr_read_b32 v196, a176\n"
      "v_accvgpr_read_b32 v197, a177\n"
      "v_accvgpr_read_b32 v198, a178\n"
      "v_accvgpr_read_b32 v199, a179\n"
      "v_mul_f32 v196, v196, v194\n"
      "v_mul_f32 v197, v197, v194\n"
      "v_mul_f32 v198, v198, v194\n"
      "v_mul_f32 v199, v199, v194\n"
      "v_accvgpr_write_b32 a176, v196\n"
      "v_accvgpr_write_b32 a177, v197\n"
      "v_accvgpr_write_b32 a178, v198\n"
      "v_accvgpr_write_b32 a179, v199\n"
      "v_accvgpr_read_b32 v196, a180\n"
      "v_accvgpr_read_b32 v197, a181\n"
      "v_accvgpr_read_b32 v198, a182\n"
      "v_accvgpr_read_b32 v199, a183\n"
      "v_mul_f32 v196, v196, v195\n"
      "v_mul_f32 v197, v197, v195\n"
      "v_mul_f32 v198, v198, v195\n"
      "v_mul_f32 v199, v199, v195\n"
      "v_accvgpr_write_b32 a180, v196\n"
      "v_accvgpr_write_b32 a181, v197\n"
      "v_accvgpr_write_b32 a182, v198\n"
      "v_accvgpr_write_b32 a183, v199\n"
      "v_accvgpr_read_b32 v196, a184\n"
      "v_accvgpr_read_b32 v197, a185\n"
      "v_accvgpr_read_b32 v198, a186\n"
      "v_accvgpr_read_b32 v199, a187\n"
      "v_mul_f32 v196, v196, v194\n"
      "v_mul_f32 v197, v197, v194\n"
      "v_mul_f32 v198, v198, v194\n"
      "v_mul_f32 v199, v199, v194\n"
      "v_accvgpr_write_b32 a184, v196\n"
      "v_accvgpr_write_b32 a185, v197\n"
      "v_accvgpr_write_b32 a186, v198\n"
      "v_accvgpr_write_b32 a187, v199\n"
      "v_accvgpr_read_b32 v196, a188\n"
      "v_accvgpr_read_b32 v197, a189\n"
      "v_accvgpr_read_b32 v198, a190\n"
      "v_accvgpr_read_b32 v199, a191\n"
      "v_mul_f32 v196, v196, v195\n"
      "v_mul_f32 v197, v197, v195\n"
      "v_mul_f32 v198, v198, v195\n"
      "v_mul_f32 v199, v199, v195\n"
      "v_accvgpr_write_b32 a188, v196\n"
      "v_accvgpr_write_b32 a189, v197\n"
      "v_accvgpr_write_b32 a190, v198\n"
      "v_accvgpr_write_b32 a191, v199\n"
      "v_accvgpr_read_b32 v196, a192\n"
      "v_accvgpr_read_b32 v197, a193\n"
      "v_accvgpr_read_b32 v198, a194\n"
      "v_accvgpr_read_b32 v199, a195\n"
      "v_mul_f32 v196, v196, v194\n"
      "v_mul_f32 v197, v197, v194\n"
      "v_mul_f32 v198, v198, v194\n"
      "v_mul_f32 v199, v199, v194\n"
      "v_accvgpr_write_b32 a192, v196\n"
      "v_accvgpr_write_b32 a193, v197\n"
      "v_accvgpr_write_b32 a194, v198\n"
      "v_accvgpr_write_b32 a195, v199\n"
      "v_accvgpr_read_b32 v196, a196\n"
      "v_accvgpr_read_b32 v197, a197\n"
      "v_accvgpr_read_b32 v198, a198\n"
      "v_accvgpr_read_b32 v199, a199\n"
      "v_mul_f32 v196, v196, v195\n"
      "v_mul_f32 v197, v197, v195\n"
      "v_mul_f32 v198, v198, v195\n"
      "v_mul_f32 v199, v199, v195\n"
      "v_accvgpr_write_b32 a196, v196\n"
      "v_accvgpr_write_b32 a197, v197\n"
      "v_accvgpr_write_b32 a198, v198\n"
      "v_accvgpr_write_b32 a199, v199\n"
      "v_accvgpr_read_b32 v196, a200\n"
      "v_accvgpr_read_b32 v197, a201\n"
      "v_accvgpr_read_b32 v198, a202\n"
      "v_accvgpr_read_b32 v199, a203\n"
      "v_mul_f32 v196, v196, v194\n"
      "v_mul_f32 v197, v197, v194\n"
      "v_mul_f32 v198, v198, v194\n"
      "v_mul_f32 v199, v199, v194\n"
      "v_accvgpr_write_b32 a200, v196\n"
      "v_accvgpr_write_b32 a201, v197\n"
      "v_accvgpr_write_b32 a202, v198\n"
      "v_accvgpr_write_b32 a203, v199\n"
      "v_accvgpr_read_b32 v196, a204\n"
      "v_accvgpr_read_b32 v197, a205\n"
      "v_accvgpr_read_b32 v198, a206\n"
      "v_accvgpr_read_b32 v199, a207\n"
      "v_mul_f32 v196, v196, v195\n"
      "v_mul_f32 v197, v197, v195\n"
      "v_mul_f32 v198, v198, v195\n"
      "v_mul_f32 v199, v199, v195\n"
      "v_accvgpr_write_b32 a204, v196\n"
      "v_accvgpr_write_b32 a205, v197\n"
      "v_accvgpr_write_b32 a206, v198\n"
      "v_accvgpr_write_b32 a207, v199\n"
      "v_mul_f32 v180, v180, v192\n"
      "v_mul_f32 v181, v181, v193\n"
      "v_mul_f32 v182, v182, v194\n"
      "v_mul_f32 v183, v183, v195\n"
      "v_exp_f32 v188, v64\n"
      "v_exp_f32 v189, v65\n"
      "v_exp_f32 v190, v66\n"
      "v_exp_f32 v191, v67\n"
      "v_cvt_pk_bf16_f32 v128, v188, v189\n"
      "v_cvt_pk_bf16_f32 v129, v190, v191\n"
      "v_exp_f32 v192, v68\n"
      "v_exp_f32 v193, v69\n"
      "v_exp_f32 v194, v70\n"
      "v_exp_f32 v195, v71\n"
      "v_cvt_pk_bf16_f32 v130, v192, v193\n"
      "v_cvt_pk_bf16_f32 v131, v194, v195\n"
      "v_exp_f32 v188, v72\n"
      "v_exp_f32 v189, v73\n"
      "v_exp_f32 v190, v74\n"
      "v_exp_f32 v191, v75\n"
      "v_cvt_pk_bf16_f32 v132, v188, v189\n"
      "v_cvt_pk_bf16_f32 v133, v190, v191\n"
      "v_exp_f32 v192, v76\n"
      "v_exp_f32 v193, v77\n"
      "v_exp_f32 v194, v78\n"
      "v_exp_f32 v195, v79\n"
      "v_cvt_pk_bf16_f32 v134, v192, v193\n"
      "v_cvt_pk_bf16_f32 v135, v194, v195\n"
      "v_exp_f32 v188, v80\n"
      "v_exp_f32 v189, v81\n"
      "v_exp_f32 v190, v82\n"
      "v_exp_f32 v191, v83\n"
      "v_cvt_pk_bf16_f32 v136, v188, v189\n"
      "v_cvt_pk_bf16_f32 v137, v190, v191\n"
      "v_exp_f32 v192, v84\n"
      "v_exp_f32 v193, v85\n"
      "v_exp_f32 v194, v86\n"
      "v_exp_f32 v195, v87\n"
      "v_cvt_pk_bf16_f32 v138, v192, v193\n"
      "v_cvt_pk_bf16_f32 v139, v194, v195\n"
      "v_exp_f32 v188, v88\n"
      "v_exp_f32 v189, v89\n"
      "v_exp_f32 v190, v90\n"
      "v_exp_f32 v191, v91\n"
      "v_cvt_pk_bf16_f32 v140, v188, v189\n"
      "v_cvt_pk_bf16_f32 v141, v190, v191\n"
      "v_exp_f32 v192, v92\n"
      "v_exp_f32 v193, v93\n"
      "v_exp_f32 v194, v94\n"
      "v_exp_f32 v195, v95\n"
      "v_cvt_pk_bf16_f32 v142, v192, v193\n"
      "v_cvt_pk_bf16_f32 v143, v194, v195\n"
      "s_nop 1\n"
      "v_mfma_f32_16x16x32_bf16 v[184:187], a[128:131], v[128:131], 0\n"
      "v_mfma_f32_16x16x32_bf16 v[184:187], a[132:135], v[132:135], v[184:187]\n"
      "v_mfma_f32_16x16x32_bf16 v[184:187], a[136:139], v[136:139], v[184:187]\n"
      "v_mfma_f32_16x16x32_bf16 v[184:187], a[140:143], v[140:143], v[184:187]\n"
      "s_nop 7\n"
      "s_setpc_b64 s[96:97]\n"
      "FB1_%=:\n"
      "v_mov_b32 v196, 139248\n"
      "s_nop 7\n"
      "ds_write_b32 v196, v196\n"
      "v_max3_f32 v188, v96, v97, 0\n"
      "v_max3_f32 v189, v104, v105, 0\n"
      "v_max3_f32 v190, v112, v113, 0\n"
      "v_max3_f32 v191, v120, v121, 0\n"
      "v_max3_f32 v188, v188, v98, v99\n"
      "v_max3_f32 v189, v189, v106, v107\n"
      "v_max3_f32 v190, v190, v114, v115\n"
      "v_max3_f32 v191, v191, v122, v123\n"
      "v_max3_f32 v188, v188, v100, v101\n"
      "v_max3_f32 v189, v189, v108, v109\n"
      "v_max3_f32 v190, v190, v116, v117\n"
      "v_max3_f32 v191, v191, v124, v125\n"
      "v_max3_f32 v188, v188, v102, v103\n"
      "v_max3_f32 v189, v189, v110, v111\n"
      "v_max3_f32 v190, v190, v118, v119\n"
      "v_max3_f32 v191, v191, v126, v127\n"
      "v_mov_b32 v192, v188\n"
      "v_mov_b32 v193, v189\n"
      "v_mov_b32 v194, v190\n"
      "v_mov_b32 v195, v191\n"
      "v_permlane16_swap_b32 v188, v192\n"
      "v_permlane16_swap_b32 v189, v193\n"
      "v_permlane16_swap_b32 v190, v194\n"
      "v_permlane16_swap_b32 v191, v195\n"
      "v_max_f32 v188, v188, v192\n"
      "v_max_f32 v189, v189, v193\n"
      "v_max_f32 v190, v190, v194\n"
      "v_max_f32 v191, v191, v195\n"
      "v_mov_b32 v192, v188\n"
      "v_mov_b32 v193, v189\n"
      "v_mov_b32 v194, v190\n"
      "v_mov_b32 v195, v191\n"
      "v_permlane32_swap_b32 v188, v192\n"
      "v_permlane32_swap_b32 v189, v193\n"
      "v_permlane32_swap_b32 v190, v194\n"
      "v_permlane32_swap_b32 v191, v195\n"
      "v_max_f32 v188, v188, v192\n"
      "v_max_f32 v189, v189, v193\n"
      "v_max_f32 v190, v190, v194\n"
      "v_max_f32 v191, v191, v195\n"
      "RS1_%=:\n"
      "v_exp_f32 v192, -v188\n"
      "v_exp_f32 v193, -v189\n"
      "v_exp_f32 v194, -v190\n"
      "v_exp_f32 v195, -v191\n"
      "v_sub_f32 v176, v176, v188\n"
      "v_sub_f32 v177, v177, v189\n"
      "v_sub_f32 v178, v178, v190\n"
      "v_sub_f32 v179, v179, v191\n"
      "v_mov_b32 v160, v176\n"
      "v_mov_b32 v161, v176\n"
      "v_mov_b32 v162, v176\n"
      "v_mov_b32 v163, v176\n"
      "v_mov_b32 v164, v177\n"
      "v_mov_b32 v165, v177\n"
      "v_mov_b32 v166, v177\n"
      "v_mov_b32 v167, v177\n"
      "v_mov_b32 v168, v178\n"
      "v_mov_b32 v169, v178\n"
      "v_mov_b32 v170, v178\n"
      "v_mov_b32 v171, v178\n"
      "v_mov_b32 v172, v179\n"
      "v_mov_b32 v173, v179\n"
      "v_mov_b32 v174, v179\n"
      "v_mov_b32 v175, v179\n"
      "v_sub_f32 v64, v64, v188\n"
      "v_sub_f32 v65, v65, v188\n"
      "v_sub_f32 v66, v66, v188\n"
      "v_sub_f32 v67, v67, v188\n"
      "v_sub_f32 v68, v68, v188\n"
      "v_sub_f32 v69, v69, v188\n"
      "v_sub_f32 v70, v70, v188\n"
      "v_sub_f32 v71, v71, v188\n"
      "v_sub_f32 v72, v72, v189\n"
      "v_sub_f32 v73, v73, v189\n"
      "v_sub_f32 v74, v74, v189\n"
      "v_sub_f32 v75, v75, v189\n"
      "v_sub_f32 v76, v76, v189\n"
      "v_sub_f32 v77, v77, v189\n"
      "v_sub_f32 v78, v78, v189\n"
      "v_sub_f32 v79, v79, v189\n"
      "v_sub_f32 v80, v80, v190\n"
      "v_sub_f32 v81, v81, v190\n"
      "v_sub_f32 v82, v82, v190\n"
      "v_sub_f32 v83, v83, v190\n"
      "v_sub_f32 v84, v84, v190\n"
      "v_sub_f32 v85, v85, v190\n"
      "v_sub_f32 v86, v86, v190\n"
      "v_sub_f32 v87, v87, v190\n"
      "v_sub_f32 v88, v88, v191\n"
      "v_sub_f32 v89, v89, v191\n"
      "v_sub_f32 v90, v90, v191\n"
      "v_sub_f32 v91, v91, v191\n"
      "v_sub_f32 v92, v92, v191\n"
      "v_sub_f32 v93, v93, v191\n"
      "v_sub_f32 v94, v94, v191\n"
      "v_sub_f32 v95, v95, v191\n"
      "v_sub_f32 v96, v96, v188\n"
      "v_sub_f32 v97, v97, v188\n"
      "v_sub_f32 v98, v98, v188\n"
      "v_sub_f32 v99, v99, v188\n"
      "v_sub_f32 v100, v100, v188\n"
      "v_sub_f32 v101, v101, v188\n"
      "v_sub_f32 v102, v102, v188\n"
      "v_sub_f32 v103, v103, v188\n"
      "v_sub_f32 v104, v104, v189\n"
      "v_sub_f32 v105, v105, v189\n"
      "v_sub_f32 v106, v106, v189\n"
      "v_sub_f32 v107, v107, v189\n"
      "v_sub_f32 v108, v108, v189\n"
      "v_sub_f32 v109, v109, v189\n"
      "v_sub_f32 v110, v110, v189\n"
      "v_sub_f32 v111, v111, v189\n"
      "v_sub_f32 v112, v112, v190\n"
      "v_sub_f32 v113, v113, v190\n"
      "v_sub_f32 v114, v114, v190\n"
      "v_sub_f32 v115, v115, v190\n"
      "v_sub_f32 v116, v116, v190\n"
      "v_sub_f32 v117, v117, v190\n"
      "v_sub_f32 v118, v118, v190\n"
      "v_sub_f32 v119, v119, v190\n"
      "v_sub_f32 v120, v120, v191\n"
      "v_sub_f32 v121, v121, v191\n"
      "v_sub_f32 v122, v122, v191\n"
      "v_sub_f32 v123, v123, v191\n"
      "v_sub_f32 v124, v124, v191\n"
      "v_sub_f32 v125, v125, v191\n"
      "v_sub_f32 v126, v126, v191\n"
      "v_sub_f32 v127, v127, v191\n"
      "v_mul_f32 v0, v0, v192\n"
      "v_mul_f32 v1, v1, v192\n"
      "v_mul_f32 v2, v2, v192\n"
      "v_mul_f32 v3, v3, v192\n"
      "v_mul_f32 v4, v4, v193\n"
      "v_mul_f32 v5, v5, v193\n"
      "v_mul_f32 v6, v6, v193\n"
      "v_mul_f32 v7, v7, v193\n"
      "v_mul_f32 v8, v8, v192\n"
      "v_mul_f32 v9, v9, v192\n"
      "v_mul_f32 v10, v10, v192\n"
      "v_mul_f32 v11, v11, v192\n"
      "v_mul_f32 v12, v12, v193\n"
      "v_mul_f32 v13, v13, v193\n"
      "v_mul_f32 v14, v14, v193\n"
      "v_mul_f32 v15, v15, v193\n"
      "v_mul_f32 v16, v16, v192\n"
      "v_mul_f32 v17, v17, v192\n"
      "v_mul_f32 v18, v18, v192\n"
      "v_mul_f32 v19, v19, v192\n"
      "v_mul_f32 v20, v20, v193\n"
      "v_mul_f32 v21, v21, v193\n"
      "v_mul_f32 v22, v22, v193\n"
      "v_mul_f32 v23, v23, v193\n"
      "v_mul_f32 v24, v24, v192\n"
      "v_mul_f32 v25, v25, v192\n"
      "v_mul_f32 v26, v26, v192\n"
      "v_mul_f32 v27, v27, v192\n"
      "v_mul_f32 v28, v28, v193\n"
      "v_mul_f32 v29, v29, v193\n"
      "v_mul_f32 v30, v30, v193\n"
      "v_mul_f32 v31, v31, v193\n"
      "v_mul_f32 v32, v32, v192\n"
      "v_mul_f32 v33, v33, v192\n"
      "v_mul_f32 v34, v34, v192\n"
      "v_mul_f32 v35, v35, v192\n"
      "v_mul_f32 v36, v36, v193\n"
      "v_mul_f32 v37, v37, v193\n"
      "v_mul_f32 v38, v38, v193\n"
      "v_mul_f32 v39, v39, v193\n"
      "v_mul_f32 v40, v40, v192\n"
      "v_mul_f32 v41, v41, v192\n"
      "v_mul_f32 v42, v42, v192\n"
      "v_mul_f32 v43, v43, v192\n"
      "v_mul_f32 v44, v44, v193\n"
      "v_mul_f32 v45, v45, v193\n"
      "v_mul_f32 v46, v46, v193\n"
      "v_mul_f32 v47, v47, v193\n"
      "v_mul_f32 v48, v48, v192\n"
      "v_mul_f32 v49, v49, v192\n"
      "v_mul_f32 v50, v50, v192\n"
      "v_mul_f32 v51, v51, v192\n"
      "v_mul_f32 v52, v52, v193\n"
      "v_mul_f32 v53, v53, v193\n"
      "v_mul_f32 v54, v54, v193\n"
      "v_mul_f32 v55, v55, v193\n"
      "v_mul_f32 v56, v56, v192\n"
      "v_mul_f32 v57, v57, v192\n"
      "v_mul_f32 v58, v58, v192\n"
      "v_mul_f32 v59, v59, v192\n"
      "v_mul_f32 v60, v60, v193\n"
      "v_mul_f32 v61, v61, v193\n"
      "v_mul_f32 v62, v62, v193\n"
      "v_mul_f32 v63, v63, v193\n"
      "v_accvgpr_read_b32 v196, a144\n"
      "v_accvgpr_read_b32 v197, a145\n"
      "v_accvgpr_read_b32 v198, a146\n"
      "v_accvgpr_read_b32 v199, a147\n"
      "v_mul_f32 v196, v196, v194\n"
      "v_mul_f32 v197, v197, v194\n"
      "v_mul_f32 v198, v198, v194\n"
      "v_mul_f32 v199, v199, v194\n"
      "v_accvgpr_write_b32 a144, v196\n"
      "v_accvgpr_write_b32 a145, v197\n"
      "v_accvgpr_write_b32 a146, v198\n"
      "v_accvgpr_write_b32 a147, v199\n"
      "v_accvgpr_read_b32 v196, a148\n"
      "v_accvgpr_read_b32 v197, a149\n"
      "v_accvgpr_read_b32 v198, a150\n"
      "v_accvgpr_read_b32 v199, a151\n"
      "v_mul_f32 v196, v196, v195\n"
      "v_mul_f32 v197, v197, v195\n"
      "v_mul_f32 v198, v198, v195\n"
      "v_mul_f32 v199, v199, v195\n"
      "v_accvgpr_write_b32 a148, v196\n"
      "v_accvgpr_write_b32 a149, v197\n"
      "v_accvgpr_write_b32 a150, v198\n"
      "v_accvgpr_write_b32 a151, v199\n"
      "v_accvgpr_read_b32 v196, a152\n"
      "v_accvgpr_read_b32 v197, a153\n"
      "v_accvgpr_read_b32 v198, a154\n"
      "v_accvgpr_read_b32 v199, a155\n"
      "v_mul_f32 v196, v196, v194\n"
      "v_mul_f32 v197, v197, v194\n"
      "v_mul_f32 v198, v198, v194\n"
      "v_mul_f32 v199, v199, v194\n"
      "v_accvgpr_write_b32 a152, v196\n"
      "v_accvgpr_write_b32 a153, v197\n"
      "v_accvgpr_write_b32 a154, v198\n"
      "v_accvgpr_write_b32 a155, v199\n"
      "v_accvgpr_read_b32 v196, a156\n"
      "v_accvgpr_read_b32 v197, a157\n"
      "v_accvgpr_read_b32 v198, a158\n"
      "v_accvgpr_read_b32 v199, a159\n"
      "v_mul_f32 v196, v196, v195\n"
      "v_mul_f32 v197, v197, v195\n"
      "v_mul_f32 v198, v198, v195\n"
      "v_mul_f32 v199, v199, v195\n"
      "v_accvgpr_write_b32 a156, v196\n"
      "v_accvgpr_write_b32 a157, v197\n"
      "v_accvgpr_write_b32 a158, v198\n"
      "v_accvgpr_write_b32 a159, v199\n"
      "v_accvgpr_read_b32 v196, a160\n"
      "v_accvgpr_read_b32 v197, a161\n"
      "v_accvgpr_read_b32 v198, a162\n"
      "v_accvgpr_read_b32 v199, a163\n"
      "v_mul_f32 v196, v196, v194\n"
      "v_mul_f32 v197, v197, v194\n"
      "v_mul_f32 v198, v198, v194\n"
      "v_mul_f32 v199, v199, v194\n"
      "v_accvgpr_write_b32 a160, v196\n"
      "v_accvgpr_write_b32 a161, v197\n"
      "v_accvgpr_write_b32 a162, v198\n"
      "v_accvgpr_write_b32 a163, v199\n"
      "v_accvgpr_read_b32 v196, a164\n"
      "v_accvgpr_read_b32 v197, a165\n"
      "v_accvgpr_read_b32 v198, a166\n"
      "v_accvgpr_read_b32 v199, a167\n"
      "v_mul_f32 v196, v196, v195\n"
      "v_mul_f32 v197, v197, v195\n"
      "v_mul_f32 v198, v198, v195\n"
      "v_mul_f32 v199, v199, v195\n"
      "v_accvgpr_write_b32 a164, v196\n"
      "v_accvgpr_write_b32 a165, v197\n"
      "v_accvgpr_write_b32 a166, v198\n"
      "v_accvgpr_write_b32 a167, v199\n"
      "v_accvgpr_read_b32 v196, a168\n"
      "v_accvgpr_read_b32 v197, a169\n"
      "v_accvgpr_read_b32 v198, a170\n"
      "v_accvgpr_read_b32 v199, a171\n"
      "v_mul_f32 v196, v196, v194\n"
      "v_mul_f32 v197, v197, v194\n"
      "v_mul_f32 v198, v198, v194\n"
      "v_mul_f32 v199, v199, v194\n"
      "v_accvgpr_write_b32 a168, v196\n"
      "v_accvgpr_write_b32 a169, v197\n"
      "v_accvgpr_write_b32 a170, v198\n"
      "v_accvgpr_write_b32 a171, v199\n"
      "v_accvgpr_read_b32 v196, a172\n"
      "v_accvgpr_read_b32 v197, a173\n"
      "v_accvgpr_read_b32 v198, a174\n"
      "v_accvgpr_read_b32 v199, a175\n"
      "v_mul_f32 v196, v196, v195\n"
      "v_mul_f32 v197, v197, v195\n"
      "v_mul_f32 v198, v198, v195\n"
      "v_mul_f32 v199, v199, v195\n"
      "v_accvgpr_write_b32 a172, v196\n"
      "v_accvgpr_write_b32 a173, v197\n"
      "v_accvgpr_write_b32 a174, v198\n"
      "v_accvgpr_write_b32 a175, v199\n"
      "v_accvgpr_read_b32 v196, a176\n"
      "v_accvgpr_read_b32 v197, a177\n"
      "v_accvgpr_read_b32 v198, a178\n"
      "v_accvgpr_read_b32 v199, a179\n"
      "v_mul_f32 v196, v196, v194\n"
      "v_mul_f32 v197, v197, v194\n"
      "v_mul_f32 v198, v198, v194\n"
      "v_mul_f32 v199, v199, v194\n"
      "v_accvgpr_write_b32 a176, v196\n"
      "v_accvgpr_write_b32 a177, v197\n"
      "v_accvgpr_write_b32 a178, v198\n"
      "v_accvgpr_write_b32 a179, v199\n"
      "v_accvgpr_read_b32 v196, a180\n"
      "v_accvgpr_read_b32 v197, a181\n"
      "v_accvgpr_read_b32 v198, a182\n"
      "v_accvgpr_read_b32 v199, a183\n"
      "v_mul_f32 v196, v196, v195\n"
      "v_mul_f32 v197, v197, v195\n"
      "v_mul_f32 v198, v198, v195\n"
      "v_mul_f32 v199, v199, v195\n"
      "v_accvgpr_write_b32 a180, v196\n"
      "v_accvgpr_write_b32 a181, v197\n"
      "v_accvgpr_write_b32 a182, v198\n"
      "v_accvgpr_write_b32 a183, v199\n"
      "v_accvgpr_read_b32 v196, a184\n"
      "v_accvgpr_read_b32 v197, a185\n"
      "v_accvgpr_read_b32 v198, a186\n"
      "v_accvgpr_read_b32 v199, a187\n"
      "v_mul_f32 v196, v196, v194\n"
      "v_mul_f32 v197, v197, v194\n"
      "v_mul_f32 v198, v198, v194\n"
      "v_mul_f32 v199, v199, v194\n"
      "v_accvgpr_write_b32 a184, v196\n"
      "v_accvgpr_write_b32 a185, v197\n"
      "v_accvgpr_write_b32 a186, v198\n"
      "v_accvgpr_write_b32 a187, v199\n"
      "v_accvgpr_read_b32 v196, a188\n"
      "v_accvgpr_read_b32 v197, a189\n"
      "v_accvgpr_read_b32 v198, a190\n"
      "v_accvgpr_read_b32 v199, a191\n"
      "v_mul_f32 v196, v196, v195\n"
      "v_mul_f32 v197, v197, v195\n"
      "v_mul_f32 v198, v198, v195\n"
      "v_mul_f32 v199, v199, v195\n"
      "v_accvgpr_write_b32 a188, v196\n"
      "v_accvgpr_write_b32 a189, v197\n"
      "v_accvgpr_write_b32 a190, v198\n"
      "v_accvgpr_write_b32 a191, v199\n"
      "v_accvgpr_read_b32 v196, a192\n"
      "v_accvgpr_read_b32 v197, a193\n"
      "v_accvgpr_read_b32 v198, a194\n"
      "v_accvgpr_read_b32 v199, a195\n"
      "v_mul_f32 v196, v196, v194\n"
      "v_mul_f32 v197, v197, v194\n"
      "v_mul_f32 v198, v198, v194\n"
      "v_mul_f32 v199, v199, v194\n"
      "v_accvgpr_write_b32 a192, v196\n"
      "v_accvgpr_write_b32 a193, v197\n"
      "v_accvgpr_write_b32 a194, v198\n"
      "v_accvgpr_write_b32 a195, v199\n"
      "v_accvgpr_read_b32 v196, a196\n"
      "v_accvgpr_read_b32 v197, a197\n"
      "v_accvgpr_read_b32 v198, a198\n"
      "v_accvgpr_read_b32 v199, a199\n"
      "v_mul_f32 v196, v196, v195\n"
      "v_mul_f32 v197, v197, v195\n"
      "v_mul_f32 v198, v198, v195\n"
      "v_mul_f32 v199, v199, v195\n"
      "v_accvgpr_write_b32 a196, v196\n"
      "v_accvgpr_write_b32 a197, v197\n"
      "v_accvgpr_write_b32 a198, v198\n"
      "v_accvgpr_write_b32 a199, v199\n"
      "v_accvgpr_read_b32 v196, a200\n"
      "v_accvgpr_read_b32 v197, a201\n"
      "v_accvgpr_read_b32 v198, a202\n"
      "v_accvgpr_read_b32 v199, a203\n"
      "v_mul_f32 v196, v196, v194\n"
      "v_mul_f32 v197, v197, v194\n"
      "v_mul_f32 v198, v198, v194\n"
      "v_mul_f32 v199, v199, v194\n"
      "v_accvgpr_write_b32 a200, v196\n"
      "v_accvgpr_write_b32 a201, v197\n"
      "v_accvgpr_write_b32 a202, v198\n"
      "v_accvgpr_write_b32 a203, v199\n"
      "v_accvgpr_read_b32 v196, a204\n"
      "v_accvgpr_read_b32 v197, a205\n"
      "v_accvgpr_read_b32 v198, a206\n"
      "v_accvgpr_read_b32 v199, a207\n"
      "v_mul_f32 v196, v196, v195\n"
      "v_mul_f32 v197, v197, v195\n"
      "v_mul_f32 v198, v198, v195\n"
      "v_mul_f32 v199, v199, v195\n"
      "v_accvgpr_write_b32 a204, v196\n"
      "v_accvgpr_write_b32 a205, v197\n"
      "v_accvgpr_write_b32 a206, v198\n"
      "v_accvgpr_write_b32 a207, v199\n"
      "v_mul_f32 v180, v180, v192\n"
      "v_mul_f32 v181, v181, v193\n"
      "v_mul_f32 v182, v182, v194\n"
      "v_mul_f32 v183, v183, v195\n"
      "v_lshlrev_b32 v196, 16, v128\n"
      "v_and_b32 v197, 0xffff0000, v128\n"
      "v_mul_f32 v196, v196, v192\n"
      "v_mul_f32 v197, v197, v192\n"
      "v_cvt_pk_bf16_f32 v128, v196, v197\n"
      "v_lshlrev_b32 v196, 16, v129\n"
      "v_and_b32 v197, 0xffff0000, v129\n"
      "v_mul_f32 v196, v196, v192\n"
      "v_mul_f32 v197, v197, v192\n"
      "v_cvt_pk_bf16_f32 v129, v196, v197\n"
      "v_lshlrev_b32 v196, 16, v130\n"
      "v_and_b32 v197, 0xffff0000, v130\n"
      "v_mul_f32 v196, v196, v192\n"
      "v_mul_f32 v197, v197, v192\n"
      "v_cvt_pk_bf16_f32 v130, v196, v197\n"
      "v_lshlrev_b32 v196, 16, v131\n"
      "v_and_b32 v197, 0xffff0000, v131\n"
      "v_mul_f32 v196, v196, v192\n"
      "v_mul_f32 v197, v197, v192\n"
      "v_cvt_pk_bf16_f32 v131, v196, v197\n"
      "v_lshlrev_b32 v196, 16, v132\n"
      "v_and_b32 v197, 0xffff0000, v132\n"
      "v_mul_f32 v196, v196, v193\n"
      "v_mul_f32 v197, v197, v193\n"
      "v_cvt_pk_bf16_f32 v132, v196, v197\n"
      "v_lshlrev_b32 v196, 16, v133\n"
      "v_and_b32 v197, 0xffff0000, v133\n"
      "v_mul_f32 v196, v196, v193\n"
      "v_mul_f32 v197, v197, v193\n"
      "v_cvt_pk_bf16_f32 v133, v196, v197\n"
      "v_lshlrev_b32 v196, 16, v134\n"
      "v_and_b32 v197, 0xffff0000, v134\n"
      "v_mul_f32 v196, v196, v193\n"
      "v_mul_f32 v197, v197, v193\n"
      "v_cvt_pk_bf16_f32 v134, v196, v197\n"
      "v_lshlrev_b32 v196, 16, v135\n"
      "v_and_b32 v197, 0xffff0000, v135\n"
      "v_mul_f32 v196, v196, v193\n"
      "v_mul_f32 v197, v197, v193\n"
      "v_cvt_pk_bf16_f32 v135, v196, v197\n"
      "v_lshlrev_b32 v196, 16, v136\n"
      "v_and_b32 v197, 0xffff0000, v136\n"
      "v_mul_f32 v196, v196, v194\n"
      "v_mul_f32 v197, v197, v194\n"
      "v_cvt_pk_bf16_f32 v136, v196, v197\n"
      "v_lshlrev_b32 v196, 16, v137\n"
      "v_and_b32 v197, 0xffff0000, v137\n"
      "v_mul_f32 v196, v196, v194\n"
      "v_mul_f32 v197, v197, v194\n"
      "v_cvt_pk_bf16_f32 v137, v196, v197\n"
      "v_lshlrev_b32 v196, 16, v138\n"
      "v_and_b32 v197, 0xffff0000, v138\n"
      "v_mul_f32 v196, v196, v194\n"
      "v_mul_f32 v197, v197, v194\n"
      "v_cvt_pk_bf16_f32 v138, v196, v197\n"
      "v_lshlrev_b32 v196, 16, v139\n"
      "v_and_b32 v197, 0xffff0000, v139\n"
      "v_mul_f32 v196, v196, v194\n"
      "v_mul_f32 v197, v197, v194\n"
      "v_cvt_pk_bf16_f32 v139, v196, v197\n"
      "v_lshlrev_b32 v196, 16, v140\n"
      "v_and_b32 v197, 0xffff0000, v140\n"
      "v_mul_f32 v196, v196, v195\n"
      "v_mul_f32 v197, v197, v195\n"
      "v_cvt_pk_bf16_f32 v140, v196, v197\n"
      "v_lshlrev_b32 v196, 16, v141\n"
      "v_and_b32 v197, 0xffff0000, v141\n"
      "v_mul_f32 v196, v196, v195\n"
      "v_mul_f32 v197, v197, v195\n"
      "v_cvt_pk_bf16_f32 v141, v196, v197\n"
      "v_lshlrev_b32 v196, 16, v142\n"
      "v_and_b32 v197, 0xffff0000, v142\n"
      "v_mul_f32 v196, v196, v195\n"
      "v_mul_f32 v197, v197, v195\n"
      "v_cvt_pk_bf16_f32 v142, v196, v197\n"
      "v_lshlrev_b32 v196, 16, v143\n"
      "v_and_b32 v197, 0xffff0000, v143\n"
      "v_mul_f32 v196, v196, v195\n"
      "v_mul_f32 v197, v197, v195\n"
      "v_cvt_pk_bf16_f32 v143, v196, v197\n"
      "v_exp_f32 v188, v96\n"
      "v_exp_f32 v189, v97\n"
      "v_exp_f32 v190, v98\n"
      "v_exp_f32 v191, v99\n"
      "v_cvt_pk_bf16_f32 v144, v188, v189\n"
      "v_cvt_pk_bf16_f32 v145, v190, v191\n"
      "v_exp_f32 v192, v100\n"
      "v_exp_f32 v193, v101\n"
      "v_exp_f32 v194, v102\n"
      "v_exp_f32 v195, v103\n"
      "v_cvt_pk_bf16_f32 v146, v192, v193\n"
      "v_cvt_pk_bf16_f32 v147, v194, v195\n"
      "v_exp_f32 v188, v104\n"
      "v_exp_f32 v189, v105\n"
      "v_exp_f32 v190, v106\n"
      "v_exp_f32 v191, v107\n"
      "v_cvt_pk_bf16_f32 v148, v188, v189\n"
      "v_cvt_pk_bf16_f32 v149, v190, v191\n"
      "v_exp_f32 v192, v108\n"
      "v_exp_f32 v193, v109\n"
      "v_exp_f32 v194, v110\n"
      "v_exp_f32 v195, v111\n"
      "v_cvt_pk_bf16_f32 v150, v192, v193\n"
      "v_cvt_pk_bf16_f32 v151, v194, v195\n"
      "v_exp_f32 v188, v112\n"
      "v_exp_f32 v189, v113\n"
      "v_exp_f32 v190, v114\n"
      "v_exp_f32 v191, v115\n"
      "v_cvt_pk_bf16_f32 v152, v188, v189\n"
      "v_cvt_pk_bf16_f32 v153, v190, v191\n"
      "v_exp_f32 v192, v116\n"
      "v_exp_f32 v193, v117\n"
      "v_exp_f32 v194, v118\n"
      "v_exp_f32 v195, v119\n"
      "v_cvt_pk_bf16_f32 v154, v192, v193\n"
      "v_cvt_pk_bf16_f32 v155, v194, v195\n"
      "v_exp_f32 v188, v120\n"
      "v_exp_f32 v189, v121\n"
      "v_exp_f32 v190, v122\n"
      "v_exp_f32 v191, v123\n"
      "v_cvt_pk_bf16_f32 v156, v188, v189\n"
      "v_cvt_pk_bf16_f32 v157, v190, v191\n"
      "v_exp_f32 v192, v124\n"
      "v_exp_f32 v193, v125\n"
      "v_exp_f32 v194, v126\n"
      "v_exp_f32 v195, v127\n"
      "v_cvt_pk_bf16_f32 v158, v192, v193\n"
      "v_cvt_pk_bf16_f32 v159, v194, v195\n"
      "s_nop 1\n"
      "v_mfma_f32_16x16x32_bf16 v[184:187], a[128:131], v[144:147], 0\n"
      "v_mfma_f32_16x16x32_bf16 v[184:187], a[132:135], v[148:151], v[184:187]\n"
      "v_mfma_f32_16x16x32_bf16 v[184:187], a[136:139], v[152:155], v[184:187]\n"
      "v_mfma_f32_16x16x32_bf16 v[184:187], a[140:143], v[156:159], v[184:187]\n"
      "s_nop 7\n"
      "s_setpc_b64 s[96:97]\n"
      "SLOW_%=:\n"
      "ds_write_b128 v234, v[176:179] offset:5120\n"
      "s_waitcnt lgkmcnt(0)\n"
      "s_barrier\n"
      "ds_read_b128 v[192:195], v235 offset:5120\n"
      "s_waitcnt lgkmcnt(0)\n"
      "v_sub_f32 v188, v176, v194\n"
      "v_sub_f32 v189, v177, v195\n"
      "v_sub_f32 v190, v178, v192\n"
      "v_sub_f32 v191, v179, v193\n"
      "v_max_f32 v188, 0, v188\n"
      "v_max_f32 v189, 0, v189\n"
      "v_max_f32 v190, 0, v190\n"
      "v_max_f32 v191, 0, v191\n"
      "s_call_b64 s[96:97], RS1_%=\n"
      "ds_write_b128 v232, a[176:179] offset:0\n"
      "ds_write_b128 v232, a[180:183] offset:1024\n"
      "ds_write_b128 v232, a[184:187] offset:2048\n"
      "ds_write_b128 v232, a[188:191] offset:3072\n"
      "ds_write_b128 v232, a[192:195] offset:4096\n"
      "ds_write_b128 v232, a[196:199] offset:5120\n"
      "ds_write_b128 v232, a[200:203] offset:6144\n"
      "ds_write_b128 v232, a[204:207] offset:7168\n"
      "ds_write_b128 v234, v[152:155] offset:0\n"
      "ds_write_b128 v234, v[156:159] offset:1024\n"
      "v_cndmask_b32 v229, v180, v181, s[74:75]\n"
      "v_cndmask_b32 v200, v182, v183, s[74:75]\n"
      "v_cndmask_b32 v228, v176, v177, s[74:75]\n"
      "v_cndmask_b32 v202, v186, v187, s[74:75]\n"
      "v_cndmask_b32 v201, v184, v185, s[74:75]\n"
      "v_add_f32 v200, v200, v202\n"
      "ds_write_b32 v234, v200 offset:4096\n"
      "v_add_f32 v229, v229, v201\n"
      "s_waitcnt lgkmcnt(0)\n"
      "s_barrier\n"
      "ds_read_b32 v208, v209\n"
      "ds_read_b128 v[160:163], v235 offset:0\n"
      "ds_read_b128 v[164:167], v235 offset:1024\n"
      "ds_read_b128 v[64:67], v233 offset:0\n"
      "ds_read_b128 v[68:71], v233 offset:1024\n"
      "ds_read_b128 v[72:75], v233 offset:2048\n"
      "ds_read_b128 v[76:79], v233 offset:3072\n"
      "ds_read_b128 v[80:83], v233 offset:4096\n"
      "ds_read_b128 v[84:87], v233 offset:5120\n"
      "ds_read_b128 v[88:91], v233 offset:6144\n"
      "ds_read_b128 v[92:95], v233 offset:7168\n"
      "ds_read_b32 v204, v235 offset:4096\n"
      "s_branch AGAIN_%=\n"
      ".subsection 0\n"
      : "+{v0}"(tid)
      : [blk] "s"(blockIdx.x), [kp] "s"(__builtin_amdgcn_kernarg_segment_ptr()), "s"(lds0)
      : "v1", "v2", "v3", "v4", "v5", "v6", "v7", "v8", "v9", "v10", "v11", "v12", "v13", "v14", "v15", "v16", "v17", "v18", "v19", "v20", "v21", "v22", "v23", "v24", "v25", "v26", "v27", "v28", "v29", "v30", "v31", "v32", "v33", "v34", "v35", "v36", "v37", "v38", "v39", "v40", "v41", "v42", "v43", "v44", "v45", "v46", "v47", "v48", "v49", "v50", "v51", "v52", "v53", "v54", "v55", "v56", "v57", "v58", "v59", "v60", "v61", "v62", "v63", "v64", "v65", "v66", "v67", "v68", "v69", "v70", "v71", "v72", "v73", "v74", "v75", "v76", "v77", "v78", "v79", "v80", "v81", "v82", "v83", "v84", "v85", "v86", "v87", "v88", "v89", "v90", "v91", "v92", "v93", "v94", "v95", "v96", "v97", "v98", "v99", "v100", "v101", "v102", "v103", "v104", "v105", "v106", "v107", "v108", "v109", "v110", "v111", "v112", "v113", "v114", "v115", "v116", "v117", "v118", "v119", "v120", "v121", "v122", "v123", "v124", "v125", "v126", "v127", "v128", "v129", "v130", "v131", "v132", "v133", "v134", "v135", "v136", "v137", "v138", "v139", "v140", "v141", "v142", "v143", "v144", "v145", "v146", "v147", "v148", "v149", "v150", "v151", "v152", "v153", "v154", "v155", "v156", "v157", "v158", "v159", "v160", "v161", "v162", "v163", "v164", "v165", "v166", "v167", "v168", "v169", "v170", "v171", "v172", "v173", "v174", "v175", "v176", "v177", "v178", "v179", "v180", "v181", "v182", "v183", "v184", "v185", "v186", "v187", "v188", "v189", "v190", "v191", "v192", "v193", "v194", "v195", "v196", "v197", "v198", "v199", "v200", "v201", "v202", "v203", "v204", "v205", "v206", "v207", "v208", "v209", "v210", "v211", "v212", "v213", "v214", "v215", "v216", "v217", "v218", "v219", "v220", "v221", "v222", "v223", "v224", "v225", "v226", "v227", "v228", "v229", "v230", "v231", "v232", "v233", "v234", "v235", "v236", "v237", "v238", "v239", "v240", "v241", "v242", "v243", "v244", "v245", "v246", "v247", "v248", "v249", "v250", "v251", "a0", "a1", "a2", "a3", "a4", "a5", "a6", "a7", "a8", "a9", "a10", "a11", "a12", "a13", "a14", "a15", "a16", "a17", "a18", "a19", "a20", "a21", "a22", "a23", "a24", "a25", "a26", "a27", "a28", "a29", "a30", "a31", "a32", "a33", "a34", "a35", "a36", "a37", "a38", "a39", "a40", "a41", "a42", "a43", "a44", "a45", "a46", "a47", "a48", "a49", "a50", "a51", "a52", "a53", "a54", "a55", "a56", "a57", "a58", "a59", "a60", "a61", "a62", "a63", "a64", "a65", "a66", "a67", "a68", "a69", "a70", "a71", "a72", "a73", "a74", "a75", "a76", "a77", "a78", "a79", "a80", "a81", "a82", "a83", "a84", "a85", "a86", "a87", "a88", "a89", "a90", "a91", "a92", "a93", "a94", "a95", "a96", "a97", "a98", "a99", "a100", "a101", "a102", "a103", "a104", "a105", "a106", "a107", "a108", "a109", "a110", "a111", "a112", "a113", "a114", "a115", "a116", "a117", "a118", "a119", "a120", "a121", "a122", "a123", "a124", "a125", "a126", "a127", "a128", "a129", "a130", "a131", "a132", "a133", "a134", "a135", "a136", "a137", "a138", "a139", "a140", "a141", "a142", "a143", "a144", "a145", "a146", "a147", "a148", "a149", "a150", "a151", "a152", "a153", "a154", "a155", "a156", "a157", "a158", "a159", "a160", "a161", "a162", "a163", "a164", "a165", "a166", "a167", "a168", "a169", "a170", "a171", "a172", "a173", "a174", "a175", "a176", "a177", "a178", "a179", "a180", "a181", "a182", "a183", "a184", "a185", "a186", "a187", "a188", "a189", "a190", "a191", "a192", "a193", "a194", "a195", "a196", "a197", "a198", "a199", "a200", "a201", "a202", "a203", "a204", "a205", "a206", "a207", "s4", "s5", "s6", "s7", "s8", "s9", "s10", "s11", "s12", "s13", "s14", "s15", "s16", "s17", "s18", "s19", "s20", "s21", "s22", "s23", "s24", "s25", "s26", "s27", "s28", "s29", "s30", "s31", "s32", "s33", "s34", "s35", "s38", "s39", "s40", "s43", "s44", "s45", "s46", "s47", "s50", "s52", "s53", "s54", "s55", "s56", "s57", "s58", "s59", "s60", "s61", "s62", "s63", "s64", "s65", "s66", "s67", "s68", "s69", "s70", "s71", "s72", "s74", "s75", "s76", "s77", "s78", "s79", "s80", "s81", "s82", "s83", "s84", "s85", "s86", "s87", "s88", "s89", "s90", "s91", "s92", "s93", "s94", "s95", "s96", "s97", "vcc", "scc", "memory");
}

}

extern "C" void kernel_launch(void* const* d_in, const int* in_sizes, int n_in, void* d_out, int out_size,
                              void* d_ws, size_t ws_size, hipStream_t stream) {
  if (n_in != 6 || ws_size < WS_END) return;
  const __hip_bfloat16* q = (const __hip_bfloat16*)d_in[0];
  const __hip_bfloat16* kc = (const __hip_bfloat16*)d_in[1];
  const __hip_bfloat16* vc = (const __hip_bfloat16*)d_in[2];
  const __hip_bfloat16* kt = (const __hip_bfloat16*)d_in[3];
  const __hip_bfloat16* vt = (const __hip_bfloat16*)d_in[4];
  const __hip_bfloat16* tm = (const __hip_bfloat16*)d_in[5];
  hipLaunchKernelGGL(tree_attn_main, dim3(B_ * HKV_ * 2), dim3(256), 0, stream, q, kc, vc, kt, vt, tm,
                     (__hip_bfloat16*)d_out, (char*)d_ws);
}
```

```cpp
#include <hip/hip_runtime.h>
#include <hip/hip_bf16.h>
#include <stdint.h>

#pragma clang diagnostic ignored "-Wint-to-pointer-cast"
#pragma clang diagnostic ignored "-Winline-asm"

namespace {

constexpr int B_ = 16, HKV_ = 8, D_ = 128;
constexpr int SLOT = 64 * D_ * 2;
constexpr int LDS_BYTES = 10 * SLOT;

constexpr size_t WS_EPOCH = 0;
constexpr size_t WS_REF = WS_EPOCH + 128 * 4 * 128;
constexpr size_t WS_X = WS_REF + 16384;
constexpr size_t XW_BYTES = 4608;
constexpr size_t WS_END = WS_X + (size_t)128 * 2 * 4 * XW_BYTES;

__global__ __launch_bounds__(256, 1)
void tree_attn_main(const __hip_bfloat16*, const __hip_bfloat16*, const __hip_bfloat16*, const __hip_bfloat16*,
                    const __hip_bfloat16*, const __hip_bfloat16*, __hip_bfloat16*, char*) {
  __shared__ __attribute__((aligned(1024))) char smem[LDS_BYTES];
  const unsigned lds0 = (unsigned)(uintptr_t)smem;
  unsigned tid = threadIdx.x;
  asm volatile(
      "s_load_dwordx16 s[4:19], %[kp], 0x0\n"
      "s_getpc_b64 s[60:61]\n"
      "v_lshlrev_b32 v241, 4, v0\n"
      "s_lshl_b32 s62, %[blk], 9\n"
      "s_and_b32 s62, s62, 0x3000\n"
      "s_add_u32 s60, s60, s62\n"
      "s_addc_u32 s61, s61, 0\n"
      "global_load_dwordx4 v[236:239], v241, s[60:61]\n"
      "v_and_b32 v1, 0xc0, v0\n"
      "v_lshlrev_b32 v248, 1, v0\n"
      "v_readfirstlane_b32 s50, v1\n"
      "v_and_b32 v248, 0x60, v248\n"
      "v_and_b32 v249, 0x70, v241\n"
      "v_xor_b32 v248, v248, v249\n"
      "v_lshl_or_b32 v248, v1, 6, v248\n"
      "v_and_b32 v1, 56, v0\n"
      "v_lshl_or_b32 v248, v1, 5, v248\n"
      "s_mov_b32 s64, 0x14100400\n"
      "s_mov_b32 s65, 0x54504440\n"
      "s_lshl_b32 s66, %[blk], 3\n"
      "s_lshr_b64 s[64:65], s[64:65], s66\n"
      "s_mov_b32 s66, 0x3020100\n"
      "s_mov_b32 s67, 0x83828180\n"
      "s_and_b32 s65, %[blk], 56\n"
      "s_lshr_b64 s[66:67], s[66:67], s65\n"
      "s_or_b32 s64, s64, s66\n"
      "s_lshr_b32 s65, %[blk], 3\n"
      "s_and_b32 s65, s65, 24\n"
      "s_lshr_b32 s65, 0x28082000, s65\n"
      "s_or_b32 s64, s64, s65\n"
      "s_and_b32 s63, s64, 0x80\n"
      "s_and_b32 s44, s64, 127\n"
      "v_or_b32 v248, s63, v248\n"
      "v_xor_b32 v249, 0x80, v248\n"
      "s_lshl_b32 s64, s44, 20\n"
      "s_lshl_b32 s62, s44, 15\n"
      "s_lshl_b32 s50, s50, 6\n"
      "s_add_u32 m0, s50, 131072\n"
      "s_mov_b32 s58, 0x120000\n"
      "s_mov_b32 s59, 0x20000\n"
      "s_movk_i32 s22, 0x4000\n"
      "s_waitcnt lgkmcnt(0)\n"
      "s_add_u32 s56, s4, s62\n"
      "s_addc_u32 s57, s5, 0\n"
      "buffer_load_dwordx4 v248, s[56:59], 0 offen nt lds\n"
      "buffer_load_dwordx4 v248, s[56:59], 0 offen offset:2048 nt lds\n"
      "s_bitset1_b32 m0, 10\n"
      "s_memrealtime s[82:83]\n"
      "s_add_u32 s20, s6, s64\n"
      "s_addc_u32 s21, s7, 0\n"
      "s_add_u32 s32, s8, s64\n"
      "s_addc_u32 s33, s9, 0\n"
      "s_load_dword s92, s[20:21], 0x0\n"
      "s_load_dword s92, s[10:11], 0x0\n"
      "s_load_dword s92, s[32:33], 0x0\n"
      "buffer_load_dwordx4 v249, s[56:59], 0 offen nt lds\n"
      "buffer_load_dwordx4 v249, s[56:59], 0 offen offset:2048 nt lds\n"
      "s_add_u32 m0, s50, 147456\n"
      "s_load_dword s92, s[18:19], 0x0\n"
      "buffer_load_dwordx4 v248, s[56:59], s22 offen nt lds\n"
      "buffer_load_dwordx4 v248, s[56:59], s22 offen offset:2048 nt lds\n"
      "s_bitset1_b32 m0, 10\n"
      "s_nop 0\n"
      "buffer_load_dwordx4 v249, s[56:59], s22 offen nt lds\n"
      "buffer_load_dwordx4 v249, s[56:59], s22 offen offset:2048 nt lds\n"
      "s_mov_b64 s[56:57], s[20:21]\n"
      "s_load_dword s92, s[14:15], 0x0\n"
      "s_load_dword s92, s[12:13], 0x0\n"
      "s_lshr_b32 s74, s50, 2\n"
      "s_lshr_b32 s75, s50, 5\n"
      "s_xor_b32 s75, s75, s63\n"
      "s_and_b32 s75, s75, 0x80\n"
      "s_and_b32 s76, s74, 0x800\n"
      "s_add_u32 s75, s75, s76\n"
      "s_add_u32 s70, s63, s50\n"
      "s_sub_u32 s70, s70, s75\n"
      "s_lshr_b32 s75, s50, 1\n"
      "s_lshr_b32 s71, s62, 2\n"
      "s_sub_u32 s71, s71, s75\n"
      "s_ashr_i32 s72, s71, 31\n"
      "s_mov_b32 s23, s59\n"
      "s_mov_b64 s[26:27], s[22:23]\n"
      "s_sub_u32 s20, s56, s70\n"
      "s_subb_u32 s21, s57, 0\n"
      "s_add_u32 m0, s74, 24576\n"
      "s_add_u32 s24, s10, s71\n"
      "buffer_load_dwordx4 v248, s[20:23], 0 offen nt lds\n"
      "s_addc_u32 s25, s11, s72\n"
      "s_add_u32 m0, s75, 16384\n"
      "s_nop 0\n"
      "buffer_load_dwordx4 v248, s[24:27], 0 offen nt lds\n"
      "s_bitset1_b32 m0, 10\n"
      "s_nop 0\n"
      "buffer_load_dwordx4 v249, s[24:27], 0 offen nt lds\n"
      "s_lshr_b32 s43, s63, 7\n"
      "s_lshl_b32 s76, s44, 9\n"
      "s_lshr_b32 s69, s50, 5\n"
      "s_add_u32 s76, s76, s69\n"
      "s_add_u32 s46, s18, s76\n"
      "s_addc_u32 s47, s19, 0\n"
      "s_mul_i32 s76, s43, 0x3000\n"
      "s_lshl_b32 s69, s44, 8\n"
      "s_and_b32 s69, s69, 0x7800\n"
      "s_add_u32 s14, s14, s69\n"
      "s_addc_u32 s15, s15, 0\n"
      "v_mov_b64 v[192:193], 0\n"
      "v_mov_b64 v[194:195], 0\n"
      "v_lshlrev_b32 v3, 6, v0\n"
      "v_and_b32 v3, 0x3c0, v3\n"
      "v_lshrrev_b32 v4, 1, v0\n"
      "v_and_or_b32 v211, v4, 24, v3\n"
      "v_mov_b64 v[200:201], 0\n"
      "v_mov_b64 v[202:203], 0\n"
      "v_mov_b64 v[204:205], 0\n"
      "v_mov_b64 v[206:207], 0\n"
      "s_cmp_lg_u32 s76, s50\n"
      "s_cbranch_scc1 NOMASK_%=\n"
      "global_load_dwordx2 v[200:201], v211, s[14:15] offset:0\n"
      "global_load_dwordx2 v[202:203], v211, s[14:15] offset:32\n"
      "global_load_dwordx2 v[204:205], v211, s[14:15] offset:1024\n"
      "global_load_dwordx2 v[206:207], v211, s[14:15] offset:1056\n"
      "NOMASK_%=:\n"
      "global_load_dword v208, v192, s[46:47] sc1\n"
      "s_mul_i32 s69, s43, 0x8000\n"
      "s_bitcmp1_b32 s44, 0\n"
      "s_cselect_b32 s66, 0x8000, 0\n"
      "s_xor_b32 s69, s69, s66\n"
      "v_and_b32 v228, 0xff, v248\n"
      "v_xor_b32 v228, s69, v228\n"
      "v_bfe_i32 v2, v0, 3, 1\n"
      "v_and_b32 v2, 0x1000, v2\n"
      "v_xor_b32 v228, v228, v2\n"
      "v_bfe_i32 v2, v0, 4, 1\n"
      "v_and_b32 v2, 0x20400, v2\n"
      "v_xor_b32 v228, v228, v2\n"
      "v_bfe_i32 v2, v0, 5, 1\n"
      "v_and_b32 v2, 0x100, v2\n"
      "v_xor_b32 v228, v228, v2\n"
      "v_bfe_i32 v2, v0, 6, 1\n"
      "v_and_b32 v2, 0x400, v2\n"
      "v_xor_b32 v228, v228, v2\n"
      "v_bfe_i32 v2, v0, 7, 1\n"
      "v_and_b32 v2, 0x2000, v2\n"
      "v_xor_b32 v228, v228, v2\n"
      "v_xor_b32 v231, 0x280, v228\n"
      "v_xor_b32 v230, 0x200, v228\n"
      "v_xor_b32 v229, 0x80, v228\n"
      "v_add_u32 v228, 0xc00, v228\n"
      "v_add_u32 v229, 0x800, v229\n"
      "v_add_u32 v230, 0x400, v230\n"
      "s_sub_u32 s56, s56, 0xc00\n"
      "s_subb_u32 s57, s57, 0\n"
      "s_sub_u32 s32, s32, 0xc00\n"
      "s_subb_u32 s33, s33, 0\n"
      "s_xor_b64 s[32:33], s[32:33], s[56:57]\n"
      "s_mov_b32 s69, 0\n"
      "s_bitcmp1_b32 s44, 4\n"
      "s_cselect_b32 s66, 0x10000, 0\n"
      "s_xor_b32 s69, s69, s66\n"
      "s_bitcmp1_b32 s44, 0\n"
      "s_cselect_b32 s66, 0x800, 0\n"
      "s_xor_b32 s69, s69, s66\n"
      "s_bitcmp1_b32 s44, 2\n"
      "s_cselect_b32 s66, 0x40800, 0\n"
      "s_xor_b32 s69, s69, s66\n"
      "s_bitcmp1_b32 s44, 4\n"
      "s_cselect_b32 s66, 0x4000, 0\n"
      "s_xor_b32 s69, s69, s66\n"
      "s_mov_b32 s60, s69\n"
      "s_mov_b32 s62, s50\n"
      "s_mov_b64 s[30:31], s[22:23]\n"
      "s_add_u32 s28, s12, s71\n"
      "s_addc_u32 s29, s13, s72\n"
      "s_lshl_b32 s69, s43, 13\n"
      "s_xor_b32 s69, s69, 0x2000\n"
      "v_add_u32 v2, s69, v241\n"
      "ds_write_b128 v241, v[192:195] offset:28672\n"
      "ds_write_b128 v2, v[192:195] offset:49152\n"
      "ds_write_b128 v2, v[192:195] offset:53248\n"
      "v_and_b32 v241, 0x3f0, v241\n"
      "s_and_b32 s91, s50, 8192\n"
      "v_lshlrev_b32 v4, 8, v0\n"
      "v_and_b32 v4, 0x800, v4\n"
      "v_lshlrev_b32 v5, 7, v0\n"
      "v_and_b32 v6, 0x380, v5\n"
      "v_or3_b32 v4, v4, v6, s91\n"
      "v_and_b32 v6, 0x60, v241\n"
      "v_and_b32 v2, 48, v0\n"
      "v_xor_b32 v6, v6, v2\n"
      "v_or_b32 v242, v4, v6\n"
      "v_xor_b32 v243, 64, v242\n"
      "v_lshlrev_b32 v4, 6, v0\n"
      "v_and_b32 v6, 0x800, v4\n"
      "v_lshlrev_b32 v2, 5, v0\n"
      "v_and_b32 v2, 0x380, v2\n"
      "s_add_u32 s69, s91, 16384\n"
      "v_or3_b32 v6, v6, v2, s69\n"
      "v_lshlrev_b32 v2, 3, v0\n"
      "v_and_or_b32 v6, v2, 24, v6\n"
      "v_lshlrev_b32 v2, 2, v0\n"
      "v_and_b32 v2, 0x60, v2\n"
      "v_xor_b32 v244, v6, v2\n"
      "v_xor_b32 v245, 0x20, v244\n"
      "v_xor_b32 v246, 0x40, v244\n"
      "v_xor_b32 v247, 0x60, v244\n"
      "v_and_b32 v5, 0x1e00, v5\n"
      "v_and_or_b32 v250, v241, 48, v5\n"
      "s_lshl_b32 s69, %[blk], 9\n"
      "s_and_b32 s69, s69, 0x1e00\n"
      "v_and_b32 v4, 0x1c0, v4\n"
      "v_or_b32 v251, s69, v4\n"
      "s_and_b32 s69, s50, 4096\n"
      "s_lshl_b32 s69, s69, 2\n"
      "s_add_u32 s69, s69, 131072\n"
      "s_sub_u32 s91, 16384, s91\n"
      "v_add_u32 v196, s69, v242\n"
      "v_add_u32 v198, s91, v242\n"
      "v_xor_b32 v214, 0x2000, v196\n"
      "v_add_u32 v197, s69, v243\n"
      "v_add_u32 v199, s91, v243\n"
      "v_xor_b32 v215, 0x2000, v197\n"
      "v_and_b32 v3, 1, v0\n"
      "v_bfe_u32 v4, v0, 1, 5\n"
      "v_lshlrev_b32 v3, 1, v3\n"
      "v_sub_u32 v4, v4, v3\n"
      "v_mov_b32 v219, s60\n"
      "v_bfe_i32 v2, v4, 0, 1\n"
      "v_and_b32 v2, 0x10000, v2\n"
      "v_xor_b32 v219, v219, v2\n"
      "v_bfe_i32 v2, v4, 1, 1\n"
      "v_and_b32 v2, 0x800, v2\n"
      "v_xor_b32 v219, v219, v2\n"
      "v_bfe_i32 v2, v4, 2, 1\n"
      "v_and_b32 v2, 0x40800, v2\n"
      "v_xor_b32 v219, v219, v2\n"
      "v_bfe_i32 v2, v4, 3, 1\n"
      "v_and_b32 v2, 0x80000, v2\n"
      "v_xor_b32 v219, v219, v2\n"
      "v_bfe_i32 v2, v4, 4, 1\n"
      "v_and_b32 v2, 0x4000, v2\n"
      "v_xor_b32 v219, v219, v2\n"
      "v_writelane_b32 v219, 0, 1\n"
      "s_mov_b32 s61, 0\n"
      "s_add_u32 s69, s50, 131072\n"
      "v_add_u32 v210, s69, v241\n"
      "v_and_b32 v2, 3, v0\n"
      "v_mov_b32 v4, 0x3f803f80\n"
      "v_cmp_eq_u32 vcc, 0, v2\n"
      "s_nop 1\n"
      "v_cndmask_b32 v5, 0, v4, vcc\n"
      "v_accvgpr_write_b32 a128, v5\n"
      "v_accvgpr_write_b32 a129, v5\n"
      "v_accvgpr_write_b32 a130, v5\n"
      "v_accvgpr_write_b32 a131, v5\n"
      "v_cmp_eq_u32 vcc, 1, v2\n"
      "s_nop 1\n"
      "v_cndmask_b32 v5, 0, v4, vcc\n"
      "v_accvgpr_write_b32 a132, v5\n"
      "v_accvgpr_write_b32 a133, v5\n"
      "v_accvgpr_write_b32 a134, v5\n"
      "v_accvgpr_write_b32 a135, v5\n"
      "v_cmp_eq_u32 vcc, 2, v2\n"
      "s_nop 1\n"
      "v_cndmask_b32 v5, 0, v4, vcc\n"
      "v_accvgpr_write_b32 a136, v5\n"
      "v_accvgpr_write_b32 a137, v5\n"
      "v_accvgpr_write_b32 a138, v5\n"
      "v_accvgpr_write_b32 a139, v5\n"
      "v_cmp_eq_u32 vcc, 3, v2\n"
      "s_nop 1\n"
      "v_cndmask_b32 v5, 0, v4, vcc\n"
      "v_accvgpr_write_b32 a140, v5\n"
      "v_accvgpr_write_b32 a141, v5\n"
      "v_accvgpr_write_b32 a142, v5\n"
      "v_accvgpr_write_b32 a143, v5\n"
      "v_mfma_f32_32x32x16_bf16 a[144:159], v[192:195], v[192:195], 0\n"
      "v_mfma_f32_32x32x16_bf16 a[160:175], v[192:195], v[192:195], 0\n"
      "v_mfma_f32_32x32x16_bf16 a[176:191], v[192:195], v[192:195], 0\n"
      "v_mfma_f32_32x32x16_bf16 a[192:207], v[192:195], v[192:195], 0\n"
      "v_mfma_f32_32x32x16_bf16 v[0:15], v[192:195], v[192:195], 0\n"
      "v_mfma_f32_32x32x16_bf16 v[16:31], v[192:195], v[192:195], 0\n"
      "v_mfma_f32_32x32x16_bf16 v[32:47], v[192:195], v[192:195], 0\n"
      "v_mfma_f32_32x32x16_bf16 v[48:63], v[192:195], v[192:195], 0\n"
      "v_mfma_f32_32x32x16_bf16 v[128:143], v[192:195], v[192:195], 0\n"
      "v_mov_b64 v[180:181], 0\n"
      "v_mov_b64 v[182:183], 0\n"
      "v_mov_b64 v[184:185], 0\n"
      "v_mov_b64 v[186:187], 0\n"
      "s_mov_b32 s94, 0x3e0293ee\n"
      "s_mov_b32 s95, s94\n"
      "s_lshl_b32 s69, s43, 13\n"
      "s_add_u32 s45, s69, s75\n"
      "s_mov_b32 m0, s62\n"
      "s_nop 0\n"
      "buffer_load_dwordx4 v228, s[56:59], s60 offen nt lds\n"
      "buffer_load_dwordx4 v229, s[56:59], s60 offen offset:1024 nt lds\n"
      "buffer_load_dwordx4 v230, s[56:59], s60 offen offset:2048 nt lds\n"
      "buffer_load_dwordx4 v231, s[56:59], s60 offen offset:3072 nt lds\n"
      "s_xor_b64 s[56:57], s[56:57], s[32:33]\n"
      "s_add_u32 s61, s61, 1\n"
      "s_add_u32 s62, s62, 0x4000\n"
      "s_and_b32 s62, s62, 0x1ffff\n"
      "v_readlane_b32 s60, v219, s61\n"
      "s_xor_b64 s[56:57], s[56:57], s[32:33]\n"
      "s_add_u32 s61, s61, 1\n"
      "s_add_u32 s62, s62, 0x4000\n"
      "s_and_b32 s62, s62, 0x1ffff\n"
      "v_readlane_b32 s60, v219, s61\n"
      "s_waitcnt vmcnt(8)\n"
      "s_memrealtime s[68:69]\n"
      "ds_read_b128 v[64:67], v210 offset:0\n"
      "ds_read_b128 v[68:71], v210 offset:1024\n"
      "ds_read_b128 v[72:75], v210 offset:2048\n"
      "ds_read_b128 v[76:79], v210 offset:3072\n"
      "ds_read_b128 v[80:83], v210 offset:16384\n"
      "ds_read_b128 v[84:87], v210 offset:17408\n"
      "ds_read_b128 v[88:91], v210 offset:18432\n"
      "ds_read_b128 v[92:95], v210 offset:19456\n"
      "s_add_u32 s20, s18, 65536\n"
      "s_addc_u32 s21, s19, 0\n"
      "s_lshl_b32 s75, %[blk], 6\n"
      "s_add_u32 s75, s75, s50\n"
      "s_waitcnt lgkmcnt(0)\n"
      "s_sub_u32 s67, s68, s82\n"
      "s_min_u32 s67, s67, 165\n"
      "s_add_u32 s93, s82, s67\n"
      "s_sub_u32 s71, s68, 400\n"
      "s_cmp_lt_u32 s75, 512\n"
      "s_cbranch_scc0 NOLEAD_%=\n"
      "v_mov_b32 v188, s68\n"
      "v_mov_b32 v189, s68\n"
      "v_mov_b32 v190, s68\n"
      "v_mov_b32 v191, s68\n"
      "buffer_store_dwordx4 v[188:191], v250, s[20:23], s75 offen sc1\n"
      "s_nop 1\n"
      "NOLEAD_%=:\n"
      "s_mov_b32 m0, s62\n"
      "s_nop 0\n"
      "buffer_load_dwordx4 v228, s[56:59], s60 offen nt lds\n"
      "buffer_load_dwordx4 v229, s[56:59], s60 offen offset:1024 nt lds\n"
      "buffer_load_dwordx4 v230, s[56:59], s60 offen offset:2048 nt lds\n"
      "buffer_load_dwordx4 v231, s[56:59], s60 offen offset:3072 nt lds\n"
      "s_xor_b64 s[56:57], s[56:57], s[32:33]\n"
      "s_add_u32 s61, s61, 1\n"
      "s_add_u32 s62, s62, 0x4000\n"
      "s_and_b32 s62, s62, 0x1ffff\n"
      "v_readlane_b32 s60, v219, s61\n"
      "s_xor_b64 s[56:57], s[56:57], s[32:33]\n"
      "s_add_u32 s61, s61, 1\n"
      "s_add_u32 s62, s62, 0x4000\n"
      "s_and_b32 s62, s62, 0x1ffff\n"
      "v_readlane_b32 s60, v219, s61\n"
      "s_add_u32 m0, s45, 49152\n"
      "s_nop 0\n"
      "buffer_load_dwordx4 v248, s[28:31], 0 offen nt lds\n"
      "s_bitset1_b32 m0, 10\n"
      "s_nop 0\n"
      "buffer_load_dwordx4 v249, s[28:31], 0 offen nt lds\n"
      "v_lshlrev_b32 v188, 16, v64\n"
      "v_and_b32 v189, 0xffff0000, v64\n"
      "v_lshlrev_b32 v190, 16, v65\n"
      "v_and_b32 v191, 0xffff0000, v65\n"
      "v_lshlrev_b32 v192, 16, v66\n"
      "v_and_b32 v193, 0xffff0000, v66\n"
      "v_lshlrev_b32 v194, 16, v67\n"
      "v_and_b32 v195, 0xffff0000, v67\n"
      "v_pk_mul_f32 v[188:189], v[188:189], s[94:95]\n"
      "v_pk_mul_f32 v[190:191], v[190:191], s[94:95]\n"
      "v_pk_mul_f32 v[192:193], v[192:193], s[94:95]\n"
      "v_pk_mul_f32 v[194:195], v[194:195], s[94:95]\n"
      "v_cvt_pk_bf16_f32 v64, v188, v189\n"
      "v_cvt_pk_bf16_f32 v65, v190, v191\n"
      "v_cvt_pk_bf16_f32 v66, v192, v193\n"
      "v_cvt_pk_bf16_f32 v67, v194, v195\n"
      "ds_write_b128 v210, v[64:67] offset:0\n"
      "v_lshlrev_b32 v188, 16, v68\n"
      "v_and_b32 v189, 0xffff0000, v68\n"
      "v_lshlrev_b32 v190, 16, v69\n"
      "v_and_b32 v191, 0xffff0000, v69\n"
      "v_lshlrev_b32 v192, 16, v70\n"
      "v_and_b32 v193, 0xffff0000, v70\n"
      "v_lshlrev_b32 v194, 16, v71\n"
      "v_and_b32 v195, 0xffff0000, v71\n"
      "v_pk_mul_f32 v[188:189], v[188:189], s[94:95]\n"
      "v_pk_mul_f32 v[190:191], v[190:191], s[94:95]\n"
      "v_pk_mul_f32 v[192:193], v[192:193], s[94:95]\n"
      "v_pk_mul_f32 v[194:195], v[194:195], s[94:95]\n"
      "v_cvt_pk_bf16_f32 v68, v188, v189\n"
      "v_cvt_pk_bf16_f32 v69, v190, v191\n"
      "v_cvt_pk_bf16_f32 v70, v192, v193\n"
      "v_cvt_pk_bf16_f32 v71, v194, v195\n"
      "ds_write_b128 v210, v[68:71] offset:1024\n"
      "v_lshlrev_b32 v188, 16, v72\n"
      "v_and_b32 v189, 0xffff0000, v72\n"
      "v_lshlrev_b32 v190, 16, v73\n"
      "v_and_b32 v191, 0xffff0000, v73\n"
      "v_lshlrev_b32 v192, 16, v74\n"
      "v_and_b32 v193, 0xffff0000, v74\n"
      "v_lshlrev_b32 v194, 16, v75\n"
      "v_and_b32 v195, 0xffff0000, v75\n"
      "v_pk_mul_f32 v[188:189], v[188:189], s[94:95]\n"
      "v_pk_mul_f32 v[190:191], v[190:191], s[94:95]\n"
      "v_pk_mul_f32 v[192:193], v[192:193], s[94:95]\n"
      "v_pk_mul_f32 v[194:195], v[194:195], s[94:95]\n"
      "v_cvt_pk_bf16_f32 v72, v188, v189\n"
      "v_cvt_pk_bf16_f32 v73, v190, v191\n"
      "v_cvt_pk_bf16_f32 v74, v192, v193\n"
      "v_cvt_pk_bf16_f32 v75, v194, v195\n"
      "ds_write_b128 v210, v[72:75] offset:2048\n"
      "v_lshlrev_b32 v188, 16, v76\n"
      "v_and_b32 v189, 0xffff0000, v76\n"
      "v_lshlrev_b32 v190, 16, v77\n"
      "v_and_b32 v191, 0xffff0000, v77\n"
      "v_lshlrev_b32 v192, 16, v78\n"
      "v_and_b32 v193, 0xffff0000, v78\n"
      "v_lshlrev_b32 v194, 16, v79\n"
      "v_and_b32 v195, 0xffff0000, v79\n"
      "v_pk_mul_f32 v[188:189], v[188:189], s[94:95]\n"
      "v_pk_mul_f32 v[190:191], v[190:191], s[94:95]\n"
      "v_pk_mul_f32 v[192:193], v[192:193], s[94:95]\n"
      "v_pk_mul_f32 v[194:195], v[194:195], s[94:95]\n"
      "v_cvt_pk_bf16_f32 v76, v188, v189\n"
      "v_cvt_pk_bf16_f32 v77, v190, v191\n"
      "v_cvt_pk_bf16_f32 v78, v192, v193\n"
      "v_cvt_pk_bf16_f32 v79, v194, v195\n"
      "ds_write_b128 v210, v[76:79] offset:3072\n"
      "v_lshlrev_b32 v188, 16, v80\n"
      "v_and_b32 v189, 0xffff0000, v80\n"
      "v_lshlrev_b32 v190, 16, v81\n"
      "v_and_b32 v191, 0xffff0000, v81\n"
      "v_lshlrev_b32 v192, 16, v82\n"
      "v_and_b32 v193, 0xffff0000, v82\n"
      "v_lshlrev_b32 v194, 16, v83\n"
      "v_and_b32 v195, 0xffff0000, v83\n"
      "v_pk_mul_f32 v[188:189], v[188:189], s[94:95]\n"
      "v_pk_mul_f32 v[190:191], v[190:191], s[94:95]\n"
      "v_pk_mul_f32 v[192:193], v[192:193], s[94:95]\n"
      "v_pk_mul_f32 v[194:195], v[194:195], s[94:95]\n"
      "v_cvt_pk_bf16_f32 v80, v188, v189\n"
      "v_cvt_pk_bf16_f32 v81, v190, v191\n"
      "v_cvt_pk_bf16_f32 v82, v192, v193\n"
      "v_cvt_pk_bf16_f32 v83, v194, v195\n"
      "ds_write_b128 v210, v[80:83] offset:16384\n"
      "v_lshlrev_b32 v188, 16, v84\n"
      "v_and_b32 v189, 0xffff0000, v84\n"
      "v_lshlrev_b32 v190, 16, v85\n"
      "v_and_b32 v191, 0xffff0000, v85\n"
      "v_lshlrev_b32 v192, 16, v86\n"
      "v_and_b32 v193, 0xffff0000, v86\n"
      "v_lshlrev_b32 v194, 16, v87\n"
      "v_and_b32 v195, 0xffff0000, v87\n"
      "v_pk_mul_f32 v[188:189], v[188:189], s[94:95]\n"
      "v_pk_mul_f32 v[190:191], v[190:191], s[94:95]\n"
      "v_pk_mul_f32 v[192:193], v[192:193], s[94:95]\n"
      "v_pk_mul_f32 v[194:195], v[194:195], s[94:95]\n"
      "v_cvt_pk_bf16_f32 v84, v188, v189\n"
      "v_cvt_pk_bf16_f32 v85, v190, v191\n"
      "v_cvt_pk_bf16_f32 v86, v192, v193\n"
      "v_cvt_pk_bf16_f32 v87, v194, v195\n"
      "ds_write_b128 v210, v[84:87] offset:17408\n"
      "v_lshlrev_b32 v188, 16, v88\n"
      "v_and_b32 v189, 0xffff0000, v88\n"
      "v_lshlrev_b32 v190, 16, v89\n"
      "v_and_b32 v191, 0xffff0000, v89\n"
      "v_lshlrev_b32 v192, 16, v90\n"
      "v_and_b32 v193, 0xffff0000, v90\n"
      "v_lshlrev_b32 v194, 16, v91\n"
      "v_and_b32 v195, 0xffff0000, v91\n"
      "v_pk_mul_f32 v[188:189], v[188:189], s[94:95]\n"
      "v_pk_mul_f32 v[190:191], v[190:191], s[94:95]\n"
      "v_pk_mul_f32 v[192:193], v[192:193], s[94:95]\n"
      "v_pk_mul_f32 v[194:195], v[194:195], s[94:95]\n"
      "v_cvt_pk_bf16_f32 v88, v188, v189\n"
      "v_cvt_pk_bf16_f32 v89, v190, v191\n"
      "v_cvt_pk_bf16_f32 v90, v192, v193\n"
      "v_cvt_pk_bf16_f32 v91, v194, v195\n"
      "ds_write_b128 v210, v[88:91] offset:18432\n"
      "v_lshlrev_b32 v188, 16, v92\n"
      "v_and_b32 v189, 0xffff0000, v92\n"
      "v_lshlrev_b32 v190, 16, v93\n"
      "v_and_b32 v191, 0xffff0000, v93\n"
      "v_lshlrev_b32 v192, 16, v94\n"
      "v_and_b32 v193, 0xffff0000, v94\n"
      "v_lshlrev_b32 v194, 16, v95\n"
      "v_and_b32 v195, 0xffff0000, v95\n"
      "v_pk_mul_f32 v[188:189], v[188:189], s[94:95]\n"
      "v_pk_mul_f32 v[190:191], v[190:191], s[94:95]\n"
      "v_pk_mul_f32 v[192:193], v[192:193], s[94:95]\n"
      "v_pk_mul_f32 v[194:195], v[194:195], s[94:95]\n"
      "v_cvt_pk_bf16_f32 v92, v188, v189\n"
      "v_cvt_pk_bf16_f32 v93, v190, v191\n"
      "v_cvt_pk_bf16_f32 v94, v192, v193\n"
      "v_cvt_pk_bf16_f32 v95, v194, v195\n"
      "ds_write_b128 v210, v[92:95] offset:19456\n"
      "s_mov_b32 m0, s62\n"
      "s_nop 0\n"
      "buffer_load_dwordx4 v228, s[56:59], s60 offen nt lds\n"
      "buffer_load_dwordx4 v229, s[56:59], s60 offen offset:1024 nt lds\n"
      "buffer_load_dwordx4 v230, s[56:59], s60 offen offset:2048 nt lds\n"
      "buffer_load_dwordx4 v231, s[56:59], s60 offen offset:3072 nt lds\n"
      "s_xor_b64 s[56:57], s[56:57], s[32:33]\n"
      "s_add_u32 s61, s61, 1\n"
      "s_add_u32 s62, s62, 0x4000\n"
      "s_and_b32 s62, s62, 0x1ffff\n"
      "v_readlane_b32 s60, v219, s61\n"
      "s_cmp_eq_u32 s50, 0\n"
      "s_cselect_b32 s38, 0x41554001, 1\n"
      "s_cselect_b32 s39, 0x3f440083, 3\n"
      "s_cselect_b32 s52, 0x3133aa90, 0\n"
      "s_cselect_b32 s53, 0x80011, 0\n"
      "s_mov_b64 s[34:35], 0\n"
      "s_and_b32 s69, %[blk], 7\n"
      "s_mul_i32 s69, s69, 5\n"
      "s_lshr_b32 s74, %[blk], 4\n"
      "s_add_u32 s69, s69, s74\n"
      "s_and_b32 s69, s69, 15\n"
      "s_mul_i32 s74, s69, 16\n"
      "s_lshr_b32 s74, s74, 6\n"
      "s_lshl2_add_u32 s76, s74, 9\n"
      "s_bitset1_b64 s[34:35], s76\n"
      "s_lshl_b32 s74, s74, 1\n"
      "s_sub_u32 s74, 28, s74\n"
      "s_bitset1_b32 s39, s74\n"
      "s_mul_i32 s74, s69, 20\n"
      "s_lshr_b32 s74, s74, 6\n"
      "s_lshl2_add_u32 s76, s74, 25\n"
      "s_bitset1_b64 s[34:35], s76\n"
      "s_lshl_b32 s74, s74, 1\n"
      "s_sub_u32 s74, 20, s74\n"
      "s_bitset1_b32 s39, s74\n"
      "s_mul_i32 s74, s69, 20\n"
      "s_lshr_b32 s74, s74, 6\n"
      "s_lshl2_add_u32 s76, s74, 45\n"
      "s_bitset1_b64 s[34:35], s76\n"
      "s_lshl_b32 s74, s74, 1\n"
      "s_sub_u32 s74, 10, s74\n"
      "s_bitset1_b32 s39, s74\n"
      "s_mov_b32 s87, 0\n"
      "s_mov_b32 s55, 0\n"
      "s_mov_b32 s54, 3\n"
      "s_mov_b32 s70, 250\n"
      "s_mov_b32 s86, 59\n"
      "s_mov_b32 s77, 177\n"
      "s_lshl_b32 s91, s50, 16\n"
      "s_sub_u32 s80, 46, s91\n"
      "s_sub_u32 s90, 34, s91\n"
      "s_sub_u32 s72, s75, 14336\n"
      "s_and_b32 s79, %[blk], 31\n"
      "s_lshl_b32 s79, s79, 8\n"
      "s_add_u32 s79, s79, 8192\n"
      "s_andn2_b32 s69, %[blk], 0\n"
      "s_mul_i32 s69, s69, 53\n"
      "s_lshr_b32 s69, s69, 8\n"
      "s_add_u32 s66, s93, 150\n"
      "s_sub_u32 s81, s93, s71\n"
      "s_sub_u32 s81, s81, s69\n"
      "s_add_u32 s81, s81, 24\n"
      "s_sub_u32 s66, s66, s91\n"
      "s_mov_b32 s88, s66\n"
      "s_mov_b32 s65, s66\n"
      "s_getpc_b64 s[84:85]\n"
      "PC_%=:\n"
      "s_add_u32 s84, s84, HOOK_%=-PC_%=\n"
      "s_addc_u32 s85, s85, 0\n"
      "s_waitcnt vmcnt(14) lgkmcnt(0)\n"
      "s_barrier\n"
      "ds_read_b128 a[64:67], v198 offset:8192\n"
      "ds_read_b128 a[72:75], v199 offset:8192\n"
      "ds_read_b128 a[80:83], v198 offset:9216\n"
      "ds_read_b128 a[88:91], v199 offset:9216\n"
      "ds_read_b128 a[0:3], v196 offset:0\n"
      "ds_read_b128 a[16:19], v197 offset:0\n"
      "ds_read_b128 a[32:35], v196 offset:1024\n"
      "ds_read_b128 a[48:51], v197 offset:1024\n"
      "ds_read_b128 a[4:7], v196 offset:4096\n"
      "ds_read_b128 a[20:23], v197 offset:4096\n"
      "ds_read_b128 a[36:39], v196 offset:5120\n"
      "ds_read_b128 a[52:55], v197 offset:5120\n"
      "v_readfirstlane_b32 s40, v208\n"
      "s_mov_b32 s94, 0xceabfb8f\n"
      "v_lshlrev_b32 v144, 16, v200\n"
      "v_and_b32 v145, 0xffff0000, v200\n"
      "v_lshlrev_b32 v146, 16, v201\n"
      "v_and_b32 v147, 0xffff0000, v201\n"
      "v_lshlrev_b32 v148, 16, v202\n"
      "v_and_b32 v149, 0xffff0000, v202\n"
      "v_lshlrev_b32 v150, 16, v203\n"
      "v_and_b32 v151, 0xffff0000, v203\n"
      "v_lshlrev_b32 v152, 16, v204\n"
      "v_and_b32 v153, 0xffff0000, v204\n"
      "v_lshlrev_b32 v154, 16, v205\n"
      "v_and_b32 v155, 0xffff0000, v205\n"
      "v_lshlrev_b32 v156, 16, v206\n"
      "v_and_b32 v157, 0xffff0000, v206\n"
      "v_lshlrev_b32 v158, 16, v207\n"
      "v_and_b32 v159, 0xffff0000, v207\n"
      "s_waitcnt lgkmcnt(4)\n"
      "v_mfma_f32_16x16x32_bf16 v[64:67], a[64:67], a[0:3], 0\n"
      "ds_read_b128 a[8:11], v214 offset:0\n"
      "v_fma_f32 v144, -v144, s94, s94\n"
      "v_mfma_f32_16x16x32_bf16 v[64:67], a[72:75], a[16:19], v[64:67]\n"
      "ds_read_b128 a[24:27], v215 offset:0\n"
      "v_fma_f32 v145, -v145, s94, s94\n"
      "v_mfma_f32_16x16x32_bf16 v[64:67], a[80:83], a[32:35], v[64:67]\n"
      "ds_read_b128 a[40:43], v214 offset:1024\n"
      "v_fma_f32 v146, -v146, s94, s94\n"
      "v_mfma_f32_16x16x32_bf16 v[64:67], a[88:91], a[48:51], v[64:67]\n"
      "ds_read_b128 a[56:59], v215 offset:1024\n"
      "v_fma_f32 v147, -v147, s94, s94\n"
      "s_waitcnt lgkmcnt(4)\n"
      "v_mfma_f32_16x16x32_bf16 v[72:75], a[64:67], a[4:7], 0\n"
      "ds_read_b128 a[12:15], v214 offset:4096\n"
      "v_fma_f32 v148, -v148, s94, s94\n"
      "v_mfma_f32_16x16x32_bf16 v[72:75], a[72:75], a[20:23], v[72:75]\n"
      "ds_read_b128 a[28:31], v215 offset:4096\n"
      "v_fma_f32 v149, -v149, s94, s94\n"
      "v_mfma_f32_16x16x32_bf16 v[72:75], a[80:83], a[36:39], v[72:75]\n"
      "ds_read_b128 a[44:47], v214 offset:5120\n"
      "v_fma_f32 v150, -v150, s94, s94\n"
      "v_mfma_f32_16x16x32_bf16 v[72:75], a[88:91], a[52:55], v[72:75]\n"
      "ds_read_b128 a[60:63], v215 offset:5120\n"
      "v_fma_f32 v151, -v151, s94, s94\n"
      "s_waitcnt lgkmcnt(4)\n"
      "v_mfma_f32_16x16x32_bf16 v[80:83], a[64:67], a[8:11], 0\n"
      "ds_read_b128 a[96:99], v198 offset:0\n"
      "v_fma_f32 v152, -v152, s94, s94\n"
      "v_mfma_f32_16x16x32_bf16 v[80:83], a[72:75], a[24:27], v[80:83]\n"
      "ds_read_b128 a[100:103], v198 offset:4096\n"
      "v_fma_f32 v153, -v153, s94, s94\n"
      "v_mfma_f32_16x16x32_bf16 v[80:83], a[80:83], a[40:43], v[80:83]\n"
      "ds_read_b128 a[104:107], v199 offset:0\n"
      "v_fma_f32 v154, -v154, s94, s94\n"
      "v_mfma_f32_16x16x32_bf16 v[80:83], a[88:91], a[56:59], v[80:83]\n"
      "ds_read_b128 a[108:111], v199 offset:4096\n"
      "v_fma_f32 v155, -v155, s94, s94\n"
      "s_waitcnt lgkmcnt(4)\n"
      "v_mfma_f32_16x16x32_bf16 v[88:91], a[64:67], a[12:15], 0\n"
      "ds_read_b128 a[112:115], v198 offset:1024\n"
      "v_fma_f32 v156, -v156, s94, s94\n"
      "v_mfma_f32_16x16x32_bf16 v[88:91], a[72:75], a[28:31], v[88:91]\n"
      "ds_read_b128 a[116:119], v198 offset:5120\n"
      "v_fma_f32 v157, -v157, s94, s94\n"
      "v_mfma_f32_16x16x32_bf16 v[88:91], a[80:83], a[44:47], v[88:91]\n"
      "ds_read_b128 a[120:123], v199 offset:1024\n"
      "v_fma_f32 v158, -v158, s94, s94\n"
      "v_mfma_f32_16x16x32_bf16 v[88:91], a[88:91], a[60:63], v[88:91]\n"
      "ds_read_b128 a[124:127], v199 offset:5120\n"
      "v_fma_f32 v159, -v159, s94, s94\n"
      "s_mov_b32 m0, s62\n"
      "s_nop 0\n"
      "buffer_load_dwordx4 v228, s[56:59], s60 offen nt lds\n"
      "buffer_load_dwordx4 v229, s[56:59], s60 offen offset:1024 nt lds\n"
      "buffer_load_dwordx4 v230, s[56:59], s60 offen offset:2048 nt lds\n"
      "buffer_load_dwordx4 v231, s[56:59], s60 offen offset:3072 nt lds\n"
      "s_waitcnt lgkmcnt(0)\n"
      "v_mfma_f32_16x16x32_bf16 v[96:99], a[96:99], a[0:3], v[144:147]\n"
      "s_add_u32 s40, s40, 0x7f4a7c15\n"
      "v_mfma_f32_16x16x32_bf16 v[96:99], a[104:107], a[16:19], v[96:99]\n"
      "s_lshl_b32 s69, s44, 3\n"
      "v_max3_f32 v188, v64, v65, v66\n"
      "v_max3_f32 v189, v72, v73, v74\n"
      "v_mfma_f32_16x16x32_bf16 v[96:99], a[112:115], a[32:35], v[96:99]\n"
      "s_lshr_b32 s74, s50, 12\n"
      "v_max3_f32 v190, v80, v81, v82\n"
      "v_max3_f32 v191, v88, v89, v90\n"
      "v_mfma_f32_16x16x32_bf16 v[96:99], a[120:123], a[48:51], v[96:99]\n"
      "s_add_u32 s69, s69, s74\n"
      "v_max_f32 v188, v188, v67\n"
      "v_max_f32 v189, v189, v75\n"
      "v_mfma_f32_16x16x32_bf16 v[104:107], a[96:99], a[4:7], v[152:155]\n"
      "s_mul_i32 s69, s69, 4608\n"
      "v_max_f32 v190, v190, v83\n"
      "v_max_f32 v191, v191, v91\n"
      "v_mfma_f32_16x16x32_bf16 v[104:107], a[104:107], a[20:23], v[104:107]\n"
      "s_add_u32 s69, s69, 81920\n"
      "v_mov_b32 v192, v188\n"
      "v_mov_b32 v193, v189\n"
      "v_mfma_f32_16x16x32_bf16 v[104:107], a[112:115], a[36:39], v[104:107]\n"
      "s_add_u32 s24, s18, s69\n"
      "v_mov_b32 v194, v190\n"
      "v_mov_b32 v195, v191\n"
      "v_mfma_f32_16x16x32_bf16 v[104:107], a[120:123], a[52:55], v[104:107]\n"
      "s_addc_u32 s25, s19, 0\n"
      "v_permlane16_swap_b32 v188, v192\n"
      "v_permlane16_swap_b32 v189, v193\n"
      "v_mfma_f32_16x16x32_bf16 v[112:115], a[96:99], a[8:11], v[144:147]\n"
      "s_movk_i32 s26, 23040\n"
      "v_permlane16_swap_b32 v190, v194\n"
      "v_permlane16_swap_b32 v191, v195\n"
      "v_mfma_f32_16x16x32_bf16 v[112:115], a[104:107], a[24:27], v[112:115]\n"
      "s_mul_i32 s78, s43, 18432\n"
      "v_max_f32 v188, v188, v192\n"
      "v_max_f32 v189, v189, v193\n"
      "v_mfma_f32_16x16x32_bf16 v[112:115], a[112:115], a[40:43], v[112:115]\n"
      "s_and_b32 s69, s50, 4096\n"
      "v_max_f32 v190, v190, v194\n"
      "v_max_f32 v191, v191, v195\n"
      "v_mfma_f32_16x16x32_bf16 v[112:115], a[120:123], a[56:59], v[112:115]\n"
      "s_lshl_b32 s69, s69, 2\n"
      "v_mov_b32 v192, v188\n"
      "v_mov_b32 v193, v189\n"
      "v_mfma_f32_16x16x32_bf16 v[120:123], a[96:99], a[12:15], v[152:155]\n"
      "s_and_b32 s74, s50, 8192\n"
      "v_mov_b32 v194, v190\n"
      "v_mov_b32 v195, v191\n"
      "v_mfma_f32_16x16x32_bf16 v[120:123], a[104:107], a[28:31], v[120:123]\n"
      "s_add_u32 s74, s74, 4096\n"
      "v_permlane32_swap_b32 v188, v192\n"
      "v_permlane32_swap_b32 v189, v193\n"
      "v_mfma_f32_16x16x32_bf16 v[120:123], a[112:115], a[44:47], v[120:123]\n"
      "s_add_u32 s69, s69, s74\n"
      "v_permlane32_swap_b32 v190, v194\n"
      "v_permlane32_swap_b32 v191, v195\n"
      "v_mfma_f32_16x16x32_bf16 v[120:123], a[120:123], a[60:63], v[120:123]\n"
      "s_lshl_b32 s74, s43, 7\n"
      "v_max_f32 v188, v188, v192\n"
      "v_max_f32 v189, v189, v193\n"
      "v_mfma_f32_16x16x32_bf16 v[100:103], a[100:103], a[0:3], v[148:151]\n"
      "s_add_u32 s69, s69, s74\n"
      "v_max_f32 v190, v190, v194\n"
      "v_max_f32 v191, v191, v195\n"
      "v_mfma_f32_16x16x32_bf16 v[100:103], a[108:111], a[16:19], v[100:103]\n"
      "s_lshl_b32 s74, s44, 15\n"
      "v_xor_b32 v176, 0x80000000, v188\n"
      "v_xor_b32 v177, 0x80000000, v189\n"
      "v_mfma_f32_16x16x32_bf16 v[100:103], a[116:119], a[32:35], v[100:103]\n"
      "s_add_u32 s69, s69, s74\n"
      "v_xor_b32 v178, 0x80000000, v190\n"
      "v_xor_b32 v179, 0x80000000, v191\n"
      "v_mfma_f32_16x16x32_bf16 v[100:103], a[124:127], a[48:51], v[100:103]\n"
      "s_add_u32 s30, s16, s69\n"
      "v_mov_b32 v160, v176\n"
      "v_mov_b32 v161, v176\n"
      "v_mfma_f32_16x16x32_bf16 v[108:111], a[100:103], a[4:7], v[156:159]\n"
      "s_addc_u32 s31, s17, 0\n"
      "v_mov_b32 v162, v176\n"
      "v_mov_b32 v163, v176\n"
      "v_mfma_f32_16x16x32_bf16 v[108:111], a[108:111], a[20:23], v[108:111]\n"
      "v_mov_b32 v164, v177\n"
      "v_mov_b32 v165, v177\n"
      "v_mfma_f32_16x16x32_bf16 v[108:111], a[116:119], a[36:39], v[108:111]\n"
      "v_mov_b32 v166, v177\n"
      "v_mov_b32 v167, v177\n"
      "v_mfma_f32_16x16x32_bf16 v[108:111], a[124:127], a[52:55], v[108:111]\n"
      "v_mov_b32 v168, v178\n"
      "v_mov_b32 v169, v178\n"
      "v_mfma_f32_16x16x32_bf16 v[116:119], a[100:103], a[8:11], v[148:151]\n"
      "v_mov_b32 v170, v178\n"
      "v_mov_b32 v171, v178\n"
      "v_mfma_f32_16x16x32_bf16 v[116:119], a[108:111], a[24:27], v[116:119]\n"
      "v_mov_b32 v172, v179\n"
      "v_mov_b32 v173, v179\n"
      "v_mfma_f32_16x16x32_bf16 v[116:119], a[116:119], a[40:43], v[116:119]\n"
      "v_mov_b32 v174, v179\n"
      "v_mov_b32 v175, v179\n"
      "v_mfma_f32_16x16x32_bf16 v[116:119], a[124:127], a[56:59], v[116:119]\n"
      "v_mov_b32 v209, 139248\n"
      "v_lshl_add_u32 v232, s50, 1, v241\n"
      "v_mfma_f32_16x16x32_bf16 v[124:127], a[100:103], a[12:15], v[156:159]\n"
      "v_add_u32 v232, 0x10000, v232\n"
      "v_xor_b32 v233, 0x4000, v232\n"
      "v_mfma_f32_16x16x32_bf16 v[124:127], a[108:111], a[28:31], v[124:127]\n"
      "v_add_u32 v234, 0x10000, v232\n"
      "v_xor_b32 v235, 0x4000, v234\n"
      "v_mfma_f32_16x16x32_bf16 v[124:127], a[116:119], a[44:47], v[124:127]\n"
      "v_lshrrev_b32 v240, 1, v241\n"
      "v_add_u32 v240, 0x1000, v240\n"
      "v_mfma_f32_16x16x32_bf16 v[124:127], a[124:127], a[60:63], v[124:127]\n"
      "v_mov_b32 v196, 0\n"
      "v_pk_add_f32 v[96:97], v[96:97], v[160:161]\n"
      "v_pk_add_f32 v[98:99], v[98:99], v[160:161]\n"
      "v_pk_add_f32 v[104:105], v[104:105], v[164:165]\n"
      "v_pk_add_f32 v[106:107], v[106:107], v[164:165]\n"
      "v_pk_add_f32 v[112:113], v[112:113], v[168:169]\n"
      "v_pk_add_f32 v[114:115], v[114:115], v[168:169]\n"
      "v_pk_add_f32 v[120:121], v[120:121], v[172:173]\n"
      "v_pk_add_f32 v[122:123], v[122:123], v[172:173]\n"
      "v_pk_add_f32 v[100:101], v[100:101], v[160:161]\n"
      "v_pk_add_f32 v[102:103], v[102:103], v[160:161]\n"
      "v_pk_add_f32 v[108:109], v[108:109], v[164:165]\n"
      "v_pk_add_f32 v[110:111], v[110:111], v[164:165]\n"
      "v_pk_add_f32 v[116:117], v[116:117], v[168:169]\n"
      "v_pk_add_f32 v[118:119], v[118:119], v[168:169]\n"
      "v_pk_add_f32 v[124:125], v[124:125], v[172:173]\n"
      "v_pk_add_f32 v[126:127], v[126:127], v[172:173]\n"
      "v_mov_b32 v224, v244\n"
      "v_mov_b32 v225, v245\n"
      "v_mov_b32 v226, v246\n"
      "v_mov_b32 v227, v247\n"
      "v_xor_b32 v220, 0x2000, v244\n"
      "v_xor_b32 v221, 0x2000, v245\n"
      "v_xor_b32 v222, 0x2000, v246\n"
      "v_xor_b32 v223, 0x2000, v247\n"
      "s_lshl_b32 s76, s43, 3\n"
      "v_mov_b32 v237, s40\n"
      "v_mov_b32 v238, s76\n"
      "v_xor_b32 v239, 8, v238\n"
      "s_memrealtime s[68:69]\n"
      "s_waitcnt vmcnt(14) lgkmcnt(0)\n"
      "s_sub_u32 s67, s68, s88\n"
      "s_cmp_lt_i32 s67, -8\n"
      "s_cbranch_scc1 GW0_%=\n"
      "GX0_%=:\n"
      "s_barrier\n"
      "ds_read_b128 a[64:67], v242 offset:0\n"
      "ds_read_b128 a[68:71], v242 offset:4096\n"
      "ds_read_b128 a[72:75], v243 offset:0\n"
      "ds_read_b128 a[76:79], v243 offset:4096\n"
      "ds_read_b128 a[80:83], v242 offset:1024\n"
      "ds_read_b128 a[84:87], v242 offset:5120\n"
      "ds_read_b128 a[88:91], v243 offset:1024\n"
      "ds_read_b128 a[92:95], v243 offset:5120\n"
      "ds_write_b32 v209, v196\n"
      "s_add_u32 s38, s38, s38\n"
      "s_memrealtime s[68:69]\n"
      "s_xor_b64 s[56:57], s[56:57], s[32:33]\n"
      "s_add_u32 s61, s61, 1\n"
      "s_add_u32 s62, s62, 0x4000\n"
      "s_and_b32 s62, s62, 0x1ffff\n"
      "v_readlane_b32 s60, v219, s61\n"
      "s_waitcnt lgkmcnt(0)\n"
      "s_sub_u32 s67, s68, s66\n"
      "s_cmp_gt_i32 s67, s70\n"
      "s_cselect_b32 s69, s77, s86\n"
      "s_cselect_b32 s70, 60, s70\n"
      "s_cselect_b32 s90, s80, s90\n"
      "s_add_u32 s66, s66, s69\n"
      "s_mov_b32 s82, s68\n"
      "LOOP_%=:\n"
      "v_mfma_f32_16x16x32_bf16 v[64:67], a[64:67], a[0:3], v[160:163]\n"
      "s_add_u32 s65, s68, s90\n"
      "v_exp_f32 v188, v96\n"
      "v_mfma_f32_16x16x32_bf16 v[64:67], a[72:75], a[16:19], v[64:67]\n"
      "s_max_u32 s88, s66, s65\n"
      "v_add_f32 v180, v180, v184\n"
      "v_add_f32 v181, v181, v185\n"
      "v_mfma_f32_16x16x32_bf16 v[64:67], a[80:83], a[32:35], v[64:67]\n"
      "v_exp_f32 v189, v97\n"
      "v_mfma_f32_16x16x32_bf16 v[64:67], a[88:91], a[48:51], v[64:67]\n"
      "v_add_f32 v182, v182, v186\n"
      "v_add_f32 v183, v183, v187\n"
      "v_mfma_f32_16x16x32_bf16 v[72:75], a[64:67], a[4:7], v[164:167]\n"
      "v_exp_f32 v190, v98\n"
      "v_mfma_f32_16x16x32_bf16 v[72:75], a[72:75], a[20:23], v[72:75]\n"
      "v_exp_f32 v191, v99\n"
      "v_mfma_f32_16x16x32_bf16 v[72:75], a[80:83], a[36:39], v[72:75]\n"
      "v_cvt_pk_bf16_f32 v144, v188, v189\n"
      "v_cvt_pk_bf16_f32 v145, v190, v191\n"
      "v_mfma_f32_16x16x32_bf16 v[72:75], a[88:91], a[52:55], v[72:75]\n"
      "v_exp_f32 v192, v100\n"
      "v_mfma_f32_16x16x32_bf16 v[80:83], a[64:67], a[8:11], v[168:171]\n"
      "s_add_u32 s39, s39, s39\n"
      "v_exp_f32 v193, v101\n"
      "v_mfma_f32_16x16x32_bf16 v[80:83], a[72:75], a[24:27], v[80:83]\n"
      "s_memrealtime s[68:69]\n"
      "v_exp_f32 v194, v102\n"
      "v_mfma_f32_16x16x32_bf16 v[80:83], a[80:83], a[40:43], v[80:83]\n"
      "v_exp_f32 v195, v103\n"
      "v_mfma_f32_16x16x32_bf16 v[80:83], a[88:91], a[56:59], v[80:83]\n"
      "s_mov_b32 m0, s62\n"
      "v_cvt_pk_bf16_f32 v146, v192, v193\n"
      "v_cvt_pk_bf16_f32 v147, v194, v195\n"
      "v_mfma_f32_16x16x32_bf16 v[88:91], a[64:67], a[12:15], v[172:175]\n"
      "buffer_load_dwordx4 v228, s[56:59], s60 offen nt lds\n"
      "v_mfma_f32_16x16x32_bf16 v[184:187], a[128:131], v[144:147], 0\n"
      "buffer_load_dwordx4 v229, s[56:59], s60 offen offset:1024 nt lds\n"
      "v_mfma_f32_16x16x32_bf16 v[88:91], a[72:75], a[28:31], v[88:91]\n"
      "buffer_load_dwordx4 v230, s[56:59], s60 offen offset:2048 nt lds\n"
      "v_mfma_f32_16x16x32_bf16 v[88:91], a[80:83], a[44:47], v[88:91]\n"
      "buffer_load_dwordx4 v231, s[56:59], s60 offen offset:3072 nt lds\n"
      "s_cbranch_scc1 SP1_%=\n"
      "HD1_%=:\n"
      "s_cmp_lg_u32 s54, 0\n"
      "s_waitcnt vmcnt(12) lgkmcnt(0)\n"
      "s_cbranch_scc1 W1_%=\n"
      "s_waitcnt vmcnt(8)\n"
      "W1_%=:\n"
      "s_sub_u32 s67, s68, s88\n"
      "s_cmp_lt_i32 s67, -8\n"
      "s_cbranch_scc1 GW4_%=\n"
      "GX4_%=:\n"
      "s_barrier\n"
      "v_mfma_f32_16x16x32_bf16 v[88:91], a[88:91], a[60:63], v[88:91]\n"
      "s_memrealtime s[68:69]\n"
      "ds_read_b64_tr_b16 a[96:97], v244 offset:0\n"
      "ds_read_b64_tr_b16 a[98:99], v244 offset:4096\n"
      "v_mfma_f32_16x16x32_bf16 v[68:71], a[68:71], a[0:3], v[160:163]\n"
      "s_xor_b64 s[56:57], s[56:57], s[32:33]\n"
      "ds_read_b64_tr_b16 a[100:101], v245 offset:0\n"
      "ds_read_b64_tr_b16 a[102:103], v245 offset:4096\n"
      "v_mfma_f32_16x16x32_bf16 v[68:71], a[76:79], a[16:19], v[68:71]\n"
      "s_add_u32 s61, s61, 1\n"
      "ds_read_b64_tr_b16 a[104:105], v246 offset:0\n"
      "ds_read_b64_tr_b16 a[106:107], v246 offset:4096\n"
      "v_mfma_f32_16x16x32_bf16 v[68:71], a[84:87], a[32:35], v[68:71]\n"
      "s_add_u32 s62, s62, 0x4000\n"
      "ds_read_b64_tr_b16 a[108:109], v247 offset:0\n"
      "ds_read_b64_tr_b16 a[110:111], v247 offset:4096\n"
      "v_mfma_f32_16x16x32_bf16 v[68:71], a[92:95], a[48:51], v[68:71]\n"
      "s_and_b32 s62, s62, 0x1ffff\n"
      "v_exp_f32 v188, v104\n"
      "v_mfma_f32_16x16x32_bf16 v[76:79], a[68:71], a[4:7], v[164:167]\n"
      "v_readlane_b32 s60, v219, s61\n"
      "v_exp_f32 v189, v105\n"
      "v_mfma_f32_16x16x32_bf16 v[76:79], a[76:79], a[20:23], v[76:79]\n"
      "v_exp_f32 v190, v106\n"
      "v_mfma_f32_16x16x32_bf16 v[76:79], a[84:87], a[36:39], v[76:79]\n"
      "v_exp_f32 v191, v107\n"
      "v_mfma_f32_16x16x32_bf16 v[76:79], a[92:95], a[52:55], v[76:79]\n"
      "v_cvt_pk_bf16_f32 v148, v188, v189\n"
      "v_cvt_pk_bf16_f32 v149, v190, v191\n"
      "v_mfma_f32_16x16x32_bf16 v[84:87], a[68:71], a[8:11], v[168:171]\n"
      "v_exp_f32 v192, v108\n"
      "v_mfma_f32_16x16x32_bf16 v[84:87], a[76:79], a[24:27], v[84:87]\n"
      "v_exp_f32 v193, v109\n"
      "s_waitcnt lgkmcnt(0)\n"
      "v_mfma_f32_16x16x32_bf16 v[84:87], a[84:87], a[40:43], v[84:87]\n"
      "s_sub_u32 s67, s68, s66\n"
      "ds_read_b64_tr_b16 a[112:113], v244 offset:1024\n"
      "v_cvt_pk_bf16_f32 v150, v192, v193\n"
      "v_mfma_f32_16x16x32_bf16 v[84:87], a[92:95], a[56:59], v[84:87]\n"
      "s_cmp_gt_i32 s67, s70\n"
      "v_exp_f32 v194, v110\n"
      "v_mfma_f32_16x16x32_bf16 v[92:95], a[68:71], a[12:15], v[172:175]\n"
      "s_cselect_b32 s69, s77, s86\n"
      "ds_read_b64_tr_b16 a[114:115], v244 offset:5120\n"
      "ds_read_b64_tr_b16 a[116:117], v245 offset:1024\n"
      "v_mfma_f32_16x16x32_bf16 v[92:95], a[76:79], a[28:31], v[92:95]\n"
      "s_cselect_b32 s70, 60, s70\n"
      "v_exp_f32 v195, v111\n"
      "v_mfma_f32_16x16x32_bf16 v[92:95], a[84:87], a[44:47], v[92:95]\n"
      "s_cselect_b32 s90, s80, s90\n"
      "ds_read_b64_tr_b16 a[118:119], v245 offset:5120\n"
      "v_cvt_pk_bf16_f32 v151, v194, v195\n"
      "v_mfma_f32_16x16x32_bf16 v[92:95], a[92:95], a[60:63], v[92:95]\n"
      "s_add_u32 s66, s66, s69\n"
      "v_exp_f32 v188, v112\n"
      "v_mfma_f32_16x16x32_bf16 v[184:187], a[132:135], v[148:151], v[184:187]\n"
      "s_mov_b32 s82, s68\n"
      "ds_read_b64_tr_b16 a[120:121], v246 offset:1024\n"
      "ds_read_b64_tr_b16 a[122:123], v246 offset:5120\n"
      "v_mfma_f32_16x16x32_bf16 v[0:3], a[96:99], v[128:131], v[0:3]\n"
      "s_add_u32 s65, s68, s90\n"
      "v_exp_f32 v189, v113\n"
      "v_mfma_f32_16x16x32_bf16 v[4:7], a[96:99], v[132:135], v[4:7]\n"
      "s_max_u32 s88, s66, s65\n"
      "ds_read_b64_tr_b16 a[124:125], v247 offset:1024\n"
      "v_cvt_pk_bf16_f32 v152, v188, v189\n"
      "v_mfma_f32_16x16x32_bf16 a[144:147], a[96:99], v[136:139], a[144:147]\n"
      "v_exp_f32 v190, v114\n"
      "v_mfma_f32_16x16x32_bf16 a[148:151], a[96:99], v[140:143], a[148:151]\n"
      "v_exp_f32 v191, v115\n"
      "v_mfma_f32_16x16x32_bf16 a[156:159], a[100:103], v[140:143], a[156:159]\n"
      "ds_read_b64_tr_b16 a[126:127], v247 offset:5120\n"
      "v_cvt_pk_bf16_f32 v153, v190, v191\n"
      "v_mfma_f32_16x16x32_bf16 a[152:155], a[100:103], v[136:139], a[152:155]\n"
      "v_exp_f32 v192, v116\n"
      "v_mfma_f32_16x16x32_bf16 v[12:15], a[100:103], v[132:135], v[12:15]\n"
      "v_exp_f32 v193, v117\n"
      "v_mfma_f32_16x16x32_bf16 v[8:11], a[100:103], v[128:131], v[8:11]\n"
      "v_exp_f32 v194, v118\n"
      "v_mfma_f32_16x16x32_bf16 v[16:19], a[104:107], v[128:131], v[16:19]\n"
      "s_add_u32 s38, s38, s38\n"
      "v_exp_f32 v195, v119\n"
      "v_mfma_f32_16x16x32_bf16 v[20:23], a[104:107], v[132:135], v[20:23]\n"
      "s_memrealtime s[68:69]\n"
      "v_cvt_pk_bf16_f32 v154, v192, v193\n"
      "v_cvt_pk_bf16_f32 v155, v194, v195\n"
      "v_mfma_f32_16x16x32_bf16 a[160:163], a[104:107], v[136:139], a[160:163]\n"
      "v_exp_f32 v188, v120\n"
      "v_mfma_f32_16x16x32_bf16 v[184:187], a[136:139], v[152:155], v[184:187]\n"
      "s_mov_b32 m0, s62\n"
      "v_exp_f32 v189, v121\n"
      "v_mfma_f32_16x16x32_bf16 a[164:167], a[104:107], v[140:143], a[164:167]\n"
      "buffer_load_dwordx4 v228, s[56:59], s60 offen nt lds\n"
      "v_mfma_f32_16x16x32_bf16 a[172:175], a[108:111], v[140:143], a[172:175]\n"
      "buffer_load_dwordx4 v229, s[56:59], s60 offen offset:1024 nt lds\n"
      "v_mfma_f32_16x16x32_bf16 a[168:171], a[108:111], v[136:139], a[168:171]\n"
      "buffer_load_dwordx4 v230, s[56:59], s60 offen offset:2048 nt lds\n"
      "v_mfma_f32_16x16x32_bf16 v[28:31], a[108:111], v[132:135], v[28:31]\n"
      "buffer_load_dwordx4 v231, s[56:59], s60 offen offset:3072 nt lds\n"
      "s_cbranch_scc1 SP2_%=\n"
      "HD2_%=:\n"
      "s_cmp_lg_u32 s54, 0\n"
      "s_waitcnt vmcnt(12) lgkmcnt(0)\n"
      "s_cbranch_scc1 W2_%=\n"
      "s_waitcnt vmcnt(8)\n"
      "W2_%=:\n"
      "s_sub_u32 s67, s68, s88\n"
      "s_cmp_lt_i32 s67, -8\n"
      "s_cbranch_scc1 GW5_%=\n"
      "GX5_%=:\n"
      "s_barrier\n"
      "v_mfma_f32_16x16x32_bf16 v[24:27], a[108:111], v[128:131], v[24:27]\n"
      "s_memrealtime s[68:69]\n"
      "ds_read_b128 a[64:67], v242 offset:32768\n"
      "ds_read_b128 a[72:75], v243 offset:32768\n"
      "v_mfma_f32_16x16x32_bf16 v[32:35], a[112:115], v[128:131], v[32:35]\n"
      "s_xor_b64 s[56:57], s[56:57], s[32:33]\n"
      "ds_read_b128 a[80:83], v242 offset:33792\n"
      "ds_read_b128 a[88:91], v243 offset:33792\n"
      "v_mfma_f32_16x16x32_bf16 v[36:39], a[112:115], v[132:135], v[36:39]\n"
      "s_add_u32 s61, s61, 1\n"
      "v_exp_f32 v190, v122\n"
      "v_mfma_f32_16x16x32_bf16 a[176:179], a[112:115], v[136:139], a[176:179]\n"
      "s_add_u32 s62, s62, 0x4000\n"
      "v_exp_f32 v191, v123\n"
      "v_mfma_f32_16x16x32_bf16 a[180:183], a[112:115], v[140:143], a[180:183]\n"
      "s_and_b32 s62, s62, 0x1ffff\n"
      "v_cvt_pk_bf16_f32 v156, v188, v189\n"
      "v_cvt_pk_bf16_f32 v157, v190, v191\n"
      "v_mfma_f32_16x16x32_bf16 a[188:191], a[116:119], v[140:143], a[188:191]\n"
      "v_readlane_b32 s60, v219, s61\n"
      "v_exp_f32 v192, v124\n"
      "v_mfma_f32_16x16x32_bf16 a[184:187], a[116:119], v[136:139], a[184:187]\n"
      "v_exp_f32 v193, v125\n"
      "v_mfma_f32_16x16x32_bf16 v[44:47], a[116:119], v[132:135], v[44:47]\n"
      "v_exp_f32 v194, v126\n"
      "v_mfma_f32_16x16x32_bf16 v[40:43], a[116:119], v[128:131], v[40:43]\n"
      "v_exp_f32 v195, v127\n"
      "v_mfma_f32_16x16x32_bf16 v[48:51], a[120:123], v[128:131], v[48:51]\n"
      "v_cvt_pk_bf16_f32 v158, v192, v193\n"
      "v_cvt_pk_bf16_f32 v159, v194, v195\n"
      "v_mfma_f32_16x16x32_bf16 v[52:55], a[120:123], v[132:135], v[52:55]\n"
      "s_waitcnt lgkmcnt(0)\n"
      "v_mfma_f32_16x16x32_bf16 v[184:187], a[140:143], v[156:159], v[184:187]\n"
      "s_sub_u32 s67, s68, s66\n"
      "ds_read_b128 a[68:71], v242 offset:36864\n"
      "ds_read_b128 a[76:79], v243 offset:36864\n"
      "v_mfma_f32_16x16x32_bf16 a[192:195], a[120:123], v[136:139], a[192:195]\n"
      "s_cmp_gt_i32 s67, s70\n"
      "ds_read_b128 a[84:87], v242 offset:37888\n"
      "ds_read_b128 a[92:95], v243 offset:37888\n"
      "v_mfma_f32_16x16x32_bf16 a[196:199], a[120:123], v[140:143], a[196:199]\n"
      "s_cselect_b32 s69, s77, s86\n"
      "v_max3_f32 v196, v184, v185, v186\n"
      "v_max_f32 v196, v196, v187\n"
      "v_mfma_f32_16x16x32_bf16 a[204:207], a[124:127], v[140:143], a[204:207]\n"
      "s_cselect_b32 s70, 60, s70\n"
      "v_cmp_nge_f32 vcc, 0x4b800000, v196\n"
      "v_mfma_f32_16x16x32_bf16 a[200:203], a[124:127], v[136:139], a[200:203]\n"
      "s_cselect_b32 s90, s80, s90\n"
      "v_mfma_f32_16x16x32_bf16 v[60:63], a[124:127], v[132:135], v[60:63]\n"
      "s_add_u32 s66, s66, s69\n"
      "v_mfma_f32_16x16x32_bf16 v[56:59], a[124:127], v[128:131], v[56:59]\n"
      "s_mov_b32 s82, s68\n"
      "s_cbranch_vccnz FS1_%=\n"
      "FR1_%=:\n"
      "v_mfma_f32_16x16x32_bf16 v[96:99], a[64:67], a[0:3], v[160:163]\n"
      "s_add_u32 s65, s68, s90\n"
      "v_exp_f32 v188, v64\n"
      "v_mfma_f32_16x16x32_bf16 v[96:99], a[72:75], a[16:19], v[96:99]\n"
      "s_max_u32 s88, s66, s65\n"
      "v_add_f32 v180, v180, v184\n"
      "v_add_f32 v181, v181, v185\n"
      "v_mfma_f32_16x16x32_bf16 v[96:99], a[80:83], a[32:35], v[96:99]\n"
      "v_exp_f32 v189, v65\n"
      "v_mfma_f32_16x16x32_bf16 v[96:99], a[88:91], a[48:51], v[96:99]\n"
      "v_add_f32 v182, v182, v186\n"
      "v_add_f32 v183, v183, v187\n"
      "v_mfma_f32_16x16x32_bf16 v[104:107], a[64:67], a[4:7], v[164:167]\n"
      "v_exp_f32 v190, v66\n"
      "v_mfma_f32_16x16x32_bf16 v[104:107], a[72:75], a[20:23], v[104:107]\n"
      "v_exp_f32 v191, v67\n"
      "v_mfma_f32_16x16x32_bf16 v[104:107], a[80:83], a[36:39], v[104:107]\n"
      "v_cvt_pk_bf16_f32 v128, v188, v189\n"
      "v_cvt_pk_bf16_f32 v129, v190, v191\n"
      "v_mfma_f32_16x16x32_bf16 v[104:107], a[88:91], a[52:55], v[104:107]\n"
      "v_exp_f32 v192, v68\n"
      "v_mfma_f32_16x16x32_bf16 v[112:115], a[64:67], a[8:11], v[168:171]\n"
      "s_add_u32 s39, s39, s39\n"
      "v_exp_f32 v193, v69\n"
      "v_mfma_f32_16x16x32_bf16 v[112:115], a[72:75], a[24:27], v[112:115]\n"
      "s_memrealtime s[68:69]\n"
      "v_exp_f32 v194, v70\n"
      "v_mfma_f32_16x16x32_bf16 v[112:115], a[80:83], a[40:43], v[112:115]\n"
      "v_exp_f32 v195, v71\n"
      "v_mfma_f32_16x16x32_bf16 v[112:115], a[88:91], a[56:59], v[112:115]\n"
      "s_mov_b32 m0, s62\n"
      "v_cvt_pk_bf16_f32 v130, v192, v193\n"
      "v_cvt_pk_bf16_f32 v131, v194, v195\n"
      "v_mfma_f32_16x16x32_bf16 v[120:123], a[64:67], a[12:15], v[172:175]\n"
      "buffer_load_dwordx4 v228, s[56:59], s60 offen nt lds\n"
      "v_mfma_f32_16x16x32_bf16 v[184:187], a[128:131], v[128:131], 0\n"
      "buffer_load_dwordx4 v229, s[56:59], s60 offen offset:1024 nt lds\n"
      "v_mfma_f32_16x16x32_bf16 v[120:123], a[72:75], a[28:31], v[120:123]\n"
      "buffer_load_dwordx4 v230, s[56:59], s60 offen offset:2048 nt lds\n"
      "v_mfma_f32_16x16x32_bf16 v[120:123], a[80:83], a[44:47], v[120:123]\n"
      "buffer_load_dwordx4 v231, s[56:59], s60 offen offset:3072 nt lds\n"
      "s_cbranch_scc1 SP3_%=\n"
      "HD3_%=:\n"
      "s_cmp_lg_u32 s54, 0\n"
      "s_waitcnt vmcnt(12) lgkmcnt(0)\n"
      "s_cbranch_scc1 W3_%=\n"
      "s_waitcnt vmcnt(8)\n"
      "W3_%=:\n"
      "s_sub_u32 s67, s68, s88\n"
      "s_cmp_lt_i32 s67, -8\n"
      "s_cbranch_scc1 GW6_%=\n"
      "GX6_%=:\n"
      "s_barrier\n"
      "v_mfma_f32_16x16x32_bf16 v[120:123], a[88:91], a[60:63], v[120:123]\n"
      "s_memrealtime s[68:69]\n"
      "ds_read_b64_tr_b16 a[96:97], v244 offset:32768\n"
      "ds_read_b64_tr_b16 a[98:99], v244 offset:36864\n"
      "v_mfma_f32_16x16x32_bf16 v[100:103], a[68:71], a[0:3], v[160:163]\n"
      "s_xor_b64 s[56:57], s[56:57], s[32:33]\n"
      "ds_read_b64_tr_b16 a[100:101], v245 offset:32768\n"
      "ds_read_b64_tr_b16 a[102:103], v245 offset:36864\n"
      "v_mfma_f32_16x16x32_bf16 v[100:103], a[76:79], a[16:19], v[100:103]\n"
      "s_add_u32 s61, s61, 1\n"
      "ds_read_b64_tr_b16 a[104:105], v246 offset:32768\n"
      "ds_read_b64_tr_b16 a[106:107], v246 offset:36864\n"
      "v_mfma_f32_16x16x32_bf16 v[100:103], a[84:87], a[32:35], v[100:103]\n"
      "s_add_u32 s62, s62, 0x4000\n"
      "ds_read_b64_tr_b16 a[108:109], v247 offset:32768\n"
      "ds_read_b64_tr_b16 a[110:111], v247 offset:36864\n"
      "v_mfma_f32_16x16x32_bf16 v[100:103], a[92:95], a[48:51], v[100:103]\n"
      "s_and_b32 s62, s62, 0x1ffff\n"
      "v_exp_f32 v188, v72\n"
      "v_mfma_f32_16x16x32_bf16 v[108:111], a[68:71], a[4:7], v[164:167]\n"
      "v_readlane_b32 s60, v219, s61\n"
      "v_exp_f32 v189, v73\n"
      "v_mfma_f32_16x16x32_bf16 v[108:111], a[76:79], a[20:23], v[108:111]\n"
      "v_xor_b32 v242, 0x10000, v242\n"
      "v_xor_b32 v243, 0x10000, v243\n"
      "v_mfma_f32_16x16x32_bf16 v[108:111], a[84:87], a[36:39], v[108:111]\n"
      "v_exp_f32 v190, v74\n"
      "v_mfma_f32_16x16x32_bf16 v[108:111], a[92:95], a[52:55], v[108:111]\n"
      "v_exp_f32 v191, v75\n"
      "v_mfma_f32_16x16x32_bf16 v[116:119], a[68:71], a[8:11], v[168:171]\n"
      "v_cvt_pk_bf16_f32 v132, v188, v189\n"
      "v_cvt_pk_bf16_f32 v133, v190, v191\n"
      "v_mfma_f32_16x16x32_bf16 v[116:119], a[76:79], a[24:27], v[116:119]\n"
      "v_exp_f32 v192, v76\n"
      "s_waitcnt lgkmcnt(0)\n"
      "v_mfma_f32_16x16x32_bf16 v[116:119], a[84:87], a[40:43], v[116:119]\n"
      "s_sub_u32 s67, s68, s66\n"
      "ds_read_b64_tr_b16 a[112:113], v244 offset:33792\n"
      "ds_read_b64_tr_b16 a[114:115], v244 offset:37888\n"
      "v_mfma_f32_16x16x32_bf16 v[116:119], a[92:95], a[56:59], v[116:119]\n"
      "s_cmp_gt_i32 s67, s70\n"
      "v_exp_f32 v193, v77\n"
      "v_mfma_f32_16x16x32_bf16 v[124:127], a[68:71], a[12:15], v[172:175]\n"
      "s_cselect_b32 s69, s77, s86\n"
      "ds_read_b64_tr_b16 a[116:117], v245 offset:33792\n"
      "v_cvt_pk_bf16_f32 v134, v192, v193\n"
      "v_mfma_f32_16x16x32_bf16 v[124:127], a[76:79], a[28:31], v[124:127]\n"
      "s_cselect_b32 s70, 60, s70\n"
      "v_exp_f32 v194, v78\n"
      "v_mfma_f32_16x16x32_bf16 v[124:127], a[84:87], a[44:47], v[124:127]\n"
      "s_cselect_b32 s90, s80, s90\n"
      "ds_read_b64_tr_b16 a[118:119], v245 offset:37888\n"
      "ds_read_b64_tr_b16 a[120:121], v246 offset:33792\n"
      "v_mfma_f32_16x16x32_bf16 v[124:127], a[92:95], a[60:63], v[124:127]\n"
      "s_add_u32 s66, s66, s69\n"
      "v_exp_f32 v195, v79\n"
      "v_mfma_f32_16x16x32_bf16 v[0:3], a[96:99], v[144:147], v[0:3]\n"
      "s_mov_b32 s82, s68\n"
      "ds_read_b64_tr_b16 a[122:123], v246 offset:37888\n"
      "v_cvt_pk_bf16_f32 v135, v194, v195\n"
      "v_mfma_f32_16x16x32_bf16 v[4:7], a[96:99], v[148:151], v[4:7]\n"
      "s_add_u32 s65, s68, s90\n"
      "v_exp_f32 v188, v80\n"
      "v_mfma_f32_16x16x32_bf16 v[184:187], a[132:135], v[132:135], v[184:187]\n"
      "s_max_u32 s88, s66, s65\n"
      "ds_read_b64_tr_b16 a[124:125], v247 offset:33792\n"
      "ds_read_b64_tr_b16 a[126:127], v247 offset:37888\n"
      "v_mfma_f32_16x16x32_bf16 a[144:147], a[96:99], v[152:155], a[144:147]\n"
      "v_exp_f32 v189, v81\n"
      "v_mfma_f32_16x16x32_bf16 a[148:151], a[96:99], v[156:159], a[148:151]\n"
      "v_exp_f32 v190, v82\n"
      "v_mfma_f32_16x16x32_bf16 a[156:159], a[100:103], v[156:159], a[156:159]\n"
      "v_exp_f32 v191, v83\n"
      "v_mfma_f32_16x16x32_bf16 a[152:155], a[100:103], v[152:155], a[152:155]\n"
      "v_cvt_pk_bf16_f32 v136, v188, v189\n"
      "v_cvt_pk_bf16_f32 v137, v190, v191\n"
      "v_mfma_f32_16x16x32_bf16 v[12:15], a[100:103], v[148:151], v[12:15]\n"
      "v_exp_f32 v192, v84\n"
      "v_mfma_f32_16x16x32_bf16 v[8:11], a[100:103], v[144:147], v[8:11]\n"
      "v_exp_f32 v193, v85\n"
      "v_mfma_f32_16x16x32_bf16 v[16:19], a[104:107], v[144:147], v[16:19]\n"
      "s_add_u32 s38, s38, s38\n"
      "v_exp_f32 v194, v86\n"
      "v_mfma_f32_16x16x32_bf16 v[20:23], a[104:107], v[148:151], v[20:23]\n"
      "s_memrealtime s[68:69]\n"
      "v_exp_f32 v195, v87\n"
      "v_mfma_f32_16x16x32_bf16 a[160:163], a[104:107], v[152:155], a[160:163]\n"
      "v_cvt_pk_bf16_f32 v138, v192, v193\n"
      "v_cvt_pk_bf16_f32 v139, v194, v195\n"
      "v_mfma_f32_16x16x32_bf16 a[164:167], a[104:107], v[156:159], a[164:167]\n"
      "s_mov_b32 m0, s62\n"
      "v_exp_f32 v188, v88\n"
      "v_mfma_f32_16x16x32_bf16 v[184:187], a[136:139], v[136:139], v[184:187]\n"
      "buffer_load_dwordx4 v228, s[56:59], s60 offen nt lds\n"
      "v_mfma_f32_16x16x32_bf16 a[172:175], a[108:111], v[156:159], a[172:175]\n"
      "buffer_load_dwordx4 v229, s[56:59], s60 offen offset:1024 nt lds\n"
      "v_mfma_f32_16x16x32_bf16 a[168:171], a[108:111], v[152:155], a[168:171]\n"
      "buffer_load_dwordx4 v230, s[56:59], s60 offen offset:2048 nt lds\n"
      "v_mfma_f32_16x16x32_bf16 v[28:31], a[108:111], v[148:151], v[28:31]\n"
      "buffer_load_dwordx4 v231, s[56:59], s60 offen offset:3072 nt lds\n"
      "HD0_%=:\n"
      "s_cmp_lg_u32 s54, 0\n"
      "s_waitcnt vmcnt(12) lgkmcnt(0)\n"
      "s_cbranch_scc1 W4_%=\n"
      "s_waitcnt vmcnt(8)\n"
      "W4_%=:\n"
      "s_sub_u32 s67, s68, s88\n"
      "s_cmp_lt_i32 s67, -8\n"
      "s_cbranch_scc1 GW7_%=\n"
      "GX7_%=:\n"
      "s_barrier\n"
      "v_mfma_f32_16x16x32_bf16 v[24:27], a[108:111], v[144:147], v[24:27]\n"
      "s_memrealtime s[68:69]\n"
      "ds_read_b128 a[64:67], v242 offset:0\n"
      "ds_read_b128 a[72:75], v243 offset:0\n"
      "v_mfma_f32_16x16x32_bf16 v[32:35], a[112:115], v[144:147], v[32:35]\n"
      "s_xor_b64 s[56:57], s[56:57], s[32:33]\n"
      "ds_read_b128 a[80:83], v242 offset:1024\n"
      "ds_read_b128 a[88:91], v243 offset:1024\n"
      "v_mfma_f32_16x16x32_bf16 v[36:39], a[112:115], v[148:151], v[36:39]\n"
      "s_add_u32 s61, s61, 1\n"
      "v_exp_f32 v189, v89\n"
      "v_mfma_f32_16x16x32_bf16 a[176:179], a[112:115], v[152:155], a[176:179]\n"
      "s_add_u32 s62, s62, 0x4000\n"
      "v_exp_f32 v190, v90\n"
      "v_mfma_f32_16x16x32_bf16 a[180:183], a[112:115], v[156:159], a[180:183]\n"
      "s_and_b32 s62, s62, 0x1ffff\n"
      "v_exp_f32 v191, v91\n"
      "v_mfma_f32_16x16x32_bf16 a[188:191], a[116:119], v[156:159], a[188:191]\n"
      "v_cvt_pk_bf16_f32 v140, v188, v189\n"
      "v_readlane_b32 s60, v219, s61\n"
      "v_mfma_f32_16x16x32_bf16 a[184:187], a[116:119], v[152:155], a[184:187]\n"
      "v_exp_f32 v192, v92\n"
      "v_mfma_f32_16x16x32_bf16 v[44:47], a[116:119], v[148:151], v[44:47]\n"
      "v_exp_f32 v193, v93\n"
      "v_mfma_f32_16x16x32_bf16 v[40:43], a[116:119], v[144:147], v[40:43]\n"
      "v_cvt_pk_bf16_f32 v141, v190, v191\n"
      "v_cvt_pk_bf16_f32 v142, v192, v193\n"
      "v_mfma_f32_16x16x32_bf16 v[48:51], a[120:123], v[144:147], v[48:51]\n"
      "v_exp_f32 v194, v94\n"
      "v_mfma_f32_16x16x32_bf16 v[52:55], a[120:123], v[148:151], v[52:55]\n"
      "v_exp_f32 v195, v95\n"
      "s_waitcnt lgkmcnt(0)\n"
      "v_mfma_f32_16x16x32_bf16 a[192:195], a[120:123], v[152:155], a[192:195]\n"
      "s_sub_u32 s67, s68, s66\n"
      "v_cvt_pk_bf16_f32 v143, v194, v195\n"
      "v_xor_b32 v244, 0x10000, v244\n"
      "v_mfma_f32_16x16x32_bf16 a[196:199], a[120:123], v[156:159], a[196:199]\n"
      "s_cmp_gt_i32 s67, s70\n"
      "ds_read_b128 a[68:71], v242 offset:4096\n"
      "ds_read_b128 a[76:79], v243 offset:4096\n"
      "v_mfma_f32_16x16x32_bf16 v[184:187], a[140:143], v[140:143], v[184:187]\n"
      "s_cselect_b32 s69, s77, s86\n"
      "ds_read_b128 a[84:87], v242 offset:5120\n"
      "ds_read_b128 a[92:95], v243 offset:5120\n"
      "v_mfma_f32_16x16x32_bf16 a[204:207], a[124:127], v[156:159], a[204:207]\n"
      "s_cselect_b32 s70, 60, s70\n"
      "v_xor_b32 v245, 0x10000, v245\n"
      "v_xor_b32 v246, 0x10000, v246\n"
      "v_mfma_f32_16x16x32_bf16 a[200:203], a[124:127], v[152:155], a[200:203]\n"
      "s_cselect_b32 s90, s80, s90\n"
      "v_xor_b32 v247, 0x10000, v247\n"
      "v_max3_f32 v196, v184, v185, v186\n"
      "v_mfma_f32_16x16x32_bf16 v[60:63], a[124:127], v[148:151], v[60:63]\n"
      "s_add_u32 s66, s66, s69\n"
      "v_max_f32 v196, v196, v187\n"
      "v_cmp_nge_f32 vcc, 0x4b800000, v196\n"
      "v_mfma_f32_16x16x32_bf16 v[56:59], a[124:127], v[144:147], v[56:59]\n"
      "s_mov_b32 s82, s68\n"
      "s_cbranch_vccnz FS2_%=\n"
      "FR2_%=:\n"
      "s_branch LOOP_%=\n"
      "SP1_%=:\n"
      "s_ff1_i32_b32 s64, s39\n"
      "s_lshl1_add_u32 s64, s64, 1\n"
      "s_bitcmp1_b64 s[52:53], s64\n"
      "s_cbranch_scc0 SH1_%=\n"
      "s_swappc_b64 s[84:85], s[84:85]\n"
      "SH1_%=:\n"
      "s_cmp_lt_u32 s64, 63\n"
      "s_cbranch_scc1 HD1_%=\n"
      "s_xor_b64 s[56:57], s[56:57], s[32:33]\n"
      "s_add_u32 s61, s61, 1\n"
      "s_add_u32 s62, s62, 0x4000\n"
      "s_and_b32 s62, s62, 0x1ffff\n"
      "v_readlane_b32 s74, v219, 60\n"
      "v_readlane_b32 s76, v219, 62\n"
      "s_mov_b32 s61, 0\n"
      "s_add_u32 s74, s74, 0x400\n"
      "v_add_u32 v228, s74, v229\n"
      "v_add_u32 v230, s74, v231\n"
      "v_add_u32 v229, s76, v229\n"
      "v_add_u32 v231, s76, v231\n"
      "s_branch HD1_%=\n"
      "SP2_%=:\n"
      "s_cmp_eq_u32 s38, 0\n"
      "s_cbranch_scc1 L62_%=\n"
      "s_ff1_i32_b32 s64, s38\n"
      "s_lshl1_add_u32 s64, s64, 0\n"
      "s_bitcmp1_b64 s[52:53], s64\n"
      "s_cbranch_scc0 SH2_%=\n"
      "s_swappc_b64 s[84:85], s[84:85]\n"
      "SH2_%=:\n"
      "s_branch HD2_%=\n"
      "L62_%=:\n"
      "s_xor_b64 s[56:57], s[56:57], s[32:33]\n"
      "s_mov_b32 s61, 0\n"
      "v_xor_b32 v228, 0x80, v228\n"
      "v_xor_b32 v229, 0x80, v229\n"
      "v_xor_b32 v230, 0x80, v230\n"
      "v_xor_b32 v231, 0x80, v231\n"
      "s_mov_b32 s38, 0x80000000\n"
      "s_branch HD2_%=\n"
      "SP3_%=:\n"
      "s_cmp_eq_u32 s39, 0\n"
      "s_cbranch_scc1 NH_%=\n"
      "s_ff1_i32_b32 s64, s39\n"
      "s_lshl1_add_u32 s64, s64, 1\n"
      "s_bitcmp1_b64 s[52:53], s64\n"
      "s_cbranch_scc0 SH3_%=\n"
      "s_swappc_b64 s[84:85], s[84:85]\n"
      "SH3_%=:\n"
      "s_bitcmp1_b64 s[34:35], s64\n"
      "s_cbranch_scc0 HD3_%=\n"
      "NH_%=:\n"
      "s_cmp_lg_u32 s54, 0\n"
      "s_waitcnt vmcnt(12) lgkmcnt(0)\n"
      "s_cbranch_scc1 W5_%=\n"
      "s_waitcnt vmcnt(8)\n"
      "W5_%=:\n"
      "s_barrier\n"
      "v_mfma_f32_16x16x32_bf16 v[120:123], a[88:91], a[60:63], v[120:123]\n"
      "s_sub_u32 s54, s54, 1\n"
      "ds_read_b64_tr_b16 a[96:97], v244 offset:32768\n"
      "ds_read_b64_tr_b16 a[98:99], v244 offset:36864\n"
      "v_mfma_f32_16x16x32_bf16 v[100:103], a[68:71], a[0:3], v[160:163]\n"
      "ds_read_b64_tr_b16 a[100:101], v245 offset:32768\n"
      "ds_read_b64_tr_b16 a[102:103], v245 offset:36864\n"
      "v_mfma_f32_16x16x32_bf16 v[100:103], a[76:79], a[16:19], v[100:103]\n"
      "ds_read_b64_tr_b16 a[104:105], v246 offset:32768\n"
      "ds_read_b64_tr_b16 a[106:107], v246 offset:36864\n"
      "v_mfma_f32_16x16x32_bf16 v[100:103], a[84:87], a[32:35], v[100:103]\n"
      "ds_read_b64_tr_b16 a[108:109], v247 offset:32768\n"
      "ds_read_b64_tr_b16 a[110:111], v247 offset:36864\n"
      "v_mfma_f32_16x16x32_bf16 v[100:103], a[92:95], a[48:51], v[100:103]\n"
      "v_exp_f32 v188, v72\n"
      "v_mfma_f32_16x16x32_bf16 v[108:111], a[68:71], a[4:7], v[164:167]\n"
      "v_exp_f32 v189, v73\n"
      "v_mfma_f32_16x16x32_bf16 v[108:111], a[76:79], a[20:23], v[108:111]\n"
      "v_xor_b32 v242, 0x10000, v242\n"
      "v_xor_b32 v243, 0x10000, v243\n"
      "v_mfma_f32_16x16x32_bf16 v[108:111], a[84:87], a[36:39], v[108:111]\n"
      "v_exp_f32 v190, v74\n"
      "v_mfma_f32_16x16x32_bf16 v[108:111], a[92:95], a[52:55], v[108:111]\n"
      "v_exp_f32 v191, v75\n"
      "v_mfma_f32_16x16x32_bf16 v[116:119], a[68:71], a[8:11], v[168:171]\n"
      "v_cvt_pk_bf16_f32 v132, v188, v189\n"
      "v_cvt_pk_bf16_f32 v133, v190, v191\n"
      "v_mfma_f32_16x16x32_bf16 v[116:119], a[76:79], a[24:27], v[116:119]\n"
      "v_exp_f32 v192, v76\n"
      "s_waitcnt lgkmcnt(0)\n"
      "v_mfma_f32_16x16x32_bf16 v[116:119], a[84:87], a[40:43], v[116:119]\n"
      "ds_read_b64_tr_b16 a[112:113], v244 offset:33792\n"
      "ds_read_b64_tr_b16 a[114:115], v244 offset:37888\n"
      "v_mfma_f32_16x16x32_bf16 v[116:119], a[92:95], a[56:59], v[116:119]\n"
      "v_exp_f32 v193, v77\n"
      "v_mfma_f32_16x16x32_bf16 v[124:127], a[68:71], a[12:15], v[172:175]\n"
      "ds_read_b64_tr_b16 a[116:117], v245 offset:33792\n"
      "v_cvt_pk_bf16_f32 v134, v192, v193\n"
      "v_mfma_f32_16x16x32_bf16 v[124:127], a[76:79], a[28:31], v[124:127]\n"
      "v_exp_f32 v194, v78\n"
      "v_mfma_f32_16x16x32_bf16 v[124:127], a[84:87], a[44:47], v[124:127]\n"
      "ds_read_b64_tr_b16 a[118:119], v245 offset:37888\n"
      "ds_read_b64_tr_b16 a[120:121], v246 offset:33792\n"
      "v_mfma_f32_16x16x32_bf16 v[124:127], a[92:95], a[60:63], v[124:127]\n"
      "v_exp_f32 v195, v79\n"
      "v_mfma_f32_16x16x32_bf16 v[0:3], a[96:99], v[144:147], v[0:3]\n"
      "ds_read_b64_tr_b16 a[122:123], v246 offset:37888\n"
      "v_cvt_pk_bf16_f32 v135, v194, v195\n"
      "v_mfma_f32_16x16x32_bf16 v[4:7], a[96:99], v[148:151], v[4:7]\n"
      "v_exp_f32 v188, v80\n"
      "v_mfma_f32_16x16x32_bf16 v[184:187], a[132:135], v[132:135], v[184:187]\n"
      "ds_read_b64_tr_b16 a[124:125], v247 offset:33792\n"
      "ds_read_b64_tr_b16 a[126:127], v247 offset:37888\n"
      "v_mfma_f32_16x16x32_bf16 a[144:147], a[96:99], v[152:155], a[144:147]\n"
      "v_exp_f32 v189, v81\n"
      "v_mfma_f32_16x16x32_bf16 a[148:151], a[96:99], v[156:159], a[148:151]\n"
      "v_exp_f32 v190, v82\n"
      "v_mfma_f32_16x16x32_bf16 a[156:159], a[100:103], v[156:159], a[156:159]\n"
      "v_exp_f32 v191, v83\n"
      "v_mfma_f32_16x16x32_bf16 a[152:155], a[100:103], v[152:155], a[152:155]\n"
      "v_cvt_pk_bf16_f32 v136, v188, v189\n"
      "v_cvt_pk_bf16_f32 v137, v190, v191\n"
      "v_mfma_f32_16x16x32_bf16 v[12:15], a[100:103], v[148:151], v[12:15]\n"
      "v_exp_f32 v192, v84\n"
      "v_mfma_f32_16x16x32_bf16 v[8:11], a[100:103], v[144:147], v[8:11]\n"
      "v_exp_f32 v193, v85\n"
      "v_mfma_f32_16x16x32_bf16 v[16:19], a[104:107], v[144:147], v[16:19]\n"
      "s_add_u32 s38, s38, s38\n"
      "v_exp_f32 v194, v86\n"
      "v_mfma_f32_16x16x32_bf16 v[20:23], a[104:107], v[148:151], v[20:23]\n"
      "s_memrealtime s[68:69]\n"
      "v_exp_f32 v195, v87\n"
      "v_mfma_f32_16x16x32_bf16 a[160:163], a[104:107], v[152:155], a[160:163]\n"
      "v_cvt_pk_bf16_f32 v138, v192, v193\n"
      "v_cvt_pk_bf16_f32 v139, v194, v195\n"
      "v_mfma_f32_16x16x32_bf16 a[164:167], a[104:107], v[156:159], a[164:167]\n"
      "v_exp_f32 v188, v88\n"
      "v_mfma_f32_16x16x32_bf16 v[184:187], a[136:139], v[136:139], v[184:187]\n"
      "s_nop 0\n"
      "v_mfma_f32_16x16x32_bf16 a[172:175], a[108:111], v[156:159], a[172:175]\n"
      "s_nop 0\n"
      "v_mfma_f32_16x16x32_bf16 a[168:171], a[108:111], v[152:155], a[168:171]\n"
      "s_nop 0\n"
      "v_mfma_f32_16x16x32_bf16 v[28:31], a[108:111], v[148:151], v[28:31]\n"
      "s_nop 0\n"
      "s_cbranch_scc0 MH_%=\n"
      "END_%=:\n"
      "s_waitcnt lgkmcnt(0)\n"
      "v_mfma_f32_16x16x32_bf16 v[24:27], a[108:111], v[144:147], v[24:27]\n"
      "v_mov_b32 v236, s68\n"
      "v_exp_f32 v189, v89\n"
      "v_mfma_f32_16x16x32_bf16 v[32:35], a[112:115], v[144:147], v[32:35]\n"
      "s_mov_b64 exec, 1\n"
      "global_store_dwordx2 v238, v[236:237], s[46:47] offset:8 sc1\n"
      "s_mov_b64 exec, -1\n"
      "v_mfma_f32_16x16x32_bf16 v[36:39], a[112:115], v[148:151], v[36:39]\n"
      "s_mov_b32 s82, s68\n"
      "v_exp_f32 v190, v90\n"
      "v_mfma_f32_16x16x32_bf16 a[176:179], a[112:115], v[152:155], a[176:179]\n"
      "v_exp_f32 v191, v91\n"
      "v_mfma_f32_16x16x32_bf16 a[180:183], a[112:115], v[156:159], a[180:183]\n"
      "v_cvt_pk_bf16_f32 v140, v188, v189\n"
      "v_cvt_pk_bf16_f32 v141, v190, v191\n"
      "v_mfma_f32_16x16x32_bf16 a[188:191], a[116:119], v[156:159], a[188:191]\n"
      "v_exp_f32 v192, v92\n"
      "s_waitcnt vmcnt(5)\n"
      "s_barrier\n"
      "v_mfma_f32_16x16x32_bf16 a[184:187], a[116:119], v[152:155], a[184:187]\n"
      "ds_read_b64_tr_b16 a[80:81], v224 offset:0\n"
      "ds_read_b64_tr_b16 a[82:83], v224 offset:4096\n"
      "v_mfma_f32_16x16x32_bf16 v[44:47], a[116:119], v[148:151], v[44:47]\n"
      "ds_read_b64_tr_b16 a[84:85], v225 offset:0\n"
      "ds_read_b64_tr_b16 a[86:87], v225 offset:4096\n"
      "v_mfma_f32_16x16x32_bf16 v[40:43], a[116:119], v[144:147], v[40:43]\n"
      "s_mov_b32 s74, 0xffff0000\n"
      "ds_read_b64_tr_b16 a[88:89], v226 offset:0\n"
      "ds_read_b64_tr_b16 a[90:91], v226 offset:4096\n"
      "v_mfma_f32_16x16x32_bf16 v[48:51], a[120:123], v[144:147], v[48:51]\n"
      "s_mov_b32 s75, 0xffff0000\n"
      "ds_read_b64_tr_b16 a[92:93], v227 offset:0\n"
      "ds_read_b64_tr_b16 a[94:95], v227 offset:4096\n"
      "v_mfma_f32_16x16x32_bf16 v[52:55], a[120:123], v[148:151], v[52:55]\n"
      "s_mov_b32 s94, 0\n"
      "v_exp_f32 v193, v93\n"
      "v_mfma_f32_16x16x32_bf16 a[192:195], a[120:123], v[152:155], a[192:195]\n"
      "s_mov_b32 s95, -1\n"
      "ds_read_b64_tr_b16 a[16:17], v224 offset:1024\n"
      "v_cvt_pk_bf16_f32 v142, v192, v193\n"
      "v_mfma_f32_16x16x32_bf16 a[196:199], a[120:123], v[156:159], a[196:199]\n"
      "v_exp_f32 v194, v94\n"
      "v_mfma_f32_16x16x32_bf16 a[204:207], a[124:127], v[156:159], a[204:207]\n"
      "ds_read_b64_tr_b16 a[18:19], v224 offset:5120\n"
      "ds_read_b64_tr_b16 a[20:21], v225 offset:1024\n"
      "v_mfma_f32_16x16x32_bf16 a[200:203], a[124:127], v[152:155], a[200:203]\n"
      "v_exp_f32 v195, v95\n"
      "v_mfma_f32_16x16x32_bf16 v[60:63], a[124:127], v[148:151], v[60:63]\n"
      "ds_read_b64_tr_b16 a[22:23], v225 offset:5120\n"
      "v_cvt_pk_bf16_f32 v143, v194, v195\n"
      "v_mfma_f32_16x16x32_bf16 v[56:59], a[124:127], v[144:147], v[56:59]\n"
      "ds_read_b64_tr_b16 a[24:25], v226 offset:1024\n"
      "ds_read_b64_tr_b16 a[26:27], v226 offset:5120\n"
      "v_mfma_f32_16x16x32_bf16 v[184:187], a[140:143], v[140:143], v[184:187]\n"
      "ds_read_b64_tr_b16 a[28:29], v227 offset:1024\n"
      "ds_read_b64_tr_b16 a[30:31], v227 offset:5120\n"
      "ds_read_b64_tr_b16 a[0:1], v220 offset:1024\n"
      "ds_read_b64_tr_b16 a[2:3], v220 offset:5120\n"
      "ds_read_b64_tr_b16 a[4:5], v221 offset:1024\n"
      "ds_read_b64_tr_b16 a[6:7], v221 offset:5120\n"
      "ds_read_b64_tr_b16 a[8:9], v222 offset:1024\n"
      "ds_read_b64_tr_b16 a[10:11], v222 offset:5120\n"
      "ds_read_b64_tr_b16 a[12:13], v223 offset:1024\n"
      "ds_read_b64_tr_b16 a[14:15], v223 offset:5120\n"
      "v_max3_f32 v196, v184, v185, v186\n"
      "v_max_f32 v196, v196, v187\n"
      "v_cmp_nge_f32 vcc, 0x4b800000, v196\n"
      "s_cbranch_vccnz FS3_%=\n"
      "FR3_%=:\n"
      "s_waitcnt lgkmcnt(15)\n"
      "v_mfma_f32_16x16x32_bf16 a[176:179], a[80:83], v[136:139], a[176:179]\n"
      "v_add_f32 v180, v180, v184\n"
      "v_add_f32 v181, v181, v185\n"
      "v_mfma_f32_16x16x32_bf16 a[180:183], a[80:83], v[140:143], a[180:183]\n"
      "v_add_f32 v182, v182, v186\n"
      "v_add_f32 v183, v183, v187\n"
      "v_mfma_f32_16x16x32_bf16 a[184:187], a[84:87], v[136:139], a[184:187]\n"
      "v_exp_f32 v188, v112\n"
      "v_mfma_f32_16x16x32_bf16 a[188:191], a[84:87], v[140:143], a[188:191]\n"
      "ds_write_b128 v232, a[176:179] offset:0\n"
      "v_exp_f32 v189, v113\n"
      "v_mfma_f32_16x16x32_bf16 a[192:195], a[88:91], v[136:139], a[192:195]\n"
      "v_exp_f32 v190, v114\n"
      "v_mfma_f32_16x16x32_bf16 a[196:199], a[88:91], v[140:143], a[196:199]\n"
      "v_exp_f32 v191, v115\n"
      "v_mfma_f32_16x16x32_bf16 a[200:203], a[92:95], v[136:139], a[200:203]\n"
      "ds_write_b128 v232, a[180:183] offset:1024\n"
      "v_exp_f32 v192, v116\n"
      "v_mfma_f32_16x16x32_bf16 a[204:207], a[92:95], v[140:143], a[204:207]\n"
      "v_exp_f32 v193, v117\n"
      "v_exp_f32 v194, v118\n"
      "v_cvt_pk_bf16_f32 v152, v188, v189\n"
      "v_exp_f32 v195, v119\n"
      "v_cvt_pk_bf16_f32 v153, v190, v191\n"
      "ds_write_b128 v232, a[184:187] offset:2048\n"
      "v_exp_f32 v188, v120\n"
      "v_cvt_pk_bf16_f32 v154, v192, v193\n"
      "v_mfma_f32_16x16x32_bf16 v[32:35], a[80:83], v[128:131], v[32:35]\n"
      "v_exp_f32 v189, v121\n"
      "v_cvt_pk_bf16_f32 v155, v194, v195\n"
      "v_exp_f32 v190, v122\n"
      "v_cvt_pk_bf16_f32 v156, v188, v189\n"
      "v_mfma_f32_16x16x32_bf16 v[184:187], a[136:139], v[152:155], 0\n"
      "ds_write_b128 v234, v[152:155] offset:0\n"
      "v_exp_f32 v191, v123\n"
      "v_mfma_f32_16x16x32_bf16 v[36:39], a[80:83], v[132:135], v[36:39]\n"
      "v_exp_f32 v192, v124\n"
      "v_cvt_pk_bf16_f32 v157, v190, v191\n"
      "v_exp_f32 v193, v125\n"
      "v_exp_f32 v194, v126\n"
      "v_cvt_pk_bf16_f32 v158, v192, v193\n"
      "ds_write_b128 v232, a[188:191] offset:3072\n"
      "v_mfma_f32_16x16x32_bf16 v[40:43], a[84:87], v[128:131], v[40:43]\n"
      "v_exp_f32 v195, v127\n"
      "v_exp_f32 v188, v96\n"
      "v_cvt_pk_bf16_f32 v159, v194, v195\n"
      "v_exp_f32 v189, v97\n"
      "v_mfma_f32_16x16x32_bf16 v[44:47], a[84:87], v[132:135], v[44:47]\n"
      "v_exp_f32 v190, v98\n"
      "ds_write_b128 v234, v[156:159] offset:1024\n"
      "v_cvt_pk_bf16_f32 v144, v188, v189\n"
      "v_mfma_f32_16x16x32_bf16 v[184:187], a[140:143], v[156:159], v[184:187]\n"
      "v_exp_f32 v191, v99\n"
      "v_exp_f32 v192, v100\n"
      "v_cvt_pk_bf16_f32 v145, v190, v191\n"
      "v_mfma_f32_16x16x32_bf16 v[48:51], a[88:91], v[128:131], v[48:51]\n"
      "v_exp_f32 v193, v101\n"
      "ds_write_b128 v232, a[192:195] offset:4096\n"
      "v_exp_f32 v194, v102\n"
      "v_cvt_pk_bf16_f32 v146, v192, v193\n"
      "v_exp_f32 v195, v103\n"
      "v_mfma_f32_16x16x32_bf16 v[52:55], a[88:91], v[132:135], v[52:55]\n"
      "v_exp_f32 v188, v104\n"
      "v_cvt_pk_bf16_f32 v147, v194, v195\n"
      "v_exp_f32 v189, v105\n"
      "ds_write_b128 v232, a[196:199] offset:5120\n"
      "v_exp_f32 v190, v106\n"
      "v_cvt_pk_bf16_f32 v148, v188, v189\n"
      "v_mfma_f32_16x16x32_bf16 v[184:187], a[128:131], v[144:147], v[184:187]\n"
      "v_exp_f32 v191, v107\n"
      "v_exp_f32 v192, v108\n"
      "v_cvt_pk_bf16_f32 v149, v190, v191\n"
      "v_exp_f32 v193, v109\n"
      "ds_write_b128 v232, a[200:203] offset:6144\n"
      "v_mfma_f32_16x16x32_bf16 v[56:59], a[92:95], v[128:131], v[56:59]\n"
      "v_exp_f32 v194, v110\n"
      "v_cvt_pk_bf16_f32 v150, v192, v193\n"
      "v_exp_f32 v195, v111\n"
      "v_cndmask_b32 v229, v180, v181, s[74:75]\n"
      "v_cvt_pk_bf16_f32 v151, v194, v195\n"
      "v_mfma_f32_16x16x32_bf16 v[60:63], a[92:95], v[132:135], v[60:63]\n"
      "v_cndmask_b32 v200, v182, v183, s[74:75]\n"
      "ds_write_b128 v232, a[204:207] offset:7168\n"
      "v_cndmask_b32 v228, v176, v177, s[74:75]\n"
      "v_mfma_f32_16x16x32_bf16 v[184:187], a[132:135], v[148:151], v[184:187]\n"
      "s_nop 7\n"
      "v_max3_f32 v196, v184, v185, v186\n"
      "v_max_f32 v196, v196, v187\n"
      "v_cmp_nge_f32 vcc, 0x4b800000, v196\n"
      "s_cbranch_vccnz FS8_%=\n"
      "FR8_%=:\n"
      "v_cndmask_b32 v202, v186, v187, s[74:75]\n"
      "v_cndmask_b32 v201, v184, v185, s[74:75]\n"
      "v_add_f32 v200, v200, v202\n"
      "ds_write_b32 v234, v200 offset:4096\n"
      "v_add_f32 v229, v229, v201\n"
      "s_waitcnt lgkmcnt(8)\n"
      "v_mfma_f32_16x16x32_bf16 v[32:35], a[16:19], v[144:147], v[32:35]\n"
      "v_mfma_f32_16x16x32_bf16 v[36:39], a[16:19], v[148:151], v[36:39]\n"
      "v_mfma_f32_16x16x32_bf16 v[40:43], a[20:23], v[144:147], v[40:43]\n"
      "s_waitcnt lgkmcnt(0)\n"
      "s_barrier\n"
      "v_mfma_f32_16x16x32_bf16 v[44:47], a[20:23], v[148:151], v[44:47]\n"
      "ds_read_b32 v208, v209\n"
      "ds_read_b128 v[160:163], v235 offset:0\n"
      "v_mfma_f32_16x16x32_bf16 v[48:51], a[24:27], v[144:147], v[48:51]\n"
      "ds_read_b128 v[164:167], v235 offset:1024\n"
      "ds_read_b128 v[64:67], v233 offset:0\n"
      "v_mfma_f32_16x16x32_bf16 v[52:55], a[24:27], v[148:151], v[52:55]\n"
      "ds_read_b128 v[68:71], v233 offset:1024\n"
      "ds_read_b128 v[72:75], v233 offset:2048\n"
      "ds_read_b128 v[76:79], v233 offset:3072\n"
      "v_mfma_f32_16x16x32_bf16 v[56:59], a[28:31], v[144:147], v[56:59]\n"
      "ds_read_b128 v[80:83], v233 offset:4096\n"
      "ds_read_b128 v[84:87], v233 offset:5120\n"
      "v_mfma_f32_16x16x32_bf16 v[60:63], a[28:31], v[148:151], v[60:63]\n"
      "ds_read_b128 v[88:91], v233 offset:6144\n"
      "ds_read_b128 v[92:95], v233 offset:7168\n"
      "ds_read_b32 v204, v235 offset:4096\n"
      "s_waitcnt lgkmcnt(11)\n"
      "v_cmp_ne_u32 vcc, 0, v208\n"
      "s_cbranch_vccnz SLOW_%=\n"
      "AGAIN_%=:\n"
      "s_waitcnt lgkmcnt(9)\n"
      "v_mfma_f32_16x16x32_bf16 v[32:35], a[0:3], v[160:163], v[32:35]\n"
      "v_mfma_f32_16x16x32_bf16 v[36:39], a[0:3], v[164:167], v[36:39]\n"
      "v_mfma_f32_16x16x32_bf16 v[40:43], a[4:7], v[160:163], v[40:43]\n"
      "v_mfma_f32_16x16x32_bf16 v[44:47], a[4:7], v[164:167], v[44:47]\n"
      "v_mfma_f32_16x16x32_bf16 v[48:51], a[8:11], v[160:163], v[48:51]\n"
      "v_mfma_f32_16x16x32_bf16 v[52:55], a[8:11], v[164:167], v[52:55]\n"
      "v_mfma_f32_16x16x32_bf16 v[56:59], a[12:15], v[160:163], v[56:59]\n"
      "v_mfma_f32_16x16x32_bf16 v[60:63], a[12:15], v[164:167], v[60:63]\n"
      "global_load_dwordx2 v[206:207], v239, s[46:47] offset:8 sc1\n"
      "s_waitcnt lgkmcnt(7)\n"
      "v_pk_add_f32 v[32:33], v[32:33], v[64:65]\n"
      "v_pk_add_f32 v[34:35], v[34:35], v[66:67]\n"
      "v_pk_add_f32 v[36:37], v[36:37], v[68:69]\n"
      "v_pk_add_f32 v[38:39], v[38:39], v[70:71]\n"
      "v_cvt_pk_bf16_f32 v212, v32, v33\n"
      "v_cvt_pk_bf16_f32 v213, v34, v35\n"
      "v_cvt_pk_bf16_f32 v214, v36, v37\n"
      "v_cvt_pk_bf16_f32 v215, v38, v39\n"
      "buffer_store_dwordx4 v[212:215], v241, s[24:27], s78 offen offset:0 sc1\n"
      "s_waitcnt lgkmcnt(5)\n"
      "v_pk_add_f32 v[40:41], v[40:41], v[72:73]\n"
      "v_pk_add_f32 v[42:43], v[42:43], v[74:75]\n"
      "v_pk_add_f32 v[44:45], v[44:45], v[76:77]\n"
      "v_pk_add_f32 v[46:47], v[46:47], v[78:79]\n"
      "v_cvt_pk_bf16_f32 v216, v40, v41\n"
      "v_cvt_pk_bf16_f32 v217, v42, v43\n"
      "v_cvt_pk_bf16_f32 v218, v44, v45\n"
      "v_cvt_pk_bf16_f32 v219, v46, v47\n"
      "buffer_store_dwordx4 v[216:219], v241, s[24:27], s78 offen offset:1024 sc1\n"
      "s_waitcnt lgkmcnt(3)\n"
      "v_pk_add_f32 v[48:49], v[48:49], v[80:81]\n"
      "v_pk_add_f32 v[50:51], v[50:51], v[82:83]\n"
      "v_pk_add_f32 v[52:53], v[52:53], v[84:85]\n"
      "v_pk_add_f32 v[54:55], v[54:55], v[86:87]\n"
      "v_cvt_pk_bf16_f32 v220, v48, v49\n"
      "v_cvt_pk_bf16_f32 v221, v50, v51\n"
      "v_cvt_pk_bf16_f32 v222, v52, v53\n"
      "v_cvt_pk_bf16_f32 v223, v54, v55\n"
      "buffer_store_dwordx4 v[220:223], v241, s[24:27], s78 offen offset:2048 sc1\n"
      "s_waitcnt lgkmcnt(0)\n"
      "v_pk_add_f32 v[56:57], v[56:57], v[88:89]\n"
      "v_pk_add_f32 v[58:59], v[58:59], v[90:91]\n"
      "v_pk_add_f32 v[60:61], v[60:61], v[92:93]\n"
      "v_pk_add_f32 v[62:63], v[62:63], v[94:95]\n"
      "v_cvt_pk_bf16_f32 v224, v56, v57\n"
      "v_cvt_pk_bf16_f32 v225, v58, v59\n"
      "v_cvt_pk_bf16_f32 v226, v60, v61\n"
      "v_cvt_pk_bf16_f32 v227, v62, v63\n"
      "buffer_store_dwordx4 v[224:227], v241, s[24:27], s78 offen offset:3072 sc1\n"
      "v_add_f32 v229, v229, v204\n"
      "v_cndmask_b32 v230, v228, v229, s[94:95]\n"
      "v_add_u32 v231, s40, v230\n"
      "v_add3_u32 v231, v231, v212, v213\n"
      "v_add3_u32 v231, v231, v214, v215\n"
      "v_add3_u32 v231, v231, v216, v217\n"
      "v_add3_u32 v231, v231, v218, v219\n"
      "v_add3_u32 v231, v231, v220, v221\n"
      "v_add3_u32 v231, v231, v222, v223\n"
      "v_add3_u32 v231, v231, v224, v225\n"
      "v_add3_u32 v231, v231, v226, v227\n"
      "buffer_store_dwordx2 v[230:231], v240, s[24:27], s78 offen sc1\n"
      "v_xor_b32 v244, 0x10000, v244\n"
      "v_xor_b32 v245, 0x10000, v245\n"
      "v_xor_b32 v246, 0x10000, v246\n"
      "v_xor_b32 v247, 0x10000, v247\n"
      "s_waitcnt vmcnt(7)\n"
      "s_barrier\n"
      "ds_read_b64_tr_b16 a[64:65], v244 offset:16384\n"
      "ds_read_b64_tr_b16 a[66:67], v244 offset:20480\n"
      "ds_read_b64_tr_b16 a[68:69], v245 offset:16384\n"
      "ds_read_b64_tr_b16 a[70:71], v245 offset:20480\n"
      "ds_read_b64_tr_b16 a[72:73], v246 offset:16384\n"
      "ds_read_b64_tr_b16 a[74:75], v246 offset:20480\n"
      "ds_read_b64_tr_b16 a[76:77], v247 offset:16384\n"
      "ds_read_b64_tr_b16 a[78:79], v247 offset:20480\n"
      "s_waitcnt lgkmcnt(4)\n"
      "v_mfma_f32_16x16x32_bf16 a[144:147], a[64:67], v[136:139], a[144:147]\n"
      "ds_read_b64_tr_b16 a[96:97], v244 offset:17408\n"
      "ds_read_b64_tr_b16 a[98:99], v244 offset:21504\n"
      "v_mfma_f32_16x16x32_bf16 a[148:151], a[64:67], v[140:143], a[148:151]\n"
      "ds_read_b64_tr_b16 a[100:101], v245 offset:17408\n"
      "ds_read_b64_tr_b16 a[102:103], v245 offset:21504\n"
      "v_mfma_f32_16x16x32_bf16 a[152:155], a[68:71], v[136:139], a[152:155]\n"
      "ds_read_b64_tr_b16 a[104:105], v246 offset:17408\n"
      "ds_read_b64_tr_b16 a[106:107], v246 offset:21504\n"
      "v_mfma_f32_16x16x32_bf16 a[156:159], a[68:71], v[140:143], a[156:159]\n"
      "ds_read_b64_tr_b16 a[108:109], v247 offset:17408\n"
      "ds_read_b64_tr_b16 a[110:111], v247 offset:21504\n"
      "s_waitcnt lgkmcnt(8)\n"
      "v_mfma_f32_16x16x32_bf16 a[160:163], a[72:75], v[136:139], a[160:163]\n"
      "s_sub_u32 s76, 18432, s78\n"
      "v_mfma_f32_16x16x32_bf16 a[164:167], a[72:75], v[140:143], a[164:167]\n"
      "s_mov_b32 s63, 0x40000\n"
      "v_mfma_f32_16x16x32_bf16 a[168:171], a[76:79], v[136:139], a[168:171]\n"
      "v_mov_b32 v198, 0\n"
      "v_mfma_f32_16x16x32_bf16 a[172:175], a[76:79], v[140:143], a[172:175]\n"
      "v_and_b32 v197, 0xf0, v241\n"
      "s_waitcnt lgkmcnt(0)\n"
      "v_mfma_f32_16x16x32_bf16 a[144:147], a[96:99], v[152:155], a[144:147]\n"
      "v_lshrrev_b32 v196, 3, v241\n"
      "v_mfma_f32_16x16x32_bf16 a[148:151], a[96:99], v[156:159], a[148:151]\n"
      "v_and_b32 v196, 32, v196\n"
      "v_mfma_f32_16x16x32_bf16 a[152:155], a[100:103], v[152:155], a[152:155]\n"
      "v_lshl_or_b32 v196, v197, 4, v196\n"
      "v_mfma_f32_16x16x32_bf16 a[156:159], a[100:103], v[156:159], a[156:159]\n"
      "v_lshrrev_b32 v199, 5, v241\n"
      "v_mfma_f32_16x16x32_bf16 a[160:163], a[104:107], v[152:155], a[160:163]\n"
      "v_and_or_b32 v196, v199, 16, v196\n"
      "v_mfma_f32_16x16x32_bf16 a[164:167], a[104:107], v[156:159], a[164:167]\n"
      "v_mov_b32 v191, s40\n"
      "v_mfma_f32_16x16x32_bf16 a[168:171], a[108:111], v[152:155], a[168:171]\n"
      "v_mfma_f32_16x16x32_bf16 a[172:175], a[108:111], v[156:159], a[172:175]\n"
      "s_waitcnt vmcnt(0)\n"
      "v_subrev_u32 v205, s82, v206\n"
      "v_cmp_ne_u32 vcc, s40, v207\n"
      "s_cbranch_vccnz LAG_%=\n"
      "v_cmp_lt_i32 vcc, 10, v205\n"
      "s_cbranch_vccnz LAG_%=\n"
      "READ_%=:\n"
      "buffer_load_dwordx2 v[160:161], v240, s[24:27], s76 offen sc1\n"
      "buffer_load_dwordx4 v[96:99], v241, s[24:27], s76 offen offset:0 sc1\n"
      "buffer_load_dwordx4 v[100:103], v241, s[24:27], s76 offen offset:1024 sc1\n"
      "buffer_load_dwordx4 v[104:107], v241, s[24:27], s76 offen offset:2048 sc1\n"
      "buffer_load_dwordx4 v[108:111], v241, s[24:27], s76 offen offset:3072 sc1\n"
      "v_mfma_f32_16x16x32_bf16 v[0:3], a[64:67], v[128:131], v[0:3]\n"
      "v_mfma_f32_16x16x32_bf16 v[4:7], a[64:67], v[132:135], v[4:7]\n"
      "ds_write_b128 v232, a[144:147] offset:32768\n"
      "v_mfma_f32_16x16x32_bf16 v[8:11], a[68:71], v[128:131], v[8:11]\n"
      "v_mfma_f32_16x16x32_bf16 v[12:15], a[68:71], v[132:135], v[12:15]\n"
      "ds_write_b128 v232, a[148:151] offset:33792\n"
      "v_mfma_f32_16x16x32_bf16 v[16:19], a[72:75], v[128:131], v[16:19]\n"
      "v_mfma_f32_16x16x32_bf16 v[20:23], a[72:75], v[132:135], v[20:23]\n"
      "ds_write_b128 v232, a[152:155] offset:34816\n"
      "v_mfma_f32_16x16x32_bf16 v[24:27], a[76:79], v[128:131], v[24:27]\n"
      "v_mfma_f32_16x16x32_bf16 v[28:31], a[76:79], v[132:135], v[28:31]\n"
      "ds_write_b128 v232, a[156:159] offset:35840\n"
      "v_mfma_f32_16x16x32_bf16 v[0:3], a[96:99], v[144:147], v[0:3]\n"
      "v_mfma_f32_16x16x32_bf16 v[4:7], a[96:99], v[148:151], v[4:7]\n"
      "ds_write_b128 v232, a[160:163] offset:36864\n"
      "v_mfma_f32_16x16x32_bf16 v[8:11], a[100:103], v[144:147], v[8:11]\n"
      "v_mfma_f32_16x16x32_bf16 v[12:15], a[100:103], v[148:151], v[12:15]\n"
      "ds_write_b128 v232, a[164:167] offset:37888\n"
      "v_mfma_f32_16x16x32_bf16 v[16:19], a[104:107], v[144:147], v[16:19]\n"
      "v_mfma_f32_16x16x32_bf16 v[20:23], a[104:107], v[148:151], v[20:23]\n"
      "ds_write_b128 v232, a[168:171] offset:38912\n"
      "v_mfma_f32_16x16x32_bf16 v[24:27], a[108:111], v[144:147], v[24:27]\n"
      "v_mfma_f32_16x16x32_bf16 v[28:31], a[108:111], v[148:151], v[28:31]\n"
      "ds_write_b128 v232, a[172:175] offset:39936\n"
      "s_waitcnt lgkmcnt(0)\n"
      "s_barrier\n"
      "ds_read_b128 v[64:67], v233 offset:32768\n"
      "ds_read_b128 v[68:71], v233 offset:33792\n"
      "ds_read_b128 v[72:75], v233 offset:34816\n"
      "ds_read_b128 v[76:79], v233 offset:35840\n"
      "ds_read_b128 v[80:83], v233 offset:36864\n"
      "ds_read_b128 v[84:87], v233 offset:37888\n"
      "ds_read_b128 v[88:91], v233 offset:38912\n"
      "ds_read_b128 v[92:95], v233 offset:39936\n"
      "s_waitcnt lgkmcnt(6)\n"
      "v_pk_add_f32 v[0:1], v[0:1], v[64:65]\n"
      "v_pk_add_f32 v[2:3], v[2:3], v[66:67]\n"
      "v_pk_add_f32 v[4:5], v[4:5], v[68:69]\n"
      "v_pk_add_f32 v[6:7], v[6:7], v[70:71]\n"
      "s_waitcnt lgkmcnt(4)\n"
      "v_pk_add_f32 v[8:9], v[8:9], v[72:73]\n"
      "v_pk_add_f32 v[10:11], v[10:11], v[74:75]\n"
      "v_pk_add_f32 v[12:13], v[12:13], v[76:77]\n"
      "v_pk_add_f32 v[14:15], v[14:15], v[78:79]\n"
      "s_waitcnt lgkmcnt(2)\n"
      "v_pk_add_f32 v[16:17], v[16:17], v[80:81]\n"
      "v_pk_add_f32 v[18:19], v[18:19], v[82:83]\n"
      "v_pk_add_f32 v[20:21], v[20:21], v[84:85]\n"
      "v_pk_add_f32 v[22:23], v[22:23], v[86:87]\n"
      "s_waitcnt lgkmcnt(0)\n"
      "v_pk_add_f32 v[24:25], v[24:25], v[88:89]\n"
      "v_pk_add_f32 v[26:27], v[26:27], v[90:91]\n"
      "v_pk_add_f32 v[28:29], v[28:29], v[92:93]\n"
      "v_pk_add_f32 v[30:31], v[30:31], v[94:95]\n"
      "TRY_%=:\n"
      "s_waitcnt vmcnt(4)\n"
      "v_mov_b32 v162, v160\n"
      "v_add_u32 v231, s40, v160\n"
      "s_nop 0\n"
      "v_permlane32_swap_b32 v160, v162\n"
      "v_sub_f32 v168, v160, v228\n"
      "v_min_f32 v172, 0, -v168\n"
      "v_min_f32 v168, 0, v168\n"
      "v_exp_f32 v168, v168\n"
      "v_exp_f32 v172, v172\n"
      "v_mul_f32 v163, v229, v168\n"
      "v_fmac_f32 v163, v162, v172\n"
      "v_rcp_f32 v163, v163\n"
      "s_nop 0\n"
      "v_mul_f32 v168, v168, v163\n"
      "v_mul_f32 v172, v172, v163\n"
      "v_mov_b32 v169, v168\n"
      "v_mov_b32 v173, v172\n"
      "s_nop 0\n"
      "v_permlane16_swap_b32 v168, v169\n"
      "v_permlane16_swap_b32 v172, v173\n"
      "v_pk_mul_f32 v[32:33], v[0:1], v[168:169] op_sel:[0,0] op_sel_hi:[1,0]\n"
      "v_pk_mul_f32 v[34:35], v[2:3], v[168:169] op_sel:[0,0] op_sel_hi:[1,0]\n"
      "v_pk_mul_f32 v[36:37], v[4:5], v[168:169] op_sel:[0,1] op_sel_hi:[1,1]\n"
      "v_pk_mul_f32 v[38:39], v[6:7], v[168:169] op_sel:[0,1] op_sel_hi:[1,1]\n"
      "v_pk_mul_f32 v[40:41], v[8:9], v[168:169] op_sel:[0,0] op_sel_hi:[1,0]\n"
      "v_pk_mul_f32 v[42:43], v[10:11], v[168:169] op_sel:[0,0] op_sel_hi:[1,0]\n"
      "v_pk_mul_f32 v[44:45], v[12:13], v[168:169] op_sel:[0,1] op_sel_hi:[1,1]\n"
      "v_pk_mul_f32 v[46:47], v[14:15], v[168:169] op_sel:[0,1] op_sel_hi:[1,1]\n"
      "v_pk_mul_f32 v[48:49], v[16:17], v[168:169] op_sel:[0,0] op_sel_hi:[1,0]\n"
      "v_pk_mul_f32 v[50:51], v[18:19], v[168:169] op_sel:[0,0] op_sel_hi:[1,0]\n"
      "v_pk_mul_f32 v[52:53], v[20:21], v[168:169] op_sel:[0,1] op_sel_hi:[1,1]\n"
      "v_pk_mul_f32 v[54:55], v[22:23], v[168:169] op_sel:[0,1] op_sel_hi:[1,1]\n"
      "v_pk_mul_f32 v[56:57], v[24:25], v[168:169] op_sel:[0,0] op_sel_hi:[1,0]\n"
      "v_pk_mul_f32 v[58:59], v[26:27], v[168:169] op_sel:[0,0] op_sel_hi:[1,0]\n"
      "v_pk_mul_f32 v[60:61], v[28:29], v[168:169] op_sel:[0,1] op_sel_hi:[1,1]\n"
      "v_pk_mul_f32 v[62:63], v[30:31], v[168:169] op_sel:[0,1] op_sel_hi:[1,1]\n"
      "s_waitcnt vmcnt(3)\n"
      "v_add3_u32 v231, v231, v96, v97\n"
      "v_add3_u32 v231, v231, v98, v99\n"
      "s_waitcnt vmcnt(2)\n"
      "v_add3_u32 v231, v231, v100, v101\n"
      "v_add3_u32 v231, v231, v102, v103\n"
      "s_waitcnt vmcnt(1)\n"
      "v_add3_u32 v231, v231, v104, v105\n"
      "v_add3_u32 v231, v231, v106, v107\n"
      "s_waitcnt vmcnt(0)\n"
      "v_add3_u32 v231, v231, v108, v109\n"
      "v_add3_u32 v231, v231, v110, v111\n"
      "v_cmp_ne_u32 vcc, v231, v161\n"
      "s_cbranch_vccnz RETRY_%=\n"
      "GOT_%=:\n"
      "v_lshlrev_b32 v0, 16, v96\n"
      "v_and_b32 v1, 0xffff0000, v96\n"
      "v_lshlrev_b32 v2, 16, v97\n"
      "v_and_b32 v3, 0xffff0000, v97\n"
      "v_lshlrev_b32 v8, 16, v100\n"
      "v_and_b32 v9, 0xffff0000, v100\n"
      "v_lshlrev_b32 v10, 16, v101\n"
      "v_and_b32 v11, 0xffff0000, v101\n"
      "v_pk_fma_f32 v[32:33], v[0:1], v[172:173], v[32:33] op_sel:[0,0,0] op_sel_hi:[1,0,1]\n"
      "v_pk_fma_f32 v[34:35], v[2:3], v[172:173], v[34:35] op_sel:[0,0,0] op_sel_hi:[1,0,1]\n"
      "v_pk_fma_f32 v[40:41], v[8:9], v[172:173], v[40:41] op_sel:[0,0,0] op_sel_hi:[1,0,1]\n"
      "v_pk_fma_f32 v[42:43], v[10:11], v[172:173], v[42:43] op_sel:[0,0,0] op_sel_hi:[1,0,1]\n"
      "v_cvt_pk_bf16_f32 v112, v32, v33\n"
      "v_cvt_pk_bf16_f32 v114, v40, v41\n"
      "v_cvt_pk_bf16_f32 v113, v34, v35\n"
      "v_cvt_pk_bf16_f32 v115, v42, v43\n"
      "v_permlane16_swap_b32 v112, v114\n"
      "s_nop 0\n"
      "v_permlane16_swap_b32 v113, v115\n"
      "global_store_dwordx4 v196, v[112:115], s[30:31] offset:-4096 sc1 nt\n"
      "v_lshlrev_b32 v16, 16, v104\n"
      "v_and_b32 v17, 0xffff0000, v104\n"
      "v_lshlrev_b32 v18, 16, v105\n"
      "v_and_b32 v19, 0xffff0000, v105\n"
      "v_lshlrev_b32 v24, 16, v108\n"
      "v_and_b32 v25, 0xffff0000, v108\n"
      "v_lshlrev_b32 v26, 16, v109\n"
      "v_and_b32 v27, 0xffff0000, v109\n"
      "v_pk_fma_f32 v[48:49], v[16:17], v[172:173], v[48:49] op_sel:[0,0,0] op_sel_hi:[1,0,1]\n"
      "v_pk_fma_f32 v[50:51], v[18:19], v[172:173], v[50:51] op_sel:[0,0,0] op_sel_hi:[1,0,1]\n"
      "v_pk_fma_f32 v[56:57], v[24:25], v[172:173], v[56:57] op_sel:[0,0,0] op_sel_hi:[1,0,1]\n"
      "v_pk_fma_f32 v[58:59], v[26:27], v[172:173], v[58:59] op_sel:[0,0,0] op_sel_hi:[1,0,1]\n"
      "v_cvt_pk_bf16_f32 v116, v48, v49\n"
      "v_cvt_pk_bf16_f32 v118, v56, v57\n"
      "v_cvt_pk_bf16_f32 v117, v50, v51\n"
      "v_cvt_pk_bf16_f32 v119, v58, v59\n"
      "v_permlane16_swap_b32 v116, v118\n"
      "s_nop 0\n"
      "v_permlane16_swap_b32 v117, v119\n"
      "global_store_dwordx4 v196, v[116:119], s[30:31] offset:-4032 sc1 nt\n"
      "v_lshlrev_b32 v4, 16, v98\n"
      "v_and_b32 v5, 0xffff0000, v98\n"
      "v_lshlrev_b32 v6, 16, v99\n"
      "v_and_b32 v7, 0xffff0000, v99\n"
      "v_lshlrev_b32 v12, 16, v102\n"
      "v_and_b32 v13, 0xffff0000, v102\n"
      "v_lshlrev_b32 v14, 16, v103\n"
      "v_and_b32 v15, 0xffff0000, v103\n"
      "v_pk_fma_f32 v[36:37], v[4:5], v[172:173], v[36:37] op_sel:[0,1,0] op_sel_hi:[1,1,1]\n"
      "v_pk_fma_f32 v[38:39], v[6:7], v[172:173], v[38:39] op_sel:[0,1,0] op_sel_hi:[1,1,1]\n"
      "v_pk_fma_f32 v[44:45], v[12:13], v[172:173], v[44:45] op_sel:[0,1,0] op_sel_hi:[1,1,1]\n"
      "v_pk_fma_f32 v[46:47], v[14:15], v[172:173], v[46:47] op_sel:[0,1,0] op_sel_hi:[1,1,1]\n"
      "v_cvt_pk_bf16_f32 v120, v36, v37\n"
      "v_cvt_pk_bf16_f32 v122, v44, v45\n"
      "v_cvt_pk_bf16_f32 v121, v38, v39\n"
      "v_cvt_pk_bf16_f32 v123, v46, v47\n"
      "v_permlane16_swap_b32 v120, v122\n"
      "s_nop 0\n"
      "v_permlane16_swap_b32 v121, v123\n"
      "global_store_dwordx4 v196, v[120:123], s[30:31] offset:0 sc1 nt\n"
      "v_lshlrev_b32 v20, 16, v106\n"
      "v_and_b32 v21, 0xffff0000, v106\n"
      "v_lshlrev_b32 v22, 16, v107\n"
      "v_and_b32 v23, 0xffff0000, v107\n"
      "v_lshlrev_b32 v28, 16, v110\n"
      "v_and_b32 v29, 0xffff0000, v110\n"
      "v_lshlrev_b32 v30, 16, v111\n"
      "v_and_b32 v31, 0xffff0000, v111\n"
      "v_pk_fma_f32 v[52:53], v[20:21], v[172:173], v[52:53] op_sel:[0,1,0] op_sel_hi:[1,1,1]\n"
      "v_pk_fma_f32 v[54:55], v[22:23], v[172:173], v[54:55] op_sel:[0,1,0] op_sel_hi:[1,1,1]\n"
      "v_pk_fma_f32 v[60:61], v[28:29], v[172:173], v[60:61] op_sel:[0,1,0] op_sel_hi:[1,1,1]\n"
      "v_pk_fma_f32 v[62:63], v[30:31], v[172:173], v[62:63] op_sel:[0,1,0] op_sel_hi:[1,1,1]\n"
      "v_cvt_pk_bf16_f32 v124, v52, v53\n"
      "v_cvt_pk_bf16_f32 v126, v60, v61\n"
      "v_cvt_pk_bf16_f32 v125, v54, v55\n"
      "v_cvt_pk_bf16_f32 v127, v62, v63\n"
      "v_permlane16_swap_b32 v124, v126\n"
      "s_nop 0\n"
      "v_permlane16_swap_b32 v125, v127\n"
      "global_store_dwordx4 v196, v[124:127], s[30:31] offset:64 sc1 nt\n"
      "s_mov_b64 exec, 1\n"
      "global_store_dword v198, v191, s[46:47] sc1\n"
      "s_mov_b64 exec, -1\n"
      "ENDE_%=:\n"
      ".subsection 1\n"
      "MH_%=:\n"
      "s_cmp_lg_u32 s54, 0\n"
      "s_waitcnt vmcnt(12) lgkmcnt(0)\n"
      "s_cbranch_scc1 W6_%=\n"
      "s_waitcnt vmcnt(8)\n"
      "W6_%=:\n"
      "s_barrier\n"
      "v_mfma_f32_16x16x32_bf16 v[24:27], a[108:111], v[144:147], v[24:27]\n"
      "s_xor_b64 s[56:57], s[56:57], s[32:33]\n"
      "ds_read_b128 a[64:67], v242 offset:0\n"
      "ds_read_b128 a[72:75], v243 offset:0\n"
      "v_mfma_f32_16x16x32_bf16 v[32:35], a[112:115], v[144:147], v[32:35]\n"
      "s_add_u32 s61, s61, 1\n"
      "ds_read_b128 a[80:83], v242 offset:1024\n"
      "ds_read_b128 a[88:91], v243 offset:1024\n"
      "v_mfma_f32_16x16x32_bf16 v[36:39], a[112:115], v[148:151], v[36:39]\n"
      "s_add_u32 s62, s62, 0x4000\n"
      "v_exp_f32 v189, v89\n"
      "v_mfma_f32_16x16x32_bf16 a[176:179], a[112:115], v[152:155], a[176:179]\n"
      "s_and_b32 s62, s62, 0x1ffff\n"
      "v_exp_f32 v190, v90\n"
      "v_mfma_f32_16x16x32_bf16 a[180:183], a[112:115], v[156:159], a[180:183]\n"
      "v_readlane_b32 s60, v219, s61\n"
      "v_exp_f32 v191, v91\n"
      "v_mfma_f32_16x16x32_bf16 a[188:191], a[116:119], v[156:159], a[188:191]\n"
      "s_add_u32 s66, s66, s86\n"
      "v_cvt_pk_bf16_f32 v140, v188, v189\n"
      "v_mfma_f32_16x16x32_bf16 a[184:187], a[116:119], v[152:155], a[184:187]\n"
      "v_exp_f32 v192, v92\n"
      "v_mfma_f32_16x16x32_bf16 v[44:47], a[116:119], v[148:151], v[44:47]\n"
      "s_add_u32 s82, s82, s86\n"
      "v_exp_f32 v193, v93\n"
      "v_mfma_f32_16x16x32_bf16 v[40:43], a[116:119], v[144:147], v[40:43]\n"
      "s_add_u32 s65, s65, s86\n"
      "v_cvt_pk_bf16_f32 v141, v190, v191\n"
      "v_cvt_pk_bf16_f32 v142, v192, v193\n"
      "v_mfma_f32_16x16x32_bf16 v[48:51], a[120:123], v[144:147], v[48:51]\n"
      "s_max_u32 s88, s66, s65\n"
      "v_exp_f32 v194, v94\n"
      "v_mfma_f32_16x16x32_bf16 v[52:55], a[120:123], v[148:151], v[52:55]\n"
      "v_exp_f32 v195, v95\n"
      "s_waitcnt lgkmcnt(0)\n"
      "v_mfma_f32_16x16x32_bf16 a[192:195], a[120:123], v[152:155], a[192:195]\n"
      "v_cvt_pk_bf16_f32 v143, v194, v195\n"
      "v_xor_b32 v244, 0x10000, v244\n"
      "v_mfma_f32_16x16x32_bf16 a[196:199], a[120:123], v[156:159], a[196:199]\n"
      "ds_read_b128 a[68:71], v242 offset:4096\n"
      "ds_read_b128 a[76:79], v243 offset:4096\n"
      "v_mfma_f32_16x16x32_bf16 v[184:187], a[140:143], v[140:143], v[184:187]\n"
      "s_mov_b32 m0, s62\n"
      "ds_read_b128 a[84:87], v242 offset:5120\n"
      "ds_read_b128 a[92:95], v243 offset:5120\n"
      "v_mfma_f32_16x16x32_bf16 a[204:207], a[124:127], v[156:159], a[204:207]\n"
      "buffer_load_dwordx4 v228, s[56:59], s60 offen nt lds\n"
      "v_xor_b32 v245, 0x10000, v245\n"
      "v_xor_b32 v246, 0x10000, v246\n"
      "v_mfma_f32_16x16x32_bf16 a[200:203], a[124:127], v[152:155], a[200:203]\n"
      "buffer_load_dwordx4 v229, s[56:59], s60 offen offset:1024 nt lds\n"
      "v_xor_b32 v247, 0x10000, v247\n"
      "v_max3_f32 v196, v184, v185, v186\n"
      "v_mfma_f32_16x16x32_bf16 v[60:63], a[124:127], v[148:151], v[60:63]\n"
      "buffer_load_dwordx4 v230, s[56:59], s60 offen offset:2048 nt lds\n"
      "v_max_f32 v196, v196, v187\n"
      "v_cmp_nge_f32 vcc, 0x4b800000, v196\n"
      "v_mfma_f32_16x16x32_bf16 v[56:59], a[124:127], v[144:147], v[56:59]\n"
      "buffer_load_dwordx4 v231, s[56:59], s60 offen offset:3072 nt lds\n"
      "s_cbranch_vccnz FS11_%=\n"
      "FR11_%=:\n"
      "v_mfma_f32_16x16x32_bf16 v[64:67], a[64:67], a[0:3], v[160:163]\n"
      "v_exp_f32 v188, v96\n"
      "v_mfma_f32_16x16x32_bf16 v[64:67], a[72:75], a[16:19], v[64:67]\n"
      "v_add_f32 v180, v180, v184\n"
      "v_add_f32 v181, v181, v185\n"
      "v_mfma_f32_16x16x32_bf16 v[64:67], a[80:83], a[32:35], v[64:67]\n"
      "v_exp_f32 v189, v97\n"
      "v_mfma_f32_16x16x32_bf16 v[64:67], a[88:91], a[48:51], v[64:67]\n"
      "v_add_f32 v182, v182, v186\n"
      "v_add_f32 v183, v183, v187\n"
      "v_mfma_f32_16x16x32_bf16 v[72:75], a[64:67], a[4:7], v[164:167]\n"
      "v_exp_f32 v190, v98\n"
      "v_mfma_f32_16x16x32_bf16 v[72:75], a[72:75], a[20:23], v[72:75]\n"
      "v_exp_f32 v191, v99\n"
      "v_mfma_f32_16x16x32_bf16 v[72:75], a[80:83], a[36:39], v[72:75]\n"
      "v_cvt_pk_bf16_f32 v144, v188, v189\n"
      "v_cvt_pk_bf16_f32 v145, v190, v191\n"
      "v_mfma_f32_16x16x32_bf16 v[72:75], a[88:91], a[52:55], v[72:75]\n"
      "v_exp_f32 v192, v100\n"
      "v_mfma_f32_16x16x32_bf16 v[80:83], a[64:67], a[8:11], v[168:171]\n"
      "s_add_u32 s39, s39, s39\n"
      "v_exp_f32 v193, v101\n"
      "v_mfma_f32_16x16x32_bf16 v[80:83], a[72:75], a[24:27], v[80:83]\n"
      "s_memrealtime s[68:69]\n"
      "v_exp_f32 v194, v102\n"
      "v_mfma_f32_16x16x32_bf16 v[80:83], a[80:83], a[40:43], v[80:83]\n"
      "v_exp_f32 v195, v103\n"
      "v_mfma_f32_16x16x32_bf16 v[80:83], a[88:91], a[56:59], v[80:83]\n"
      "v_cvt_pk_bf16_f32 v146, v192, v193\n"
      "v_cvt_pk_bf16_f32 v147, v194, v195\n"
      "v_mfma_f32_16x16x32_bf16 v[88:91], a[64:67], a[12:15], v[172:175]\n"
      "s_nop 0\n"
      "v_mfma_f32_16x16x32_bf16 v[184:187], a[128:131], v[144:147], 0\n"
      "s_nop 0\n"
      "v_mfma_f32_16x16x32_bf16 v[88:91], a[72:75], a[28:31], v[88:91]\n"
      "s_nop 0\n"
      "v_mfma_f32_16x16x32_bf16 v[88:91], a[80:83], a[44:47], v[88:91]\n"
      "s_nop 0\n"
      "s_cbranch_scc1 SP1_%=\n"
      "s_branch HD1_%=\n"
      "HOOK_%=:\n"
      "buffer_load_dword v250, v251, s[20:23], 0 offen sc1\n"
      "s_swappc_b64 s[84:85], s[84:85]\n"
      "s_waitcnt vmcnt(12)\n"
      "v_subrev_u32 v250, s71, v250\n"
      "v_min_u32 v250, 0x258, v250\n"
      "s_nop 1\n"
      "v_add_u32_dpp v250, v250, v250 quad_perm:[1,0,3,2] row_mask:0xf bank_mask:0xf\n"
      "s_nop 1\n"
      "v_add_u32_dpp v250, v250, v250 quad_perm:[2,3,0,1] row_mask:0xf bank_mask:0xf\n"
      "s_nop 1\n"
      "v_add_u32_dpp v250, v250, v250 row_half_mirror row_mask:0xf bank_mask:0xf\n"
      "s_nop 0\n"
      "v_readfirstlane_b32 s67, v250\n"
      "s_sub_u32 s66, s66, s81\n"
      "s_lshr_b32 s67, s67, 3\n"
      "s_add_u32 s66, s66, s67\n"
      "s_max_u32 s88, s66, s65\n"
      "s_mov_b32 s86, 57\n"
      "s_mov_b32 s77, 171\n"
      "v_bfe_u32 v251, v241, 2, 7\n"
      "v_add_u32 v251, s79, v251\n"
      "buffer_load_dword v250, v251, s[20:23], 0 offen sc1\n"
      "s_swappc_b64 s[84:85], s[84:85]\n"
      "s_mov_b32 s89, 0\n"
      "s_swappc_b64 s[84:85], s[84:85]\n"
      "s_mov_b32 s86, 55\n"
      "s_mov_b32 s77, 165\n"
      "s_swappc_b64 s[84:85], s[84:85]\n"
      "s_cmp_lt_u32 s89, 1\n"
      "s_cselect_b32 s67, 2048, s72\n"
      "s_mov_b32 s89, 0\n"
      "s_cmp_lt_u32 s67, 2048\n"
      "s_cbranch_scc0 NV0_%=\n"
      "v_and_b32 v216, 0x1f0, v241\n"
      "v_lshlrev_b32 v216, 4, v216\n"
      "v_mov_b32 v215, s82\n"
      "s_lshr_b32 s67, s67, 4\n"
      "s_add_u32 s67, s67, 8192\n"
      "buffer_store_dword v215, v216, s[20:23], s67 offen sc1\n"
      "NV0_%=:\n"
      "s_swappc_b64 s[84:85], s[84:85]\n"
      "s_mov_b32 s86, 53\n"
      "s_mov_b32 s77, 159\n"
      "s_swappc_b64 s[84:85], s[84:85]\n"
      "buffer_load_dword v250, v251, s[20:23], 0 offen sc1\n"
      "s_swappc_b64 s[84:85], s[84:85]\n"
      "s_cmp_lt_u32 s89, 2\n"
      "s_cselect_b32 s67, 2048, s72\n"
      "s_mov_b32 s89, 0\n"
      "s_cmp_lt_u32 s67, 2048\n"
      "s_cbranch_scc0 NV1_%=\n"
      "v_and_b32 v216, 0x1f0, v241\n"
      "v_lshlrev_b32 v216, 4, v216\n"
      "v_mov_b32 v215, s82\n"
      "s_lshr_b32 s67, s67, 4\n"
      "s_add_u32 s67, s67, 8192\n"
      "buffer_store_dword v215, v216, s[20:23], s67 offen sc1\n"
      "NV1_%=:\n"
      "s_swappc_b64 s[84:85], s[84:85]\n"
      "s_waitcnt vmcnt(12)\n"
      "v_sub_u32 v250, s82, v250\n"
      "v_cmp_gt_u32 vcc, 1600, v250\n"
      "s_movk_i32 s76, 50\n"
      "s_nop 2\n"
      "s_bcnt1_i32_b32 s67, vcc_lo\n"
      "s_cmp_eq_u32 s70, 60\n"
      "s_cselect_b32 s67, 0, s67\n"
      "s_cmp_ge_u32 s67, 7\n"
      "s_cbranch_scc1 SM0_%=\n"
      "buffer_load_dword v250, v251, s[20:23], 0 offen sc1\n"
      "s_swappc_b64 s[84:85], s[84:85]\n"
      "s_cmp_lt_u32 s89, 2\n"
      "s_cselect_b32 s67, 2048, s72\n"
      "s_mov_b32 s89, 0\n"
      "s_cmp_lt_u32 s67, 2048\n"
      "s_cbranch_scc0 NV2_%=\n"
      "v_and_b32 v216, 0x1f0, v241\n"
      "v_lshlrev_b32 v216, 4, v216\n"
      "v_mov_b32 v215, s82\n"
      "s_lshr_b32 s67, s67, 4\n"
      "s_add_u32 s67, s67, 8192\n"
      "buffer_store_dword v215, v216, s[20:23], s67 offen sc1\n"
      "NV2_%=:\n"
      "s_swappc_b64 s[84:85], s[84:85]\n"
      "s_waitcnt vmcnt(12)\n"
      "v_sub_u32 v250, s82, v250\n"
      "v_cmp_gt_u32 vcc, 1600, v250\n"
      "s_movk_i32 s76, 66\n"
      "s_nop 2\n"
      "s_bcnt1_i32_b32 s67, vcc_lo\n"
      "s_cmp_eq_u32 s70, 60\n"
      "s_cselect_b32 s67, 0, s67\n"
      "s_cmp_ge_u32 s67, 7\n"
      "s_cbranch_scc1 SM1_%=\n"
      "buffer_load_dword v250, v251, s[20:23], 0 offen sc1\n"
      "s_swappc_b64 s[84:85], s[84:85]\n"
      "s_min_i32 s70, s70, 150\n"
      "s_waitcnt vmcnt(12)\n"
      "v_sub_u32 v250, s82, v250\n"
      "v_cmp_gt_u32 vcc, 1600, v250\n"
      "s_movk_i32 s76, 70\n"
      "s_nop 2\n"
      "s_bcnt1_i32_b32 s67, vcc_lo\n"
      "s_cmp_eq_u32 s70, 60\n"
      "s_cselect_b32 s67, 0, s67\n"
      "s_cmp_ge_u32 s67, 7\n"
      "s_cbranch_scc1 SM1_%=\n"
      "s_swappc_b64 s[84:85], s[84:85]\n"
      "s_cmp_lt_u32 s89, 2\n"
      "s_cselect_b32 s67, 2048, s72\n"
      "s_mov_b32 s89, 0\n"
      "s_cmp_lt_u32 s67, 2048\n"
      "s_cbranch_scc0 NV3_%=\n"
      "v_and_b32 v216, 0x1f0, v241\n"
      "v_lshlrev_b32 v216, 4, v216\n"
      "v_mov_b32 v215, s82\n"
      "s_lshr_b32 s67, s67, 4\n"
      "s_add_u32 s67, s67, 8192\n"
      "buffer_store_dword v215, v216, s[20:23], s67 offen sc1\n"
      "NV3_%=:\n"
      "s_swappc_b64 s[84:85], s[84:85]\n"
      "buffer_load_dword v250, v251, s[20:23], 0 offen sc1\n"
      "s_swappc_b64 s[84:85], s[84:85]\n"
      "s_waitcnt vmcnt(12)\n"
      "v_sub_u32 v250, s82, v250\n"
      "v_cmp_gt_u32 vcc, 1600, v250\n"
      "s_movk_i32 s76, 78\n"
      "s_nop 2\n"
      "s_bcnt1_i32_b32 s67, vcc_lo\n"
      "s_cmp_eq_u32 s70, 60\n"
      "s_cselect_b32 s67, 0, s67\n"
      "s_cmp_ge_u32 s67, 7\n"
      "s_cbranch_scc1 SM1_%=\n"
      "s_swappc_b64 s[84:85], s[84:85]\n"
      "s_getpc_b64 s[74:75]\n"
      "PF_%=:\n"
      "s_sub_u32 s74, s74, PF_%=-END_%=\n"
      "s_subb_u32 s75, s75, 0\n"
      "s_lshl_b32 s76, %[blk], 7\n"
      "s_and_b32 s76, s76, 0x1c00\n"
      "s_min_u32 s76, s76, ENDE_%=-END_%=-1024\n"
      "v_add_u32 v200, s76, v241\n"
      "global_load_dwordx4 v[200:203], v200, s[74:75]\n"
      "v_mov_b32 v212, 0\n"
      "global_load_dword v213, v212, s[30:31] nt\n"
      "buffer_load_dword v214, off, s[24:27], s78 nt\n"
      "s_swappc_b64 s[84:85], s[84:85]\n"
      "SM1_%=:\n"
      "s_add_u32 s66, s66, s76\n"
      "s_max_u32 s88, s66, s65\n"
      "s_min_i32 s70, s70, 150\n"
      "s_mov_b32 s87, 0\n"
      "s_branch SM2_%=\n"
      "SM0_%=:\n"
      "s_or_b32 s39, s39, 0xc0000000\n"
      "s_mov_b32 s52, 0xaaaaaaaa\n"
      "s_mov_b32 s53, 0xaaaaaaaa\n"
      "s_add_u32 s66, s66, s76\n"
      "s_max_u32 s88, s66, s65\n"
      "s_min_i32 s70, s70, 150\n"
      "s_mov_b32 s87, 0\n"
      "s_mov_b32 s86, 59\n"
      "s_mov_b32 s77, 177\n"
      "s_swappc_b64 s[84:85], s[84:85]\n"
      "s_mov_b32 s86, 57\n"
      "s_mov_b32 s77, 171\n"
      "s_swappc_b64 s[84:85], s[84:85]\n"
      "SM2_%=:\n"
      "s_mov_b32 s86, 55\n"
      "s_mov_b32 s77, 165\n"
      "s_mov_b32 s52, 0x0\n"
      "s_mov_b32 s53, 0x80000\n"
      "s_swappc_b64 s[84:85], s[84:85]\n"
      "s_getpc_b64 s[74:75]\n"
      "PG_%=:\n"
      "s_sub_u32 s74, s74, PG_%=-END_%=\n"
      "s_subb_u32 s75, s75, 0\n"
      "s_lshl_b32 s76, %[blk], 7\n"
      "s_and_b32 s76, s76, 0x1c00\n"
      "s_min_u32 s76, s76, ENDE_%=-END_%=-1024\n"
      "v_add_u32 v200, s76, v241\n"
      "global_load_dwordx4 v[200:203], v200, s[74:75]\n"
      "v_mov_b32 v212, 0\n"
      "global_load_dword v213, v212, s[30:31] nt\n"
      "buffer_load_dword v214, off, s[24:27], s78 nt\n"
      "s_swappc_b64 s[84:85], s[84:85]\n"
      "GW0_%=:\n"
      "s_memrealtime s[68:69]\n"
      "s_waitcnt lgkmcnt(0)\n"
      "s_getreg_b32 s67, hwreg(HW_REG_IB_STS, 0, 4)\n"
      "s_cmp_gt_u32 s67, 9\n"
      "s_cselect_b32 s67, s87, 0\n"
      "s_addc_u32 s89, s89, 0\n"
      "s_add_u32 s66, s66, s67\n"
      "s_add_u32 s88, s88, s67\n"
      "GS0_%=:\n"
      "s_sub_u32 s67, s68, s88\n"
      "s_cmp_ge_i32 s67, 0\n"
      "s_cbranch_scc1 GX0_%=\n"
      "s_memrealtime s[68:69]\n"
      "s_waitcnt lgkmcnt(0)\n"
      "s_branch GS0_%=\n"
      "FS1_%=:\n"
      "s_call_b64 s[96:97], FB1_%=\n"
      "s_branch FR1_%=\n"
      "FS2_%=:\n"
      "s_call_b64 s[96:97], FB0_%=\n"
      "s_branch FR2_%=\n"
      "FS3_%=:\n"
      "s_call_b64 s[96:97], FB0_%=\n"
      "s_branch FR3_%=\n"
      "GW4_%=:\n"
      "s_memrealtime s[68:69]\n"
      "s_waitcnt lgkmcnt(0)\n"
      "s_getreg_b32 s67, hwreg(HW_REG_IB_STS, 0, 4)\n"
      "s_cmp_gt_u32 s67, 9\n"
      "s_cselect_b32 s67, s87, 0\n"
      "s_addc_u32 s89, s89, 0\n"
      "s_add_u32 s66, s66, s67\n"
      "s_add_u32 s88, s88, s67\n"
      "GS4_%=:\n"
      "s_sub_u32 s67, s68, s88\n"
      "s_cmp_ge_i32 s67, 0\n"
      "s_cbranch_scc1 GX4_%=\n"
      "s_memrealtime s[68:69]\n"
      "s_waitcnt lgkmcnt(0)\n"
      "s_branch GS4_%=\n"
      "GW5_%=:\n"
      "s_memrealtime s[68:69]\n"
      "s_waitcnt lgkmcnt(0)\n"
      "s_getreg_b32 s67, hwreg(HW_REG_IB_STS, 0, 4)\n"
      "s_cmp_gt_u32 s67, 9\n"
      "s_cselect_b32 s67, s87, 0\n"
      "s_addc_u32 s89, s89, 0\n"
      "s_add_u32 s66, s66, s67\n"
      "s_add_u32 s88, s88, s67\n"
      "GS5_%=:\n"
      "s_sub_u32 s67, s68, s88\n"
      "s_cmp_ge_i32 s67, 0\n"
      "s_cbranch_scc1 GX5_%=\n"
      "s_memrealtime s[68:69]\n"
      "s_waitcnt lgkmcnt(0)\n"
      "s_branch GS5_%=\n"
      "GW6_%=:\n"
      "s_memrealtime s[68:69]\n"
      "s_waitcnt lgkmcnt(0)\n"
      "s_getreg_b32 s67, hwreg(HW_REG_IB_STS, 0, 4)\n"
      "s_cmp_gt_u32 s67, 9\n"
      "s_cselect_b32 s67, s87, 0\n"
      "s_addc_u32 s89, s89, 0\n"
      "s_add_u32 s66, s66, s67\n"
      "s_add_u32 s88, s88, s67\n"
      "GS6_%=:\n"
      "s_sub_u32 s67, s68, s88\n"
      "s_cmp_ge_i32 s67, 0\n"
      "s_cbranch_scc1 GX6_%=\n"
      "s_memrealtime s[68:69]\n"
      "s_waitcnt lgkmcnt(0)\n"
      "s_branch GS6_%=\n"
      "GW7_%=:\n"
      "s_memrealtime s[68:69]\n"
      "s_waitcnt lgkmcnt(0)\n"
      "s_getreg_b32 s67, hwreg(HW_REG_IB_STS, 0, 4)\n"
      "s_cmp_gt_u32 s67, 9\n"
      "s_cselect_b32 s67, s87, 0\n"
      "s_addc_u32 s89, s89, 0\n"
      "s_add_u32 s66, s66, s67\n"
      "s_add_u32 s88, s88, s67\n"
      "GS7_%=:\n"
      "s_sub_u32 s67, s68, s88\n"
      "s_cmp_ge_i32 s67, 0\n"
      "s_cbranch_scc1 GX7_%=\n"
      "s_memrealtime s[68:69]\n"
      "s_waitcnt lgkmcnt(0)\n"
      "s_branch GS7_%=\n"
      "FS8_%=:\n"
      "s_call_b64 s[96:97], FB1_%=\n"
      "s_branch FR8_%=\n"
      "LAG_%=:\n"
      "s_memrealtime s[68:69]\n"
      "s_waitcnt lgkmcnt(0)\n"
      "s_mov_b32 s88, s68\n"
      "LG1_%=:\n"
      "v_cmp_ne_u32 vcc, s40, v207\n"
      "s_cbranch_vccz LG2_%=\n"
      "s_sub_u32 s63, s63, 1\n"
      "s_cmp_eq_u32 s63, 0\n"
      "s_cbranch_scc1 READ_%=\n"
      "s_sleep 4\n"
      "global_load_dwordx2 v[206:207], v239, s[46:47] offset:8 sc1\n"
      "s_waitcnt vmcnt(0)\n"
      "s_branch LG1_%=\n"
      "LG2_%=:\n"
      "v_readfirstlane_b32 s67, v206\n"
      "s_sub_u32 s88, s88, s82\n"
      "s_min_u32 s88, s88, 400\n"
      "s_add_u32 s88, s88, s67\n"
      "LG3_%=:\n"
      "s_memrealtime s[68:69]\n"
      "s_waitcnt lgkmcnt(0)\n"
      "s_sub_u32 s67, s88, s68\n"
      "s_sub_u32 s67, s67, 11\n"
      "s_cmp_lt_u32 s67, 400\n"
      "s_cbranch_scc1 LG3_%=\n"
      "s_branch READ_%=\n"
      "RETRY_%=:\n"
      "s_sub_u32 s63, s63, 1\n"
      "s_cbranch_scc1 GOT_%=\n"
      "buffer_load_dwordx2 v[160:161], v240, s[24:27], s76 offen sc1\n"
      "buffer_load_dwordx4 v[96:99], v241, s[24:27], s76 offen offset:0 sc1\n"
      "buffer_load_dwordx4 v[100:103], v241, s[24:27], s76 offen offset:1024 sc1\n"
      "buffer_load_dwordx4 v[104:107], v241, s[24:27], s76 offen offset:2048 sc1\n"
      "buffer_load_dwordx4 v[108:111], v241, s[24:27], s76 offen offset:3072 sc1\n"
      "s_branch TRY_%=\n"
      "FS11_%=:\n"
      "s_call_b64 s[96:97], FB0_%=\n"
      "s_branch FR11_%=\n"
      "COLD_%=:\n"
      "FB0_%=:\n"
      "v_mov_b32 v196, 139248\n"
      "s_nop 7\n"
      "ds_write_b32 v196, v196\n"
      "v_max3_f32 v188, v64, v65, 0\n"
      "v_max3_f32 v189, v72, v73, 0\n"
      "v_max3_f32 v190, v80, v81, 0\n"
      "v_max3_f32 v191, v88, v89, 0\n"
      "v_max3_f32 v188, v188, v66, v67\n"
      "v_max3_f32 v189, v189, v74, v75\n"
      "v_max3_f32 v190, v190, v82, v83\n"
      "v_max3_f32 v191, v191, v90, v91\n"
      "v_max3_f32 v188, v188, v68, v69\n"
      "v_max3_f32 v189, v189, v76, v77\n"
      "v_max3_f32 v190, v190, v84, v85\n"
      "v_max3_f32 v191, v191, v92, v93\n"
      "v_max3_f32 v188, v188, v70, v71\n"
      "v_max3_f32 v189, v189, v78, v79\n"
      "v_max3_f32 v190, v190, v86, v87\n"
      "v_max3_f32 v191, v191, v94, v95\n"
      "v_mov_b32 v192, v188\n"
      "v_mov_b32 v193, v189\n"
      "v_mov_b32 v194, v190\n"
      "v_mov_b32 v195, v191\n"
      "v_permlane16_swap_b32 v188, v192\n"
      "v_permlane16_swap_b32 v189, v193\n"
      "v_permlane16_swap_b32 v190, v194\n"
      "v_permlane16_swap_b32 v191, v195\n"
      "v_max_f32 v188, v188, v192\n"
      "v_max_f32 v189, v189, v193\n"
      "v_max_f32 v190, v190, v194\n"
      "v_max_f32 v191, v191, v195\n"
      "v_mov_b32 v192, v188\n"
      "v_mov_b32 v193, v189\n"
      "v_mov_b32 v194, v190\n"
      "v_mov_b32 v195, v191\n"
      "v_permlane32_swap_b32 v188, v192\n"
      "v_permlane32_swap_b32 v189, v193\n"
      "v_permlane32_swap_b32 v190, v194\n"
      "v_permlane32_swap_b32 v191, v195\n"
      "v_max_f32 v188, v188, v192\n"
      "v_max_f32 v189, v189, v193\n"
      "v_max_f32 v190, v190, v194\n"
      "v_max_f32 v191, v191, v195\n"
      "RS0_%=:\n"
      "v_exp_f32 v192, -v188\n"
      "v_exp_f32 v193, -v189\n"
      "v_exp_f32 v194, -v190\n"
      "v_exp_f32 v195, -v191\n"
      "v_sub_f32 v176, v176, v188\n"
      "v_sub_f32 v177, v177, v189\n"
      "v_sub_f32 v178, v178, v190\n"
      "v_sub_f32 v179, v179, v191\n"
      "v_mov_b32 v160, v176\n"
      "v_mov_b32 v161, v176\n"
      "v_mov_b32 v162, v176\n"
      "v_mov_b32 v163, v176\n"
      "v_mov_b32 v164, v177\n"
      "v_mov_b32 v165, v177\n"
      "v_mov_b32 v166, v177\n"
      "v_mov_b32 v167, v177\n"
      "v_mov_b32 v168, v178\n"
      "v_mov_b32 v169, v178\n"
      "v_mov_b32 v170, v178\n"
      "v_mov_b32 v171, v178\n"
      "v_mov_b32 v172, v179\n"
      "v_mov_b32 v173, v179\n"
      "v_mov_b32 v174, v179\n"
      "v_mov_b32 v175, v179\n"
      "v_sub_f32 v64, v64, v188\n"
      "v_sub_f32 v65, v65, v188\n"
      "v_sub_f32 v66, v66, v188\n"
      "v_sub_f32 v67, v67, v188\n"
      "v_sub_f32 v68, v68, v188\n"
      "v_sub_f32 v69, v69, v188\n"
      "v_sub_f32 v70, v70, v188\n"
      "v_sub_f32 v71, v71, v188\n"
      "v_sub_f32 v72, v72, v189\n"
      "v_sub_f32 v73, v73, v189\n"
      "v_sub_f32 v74, v74, v189\n"
      "v_sub_f32 v75, v75, v189\n"
      "v_sub_f32 v76, v76, v189\n"
      "v_sub_f32 v77, v77, v189\n"
      "v_sub_f32 v78, v78, v189\n"
      "v_sub_f32 v79, v79, v189\n"
      "v_sub_f32 v80, v80, v190\n"
      "v_sub_f32 v81, v81, v190\n"
      "v_sub_f32 v82, v82, v190\n"
      "v_sub_f32 v83, v83, v190\n"
      "v_sub_f32 v84, v84, v190\n"
      "v_sub_f32 v85, v85, v190\n"
      "v_sub_f32 v86, v86, v190\n"
      "v_sub_f32 v87, v87, v190\n"
      "v_sub_f32 v88, v88, v191\n"
      "v_sub_f32 v89, v89, v191\n"
      "v_sub_f32 v90, v90, v191\n"
      "v_sub_f32 v91, v91, v191\n"
      "v_sub_f32 v92, v92, v191\n"
      "v_sub_f32 v93, v93, v191\n"
      "v_sub_f32 v94, v94, v191\n"
      "v_sub_f32 v95, v95, v191\n"
      "v_sub_f32 v96, v96, v188\n"
      "v_sub_f32 v97, v97, v188\n"
      "v_sub_f32 v98, v98, v188\n"
      "v_sub_f32 v99, v99, v188\n"
      "v_sub_f32 v100, v100, v188\n"
      "v_sub_f32 v101, v101, v188\n"
      "v_sub_f32 v102, v102, v188\n"
      "v_sub_f32 v103, v103, v188\n"
      "v_sub_f32 v104, v104, v189\n"
      "v_sub_f32 v105, v105, v189\n"
      "v_sub_f32 v106, v106, v189\n"
      "v_sub_f32 v107, v107, v189\n"
      "v_sub_f32 v108, v108, v189\n"
      "v_sub_f32 v109, v109, v189\n"
      "v_sub_f32 v110, v110, v189\n"
      "v_sub_f32 v111, v111, v189\n"
      "v_sub_f32 v112, v112, v190\n"
      "v_sub_f32 v113, v113, v190\n"
      "v_sub_f32 v114, v114, v190\n"
      "v_sub_f32 v115, v115, v190\n"
      "v_sub_f32 v116, v116, v190\n"
      "v_sub_f32 v117, v117, v190\n"
      "v_sub_f32 v118, v118, v190\n"
      "v_sub_f32 v119, v119, v190\n"
      "v_sub_f32 v120, v120, v191\n"
      "v_sub_f32 v121, v121, v191\n"
      "v_sub_f32 v122, v122, v191\n"
      "v_sub_f32 v123, v123, v191\n"
      "v_sub_f32 v124, v124, v191\n"
      "v_sub_f32 v125, v125, v191\n"
      "v_sub_f32 v126, v126, v191\n"
      "v_sub_f32 v127, v127, v191\n"
      "v_mul_f32 v0, v0, v192\n"
      "v_mul_f32 v1, v1, v192\n"
      "v_mul_f32 v2, v2, v192\n"
      "v_mul_f32 v3, v3, v192\n"
      "v_mul_f32 v4, v4, v193\n"
      "v_mul_f32 v5, v5, v193\n"
      "v_mul_f32 v6, v6, v193\n"
      "v_mul_f32 v7, v7, v193\n"
      "v_mul_f32 v8, v8, v192\n"
      "v_mul_f32 v9, v9, v192\n"
      "v_mul_f32 v10, v10, v192\n"
      "v_mul_f32 v11, v11, v192\n"
      "v_mul_f32 v12, v12, v193\n"
      "v_mul_f32 v13, v13, v193\n"
      "v_mul_f32 v14, v14, v193\n"
      "v_mul_f32 v15, v15, v193\n"
      "v_mul_f32 v16, v16, v192\n"
      "v_mul_f32 v17, v17, v192\n"
      "v_mul_f32 v18, v18, v192\n"
      "v_mul_f32 v19, v19, v192\n"
      "v_mul_f32 v20, v20, v193\n"
      "v_mul_f32 v21, v21, v193\n"
      "v_mul_f32 v22, v22, v193\n"
      "v_mul_f32 v23, v23, v193\n"
      "v_mul_f32 v24, v24, v192\n"
      "v_mul_f32 v25, v25, v192\n"
      "v_mul_f32 v26, v26, v192\n"
      "v_mul_f32 v27, v27, v192\n"
      "v_mul_f32 v28, v28, v193\n"
      "v_mul_f32 v29, v29, v193\n"
      "v_mul_f32 v30, v30, v193\n"
      "v_mul_f32 v31, v31, v193\n"
      "v_mul_f32 v32, v32, v192\n"
      "v_mul_f32 v33, v33, v192\n"
      "v_mul_f32 v34, v34, v192\n"
      "v_mul_f32 v35, v35, v192\n"
      "v_mul_f32 v36, v36, v193\n"
      "v_mul_f32 v37, v37, v193\n"
      "v_mul_f32 v38, v38, v193\n"
      "v_mul_f32 v39, v39, v193\n"
      "v_mul_f32 v40, v40, v192\n"
      "v_mul_f32 v41, v41, v192\n"
      "v_mul_f32 v42, v42, v192\n"
      "v_mul_f32 v43, v43, v192\n"
      "v_mul_f32 v44, v44, v193\n"
      "v_mul_f32 v45, v45, v193\n"
      "v_mul_f32 v46, v46, v193\n"
      "v_mul_f32 v47, v47, v193\n"
      "v_mul_f32 v48, v48, v192\n"
      "v_mul_f32 v49, v49, v192\n"
      "v_mul_f32 v50, v50, v192\n"
      "v_mul_f32 v51, v51, v192\n"
      "v_mul_f32 v52, v52, v193\n"
      "v_mul_f32 v53, v53, v193\n"
      "v_mul_f32 v54, v54, v193\n"
      "v_mul_f32 v55, v55, v193\n"
      "v_mul_f32 v56, v56, v192\n"
      "v_mul_f32 v57, v57, v192\n"
      "v_mul_f32 v58, v58, v192\n"
      "v_mul_f32 v59, v59, v192\n"
      "v_mul_f32 v60, v60, v193\n"
      "v_mul_f32 v61, v61, v193\n"
      "v_mul_f32 v62, v62, v193\n"
      "v_mul_f32 v63, v63, v193\n"
      "v_accvgpr_read_b32 v196, a144\n"
      "v_accvgpr_read_b32 v197, a145\n"
      "v_accvgpr_read_b32 v198, a146\n"
      "v_accvgpr_read_b32 v199, a147\n"
      "v_mul_f32 v196, v196, v194\n"
      "v_mul_f32 v197, v197, v194\n"
      "v_mul_f32 v198, v198, v194\n"
      "v_mul_f32 v199, v199, v194\n"
      "v_accvgpr_write_b32 a144, v196\n"
      "v_accvgpr_write_b32 a145, v197\n"
      "v_accvgpr_write_b32 a146, v198\n"
      "v_accvgpr_write_b32 a147, v199\n"
      "v_accvgpr_read_b32 v196, a148\n"
      "v_accvgpr_read_b32 v197, a149\n"
      "v_accvgpr_read_b32 v198, a150\n"
      "v_accvgpr_read_b32 v199, a151\n"
      "v_mul_f32 v196, v196, v195\n"
      "v_mul_f32 v197, v197, v195\n"
      "v_mul_f32 v198, v198, v195\n"
      "v_mul_f32 v199, v199, v195\n"
      "v_accvgpr_write_b32 a148, v196\n"
      "v_accvgpr_write_b32 a149, v197\n"
      "v_accvgpr_write_b32 a150, v198\n"
      "v_accvgpr_write_b32 a151, v199\n"
      "v_accvgpr_read_b32 v196, a152\n"
      "v_accvgpr_read_b32 v197, a153\n"
      "v_accvgpr_read_b32 v198, a154\n"
      "v_accvgpr_read_b32 v199, a155\n"
      "v_mul_f32 v196, v196, v194\n"
      "v_mul_f32 v197, v197, v194\n"
      "v_mul_f32 v198, v198, v194\n"
      "v_mul_f32 v199, v199, v194\n"
      "v_accvgpr_write_b32 a152, v196\n"
      "v_accvgpr_write_b32 a153, v197\n"
      "v_accvgpr_write_b32 a154, v198\n"
      "v_accvgpr_write_b32 a155, v199\n"
      "v_accvgpr_read_b32 v196, a156\n"
      "v_accvgpr_read_b32 v197, a157\n"
      "v_accvgpr_read_b32 v198, a158\n"
      "v_accvgpr_read_b32 v199, a159\n"
      "v_mul_f32 v196, v196, v195\n"
      "v_mul_f32 v197, v197, v195\n"
      "v_mul_f32 v198, v198, v195\n"
      "v_mul_f32 v199, v199, v195\n"
      "v_accvgpr_write_b32 a156, v196\n"
      "v_accvgpr_write_b32 a157, v197\n"
      "v_accvgpr_write_b32 a158, v198\n"
      "v_accvgpr_write_b32 a159, v199\n"
      "v_accvgpr_read_b32 v196, a160\n"
      "v_accvgpr_read_b32 v197, a161\n"
      "v_accvgpr_read_b32 v198, a162\n"
      "v_accvgpr_read_b32 v199, a163\n"
      "v_mul_f32 v196, v196, v194\n"
      "v_mul_f32 v197, v197, v194\n"
      "v_mul_f32 v198, v198, v194\n"
      "v_mul_f32 v199, v199, v194\n"
      "v_accvgpr_write_b32 a160, v196\n"
      "v_accvgpr_write_b32 a161, v197\n"
      "v_accvgpr_write_b32 a162, v198\n"
      "v_accvgpr_write_b32 a163, v199\n"
      "v_accvgpr_read_b32 v196, a164\n"
      "v_accvgpr_read_b32 v197, a165\n"
      "v_accvgpr_read_b32 v198, a166\n"
      "v_accvgpr_read_b32 v199, a167\n"
      "v_mul_f32 v196, v196, v195\n"
      "v_mul_f32 v197, v197, v195\n"
      "v_mul_f32 v198, v198, v195\n"
      "v_mul_f32 v199, v199, v195\n"
      "v_accvgpr_write_b32 a164, v196\n"
      "v_accvgpr_write_b32 a165, v197\n"
      "v_accvgpr_write_b32 a166, v198\n"
      "v_accvgpr_write_b32 a167, v199\n"
      "v_accvgpr_read_b32 v196, a168\n"
      "v_accvgpr_read_b32 v197, a169\n"
      "v_accvgpr_read_b32 v198, a170\n"
      "v_accvgpr_read_b32 v199, a171\n"
      "v_mul_f32 v196, v196, v194\n"
      "v_mul_f32 v197, v197, v194\n"
      "v_mul_f32 v198, v198, v194\n"
      "v_mul_f32 v199, v199, v194\n"
      "v_accvgpr_write_b32 a168, v196\n"
      "v_accvgpr_write_b32 a169, v197\n"
      "v_accvgpr_write_b32 a170, v198\n"
      "v_accvgpr_write_b32 a171, v199\n"
      "v_accvgpr_read_b32 v196, a172\n"
      "v_accvgpr_read_b32 v197, a173\n"
      "v_accvgpr_read_b32 v198, a174\n"
      "v_accvgpr_read_b32 v199, a175\n"
      "v_mul_f32 v196, v196, v195\n"
      "v_mul_f32 v197, v197, v195\n"
      "v_mul_f32 v198, v198, v195\n"
      "v_mul_f32 v199, v199, v195\n"
      "v_accvgpr_write_b32 a172, v196\n"
      "v_accvgpr_write_b32 a173, v197\n"
      "v_accvgpr_write_b32 a174, v198\n"
      "v_accvgpr_write_b32 a175, v199\n"
      "v_accvgpr_read_b32 v196, a176\n"
      "v_accvgpr_read_b32 v197, a177\n"
      "v_accvgpr_read_b32 v198, a178\n"
      "v_accvgpr_read_b32 v199, a179\n"
      "v_mul_f32 v196, v196, v194\n"
      "v_mul_f32 v197, v197, v194\n"
      "v_mul_f32 v198, v198, v194\n"
      "v_mul_f32 v199, v199, v194\n"
      "v_accvgpr_write_b32 a176, v196\n"
      "v_accvgpr_write_b32 a177, v197\n"
      "v_accvgpr_write_b32 a178, v198\n"
      "v_accvgpr_write_b32 a179, v199\n"
      "v_accvgpr_read_b32 v196, a180\n"
      "v_accvgpr_read_b32 v197, a181\n"
      "v_accvgpr_read_b32 v198, a182\n"
      "v_accvgpr_read_b32 v199, a183\n"
      "v_mul_f32 v196, v196, v195\n"
      "v_mul_f32 v197, v197, v195\n"
      "v_mul_f32 v198, v198, v195\n"
      "v_mul_f32 v199, v199, v195\n"
      "v_accvgpr_write_b32 a180, v196\n"
      "v_accvgpr_write_b32 a181, v197\n"
      "v_accvgpr_write_b32 a182, v198\n"
      "v_accvgpr_write_b32 a183, v199\n"
      "v_accvgpr_read_b32 v196, a184\n"
      "v_accvgpr_read_b32 v197, a185\n"
      "v_accvgpr_read_b32 v198, a186\n"
      "v_accvgpr_read_b32 v199, a187\n"
      "v_mul_f32 v196, v196, v194\n"
      "v_mul_f32 v197, v197, v194\n"
      "v_mul_f32 v198, v198, v194\n"
      "v_mul_f32 v199, v199, v194\n"
      "v_accvgpr_write_b32 a184, v196\n"
      "v_accvgpr_write_b32 a185, v197\n"
      "v_accvgpr_write_b32 a186, v198\n"
      "v_accvgpr_write_b32 a187, v199\n"
      "v_accvgpr_read_b32 v196, a188\n"
      "v_accvgpr_read_b32 v197, a189\n"
      "v_accvgpr_read_b32 v198, a190\n"
      "v_accvgpr_read_b32 v199, a191\n"
      "v_mul_f32 v196, v196, v195\n"
      "v_mul_f32 v197, v197, v195\n"
      "v_mul_f32 v198, v198, v195\n"
      "v_mul_f32 v199, v199, v195\n"
      "v_accvgpr_write_b32 a188, v196\n"
      "v_accvgpr_write_b32 a189, v197\n"
      "v_accvgpr_write_b32 a190, v198\n"
      "v_accvgpr_write_b32 a191, v199\n"
      "v_accvgpr_read_b32 v196, a192\n"
      "v_accvgpr_read_b32 v197, a193\n"
      "v_accvgpr_read_b32 v198, a194\n"
      "v_accvgpr_read_b32 v199, a195\n"
      "v_mul_f32 v196, v196, v194\n"
      "v_mul_f32 v197, v197, v194\n"
      "v_mul_f32 v198, v198, v194\n"
      "v_mul_f32 v199, v199, v194\n"
      "v_accvgpr_write_b32 a192, v196\n"
      "v_accvgpr_write_b32 a193, v197\n"
      "v_accvgpr_write_b32 a194, v198\n"
      "v_accvgpr_write_b32 a195, v199\n"
      "v_accvgpr_read_b32 v196, a196\n"
      "v_accvgpr_read_b32 v197, a197\n"
      "v_accvgpr_read_b32 v198, a198\n"
      "v_accvgpr_read_b32 v199, a199\n"
      "v_mul_f32 v196, v196, v195\n"
      "v_mul_f32 v197, v197, v195\n"
      "v_mul_f32 v198, v198, v195\n"
      "v_mul_f32 v199, v199, v195\n"
      "v_accvgpr_write_b32 a196, v196\n"
      "v_accvgpr_write_b32 a197, v197\n"
      "v_accvgpr_write_b32 a198, v198\n"
      "v_accvgpr_write_b32 a199, v199\n"
      "v_accvgpr_read_b32 v196, a200\n"
      "v_accvgpr_read_b32 v197, a201\n"
      "v_accvgpr_read_b32 v198, a202\n"
      "v_accvgpr_read_b32 v199, a203\n"
      "v_mul_f32 v196, v196, v194\n"
      "v_mul_f32 v197, v197, v194\n"
      "v_mul_f32 v198, v198, v194\n"
      "v_mul_f32 v199, v199, v194\n"
      "v_accvgpr_write_b32 a200, v196\n"
      "v_accvgpr_write_b32 a201, v197\n"
      "v_accvgpr_write_b32 a202, v198\n"
      "v_accvgpr_write_b32 a203, v199\n"
      "v_accvgpr_read_b32 v196, a204\n"
      "v_accvgpr_read_b32 v197, a205\n"
      "v_accvgpr_read_b32 v198, a206\n"
      "v_accvgpr_read_b32 v199, a207\n"
      "v_mul_f32 v196, v196, v195\n"
      "v_mul_f32 v197, v197, v195\n"
      "v_mul_f32 v198, v198, v195\n"
      "v_mul_f32 v199, v199, v195\n"
      "v_accvgpr_write_b32 a204, v196\n"
      "v_accvgpr_write_b32 a205, v197\n"
      "v_accvgpr_write_b32 a206, v198\n"
      "v_accvgpr_write_b32 a207, v199\n"
      "v_mul_f32 v180, v180, v192\n"
      "v_mul_f32 v181, v181, v193\n"
      "v_mul_f32 v182, v182, v194\n"
      "v_mul_f32 v183, v183, v195\n"
      "v_exp_f32 v188, v64\n"
      "v_exp_f32 v189, v65\n"
      "v_exp_f32 v190, v66\n"
      "v_exp_f32 v191, v67\n"
      "v_cvt_pk_bf16_f32 v128, v188, v189\n"
      "v_cvt_pk_bf16_f32 v129, v190, v191\n"
      "v_exp_f32 v192, v68\n"
      "v_exp_f32 v193, v69\n"
      "v_exp_f32 v194, v70\n"
      "v_exp_f32 v195, v71\n"
      "v_cvt_pk_bf16_f32 v130, v192, v193\n"
      "v_cvt_pk_bf16_f32 v131, v194, v195\n"
      "v_exp_f32 v188, v72\n"
      "v_exp_f32 v189, v73\n"
      "v_exp_f32 v190, v74\n"
      "v_exp_f32 v191, v75\n"
      "v_cvt_pk_bf16_f32 v132, v188, v189\n"
      "v_cvt_pk_bf16_f32 v133, v190, v191\n"
      "v_exp_f32 v192, v76\n"
      "v_exp_f32 v193, v77\n"
      "v_exp_f32 v194, v78\n"
      "v_exp_f32 v195, v79\n"
      "v_cvt_pk_bf16_f32 v134, v192, v193\n"
      "v_cvt_pk_bf16_f32 v135, v194, v195\n"
      "v_exp_f32 v188, v80\n"
      "v_exp_f32 v189, v81\n"
      "v_exp_f32 v190, v82\n"
      "v_exp_f32 v191, v83\n"
      "v_cvt_pk_bf16_f32 v136, v188, v189\n"
      "v_cvt_pk_bf16_f32 v137, v190, v191\n"
      "v_exp_f32 v192, v84\n"
      "v_exp_f32 v193, v85\n"
      "v_exp_f32 v194, v86\n"
      "v_exp_f32 v195, v87\n"
      "v_cvt_pk_bf16_f32 v138, v192, v193\n"
      "v_cvt_pk_bf16_f32 v139, v194, v195\n"
      "v_exp_f32 v188, v88\n"
      "v_exp_f32 v189, v89\n"
      "v_exp_f32 v190, v90\n"
      "v_exp_f32 v191, v91\n"
      "v_cvt_pk_bf16_f32 v140, v188, v189\n"
      "v_cvt_pk_bf16_f32 v141, v190, v191\n"
      "v_exp_f32 v192, v92\n"
      "v_exp_f32 v193, v93\n"
      "v_exp_f32 v194, v94\n"
      "v_exp_f32 v195, v95\n"
      "v_cvt_pk_bf16_f32 v142, v192, v193\n"
      "v_cvt_pk_bf16_f32 v143, v194, v195\n"
      "s_nop 1\n"
      "v_mfma_f32_16x16x32_bf16 v[184:187], a[128:131], v[128:131], 0\n"
      "v_mfma_f32_16x16x32_bf16 v[184:187], a[132:135], v[132:135], v[184:187]\n"
      "v_mfma_f32_16x16x32_bf16 v[184:187], a[136:139], v[136:139], v[184:187]\n"
      "v_mfma_f32_16x16x32_bf16 v[184:187], a[140:143], v[140:143], v[184:187]\n"
      "s_nop 7\n"
      "s_setpc_b64 s[96:97]\n"
      "FB1_%=:\n"
      "v_mov_b32 v196, 139248\n"
      "s_nop 7\n"
      "ds_write_b32 v196, v196\n"
      "v_max3_f32 v188, v96, v97, 0\n"
      "v_max3_f32 v189, v104, v105, 0\n"
      "v_max3_f32 v190, v112, v113, 0\n"
      "v_max3_f32 v191, v120, v121, 0\n"
      "v_max3_f32 v188, v188, v98, v99\n"
      "v_max3_f32 v189, v189, v106, v107\n"
      "v_max3_f32 v190, v190, v114, v115\n"
      "v_max3_f32 v191, v191, v122, v123\n"
      "v_max3_f32 v188, v188, v100, v101\n"
      "v_max3_f32 v189, v189, v108, v109\n"
      "v_max3_f32 v190, v190, v116, v117\n"
      "v_max3_f32 v191, v191, v124, v125\n"
      "v_max3_f32 v188, v188, v102, v103\n"
      "v_max3_f32 v189, v189, v110, v111\n"
      "v_max3_f32 v190, v190, v118, v119\n"
      "v_max3_f32 v191, v191, v126, v127\n"
      "v_mov_b32 v192, v188\n"
      "v_mov_b32 v193, v189\n"
      "v_mov_b32 v194, v190\n"
      "v_mov_b32 v195, v191\n"
      "v_permlane16_swap_b32 v188, v192\n"
      "v_permlane16_swap_b32 v189, v193\n"
      "v_permlane16_swap_b32 v190, v194\n"
      "v_permlane16_swap_b32 v191, v195\n"
      "v_max_f32 v188, v188, v192\n"
      "v_max_f32 v189, v189, v193\n"
      "v_max_f32 v190, v190, v194\n"
      "v_max_f32 v191, v191, v195\n"
      "v_mov_b32 v192, v188\n"
      "v_mov_b32 v193, v189\n"
      "v_mov_b32 v194, v190\n"
      "v_mov_b32 v195, v191\n"
      "v_permlane32_swap_b32 v188, v192\n"
      "v_permlane32_swap_b32 v189, v193\n"
      "v_permlane32_swap_b32 v190, v194\n"
      "v_permlane32_swap_b32 v191, v195\n"
      "v_max_f32 v188, v188, v192\n"
      "v_max_f32 v189, v189, v193\n"
      "v_max_f32 v190, v190, v194\n"
      "v_max_f32 v191, v191, v195\n"
      "RS1_%=:\n"
      "v_exp_f32 v192, -v188\n"
      "v_exp_f32 v193, -v189\n"
      "v_exp_f32 v194, -v190\n"
      "v_exp_f32 v195, -v191\n"
      "v_sub_f32 v176, v176, v188\n"
      "v_sub_f32 v177, v177, v189\n"
      "v_sub_f32 v178, v178, v190\n"
      "v_sub_f32 v179, v179, v191\n"
      "v_mov_b32 v160, v176\n"
      "v_mov_b32 v161, v176\n"
      "v_mov_b32 v162, v176\n"
      "v_mov_b32 v163, v176\n"
      "v_mov_b32 v164, v177\n"
      "v_mov_b32 v165, v177\n"
      "v_mov_b32 v166, v177\n"
      "v_mov_b32 v167, v177\n"
      "v_mov_b32 v168, v178\n"
      "v_mov_b32 v169, v178\n"
      "v_mov_b32 v170, v178\n"
      "v_mov_b32 v171, v178\n"
      "v_mov_b32 v172, v179\n"
      "v_mov_b32 v173, v179\n"
      "v_mov_b32 v174, v179\n"
      "v_mov_b32 v175, v179\n"
      "v_sub_f32 v64, v64, v188\n"
      "v_sub_f32 v65, v65, v188\n"
      "v_sub_f32 v66, v66, v188\n"
      "v_sub_f32 v67, v67, v188\n"
      "v_sub_f32 v68, v68, v188\n"
      "v_sub_f32 v69, v69, v188\n"
      "v_sub_f32 v70, v70, v188\n"
      "v_sub_f32 v71, v71, v188\n"
      "v_sub_f32 v72, v72, v189\n"
      "v_sub_f32 v73, v73, v189\n"
      "v_sub_f32 v74, v74, v189\n"
      "v_sub_f32 v75, v75, v189\n"
      "v_sub_f32 v76, v76, v189\n"
      "v_sub_f32 v77, v77, v189\n"
      "v_sub_f32 v78, v78, v189\n"
      "v_sub_f32 v79, v79, v189\n"
      "v_sub_f32 v80, v80, v190\n"
      "v_sub_f32 v81, v81, v190\n"
      "v_sub_f32 v82, v82, v190\n"
      "v_sub_f32 v83, v83, v190\n"
      "v_sub_f32 v84, v84, v190\n"
      "v_sub_f32 v85, v85, v190\n"
      "v_sub_f32 v86, v86, v190\n"
      "v_sub_f32 v87, v87, v190\n"
      "v_sub_f32 v88, v88, v191\n"
      "v_sub_f32 v89, v89, v191\n"
      "v_sub_f32 v90, v90, v191\n"
      "v_sub_f32 v91, v91, v191\n"
      "v_sub_f32 v92, v92, v191\n"
      "v_sub_f32 v93, v93, v191\n"
      "v_sub_f32 v94, v94, v191\n"
      "v_sub_f32 v95, v95, v191\n"
      "v_sub_f32 v96, v96, v188\n"
      "v_sub_f32 v97, v97, v188\n"
      "v_sub_f32 v98, v98, v188\n"
      "v_sub_f32 v99, v99, v188\n"
      "v_sub_f32 v100, v100, v188\n"
      "v_sub_f32 v101, v101, v188\n"
      "v_sub_f32 v102, v102, v188\n"
      "v_sub_f32 v103, v103, v188\n"
      "v_sub_f32 v104, v104, v189\n"
      "v_sub_f32 v105, v105, v189\n"
      "v_sub_f32 v106, v106, v189\n"
      "v_sub_f32 v107, v107, v189\n"
      "v_sub_f32 v108, v108, v189\n"
      "v_sub_f32 v109, v109, v189\n"
      "v_sub_f32 v110, v110, v189\n"
      "v_sub_f32 v111, v111, v189\n"
      "v_sub_f32 v112, v112, v190\n"
      "v_sub_f32 v113, v113, v190\n"
      "v_sub_f32 v114, v114, v190\n"
      "v_sub_f32 v115, v115, v190\n"
      "v_sub_f32 v116, v116, v190\n"
      "v_sub_f32 v117, v117, v190\n"
      "v_sub_f32 v118, v118, v190\n"
      "v_sub_f32 v119, v119, v190\n"
      "v_sub_f32 v120, v120, v191\n"
      "v_sub_f32 v121, v121, v191\n"
      "v_sub_f32 v122, v122, v191\n"
      "v_sub_f32 v123, v123, v191\n"
      "v_sub_f32 v124, v124, v191\n"
      "v_sub_f32 v125, v125, v191\n"
      "v_sub_f32 v126, v126, v191\n"
      "v_sub_f32 v127, v127, v191\n"
      "v_mul_f32 v0, v0, v192\n"
      "v_mul_f32 v1, v1, v192\n"
      "v_mul_f32 v2, v2, v192\n"
      "v_mul_f32 v3, v3, v192\n"
      "v_mul_f32 v4, v4, v193\n"
      "v_mul_f32 v5, v5, v193\n"
      "v_mul_f32 v6, v6, v193\n"
      "v_mul_f32 v7, v7, v193\n"
      "v_mul_f32 v8, v8, v192\n"
      "v_mul_f32 v9, v9, v192\n"
      "v_mul_f32 v10, v10, v192\n"
      "v_mul_f32 v11, v11, v192\n"
      "v_mul_f32 v12, v12, v193\n"
      "v_mul_f32 v13, v13, v193\n"
      "v_mul_f32 v14, v14, v193\n"
      "v_mul_f32 v15, v15, v193\n"
      "v_mul_f32 v16, v16, v192\n"
      "v_mul_f32 v17, v17, v192\n"
      "v_mul_f32 v18, v18, v192\n"
      "v_mul_f32 v19, v19, v192\n"
      "v_mul_f32 v20, v20, v193\n"
      "v_mul_f32 v21, v21, v193\n"
      "v_mul_f32 v22, v22, v193\n"
      "v_mul_f32 v23, v23, v193\n"
      "v_mul_f32 v24, v24, v192\n"
      "v_mul_f32 v25, v25, v192\n"
      "v_mul_f32 v26, v26, v192\n"
      "v_mul_f32 v27, v27, v192\n"
      "v_mul_f32 v28, v28, v193\n"
      "v_mul_f32 v29, v29, v193\n"
      "v_mul_f32 v30, v30, v193\n"
      "v_mul_f32 v31, v31, v193\n"
      "v_mul_f32 v32, v32, v192\n"
      "v_mul_f32 v33, v33, v192\n"
      "v_mul_f32 v34, v34, v192\n"
      "v_mul_f32 v35, v35, v192\n"
      "v_mul_f32 v36, v36, v193\n"
      "v_mul_f32 v37, v37, v193\n"
      "v_mul_f32 v38, v38, v193\n"
      "v_mul_f32 v39, v39, v193\n"
      "v_mul_f32 v40, v40, v192\n"
      "v_mul_f32 v41, v41, v192\n"
      "v_mul_f32 v42, v42, v192\n"
      "v_mul_f32 v43, v43, v192\n"
      "v_mul_f32 v44, v44, v193\n"
      "v_mul_f32 v45, v45, v193\n"
      "v_mul_f32 v46, v46, v193\n"
      "v_mul_f32 v47, v47, v193\n"
      "v_mul_f32 v48, v48, v192\n"
      "v_mul_f32 v49, v49, v192\n"
      "v_mul_f32 v50, v50, v192\n"
      "v_mul_f32 v51, v51, v192\n"
      "v_mul_f32 v52, v52, v193\n"
      "v_mul_f32 v53, v53, v193\n"
      "v_mul_f32 v54, v54, v193\n"
      "v_mul_f32 v55, v55, v193\n"
      "v_mul_f32 v56, v56, v192\n"
      "v_mul_f32 v57, v57, v192\n"
      "v_mul_f32 v58, v58, v192\n"
      "v_mul_f32 v59, v59, v192\n"
      "v_mul_f32 v60, v60, v193\n"
      "v_mul_f32 v61, v61, v193\n"
      "v_mul_f32 v62, v62, v193\n"
      "v_mul_f32 v63, v63, v193\n"
      "v_accvgpr_read_b32 v196, a144\n"
      "v_accvgpr_read_b32 v197, a145\n"
      "v_accvgpr_read_b32 v198, a146\n"
      "v_accvgpr_read_b32 v199, a147\n"
      "v_mul_f32 v196, v196, v194\n"
      "v_mul_f32 v197, v197, v194\n"
      "v_mul_f32 v198, v198, v194\n"
      "v_mul_f32 v199, v199, v194\n"
      "v_accvgpr_write_b32 a144, v196\n"
      "v_accvgpr_write_b32 a145, v197\n"
      "v_accvgpr_write_b32 a146, v198\n"
      "v_accvgpr_write_b32 a147, v199\n"
      "v_accvgpr_read_b32 v196, a148\n"
      "v_accvgpr_read_b32 v197, a149\n"
      "v_accvgpr_read_b32 v198, a150\n"
      "v_accvgpr_read_b32 v199, a151\n"
      "v_mul_f32 v196, v196, v195\n"
      "v_mul_f32 v197, v197, v195\n"
      "v_mul_f32 v198, v198, v195\n"
      "v_mul_f32 v199, v199, v195\n"
      "v_accvgpr_write_b32 a148, v196\n"
      "v_accvgpr_write_b32 a149, v197\n"
      "v_accvgpr_write_b32 a150, v198\n"
      "v_accvgpr_write_b32 a151, v199\n"
      "v_accvgpr_read_b32 v196, a152\n"
      "v_accvgpr_read_b32 v197, a153\n"
      "v_accvgpr_read_b32 v198, a154\n"
      "v_accvgpr_read_b32 v199, a155\n"
      "v_mul_f32 v196, v196, v194\n"
      "v_mul_f32 v197, v197, v194\n"
      "v_mul_f32 v198, v198, v194\n"
      "v_mul_f32 v199, v199, v194\n"
      "v_accvgpr_write_b32 a152, v196\n"
      "v_accvgpr_write_b32 a153, v197\n"
      "v_accvgpr_write_b32 a154, v198\n"
      "v_accvgpr_write_b32 a155, v199\n"
      "v_accvgpr_read_b32 v196, a156\n"
      "v_accvgpr_read_b32 v197, a157\n"
      "v_accvgpr_read_b32 v198, a158\n"
      "v_accvgpr_read_b32 v199, a159\n"
      "v_mul_f32 v196, v196, v195\n"
      "v_mul_f32 v197, v197, v195\n"
      "v_mul_f32 v198, v198, v195\n"
      "v_mul_f32 v199, v199, v195\n"
      "v_accvgpr_write_b32 a156, v196\n"
      "v_accvgpr_write_b32 a157, v197\n"
      "v_accvgpr_write_b32 a158, v198\n"
      "v_accvgpr_write_b32 a159, v199\n"
      "v_accvgpr_read_b32 v196, a160\n"
      "v_accvgpr_read_b32 v197, a161\n"
      "v_accvgpr_read_b32 v198, a162\n"
      "v_accvgpr_read_b32 v199, a163\n"
      "v_mul_f32 v196, v196, v194\n"
      "v_mul_f32 v197, v197, v194\n"
      "v_mul_f32 v198, v198, v194\n"
      "v_mul_f32 v199, v199, v194\n"
      "v_accvgpr_write_b32 a160, v196\n"
      "v_accvgpr_write_b32 a161, v197\n"
      "v_accvgpr_write_b32 a162, v198\n"
      "v_accvgpr_write_b32 a163, v199\n"
      "v_accvgpr_read_b32 v196, a164\n"
      "v_accvgpr_read_b32 v197, a165\n"
      "v_accvgpr_read_b32 v198, a166\n"
      "v_accvgpr_read_b32 v199, a167\n"
      "v_mul_f32 v196, v196, v195\n"
      "v_mul_f32 v197, v197, v195\n"
      "v_mul_f32 v198, v198, v195\n"
      "v_mul_f32 v199, v199, v195\n"
      "v_accvgpr_write_b32 a164, v196\n"
      "v_accvgpr_write_b32 a165, v197\n"
      "v_accvgpr_write_b32 a166, v198\n"
      "v_accvgpr_write_b32 a167, v199\n"
      "v_accvgpr_read_b32 v196, a168\n"
      "v_accvgpr_read_b32 v197, a169\n"
      "v_accvgpr_read_b32 v198, a170\n"
      "v_accvgpr_read_b32 v199, a171\n"
      "v_mul_f32 v196, v196, v194\n"
      "v_mul_f32 v197, v197, v194\n"
      "v_mul_f32 v198, v198, v194\n"
      "v_mul_f32 v199, v199, v194\n"
      "v_accvgpr_write_b32 a168, v196\n"
      "v_accvgpr_write_b32 a169, v197\n"
      "v_accvgpr_write_b32 a170, v198\n"
      "v_accvgpr_write_b32 a171, v199\n"
      "v_accvgpr_read_b32 v196, a172\n"
      "v_accvgpr_read_b32 v197, a173\n"
      "v_accvgpr_read_b32 v198, a174\n"
      "v_accvgpr_read_b32 v199, a175\n"
      "v_mul_f32 v196, v196, v195\n"
      "v_mul_f32 v197, v197, v195\n"
      "v_mul_f32 v198, v198, v195\n"
      "v_mul_f32 v199, v199, v195\n"
      "v_accvgpr_write_b32 a172, v196\n"
      "v_accvgpr_write_b32 a173, v197\n"
      "v_accvgpr_write_b32 a174, v198\n"
      "v_accvgpr_write_b32 a175, v199\n"
      "v_accvgpr_read_b32 v196, a176\n"
      "v_accvgpr_read_b32 v197, a177\n"
      "v_accvgpr_read_b32 v198, a178\n"
      "v_accvgpr_read_b32 v199, a179\n"
      "v_mul_f32 v196, v196, v194\n"
      "v_mul_f32 v197, v197, v194\n"
      "v_mul_f32 v198, v198, v194\n"
      "v_mul_f32 v199, v199, v194\n"
      "v_accvgpr_write_b32 a176, v196\n"
      "v_accvgpr_write_b32 a177, v197\n"
      "v_accvgpr_write_b32 a178, v198\n"
      "v_accvgpr_write_b32 a179, v199\n"
      "v_accvgpr_read_b32 v196, a180\n"
      "v_accvgpr_read_b32 v197, a181\n"
      "v_accvgpr_read_b32 v198, a182\n"
      "v_accvgpr_read_b32 v199, a183\n"
      "v_mul_f32 v196, v196, v195\n"
      "v_mul_f32 v197, v197, v195\n"
      "v_mul_f32 v198, v198, v195\n"
      "v_mul_f32 v199, v199, v195\n"
      "v_accvgpr_write_b32 a180, v196\n"
      "v_accvgpr_write_b32 a181, v197\n"
      "v_accvgpr_write_b32 a182, v198\n"
      "v_accvgpr_write_b32 a183, v199\n"
      "v_accvgpr_read_b32 v196, a184\n"
      "v_accvgpr_read_b32 v197, a185\n"
      "v_accvgpr_read_b32 v198, a186\n"
      "v_accvgpr_read_b32 v199, a187\n"
      "v_mul_f32 v196, v196, v194\n"
      "v_mul_f32 v197, v197, v194\n"
      "v_mul_f32 v198, v198, v194\n"
      "v_mul_f32 v199, v199, v194\n"
      "v_accvgpr_write_b32 a184, v196\n"
      "v_accvgpr_write_b32 a185, v197\n"
      "v_accvgpr_write_b32 a186, v198\n"
      "v_accvgpr_write_b32 a187, v199\n"
      "v_accvgpr_read_b32 v196, a188\n"
      "v_accvgpr_read_b32 v197, a189\n"
      "v_accvgpr_read_b32 v198, a190\n"
      "v_accvgpr_read_b32 v199, a191\n"
      "v_mul_f32 v196, v196, v195\n"
      "v_mul_f32 v197, v197, v195\n"
      "v_mul_f32 v198, v198, v195\n"
      "v_mul_f32 v199, v199, v195\n"
      "v_accvgpr_write_b32 a188, v196\n"
      "v_accvgpr_write_b32 a189, v197\n"
      "v_accvgpr_write_b32 a190, v198\n"
      "v_accvgpr_write_b32 a191, v199\n"
      "v_accvgpr_read_b32 v196, a192\n"
      "v_accvgpr_read_b32 v197, a193\n"
      "v_accvgpr_read_b32 v198, a194\n"
      "v_accvgpr_read_b32 v199, a195\n"
      "v_mul_f32 v196, v196, v194\n"
      "v_mul_f32 v197, v197, v194\n"
      "v_mul_f32 v198, v198, v194\n"
      "v_mul_f32 v199, v199, v194\n"
      "v_accvgpr_write_b32 a192, v196\n"
      "v_accvgpr_write_b32 a193, v197\n"
      "v_accvgpr_write_b32 a194, v198\n"
      "v_accvgpr_write_b32 a195, v199\n"
      "v_accvgpr_read_b32 v196, a196\n"
      "v_accvgpr_read_b32 v197, a197\n"
      "v_accvgpr_read_b32 v198, a198\n"
      "v_accvgpr_read_b32 v199, a199\n"
      "v_mul_f32 v196, v196, v195\n"
      "v_mul_f32 v197, v197, v195\n"
      "v_mul_f32 v198, v198, v195\n"
      "v_mul_f32 v199, v199, v195\n"
      "v_accvgpr_write_b32 a196, v196\n"
      "v_accvgpr_write_b32 a197, v197\n"
      "v_accvgpr_write_b32 a198, v198\n"
      "v_accvgpr_write_b32 a199, v199\n"
      "v_accvgpr_read_b32 v196, a200\n"
      "v_accvgpr_read_b32 v197, a201\n"
      "v_accvgpr_read_b32 v198, a202\n"
      "v_accvgpr_read_b32 v199, a203\n"
      "v_mul_f32 v196, v196, v194\n"
      "v_mul_f32 v197, v197, v194\n"
      "v_mul_f32 v198, v198, v194\n"
      "v_mul_f32 v199, v199, v194\n"
      "v_accvgpr_write_b32 a200, v196\n"
      "v_accvgpr_write_b32 a201, v197\n"
      "v_accvgpr_write_b32 a202, v198\n"
      "v_accvgpr_write_b32 a203, v199\n"
      "v_accvgpr_read_b32 v196, a204\n"
      "v_accvgpr_read_b32 v197, a205\n"
      "v_accvgpr_read_b32 v198, a206\n"
      "v_accvgpr_read_b32 v199, a207\n"
      "v_mul_f32 v196, v196, v195\n"
      "v_mul_f32 v197, v197, v195\n"
      "v_mul_f32 v198, v198, v195\n"
      "v_mul_f32 v199, v199, v195\n"
      "v_accvgpr_write_b32 a204, v196\n"
      "v_accvgpr_write_b32 a205, v197\n"
      "v_accvgpr_write_b32 a206, v198\n"
      "v_accvgpr_write_b32 a207, v199\n"
      "v_mul_f32 v180, v180, v192\n"
      "v_mul_f32 v181, v181, v193\n"
      "v_mul_f32 v182, v182, v194\n"
      "v_mul_f32 v183, v183, v195\n"
      "v_lshlrev_b32 v196, 16, v128\n"
      "v_and_b32 v197, 0xffff0000, v128\n"
      "v_mul_f32 v196, v196, v192\n"
      "v_mul_f32 v197, v197, v192\n"
      "v_cvt_pk_bf16_f32 v128, v196, v197\n"
      "v_lshlrev_b32 v196, 16, v129\n"
      "v_and_b32 v197, 0xffff0000, v129\n"
      "v_mul_f32 v196, v196, v192\n"
      "v_mul_f32 v197, v197, v192\n"
      "v_cvt_pk_bf16_f32 v129, v196, v197\n"
      "v_lshlrev_b32 v196, 16, v130\n"
      "v_and_b32 v197, 0xffff0000, v130\n"
      "v_mul_f32 v196, v196, v192\n"
      "v_mul_f32 v197, v197, v192\n"
      "v_cvt_pk_bf16_f32 v130, v196, v197\n"
      "v_lshlrev_b32 v196, 16, v131\n"
      "v_and_b32 v197, 0xffff0000, v131\n"
      "v_mul_f32 v196, v196, v192\n"
      "v_mul_f32 v197, v197, v192\n"
      "v_cvt_pk_bf16_f32 v131, v196, v197\n"
      "v_lshlrev_b32 v196, 16, v132\n"
      "v_and_b32 v197, 0xffff0000, v132\n"
      "v_mul_f32 v196, v196, v193\n"
      "v_mul_f32 v197, v197, v193\n"
      "v_cvt_pk_bf16_f32 v132, v196, v197\n"
      "v_lshlrev_b32 v196, 16, v133\n"
      "v_and_b32 v197, 0xffff0000, v133\n"
      "v_mul_f32 v196, v196, v193\n"
      "v_mul_f32 v197, v197, v193\n"
      "v_cvt_pk_bf16_f32 v133, v196, v197\n"
      "v_lshlrev_b32 v196, 16, v134\n"
      "v_and_b32 v197, 0xffff0000, v134\n"
      "v_mul_f32 v196, v196, v193\n"
      "v_mul_f32 v197, v197, v193\n"
      "v_cvt_pk_bf16_f32 v134, v196, v197\n"
      "v_lshlrev_b32 v196, 16, v135\n"
      "v_and_b32 v197, 0xffff0000, v135\n"
      "v_mul_f32 v196, v196, v193\n"
      "v_mul_f32 v197, v197, v193\n"
      "v_cvt_pk_bf16_f32 v135, v196, v197\n"
      "v_lshlrev_b32 v196, 16, v136\n"
      "v_and_b32 v197, 0xffff0000, v136\n"
      "v_mul_f32 v196, v196, v194\n"
      "v_mul_f32 v197, v197, v194\n"
      "v_cvt_pk_bf16_f32 v136, v196, v197\n"
      "v_lshlrev_b32 v196, 16, v137\n"
      "v_and_b32 v197, 0xffff0000, v137\n"
      "v_mul_f32 v196, v196, v194\n"
      "v_mul_f32 v197, v197, v194\n"
      "v_cvt_pk_bf16_f32 v137, v196, v197\n"
      "v_lshlrev_b32 v196, 16, v138\n"
      "v_and_b32 v197, 0xffff0000, v138\n"
      "v_mul_f32 v196, v196, v194\n"
      "v_mul_f32 v197, v197, v194\n"
      "v_cvt_pk_bf16_f32 v138, v196, v197\n"
      "v_lshlrev_b32 v196, 16, v139\n"
      "v_and_b32 v197, 0xffff0000, v139\n"
      "v_mul_f32 v196, v196, v194\n"
      "v_mul_f32 v197, v197, v194\n"
      "v_cvt_pk_bf16_f32 v139, v196, v197\n"
      "v_lshlrev_b32 v196, 16, v140\n"
      "v_and_b32 v197, 0xffff0000, v140\n"
      "v_mul_f32 v196, v196, v195\n"
      "v_mul_f32 v197, v197, v195\n"
      "v_cvt_pk_bf16_f32 v140, v196, v197\n"
      "v_lshlrev_b32 v196, 16, v141\n"
      "v_and_b32 v197, 0xffff0000, v141\n"
      "v_mul_f32 v196, v196, v195\n"
      "v_mul_f32 v197, v197, v195\n"
      "v_cvt_pk_bf16_f32 v141, v196, v197\n"
      "v_lshlrev_b32 v196, 16, v142\n"
      "v_and_b32 v197, 0xffff0000, v142\n"
      "v_mul_f32 v196, v196, v195\n"
      "v_mul_f32 v197, v197, v195\n"
      "v_cvt_pk_bf16_f32 v142, v196, v197\n"
      "v_lshlrev_b32 v196, 16, v143\n"
      "v_and_b32 v197, 0xffff0000, v143\n"
      "v_mul_f32 v196, v196, v195\n"
      "v_mul_f32 v197, v197, v195\n"
      "v_cvt_pk_bf16_f32 v143, v196, v197\n"
      "v_exp_f32 v188, v96\n"
      "v_exp_f32 v189, v97\n"
      "v_exp_f32 v190, v98\n"
      "v_exp_f32 v191, v99\n"
      "v_cvt_pk_bf16_f32 v144, v188, v189\n"
      "v_cvt_pk_bf16_f32 v145, v190, v191\n"
      "v_exp_f32 v192, v100\n"
      "v_exp_f32 v193, v101\n"
      "v_exp_f32 v194, v102\n"
      "v_exp_f32 v195, v103\n"
      "v_cvt_pk_bf16_f32 v146, v192, v193\n"
      "v_cvt_pk_bf16_f32 v147, v194, v195\n"
      "v_exp_f32 v188, v104\n"
      "v_exp_f32 v189, v105\n"
      "v_exp_f32 v190, v106\n"
      "v_exp_f32 v191, v107\n"
      "v_cvt_pk_bf16_f32 v148, v188, v189\n"
      "v_cvt_pk_bf16_f32 v149, v190, v191\n"
      "v_exp_f32 v192, v108\n"
      "v_exp_f32 v193, v109\n"
      "v_exp_f32 v194, v110\n"
      "v_exp_f32 v195, v111\n"
      "v_cvt_pk_bf16_f32 v150, v192, v193\n"
      "v_cvt_pk_bf16_f32 v151, v194, v195\n"
      "v_exp_f32 v188, v112\n"
      "v_exp_f32 v189, v113\n"
      "v_exp_f32 v190, v114\n"
      "v_exp_f32 v191, v115\n"
      "v_cvt_pk_bf16_f32 v152, v188, v189\n"
      "v_cvt_pk_bf16_f32 v153, v190, v191\n"
      "v_exp_f32 v192, v116\n"
      "v_exp_f32 v193, v117\n"
      "v_exp_f32 v194, v118\n"
      "v_exp_f32 v195, v119\n"
      "v_cvt_pk_bf16_f32 v154, v192, v193\n"
      "v_cvt_pk_bf16_f32 v155, v194, v195\n"
      "v_exp_f32 v188, v120\n"
      "v_exp_f32 v189, v121\n"
      "v_exp_f32 v190, v122\n"
      "v_exp_f32 v191, v123\n"
      "v_cvt_pk_bf16_f32 v156, v188, v189\n"
      "v_cvt_pk_bf16_f32 v157, v190, v191\n"
      "v_exp_f32 v192, v124\n"
      "v_exp_f32 v193, v125\n"
      "v_exp_f32 v194, v126\n"
      "v_exp_f32 v195, v127\n"
      "v_cvt_pk_bf16_f32 v158, v192, v193\n"
      "v_cvt_pk_bf16_f32 v159, v194, v195\n"
      "s_nop 1\n"
      "v_mfma_f32_16x16x32_bf16 v[184:187], a[128:131], v[144:147], 0\n"
      "v_mfma_f32_16x16x32_bf16 v[184:187], a[132:135], v[148:151], v[184:187]\n"
      "v_mfma_f32_16x16x32_bf16 v[184:187], a[136:139], v[152:155], v[184:187]\n"
      "v_mfma_f32_16x16x32_bf16 v[184:187], a[140:143], v[156:159], v[184:187]\n"
      "s_nop 7\n"
      "s_setpc_b64 s[96:97]\n"
      "SLOW_%=:\n"
      "ds_write_b128 v234, v[176:179] offset:5120\n"
      "s_waitcnt lgkmcnt(0)\n"
      "s_barrier\n"
      "ds_read_b128 v[192:195], v235 offset:5120\n"
      "s_waitcnt lgkmcnt(0)\n"
      "v_sub_f32 v188, v176, v194\n"
      "v_sub_f32 v189, v177, v195\n"
      "v_sub_f32 v190, v178, v192\n"
      "v_sub_f32 v191, v179, v193\n"
      "v_max_f32 v188, 0, v188\n"
      "v_max_f32 v189, 0, v189\n"
      "v_max_f32 v190, 0, v190\n"
      "v_max_f32 v191, 0, v191\n"
      "s_call_b64 s[96:97], RS1_%=\n"
      "ds_write_b128 v232, a[176:179] offset:0\n"
      "ds_write_b128 v232, a[180:183] offset:1024\n"
      "ds_write_b128 v232, a[184:187] offset:2048\n"
      "ds_write_b128 v232, a[188:191] offset:3072\n"
      "ds_write_b128 v232, a[192:195] offset:4096\n"
      "ds_write_b128 v232, a[196:199] offset:5120\n"
      "ds_write_b128 v232, a[200:203] offset:6144\n"
      "ds_write_b128 v232, a[204:207] offset:7168\n"
      "ds_write_b128 v234, v[152:155] offset:0\n"
      "ds_write_b128 v234, v[156:159] offset:1024\n"
      "v_cndmask_b32 v229, v180, v181, s[74:75]\n"
      "v_cndmask_b32 v200, v182, v183, s[74:75]\n"
      "v_cndmask_b32 v228, v176, v177, s[74:75]\n"
      "v_cndmask_b32 v202, v186, v187, s[74:75]\n"
      "v_cndmask_b32 v201, v184, v185, s[74:75]\n"
      "v_add_f32 v200, v200, v202\n"
      "ds_write_b32 v234, v200 offset:4096\n"
      "v_add_f32 v229, v229, v201\n"
      "s_waitcnt lgkmcnt(0)\n"
      "s_barrier\n"
      "ds_read_b32 v208, v209\n"
      "ds_read_b128 v[160:163], v235 offset:0\n"
      "ds_read_b128 v[164:167], v235 offset:1024\n"
      "ds_read_b128 v[64:67], v233 offset:0\n"
      "ds_read_b128 v[68:71], v233 offset:1024\n"
      "ds_read_b128 v[72:75], v233 offset:2048\n"
      "ds_read_b128 v[76:79], v233 offset:3072\n"
      "ds_read_b128 v[80:83], v233 offset:4096\n"
      "ds_read_b128 v[84:87], v233 offset:5120\n"
      "ds_read_b128 v[88:91], v233 offset:6144\n"
      "ds_read_b128 v[92:95], v233 offset:7168\n"
      "ds_read_b32 v204, v235 offset:4096\n"
      "s_branch AGAIN_%=\n"
      ".subsection 0\n"
      : "+{v0}"(tid)
      : [blk] "s"(blockIdx.x), [kp] "s"(__builtin_amdgcn_kernarg_segment_ptr()), "s"(lds0)
      : "v1", "v2", "v3", "v4", "v5", "v6", "v7", "v8", "v9", "v10", "v11", "v12", "v13", "v14", "v15", "v16", "v17", "v18", "v19", "v20", "v21", "v22", "v23", "v24", "v25", "v26", "v27", "v28", "v29", "v30", "v31", "v32", "v33", "v34", "v35", "v36", "v37", "v38", "v39", "v40", "v41", "v42", "v43", "v44", "v45", "v46", "v47", "v48", "v49", "v50", "v51", "v52", "v53", "v54", "v55", "v56", "v57", "v58", "v59", "v60", "v61", "v62", "v63", "v64", "v65", "v66", "v67", "v68", "v69", "v70", "v71", "v72", "v73", "v74", "v75", "v76", "v77", "v78", "v79", "v80", "v81", "v82", "v83", "v84", "v85", "v86", "v87", "v88", "v89", "v90", "v91", "v92", "v93", "v94", "v95", "v96", "v97", "v98", "v99", "v100", "v101", "v102", "v103", "v104", "v105", "v106", "v107", "v108", "v109", "v110", "v111", "v112", "v113", "v114", "v115", "v116", "v117", "v118", "v119", "v120", "v121", "v122", "v123", "v124", "v125", "v126", "v127", "v128", "v129", "v130", "v131", "v132", "v133", "v134", "v135", "v136", "v137", "v138", "v139", "v140", "v141", "v142", "v143", "v144", "v145", "v146", "v147", "v148", "v149", "v150", "v151", "v152", "v153", "v154", "v155", "v156", "v157", "v158", "v159", "v160", "v161", "v162", "v163", "v164", "v165", "v166", "v167", "v168", "v169", "v170", "v171", "v172", "v173", "v174", "v175", "v176", "v177", "v178", "v179", "v180", "v181", "v182", "v183", "v184", "v185", "v186", "v187", "v188", "v189", "v190", "v191", "v192", "v193", "v194", "v195", "v196", "v197", "v198", "v199", "v200", "v201", "v202", "v203", "v204", "v205", "v206", "v207", "v208", "v209", "v210", "v211", "v212", "v213", "v214", "v215", "v216", "v217", "v218", "v219", "v220", "v221", "v222", "v223", "v224", "v225", "v226", "v227", "v228", "v229", "v230", "v231", "v232", "v233", "v234", "v235", "v236", "v237", "v238", "v239", "v240", "v241", "v242", "v243", "v244", "v245", "v246", "v247", "v248", "v249", "v250", "v251", "a0", "a1", "a2", "a3", "a4", "a5", "a6", "a7", "a8", "a9", "a10", "a11", "a12", "a13", "a14", "a15", "a16", "a17", "a18", "a19", "a20", "a21", "a22", "a23", "a24", "a25", "a26", "a27", "a28", "a29", "a30", "a31", "a32", "a33", "a34", "a35", "a36", "a37", "a38", "a39", "a40", "a41", "a42", "a43", "a44", "a45", "a46", "a47", "a48", "a49", "a50", "a51", "a52", "a53", "a54", "a55", "a56", "a57", "a58", "a59", "a60", "a61", "a62", "a63", "a64", "a65", "a66", "a67", "a68", "a69", "a70", "a71", "a72", "a73", "a74", "a75", "a76", "a77", "a78", "a79", "a80", "a81", "a82", "a83", "a84", "a85", "a86", "a87", "a88", "a89", "a90", "a91", "a92", "a93", "a94", "a95", "a96", "a97", "a98", "a99", "a100", "a101", "a102", "a103", "a104", "a105", "a106", "a107", "a108", "a109", "a110", "a111", "a112", "a113", "a114", "a115", "a116", "a117", "a118", "a119", "a120", "a121", "a122", "a123", "a124", "a125", "a126", "a127", "a128", "a129", "a130", "a131", "a132", "a133", "a134", "a135", "a136", "a137", "a138", "a139", "a140", "a141", "a142", "a143", "a144", "a145", "a146", "a147", "a148", "a149", "a150", "a151", "a152", "a153", "a154", "a155", "a156", "a157", "a158", "a159", "a160", "a161", "a162", "a163", "a164", "a165", "a166", "a167", "a168", "a169", "a170", "a171", "a172", "a173", "a174", "a175", "a176", "a177", "a178", "a179", "a180", "a181", "a182", "a183", "a184", "a185", "a186", "a187", "a188", "a189", "a190", "a191", "a192", "a193", "a194", "a195", "a196", "a197", "a198", "a199", "a200", "a201", "a202", "a203", "a204", "a205", "a206", "a207", "s4", "s5", "s6", "s7", "s8", "s9", "s10", "s11", "s12", "s13", "s14", "s15", "s16", "s17", "s18", "s19", "s20", "s21", "s22", "s23", "s24", "s25", "s26", "s27", "s28", "s29", "s30", "s31", "s32", "s33", "s34", "s35", "s38", "s39", "s40", "s43", "s44", "s45", "s46", "s47", "s50", "s52", "s53", "s54", "s55", "s56", "s57", "s58", "s59", "s60", "s61", "s62", "s63", "s64", "s65", "s66", "s67", "s68", "s69", "s70", "s71", "s72", "s74", "s75", "s76", "s77", "s78", "s79", "s80", "s81", "s82", "s83", "s84", "s85", "s86", "s87", "s88", "s89", "s90", "s91", "s92", "s93", "s94", "s95", "s96", "s97", "vcc", "scc", "memory");
}

}

extern "C" void kernel_launch(void* const* d_in, const int* in_sizes, int n_in, void* d_out, int out_size,
                              void* d_ws, size_t ws_size, hipStream_t stream) {
  if (n_in != 6 || ws_size < WS_END) return;
  const __hip_bfloat16* q = (const __hip_bfloat16*)d_in[0];
  const __hip_bfloat16* kc = (const __hip_bfloat16*)d_in[1];
  const __hip_bfloat16* vc = (const __hip_bfloat16*)d_in[2];
  const __hip_bfloat16* kt = (const __hip_bfloat16*)d_in[3];
  const __hip_bfloat16* vt = (const __hip_bfloat16*)d_in[4];
  const __hip_bfloat16* tm = (const __hip_bfloat16*)d_in[5];
  hipLaunchKernelGGL(tree_attn_main, dim3(B_ * HKV_ * 2), dim3(256), 0, stream, q, kc, vc, kt, vt, tm,
                     (__hip_bfloat16*)d_out, (char*)d_ws);
}
```

```cpp
#include <hip/hip_runtime.h>
#include <hip/hip_bf16.h>
#include <stdint.h>

#pragma clang diagnostic ignored "-Wint-to-pointer-cast"
#pragma clang diagnostic ignored "-Winline-asm"

namespace {

constexpr int B_ = 16, HKV_ = 8, D_ = 128;
constexpr int SLOT = 64 * D_ * 2;
constexpr int LDS_BYTES = 10 * SLOT;

constexpr size_t WS_EPOCH = 0;
constexpr size_t WS_REF = WS_EPOCH + 128 * 4 * 128;
constexpr size_t WS_X = WS_REF + 16384;
constexpr size_t XW_BYTES = 4608;
constexpr size_t WS_END = WS_X + (size_t)128 * 2 * 4 * XW_BYTES;

__global__ __launch_bounds__(256, 1)
void tree_attn_main(const __hip_bfloat16*, const __hip_bfloat16*, const __hip_bfloat16*, const __hip_bfloat16*,
                    const __hip_bfloat16*, const __hip_bfloat16*, __hip_bfloat16*, char*) {
  __shared__ __attribute__((aligned(1024))) char smem[LDS_BYTES];
  const unsigned lds0 = (unsigned)(uintptr_t)smem;
  unsigned tid = threadIdx.x;
  asm volatile(
      "s_load_dwordx16 s[4:19], %[kp], 0x0\n"
      "s_getpc_b64 s[60:61]\n"
      "v_lshlrev_b32 v241, 4, v0\n"
      "s_lshl_b32 s62, %[blk], 9\n"
      "s_and_b32 s62, s62, 0x3000\n"
      "s_add_u32 s60, s60, s62\n"
      "s_addc_u32 s61, s61, 0\n"
      "global_load_dwordx4 v[236:239], v241, s[60:61]\n"
      "v_and_b32 v1, 0xc0, v0\n"
      "v_lshlrev_b32 v248, 1, v0\n"
      "v_readfirstlane_b32 s50, v1\n"
      "v_and_b32 v248, 0x60, v248\n"
      "v_and_b32 v249, 0x70, v241\n"
      "v_xor_b32 v248, v248, v249\n"
      "v_lshl_or_b32 v248, v1, 6, v248\n"
      "v_and_b32 v1, 56, v0\n"
      "v_lshl_or_b32 v248, v1, 5, v248\n"
      "s_mov_b32 s64, 0x14100400\n"
      "s_mov_b32 s65, 0x54504440\n"
      "s_lshl_b32 s66, %[blk], 3\n"
      "s_lshr_b64 s[64:65], s[64:65], s66\n"
      "s_mov_b32 s66, 0x3020100\n"
      "s_mov_b32 s67, 0x83828180\n"
      "s_and_b32 s65, %[blk], 56\n"
      "s_lshr_b64 s[66:67], s[66:67], s65\n"
      "s_or_b32 s64, s64, s66\n"
      "s_lshr_b32 s65, %[blk], 3\n"
      "s_and_b32 s65, s65, 24\n"
      "s_lshr_b32 s65, 0x28082000, s65\n"
      "s_or_b32 s64, s64, s65\n"
      "s_and_b32 s63, s64, 0x80\n"
      "s_and_b32 s44, s64, 127\n"
      "v_or_b32 v248, s63, v248\n"
      "v_xor_b32 v249, 0x80, v248\n"
      "s_lshl_b32 s64, s44, 20\n"
      "s_lshl_b32 s62, s44, 15\n"
      "s_lshl_b32 s50, s50, 6\n"
      "s_add_u32 m0, s50, 131072\n"
      "s_mov_b32 s58, 0x120000\n"
      "s_mov_b32 s59, 0x20000\n"
      "s_movk_i32 s22, 0x4000\n"
      "s_waitcnt lgkmcnt(0)\n"
      "s_add_u32 s56, s4, s62\n"
      "s_addc_u32 s57, s5, 0\n"
      "buffer_load_dwordx4 v248, s[56:59], 0 offen nt lds\n"
      "buffer_load_dwordx4 v248, s[56:59], 0 offen offset:2048 nt lds\n"
      "s_bitset1_b32 m0, 10\n"
      "s_memrealtime s[82:83]\n"
      "s_add_u32 s20, s6, s64\n"
      "s_addc_u32 s21, s7, 0\n"
      "s_add_u32 s32, s8, s64\n"
      "s_addc_u32 s33, s9, 0\n"
      "s_load_dword s92, s[20:21], 0x0\n"
      "s_load_dword s92, s[10:11], 0x0\n"
      "s_load_dword s92, s[32:33], 0x0\n"
      "buffer_load_dwordx4 v249, s[56:59], 0 offen nt lds\n"
      "buffer_load_dwordx4 v249, s[56:59], 0 offen offset:2048 nt lds\n"
      "s_add_u32 m0, s50, 147456\n"
      "s_load_dword s92, s[18:19], 0x0\n"
      "buffer_load_dwordx4 v248, s[56:59], s22 offen nt lds\n"
      "buffer_load_dwordx4 v248, s[56:59], s22 offen offset:2048 nt lds\n"
      "s_bitset1_b32 m0, 10\n"
      "s_nop 0\n"
      "buffer_load_dwordx4 v249, s[56:59], s22 offen nt lds\n"
      "buffer_load_dwordx4 v249, s[56:59], s22 offen offset:2048 nt lds\n"
      "s_mov_b64 s[56:57], s[20:21]\n"
      "s_load_dword s92, s[14:15], 0x0\n"
      "s_load_dword s92, s[12:13], 0x0\n"
      "s_lshr_b32 s74, s50, 2\n"
      "s_lshr_b32 s75, s50, 5\n"
      "s_xor_b32 s75, s75, s63\n"
      "s_and_b32 s75, s75, 0x80\n"
      "s_and_b32 s76, s74, 0x800\n"
      "s_add_u32 s75, s75, s76\n"
      "s_add_u32 s70, s63, s50\n"
      "s_sub_u32 s70, s70, s75\n"
      "s_lshr_b32 s75, s50, 1\n"
      "s_lshr_b32 s71, s62, 2\n"
      "s_sub_u32 s71, s71, s75\n"
      "s_ashr_i32 s72, s71, 31\n"
      "s_mov_b32 s23, s59\n"
      "s_mov_b64 s[26:27], s[22:23]\n"
      "s_sub_u32 s20, s56, s70\n"
      "s_subb_u32 s21, s57, 0\n"
      "s_add_u32 m0, s74, 24576\n"
      "s_add_u32 s24, s10, s71\n"
      "buffer_load_dwordx4 v248, s[20:23], 0 offen nt lds\n"
      "s_addc_u32 s25, s11, s72\n"
      "s_add_u32 m0, s75, 16384\n"
      "s_nop 0\n"
      "buffer_load_dwordx4 v248, s[24:27], 0 offen nt lds\n"
      "s_bitset1_b32 m0, 10\n"
      "s_nop 0\n"
      "buffer_load_dwordx4 v249, s[24:27], 0 offen nt lds\n"
      "s_lshr_b32 s43, s63, 7\n"
      "s_lshl_b32 s76, s44, 9\n"
      "s_lshr_b32 s69, s50, 5\n"
      "s_add_u32 s76, s76, s69\n"
      "s_add_u32 s46, s18, s76\n"
      "s_addc_u32 s47, s19, 0\n"
      "s_mul_i32 s76, s43, 0x3000\n"
      "s_lshl_b32 s69, s44, 8\n"
      "s_and_b32 s69, s69, 0x7800\n"
      "s_add_u32 s14, s14, s69\n"
      "s_addc_u32 s15, s15, 0\n"
      "v_mov_b64 v[192:193], 0\n"
      "v_mov_b64 v[194:195], 0\n"
      "v_lshlrev_b32 v3, 6, v0\n"
      "v_and_b32 v3, 0x3c0, v3\n"
      "v_lshrrev_b32 v4, 1, v0\n"
      "v_and_or_b32 v211, v4, 24, v3\n"
      "v_mov_b64 v[200:201], 0\n"
      "v_mov_b64 v[202:203], 0\n"
      "v_mov_b64 v[204:205], 0\n"
      "v_mov_b64 v[206:207], 0\n"
      "s_cmp_lg_u32 s76, s50\n"
      "s_cbranch_scc1 NOMASK_%=\n"
      "global_load_dwordx2 v[200:201], v211, s[14:15] offset:0\n"
      "global_load_dwordx2 v[202:203], v211, s[14:15] offset:32\n"
      "global_load_dwordx2 v[204:205], v211, s[14:15] offset:1024\n"
      "global_load_dwordx2 v[206:207], v211, s[14:15] offset:1056\n"
      "NOMASK_%=:\n"
      "global_load_dword v208, v192, s[46:47] sc1\n"
      "s_mul_i32 s69, s43, 0x8000\n"
      "s_bitcmp1_b32 s44, 0\n"
      "s_cselect_b32 s66, 0x8000, 0\n"
      "s_xor_b32 s69, s69, s66\n"
      "v_and_b32 v228, 0xff, v248\n"
      "v_xor_b32 v228, s69, v228\n"
      "v_bfe_i32 v2, v0, 3, 1\n"
      "v_and_b32 v2, 0x1000, v2\n"
      "v_xor_b32 v228, v228, v2\n"
      "v_bfe_i32 v2, v0, 4, 1\n"
      "v_and_b32 v2, 0x20400, v2\n"
      "v_xor_b32 v228, v228, v2\n"
      "v_bfe_i32 v2, v0, 5, 1\n"
      "v_and_b32 v2, 0x100, v2\n"
      "v_xor_b32 v228, v228, v2\n"
      "v_bfe_i32 v2, v0, 6, 1\n"
      "v_and_b32 v2, 0x400, v2\n"
      "v_xor_b32 v228, v228, v2\n"
      "v_bfe_i32 v2, v0, 7, 1\n"
      "v_and_b32 v2, 0x2000, v2\n"
      "v_xor_b32 v228, v228, v2\n"
      "v_xor_b32 v231, 0x280, v228\n"
      "v_xor_b32 v230, 0x200, v228\n"
      "v_xor_b32 v229, 0x80, v228\n"
      "v_add_u32 v228, 0xc00, v228\n"
      "v_add_u32 v229, 0x800, v229\n"
      "v_add_u32 v230, 0x400, v230\n"
      "s_sub_u32 s56, s56, 0xc00\n"
      "s_subb_u32 s57, s57, 0\n"
      "s_sub_u32 s32, s32, 0xc00\n"
      "s_subb_u32 s33, s33, 0\n"
      "s_xor_b64 s[32:33], s[32:33], s[56:57]\n"
      "s_mov_b32 s69, 0\n"
      "s_bitcmp1_b32 s44, 4\n"
      "s_cselect_b32 s66, 0x10000, 0\n"
      "s_xor_b32 s69, s69, s66\n"
      "s_bitcmp1_b32 s44, 0\n"
      "s_cselect_b32 s66, 0x800, 0\n"
      "s_xor_b32 s69, s69, s66\n"
      "s_bitcmp1_b32 s44, 2\n"
      "s_cselect_b32 s66, 0x40800, 0\n"
      "s_xor_b32 s69, s69, s66\n"
      "s_bitcmp1_b32 s44, 4\n"
      "s_cselect_b32 s66, 0x4000, 0\n"
      "s_xor_b32 s69, s69, s66\n"
      "s_mov_b32 s60, s69\n"
      "s_mov_b32 s62, s50\n"
      "s_mov_b64 s[30:31], s[22:23]\n"
      "s_add_u32 s28, s12, s71\n"
      "s_addc_u32 s29, s13, s72\n"
      "s_lshl_b32 s69, s43, 13\n"
      "s_xor_b32 s69, s69, 0x2000\n"
      "v_add_u32 v2, s69, v241\n"
      "ds_write_b128 v241, v[192:195] offset:28672\n"
      "ds_write_b128 v2, v[192:195] offset:49152\n"
      "ds_write_b128 v2, v[192:195] offset:53248\n"
      "v_and_b32 v241, 0x3f0, v241\n"
      "s_and_b32 s91, s50, 8192\n"
      "v_lshlrev_b32 v4, 8, v0\n"
      "v_and_b32 v4, 0x800, v4\n"
      "v_lshlrev_b32 v5, 7, v0\n"
      "v_and_b32 v6, 0x380, v5\n"
      "v_or3_b32 v4, v4, v6, s91\n"
      "v_and_b32 v6, 0x60, v241\n"
      "v_and_b32 v2, 48, v0\n"
      "v_xor_b32 v6, v6, v2\n"
      "v_or_b32 v242, v4, v6\n"
      "v_xor_b32 v243, 64, v242\n"
      "v_lshlrev_b32 v4, 6, v0\n"
      "v_and_b32 v6, 0x800, v4\n"
      "v_lshlrev_b32 v2, 5, v0\n"
      "v_and_b32 v2, 0x380, v2\n"
      "s_add_u32 s69, s91, 16384\n"
      "v_or3_b32 v6, v6, v2, s69\n"
      "v_lshlrev_b32 v2, 3, v0\n"
      "v_and_or_b32 v6, v2, 24, v6\n"
      "v_lshlrev_b32 v2, 2, v0\n"
      "v_and_b32 v2, 0x60, v2\n"
      "v_xor_b32 v244, v6, v2\n"
      "v_xor_b32 v245, 0x20, v244\n"
      "v_xor_b32 v246, 0x40, v244\n"
      "v_xor_b32 v247, 0x60, v244\n"
      "v_and_b32 v5, 0x1e00, v5\n"
      "v_and_or_b32 v250, v241, 48, v5\n"
      "s_lshl_b32 s69, %[blk], 9\n"
      "s_and_b32 s69, s69, 0x1e00\n"
      "v_and_b32 v4, 0x1c0, v4\n"
      "v_or_b32 v251, s69, v4\n"
      "s_and_b32 s69, s50, 4096\n"
      "s_lshl_b32 s69, s69, 2\n"
      "s_add_u32 s69, s69, 131072\n"
      "s_sub_u32 s91, 16384, s91\n"
      "v_add_u32 v196, s69, v242\n"
      "v_add_u32 v198, s91, v242\n"
      "v_xor_b32 v214, 0x2000, v196\n"
      "v_add_u32 v197, s69, v243\n"
      "v_add_u32 v199, s91, v243\n"
      "v_xor_b32 v215, 0x2000, v197\n"
      "v_and_b32 v3, 1, v0\n"
      "v_bfe_u32 v4, v0, 1, 5\n"
      "v_lshlrev_b32 v3, 1, v3\n"
      "v_sub_u32 v4, v4, v3\n"
      "v_mov_b32 v219, s60\n"
      "v_bfe_i32 v2, v4, 0, 1\n"
      "v_and_b32 v2, 0x10000, v2\n"
      "v_xor_b32 v219, v219, v2\n"
      "v_bfe_i32 v2, v4, 1, 1\n"
      "v_and_b32 v2, 0x800, v2\n"
      "v_xor_b32 v219, v219, v2\n"
      "v_bfe_i32 v2, v4, 2, 1\n"
      "v_and_b32 v2, 0x40800, v2\n"
      "v_xor_b32 v219, v219, v2\n"
      "v_bfe_i32 v2, v4, 3, 1\n"
      "v_and_b32 v2, 0x80000, v2\n"
      "v_xor_b32 v219, v219, v2\n"
      "v_bfe_i32 v2, v4, 4, 1\n"
      "v_and_b32 v2, 0x4000, v2\n"
      "v_xor_b32 v219, v219, v2\n"
      "v_writelane_b32 v219, 0, 1\n"
      "s_mov_b32 s61, 0\n"
      "s_add_u32 s69, s50, 131072\n"
      "v_add_u32 v210, s69, v241\n"
      "v_and_b32 v2, 3, v0\n"
      "v_mov_b32 v4, 0x3f803f80\n"
      "v_cmp_eq_u32 vcc, 0, v2\n"
      "s_nop 1\n"
      "v_cndmask_b32 v5, 0, v4, vcc\n"
      "v_accvgpr_write_b32 a128, v5\n"
      "v_accvgpr_write_b32 a129, v5\n"
      "v_accvgpr_write_b32 a130, v5\n"
      "v_accvgpr_write_b32 a131, v5\n"
      "v_cmp_eq_u32 vcc, 1, v2\n"
      "s_nop 1\n"
      "v_cndmask_b32 v5, 0, v4, vcc\n"
      "v_accvgpr_write_b32 a132, v5\n"
      "v_accvgpr_write_b32 a133, v5\n"
      "v_accvgpr_write_b32 a134, v5\n"
      "v_accvgpr_write_b32 a135, v5\n"
      "v_cmp_eq_u32 vcc, 2, v2\n"
      "s_nop 1\n"
      "v_cndmask_b32 v5, 0, v4, vcc\n"
      "v_accvgpr_write_b32 a136, v5\n"
      "v_accvgpr_write_b32 a137, v5\n"
      "v_accvgpr_write_b32 a138, v5\n"
      "v_accvgpr_write_b32 a139, v5\n"
      "v_cmp_eq_u32 vcc, 3, v2\n"
      "s_nop 1\n"
      "v_cndmask_b32 v5, 0, v4, vcc\n"
      "v_accvgpr_write_b32 a140, v5\n"
      "v_accvgpr_write_b32 a141, v5\n"
      "v_accvgpr_write_b32 a142, v5\n"
      "v_accvgpr_write_b32 a143, v5\n"
      "v_mfma_f32_32x32x16_bf16 a[144:159], v[192:195], v[192:195], 0\n"
      "v_mfma_f32_32x32x16_bf16 a[160:175], v[192:195], v[192:195], 0\n"
      "v_mfma_f32_32x32x16_bf16 a[176:191], v[192:195], v[192:195], 0\n"
      "v_mfma_f32_32x32x16_bf16 a[192:207], v[192:195], v[192:195], 0\n"
      "v_mfma_f32_32x32x16_bf16 v[0:15], v[192:195], v[192:195], 0\n"
      "v_mfma_f32_32x32x16_bf16 v[16:31], v[192:195], v[192:195], 0\n"
      "v_mfma_f32_32x32x16_bf16 v[32:47], v[192:195], v[192:195], 0\n"
      "v_mfma_f32_32x32x16_bf16 v[48:63], v[192:195], v[192:195], 0\n"
      "v_mfma_f32_32x32x16_bf16 v[128:143], v[192:195], v[192:195], 0\n"
      "v_mov_b64 v[180:181], 0\n"
      "v_mov_b64 v[182:183], 0\n"
      "v_mov_b64 v[184:185], 0\n"
      "v_mov_b64 v[186:187], 0\n"
      "s_mov_b32 s94, 0x3e0293ee\n"
      "s_mov_b32 s95, s94\n"
      "s_lshl_b32 s69, s43, 13\n"
      "s_add_u32 s45, s69, s75\n"
      "s_mov_b32 m0, s62\n"
      "s_nop 0\n"
      "buffer_load_dwordx4 v228, s[56:59], s60 offen nt lds\n"
      "buffer_load_dwordx4 v229, s[56:59], s60 offen offset:1024 nt lds\n"
      "buffer_load_dwordx4 v230, s[56:59], s60 offen offset:2048 nt lds\n"
      "buffer_load_dwordx4 v231, s[56:59], s60 offen offset:3072 nt lds\n"
      "s_xor_b64 s[56:57], s[56:57], s[32:33]\n"
      "s_add_u32 s61, s61, 1\n"
      "s_add_u32 s62, s62, 0x4000\n"
      "s_and_b32 s62, s62, 0x1ffff\n"
      "v_readlane_b32 s60, v219, s61\n"
      "s_xor_b64 s[56:57], s[56:57], s[32:33]\n"
      "s_add_u32 s61, s61, 1\n"
      "s_add_u32 s62, s62, 0x4000\n"
      "s_and_b32 s62, s62, 0x1ffff\n"
      "v_readlane_b32 s60, v219, s61\n"
      "s_waitcnt vmcnt(8)\n"
      "s_memrealtime s[68:69]\n"
      "ds_read_b128 v[64:67], v210 offset:0\n"
      "ds_read_b128 v[68:71], v210 offset:1024\n"
      "ds_read_b128 v[72:75], v210 offset:2048\n"
      "ds_read_b128 v[76:79], v210 offset:3072\n"
      "ds_read_b128 v[80:83], v210 offset:16384\n"
      "ds_read_b128 v[84:87], v210 offset:17408\n"
      "ds_read_b128 v[88:91], v210 offset:18432\n"
      "ds_read_b128 v[92:95], v210 offset:19456\n"
      "s_add_u32 s20, s18, 65536\n"
      "s_addc_u32 s21, s19, 0\n"
      "s_lshl_b32 s75, %[blk], 6\n"
      "s_add_u32 s75, s75, s50\n"
      "s_waitcnt lgkmcnt(0)\n"
      "s_sub_u32 s67, s68, s82\n"
      "s_min_u32 s67, s67, 165\n"
      "s_add_u32 s93, s82, s67\n"
      "s_sub_u32 s71, s68, 400\n"
      "s_cmp_lt_u32 s75, 512\n"
      "s_cbranch_scc0 NOLEAD_%=\n"
      "v_mov_b32 v188, s68\n"
      "v_mov_b32 v189, s68\n"
      "v_mov_b32 v190, s68\n"
      "v_mov_b32 v191, s68\n"
      "buffer_store_dwordx4 v[188:191], v250, s[20:23], s75 offen sc1\n"
      "s_nop 1\n"
      "NOLEAD_%=:\n"
      "s_mov_b32 m0, s62\n"
      "s_nop 0\n"
      "buffer_load_dwordx4 v228, s[56:59], s60 offen nt lds\n"
      "buffer_load_dwordx4 v229, s[56:59], s60 offen offset:1024 nt lds\n"
      "buffer_load_dwordx4 v230, s[56:59], s60 offen offset:2048 nt lds\n"
      "buffer_load_dwordx4 v231, s[56:59], s60 offen offset:3072 nt lds\n"
      "s_xor_b64 s[56:57], s[56:57], s[32:33]\n"
      "s_add_u32 s61, s61, 1\n"
      "s_add_u32 s62, s62, 0x4000\n"
      "s_and_b32 s62, s62, 0x1ffff\n"
      "v_readlane_b32 s60, v219, s61\n"
      "s_xor_b64 s[56:57], s[56:57], s[32:33]\n"
      "s_add_u32 s61, s61, 1\n"
      "s_add_u32 s62, s62, 0x4000\n"
      "s_and_b32 s62, s62, 0x1ffff\n"
      "v_readlane_b32 s60, v219, s61\n"
      "s_add_u32 m0, s45, 49152\n"
      "s_nop 0\n"
      "buffer_load_dwordx4 v248, s[28:31], 0 offen nt lds\n"
      "s_bitset1_b32 m0, 10\n"
      "s_nop 0\n"
      "buffer_load_dwordx4 v249, s[28:31], 0 offen nt lds\n"
      "v_lshlrev_b32 v188, 16, v64\n"
      "v_and_b32 v189, 0xffff0000, v64\n"
      "v_lshlrev_b32 v190, 16, v65\n"
      "v_and_b32 v191, 0xffff0000, v65\n"
      "v_lshlrev_b32 v192, 16, v66\n"
      "v_and_b32 v193, 0xffff0000, v66\n"
      "v_lshlrev_b32 v194, 16, v67\n"
      "v_and_b32 v195, 0xffff0000, v67\n"
      "v_pk_mul_f32 v[188:189], v[188:189], s[94:95]\n"
      "v_pk_mul_f32 v[190:191], v[190:191], s[94:95]\n"
      "v_pk_mul_f32 v[192:193], v[192:193], s[94:95]\n"
      "v_pk_mul_f32 v[194:195], v[194:195], s[94:95]\n"
      "v_cvt_pk_bf16_f32 v64, v188, v189\n"
      "v_cvt_pk_bf16_f32 v65, v190, v191\n"
      "v_cvt_pk_bf16_f32 v66, v192, v193\n"
      "v_cvt_pk_bf16_f32 v67, v194, v195\n"
      "ds_write_b128 v210, v[64:67] offset:0\n"
      "v_lshlrev_b32 v188, 16, v68\n"
      "v_and_b32 v189, 0xffff0000, v68\n"
      "v_lshlrev_b32 v190, 16, v69\n"
      "v_and_b32 v191, 0xffff0000, v69\n"
      "v_lshlrev_b32 v192, 16, v70\n"
      "v_and_b32 v193, 0xffff0000, v70\n"
      "v_lshlrev_b32 v194, 16, v71\n"
      "v_and_b32 v195, 0xffff0000, v71\n"
      "v_pk_mul_f32 v[188:189], v[188:189], s[94:95]\n"
      "v_pk_mul_f32 v[190:191], v[190:191], s[94:95]\n"
      "v_pk_mul_f32 v[192:193], v[192:193], s[94:95]\n"
      "v_pk_mul_f32 v[194:195], v[194:195], s[94:95]\n"
      "v_cvt_pk_bf16_f32 v68, v188, v189\n"
      "v_cvt_pk_bf16_f32 v69, v190, v191\n"
      "v_cvt_pk_bf16_f32 v70, v192, v193\n"
      "v_cvt_pk_bf16_f32 v71, v194, v195\n"
      "ds_write_b128 v210, v[68:71] offset:1024\n"
      "v_lshlrev_b32 v188, 16, v72\n"
      "v_and_b32 v189, 0xffff0000, v72\n"
      "v_lshlrev_b32 v190, 16, v73\n"
      "v_and_b32 v191, 0xffff0000, v73\n"
      "v_lshlrev_b32 v192, 16, v74\n"
      "v_and_b32 v193, 0xffff0000, v74\n"
      "v_lshlrev_b32 v194, 16, v75\n"
      "v_and_b32 v195, 0xffff0000, v75\n"
      "v_pk_mul_f32 v[188:189], v[188:189], s[94:95]\n"
      "v_pk_mul_f32 v[190:191], v[190:191], s[94:95]\n"
      "v_pk_mul_f32 v[192:193], v[192:193], s[94:95]\n"
      "v_pk_mul_f32 v[194:195], v[194:195], s[94:95]\n"
      "v_cvt_pk_bf16_f32 v72, v188, v189\n"
      "v_cvt_pk_bf16_f32 v73, v190, v191\n"
      "v_cvt_pk_bf16_f32 v74, v192, v193\n"
      "v_cvt_pk_bf16_f32 v75, v194, v195\n"
      "ds_write_b128 v210, v[72:75] offset:2048\n"
      "v_lshlrev_b32 v188, 16, v76\n"
      "v_and_b32 v189, 0xffff0000, v76\n"
      "v_lshlrev_b32 v190, 16, v77\n"
      "v_and_b32 v191, 0xffff0000, v77\n"
      "v_lshlrev_b32 v192, 16, v78\n"
      "v_and_b32 v193, 0xffff0000, v78\n"
      "v_lshlrev_b32 v194, 16, v79\n"
      "v_and_b32 v195, 0xffff0000, v79\n"
      "v_pk_mul_f32 v[188:189], v[188:189], s[94:95]\n"
      "v_pk_mul_f32 v[190:191], v[190:191], s[94:95]\n"
      "v_pk_mul_f32 v[192:193], v[192:193], s[94:95]\n"
      "v_pk_mul_f32 v[194:195], v[194:195], s[94:95]\n"
      "v_cvt_pk_bf16_f32 v76, v188, v189\n"
      "v_cvt_pk_bf16_f32 v77, v190, v191\n"
      "v_cvt_pk_bf16_f32 v78, v192, v193\n"
      "v_cvt_pk_bf16_f32 v79, v194, v195\n"
      "ds_write_b128 v210, v[76:79] offset:3072\n"
      "v_lshlrev_b32 v188, 16, v80\n"
      "v_and_b32 v189, 0xffff0000, v80\n"
      "v_lshlrev_b32 v190, 16, v81\n"
      "v_and_b32 v191, 0xffff0000, v81\n"
      "v_lshlrev_b32 v192, 16, v82\n"
      "v_and_b32 v193, 0xffff0000, v82\n"
      "v_lshlrev_b32 v194, 16, v83\n"
      "v_and_b32 v195, 0xffff0000, v83\n"
      "v_pk_mul_f32 v[188:189], v[188:189], s[94:95]\n"
      "v_pk_mul_f32 v[190:191], v[190:191], s[94:95]\n"
      "v_pk_mul_f32 v[192:193], v[192:193], s[94:95]\n"
      "v_pk_mul_f32 v[194:195], v[194:195], s[94:95]\n"
      "v_cvt_pk_bf16_f32 v80, v188, v189\n"
      "v_cvt_pk_bf16_f32 v81, v190, v191\n"
      "v_cvt_pk_bf16_f32 v82, v192, v193\n"
      "v_cvt_pk_bf16_f32 v83, v194, v195\n"
      "ds_write_b128 v210, v[80:83] offset:16384\n"
      "v_lshlrev_b32 v188, 16, v84\n"
      "v_and_b32 v189, 0xffff0000, v84\n"
      "v_lshlrev_b32 v190, 16, v85\n"
      "v_and_b32 v191, 0xffff0000, v85\n"
      "v_lshlrev_b32 v192, 16, v86\n"
      "v_and_b32 v193, 0xffff0000, v86\n"
      "v_lshlrev_b32 v194, 16, v87\n"
      "v_and_b32 v195, 0xffff0000, v87\n"
      "v_pk_mul_f32 v[188:189], v[188:189], s[94:95]\n"
      "v_pk_mul_f32 v[190:191], v[190:191], s[94:95]\n"
      "v_pk_mul_f32 v[192:193], v[192:193], s[94:95]\n"
      "v_pk_mul_f32 v[194:195], v[194:195], s[94:95]\n"
      "v_cvt_pk_bf16_f32 v84, v188, v189\n"
      "v_cvt_pk_bf16_f32 v85, v190, v191\n"
      "v_cvt_pk_bf16_f32 v86, v192, v193\n"
      "v_cvt_pk_bf16_f32 v87, v194, v195\n"
      "ds_write_b128 v210, v[84:87] offset:17408\n"
      "v_lshlrev_b32 v188, 16, v88\n"
      "v_and_b32 v189, 0xffff0000, v88\n"
      "v_lshlrev_b32 v190, 16, v89\n"
      "v_and_b32 v191, 0xffff0000, v89\n"
      "v_lshlrev_b32 v192, 16, v90\n"
      "v_and_b32 v193, 0xffff0000, v90\n"
      "v_lshlrev_b32 v194, 16, v91\n"
      "v_and_b32 v195, 0xffff0000, v91\n"
      "v_pk_mul_f32 v[188:189], v[188:189], s[94:95]\n"
      "v_pk_mul_f32 v[190:191], v[190:191], s[94:95]\n"
      "v_pk_mul_f32 v[192:193], v[192:193], s[94:95]\n"
      "v_pk_mul_f32 v[194:195], v[194:195], s[94:95]\n"
      "v_cvt_pk_bf16_f32 v88, v188, v189\n"
      "v_cvt_pk_bf16_f32 v89, v190, v191\n"
      "v_cvt_pk_bf16_f32 v90, v192, v193\n"
      "v_cvt_pk_bf16_f32 v91, v194, v195\n"
      "ds_write_b128 v210, v[88:91] offset:18432\n"
      "v_lshlrev_b32 v188, 16, v92\n"
      "v_and_b32 v189, 0xffff0000, v92\n"
      "v_lshlrev_b32 v190, 16, v93\n"
      "v_and_b32 v191, 0xffff0000, v93\n"
      "v_lshlrev_b32 v192, 16, v94\n"
      "v_and_b32 v193, 0xffff0000, v94\n"
      "v_lshlrev_b32 v194, 16, v95\n"
      "v_and_b32 v195, 0xffff0000, v95\n"
      "v_pk_mul_f32 v[188:189], v[188:189], s[94:95]\n"
      "v_pk_mul_f32 v[190:191], v[190:191], s[94:95]\n"
      "v_pk_mul_f32 v[192:193], v[192:193], s[94:95]\n"
      "v_pk_mul_f32 v[194:195], v[194:195], s[94:95]\n"
      "v_cvt_pk_bf16_f32 v92, v188, v189\n"
      "v_cvt_pk_bf16_f32 v93, v190, v191\n"
      "v_cvt_pk_bf16_f32 v94, v192, v193\n"
      "v_cvt_pk_bf16_f32 v95, v194, v195\n"
      "ds_write_b128 v210, v[92:95] offset:19456\n"
      "s_mov_b32 m0, s62\n"
      "s_nop 0\n"
      "buffer_load_dwordx4 v228, s[56:59], s60 offen nt lds\n"
      "buffer_load_dwordx4 v229, s[56:59], s60 offen offset:1024 nt lds\n"
      "buffer_load_dwordx4 v230, s[56:59], s60 offen offset:2048 nt lds\n"
      "buffer_load_dwordx4 v231, s[56:59], s60 offen offset:3072 nt lds\n"
      "s_xor_b64 s[56:57], s[56:57], s[32:33]\n"
      "s_add_u32 s61, s61, 1\n"
      "s_add_u32 s62, s62, 0x4000\n"
      "s_and_b32 s62, s62, 0x1ffff\n"
      "v_readlane_b32 s60, v219, s61\n"
      "s_cmp_eq_u32 s50, 0\n"
      "s_cselect_b32 s38, 0x41014001, 1\n"
      "s_cselect_b32 s39, 0x3aac0083, 3\n"
      "s_cselect_b32 s52, 0x28898a90, 0\n"
      "s_cselect_b32 s53, 0x80011, 0\n"
      "s_mov_b64 s[34:35], 0\n"
      "s_and_b32 s69, %[blk], 7\n"
      "s_mul_i32 s69, s69, 5\n"
      "s_lshr_b32 s74, %[blk], 4\n"
      "s_add_u32 s69, s69, s74\n"
      "s_and_b32 s69, s69, 15\n"
      "s_mul_i32 s74, s69, 16\n"
      "s_lshr_b32 s74, s74, 6\n"
      "s_lshl2_add_u32 s76, s74, 9\n"
      "s_bitset1_b64 s[34:35], s76\n"
      "s_lshl_b32 s74, s74, 1\n"
      "s_sub_u32 s74, 28, s74\n"
      "s_bitset1_b32 s39, s74\n"
      "s_mul_i32 s74, s69, 20\n"
      "s_lshr_b32 s74, s74, 6\n"
      "s_lshl2_add_u32 s76, s74, 25\n"
      "s_bitset1_b64 s[34:35], s76\n"
      "s_lshl_b32 s74, s74, 1\n"
      "s_sub_u32 s74, 20, s74\n"
      "s_bitset1_b32 s39, s74\n"
      "s_mul_i32 s74, s69, 20\n"
      "s_lshr_b32 s74, s74, 6\n"
      "s_lshl2_add_u32 s76, s74, 45\n"
      "s_bitset1_b64 s[34:35], s76\n"
      "s_lshl_b32 s74, s74, 1\n"
      "s_sub_u32 s74, 10, s74\n"
      "s_bitset1_b32 s39, s74\n"
      "s_mov_b32 s54, 3\n"
      "s_mov_b32 s70, 250\n"
      "s_mov_b32 s86, 59\n"
      "s_mov_b32 s77, 177\n"
      "s_lshl_b32 s91, s50, 16\n"
      "s_sub_u32 s80, 46, s91\n"
      "s_sub_u32 s90, 34, s91\n"
      "s_sub_u32 s72, s75, 14336\n"
      "s_and_b32 s79, %[blk], 31\n"
      "s_lshl_b32 s79, s79, 8\n"
      "s_add_u32 s79, s79, 8192\n"
      "s_mul_i32 s69, %[blk], 53\n"
      "s_lshr_b32 s69, s69, 8\n"
      "s_add_u32 s66, s93, 150\n"
      "s_sub_u32 s81, s93, s71\n"
      "s_sub_u32 s81, s81, s69\n"
      "s_add_u32 s81, s81, 24\n"
      "s_sub_u32 s66, s66, s91\n"
      "s_mov_b32 s88, s66\n"
      "s_mov_b32 s65, s66\n"
      "s_getpc_b64 s[84:85]\n"
      "PC_%=:\n"
      "s_add_u32 s84, s84, HOOK_%=-PC_%=\n"
      "s_addc_u32 s85, s85, 0\n"
      "s_waitcnt vmcnt(14) lgkmcnt(0)\n"
      "s_barrier\n"
      "ds_read_b128 a[64:67], v198 offset:8192\n"
      "ds_read_b128 a[72:75], v199 offset:8192\n"
      "ds_read_b128 a[80:83], v198 offset:9216\n"
      "ds_read_b128 a[88:91], v199 offset:9216\n"
      "ds_read_b128 a[0:3], v196 offset:0\n"
      "ds_read_b128 a[16:19], v197 offset:0\n"
      "ds_read_b128 a[32:35], v196 offset:1024\n"
      "ds_read_b128 a[48:51], v197 offset:1024\n"
      "ds_read_b128 a[4:7], v196 offset:4096\n"
      "ds_read_b128 a[20:23], v197 offset:4096\n"
      "ds_read_b128 a[36:39], v196 offset:5120\n"
      "ds_read_b128 a[52:55], v197 offset:5120\n"
      "v_readfirstlane_b32 s40, v208\n"
      "s_mov_b32 s94, 0xceabfb8f\n"
      "v_lshlrev_b32 v144, 16, v200\n"
      "v_and_b32 v145, 0xffff0000, v200\n"
      "v_lshlrev_b32 v146, 16, v201\n"
      "v_and_b32 v147, 0xffff0000, v201\n"
      "v_lshlrev_b32 v148, 16, v202\n"
      "v_and_b32 v149, 0xffff0000, v202\n"
      "v_lshlrev_b32 v150, 16, v203\n"
      "v_and_b32 v151, 0xffff0000, v203\n"
      "v_lshlrev_b32 v152, 16, v204\n"
      "v_and_b32 v153, 0xffff0000, v204\n"
      "v_lshlrev_b32 v154, 16, v205\n"
      "v_and_b32 v155, 0xffff0000, v205\n"
      "v_lshlrev_b32 v156, 16, v206\n"
      "v_and_b32 v157, 0xffff0000, v206\n"
      "v_lshlrev_b32 v158, 16, v207\n"
      "v_and_b32 v159, 0xffff0000, v207\n"
      "s_waitcnt lgkmcnt(4)\n"
      "v_mfma_f32_16x16x32_bf16 v[64:67], a[64:67], a[0:3], 0\n"
      "ds_read_b128 a[8:11], v214 offset:0\n"
      "v_fma_f32 v144, -v144, s94, s94\n"
      "v_mfma_f32_16x16x32_bf16 v[64:67], a[72:75], a[16:19], v[64:67]\n"
      "ds_read_b128 a[24:27], v215 offset:0\n"
      "v_fma_f32 v145, -v145, s94, s94\n"
      "v_mfma_f32_16x16x32_bf16 v[64:67], a[80:83], a[32:35], v[64:67]\n"
      "ds_read_b128 a[40:43], v214 offset:1024\n"
      "v_fma_f32 v146, -v146, s94, s94\n"
      "v_mfma_f32_16x16x32_bf16 v[64:67], a[88:91], a[48:51], v[64:67]\n"
      "ds_read_b128 a[56:59], v215 offset:1024\n"
      "v_fma_f32 v147, -v147, s94, s94\n"
      "s_waitcnt lgkmcnt(4)\n"
      "v_mfma_f32_16x16x32_bf16 v[72:75], a[64:67], a[4:7], 0\n"
      "ds_read_b128 a[12:15], v214 offset:4096\n"
      "v_fma_f32 v148, -v148, s94, s94\n"
      "v_mfma_f32_16x16x32_bf16 v[72:75], a[72:75], a[20:23], v[72:75]\n"
      "ds_read_b128 a[28:31], v215 offset:4096\n"
      "v_fma_f32 v149, -v149, s94, s94\n"
      "v_mfma_f32_16x16x32_bf16 v[72:75], a[80:83], a[36:39], v[72:75]\n"
      "ds_read_b128 a[44:47], v214 offset:5120\n"
      "v_fma_f32 v150, -v150, s94, s94\n"
      "v_mfma_f32_16x16x32_bf16 v[72:75], a[88:91], a[52:55], v[72:75]\n"
      "ds_read_b128 a[60:63], v215 offset:5120\n"
      "v_fma_f32 v151, -v151, s94, s94\n"
      "s_waitcnt lgkmcnt(4)\n"
      "v_mfma_f32_16x16x32_bf16 v[80:83], a[64:67], a[8:11], 0\n"
      "ds_read_b128 a[96:99], v198 offset:0\n"
      "v_fma_f32 v152, -v152, s94, s94\n"
      "v_mfma_f32_16x16x32_bf16 v[80:83], a[72:75], a[24:27], v[80:83]\n"
      "ds_read_b128 a[100:103], v198 offset:4096\n"
      "v_fma_f32 v153, -v153, s94, s94\n"
      "v_mfma_f32_16x16x32_bf16 v[80:83], a[80:83], a[40:43], v[80:83]\n"
      "ds_read_b128 a[104:107], v199 offset:0\n"
      "v_fma_f32 v154, -v154, s94, s94\n"
      "v_mfma_f32_16x16x32_bf16 v[80:83], a[88:91], a[56:59], v[80:83]\n"
      "ds_read_b128 a[108:111], v199 offset:4096\n"
      "v_fma_f32 v155, -v155, s94, s94\n"
      "s_waitcnt lgkmcnt(4)\n"
      "v_mfma_f32_16x16x32_bf16 v[88:91], a[64:67], a[12:15], 0\n"
      "ds_read_b128 a[112:115], v198 offset:1024\n"
      "v_fma_f32 v156, -v156, s94, s94\n"
      "v_mfma_f32_16x16x32_bf16 v[88:91], a[72:75], a[28:31], v[88:91]\n"
      "ds_read_b128 a[116:119], v198 offset:5120\n"
      "v_fma_f32 v157, -v157, s94, s94\n"
      "v_mfma_f32_16x16x32_bf16 v[88:91], a[80:83], a[44:47], v[88:91]\n"
      "ds_read_b128 a[120:123], v199 offset:1024\n"
      "v_fma_f32 v158, -v158, s94, s94\n"
      "v_mfma_f32_16x16x32_bf16 v[88:91], a[88:91], a[60:63], v[88:91]\n"
      "ds_read_b128 a[124:127], v199 offset:5120\n"
      "v_fma_f32 v159, -v159, s94, s94\n"
      "s_mov_b32 m0, s62\n"
      "s_nop 0\n"
      "buffer_load_dwordx4 v228, s[56:59], s60 offen nt lds\n"
      "buffer_load_dwordx4 v229, s[56:59], s60 offen offset:1024 nt lds\n"
      "buffer_load_dwordx4 v230, s[56:59], s60 offen offset:2048 nt lds\n"
      "buffer_load_dwordx4 v231, s[56:59], s60 offen offset:3072 nt lds\n"
      "s_waitcnt lgkmcnt(0)\n"
      "v_mfma_f32_16x16x32_bf16 v[96:99], a[96:99], a[0:3], v[144:147]\n"
      "s_add_u32 s40, s40, 0x7f4a7c15\n"
      "v_mfma_f32_16x16x32_bf16 v[96:99], a[104:107], a[16:19], v[96:99]\n"
      "s_lshl_b32 s69, s44, 3\n"
      "v_max3_f32 v188, v64, v65, v66\n"
      "v_max3_f32 v189, v72, v73, v74\n"
      "v_mfma_f32_16x16x32_bf16 v[96:99], a[112:115], a[32:35], v[96:99]\n"
      "s_lshr_b32 s74, s50, 12\n"
      "v_max3_f32 v190, v80, v81, v82\n"
      "v_max3_f32 v191, v88, v89, v90\n"
      "v_mfma_f32_16x16x32_bf16 v[96:99], a[120:123], a[48:51], v[96:99]\n"
      "s_add_u32 s69, s69, s74\n"
      "v_max_f32 v188, v188, v67\n"
      "v_max_f32 v189, v189, v75\n"
      "v_mfma_f32_16x16x32_bf16 v[104:107], a[96:99], a[4:7], v[152:155]\n"
      "s_mul_i32 s69, s69, 4608\n"
      "v_max_f32 v190, v190, v83\n"
      "v_max_f32 v191, v191, v91\n"
      "v_mfma_f32_16x16x32_bf16 v[104:107], a[104:107], a[20:23], v[104:107]\n"
      "s_add_u32 s69, s69, 81920\n"
      "v_mov_b32 v192, v188\n"
      "v_mov_b32 v193, v189\n"
      "v_mfma_f32_16x16x32_bf16 v[104:107], a[112:115], a[36:39], v[104:107]\n"
      "s_add_u32 s24, s18, s69\n"
      "v_mov_b32 v194, v190\n"
      "v_mov_b32 v195, v191\n"
      "v_mfma_f32_16x16x32_bf16 v[104:107], a[120:123], a[52:55], v[104:107]\n"
      "s_addc_u32 s25, s19, 0\n"
      "v_permlane16_swap_b32 v188, v192\n"
      "v_permlane16_swap_b32 v189, v193\n"
      "v_mfma_f32_16x16x32_bf16 v[112:115], a[96:99], a[8:11], v[144:147]\n"
      "s_movk_i32 s26, 23040\n"
      "v_permlane16_swap_b32 v190, v194\n"
      "v_permlane16_swap_b32 v191, v195\n"
      "v_mfma_f32_16x16x32_bf16 v[112:115], a[104:107], a[24:27], v[112:115]\n"
      "s_mul_i32 s78, s43, 18432\n"
      "v_max_f32 v188, v188, v192\n"
      "v_max_f32 v189, v189, v193\n"
      "v_mfma_f32_16x16x32_bf16 v[112:115], a[112:115], a[40:43], v[112:115]\n"
      "s_and_b32 s69, s50, 4096\n"
      "v_max_f32 v190, v190, v194\n"
      "v_max_f32 v191, v191, v195\n"
      "v_mfma_f32_16x16x32_bf16 v[112:115], a[120:123], a[56:59], v[112:115]\n"
      "s_lshl_b32 s69, s69, 2\n"
      "v_mov_b32 v192, v188\n"
      "v_mov_b32 v193, v189\n"
      "v_mfma_f32_16x16x32_bf16 v[120:123], a[96:99], a[12:15], v[152:155]\n"
      "s_and_b32 s74, s50, 8192\n"
      "v_mov_b32 v194, v190\n"
      "v_mov_b32 v195, v191\n"
      "v_mfma_f32_16x16x32_bf16 v[120:123], a[104:107], a[28:31], v[120:123]\n"
      "s_add_u32 s74, s74, 4096\n"
      "v_permlane32_swap_b32 v188, v192\n"
      "v_permlane32_swap_b32 v189, v193\n"
      "v_mfma_f32_16x16x32_bf16 v[120:123], a[112:115], a[44:47], v[120:123]\n"
      "s_add_u32 s69, s69, s74\n"
      "v_permlane32_swap_b32 v190, v194\n"
      "v_permlane32_swap_b32 v191, v195\n"
      "v_mfma_f32_16x16x32_bf16 v[120:123], a[120:123], a[60:63], v[120:123]\n"
      "s_lshl_b32 s74, s43, 7\n"
      "v_max_f32 v188, v188, v192\n"
      "v_max_f32 v189, v189, v193\n"
      "v_mfma_f32_16x16x32_bf16 v[100:103], a[100:103], a[0:3], v[148:151]\n"
      "s_add_u32 s69, s69, s74\n"
      "v_max_f32 v190, v190, v194\n"
      "v_max_f32 v191, v191, v195\n"
      "v_mfma_f32_16x16x32_bf16 v[100:103], a[108:111], a[16:19], v[100:103]\n"
      "s_lshl_b32 s74, s44, 15\n"
      "v_xor_b32 v176, 0x80000000, v188\n"
      "v_xor_b32 v177, 0x80000000, v189\n"
      "v_mfma_f32_16x16x32_bf16 v[100:103], a[116:119], a[32:35], v[100:103]\n"
      "s_add_u32 s69, s69, s74\n"
      "v_xor_b32 v178, 0x80000000, v190\n"
      "v_xor_b32 v179, 0x80000000, v191\n"
      "v_mfma_f32_16x16x32_bf16 v[100:103], a[124:127], a[48:51], v[100:103]\n"
      "s_add_u32 s30, s16, s69\n"
      "v_mov_b32 v160, v176\n"
      "v_mov_b32 v161, v176\n"
      "v_mfma_f32_16x16x32_bf16 v[108:111], a[100:103], a[4:7], v[156:159]\n"
      "s_addc_u32 s31, s17, 0\n"
      "v_mov_b32 v162, v176\n"
      "v_mov_b32 v163, v176\n"
      "v_mfma_f32_16x16x32_bf16 v[108:111], a[108:111], a[20:23], v[108:111]\n"
      "v_mov_b32 v164, v177\n"
      "v_mov_b32 v165, v177\n"
      "v_mfma_f32_16x16x32_bf16 v[108:111], a[116:119], a[36:39], v[108:111]\n"
      "v_mov_b32 v166, v177\n"
      "v_mov_b32 v167, v177\n"
      "v_mfma_f32_16x16x32_bf16 v[108:111], a[124:127], a[52:55], v[108:111]\n"
      "v_mov_b32 v168, v178\n"
      "v_mov_b32 v169, v178\n"
      "v_mfma_f32_16x16x32_bf16 v[116:119], a[100:103], a[8:11], v[148:151]\n"
      "v_mov_b32 v170, v178\n"
      "v_mov_b32 v171, v178\n"
      "v_mfma_f32_16x16x32_bf16 v[116:119], a[108:111], a[24:27], v[116:119]\n"
      "v_mov_b32 v172, v179\n"
      "v_mov_b32 v173, v179\n"
      "v_mfma_f32_16x16x32_bf16 v[116:119], a[116:119], a[40:43], v[116:119]\n"
      "v_mov_b32 v174, v179\n"
      "v_mov_b32 v175, v179\n"
      "v_mfma_f32_16x16x32_bf16 v[116:119], a[124:127], a[56:59], v[116:119]\n"
      "v_mov_b32 v209, 139248\n"
      "v_lshl_add_u32 v232, s50, 1, v241\n"
      "v_mfma_f32_16x16x32_bf16 v[124:127], a[100:103], a[12:15], v[156:159]\n"
      "v_add_u32 v232, 0x10000, v232\n"
      "v_xor_b32 v233, 0x4000, v232\n"
      "v_mfma_f32_16x16x32_bf16 v[124:127], a[108:111], a[28:31], v[124:127]\n"
      "v_add_u32 v234, 0x10000, v232\n"
      "v_xor_b32 v235, 0x4000, v234\n"
      "v_mfma_f32_16x16x32_bf16 v[124:127], a[116:119], a[44:47], v[124:127]\n"
      "v_lshrrev_b32 v240, 1, v241\n"
      "v_add_u32 v240, 0x1000, v240\n"
      "v_mfma_f32_16x16x32_bf16 v[124:127], a[124:127], a[60:63], v[124:127]\n"
      "v_mov_b32 v196, 0\n"
      "v_pk_add_f32 v[96:97], v[96:97], v[160:161]\n"
      "v_pk_add_f32 v[98:99], v[98:99], v[160:161]\n"
      "v_pk_add_f32 v[104:105], v[104:105], v[164:165]\n"
      "v_pk_add_f32 v[106:107], v[106:107], v[164:165]\n"
      "v_pk_add_f32 v[112:113], v[112:113], v[168:169]\n"
      "v_pk_add_f32 v[114:115], v[114:115], v[168:169]\n"
      "v_pk_add_f32 v[120:121], v[120:121], v[172:173]\n"
      "v_pk_add_f32 v[122:123], v[122:123], v[172:173]\n"
      "v_pk_add_f32 v[100:101], v[100:101], v[160:161]\n"
      "v_pk_add_f32 v[102:103], v[102:103], v[160:161]\n"
      "v_pk_add_f32 v[108:109], v[108:109], v[164:165]\n"
      "v_pk_add_f32 v[110:111], v[110:111], v[164:165]\n"
      "v_pk_add_f32 v[116:117], v[116:117], v[168:169]\n"
      "v_pk_add_f32 v[118:119], v[118:119], v[168:169]\n"
      "v_pk_add_f32 v[124:125], v[124:125], v[172:173]\n"
      "v_pk_add_f32 v[126:127], v[126:127], v[172:173]\n"
      "v_mov_b32 v224, v244\n"
      "v_mov_b32 v225, v245\n"
      "v_mov_b32 v226, v246\n"
      "v_mov_b32 v227, v247\n"
      "v_xor_b32 v220, 0x2000, v244\n"
      "v_xor_b32 v221, 0x2000, v245\n"
      "v_xor_b32 v222, 0x2000, v246\n"
      "v_xor_b32 v223, 0x2000, v247\n"
      "s_lshl_b32 s76, s43, 3\n"
      "v_mov_b32 v237, s40\n"
      "v_mov_b32 v238, s76\n"
      "v_xor_b32 v239, 8, v238\n"
      "s_memrealtime s[68:69]\n"
      "s_waitcnt vmcnt(14) lgkmcnt(0)\n"
      "s_sub_u32 s67, s68, s88\n"
      "s_cmp_lt_i32 s67, -8\n"
      "s_cbranch_scc1 GW0_%=\n"
      "GX0_%=:\n"
      "s_barrier\n"
      "ds_read_b128 a[64:67], v242 offset:0\n"
      "ds_read_b128 a[68:71], v242 offset:4096\n"
      "ds_read_b128 a[72:75], v243 offset:0\n"
      "ds_read_b128 a[76:79], v243 offset:4096\n"
      "ds_read_b128 a[80:83], v242 offset:1024\n"
      "ds_read_b128 a[84:87], v242 offset:5120\n"
      "ds_read_b128 a[88:91], v243 offset:1024\n"
      "ds_read_b128 a[92:95], v243 offset:5120\n"
      "ds_write_b32 v209, v196\n"
      "s_add_u32 s38, s38, s38\n"
      "s_memrealtime s[68:69]\n"
      "s_xor_b64 s[56:57], s[56:57], s[32:33]\n"
      "s_add_u32 s61, s61, 1\n"
      "s_add_u32 s62, s62, 0x4000\n"
      "s_and_b32 s62, s62, 0x1ffff\n"
      "v_readlane_b32 s60, v219, s61\n"
      "s_waitcnt lgkmcnt(0)\n"
      "s_sub_u32 s67, s68, s66\n"
      "s_cmp_gt_i32 s67, s70\n"
      "s_cselect_b32 s69, s77, s86\n"
      "s_cselect_b32 s70, 60, s70\n"
      "s_cselect_b32 s90, s80, s90\n"
      "s_add_u32 s66, s66, s69\n"
      "s_mov_b32 s82, s68\n"
      "LOOP_%=:\n"
      "v_mfma_f32_16x16x32_bf16 v[64:67], a[64:67], a[0:3], v[160:163]\n"
      "s_add_u32 s65, s68, s90\n"
      "v_exp_f32 v188, v96\n"
      "v_mfma_f32_16x16x32_bf16 v[64:67], a[72:75], a[16:19], v[64:67]\n"
      "s_max_u32 s88, s66, s65\n"
      "v_add_f32 v180, v180, v184\n"
      "v_add_f32 v181, v181, v185\n"
      "v_mfma_f32_16x16x32_bf16 v[64:67], a[80:83], a[32:35], v[64:67]\n"
      "v_exp_f32 v189, v97\n"
      "v_mfma_f32_16x16x32_bf16 v[64:67], a[88:91], a[48:51], v[64:67]\n"
      "s_getreg_b32 s67, hwreg(HW_REG_IB_STS, 0, 4)\n"
      "v_add_f32 v182, v182, v186\n"
      "v_add_f32 v183, v183, v187\n"
      "v_mfma_f32_16x16x32_bf16 v[72:75], a[64:67], a[4:7], v[164:167]\n"
      "s_cmp_gt_u32 s67, 4\n"
      "v_exp_f32 v190, v98\n"
      "v_mfma_f32_16x16x32_bf16 v[72:75], a[72:75], a[20:23], v[72:75]\n"
      "s_addc_u32 s89, s89, 0\n"
      "v_exp_f32 v191, v99\n"
      "v_mfma_f32_16x16x32_bf16 v[72:75], a[80:83], a[36:39], v[72:75]\n"
      "v_cvt_pk_bf16_f32 v144, v188, v189\n"
      "v_cvt_pk_bf16_f32 v145, v190, v191\n"
      "v_mfma_f32_16x16x32_bf16 v[72:75], a[88:91], a[52:55], v[72:75]\n"
      "v_exp_f32 v192, v100\n"
      "v_mfma_f32_16x16x32_bf16 v[80:83], a[64:67], a[8:11], v[168:171]\n"
      "s_add_u32 s39, s39, s39\n"
      "v_exp_f32 v193, v101\n"
      "v_mfma_f32_16x16x32_bf16 v[80:83], a[72:75], a[24:27], v[80:83]\n"
      "s_memrealtime s[68:69]\n"
      "v_exp_f32 v194, v102\n"
      "v_mfma_f32_16x16x32_bf16 v[80:83], a[80:83], a[40:43], v[80:83]\n"
      "v_exp_f32 v195, v103\n"
      "v_mfma_f32_16x16x32_bf16 v[80:83], a[88:91], a[56:59], v[80:83]\n"
      "s_mov_b32 m0, s62\n"
      "v_cvt_pk_bf16_f32 v146, v192, v193\n"
      "v_cvt_pk_bf16_f32 v147, v194, v195\n"
      "v_mfma_f32_16x16x32_bf16 v[88:91], a[64:67], a[12:15], v[172:175]\n"
      "buffer_load_dwordx4 v228, s[56:59], s60 offen nt lds\n"
      "v_mfma_f32_16x16x32_bf16 v[184:187], a[128:131], v[144:147], 0\n"
      "buffer_load_dwordx4 v229, s[56:59], s60 offen offset:1024 nt lds\n"
      "v_mfma_f32_16x16x32_bf16 v[88:91], a[72:75], a[28:31], v[88:91]\n"
      "buffer_load_dwordx4 v230, s[56:59], s60 offen offset:2048 nt lds\n"
      "v_mfma_f32_16x16x32_bf16 v[88:91], a[80:83], a[44:47], v[88:91]\n"
      "buffer_load_dwordx4 v231, s[56:59], s60 offen offset:3072 nt lds\n"
      "s_cbranch_scc1 SP1_%=\n"
      "HD1_%=:\n"
      "s_cmp_lg_u32 s54, 0\n"
      "s_waitcnt vmcnt(12) lgkmcnt(0)\n"
      "s_cbranch_scc1 W1_%=\n"
      "s_waitcnt vmcnt(8)\n"
      "W1_%=:\n"
      "s_sub_u32 s67, s68, s88\n"
      "s_cmp_lt_i32 s67, -8\n"
      "s_cbranch_scc1 GW4_%=\n"
      "GX4_%=:\n"
      "s_barrier\n"
      "v_mfma_f32_16x16x32_bf16 v[88:91], a[88:91], a[60:63], v[88:91]\n"
      "s_memrealtime s[68:69]\n"
      "ds_read_b64_tr_b16 a[96:97], v244 offset:0\n"
      "ds_read_b64_tr_b16 a[98:99], v244 offset:4096\n"
      "v_mfma_f32_16x16x32_bf16 v[68:71], a[68:71], a[0:3], v[160:163]\n"
      "s_xor_b64 s[56:57], s[56:57], s[32:33]\n"
      "ds_read_b64_tr_b16 a[100:101], v245 offset:0\n"
      "ds_read_b64_tr_b16 a[102:103], v245 offset:4096\n"
      "v_mfma_f32_16x16x32_bf16 v[68:71], a[76:79], a[16:19], v[68:71]\n"
      "s_add_u32 s61, s61, 1\n"
      "ds_read_b64_tr_b16 a[104:105], v246 offset:0\n"
      "ds_read_b64_tr_b16 a[106:107], v246 offset:4096\n"
      "v_mfma_f32_16x16x32_bf16 v[68:71], a[84:87], a[32:35], v[68:71]\n"
      "s_add_u32 s62, s62, 0x4000\n"
      "ds_read_b64_tr_b16 a[108:109], v247 offset:0\n"
      "ds_read_b64_tr_b16 a[110:111], v247 offset:4096\n"
      "v_mfma_f32_16x16x32_bf16 v[68:71], a[92:95], a[48:51], v[68:71]\n"
      "s_and_b32 s62, s62, 0x1ffff\n"
      "v_exp_f32 v188, v104\n"
      "v_mfma_f32_16x16x32_bf16 v[76:79], a[68:71], a[4:7], v[164:167]\n"
      "v_readlane_b32 s60, v219, s61\n"
      "v_exp_f32 v189, v105\n"
      "v_mfma_f32_16x16x32_bf16 v[76:79], a[76:79], a[20:23], v[76:79]\n"
      "v_exp_f32 v190, v106\n"
      "v_mfma_f32_16x16x32_bf16 v[76:79], a[84:87], a[36:39], v[76:79]\n"
      "v_exp_f32 v191, v107\n"
      "v_mfma_f32_16x16x32_bf16 v[76:79], a[92:95], a[52:55], v[76:79]\n"
      "v_cvt_pk_bf16_f32 v148, v188, v189\n"
      "v_cvt_pk_bf16_f32 v149, v190, v191\n"
      "v_mfma_f32_16x16x32_bf16 v[84:87], a[68:71], a[8:11], v[168:171]\n"
      "v_exp_f32 v192, v108\n"
      "v_mfma_f32_16x16x32_bf16 v[84:87], a[76:79], a[24:27], v[84:87]\n"
      "v_exp_f32 v193, v109\n"
      "s_waitcnt lgkmcnt(0)\n"
      "v_mfma_f32_16x16x32_bf16 v[84:87], a[84:87], a[40:43], v[84:87]\n"
      "s_sub_u32 s67, s68, s66\n"
      "ds_read_b64_tr_b16 a[112:113], v244 offset:1024\n"
      "v_cvt_pk_bf16_f32 v150, v192, v193\n"
      "v_mfma_f32_16x16x32_bf16 v[84:87], a[92:95], a[56:59], v[84:87]\n"
      "s_cmp_gt_i32 s67, s70\n"
      "v_exp_f32 v194, v110\n"
      "v_mfma_f32_16x16x32_bf16 v[92:95], a[68:71], a[12:15], v[172:175]\n"
      "s_cselect_b32 s69, s77, s86\n"
      "ds_read_b64_tr_b16 a[114:115], v244 offset:5120\n"
      "ds_read_b64_tr_b16 a[116:117], v245 offset:1024\n"
      "v_mfma_f32_16x16x32_bf16 v[92:95], a[76:79], a[28:31], v[92:95]\n"
      "s_cselect_b32 s70, 60, s70\n"
      "v_exp_f32 v195, v111\n"
      "v_mfma_f32_16x16x32_bf16 v[92:95], a[84:87], a[44:47], v[92:95]\n"
      "s_cselect_b32 s90, s80, s90\n"
      "ds_read_b64_tr_b16 a[118:119], v245 offset:5120\n"
      "v_cvt_pk_bf16_f32 v151, v194, v195\n"
      "v_mfma_f32_16x16x32_bf16 v[92:95], a[92:95], a[60:63], v[92:95]\n"
      "s_add_u32 s66, s66, s69\n"
      "v_exp_f32 v188, v112\n"
      "v_mfma_f32_16x16x32_bf16 v[184:187], a[132:135], v[148:151], v[184:187]\n"
      "s_mov_b32 s82, s68\n"
      "ds_read_b64_tr_b16 a[120:121], v246 offset:1024\n"
      "ds_read_b64_tr_b16 a[122:123], v246 offset:5120\n"
      "v_mfma_f32_16x16x32_bf16 v[0:3], a[96:99], v[128:131], v[0:3]\n"
      "s_add_u32 s65, s68, s90\n"
      "v_exp_f32 v189, v113\n"
      "v_mfma_f32_16x16x32_bf16 v[4:7], a[96:99], v[132:135], v[4:7]\n"
      "s_max_u32 s88, s66, s65\n"
      "ds_read_b64_tr_b16 a[124:125], v247 offset:1024\n"
      "v_cvt_pk_bf16_f32 v152, v188, v189\n"
      "v_mfma_f32_16x16x32_bf16 a[144:147], a[96:99], v[136:139], a[144:147]\n"
      "v_exp_f32 v190, v114\n"
      "v_mfma_f32_16x16x32_bf16 a[148:151], a[96:99], v[140:143], a[148:151]\n"
      "s_getreg_b32 s67, hwreg(HW_REG_IB_STS, 0, 4)\n"
      "v_exp_f32 v191, v115\n"
      "v_mfma_f32_16x16x32_bf16 a[156:159], a[100:103], v[140:143], a[156:159]\n"
      "s_cmp_gt_u32 s67, 4\n"
      "ds_read_b64_tr_b16 a[126:127], v247 offset:5120\n"
      "v_cvt_pk_bf16_f32 v153, v190, v191\n"
      "v_mfma_f32_16x16x32_bf16 a[152:155], a[100:103], v[136:139], a[152:155]\n"
      "s_addc_u32 s89, s89, 0\n"
      "v_exp_f32 v192, v116\n"
      "v_mfma_f32_16x16x32_bf16 v[12:15], a[100:103], v[132:135], v[12:15]\n"
      "v_exp_f32 v193, v117\n"
      "v_mfma_f32_16x16x32_bf16 v[8:11], a[100:103], v[128:131], v[8:11]\n"
      "v_exp_f32 v194, v118\n"
      "v_mfma_f32_16x16x32_bf16 v[16:19], a[104:107], v[128:131], v[16:19]\n"
      "s_add_u32 s38, s38, s38\n"
      "v_exp_f32 v195, v119\n"
      "v_mfma_f32_16x16x32_bf16 v[20:23], a[104:107], v[132:135], v[20:23]\n"
      "s_memrealtime s[68:69]\n"
      "v_cvt_pk_bf16_f32 v154, v192, v193\n"
      "v_cvt_pk_bf16_f32 v155, v194, v195\n"
      "v_mfma_f32_16x16x32_bf16 a[160:163], a[104:107], v[136:139], a[160:163]\n"
      "v_exp_f32 v188, v120\n"
      "v_mfma_f32_16x16x32_bf16 v[184:187], a[136:139], v[152:155], v[184:187]\n"
      "s_mov_b32 m0, s62\n"
      "v_exp_f32 v189, v121\n"
      "v_mfma_f32_16x16x32_bf16 a[164:167], a[104:107], v[140:143], a[164:167]\n"
      "buffer_load_dwordx4 v228, s[56:59], s60 offen nt lds\n"
      "v_mfma_f32_16x16x32_bf16 a[172:175], a[108:111], v[140:143], a[172:175]\n"
      "buffer_load_dwordx4 v229, s[56:59], s60 offen offset:1024 nt lds\n"
      "v_mfma_f32_16x16x32_bf16 a[168:171], a[108:111], v[136:139], a[168:171]\n"
      "buffer_load_dwordx4 v230, s[56:59], s60 offen offset:2048 nt lds\n"
      "v_mfma_f32_16x16x32_bf16 v[28:31], a[108:111], v[132:135], v[28:31]\n"
      "buffer_load_dwordx4 v231, s[56:59], s60 offen offset:3072 nt lds\n"
      "s_cbranch_scc1 SP2_%=\n"
      "HD2_%=:\n"
      "s_cmp_lg_u32 s54, 0\n"
      "s_waitcnt vmcnt(12) lgkmcnt(0)\n"
      "s_cbranch_scc1 W2_%=\n"
      "s_waitcnt vmcnt(8)\n"
      "W2_%=:\n"
      "s_sub_u32 s67, s68, s88\n"
      "s_cmp_lt_i32 s67, -8\n"
      "s_cbranch_scc1 GW5_%=\n"
      "GX5_%=:\n"
      "s_barrier\n"
      "v_mfma_f32_16x16x32_bf16 v[24:27], a[108:111], v[128:131], v[24:27]\n"
      "s_memrealtime s[68:69]\n"
      "ds_read_b128 a[64:67], v242 offset:32768\n"
      "ds_read_b128 a[72:75], v243 offset:32768\n"
      "v_mfma_f32_16x16x32_bf16 v[32:35], a[112:115], v[128:131], v[32:35]\n"
      "s_xor_b64 s[56:57], s[56:57], s[32:33]\n"
      "ds_read_b128 a[80:83], v242 offset:33792\n"
      "ds_read_b128 a[88:91], v243 offset:33792\n"
      "v_mfma_f32_16x16x32_bf16 v[36:39], a[112:115], v[132:135], v[36:39]\n"
      "s_add_u32 s61, s61, 1\n"
      "v_exp_f32 v190, v122\n"
      "v_mfma_f32_16x16x32_bf16 a[176:179], a[112:115], v[136:139], a[176:179]\n"
      "s_add_u32 s62, s62, 0x4000\n"
      "v_exp_f32 v191, v123\n"
      "v_mfma_f32_16x16x32_bf16 a[180:183], a[112:115], v[140:143], a[180:183]\n"
      "s_and_b32 s62, s62, 0x1ffff\n"
      "v_cvt_pk_bf16_f32 v156, v188, v189\n"
      "v_cvt_pk_bf16_f32 v157, v190, v191\n"
      "v_mfma_f32_16x16x32_bf16 a[188:191], a[116:119], v[140:143], a[188:191]\n"
      "v_readlane_b32 s60, v219, s61\n"
      "v_exp_f32 v192, v124\n"
      "v_mfma_f32_16x16x32_bf16 a[184:187], a[116:119], v[136:139], a[184:187]\n"
      "v_exp_f32 v193, v125\n"
      "v_mfma_f32_16x16x32_bf16 v[44:47], a[116:119], v[132:135], v[44:47]\n"
      "v_exp_f32 v194, v126\n"
      "v_mfma_f32_16x16x32_bf16 v[40:43], a[116:119], v[128:131], v[40:43]\n"
      "v_exp_f32 v195, v127\n"
      "v_mfma_f32_16x16x32_bf16 v[48:51], a[120:123], v[128:131], v[48:51]\n"
      "v_cvt_pk_bf16_f32 v158, v192, v193\n"
      "v_cvt_pk_bf16_f32 v159, v194, v195\n"
      "v_mfma_f32_16x16x32_bf16 v[52:55], a[120:123], v[132:135], v[52:55]\n"
      "s_waitcnt lgkmcnt(0)\n"
      "v_mfma_f32_16x16x32_bf16 v[184:187], a[140:143], v[156:159], v[184:187]\n"
      "s_sub_u32 s67, s68, s66\n"
      "ds_read_b128 a[68:71], v242 offset:36864\n"
      "ds_read_b128 a[76:79], v243 offset:36864\n"
      "v_mfma_f32_16x16x32_bf16 a[192:195], a[120:123], v[136:139], a[192:195]\n"
      "s_cmp_gt_i32 s67, s70\n"
      "ds_read_b128 a[84:87], v242 offset:37888\n"
      "ds_read_b128 a[92:95], v243 offset:37888\n"
      "v_mfma_f32_16x16x32_bf16 a[196:199], a[120:123], v[140:143], a[196:199]\n"
      "s_cselect_b32 s69, s77, s86\n"
      "v_max3_f32 v196, v184, v185, v186\n"
      "v_max_f32 v196, v196, v187\n"
      "v_mfma_f32_16x16x32_bf16 a[204:207], a[124:127], v[140:143], a[204:207]\n"
      "s_cselect_b32 s70, 60, s70\n"
      "v_cmp_nge_f32 vcc, 0x4b800000, v196\n"
      "v_mfma_f32_16x16x32_bf16 a[200:203], a[124:127], v[136:139], a[200:203]\n"
      "s_cselect_b32 s90, s80, s90\n"
      "v_mfma_f32_16x16x32_bf16 v[60:63], a[124:127], v[132:135], v[60:63]\n"
      "s_add_u32 s66, s66, s69\n"
      "v_mfma_f32_16x16x32_bf16 v[56:59], a[124:127], v[128:131], v[56:59]\n"
      "s_mov_b32 s82, s68\n"
      "s_cbranch_vccnz FS1_%=\n"
      "FR1_%=:\n"
      "v_mfma_f32_16x16x32_bf16 v[96:99], a[64:67], a[0:3], v[160:163]\n"
      "s_add_u32 s65, s68, s90\n"
      "v_exp_f32 v188, v64\n"
      "v_mfma_f32_16x16x32_bf16 v[96:99], a[72:75], a[16:19], v[96:99]\n"
      "s_max_u32 s88, s66, s65\n"
      "v_add_f32 v180, v180, v184\n"
      "v_add_f32 v181, v181, v185\n"
      "v_mfma_f32_16x16x32_bf16 v[96:99], a[80:83], a[32:35], v[96:99]\n"
      "v_exp_f32 v189, v65\n"
      "v_mfma_f32_16x16x32_bf16 v[96:99], a[88:91], a[48:51], v[96:99]\n"
      "s_getreg_b32 s67, hwreg(HW_REG_IB_STS, 0, 4)\n"
      "v_add_f32 v182, v182, v186\n"
      "v_add_f32 v183, v183, v187\n"
      "v_mfma_f32_16x16x32_bf16 v[104:107], a[64:67], a[4:7], v[164:167]\n"
      "s_cmp_gt_u32 s67, 4\n"
      "v_exp_f32 v190, v66\n"
      "v_mfma_f32_16x16x32_bf16 v[104:107], a[72:75], a[20:23], v[104:107]\n"
      "s_addc_u32 s89, s89, 0\n"
      "v_exp_f32 v191, v67\n"
      "v_mfma_f32_16x16x32_bf16 v[104:107], a[80:83], a[36:39], v[104:107]\n"
      "v_cvt_pk_bf16_f32 v128, v188, v189\n"
      "v_cvt_pk_bf16_f32 v129, v190, v191\n"
      "v_mfma_f32_16x16x32_bf16 v[104:107], a[88:91], a[52:55], v[104:107]\n"
      "v_exp_f32 v192, v68\n"
      "v_mfma_f32_16x16x32_bf16 v[112:115], a[64:67], a[8:11], v[168:171]\n"
      "s_add_u32 s39, s39, s39\n"
      "v_exp_f32 v193, v69\n"
      "v_mfma_f32_16x16x32_bf16 v[112:115], a[72:75], a[24:27], v[112:115]\n"
      "s_memrealtime s[68:69]\n"
      "v_exp_f32 v194, v70\n"
      "v_mfma_f32_16x16x32_bf16 v[112:115], a[80:83], a[40:43], v[112:115]\n"
      "v_exp_f32 v195, v71\n"
      "v_mfma_f32_16x16x32_bf16 v[112:115], a[88:91], a[56:59], v[112:115]\n"
      "s_mov_b32 m0, s62\n"
      "v_cvt_pk_bf16_f32 v130, v192, v193\n"
      "v_cvt_pk_bf16_f32 v131, v194, v195\n"
      "v_mfma_f32_16x16x32_bf16 v[120:123], a[64:67], a[12:15], v[172:175]\n"
      "buffer_load_dwordx4 v228, s[56:59], s60 offen nt lds\n"
      "v_mfma_f32_16x16x32_bf16 v[184:187], a[128:131], v[128:131], 0\n"
      "buffer_load_dwordx4 v229, s[56:59], s60 offen offset:1024 nt lds\n"
      "v_mfma_f32_16x16x32_bf16 v[120:123], a[72:75], a[28:31], v[120:123]\n"
      "buffer_load_dwordx4 v230, s[56:59], s60 offen offset:2048 nt lds\n"
      "v_mfma_f32_16x16x32_bf16 v[120:123], a[80:83], a[44:47], v[120:123]\n"
      "buffer_load_dwordx4 v231, s[56:59], s60 offen offset:3072 nt lds\n"
      "s_cbranch_scc1 SP3_%=\n"
      "HD3_%=:\n"
      "s_cmp_lg_u32 s54, 0\n"
      "s_waitcnt vmcnt(12) lgkmcnt(0)\n"
      "s_cbranch_scc1 W3_%=\n"
      "s_waitcnt vmcnt(8)\n"
      "W3_%=:\n"
      "s_sub_u32 s67, s68, s88\n"
      "s_cmp_lt_i32 s67, -8\n"
      "s_cbranch_scc1 GW6_%=\n"
      "GX6_%=:\n"
      "s_barrier\n"
      "v_mfma_f32_16x16x32_bf16 v[120:123], a[88:91], a[60:63], v[120:123]\n"
      "s_memrealtime s[68:69]\n"
      "ds_read_b64_tr_b16 a[96:97], v244 offset:32768\n"
      "ds_read_b64_tr_b16 a[98:99], v244 offset:36864\n"
      "v_mfma_f32_16x16x32_bf16 v[100:103], a[68:71], a[0:3], v[160:163]\n"
      "s_xor_b64 s[56:57], s[56:57], s[32:33]\n"
      "ds_read_b64_tr_b16 a[100:101], v245 offset:32768\n"
      "ds_read_b64_tr_b16 a[102:103], v245 offset:36864\n"
      "v_mfma_f32_16x16x32_bf16 v[100:103], a[76:79], a[16:19], v[100:103]\n"
      "s_add_u32 s61, s61, 1\n"
      "ds_read_b64_tr_b16 a[104:105], v246 offset:32768\n"
      "ds_read_b64_tr_b16 a[106:107], v246 offset:36864\n"
      "v_mfma_f32_16x16x32_bf16 v[100:103], a[84:87], a[32:35], v[100:103]\n"
      "s_add_u32 s62, s62, 0x4000\n"
      "ds_read_b64_tr_b16 a[108:109], v247 offset:32768\n"
      "ds_read_b64_tr_b16 a[110:111], v247 offset:36864\n"
      "v_mfma_f32_16x16x32_bf16 v[100:103], a[92:95], a[48:51], v[100:103]\n"
      "s_and_b32 s62, s62, 0x1ffff\n"
      "v_exp_f32 v188, v72\n"
      "v_mfma_f32_16x16x32_bf16 v[108:111], a[68:71], a[4:7], v[164:167]\n"
      "v_readlane_b32 s60, v219, s61\n"
      "v_exp_f32 v189, v73\n"
      "v_mfma_f32_16x16x32_bf16 v[108:111], a[76:79], a[20:23], v[108:111]\n"
      "v_xor_b32 v242, 0x10000, v242\n"
      "v_xor_b32 v243, 0x10000, v243\n"
      "v_mfma_f32_16x16x32_bf16 v[108:111], a[84:87], a[36:39], v[108:111]\n"
      "v_exp_f32 v190, v74\n"
      "v_mfma_f32_16x16x32_bf16 v[108:111], a[92:95], a[52:55], v[108:111]\n"
      "v_exp_f32 v191, v75\n"
      "v_mfma_f32_16x16x32_bf16 v[116:119], a[68:71], a[8:11], v[168:171]\n"
      "v_cvt_pk_bf16_f32 v132, v188, v189\n"
      "v_cvt_pk_bf16_f32 v133, v190, v191\n"
      "v_mfma_f32_16x16x32_bf16 v[116:119], a[76:79], a[24:27], v[116:119]\n"
      "v_exp_f32 v192, v76\n"
      "s_waitcnt lgkmcnt(0)\n"
      "v_mfma_f32_16x16x32_bf16 v[116:119], a[84:87], a[40:43], v[116:119]\n"
      "s_sub_u32 s67, s68, s66\n"
      "ds_read_b64_tr_b16 a[112:113], v244 offset:33792\n"
      "ds_read_b64_tr_b16 a[114:115], v244 offset:37888\n"
      "v_mfma_f32_16x16x32_bf16 v[116:119], a[92:95], a[56:59], v[116:119]\n"
      "s_cmp_gt_i32 s67, s70\n"
      "v_exp_f32 v193, v77\n"
      "v_mfma_f32_16x16x32_bf16 v[124:127], a[68:71], a[12:15], v[172:175]\n"
      "s_cselect_b32 s69, s77, s86\n"
      "ds_read_b64_tr_b16 a[116:117], v245 offset:33792\n"
      "v_cvt_pk_bf16_f32 v134, v192, v193\n"
      "v_mfma_f32_16x16x32_bf16 v[124:127], a[76:79], a[28:31], v[124:127]\n"
      "s_cselect_b32 s70, 60, s70\n"
      "v_exp_f32 v194, v78\n"
      "v_mfma_f32_16x16x32_bf16 v[124:127], a[84:87], a[44:47], v[124:127]\n"
      "s_cselect_b32 s90, s80, s90\n"
      "ds_read_b64_tr_b16 a[118:119], v245 offset:37888\n"
      "ds_read_b64_tr_b16 a[120:121], v246 offset:33792\n"
      "v_mfma_f32_16x16x32_bf16 v[124:127], a[92:95], a[60:63], v[124:127]\n"
      "s_add_u32 s66, s66, s69\n"
      "v_exp_f32 v195, v79\n"
      "v_mfma_f32_16x16x32_bf16 v[0:3], a[96:99], v[144:147], v[0:3]\n"
      "s_mov_b32 s82, s68\n"
      "ds_read_b64_tr_b16 a[122:123], v246 offset:37888\n"
      "v_cvt_pk_bf16_f32 v135, v194, v195\n"
      "v_mfma_f32_16x16x32_bf16 v[4:7], a[96:99], v[148:151], v[4:7]\n"
      "s_add_u32 s65, s68, s90\n"
      "v_exp_f32 v188, v80\n"
      "v_mfma_f32_16x16x32_bf16 v[184:187], a[132:135], v[132:135], v[184:187]\n"
      "s_max_u32 s88, s66, s65\n"
      "ds_read_b64_tr_b16 a[124:125], v247 offset:33792\n"
      "ds_read_b64_tr_b16 a[126:127], v247 offset:37888\n"
      "v_mfma_f32_16x16x32_bf16 a[144:147], a[96:99], v[152:155], a[144:147]\n"
      "v_exp_f32 v189, v81\n"
      "v_mfma_f32_16x16x32_bf16 a[148:151], a[96:99], v[156:159], a[148:151]\n"
      "s_getreg_b32 s67, hwreg(HW_REG_IB_STS, 0, 4)\n"
      "v_exp_f32 v190, v82\n"
      "v_mfma_f32_16x16x32_bf16 a[156:159], a[100:103], v[156:159], a[156:159]\n"
      "s_cmp_gt_u32 s67, 4\n"
      "v_exp_f32 v191, v83\n"
      "v_mfma_f32_16x16x32_bf16 a[152:155], a[100:103], v[152:155], a[152:155]\n"
      "s_addc_u32 s89, s89, 0\n"
      "v_cvt_pk_bf16_f32 v136, v188, v189\n"
      "v_cvt_pk_bf16_f32 v137, v190, v191\n"
      "v_mfma_f32_16x16x32_bf16 v[12:15], a[100:103], v[148:151], v[12:15]\n"
      "v_exp_f32 v192, v84\n"
      "v_mfma_f32_16x16x32_bf16 v[8:11], a[100:103], v[144:147], v[8:11]\n"
      "v_exp_f32 v193, v85\n"
      "v_mfma_f32_16x16x32_bf16 v[16:19], a[104:107], v[144:147], v[16:19]\n"
      "s_add_u32 s38, s38, s38\n"
      "v_exp_f32 v194, v86\n"
      "v_mfma_f32_16x16x32_bf16 v[20:23], a[104:107], v[148:151], v[20:23]\n"
      "s_memrealtime s[68:69]\n"
      "v_exp_f32 v195, v87\n"
      "v_mfma_f32_16x16x32_bf16 a[160:163], a[104:107], v[152:155], a[160:163]\n"
      "v_cvt_pk_bf16_f32 v138, v192, v193\n"
      "v_cvt_pk_bf16_f32 v139, v194, v195\n"
      "v_mfma_f32_16x16x32_bf16 a[164:167], a[104:107], v[156:159], a[164:167]\n"
      "s_mov_b32 m0, s62\n"
      "v_exp_f32 v188, v88\n"
      "v_mfma_f32_16x16x32_bf16 v[184:187], a[136:139], v[136:139], v[184:187]\n"
      "buffer_load_dwordx4 v228, s[56:59], s60 offen nt lds\n"
      "v_mfma_f32_16x16x32_bf16 a[172:175], a[108:111], v[156:159], a[172:175]\n"
      "buffer_load_dwordx4 v229, s[56:59], s60 offen offset:1024 nt lds\n"
      "v_mfma_f32_16x16x32_bf16 a[168:171], a[108:111], v[152:155], a[168:171]\n"
      "buffer_load_dwordx4 v230, s[56:59], s60 offen offset:2048 nt lds\n"
      "v_mfma_f32_16x16x32_bf16 v[28:31], a[108:111], v[148:151], v[28:31]\n"
      "buffer_load_dwordx4 v231, s[56:59], s60 offen offset:3072 nt lds\n"
      "HD0_%=:\n"
      "s_cmp_lg_u32 s54, 0\n"
      "s_waitcnt vmcnt(12) lgkmcnt(0)\n"
      "s_cbranch_scc1 W4_%=\n"
      "s_waitcnt vmcnt(8)\n"
      "W4_%=:\n"
      "s_sub_u32 s67, s68, s88\n"
      "s_cmp_lt_i32 s67, -8\n"
      "s_cbranch_scc1 GW7_%=\n"
      "GX7_%=:\n"
      "s_barrier\n"
      "v_mfma_f32_16x16x32_bf16 v[24:27], a[108:111], v[144:147], v[24:27]\n"
      "s_memrealtime s[68:69]\n"
      "ds_read_b128 a[64:67], v242 offset:0\n"
      "ds_read_b128 a[72:75], v243 offset:0\n"
      "v_mfma_f32_16x16x32_bf16 v[32:35], a[112:115], v[144:147], v[32:35]\n"
      "s_xor_b64 s[56:57], s[56:57], s[32:33]\n"
      "ds_read_b128 a[80:83], v242 offset:1024\n"
      "ds_read_b128 a[88:91], v243 offset:1024\n"
      "v_mfma_f32_16x16x32_bf16 v[36:39], a[112:115], v[148:151], v[36:39]\n"
      "s_add_u32 s61, s61, 1\n"
      "v_exp_f32 v189, v89\n"
      "v_mfma_f32_16x16x32_bf16 a[176:179], a[112:115], v[152:155], a[176:179]\n"
      "s_add_u32 s62, s62, 0x4000\n"
      "v_exp_f32 v190, v90\n"
      "v_mfma_f32_16x16x32_bf16 a[180:183], a[112:115], v[156:159], a[180:183]\n"
      "s_and_b32 s62, s62, 0x1ffff\n"
      "v_exp_f32 v191, v91\n"
      "v_mfma_f32_16x16x32_bf16 a[188:191], a[116:119], v[156:159], a[188:191]\n"
      "v_cvt_pk_bf16_f32 v140, v188, v189\n"
      "v_readlane_b32 s60, v219, s61\n"
      "v_mfma_f32_16x16x32_bf16 a[184:187], a[116:119], v[152:155], a[184:187]\n"
      "v_exp_f32 v192, v92\n"
      "v_mfma_f32_16x16x32_bf16 v[44:47], a[116:119], v[148:151], v[44:47]\n"
      "v_exp_f32 v193, v93\n"
      "v_mfma_f32_16x16x32_bf16 v[40:43], a[116:119], v[144:147], v[40:43]\n"
      "v_cvt_pk_bf16_f32 v141, v190, v191\n"
      "v_cvt_pk_bf16_f32 v142, v192, v193\n"
      "v_mfma_f32_16x16x32_bf16 v[48:51], a[120:123], v[144:147], v[48:51]\n"
      "v_exp_f32 v194, v94\n"
      "v_mfma_f32_16x16x32_bf16 v[52:55], a[120:123], v[148:151], v[52:55]\n"
      "v_exp_f32 v195, v95\n"
      "s_waitcnt lgkmcnt(0)\n"
      "v_mfma_f32_16x16x32_bf16 a[192:195], a[120:123], v[152:155], a[192:195]\n"
      "s_sub_u32 s67, s68, s66\n"
      "v_cvt_pk_bf16_f32 v143, v194, v195\n"
      "v_xor_b32 v244, 0x10000, v244\n"
      "v_mfma_f32_16x16x32_bf16 a[196:199], a[120:123], v[156:159], a[196:199]\n"
      "s_cmp_gt_i32 s67, s70\n"
      "ds_read_b128 a[68:71], v242 offset:4096\n"
      "ds_read_b128 a[76:79], v243 offset:4096\n"
      "v_mfma_f32_16x16x32_bf16 v[184:187], a[140:143], v[140:143], v[184:187]\n"
      "s_cselect_b32 s69, s77, s86\n"
      "ds_read_b128 a[84:87], v242 offset:5120\n"
      "ds_read_b128 a[92:95], v243 offset:5120\n"
      "v_mfma_f32_16x16x32_bf16 a[204:207], a[124:127], v[156:159], a[204:207]\n"
      "s_cselect_b32 s70, 60, s70\n"
      "v_xor_b32 v245, 0x10000, v245\n"
      "v_xor_b32 v246, 0x10000, v246\n"
      "v_mfma_f32_16x16x32_bf16 a[200:203], a[124:127], v[152:155], a[200:203]\n"
      "s_cselect_b32 s90, s80, s90\n"
      "v_xor_b32 v247, 0x10000, v247\n"
      "v_max3_f32 v196, v184, v185, v186\n"
      "v_mfma_f32_16x16x32_bf16 v[60:63], a[124:127], v[148:151], v[60:63]\n"
      "s_add_u32 s66, s66, s69\n"
      "v_max_f32 v196, v196, v187\n"
      "v_cmp_nge_f32 vcc, 0x4b800000, v196\n"
      "v_mfma_f32_16x16x32_bf16 v[56:59], a[124:127], v[144:147], v[56:59]\n"
      "s_mov_b32 s82, s68\n"
      "s_cbranch_vccnz FS2_%=\n"
      "FR2_%=:\n"
      "s_branch LOOP_%=\n"
      "SP1_%=:\n"
      "s_ff1_i32_b32 s64, s39\n"
      "s_lshl1_add_u32 s64, s64, 1\n"
      "s_bitcmp1_b64 s[52:53], s64\n"
      "s_cbranch_scc0 SH1_%=\n"
      "s_swappc_b64 s[84:85], s[84:85]\n"
      "SH1_%=:\n"
      "s_cmp_lt_u32 s64, 63\n"
      "s_cbranch_scc1 HD1_%=\n"
      "s_xor_b64 s[56:57], s[56:57], s[32:33]\n"
      "s_add_u32 s61, s61, 1\n"
      "s_add_u32 s62, s62, 0x4000\n"
      "s_and_b32 s62, s62, 0x1ffff\n"
      "v_readlane_b32 s74, v219, 60\n"
      "v_readlane_b32 s76, v219, 62\n"
      "s_mov_b32 s61, 0\n"
      "s_add_u32 s74, s74, 0x400\n"
      "v_add_u32 v228, s74, v229\n"
      "v_add_u32 v230, s74, v231\n"
      "v_add_u32 v229, s76, v229\n"
      "v_add_u32 v231, s76, v231\n"
      "s_branch HD1_%=\n"
      "SP2_%=:\n"
      "s_cmp_eq_u32 s38, 0\n"
      "s_cbranch_scc1 L62_%=\n"
      "s_ff1_i32_b32 s64, s38\n"
      "s_lshl1_add_u32 s64, s64, 0\n"
      "s_bitcmp1_b64 s[52:53], s64\n"
      "s_cbranch_scc0 SH2_%=\n"
      "s_swappc_b64 s[84:85], s[84:85]\n"
      "SH2_%=:\n"
      "s_branch HD2_%=\n"
      "L62_%=:\n"
      "s_xor_b64 s[56:57], s[56:57], s[32:33]\n"
      "s_mov_b32 s61, 0\n"
      "v_xor_b32 v228, 0x80, v228\n"
      "v_xor_b32 v229, 0x80, v229\n"
      "v_xor_b32 v230, 0x80, v230\n"
      "v_xor_b32 v231, 0x80, v231\n"
      "s_mov_b32 s38, 0x80000000\n"
      "s_branch HD2_%=\n"
      "SP3_%=:\n"
      "s_cmp_eq_u32 s39, 0\n"
      "s_cbranch_scc1 NH_%=\n"
      "s_ff1_i32_b32 s64, s39\n"
      "s_lshl1_add_u32 s64, s64, 1\n"
      "s_bitcmp1_b64 s[52:53], s64\n"
      "s_cbranch_scc0 SH3_%=\n"
      "s_swappc_b64 s[84:85], s[84:85]\n"
      "SH3_%=:\n"
      "s_bitcmp1_b64 s[34:35], s64\n"
      "s_cbranch_scc0 HD3_%=\n"
      "NH_%=:\n"
      "s_cmp_lg_u32 s54, 0\n"
      "s_waitcnt vmcnt(12) lgkmcnt(0)\n"
      "s_cbranch_scc1 W5_%=\n"
      "s_waitcnt vmcnt(8)\n"
      "W5_%=:\n"
      "s_barrier\n"
      "v_mfma_f32_16x16x32_bf16 v[120:123], a[88:91], a[60:63], v[120:123]\n"
      "s_sub_u32 s54, s54, 1\n"
      "ds_read_b64_tr_b16 a[96:97], v244 offset:32768\n"
      "ds_read_b64_tr_b16 a[98:99], v244 offset:36864\n"
      "v_mfma_f32_16x16x32_bf16 v[100:103], a[68:71], a[0:3], v[160:163]\n"
      "ds_read_b64_tr_b16 a[100:101], v245 offset:32768\n"
      "ds_read_b64_tr_b16 a[102:103], v245 offset:36864\n"
      "v_mfma_f32_16x16x32_bf16 v[100:103], a[76:79], a[16:19], v[100:103]\n"
      "ds_read_b64_tr_b16 a[104:105], v246 offset:32768\n"
      "ds_read_b64_tr_b16 a[106:107], v246 offset:36864\n"
      "v_mfma_f32_16x16x32_bf16 v[100:103], a[84:87], a[32:35], v[100:103]\n"
      "ds_read_b64_tr_b16 a[108:109], v247 offset:32768\n"
      "ds_read_b64_tr_b16 a[110:111], v247 offset:36864\n"
      "v_mfma_f32_16x16x32_bf16 v[100:103], a[92:95], a[48:51], v[100:103]\n"
      "v_exp_f32 v188, v72\n"
      "v_mfma_f32_16x16x32_bf16 v[108:111], a[68:71], a[4:7], v[164:167]\n"
      "v_exp_f32 v189, v73\n"
      "v_mfma_f32_16x16x32_bf16 v[108:111], a[76:79], a[20:23], v[108:111]\n"
      "v_xor_b32 v242, 0x10000, v242\n"
      "v_xor_b32 v243, 0x10000, v243\n"
      "v_mfma_f32_16x16x32_bf16 v[108:111], a[84:87], a[36:39], v[108:111]\n"
      "v_exp_f32 v190, v74\n"
      "v_mfma_f32_16x16x32_bf16 v[108:111], a[92:95], a[52:55], v[108:111]\n"
      "v_exp_f32 v191, v75\n"
      "v_mfma_f32_16x16x32_bf16 v[116:119], a[68:71], a[8:11], v[168:171]\n"
      "v_cvt_pk_bf16_f32 v132, v188, v189\n"
      "v_cvt_pk_bf16_f32 v133, v190, v191\n"
      "v_mfma_f32_16x16x32_bf16 v[116:119], a[76:79], a[24:27], v[116:119]\n"
      "v_exp_f32 v192, v76\n"
      "s_waitcnt lgkmcnt(0)\n"
      "v_mfma_f32_16x16x32_bf16 v[116:119], a[84:87], a[40:43], v[116:119]\n"
      "ds_read_b64_tr_b16 a[112:113], v244 offset:33792\n"
      "ds_read_b64_tr_b16 a[114:115], v244 offset:37888\n"
      "v_mfma_f32_16x16x32_bf16 v[116:119], a[92:95], a[56:59], v[116:119]\n"
      "v_exp_f32 v193, v77\n"
      "v_mfma_f32_16x16x32_bf16 v[124:127], a[68:71], a[12:15], v[172:175]\n"
      "ds_read_b64_tr_b16 a[116:117], v245 offset:33792\n"
      "v_cvt_pk_bf16_f32 v134, v192, v193\n"
      "v_mfma_f32_16x16x32_bf16 v[124:127], a[76:79], a[28:31], v[124:127]\n"
      "v_exp_f32 v194, v78\n"
      "v_mfma_f32_16x16x32_bf16 v[124:127], a[84:87], a[44:47], v[124:127]\n"
      "ds_read_b64_tr_b16 a[118:119], v245 offset:37888\n"
      "ds_read_b64_tr_b16 a[120:121], v246 offset:33792\n"
      "v_mfma_f32_16x16x32_bf16 v[124:127], a[92:95], a[60:63], v[124:127]\n"
      "v_exp_f32 v195, v79\n"
      "v_mfma_f32_16x16x32_bf16 v[0:3], a[96:99], v[144:147], v[0:3]\n"
      "ds_read_b64_tr_b16 a[122:123], v246 offset:37888\n"
      "v_cvt_pk_bf16_f32 v135, v194, v195\n"
      "v_mfma_f32_16x16x32_bf16 v[4:7], a[96:99], v[148:151], v[4:7]\n"
      "v_exp_f32 v188, v80\n"
      "v_mfma_f32_16x16x32_bf16 v[184:187], a[132:135], v[132:135], v[184:187]\n"
      "ds_read_b64_tr_b16 a[124:125], v247 offset:33792\n"
      "ds_read_b64_tr_b16 a[126:127], v247 offset:37888\n"
      "v_mfma_f32_16x16x32_bf16 a[144:147], a[96:99], v[152:155], a[144:147]\n"
      "v_exp_f32 v189, v81\n"
      "v_mfma_f32_16x16x32_bf16 a[148:151], a[96:99], v[156:159], a[148:151]\n"
      "v_exp_f32 v190, v82\n"
      "v_mfma_f32_16x16x32_bf16 a[156:159], a[100:103], v[156:159], a[156:159]\n"
      "v_exp_f32 v191, v83\n"
      "v_mfma_f32_16x16x32_bf16 a[152:155], a[100:103], v[152:155], a[152:155]\n"
      "v_cvt_pk_bf16_f32 v136, v188, v189\n"
      "v_cvt_pk_bf16_f32 v137, v190, v191\n"
      "v_mfma_f32_16x16x32_bf16 v[12:15], a[100:103], v[148:151], v[12:15]\n"
      "v_exp_f32 v192, v84\n"
      "v_mfma_f32_16x16x32_bf16 v[8:11], a[100:103], v[144:147], v[8:11]\n"
      "v_exp_f32 v193, v85\n"
      "v_mfma_f32_16x16x32_bf16 v[16:19], a[104:107], v[144:147], v[16:19]\n"
      "s_add_u32 s38, s38, s38\n"
      "v_exp_f32 v194, v86\n"
      "v_mfma_f32_16x16x32_bf16 v[20:23], a[104:107], v[148:151], v[20:23]\n"
      "s_memrealtime s[68:69]\n"
      "v_exp_f32 v195, v87\n"
      "v_mfma_f32_16x16x32_bf16 a[160:163], a[104:107], v[152:155], a[160:163]\n"
      "v_cvt_pk_bf16_f32 v138, v192, v193\n"
      "v_cvt_pk_bf16_f32 v139, v194, v195\n"
      "v_mfma_f32_16x16x32_bf16 a[164:167], a[104:107], v[156:159], a[164:167]\n"
      "v_exp_f32 v188, v88\n"
      "v_mfma_f32_16x16x32_bf16 v[184:187], a[136:139], v[136:139], v[184:187]\n"
      "s_nop 0\n"
      "v_mfma_f32_16x16x32_bf16 a[172:175], a[108:111], v[156:159], a[172:175]\n"
      "s_nop 0\n"
      "v_mfma_f32_16x16x32_bf16 a[168:171], a[108:111], v[152:155], a[168:171]\n"
      "s_nop 0\n"
      "v_mfma_f32_16x16x32_bf16 v[28:31], a[108:111], v[148:151], v[28:31]\n"
      "s_nop 0\n"
      "s_cbranch_scc0 MH_%=\n"
      "END_%=:\n"
      "s_waitcnt lgkmcnt(0)\n"
      "v_mfma_f32_16x16x32_bf16 v[24:27], a[108:111], v[144:147], v[24:27]\n"
      "v_mov_b32 v236, s68\n"
      "v_exp_f32 v189, v89\n"
      "v_mfma_f32_16x16x32_bf16 v[32:35], a[112:115], v[144:147], v[32:35]\n"
      "s_mov_b64 exec, 1\n"
      "global_store_dwordx2 v238, v[236:237], s[46:47] offset:8 sc1\n"
      "s_mov_b64 exec, -1\n"
      "v_mfma_f32_16x16x32_bf16 v[36:39], a[112:115], v[148:151], v[36:39]\n"
      "s_mov_b32 s82, s68\n"
      "v_exp_f32 v190, v90\n"
      "v_mfma_f32_16x16x32_bf16 a[176:179], a[112:115], v[152:155], a[176:179]\n"
      "v_exp_f32 v191, v91\n"
      "v_mfma_f32_16x16x32_bf16 a[180:183], a[112:115], v[156:159], a[180:183]\n"
      "v_cvt_pk_bf16_f32 v140, v188, v189\n"
      "v_cvt_pk_bf16_f32 v141, v190, v191\n"
      "v_mfma_f32_16x16x32_bf16 a[188:191], a[116:119], v[156:159], a[188:191]\n"
      "v_exp_f32 v192, v92\n"
      "s_waitcnt vmcnt(5)\n"
      "s_barrier\n"
      "v_mfma_f32_16x16x32_bf16 a[184:187], a[116:119], v[152:155], a[184:187]\n"
      "ds_read_b64_tr_b16 a[80:81], v224 offset:0\n"
      "ds_read_b64_tr_b16 a[82:83], v224 offset:4096\n"
      "v_mfma_f32_16x16x32_bf16 v[44:47], a[116:119], v[148:151], v[44:47]\n"
      "ds_read_b64_tr_b16 a[84:85], v225 offset:0\n"
      "ds_read_b64_tr_b16 a[86:87], v225 offset:4096\n"
      "v_mfma_f32_16x16x32_bf16 v[40:43], a[116:119], v[144:147], v[40:43]\n"
      "s_mov_b32 s74, 0xffff0000\n"
      "ds_read_b64_tr_b16 a[88:89], v226 offset:0\n"
      "ds_read_b64_tr_b16 a[90:91], v226 offset:4096\n"
      "v_mfma_f32_16x16x32_bf16 v[48:51], a[120:123], v[144:147], v[48:51]\n"
      "s_mov_b32 s75, 0xffff0000\n"
      "ds_read_b64_tr_b16 a[92:93], v227 offset:0\n"
      "ds_read_b64_tr_b16 a[94:95], v227 offset:4096\n"
      "v_mfma_f32_16x16x32_bf16 v[52:55], a[120:123], v[148:151], v[52:55]\n"
      "s_mov_b32 s94, 0\n"
      "v_exp_f32 v193, v93\n"
      "v_mfma_f32_16x16x32_bf16 a[192:195], a[120:123], v[152:155], a[192:195]\n"
      "s_mov_b32 s95, -1\n"
      "ds_read_b64_tr_b16 a[16:17], v224 offset:1024\n"
      "v_cvt_pk_bf16_f32 v142, v192, v193\n"
      "v_mfma_f32_16x16x32_bf16 a[196:199], a[120:123], v[156:159], a[196:199]\n"
      "v_exp_f32 v194, v94\n"
      "v_mfma_f32_16x16x32_bf16 a[204:207], a[124:127], v[156:159], a[204:207]\n"
      "ds_read_b64_tr_b16 a[18:19], v224 offset:5120\n"
      "ds_read_b64_tr_b16 a[20:21], v225 offset:1024\n"
      "v_mfma_f32_16x16x32_bf16 a[200:203], a[124:127], v[152:155], a[200:203]\n"
      "v_exp_f32 v195, v95\n"
      "v_mfma_f32_16x16x32_bf16 v[60:63], a[124:127], v[148:151], v[60:63]\n"
      "ds_read_b64_tr_b16 a[22:23], v225 offset:5120\n"
      "v_cvt_pk_bf16_f32 v143, v194, v195\n"
      "v_mfma_f32_16x16x32_bf16 v[56:59], a[124:127], v[144:147], v[56:59]\n"
      "ds_read_b64_tr_b16 a[24:25], v226 offset:1024\n"
      "ds_read_b64_tr_b16 a[26:27], v226 offset:5120\n"
      "v_mfma_f32_16x16x32_bf16 v[184:187], a[140:143], v[140:143], v[184:187]\n"
      "ds_read_b64_tr_b16 a[28:29], v227 offset:1024\n"
      "ds_read_b64_tr_b16 a[30:31], v227 offset:5120\n"
      "ds_read_b64_tr_b16 a[0:1], v220 offset:1024\n"
      "ds_read_b64_tr_b16 a[2:3], v220 offset:5120\n"
      "ds_read_b64_tr_b16 a[4:5], v221 offset:1024\n"
      "ds_read_b64_tr_b16 a[6:7], v221 offset:5120\n"
      "ds_read_b64_tr_b16 a[8:9], v222 offset:1024\n"
      "ds_read_b64_tr_b16 a[10:11], v222 offset:5120\n"
      "ds_read_b64_tr_b16 a[12:13], v223 offset:1024\n"
      "ds_read_b64_tr_b16 a[14:15], v223 offset:5120\n"
      "v_max3_f32 v196, v184, v185, v186\n"
      "v_max_f32 v196, v196, v187\n"
      "v_cmp_nge_f32 vcc, 0x4b800000, v196\n"
      "s_cbranch_vccnz FS3_%=\n"
      "FR3_%=:\n"
      "s_waitcnt lgkmcnt(15)\n"
      "v_mfma_f32_16x16x32_bf16 a[176:179], a[80:83], v[136:139], a[176:179]\n"
      "v_add_f32 v180, v180, v184\n"
      "v_add_f32 v181, v181, v185\n"
      "v_mfma_f32_16x16x32_bf16 a[180:183], a[80:83], v[140:143], a[180:183]\n"
      "v_add_f32 v182, v182, v186\n"
      "v_add_f32 v183, v183, v187\n"
      "v_mfma_f32_16x16x32_bf16 a[184:187], a[84:87], v[136:139], a[184:187]\n"
      "v_exp_f32 v188, v112\n"
      "v_mfma_f32_16x16x32_bf16 a[188:191], a[84:87], v[140:143], a[188:191]\n"
      "ds_write_b128 v232, a[176:179] offset:0\n"
      "v_exp_f32 v189, v113\n"
      "v_mfma_f32_16x16x32_bf16 a[192:195], a[88:91], v[136:139], a[192:195]\n"
      "v_exp_f32 v190, v114\n"
      "v_mfma_f32_16x16x32_bf16 a[196:199], a[88:91], v[140:143], a[196:199]\n"
      "v_exp_f32 v191, v115\n"
      "v_mfma_f32_16x16x32_bf16 a[200:203], a[92:95], v[136:139], a[200:203]\n"
      "ds_write_b128 v232, a[180:183] offset:1024\n"
      "v_exp_f32 v192, v116\n"
      "v_mfma_f32_16x16x32_bf16 a[204:207], a[92:95], v[140:143], a[204:207]\n"
      "v_exp_f32 v193, v117\n"
      "v_exp_f32 v194, v118\n"
      "v_cvt_pk_bf16_f32 v152, v188, v189\n"
      "v_exp_f32 v195, v119\n"
      "v_cvt_pk_bf16_f32 v153, v190, v191\n"
      "ds_write_b128 v232, a[184:187] offset:2048\n"
      "v_exp_f32 v188, v120\n"
      "v_cvt_pk_bf16_f32 v154, v192, v193\n"
      "v_mfma_f32_16x16x32_bf16 v[32:35], a[80:83], v[128:131], v[32:35]\n"
      "v_exp_f32 v189, v121\n"
      "v_cvt_pk_bf16_f32 v155, v194, v195\n"
      "v_exp_f32 v190, v122\n"
      "v_cvt_pk_bf16_f32 v156, v188, v189\n"
      "v_mfma_f32_16x16x32_bf16 v[184:187], a[136:139], v[152:155], 0\n"
      "ds_write_b128 v234, v[152:155] offset:0\n"
      "v_exp_f32 v191, v123\n"
      "v_mfma_f32_16x16x32_bf16 v[36:39], a[80:83], v[132:135], v[36:39]\n"
      "v_exp_f32 v192, v124\n"
      "v_cvt_pk_bf16_f32 v157, v190, v191\n"
      "v_exp_f32 v193, v125\n"
      "v_exp_f32 v194, v126\n"
      "v_cvt_pk_bf16_f32 v158, v192, v193\n"
      "ds_write_b128 v232, a[188:191] offset:3072\n"
      "v_mfma_f32_16x16x32_bf16 v[40:43], a[84:87], v[128:131], v[40:43]\n"
      "v_exp_f32 v195, v127\n"
      "v_exp_f32 v188, v96\n"
      "v_cvt_pk_bf16_f32 v159, v194, v195\n"
      "v_exp_f32 v189, v97\n"
      "v_mfma_f32_16x16x32_bf16 v[44:47], a[84:87], v[132:135], v[44:47]\n"
      "v_exp_f32 v190, v98\n"
      "ds_write_b128 v234, v[156:159] offset:1024\n"
      "v_cvt_pk_bf16_f32 v144, v188, v189\n"
      "v_mfma_f32_16x16x32_bf16 v[184:187], a[140:143], v[156:159], v[184:187]\n"
      "v_exp_f32 v191, v99\n"
      "v_exp_f32 v192, v100\n"
      "v_cvt_pk_bf16_f32 v145, v190, v191\n"
      "v_mfma_f32_16x16x32_bf16 v[48:51], a[88:91], v[128:131], v[48:51]\n"
      "v_exp_f32 v193, v101\n"
      "ds_write_b128 v232, a[192:195] offset:4096\n"
      "v_exp_f32 v194, v102\n"
      "v_cvt_pk_bf16_f32 v146, v192, v193\n"
      "v_exp_f32 v195, v103\n"
      "v_mfma_f32_16x16x32_bf16 v[52:55], a[88:91], v[132:135], v[52:55]\n"
      "v_exp_f32 v188, v104\n"
      "v_cvt_pk_bf16_f32 v147, v194, v195\n"
      "v_exp_f32 v189, v105\n"
      "ds_write_b128 v232, a[196:199] offset:5120\n"
      "v_exp_f32 v190, v106\n"
      "v_cvt_pk_bf16_f32 v148, v188, v189\n"
      "v_mfma_f32_16x16x32_bf16 v[184:187], a[128:131], v[144:147], v[184:187]\n"
      "v_exp_f32 v191, v107\n"
      "v_exp_f32 v192, v108\n"
      "v_cvt_pk_bf16_f32 v149, v190, v191\n"
      "v_exp_f32 v193, v109\n"
      "ds_write_b128 v232, a[200:203] offset:6144\n"
      "v_mfma_f32_16x16x32_bf16 v[56:59], a[92:95], v[128:131], v[56:59]\n"
      "v_exp_f32 v194, v110\n"
      "v_cvt_pk_bf16_f32 v150, v192, v193\n"
      "v_exp_f32 v195, v111\n"
      "v_cndmask_b32 v229, v180, v181, s[74:75]\n"
      "v_cvt_pk_bf16_f32 v151, v194, v195\n"
      "v_mfma_f32_16x16x32_bf16 v[60:63], a[92:95], v[132:135], v[60:63]\n"
      "v_cndmask_b32 v200, v182, v183, s[74:75]\n"
      "ds_write_b128 v232, a[204:207] offset:7168\n"
      "v_cndmask_b32 v228, v176, v177, s[74:75]\n"
      "v_mfma_f32_16x16x32_bf16 v[184:187], a[132:135], v[148:151], v[184:187]\n"
      "s_nop 7\n"
      "v_max3_f32 v196, v184, v185, v186\n"
      "v_max_f32 v196, v196, v187\n"
      "v_cmp_nge_f32 vcc, 0x4b800000, v196\n"
      "s_cbranch_vccnz FS8_%=\n"
      "FR8_%=:\n"
      "v_cndmask_b32 v202, v186, v187, s[74:75]\n"
      "v_cndmask_b32 v201, v184, v185, s[74:75]\n"
      "v_add_f32 v200, v200, v202\n"
      "ds_write_b32 v234, v200 offset:4096\n"
      "v_add_f32 v229, v229, v201\n"
      "s_waitcnt lgkmcnt(8)\n"
      "v_mfma_f32_16x16x32_bf16 v[32:35], a[16:19], v[144:147], v[32:35]\n"
      "v_mfma_f32_16x16x32_bf16 v[36:39], a[16:19], v[148:151], v[36:39]\n"
      "v_mfma_f32_16x16x32_bf16 v[40:43], a[20:23], v[144:147], v[40:43]\n"
      "s_waitcnt lgkmcnt(0)\n"
      "s_barrier\n"
      "v_mfma_f32_16x16x32_bf16 v[44:47], a[20:23], v[148:151], v[44:47]\n"
      "ds_read_b32 v208, v209\n"
      "ds_read_b128 v[160:163], v235 offset:0\n"
      "v_mfma_f32_16x16x32_bf16 v[48:51], a[24:27], v[144:147], v[48:51]\n"
      "ds_read_b128 v[164:167], v235 offset:1024\n"
      "ds_read_b128 v[64:67], v233 offset:0\n"
      "v_mfma_f32_16x16x32_bf16 v[52:55], a[24:27], v[148:151], v[52:55]\n"
      "ds_read_b128 v[68:71], v233 offset:1024\n"
      "ds_read_b128 v[72:75], v233 offset:2048\n"
      "ds_read_b128 v[76:79], v233 offset:3072\n"
      "v_mfma_f32_16x16x32_bf16 v[56:59], a[28:31], v[144:147], v[56:59]\n"
      "ds_read_b128 v[80:83], v233 offset:4096\n"
      "ds_read_b128 v[84:87], v233 offset:5120\n"
      "v_mfma_f32_16x16x32_bf16 v[60:63], a[28:31], v[148:151], v[60:63]\n"
      "ds_read_b128 v[88:91], v233 offset:6144\n"
      "ds_read_b128 v[92:95], v233 offset:7168\n"
      "ds_read_b32 v204, v235 offset:4096\n"
      "s_waitcnt lgkmcnt(11)\n"
      "v_cmp_ne_u32 vcc, 0, v208\n"
      "s_cbranch_vccnz SLOW_%=\n"
      "AGAIN_%=:\n"
      "s_waitcnt lgkmcnt(9)\n"
      "v_mfma_f32_16x16x32_bf16 v[32:35], a[0:3], v[160:163], v[32:35]\n"
      "v_mfma_f32_16x16x32_bf16 v[36:39], a[0:3], v[164:167], v[36:39]\n"
      "v_mfma_f32_16x16x32_bf16 v[40:43], a[4:7], v[160:163], v[40:43]\n"
      "v_mfma_f32_16x16x32_bf16 v[44:47], a[4:7], v[164:167], v[44:47]\n"
      "v_mfma_f32_16x16x32_bf16 v[48:51], a[8:11], v[160:163], v[48:51]\n"
      "v_mfma_f32_16x16x32_bf16 v[52:55], a[8:11], v[164:167], v[52:55]\n"
      "v_mfma_f32_16x16x32_bf16 v[56:59], a[12:15], v[160:163], v[56:59]\n"
      "v_mfma_f32_16x16x32_bf16 v[60:63], a[12:15], v[164:167], v[60:63]\n"
      "global_load_dwordx2 v[206:207], v239, s[46:47] offset:8 sc1\n"
      "s_waitcnt lgkmcnt(7)\n"
      "v_pk_add_f32 v[32:33], v[32:33], v[64:65]\n"
      "v_pk_add_f32 v[34:35], v[34:35], v[66:67]\n"
      "v_pk_add_f32 v[36:37], v[36:37], v[68:69]\n"
      "v_pk_add_f32 v[38:39], v[38:39], v[70:71]\n"
      "v_cvt_pk_bf16_f32 v212, v32, v33\n"
      "v_cvt_pk_bf16_f32 v213, v34, v35\n"
      "v_cvt_pk_bf16_f32 v214, v36, v37\n"
      "v_cvt_pk_bf16_f32 v215, v38, v39\n"
      "buffer_store_dwordx4 v[212:215], v241, s[24:27], s78 offen offset:0 sc1\n"
      "s_waitcnt lgkmcnt(5)\n"
      "v_pk_add_f32 v[40:41], v[40:41], v[72:73]\n"
      "v_pk_add_f32 v[42:43], v[42:43], v[74:75]\n"
      "v_pk_add_f32 v[44:45], v[44:45], v[76:77]\n"
      "v_pk_add_f32 v[46:47], v[46:47], v[78:79]\n"
      "v_cvt_pk_bf16_f32 v216, v40, v41\n"
      "v_cvt_pk_bf16_f32 v217, v42, v43\n"
      "v_cvt_pk_bf16_f32 v218, v44, v45\n"
      "v_cvt_pk_bf16_f32 v219, v46, v47\n"
      "buffer_store_dwordx4 v[216:219], v241, s[24:27], s78 offen offset:1024 sc1\n"
      "s_waitcnt lgkmcnt(3)\n"
      "v_pk_add_f32 v[48:49], v[48:49], v[80:81]\n"
      "v_pk_add_f32 v[50:51], v[50:51], v[82:83]\n"
      "v_pk_add_f32 v[52:53], v[52:53], v[84:85]\n"
      "v_pk_add_f32 v[54:55], v[54:55], v[86:87]\n"
      "v_cvt_pk_bf16_f32 v220, v48, v49\n"
      "v_cvt_pk_bf16_f32 v221, v50, v51\n"
      "v_cvt_pk_bf16_f32 v222, v52, v53\n"
      "v_cvt_pk_bf16_f32 v223, v54, v55\n"
      "buffer_store_dwordx4 v[220:223], v241, s[24:27], s78 offen offset:2048 sc1\n"
      "s_waitcnt lgkmcnt(0)\n"
      "v_pk_add_f32 v[56:57], v[56:57], v[88:89]\n"
      "v_pk_add_f32 v[58:59], v[58:59], v[90:91]\n"
      "v_pk_add_f32 v[60:61], v[60:61], v[92:93]\n"
      "v_pk_add_f32 v[62:63], v[62:63], v[94:95]\n"
      "v_cvt_pk_bf16_f32 v224, v56, v57\n"
      "v_cvt_pk_bf16_f32 v225, v58, v59\n"
      "v_cvt_pk_bf16_f32 v226, v60, v61\n"
      "v_cvt_pk_bf16_f32 v227, v62, v63\n"
      "buffer_store_dwordx4 v[224:227], v241, s[24:27], s78 offen offset:3072 sc1\n"
      "v_add_f32 v229, v229, v204\n"
      "v_cndmask_b32 v230, v228, v229, s[94:95]\n"
      "v_add_u32 v231, s40, v230\n"
      "v_add3_u32 v231, v231, v212, v213\n"
      "v_add3_u32 v231, v231, v214, v215\n"
      "v_add3_u32 v231, v231, v216, v217\n"
      "v_add3_u32 v231, v231, v218, v219\n"
      "v_add3_u32 v231, v231, v220, v221\n"
      "v_add3_u32 v231, v231, v222, v223\n"
      "v_add3_u32 v231, v231, v224, v225\n"
      "v_add3_u32 v231, v231, v226, v227\n"
      "buffer_store_dwordx2 v[230:231], v240, s[24:27], s78 offen sc1\n"
      "v_xor_b32 v244, 0x10000, v244\n"
      "v_xor_b32 v245, 0x10000, v245\n"
      "v_xor_b32 v246, 0x10000, v246\n"
      "v_xor_b32 v247, 0x10000, v247\n"
      "s_waitcnt vmcnt(7)\n"
      "s_barrier\n"
      "ds_read_b64_tr_b16 a[64:65], v244 offset:16384\n"
      "ds_read_b64_tr_b16 a[66:67], v244 offset:20480\n"
      "ds_read_b64_tr_b16 a[68:69], v245 offset:16384\n"
      "ds_read_b64_tr_b16 a[70:71], v245 offset:20480\n"
      "ds_read_b64_tr_b16 a[72:73], v246 offset:16384\n"
      "ds_read_b64_tr_b16 a[74:75], v246 offset:20480\n"
      "ds_read_b64_tr_b16 a[76:77], v247 offset:16384\n"
      "ds_read_b64_tr_b16 a[78:79], v247 offset:20480\n"
      "s_waitcnt lgkmcnt(4)\n"
      "v_mfma_f32_16x16x32_bf16 a[144:147], a[64:67], v[136:139], a[144:147]\n"
      "ds_read_b64_tr_b16 a[96:97], v244 offset:17408\n"
      "ds_read_b64_tr_b16 a[98:99], v244 offset:21504\n"
      "v_mfma_f32_16x16x32_bf16 a[148:151], a[64:67], v[140:143], a[148:151]\n"
      "ds_read_b64_tr_b16 a[100:101], v245 offset:17408\n"
      "ds_read_b64_tr_b16 a[102:103], v245 offset:21504\n"
      "v_mfma_f32_16x16x32_bf16 a[152:155], a[68:71], v[136:139], a[152:155]\n"
      "ds_read_b64_tr_b16 a[104:105], v246 offset:17408\n"
      "ds_read_b64_tr_b16 a[106:107], v246 offset:21504\n"
      "v_mfma_f32_16x16x32_bf16 a[156:159], a[68:71], v[140:143], a[156:159]\n"
      "ds_read_b64_tr_b16 a[108:109], v247 offset:17408\n"
      "ds_read_b64_tr_b16 a[110:111], v247 offset:21504\n"
      "s_waitcnt lgkmcnt(8)\n"
      "v_mfma_f32_16x16x32_bf16 a[160:163], a[72:75], v[136:139], a[160:163]\n"
      "s_sub_u32 s76, 18432, s78\n"
      "v_mfma_f32_16x16x32_bf16 a[164:167], a[72:75], v[140:143], a[164:167]\n"
      "s_mov_b32 s63, 0x40000\n"
      "v_mfma_f32_16x16x32_bf16 a[168:171], a[76:79], v[136:139], a[168:171]\n"
      "v_mov_b32 v198, 0\n"
      "v_mfma_f32_16x16x32_bf16 a[172:175], a[76:79], v[140:143], a[172:175]\n"
      "v_and_b32 v197, 0xf0, v241\n"
      "s_waitcnt lgkmcnt(0)\n"
      "v_mfma_f32_16x16x32_bf16 a[144:147], a[96:99], v[152:155], a[144:147]\n"
      "v_lshrrev_b32 v196, 3, v241\n"
      "v_mfma_f32_16x16x32_bf16 a[148:151], a[96:99], v[156:159], a[148:151]\n"
      "v_and_b32 v196, 32, v196\n"
      "v_mfma_f32_16x16x32_bf16 a[152:155], a[100:103], v[152:155], a[152:155]\n"
      "v_lshl_or_b32 v196, v197, 4, v196\n"
      "v_mfma_f32_16x16x32_bf16 a[156:159], a[100:103], v[156:159], a[156:159]\n"
      "v_lshrrev_b32 v199, 5, v241\n"
      "v_mfma_f32_16x16x32_bf16 a[160:163], a[104:107], v[152:155], a[160:163]\n"
      "v_and_or_b32 v196, v199, 16, v196\n"
      "v_mfma_f32_16x16x32_bf16 a[164:167], a[104:107], v[156:159], a[164:167]\n"
      "v_mov_b32 v191, s40\n"
      "v_mfma_f32_16x16x32_bf16 a[168:171], a[108:111], v[152:155], a[168:171]\n"
      "v_mfma_f32_16x16x32_bf16 a[172:175], a[108:111], v[156:159], a[172:175]\n"
      "s_waitcnt vmcnt(0)\n"
      "v_subrev_u32 v205, s82, v206\n"
      "v_cmp_ne_u32 vcc, s40, v207\n"
      "s_cbranch_vccnz LAG_%=\n"
      "v_cmp_lt_i32 vcc, 10, v205\n"
      "s_cbranch_vccnz LAG_%=\n"
      "READ_%=:\n"
      "buffer_load_dwordx2 v[160:161], v240, s[24:27], s76 offen sc1\n"
      "buffer_load_dwordx4 v[96:99], v241, s[24:27], s76 offen offset:0 sc1\n"
      "buffer_load_dwordx4 v[100:103], v241, s[24:27], s76 offen offset:1024 sc1\n"
      "buffer_load_dwordx4 v[104:107], v241, s[24:27], s76 offen offset:2048 sc1\n"
      "buffer_load_dwordx4 v[108:111], v241, s[24:27], s76 offen offset:3072 sc1\n"
      "v_mfma_f32_16x16x32_bf16 v[0:3], a[64:67], v[128:131], v[0:3]\n"
      "v_mfma_f32_16x16x32_bf16 v[4:7], a[64:67], v[132:135], v[4:7]\n"
      "ds_write_b128 v232, a[144:147] offset:32768\n"
      "v_mfma_f32_16x16x32_bf16 v[8:11], a[68:71], v[128:131], v[8:11]\n"
      "v_mfma_f32_16x16x32_bf16 v[12:15], a[68:71], v[132:135], v[12:15]\n"
      "ds_write_b128 v232, a[148:151] offset:33792\n"
      "v_mfma_f32_16x16x32_bf16 v[16:19], a[72:75], v[128:131], v[16:19]\n"
      "v_mfma_f32_16x16x32_bf16 v[20:23], a[72:75], v[132:135], v[20:23]\n"
      "ds_write_b128 v232, a[152:155] offset:34816\n"
      "v_mfma_f32_16x16x32_bf16 v[24:27], a[76:79], v[128:131], v[24:27]\n"
      "v_mfma_f32_16x16x32_bf16 v[28:31], a[76:79], v[132:135], v[28:31]\n"
      "ds_write_b128 v232, a[156:159] offset:35840\n"
      "v_mfma_f32_16x16x32_bf16 v[0:3], a[96:99], v[144:147], v[0:3]\n"
      "v_mfma_f32_16x16x32_bf16 v[4:7], a[96:99], v[148:151], v[4:7]\n"
      "ds_write_b128 v232, a[160:163] offset:36864\n"
      "v_mfma_f32_16x16x32_bf16 v[8:11], a[100:103], v[144:147], v[8:11]\n"
      "v_mfma_f32_16x16x32_bf16 v[12:15], a[100:103], v[148:151], v[12:15]\n"
      "ds_write_b128 v232, a[164:167] offset:37888\n"
      "v_mfma_f32_16x16x32_bf16 v[16:19], a[104:107], v[144:147], v[16:19]\n"
      "v_mfma_f32_16x16x32_bf16 v[20:23], a[104:107], v[148:151], v[20:23]\n"
      "ds_write_b128 v232, a[168:171] offset:38912\n"
      "v_mfma_f32_16x16x32_bf16 v[24:27], a[108:111], v[144:147], v[24:27]\n"
      "v_mfma_f32_16x16x32_bf16 v[28:31], a[108:111], v[148:151], v[28:31]\n"
      "ds_write_b128 v232, a[172:175] offset:39936\n"
      "s_waitcnt lgkmcnt(0)\n"
      "s_barrier\n"
      "ds_read_b128 v[64:67], v233 offset:32768\n"
      "ds_read_b128 v[68:71], v233 offset:33792\n"
      "ds_read_b128 v[72:75], v233 offset:34816\n"
      "ds_read_b128 v[76:79], v233 offset:35840\n"
      "ds_read_b128 v[80:83], v233 offset:36864\n"
      "ds_read_b128 v[84:87], v233 offset:37888\n"
      "ds_read_b128 v[88:91], v233 offset:38912\n"
      "ds_read_b128 v[92:95], v233 offset:39936\n"
      "s_waitcnt lgkmcnt(6)\n"
      "v_pk_add_f32 v[0:1], v[0:1], v[64:65]\n"
      "v_pk_add_f32 v[2:3], v[2:3], v[66:67]\n"
      "v_pk_add_f32 v[4:5], v[4:5], v[68:69]\n"
      "v_pk_add_f32 v[6:7], v[6:7], v[70:71]\n"
      "s_waitcnt lgkmcnt(4)\n"
      "v_pk_add_f32 v[8:9], v[8:9], v[72:73]\n"
      "v_pk_add_f32 v[10:11], v[10:11], v[74:75]\n"
      "v_pk_add_f32 v[12:13], v[12:13], v[76:77]\n"
      "v_pk_add_f32 v[14:15], v[14:15], v[78:79]\n"
      "s_waitcnt lgkmcnt(2)\n"
      "v_pk_add_f32 v[16:17], v[16:17], v[80:81]\n"
      "v_pk_add_f32 v[18:19], v[18:19], v[82:83]\n"
      "v_pk_add_f32 v[20:21], v[20:21], v[84:85]\n"
      "v_pk_add_f32 v[22:23], v[22:23], v[86:87]\n"
      "s_waitcnt lgkmcnt(0)\n"
      "v_pk_add_f32 v[24:25], v[24:25], v[88:89]\n"
      "v_pk_add_f32 v[26:27], v[26:27], v[90:91]\n"
      "v_pk_add_f32 v[28:29], v[28:29], v[92:93]\n"
      "v_pk_add_f32 v[30:31], v[30:31], v[94:95]\n"
      "TRY_%=:\n"
      "s_waitcnt vmcnt(4)\n"
      "v_mov_b32 v162, v160\n"
      "v_add_u32 v231, s40, v160\n"
      "s_nop 0\n"
      "v_permlane32_swap_b32 v160, v162\n"
      "v_sub_f32 v168, v160, v228\n"
      "v_min_f32 v172, 0, -v168\n"
      "v_min_f32 v168, 0, v168\n"
      "v_exp_f32 v168, v168\n"
      "v_exp_f32 v172, v172\n"
      "v_mul_f32 v163, v229, v168\n"
      "v_fmac_f32 v163, v162, v172\n"
      "v_rcp_f32 v163, v163\n"
      "s_nop 0\n"
      "v_mul_f32 v168, v168, v163\n"
      "v_mul_f32 v172, v172, v163\n"
      "v_mov_b32 v169, v168\n"
      "v_mov_b32 v173, v172\n"
      "s_nop 0\n"
      "v_permlane16_swap_b32 v168, v169\n"
      "v_permlane16_swap_b32 v172, v173\n"
      "v_pk_mul_f32 v[32:33], v[0:1], v[168:169] op_sel:[0,0] op_sel_hi:[1,0]\n"
      "v_pk_mul_f32 v[34:35], v[2:3], v[168:169] op_sel:[0,0] op_sel_hi:[1,0]\n"
      "v_pk_mul_f32 v[36:37], v[4:5], v[168:169] op_sel:[0,1] op_sel_hi:[1,1]\n"
      "v_pk_mul_f32 v[38:39], v[6:7], v[168:169] op_sel:[0,1] op_sel_hi:[1,1]\n"
      "v_pk_mul_f32 v[40:41], v[8:9], v[168:169] op_sel:[0,0] op_sel_hi:[1,0]\n"
      "v_pk_mul_f32 v[42:43], v[10:11], v[168:169] op_sel:[0,0] op_sel_hi:[1,0]\n"
      "v_pk_mul_f32 v[44:45], v[12:13], v[168:169] op_sel:[0,1] op_sel_hi:[1,1]\n"
      "v_pk_mul_f32 v[46:47], v[14:15], v[168:169] op_sel:[0,1] op_sel_hi:[1,1]\n"
      "v_pk_mul_f32 v[48:49], v[16:17], v[168:169] op_sel:[0,0] op_sel_hi:[1,0]\n"
      "v_pk_mul_f32 v[50:51], v[18:19], v[168:169] op_sel:[0,0] op_sel_hi:[1,0]\n"
      "v_pk_mul_f32 v[52:53], v[20:21], v[168:169] op_sel:[0,1] op_sel_hi:[1,1]\n"
      "v_pk_mul_f32 v[54:55], v[22:23], v[168:169] op_sel:[0,1] op_sel_hi:[1,1]\n"
      "v_pk_mul_f32 v[56:57], v[24:25], v[168:169] op_sel:[0,0] op_sel_hi:[1,0]\n"
      "v_pk_mul_f32 v[58:59], v[26:27], v[168:169] op_sel:[0,0] op_sel_hi:[1,0]\n"
      "v_pk_mul_f32 v[60:61], v[28:29], v[168:169] op_sel:[0,1] op_sel_hi:[1,1]\n"
      "v_pk_mul_f32 v[62:63], v[30:31], v[168:169] op_sel:[0,1] op_sel_hi:[1,1]\n"
      "s_waitcnt vmcnt(3)\n"
      "v_add3_u32 v231, v231, v96, v97\n"
      "v_add3_u32 v231, v231, v98, v99\n"
      "s_waitcnt vmcnt(2)\n"
      "v_add3_u32 v231, v231, v100, v101\n"
      "v_add3_u32 v231, v231, v102, v103\n"
      "s_waitcnt vmcnt(1)\n"
      "v_add3_u32 v231, v231, v104, v105\n"
      "v_add3_u32 v231, v231, v106, v107\n"
      "s_waitcnt vmcnt(0)\n"
      "v_add3_u32 v231, v231, v108, v109\n"
      "v_add3_u32 v231, v231, v110, v111\n"
      "v_cmp_ne_u32 vcc, v231, v161\n"
      "s_cbranch_vccnz RETRY_%=\n"
      "GOT_%=:\n"
      "v_lshlrev_b32 v0, 16, v96\n"
      "v_and_b32 v1, 0xffff0000, v96\n"
      "v_lshlrev_b32 v2, 16, v97\n"
      "v_and_b32 v3, 0xffff0000, v97\n"
      "v_lshlrev_b32 v8, 16, v100\n"
      "v_and_b32 v9, 0xffff0000, v100\n"
      "v_lshlrev_b32 v10, 16, v101\n"
      "v_and_b32 v11, 0xffff0000, v101\n"
      "v_pk_fma_f32 v[32:33], v[0:1], v[172:173], v[32:33] op_sel:[0,0,0] op_sel_hi:[1,0,1]\n"
      "v_pk_fma_f32 v[34:35], v[2:3], v[172:173], v[34:35] op_sel:[0,0,0] op_sel_hi:[1,0,1]\n"
      "v_pk_fma_f32 v[40:41], v[8:9], v[172:173], v[40:41] op_sel:[0,0,0] op_sel_hi:[1,0,1]\n"
      "v_pk_fma_f32 v[42:43], v[10:11], v[172:173], v[42:43] op_sel:[0,0,0] op_sel_hi:[1,0,1]\n"
      "v_cvt_pk_bf16_f32 v112, v32, v33\n"
      "v_cvt_pk_bf16_f32 v114, v40, v41\n"
      "v_cvt_pk_bf16_f32 v113, v34, v35\n"
      "v_cvt_pk_bf16_f32 v115, v42, v43\n"
      "v_permlane16_swap_b32 v112, v114\n"
      "s_nop 0\n"
      "v_permlane16_swap_b32 v113, v115\n"
      "global_store_dwordx4 v196, v[112:115], s[30:31] offset:-4096 sc1 nt\n"
      "v_lshlrev_b32 v16, 16, v104\n"
      "v_and_b32 v17, 0xffff0000, v104\n"
      "v_lshlrev_b32 v18, 16, v105\n"
      "v_and_b32 v19, 0xffff0000, v105\n"
      "v_lshlrev_b32 v24, 16, v108\n"
      "v_and_b32 v25, 0xffff0000, v108\n"
      "v_lshlrev_b32 v26, 16, v109\n"
      "v_and_b32 v27, 0xffff0000, v109\n"
      "v_pk_fma_f32 v[48:49], v[16:17], v[172:173], v[48:49] op_sel:[0,0,0] op_sel_hi:[1,0,1]\n"
      "v_pk_fma_f32 v[50:51], v[18:19], v[172:173], v[50:51] op_sel:[0,0,0] op_sel_hi:[1,0,1]\n"
      "v_pk_fma_f32 v[56:57], v[24:25], v[172:173], v[56:57] op_sel:[0,0,0] op_sel_hi:[1,0,1]\n"
      "v_pk_fma_f32 v[58:59], v[26:27], v[172:173], v[58:59] op_sel:[0,0,0] op_sel_hi:[1,0,1]\n"
      "v_cvt_pk_bf16_f32 v116, v48, v49\n"
      "v_cvt_pk_bf16_f32 v118, v56, v57\n"
      "v_cvt_pk_bf16_f32 v117, v50, v51\n"
      "v_cvt_pk_bf16_f32 v119, v58, v59\n"
      "v_permlane16_swap_b32 v116, v118\n"
      "s_nop 0\n"
      "v_permlane16_swap_b32 v117, v119\n"
      "global_store_dwordx4 v196, v[116:119], s[30:31] offset:-4032 sc1 nt\n"
      "v_lshlrev_b32 v4, 16, v98\n"
      "v_and_b32 v5, 0xffff0000, v98\n"
      "v_lshlrev_b32 v6, 16, v99\n"
      "v_and_b32 v7, 0xffff0000, v99\n"
      "v_lshlrev_b32 v12, 16, v102\n"
      "v_and_b32 v13, 0xffff0000, v102\n"
      "v_lshlrev_b32 v14, 16, v103\n"
      "v_and_b32 v15, 0xffff0000, v103\n"
      "v_pk_fma_f32 v[36:37], v[4:5], v[172:173], v[36:37] op_sel:[0,1,0] op_sel_hi:[1,1,1]\n"
      "v_pk_fma_f32 v[38:39], v[6:7], v[172:173], v[38:39] op_sel:[0,1,0] op_sel_hi:[1,1,1]\n"
      "v_pk_fma_f32 v[44:45], v[12:13], v[172:173], v[44:45] op_sel:[0,1,0] op_sel_hi:[1,1,1]\n"
      "v_pk_fma_f32 v[46:47], v[14:15], v[172:173], v[46:47] op_sel:[0,1,0] op_sel_hi:[1,1,1]\n"
      "v_cvt_pk_bf16_f32 v120, v36, v37\n"
      "v_cvt_pk_bf16_f32 v122, v44, v45\n"
      "v_cvt_pk_bf16_f32 v121, v38, v39\n"
      "v_cvt_pk_bf16_f32 v123, v46, v47\n"
      "v_permlane16_swap_b32 v120, v122\n"
      "s_nop 0\n"
      "v_permlane16_swap_b32 v121, v123\n"
      "global_store_dwordx4 v196, v[120:123], s[30:31] offset:0 sc1 nt\n"
      "v_lshlrev_b32 v20, 16, v106\n"
      "v_and_b32 v21, 0xffff0000, v106\n"
      "v_lshlrev_b32 v22, 16, v107\n"
      "v_and_b32 v23, 0xffff0000, v107\n"
      "v_lshlrev_b32 v28, 16, v110\n"
      "v_and_b32 v29, 0xffff0000, v110\n"
      "v_lshlrev_b32 v30, 16, v111\n"
      "v_and_b32 v31, 0xffff0000, v111\n"
      "v_pk_fma_f32 v[52:53], v[20:21], v[172:173], v[52:53] op_sel:[0,1,0] op_sel_hi:[1,1,1]\n"
      "v_pk_fma_f32 v[54:55], v[22:23], v[172:173], v[54:55] op_sel:[0,1,0] op_sel_hi:[1,1,1]\n"
      "v_pk_fma_f32 v[60:61], v[28:29], v[172:173], v[60:61] op_sel:[0,1,0] op_sel_hi:[1,1,1]\n"
      "v_pk_fma_f32 v[62:63], v[30:31], v[172:173], v[62:63] op_sel:[0,1,0] op_sel_hi:[1,1,1]\n"
      "v_cvt_pk_bf16_f32 v124, v52, v53\n"
      "v_cvt_pk_bf16_f32 v126, v60, v61\n"
      "v_cvt_pk_bf16_f32 v125, v54, v55\n"
      "v_cvt_pk_bf16_f32 v127, v62, v63\n"
      "v_permlane16_swap_b32 v124, v126\n"
      "s_nop 0\n"
      "v_permlane16_swap_b32 v125, v127\n"
      "global_store_dwordx4 v196, v[124:127], s[30:31] offset:64 sc1 nt\n"
      "s_mov_b64 exec, 1\n"
      "global_store_dword v198, v191, s[46:47] sc1\n"
      "s_mov_b64 exec, -1\n"
      "ENDE_%=:\n"
      ".subsection 1\n"
      "MH_%=:\n"
      "s_cmp_lg_u32 s54, 0\n"
      "s_waitcnt vmcnt(12) lgkmcnt(0)\n"
      "s_cbranch_scc1 W6_%=\n"
      "s_waitcnt vmcnt(8)\n"
      "W6_%=:\n"
      "s_barrier\n"
      "v_mfma_f32_16x16x32_bf16 v[24:27], a[108:111], v[144:147], v[24:27]\n"
      "s_xor_b64 s[56:57], s[56:57], s[32:33]\n"
      "ds_read_b128 a[64:67], v242 offset:0\n"
      "ds_read_b128 a[72:75], v243 offset:0\n"
      "v_mfma_f32_16x16x32_bf16 v[32:35], a[112:115], v[144:147], v[32:35]\n"
      "s_add_u32 s61, s61, 1\n"
      "ds_read_b128 a[80:83], v242 offset:1024\n"
      "ds_read_b128 a[88:91], v243 offset:1024\n"
      "v_mfma_f32_16x16x32_bf16 v[36:39], a[112:115], v[148:151], v[36:39]\n"
      "s_add_u32 s62, s62, 0x4000\n"
      "v_exp_f32 v189, v89\n"
      "v_mfma_f32_16x16x32_bf16 a[176:179], a[112:115], v[152:155], a[176:179]\n"
      "s_and_b32 s62, s62, 0x1ffff\n"
      "v_exp_f32 v190, v90\n"
      "v_mfma_f32_16x16x32_bf16 a[180:183], a[112:115], v[156:159], a[180:183]\n"
      "v_readlane_b32 s60, v219, s61\n"
      "v_exp_f32 v191, v91\n"
      "v_mfma_f32_16x16x32_bf16 a[188:191], a[116:119], v[156:159], a[188:191]\n"
      "s_add_u32 s66, s66, s86\n"
      "v_cvt_pk_bf16_f32 v140, v188, v189\n"
      "v_mfma_f32_16x16x32_bf16 a[184:187], a[116:119], v[152:155], a[184:187]\n"
      "v_exp_f32 v192, v92\n"
      "v_mfma_f32_16x16x32_bf16 v[44:47], a[116:119], v[148:151], v[44:47]\n"
      "s_add_u32 s82, s82, s86\n"
      "v_exp_f32 v193, v93\n"
      "v_mfma_f32_16x16x32_bf16 v[40:43], a[116:119], v[144:147], v[40:43]\n"
      "s_add_u32 s65, s65, s86\n"
      "v_cvt_pk_bf16_f32 v141, v190, v191\n"
      "v_cvt_pk_bf16_f32 v142, v192, v193\n"
      "v_mfma_f32_16x16x32_bf16 v[48:51], a[120:123], v[144:147], v[48:51]\n"
      "s_max_u32 s88, s66, s65\n"
      "v_exp_f32 v194, v94\n"
      "v_mfma_f32_16x16x32_bf16 v[52:55], a[120:123], v[148:151], v[52:55]\n"
      "v_exp_f32 v195, v95\n"
      "s_waitcnt lgkmcnt(0)\n"
      "v_mfma_f32_16x16x32_bf16 a[192:195], a[120:123], v[152:155], a[192:195]\n"
      "v_cvt_pk_bf16_f32 v143, v194, v195\n"
      "v_xor_b32 v244, 0x10000, v244\n"
      "v_mfma_f32_16x16x32_bf16 a[196:199], a[120:123], v[156:159], a[196:199]\n"
      "ds_read_b128 a[68:71], v242 offset:4096\n"
      "ds_read_b128 a[76:79], v243 offset:4096\n"
      "v_mfma_f32_16x16x32_bf16 v[184:187], a[140:143], v[140:143], v[184:187]\n"
      "s_mov_b32 m0, s62\n"
      "ds_read_b128 a[84:87], v242 offset:5120\n"
      "ds_read_b128 a[92:95], v243 offset:5120\n"
      "v_mfma_f32_16x16x32_bf16 a[204:207], a[124:127], v[156:159], a[204:207]\n"
      "buffer_load_dwordx4 v228, s[56:59], s60 offen nt lds\n"
      "v_xor_b32 v245, 0x10000, v245\n"
      "v_xor_b32 v246, 0x10000, v246\n"
      "v_mfma_f32_16x16x32_bf16 a[200:203], a[124:127], v[152:155], a[200:203]\n"
      "buffer_load_dwordx4 v229, s[56:59], s60 offen offset:1024 nt lds\n"
      "v_xor_b32 v247, 0x10000, v247\n"
      "v_max3_f32 v196, v184, v185, v186\n"
      "v_mfma_f32_16x16x32_bf16 v[60:63], a[124:127], v[148:151], v[60:63]\n"
      "buffer_load_dwordx4 v230, s[56:59], s60 offen offset:2048 nt lds\n"
      "v_max_f32 v196, v196, v187\n"
      "v_cmp_nge_f32 vcc, 0x4b800000, v196\n"
      "v_mfma_f32_16x16x32_bf16 v[56:59], a[124:127], v[144:147], v[56:59]\n"
      "buffer_load_dwordx4 v231, s[56:59], s60 offen offset:3072 nt lds\n"
      "s_cbranch_vccnz FS11_%=\n"
      "FR11_%=:\n"
      "v_mfma_f32_16x16x32_bf16 v[64:67], a[64:67], a[0:3], v[160:163]\n"
      "v_exp_f32 v188, v96\n"
      "v_mfma_f32_16x16x32_bf16 v[64:67], a[72:75], a[16:19], v[64:67]\n"
      "v_add_f32 v180, v180, v184\n"
      "v_add_f32 v181, v181, v185\n"
      "v_mfma_f32_16x16x32_bf16 v[64:67], a[80:83], a[32:35], v[64:67]\n"
      "v_exp_f32 v189, v97\n"
      "v_mfma_f32_16x16x32_bf16 v[64:67], a[88:91], a[48:51], v[64:67]\n"
      "v_add_f32 v182, v182, v186\n"
      "v_add_f32 v183, v183, v187\n"
      "v_mfma_f32_16x16x32_bf16 v[72:75], a[64:67], a[4:7], v[164:167]\n"
      "v_exp_f32 v190, v98\n"
      "v_mfma_f32_16x16x32_bf16 v[72:75], a[72:75], a[20:23], v[72:75]\n"
      "v_exp_f32 v191, v99\n"
      "v_mfma_f32_16x16x32_bf16 v[72:75], a[80:83], a[36:39], v[72:75]\n"
      "v_cvt_pk_bf16_f32 v144, v188, v189\n"
      "v_cvt_pk_bf16_f32 v145, v190, v191\n"
      "v_mfma_f32_16x16x32_bf16 v[72:75], a[88:91], a[52:55], v[72:75]\n"
      "v_exp_f32 v192, v100\n"
      "v_mfma_f32_16x16x32_bf16 v[80:83], a[64:67], a[8:11], v[168:171]\n"
      "s_add_u32 s39, s39, s39\n"
      "v_exp_f32 v193, v101\n"
      "v_mfma_f32_16x16x32_bf16 v[80:83], a[72:75], a[24:27], v[80:83]\n"
      "s_memrealtime s[68:69]\n"
      "v_exp_f32 v194, v102\n"
      "v_mfma_f32_16x16x32_bf16 v[80:83], a[80:83], a[40:43], v[80:83]\n"
      "v_exp_f32 v195, v103\n"
      "v_mfma_f32_16x16x32_bf16 v[80:83], a[88:91], a[56:59], v[80:83]\n"
      "v_cvt_pk_bf16_f32 v146, v192, v193\n"
      "v_cvt_pk_bf16_f32 v147, v194, v195\n"
      "v_mfma_f32_16x16x32_bf16 v[88:91], a[64:67], a[12:15], v[172:175]\n"
      "s_nop 0\n"
      "v_mfma_f32_16x16x32_bf16 v[184:187], a[128:131], v[144:147], 0\n"
      "s_nop 0\n"
      "v_mfma_f32_16x16x32_bf16 v[88:91], a[72:75], a[28:31], v[88:91]\n"
      "s_nop 0\n"
      "v_mfma_f32_16x16x32_bf16 v[88:91], a[80:83], a[44:47], v[88:91]\n"
      "s_nop 0\n"
      "s_cbranch_scc1 SP1_%=\n"
      "s_branch HD1_%=\n"
      "HOOK_%=:\n"
      "buffer_load_dword v250, v251, s[20:23], 0 offen sc1\n"
      "s_swappc_b64 s[84:85], s[84:85]\n"
      "s_waitcnt vmcnt(12)\n"
      "v_subrev_u32 v250, s71, v250\n"
      "v_min_u32 v250, 0x258, v250\n"
      "s_nop 1\n"
      "v_add_u32_dpp v250, v250, v250 quad_perm:[1,0,3,2] row_mask:0xf bank_mask:0xf\n"
      "s_nop 1\n"
      "v_add_u32_dpp v250, v250, v250 quad_perm:[2,3,0,1] row_mask:0xf bank_mask:0xf\n"
      "s_nop 1\n"
      "v_add_u32_dpp v250, v250, v250 row_half_mirror row_mask:0xf bank_mask:0xf\n"
      "s_nop 0\n"
      "v_readfirstlane_b32 s67, v250\n"
      "s_sub_u32 s66, s66, s81\n"
      "s_lshr_b32 s67, s67, 3\n"
      "s_add_u32 s66, s66, s67\n"
      "s_max_u32 s88, s66, s65\n"
      "s_mov_b32 s86, 57\n"
      "s_mov_b32 s77, 171\n"
      "v_bfe_u32 v251, v241, 2, 7\n"
      "v_add_u32 v251, s79, v251\n"
      "buffer_load_dword v250, v251, s[20:23], 0 offen sc1\n"
      "s_swappc_b64 s[84:85], s[84:85]\n"
      "s_mov_b32 s89, 0\n"
      "s_swappc_b64 s[84:85], s[84:85]\n"
      "s_mov_b32 s86, 55\n"
      "s_mov_b32 s77, 165\n"
      "s_swappc_b64 s[84:85], s[84:85]\n"
      "s_mov_b32 s86, 54\n"
      "s_mov_b32 s77, 162\n"
      "s_swappc_b64 s[84:85], s[84:85]\n"
      "s_cmp_lt_u32 s89, 1\n"
      "s_cselect_b32 s67, 2048, s72\n"
      "s_mov_b32 s89, 0\n"
      "s_cmp_lt_u32 s67, 2048\n"
      "s_cbranch_scc0 NV0_%=\n"
      "v_and_b32 v216, 0x1f0, v241\n"
      "v_lshlrev_b32 v216, 4, v216\n"
      "v_mov_b32 v215, s82\n"
      "s_lshr_b32 s67, s67, 4\n"
      "s_add_u32 s67, s67, 8192\n"
      "buffer_store_dword v215, v216, s[20:23], s67 offen sc1\n"
      "NV0_%=:\n"
      "s_swappc_b64 s[84:85], s[84:85]\n"
      "buffer_load_dword v250, v251, s[20:23], 0 offen sc1\n"
      "s_swappc_b64 s[84:85], s[84:85]\n"
      "s_waitcnt vmcnt(12)\n"
      "v_sub_u32 v250, s82, v250\n"
      "v_cmp_gt_u32 vcc, 1600, v250\n"
      "s_movk_i32 s76, 36\n"
      "s_nop 2\n"
      "s_bcnt1_i32_b32 s67, vcc_lo\n"
      "s_cmp_eq_u32 s70, 60\n"
      "s_cselect_b32 s67, 0, s67\n"
      "s_cmp_ge_u32 s67, 10\n"
      "s_cbranch_scc1 SM0_%=\n"
      "s_swappc_b64 s[84:85], s[84:85]\n"
      "s_mov_b32 s86, 53\n"
      "s_mov_b32 s77, 159\n"
      "s_min_i32 s70, s70, 150\n"
      "s_swappc_b64 s[84:85], s[84:85]\n"
      "s_cmp_lt_u32 s89, 4\n"
      "s_cselect_b32 s67, 2048, s72\n"
      "s_mov_b32 s89, 0\n"
      "s_cmp_lt_u32 s67, 2048\n"
      "s_cbranch_scc0 NV1_%=\n"
      "v_and_b32 v216, 0x1f0, v241\n"
      "v_lshlrev_b32 v216, 4, v216\n"
      "v_mov_b32 v215, s82\n"
      "s_lshr_b32 s67, s67, 4\n"
      "s_add_u32 s67, s67, 8192\n"
      "buffer_store_dword v215, v216, s[20:23], s67 offen sc1\n"
      "NV1_%=:\n"
      "s_swappc_b64 s[84:85], s[84:85]\n"
      "buffer_load_dword v250, v251, s[20:23], 0 offen sc1\n"
      "s_swappc_b64 s[84:85], s[84:85]\n"
      "s_waitcnt vmcnt(12)\n"
      "v_sub_u32 v250, s82, v250\n"
      "v_cmp_gt_u32 vcc, 1600, v250\n"
      "s_movk_i32 s76, 61\n"
      "s_nop 2\n"
      "s_bcnt1_i32_b32 s67, vcc_lo\n"
      "s_cmp_eq_u32 s70, 60\n"
      "s_cselect_b32 s67, 0, s67\n"
      "s_cmp_ge_u32 s67, 10\n"
      "s_cbranch_scc1 SM1_%=\n"
      "s_swappc_b64 s[84:85], s[84:85]\n"
      "s_getpc_b64 s[74:75]\n"
      "PF_%=:\n"
      "s_sub_u32 s74, s74, PF_%=-END_%=\n"
      "s_subb_u32 s75, s75, 0\n"
      "s_lshl_b32 s76, %[blk], 7\n"
      "s_and_b32 s76, s76, 0x1c00\n"
      "s_min_u32 s76, s76, ENDE_%=-END_%=-1024\n"
      "v_add_u32 v200, s76, v241\n"
      "global_load_dwordx4 v[200:203], v200, s[74:75]\n"
      "v_mov_b32 v212, 0\n"
      "global_load_dword v213, v212, s[30:31] nt\n"
      "buffer_load_dword v214, off, s[24:27], s78 nt\n"
      "s_swappc_b64 s[84:85], s[84:85]\n"
      "SM1_%=:\n"
      "s_add_u32 s66, s66, s76\n"
      "s_max_u32 s88, s66, s65\n"
      "s_min_i32 s70, s70, 150\n"
      "s_branch SM2_%=\n"
      "SM0_%=:\n"
      "s_or_b32 s39, s39, 0xc0000000\n"
      "s_mov_b32 s52, 0xaaaaaaaa\n"
      "s_mov_b32 s53, 0xaaaaaaaa\n"
      "s_add_u32 s66, s66, s76\n"
      "s_max_u32 s88, s66, s65\n"
      "s_min_i32 s70, s70, 150\n"
      "s_mov_b32 s86, 59\n"
      "s_mov_b32 s77, 177\n"
      "s_swappc_b64 s[84:85], s[84:85]\n"
      "s_mov_b32 s86, 57\n"
      "s_mov_b32 s77, 171\n"
      "s_swappc_b64 s[84:85], s[84:85]\n"
      "SM2_%=:\n"
      "s_mov_b32 s86, 55\n"
      "s_mov_b32 s77, 165\n"
      "s_mov_b32 s52, 0x0\n"
      "s_mov_b32 s53, 0x80000\n"
      "s_swappc_b64 s[84:85], s[84:85]\n"
      "s_getpc_b64 s[74:75]\n"
      "PG_%=:\n"
      "s_sub_u32 s74, s74, PG_%=-END_%=\n"
      "s_subb_u32 s75, s75, 0\n"
      "s_lshl_b32 s76, %[blk], 7\n"
      "s_and_b32 s76, s76, 0x1c00\n"
      "s_min_u32 s76, s76, ENDE_%=-END_%=-1024\n"
      "v_add_u32 v200, s76, v241\n"
      "global_load_dwordx4 v[200:203], v200, s[74:75]\n"
      "v_mov_b32 v212, 0\n"
      "global_load_dword v213, v212, s[30:31] nt\n"
      "buffer_load_dword v214, off, s[24:27], s78 nt\n"
      "s_swappc_b64 s[84:85], s[84:85]\n"
      "GW0_%=:\n"
      "s_memrealtime s[68:69]\n"
      "s_waitcnt lgkmcnt(0)\n"
      "s_sub_u32 s67, s68, s88\n"
      "s_cmp_ge_i32 s67, 0\n"
      "s_cbranch_scc1 GX0_%=\n"
      "s_branch GW0_%=\n"
      "FS1_%=:\n"
      "s_call_b64 s[96:97], FB1_%=\n"
      "s_branch FR1_%=\n"
      "FS2_%=:\n"
      "s_call_b64 s[96:97], FB0_%=\n"
      "s_branch FR2_%=\n"
      "FS3_%=:\n"
      "s_call_b64 s[96:97], FB0_%=\n"
      "s_branch FR3_%=\n"
      "GW4_%=:\n"
      "s_memrealtime s[68:69]\n"
      "s_waitcnt lgkmcnt(0)\n"
      "s_sub_u32 s67, s68, s88\n"
      "s_cmp_ge_i32 s67, 0\n"
      "s_cbranch_scc1 GX4_%=\n"
      "s_branch GW4_%=\n"
      "GW5_%=:\n"
      "s_memrealtime s[68:69]\n"
      "s_waitcnt lgkmcnt(0)\n"
      "s_sub_u32 s67, s68, s88\n"
      "s_cmp_ge_i32 s67, 0\n"
      "s_cbranch_scc1 GX5_%=\n"
      "s_branch GW5_%=\n"
      "GW6_%=:\n"
      "s_memrealtime s[68:69]\n"
      "s_waitcnt lgkmcnt(0)\n"
      "s_sub_u32 s67, s68, s88\n"
      "s_cmp_ge_i32 s67, 0\n"
      "s_cbranch_scc1 GX6_%=\n"
      "s_branch GW6_%=\n"
      "GW7_%=:\n"
      "s_memrealtime s[68:69]\n"
      "s_waitcnt lgkmcnt(0)\n"
      "s_sub_u32 s67, s68, s88\n"
      "s_cmp_ge_i32 s67, 0\n"
      "s_cbranch_scc1 GX7_%=\n"
      "s_branch GW7_%=\n"
      "FS8_%=:\n"
      "s_call_b64 s[96:97], FB1_%=\n"
      "s_branch FR8_%=\n"
      "LAG_%=:\n"
      "s_memrealtime s[68:69]\n"
      "s_waitcnt lgkmcnt(0)\n"
      "s_mov_b32 s88, s68\n"
      "LG1_%=:\n"
      "v_cmp_ne_u32 vcc, s40, v207\n"
      "s_cbranch_vccz LG2_%=\n"
      "s_sub_u32 s63, s63, 1\n"
      "s_cmp_eq_u32 s63, 0\n"
      "s_cbranch_scc1 READ_%=\n"
      "s_sleep 4\n"
      "global_load_dwordx2 v[206:207], v239, s[46:47] offset:8 sc1\n"
      "s_waitcnt vmcnt(0)\n"
      "s_branch LG1_%=\n"
      "LG2_%=:\n"
      "v_readfirstlane_b32 s67, v206\n"
      "s_sub_u32 s88, s88, s82\n"
      "s_min_u32 s88, s88, 400\n"
      "s_add_u32 s88, s88, s67\n"
      "LG3_%=:\n"
      "s_memrealtime s[68:69]\n"
      "s_waitcnt lgkmcnt(0)\n"
      "s_sub_u32 s67, s88, s68\n"
      "s_sub_u32 s67, s67, 11\n"
      "s_cmp_lt_u32 s67, 400\n"
      "s_cbranch_scc1 LG3_%=\n"
      "s_branch READ_%=\n"
      "RETRY_%=:\n"
      "s_sub_u32 s63, s63, 1\n"
      "s_cbranch_scc1 GOT_%=\n"
      "buffer_load_dwordx2 v[160:161], v240, s[24:27], s76 offen sc1\n"
      "buffer_load_dwordx4 v[96:99], v241, s[24:27], s76 offen offset:0 sc1\n"
      "buffer_load_dwordx4 v[100:103], v241, s[24:27], s76 offen offset:1024 sc1\n"
      "buffer_load_dwordx4 v[104:107], v241, s[24:27], s76 offen offset:2048 sc1\n"
      "buffer_load_dwordx4 v[108:111], v241, s[24:27], s76 offen offset:3072 sc1\n"
      "s_branch TRY_%=\n"
      "FS11_%=:\n"
      "s_call_b64 s[96:97], FB0_%=\n"
      "s_branch FR11_%=\n"
      "COLD_%=:\n"
      "FB0_%=:\n"
      "v_mov_b32 v196, 139248\n"
      "s_nop 7\n"
      "ds_write_b32 v196, v196\n"
      "v_max3_f32 v188, v64, v65, 0\n"
      "v_max3_f32 v189, v72, v73, 0\n"
      "v_max3_f32 v190, v80, v81, 0\n"
      "v_max3_f32 v191, v88, v89, 0\n"
      "v_max3_f32 v188, v188, v66, v67\n"
      "v_max3_f32 v189, v189, v74, v75\n"
      "v_max3_f32 v190, v190, v82, v83\n"
      "v_max3_f32 v191, v191, v90, v91\n"
      "v_max3_f32 v188, v188, v68, v69\n"
      "v_max3_f32 v189, v189, v76, v77\n"
      "v_max3_f32 v190, v190, v84, v85\n"
      "v_max3_f32 v191, v191, v92, v93\n"
      "v_max3_f32 v188, v188, v70, v71\n"
      "v_max3_f32 v189, v189, v78, v79\n"
      "v_max3_f32 v190, v190, v86, v87\n"
      "v_max3_f32 v191, v191, v94, v95\n"
      "v_mov_b32 v192, v188\n"
      "v_mov_b32 v193, v189\n"
      "v_mov_b32 v194, v190\n"
      "v_mov_b32 v195, v191\n"
      "v_permlane16_swap_b32 v188, v192\n"
      "v_permlane16_swap_b32 v189, v193\n"
      "v_permlane16_swap_b32 v190, v194\n"
      "v_permlane16_swap_b32 v191, v195\n"
      "v_max_f32 v188, v188, v192\n"
      "v_max_f32 v189, v189, v193\n"
      "v_max_f32 v190, v190, v194\n"
      "v_max_f32 v191, v191, v195\n"
      "v_mov_b32 v192, v188\n"
      "v_mov_b32 v193, v189\n"
      "v_mov_b32 v194, v190\n"
      "v_mov_b32 v195, v191\n"
      "v_permlane32_swap_b32 v188, v192\n"
      "v_permlane32_swap_b32 v189, v193\n"
      "v_permlane32_swap_b32 v190, v194\n"
      "v_permlane32_swap_b32 v191, v195\n"
      "v_max_f32 v188, v188, v192\n"
      "v_max_f32 v189, v189, v193\n"
      "v_max_f32 v190, v190, v194\n"
      "v_max_f32 v191, v191, v195\n"
      "RS0_%=:\n"
      "v_exp_f32 v192, -v188\n"
      "v_exp_f32 v193, -v189\n"
      "v_exp_f32 v194, -v190\n"
      "v_exp_f32 v195, -v191\n"
      "v_sub_f32 v176, v176, v188\n"
      "v_sub_f32 v177, v177, v189\n"
      "v_sub_f32 v178, v178, v190\n"
      "v_sub_f32 v179, v179, v191\n"
      "v_mov_b32 v160, v176\n"
      "v_mov_b32 v161, v176\n"
      "v_mov_b32 v162, v176\n"
      "v_mov_b32 v163, v176\n"
      "v_mov_b32 v164, v177\n"
      "v_mov_b32 v165, v177\n"
      "v_mov_b32 v166, v177\n"
      "v_mov_b32 v167, v177\n"
      "v_mov_b32 v168, v178\n"
      "v_mov_b32 v169, v178\n"
      "v_mov_b32 v170, v178\n"
      "v_mov_b32 v171, v178\n"
      "v_mov_b32 v172, v179\n"
      "v_mov_b32 v173, v179\n"
      "v_mov_b32 v174, v179\n"
      "v_mov_b32 v175, v179\n"
      "v_sub_f32 v64, v64, v188\n"
      "v_sub_f32 v65, v65, v188\n"
      "v_sub_f32 v66, v66, v188\n"
      "v_sub_f32 v67, v67, v188\n"
      "v_sub_f32 v68, v68, v188\n"
      "v_sub_f32 v69, v69, v188\n"
      "v_sub_f32 v70, v70, v188\n"
      "v_sub_f32 v71, v71, v188\n"
      "v_sub_f32 v72, v72, v189\n"
      "v_sub_f32 v73, v73, v189\n"
      "v_sub_f32 v74, v74, v189\n"
      "v_sub_f32 v75, v75, v189\n"
      "v_sub_f32 v76, v76, v189\n"
      "v_sub_f32 v77, v77, v189\n"
      "v_sub_f32 v78, v78, v189\n"
      "v_sub_f32 v79, v79, v189\n"
      "v_sub_f32 v80, v80, v190\n"
      "v_sub_f32 v81, v81, v190\n"
      "v_sub_f32 v82, v82, v190\n"
      "v_sub_f32 v83, v83, v190\n"
      "v_sub_f32 v84, v84, v190\n"
      "v_sub_f32 v85, v85, v190\n"
      "v_sub_f32 v86, v86, v190\n"
      "v_sub_f32 v87, v87, v190\n"
      "v_sub_f32 v88, v88, v191\n"
      "v_sub_f32 v89, v89, v191\n"
      "v_sub_f32 v90, v90, v191\n"
      "v_sub_f32 v91, v91, v191\n"
      "v_sub_f32 v92, v92, v191\n"
      "v_sub_f32 v93, v93, v191\n"
      "v_sub_f32 v94, v94, v191\n"
      "v_sub_f32 v95, v95, v191\n"
      "v_sub_f32 v96, v96, v188\n"
      "v_sub_f32 v97, v97, v188\n"
      "v_sub_f32 v98, v98, v188\n"
      "v_sub_f32 v99, v99, v188\n"
      "v_sub_f32 v100, v100, v188\n"
      "v_sub_f32 v101, v101, v188\n"
      "v_sub_f32 v102, v102, v188\n"
      "v_sub_f32 v103, v103, v188\n"
      "v_sub_f32 v104, v104, v189\n"
      "v_sub_f32 v105, v105, v189\n"
      "v_sub_f32 v106, v106, v189\n"
      "v_sub_f32 v107, v107, v189\n"
      "v_sub_f32 v108, v108, v189\n"
      "v_sub_f32 v109, v109, v189\n"
      "v_sub_f32 v110, v110, v189\n"
      "v_sub_f32 v111, v111, v189\n"
      "v_sub_f32 v112, v112, v190\n"
      "v_sub_f32 v113, v113, v190\n"
      "v_sub_f32 v114, v114, v190\n"
      "v_sub_f32 v115, v115, v190\n"
      "v_sub_f32 v116, v116, v190\n"
      "v_sub_f32 v117, v117, v190\n"
      "v_sub_f32 v118, v118, v190\n"
      "v_sub_f32 v119, v119, v190\n"
      "v_sub_f32 v120, v120, v191\n"
      "v_sub_f32 v121, v121, v191\n"
      "v_sub_f32 v122, v122, v191\n"
      "v_sub_f32 v123, v123, v191\n"
      "v_sub_f32 v124, v124, v191\n"
      "v_sub_f32 v125, v125, v191\n"
      "v_sub_f32 v126, v126, v191\n"
      "v_sub_f32 v127, v127, v191\n"
      "v_mul_f32 v0, v0, v192\n"
      "v_mul_f32 v1, v1, v192\n"
      "v_mul_f32 v2, v2, v192\n"
      "v_mul_f32 v3, v3, v192\n"
      "v_mul_f32 v4, v4, v193\n"
      "v_mul_f32 v5, v5, v193\n"
      "v_mul_f32 v6, v6, v193\n"
      "v_mul_f32 v7, v7, v193\n"
      "v_mul_f32 v8, v8, v192\n"
      "v_mul_f32 v9, v9, v192\n"
      "v_mul_f32 v10, v10, v192\n"
      "v_mul_f32 v11, v11, v192\n"
      "v_mul_f32 v12, v12, v193\n"
      "v_mul_f32 v13, v13, v193\n"
      "v_mul_f32 v14, v14, v193\n"
      "v_mul_f32 v15, v15, v193\n"
      "v_mul_f32 v16, v16, v192\n"
      "v_mul_f32 v17, v17, v192\n"
      "v_mul_f32 v18, v18, v192\n"
      "v_mul_f32 v19, v19, v192\n"
      "v_mul_f32 v20, v20, v193\n"
      "v_mul_f32 v21, v21, v193\n"
      "v_mul_f32 v22, v22, v193\n"
      "v_mul_f32 v23, v23, v193\n"
      "v_mul_f32 v24, v24, v192\n"
      "v_mul_f32 v25, v25, v192\n"
      "v_mul_f32 v26, v26, v192\n"
      "v_mul_f32 v27, v27, v192\n"
      "v_mul_f32 v28, v28, v193\n"
      "v_mul_f32 v29, v29, v193\n"
      "v_mul_f32 v30, v30, v193\n"
      "v_mul_f32 v31, v31, v193\n"
      "v_mul_f32 v32, v32, v192\n"
      "v_mul_f32 v33, v33, v192\n"
      "v_mul_f32 v34, v34, v192\n"
      "v_mul_f32 v35, v35, v192\n"
      "v_mul_f32 v36, v36, v193\n"
      "v_mul_f32 v37, v37, v193\n"
      "v_mul_f32 v38, v38, v193\n"
      "v_mul_f32 v39, v39, v193\n"
      "v_mul_f32 v40, v40, v192\n"
      "v_mul_f32 v41, v41, v192\n"
      "v_mul_f32 v42, v42, v192\n"
      "v_mul_f32 v43, v43, v192\n"
      "v_mul_f32 v44, v44, v193\n"
      "v_mul_f32 v45, v45, v193\n"
      "v_mul_f32 v46, v46, v193\n"
      "v_mul_f32 v47, v47, v193\n"
      "v_mul_f32 v48, v48, v192\n"
      "v_mul_f32 v49, v49, v192\n"
      "v_mul_f32 v50, v50, v192\n"
      "v_mul_f32 v51, v51, v192\n"
      "v_mul_f32 v52, v52, v193\n"
      "v_mul_f32 v53, v53, v193\n"
      "v_mul_f32 v54, v54, v193\n"
      "v_mul_f32 v55, v55, v193\n"
      "v_mul_f32 v56, v56, v192\n"
      "v_mul_f32 v57, v57, v192\n"
      "v_mul_f32 v58, v58, v192\n"
      "v_mul_f32 v59, v59, v192\n"
      "v_mul_f32 v60, v60, v193\n"
      "v_mul_f32 v61, v61, v193\n"
      "v_mul_f32 v62, v62, v193\n"
      "v_mul_f32 v63, v63, v193\n"
      "v_accvgpr_read_b32 v196, a144\n"
      "v_accvgpr_read_b32 v197, a145\n"
      "v_accvgpr_read_b32 v198, a146\n"
      "v_accvgpr_read_b32 v199, a147\n"
      "v_mul_f32 v196, v196, v194\n"
      "v_mul_f32 v197, v197, v194\n"
      "v_mul_f32 v198, v198, v194\n"
      "v_mul_f32 v199, v199, v194\n"
      "v_accvgpr_write_b32 a144, v196\n"
      "v_accvgpr_write_b32 a145, v197\n"
      "v_accvgpr_write_b32 a146, v198\n"
      "v_accvgpr_write_b32 a147, v199\n"
      "v_accvgpr_read_b32 v196, a148\n"
      "v_accvgpr_read_b32 v197, a149\n"
      "v_accvgpr_read_b32 v198, a150\n"
      "v_accvgpr_read_b32 v199, a151\n"
      "v_mul_f32 v196, v196, v195\n"
      "v_mul_f32 v197, v197, v195\n"
      "v_mul_f32 v198, v198, v195\n"
      "v_mul_f32 v199, v199, v195\n"
      "v_accvgpr_write_b32 a148, v196\n"
      "v_accvgpr_write_b32 a149, v197\n"
      "v_accvgpr_write_b32 a150, v198\n"
      "v_accvgpr_write_b32 a151, v199\n"
      "v_accvgpr_read_b32 v196, a152\n"
      "v_accvgpr_read_b32 v197, a153\n"
      "v_accvgpr_read_b32 v198, a154\n"
      "v_accvgpr_read_b32 v199, a155\n"
      "v_mul_f32 v196, v196, v194\n"
      "v_mul_f32 v197, v197, v194\n"
      "v_mul_f32 v198, v198, v194\n"
      "v_mul_f32 v199, v199, v194\n"
      "v_accvgpr_write_b32 a152, v196\n"
      "v_accvgpr_write_b32 a153, v197\n"
      "v_accvgpr_write_b32 a154, v198\n"
      "v_accvgpr_write_b32 a155, v199\n"
      "v_accvgpr_read_b32 v196, a156\n"
      "v_accvgpr_read_b32 v197, a157\n"
      "v_accvgpr_read_b32 v198, a158\n"
      "v_accvgpr_read_b32 v199, a159\n"
      "v_mul_f32 v196, v196, v195\n"
      "v_mul_f32 v197, v197, v195\n"
      "v_mul_f32 v198, v198, v195\n"
      "v_mul_f32 v199, v199, v195\n"
      "v_accvgpr_write_b32 a156, v196\n"
      "v_accvgpr_write_b32 a157, v197\n"
      "v_accvgpr_write_b32 a158, v198\n"
      "v_accvgpr_write_b32 a159, v199\n"
      "v_accvgpr_read_b32 v196, a160\n"
      "v_accvgpr_read_b32 v197, a161\n"
      "v_accvgpr_read_b32 v198, a162\n"
      "v_accvgpr_read_b32 v199, a163\n"
      "v_mul_f32 v196, v196, v194\n"
      "v_mul_f32 v197, v197, v194\n"
      "v_mul_f32 v198, v198, v194\n"
      "v_mul_f32 v199, v199, v194\n"
      "v_accvgpr_write_b32 a160, v196\n"
      "v_accvgpr_write_b32 a161, v197\n"
      "v_accvgpr_write_b32 a162, v198\n"
      "v_accvgpr_write_b32 a163, v199\n"
      "v_accvgpr_read_b32 v196, a164\n"
      "v_accvgpr_read_b32 v197, a165\n"
      "v_accvgpr_read_b32 v198, a166\n"
      "v_accvgpr_read_b32 v199, a167\n"
      "v_mul_f32 v196, v196, v195\n"
      "v_mul_f32 v197, v197, v195\n"
      "v_mul_f32 v198, v198, v195\n"
      "v_mul_f32 v199, v199, v195\n"
      "v_accvgpr_write_b32 a164, v196\n"
      "v_accvgpr_write_b32 a165, v197\n"
      "v_accvgpr_write_b32 a166, v198\n"
      "v_accvgpr_write_b32 a167, v199\n"
      "v_accvgpr_read_b32 v196, a168\n"
      "v_accvgpr_read_b32 v197, a169\n"
      "v_accvgpr_read_b32 v198, a170\n"
      "v_accvgpr_read_b32 v199, a171\n"
      "v_mul_f32 v196, v196, v194\n"
      "v_mul_f32 v197, v197, v194\n"
      "v_mul_f32 v198, v198, v194\n"
      "v_mul_f32 v199, v199, v194\n"
      "v_accvgpr_write_b32 a168, v196\n"
      "v_accvgpr_write_b32 a169, v197\n"
      "v_accvgpr_write_b32 a170, v198\n"
      "v_accvgpr_write_b32 a171, v199\n"
      "v_accvgpr_read_b32 v196, a172\n"
      "v_accvgpr_read_b32 v197, a173\n"
      "v_accvgpr_read_b32 v198, a174\n"
      "v_accvgpr_read_b32 v199, a175\n"
      "v_mul_f32 v196, v196, v195\n"
      "v_mul_f32 v197, v197, v195\n"
      "v_mul_f32 v198, v198, v195\n"
      "v_mul_f32 v199, v199, v195\n"
      "v_accvgpr_write_b32 a172, v196\n"
      "v_accvgpr_write_b32 a173, v197\n"
      "v_accvgpr_write_b32 a174, v198\n"
      "v_accvgpr_write_b32 a175, v199\n"
      "v_accvgpr_read_b32 v196, a176\n"
      "v_accvgpr_read_b32 v197, a177\n"
      "v_accvgpr_read_b32 v198, a178\n"
      "v_accvgpr_read_b32 v199, a179\n"
      "v_mul_f32 v196, v196, v194\n"
      "v_mul_f32 v197, v197, v194\n"
      "v_mul_f32 v198, v198, v194\n"
      "v_mul_f32 v199, v199, v194\n"
      "v_accvgpr_write_b32 a176, v196\n"
      "v_accvgpr_write_b32 a177, v197\n"
      "v_accvgpr_write_b32 a178, v198\n"
      "v_accvgpr_write_b32 a179, v199\n"
      "v_accvgpr_read_b32 v196, a180\n"
      "v_accvgpr_read_b32 v197, a181\n"
      "v_accvgpr_read_b32 v198, a182\n"
      "v_accvgpr_read_b32 v199, a183\n"
      "v_mul_f32 v196, v196, v195\n"
      "v_mul_f32 v197, v197, v195\n"
      "v_mul_f32 v198, v198, v195\n"
      "v_mul_f32 v199, v199, v195\n"
      "v_accvgpr_write_b32 a180, v196\n"
      "v_accvgpr_write_b32 a181, v197\n"
      "v_accvgpr_write_b32 a182, v198\n"
      "v_accvgpr_write_b32 a183, v199\n"
      "v_accvgpr_read_b32 v196, a184\n"
      "v_accvgpr_read_b32 v197, a185\n"
      "v_accvgpr_read_b32 v198, a186\n"
      "v_accvgpr_read_b32 v199, a187\n"
      "v_mul_f32 v196, v196, v194\n"
      "v_mul_f32 v197, v197, v194\n"
      "v_mul_f32 v198, v198, v194\n"
      "v_mul_f32 v199, v199, v194\n"
      "v_accvgpr_write_b32 a184, v196\n"
      "v_accvgpr_write_b32 a185, v197\n"
      "v_accvgpr_write_b32 a186, v198\n"
      "v_accvgpr_write_b32 a187, v199\n"
      "v_accvgpr_read_b32 v196, a188\n"
      "v_accvgpr_read_b32 v197, a189\n"
      "v_accvgpr_read_b32 v198, a190\n"
      "v_accvgpr_read_b32 v199, a191\n"
      "v_mul_f32 v196, v196, v195\n"
      "v_mul_f32 v197, v197, v195\n"
      "v_mul_f32 v198, v198, v195\n"
      "v_mul_f32 v199, v199, v195\n"
      "v_accvgpr_write_b32 a188, v196\n"
      "v_accvgpr_write_b32 a189, v197\n"
      "v_accvgpr_write_b32 a190, v198\n"
      "v_accvgpr_write_b32 a191, v199\n"
      "v_accvgpr_read_b32 v196, a192\n"
      "v_accvgpr_read_b32 v197, a193\n"
      "v_accvgpr_read_b32 v198, a194\n"
      "v_accvgpr_read_b32 v199, a195\n"
      "v_mul_f32 v196, v196, v194\n"
      "v_mul_f32 v197, v197, v194\n"
      "v_mul_f32 v198, v198, v194\n"
      "v_mul_f32 v199, v199, v194\n"
      "v_accvgpr_write_b32 a192, v196\n"
      "v_accvgpr_write_b32 a193, v197\n"
      "v_accvgpr_write_b32 a194, v198\n"
      "v_accvgpr_write_b32 a195, v199\n"
      "v_accvgpr_read_b32 v196, a196\n"
      "v_accvgpr_read_b32 v197, a197\n"
      "v_accvgpr_read_b32 v198, a198\n"
      "v_accvgpr_read_b32 v199, a199\n"
      "v_mul_f32 v196, v196, v195\n"
      "v_mul_f32 v197, v197, v195\n"
      "v_mul_f32 v198, v198, v195\n"
      "v_mul_f32 v199, v199, v195\n"
      "v_accvgpr_write_b32 a196, v196\n"
      "v_accvgpr_write_b32 a197, v197\n"
      "v_accvgpr_write_b32 a198, v198\n"
      "v_accvgpr_write_b32 a199, v199\n"
      "v_accvgpr_read_b32 v196, a200\n"
      "v_accvgpr_read_b32 v197, a201\n"
      "v_accvgpr_read_b32 v198, a202\n"
      "v_accvgpr_read_b32 v199, a203\n"
      "v_mul_f32 v196, v196, v194\n"
      "v_mul_f32 v197, v197, v194\n"
      "v_mul_f32 v198, v198, v194\n"
      "v_mul_f32 v199, v199, v194\n"
      "v_accvgpr_write_b32 a200, v196\n"
      "v_accvgpr_write_b32 a201, v197\n"
      "v_accvgpr_write_b32 a202, v198\n"
      "v_accvgpr_write_b32 a203, v199\n"
      "v_accvgpr_read_b32 v196, a204\n"
      "v_accvgpr_read_b32 v197, a205\n"
      "v_accvgpr_read_b32 v198, a206\n"
      "v_accvgpr_read_b32 v199, a207\n"
      "v_mul_f32 v196, v196, v195\n"
      "v_mul_f32 v197, v197, v195\n"
      "v_mul_f32 v198, v198, v195\n"
      "v_mul_f32 v199, v199, v195\n"
      "v_accvgpr_write_b32 a204, v196\n"
      "v_accvgpr_write_b32 a205, v197\n"
      "v_accvgpr_write_b32 a206, v198\n"
      "v_accvgpr_write_b32 a207, v199\n"
      "v_mul_f32 v180, v180, v192\n"
      "v_mul_f32 v181, v181, v193\n"
      "v_mul_f32 v182, v182, v194\n"
      "v_mul_f32 v183, v183, v195\n"
      "v_exp_f32 v188, v64\n"
      "v_exp_f32 v189, v65\n"
      "v_exp_f32 v190, v66\n"
      "v_exp_f32 v191, v67\n"
      "v_cvt_pk_bf16_f32 v128, v188, v189\n"
      "v_cvt_pk_bf16_f32 v129, v190, v191\n"
      "v_exp_f32 v192, v68\n"
      "v_exp_f32 v193, v69\n"
      "v_exp_f32 v194, v70\n"
      "v_exp_f32 v195, v71\n"
      "v_cvt_pk_bf16_f32 v130, v192, v193\n"
      "v_cvt_pk_bf16_f32 v131, v194, v195\n"
      "v_exp_f32 v188, v72\n"
      "v_exp_f32 v189, v73\n"
      "v_exp_f32 v190, v74\n"
      "v_exp_f32 v191, v75\n"
      "v_cvt_pk_bf16_f32 v132, v188, v189\n"
      "v_cvt_pk_bf16_f32 v133, v190, v191\n"
      "v_exp_f32 v192, v76\n"
      "v_exp_f32 v193, v77\n"
      "v_exp_f32 v194, v78\n"
      "v_exp_f32 v195, v79\n"
      "v_cvt_pk_bf16_f32 v134, v192, v193\n"
      "v_cvt_pk_bf16_f32 v135, v194, v195\n"
      "v_exp_f32 v188, v80\n"
      "v_exp_f32 v189, v81\n"
      "v_exp_f32 v190, v82\n"
      "v_exp_f32 v191, v83\n"
      "v_cvt_pk_bf16_f32 v136, v188, v189\n"
      "v_cvt_pk_bf16_f32 v137, v190, v191\n"
      "v_exp_f32 v192, v84\n"
      "v_exp_f32 v193, v85\n"
      "v_exp_f32 v194, v86\n"
      "v_exp_f32 v195, v87\n"
      "v_cvt_pk_bf16_f32 v138, v192, v193\n"
      "v_cvt_pk_bf16_f32 v139, v194, v195\n"
      "v_exp_f32 v188, v88\n"
      "v_exp_f32 v189, v89\n"
      "v_exp_f32 v190, v90\n"
      "v_exp_f32 v191, v91\n"
      "v_cvt_pk_bf16_f32 v140, v188, v189\n"
      "v_cvt_pk_bf16_f32 v141, v190, v191\n"
      "v_exp_f32 v192, v92\n"
      "v_exp_f32 v193, v93\n"
      "v_exp_f32 v194, v94\n"
      "v_exp_f32 v195, v95\n"
      "v_cvt_pk_bf16_f32 v142, v192, v193\n"
      "v_cvt_pk_bf16_f32 v143, v194, v195\n"
      "s_nop 1\n"
      "v_mfma_f32_16x16x32_bf16 v[184:187], a[128:131], v[128:131], 0\n"
      "v_mfma_f32_16x16x32_bf16 v[184:187], a[132:135], v[132:135], v[184:187]\n"
      "v_mfma_f32_16x16x32_bf16 v[184:187], a[136:139], v[136:139], v[184:187]\n"
      "v_mfma_f32_16x16x32_bf16 v[184:187], a[140:143], v[140:143], v[184:187]\n"
      "s_nop 7\n"
      "s_setpc_b64 s[96:97]\n"
      "FB1_%=:\n"
      "v_mov_b32 v196, 139248\n"
      "s_nop 7\n"
      "ds_write_b32 v196, v196\n"
      "v_max3_f32 v188, v96, v97, 0\n"
      "v_max3_f32 v189, v104, v105, 0\n"
      "v_max3_f32 v190, v112, v113, 0\n"
      "v_max3_f32 v191, v120, v121, 0\n"
      "v_max3_f32 v188, v188, v98, v99\n"
      "v_max3_f32 v189, v189, v106, v107\n"
      "v_max3_f32 v190, v190, v114, v115\n"
      "v_max3_f32 v191, v191, v122, v123\n"
      "v_max3_f32 v188, v188, v100, v101\n"
      "v_max3_f32 v189, v189, v108, v109\n"
      "v_max3_f32 v190, v190, v116, v117\n"
      "v_max3_f32 v191, v191, v124, v125\n"
      "v_max3_f32 v188, v188, v102, v103\n"
      "v_max3_f32 v189, v189, v110, v111\n"
      "v_max3_f32 v190, v190, v118, v119\n"
      "v_max3_f32 v191, v191, v126, v127\n"
      "v_mov_b32 v192, v188\n"
      "v_mov_b32 v193, v189\n"
      "v_mov_b32 v194, v190\n"
      "v_mov_b32 v195, v191\n"
      "v_permlane16_swap_b32 v188, v192\n"
      "v_permlane16_swap_b32 v189, v193\n"
      "v_permlane16_swap_b32 v190, v194\n"
      "v_permlane16_swap_b32 v191, v195\n"
      "v_max_f32 v188, v188, v192\n"
      "v_max_f32 v189, v189, v193\n"
      "v_max_f32 v190, v190, v194\n"
      "v_max_f32 v191, v191, v195\n"
      "v_mov_b32 v192, v188\n"
      "v_mov_b32 v193, v189\n"
      "v_mov_b32 v194, v190\n"
      "v_mov_b32 v195, v191\n"
      "v_permlane32_swap_b32 v188, v192\n"
      "v_permlane32_swap_b32 v189, v193\n"
      "v_permlane32_swap_b32 v190, v194\n"
      "v_permlane32_swap_b32 v191, v195\n"
      "v_max_f32 v188, v188, v192\n"
      "v_max_f32 v189, v189, v193\n"
      "v_max_f32 v190, v190, v194\n"
      "v_max_f32 v191, v191, v195\n"
      "RS1_%=:\n"
      "v_exp_f32 v192, -v188\n"
      "v_exp_f32 v193, -v189\n"
      "v_exp_f32 v194, -v190\n"
      "v_exp_f32 v195, -v191\n"
      "v_sub_f32 v176, v176, v188\n"
      "v_sub_f32 v177, v177, v189\n"
      "v_sub_f32 v178, v178, v190\n"
      "v_sub_f32 v179, v179, v191\n"
      "v_mov_b32 v160, v176\n"
      "v_mov_b32 v161, v176\n"
      "v_mov_b32 v162, v176\n"
      "v_mov_b32 v163, v176\n"
      "v_mov_b32 v164, v177\n"
      "v_mov_b32 v165, v177\n"
      "v_mov_b32 v166, v177\n"
      "v_mov_b32 v167, v177\n"
      "v_mov_b32 v168, v178\n"
      "v_mov_b32 v169, v178\n"
      "v_mov_b32 v170, v178\n"
      "v_mov_b32 v171, v178\n"
      "v_mov_b32 v172, v179\n"
      "v_mov_b32 v173, v179\n"
      "v_mov_b32 v174, v179\n"
      "v_mov_b32 v175, v179\n"
      "v_sub_f32 v64, v64, v188\n"
      "v_sub_f32 v65, v65, v188\n"
      "v_sub_f32 v66, v66, v188\n"
      "v_sub_f32 v67, v67, v188\n"
      "v_sub_f32 v68, v68, v188\n"
      "v_sub_f32 v69, v69, v188\n"
      "v_sub_f32 v70, v70, v188\n"
      "v_sub_f32 v71, v71, v188\n"
      "v_sub_f32 v72, v72, v189\n"
      "v_sub_f32 v73, v73, v189\n"
      "v_sub_f32 v74, v74, v189\n"
      "v_sub_f32 v75, v75, v189\n"
      "v_sub_f32 v76, v76, v189\n"
      "v_sub_f32 v77, v77, v189\n"
      "v_sub_f32 v78, v78, v189\n"
      "v_sub_f32 v79, v79, v189\n"
      "v_sub_f32 v80, v80, v190\n"
      "v_sub_f32 v81, v81, v190\n"
      "v_sub_f32 v82, v82, v190\n"
      "v_sub_f32 v83, v83, v190\n"
      "v_sub_f32 v84, v84, v190\n"
      "v_sub_f32 v85, v85, v190\n"
      "v_sub_f32 v86, v86, v190\n"
      "v_sub_f32 v87, v87, v190\n"
      "v_sub_f32 v88, v88, v191\n"
      "v_sub_f32 v89, v89, v191\n"
      "v_sub_f32 v90, v90, v191\n"
      "v_sub_f32 v91, v91, v191\n"
      "v_sub_f32 v92, v92, v191\n"
      "v_sub_f32 v93, v93, v191\n"
      "v_sub_f32 v94, v94, v191\n"
      "v_sub_f32 v95, v95, v191\n"
      "v_sub_f32 v96, v96, v188\n"
      "v_sub_f32 v97, v97, v188\n"
      "v_sub_f32 v98, v98, v188\n"
      "v_sub_f32 v99, v99, v188\n"
      "v_sub_f32 v100, v100, v188\n"
      "v_sub_f32 v101, v101, v188\n"
      "v_sub_f32 v102, v102, v188\n"
      "v_sub_f32 v103, v103, v188\n"
      "v_sub_f32 v104, v104, v189\n"
      "v_sub_f32 v105, v105, v189\n"
      "v_sub_f32 v106, v106, v189\n"
      "v_sub_f32 v107, v107, v189\n"
      "v_sub_f32 v108, v108, v189\n"
      "v_sub_f32 v109, v109, v189\n"
      "v_sub_f32 v110, v110, v189\n"
      "v_sub_f32 v111, v111, v189\n"
      "v_sub_f32 v112, v112, v190\n"
      "v_sub_f32 v113, v113, v190\n"
      "v_sub_f32 v114, v114, v190\n"
      "v_sub_f32 v115, v115, v190\n"
      "v_sub_f32 v116, v116, v190\n"
      "v_sub_f32 v117, v117, v190\n"
      "v_sub_f32 v118, v118, v190\n"
      "v_sub_f32 v119, v119, v190\n"
      "v_sub_f32 v120, v120, v191\n"
      "v_sub_f32 v121, v121, v191\n"
      "v_sub_f32 v122, v122, v191\n"
      "v_sub_f32 v123, v123, v191\n"
      "v_sub_f32 v124, v124, v191\n"
      "v_sub_f32 v125, v125, v191\n"
      "v_sub_f32 v126, v126, v191\n"
      "v_sub_f32 v127, v127, v191\n"
      "v_mul_f32 v0, v0, v192\n"
      "v_mul_f32 v1, v1, v192\n"
      "v_mul_f32 v2, v2, v192\n"
      "v_mul_f32 v3, v3, v192\n"
      "v_mul_f32 v4, v4, v193\n"
      "v_mul_f32 v5, v5, v193\n"
      "v_mul_f32 v6, v6, v193\n"
      "v_mul_f32 v7, v7, v193\n"
      "v_mul_f32 v8, v8, v192\n"
      "v_mul_f32 v9, v9, v192\n"
      "v_mul_f32 v10, v10, v192\n"
      "v_mul_f32 v11, v11, v192\n"
      "v_mul_f32 v12, v12, v193\n"
      "v_mul_f32 v13, v13, v193\n"
      "v_mul_f32 v14, v14, v193\n"
      "v_mul_f32 v15, v15, v193\n"
      "v_mul_f32 v16, v16, v192\n"
      "v_mul_f32 v17, v17, v192\n"
      "v_mul_f32 v18, v18, v192\n"
      "v_mul_f32 v19, v19, v192\n"
      "v_mul_f32 v20, v20, v193\n"
      "v_mul_f32 v21, v21, v193\n"
      "v_mul_f32 v22, v22, v193\n"
      "v_mul_f32 v23, v23, v193\n"
      "v_mul_f32 v24, v24, v192\n"
      "v_mul_f32 v25, v25, v192\n"
      "v_mul_f32 v26, v26, v192\n"
      "v_mul_f32 v27, v27, v192\n"
      "v_mul_f32 v28, v28, v193\n"
      "v_mul_f32 v29, v29, v193\n"
      "v_mul_f32 v30, v30, v193\n"
      "v_mul_f32 v31, v31, v193\n"
      "v_mul_f32 v32, v32, v192\n"
      "v_mul_f32 v33, v33, v192\n"
      "v_mul_f32 v34, v34, v192\n"
      "v_mul_f32 v35, v35, v192\n"
      "v_mul_f32 v36, v36, v193\n"
      "v_mul_f32 v37, v37, v193\n"
      "v_mul_f32 v38, v38, v193\n"
      "v_mul_f32 v39, v39, v193\n"
      "v_mul_f32 v40, v40, v192\n"
      "v_mul_f32 v41, v41, v192\n"
      "v_mul_f32 v42, v42, v192\n"
      "v_mul_f32 v43, v43, v192\n"
      "v_mul_f32 v44, v44, v193\n"
      "v_mul_f32 v45, v45, v193\n"
      "v_mul_f32 v46, v46, v193\n"
      "v_mul_f32 v47, v47, v193\n"
      "v_mul_f32 v48, v48, v192\n"
      "v_mul_f32 v49, v49, v192\n"
      "v_mul_f32 v50, v50, v192\n"
      "v_mul_f32 v51, v51, v192\n"
      "v_mul_f32 v52, v52, v193\n"
      "v_mul_f32 v53, v53, v193\n"
      "v_mul_f32 v54, v54, v193\n"
      "v_mul_f32 v55, v55, v193\n"
      "v_mul_f32 v56, v56, v192\n"
      "v_mul_f32 v57, v57, v192\n"
      "v_mul_f32 v58, v58, v192\n"
      "v_mul_f32 v59, v59, v192\n"
      "v_mul_f32 v60, v60, v193\n"
      "v_mul_f32 v61, v61, v193\n"
      "v_mul_f32 v62, v62, v193\n"
      "v_mul_f32 v63, v63, v193\n"
      "v_accvgpr_read_b32 v196, a144\n"
      "v_accvgpr_read_b32 v197, a145\n"
      "v_accvgpr_read_b32 v198, a146\n"
      "v_accvgpr_read_b32 v199, a147\n"
      "v_mul_f32 v196, v196, v194\n"
      "v_mul_f32 v197, v197, v194\n"
      "v_mul_f32 v198, v198, v194\n"
      "v_mul_f32 v199, v199, v194\n"
      "v_accvgpr_write_b32 a144, v196\n"
      "v_accvgpr_write_b32 a145, v197\n"
      "v_accvgpr_write_b32 a146, v198\n"
      "v_accvgpr_write_b32 a147, v199\n"
      "v_accvgpr_read_b32 v196, a148\n"
      "v_accvgpr_read_b32 v197, a149\n"
      "v_accvgpr_read_b32 v198, a150\n"
      "v_accvgpr_read_b32 v199, a151\n"
      "v_mul_f32 v196, v196, v195\n"
      "v_mul_f32 v197, v197, v195\n"
      "v_mul_f32 v198, v198, v195\n"
      "v_mul_f32 v199, v199, v195\n"
      "v_accvgpr_write_b32 a148, v196\n"
      "v_accvgpr_write_b32 a149, v197\n"
      "v_accvgpr_write_b32 a150, v198\n"
      "v_accvgpr_write_b32 a151, v199\n"
      "v_accvgpr_read_b32 v196, a152\n"
      "v_accvgpr_read_b32 v197, a153\n"
      "v_accvgpr_read_b32 v198, a154\n"
      "v_accvgpr_read_b32 v199, a155\n"
      "v_mul_f32 v196, v196, v194\n"
      "v_mul_f32 v197, v197, v194\n"
      "v_mul_f32 v198, v198, v194\n"
      "v_mul_f32 v199, v199, v194\n"
      "v_accvgpr_write_b32 a152, v196\n"
      "v_accvgpr_write_b32 a153, v197\n"
      "v_accvgpr_write_b32 a154, v198\n"
      "v_accvgpr_write_b32 a155, v199\n"
      "v_accvgpr_read_b32 v196, a156\n"
      "v_accvgpr_read_b32 v197, a157\n"
      "v_accvgpr_read_b32 v198, a158\n"
      "v_accvgpr_read_b32 v199, a159\n"
      "v_mul_f32 v196, v196, v195\n"
      "v_mul_f32 v197, v197, v195\n"
      "v_mul_f32 v198, v198, v195\n"
      "v_mul_f32 v199, v199, v195\n"
      "v_accvgpr_write_b32 a156, v196\n"
      "v_accvgpr_write_b32 a157, v197\n"
      "v_accvgpr_write_b32 a158, v198\n"
      "v_accvgpr_write_b32 a159, v199\n"
      "v_accvgpr_read_b32 v196, a160\n"
      "v_accvgpr_read_b32 v197, a161\n"
      "v_accvgpr_read_b32 v198, a162\n"
      "v_accvgpr_read_b32 v199, a163\n"
      "v_mul_f32 v196, v196, v194\n"
      "v_mul_f32 v197, v197, v194\n"
      "v_mul_f32 v198, v198, v194\n"
      "v_mul_f32 v199, v199, v194\n"
      "v_accvgpr_write_b32 a160, v196\n"
      "v_accvgpr_write_b32 a161, v197\n"
      "v_accvgpr_write_b32 a162, v198\n"
      "v_accvgpr_write_b32 a163, v199\n"
      "v_accvgpr_read_b32 v196, a164\n"
      "v_accvgpr_read_b32 v197, a165\n"
      "v_accvgpr_read_b32 v198, a166\n"
      "v_accvgpr_read_b32 v199, a167\n"
      "v_mul_f32 v196, v196, v195\n"
      "v_mul_f32 v197, v197, v195\n"
      "v_mul_f32 v198, v198, v195\n"
      "v_mul_f32 v199, v199, v195\n"
      "v_accvgpr_write_b32 a164, v196\n"
      "v_accvgpr_write_b32 a165, v197\n"
      "v_accvgpr_write_b32 a166, v198\n"
      "v_accvgpr_write_b32 a167, v199\n"
      "v_accvgpr_read_b32 v196, a168\n"
      "v_accvgpr_read_b32 v197, a169\n"
      "v_accvgpr_read_b32 v198, a170\n"
      "v_accvgpr_read_b32 v199, a171\n"
      "v_mul_f32 v196, v196, v194\n"
      "v_mul_f32 v197, v197, v194\n"
      "v_mul_f32 v198, v198, v194\n"
      "v_mul_f32 v199, v199, v194\n"
      "v_accvgpr_write_b32 a168, v196\n"
      "v_accvgpr_write_b32 a169, v197\n"
      "v_accvgpr_write_b32 a170, v198\n"
      "v_accvgpr_write_b32 a171, v199\n"
      "v_accvgpr_read_b32 v196, a172\n"
      "v_accvgpr_read_b32 v197, a173\n"
      "v_accvgpr_read_b32 v198, a174\n"
      "v_accvgpr_read_b32 v199, a175\n"
      "v_mul_f32 v196, v196, v195\n"
      "v_mul_f32 v197, v197, v195\n"
      "v_mul_f32 v198, v198, v195\n"
      "v_mul_f32 v199, v199, v195\n"
      "v_accvgpr_write_b32 a172, v196\n"
      "v_accvgpr_write_b32 a173, v197\n"
      "v_accvgpr_write_b32 a174, v198\n"
      "v_accvgpr_write_b32 a175, v199\n"
      "v_accvgpr_read_b32 v196, a176\n"
      "v_accvgpr_read_b32 v197, a177\n"
      "v_accvgpr_read_b32 v198, a178\n"
      "v_accvgpr_read_b32 v199, a179\n"
      "v_mul_f32 v196, v196, v194\n"
      "v_mul_f32 v197, v197, v194\n"
      "v_mul_f32 v198, v198, v194\n"
      "v_mul_f32 v199, v199, v194\n"
      "v_accvgpr_write_b32 a176, v196\n"
      "v_accvgpr_write_b32 a177, v197\n"
      "v_accvgpr_write_b32 a178, v198\n"
      "v_accvgpr_write_b32 a179, v199\n"
      "v_accvgpr_read_b32 v196, a180\n"
      "v_accvgpr_read_b32 v197, a181\n"
      "v_accvgpr_read_b32 v198, a182\n"
      "v_accvgpr_read_b32 v199, a183\n"
      "v_mul_f32 v196, v196, v195\n"
      "v_mul_f32 v197, v197, v195\n"
      "v_mul_f32 v198, v198, v195\n"
      "v_mul_f32 v199, v199, v195\n"
      "v_accvgpr_write_b32 a180, v196\n"
      "v_accvgpr_write_b32 a181, v197\n"
      "v_accvgpr_write_b32 a182, v198\n"
      "v_accvgpr_write_b32 a183, v199\n"
      "v_accvgpr_read_b32 v196, a184\n"
      "v_accvgpr_read_b32 v197, a185\n"
      "v_accvgpr_read_b32 v198, a186\n"
      "v_accvgpr_read_b32 v199, a187\n"
      "v_mul_f32 v196, v196, v194\n"
      "v_mul_f32 v197, v197, v194\n"
      "v_mul_f32 v198, v198, v194\n"
      "v_mul_f32 v199, v199, v194\n"
      "v_accvgpr_write_b32 a184, v196\n"
      "v_accvgpr_write_b32 a185, v197\n"
      "v_accvgpr_write_b32 a186, v198\n"
      "v_accvgpr_write_b32 a187, v199\n"
      "v_accvgpr_read_b32 v196, a188\n"
      "v_accvgpr_read_b32 v197, a189\n"
      "v_accvgpr_read_b32 v198, a190\n"
      "v_accvgpr_read_b32 v199, a191\n"
      "v_mul_f32 v196, v196, v195\n"
      "v_mul_f32 v197, v197, v195\n"
      "v_mul_f32 v198, v198, v195\n"
      "v_mul_f32 v199, v199, v195\n"
      "v_accvgpr_write_b32 a188, v196\n"
      "v_accvgpr_write_b32 a189, v197\n"
      "v_accvgpr_write_b32 a190, v198\n"
      "v_accvgpr_write_b32 a191, v199\n"
      "v_accvgpr_read_b32 v196, a192\n"
      "v_accvgpr_read_b32 v197, a193\n"
      "v_accvgpr_read_b32 v198, a194\n"
      "v_accvgpr_read_b32 v199, a195\n"
      "v_mul_f32 v196, v196, v194\n"
      "v_mul_f32 v197, v197, v194\n"
      "v_mul_f32 v198, v198, v194\n"
      "v_mul_f32 v199, v199, v194\n"
      "v_accvgpr_write_b32 a192, v196\n"
      "v_accvgpr_write_b32 a193, v197\n"
      "v_accvgpr_write_b32 a194, v198\n"
      "v_accvgpr_write_b32 a195, v199\n"
      "v_accvgpr_read_b32 v196, a196\n"
      "v_accvgpr_read_b32 v197, a197\n"
      "v_accvgpr_read_b32 v198, a198\n"
      "v_accvgpr_read_b32 v199, a199\n"
      "v_mul_f32 v196, v196, v195\n"
      "v_mul_f32 v197, v197, v195\n"
      "v_mul_f32 v198, v198, v195\n"
      "v_mul_f32 v199, v199, v195\n"
      "v_accvgpr_write_b32 a196, v196\n"
      "v_accvgpr_write_b32 a197, v197\n"
      "v_accvgpr_write_b32 a198, v198\n"
      "v_accvgpr_write_b32 a199, v199\n"
      "v_accvgpr_read_b32 v196, a200\n"
      "v_accvgpr_read_b32 v197, a201\n"
      "v_accvgpr_read_b32 v198, a202\n"
      "v_accvgpr_read_b32 v199, a203\n"
      "v_mul_f32 v196, v196, v194\n"
      "v_mul_f32 v197, v197, v194\n"
      "v_mul_f32 v198, v198, v194\n"
      "v_mul_f32 v199, v199, v194\n"
      "v_accvgpr_write_b32 a200, v196\n"
      "v_accvgpr_write_b32 a201, v197\n"
      "v_accvgpr_write_b32 a202, v198\n"
      "v_accvgpr_write_b32 a203, v199\n"
      "v_accvgpr_read_b32 v196, a204\n"
      "v_accvgpr_read_b32 v197, a205\n"
      "v_accvgpr_read_b32 v198, a206\n"
      "v_accvgpr_read_b32 v199, a207\n"
      "v_mul_f32 v196, v196, v195\n"
      "v_mul_f32 v197, v197, v195\n"
      "v_mul_f32 v198, v198, v195\n"
      "v_mul_f32 v199, v199, v195\n"
      "v_accvgpr_write_b32 a204, v196\n"
      "v_accvgpr_write_b32 a205, v197\n"
      "v_accvgpr_write_b32 a206, v198\n"
      "v_accvgpr_write_b32 a207, v199\n"
      "v_mul_f32 v180, v180, v192\n"
      "v_mul_f32 v181, v181, v193\n"
      "v_mul_f32 v182, v182, v194\n"
      "v_mul_f32 v183, v183, v195\n"
      "v_lshlrev_b32 v196, 16, v128\n"
      "v_and_b32 v197, 0xffff0000, v128\n"
      "v_mul_f32 v196, v196, v192\n"
      "v_mul_f32 v197, v197, v192\n"
      "v_cvt_pk_bf16_f32 v128, v196, v197\n"
      "v_lshlrev_b32 v196, 16, v129\n"
      "v_and_b32 v197, 0xffff0000, v129\n"
      "v_mul_f32 v196, v196, v192\n"
      "v_mul_f32 v197, v197, v192\n"
      "v_cvt_pk_bf16_f32 v129, v196, v197\n"
      "v_lshlrev_b32 v196, 16, v130\n"
      "v_and_b32 v197, 0xffff0000, v130\n"
      "v_mul_f32 v196, v196, v192\n"
      "v_mul_f32 v197, v197, v192\n"
      "v_cvt_pk_bf16_f32 v130, v196, v197\n"
      "v_lshlrev_b32 v196, 16, v131\n"
      "v_and_b32 v197, 0xffff0000, v131\n"
      "v_mul_f32 v196, v196, v192\n"
      "v_mul_f32 v197, v197, v192\n"
      "v_cvt_pk_bf16_f32 v131, v196, v197\n"
      "v_lshlrev_b32 v196, 16, v132\n"
      "v_and_b32 v197, 0xffff0000, v132\n"
      "v_mul_f32 v196, v196, v193\n"
      "v_mul_f32 v197, v197, v193\n"
      "v_cvt_pk_bf16_f32 v132, v196, v197\n"
      "v_lshlrev_b32 v196, 16, v133\n"
      "v_and_b32 v197, 0xffff0000, v133\n"
      "v_mul_f32 v196, v196, v193\n"
      "v_mul_f32 v197, v197, v193\n"
      "v_cvt_pk_bf16_f32 v133, v196, v197\n"
      "v_lshlrev_b32 v196, 16, v134\n"
      "v_and_b32 v197, 0xffff0000, v134\n"
      "v_mul_f32 v196, v196, v193\n"
      "v_mul_f32 v197, v197, v193\n"
      "v_cvt_pk_bf16_f32 v134, v196, v197\n"
      "v_lshlrev_b32 v196, 16, v135\n"
      "v_and_b32 v197, 0xffff0000, v135\n"
      "v_mul_f32 v196, v196, v193\n"
      "v_mul_f32 v197, v197, v193\n"
      "v_cvt_pk_bf16_f32 v135, v196, v197\n"
      "v_lshlrev_b32 v196, 16, v136\n"
      "v_and_b32 v197, 0xffff0000, v136\n"
      "v_mul_f32 v196, v196, v194\n"
      "v_mul_f32 v197, v197, v194\n"
      "v_cvt_pk_bf16_f32 v136, v196, v197\n"
      "v_lshlrev_b32 v196, 16, v137\n"
      "v_and_b32 v197, 0xffff0000, v137\n"
      "v_mul_f32 v196, v196, v194\n"
      "v_mul_f32 v197, v197, v194\n"
      "v_cvt_pk_bf16_f32 v137, v196, v197\n"
      "v_lshlrev_b32 v196, 16, v138\n"
      "v_and_b32 v197, 0xffff0000, v138\n"
      "v_mul_f32 v196, v196, v194\n"
      "v_mul_f32 v197, v197, v194\n"
      "v_cvt_pk_bf16_f32 v138, v196, v197\n"
      "v_lshlrev_b32 v196, 16, v139\n"
      "v_and_b32 v197, 0xffff0000, v139\n"
      "v_mul_f32 v196, v196, v194\n"
      "v_mul_f32 v197, v197, v194\n"
      "v_cvt_pk_bf16_f32 v139, v196, v197\n"
      "v_lshlrev_b32 v196, 16, v140\n"
      "v_and_b32 v197, 0xffff0000, v140\n"
      "v_mul_f32 v196, v196, v195\n"
      "v_mul_f32 v197, v197, v195\n"
      "v_cvt_pk_bf16_f32 v140, v196, v197\n"
      "v_lshlrev_b32 v196, 16, v141\n"
      "v_and_b32 v197, 0xffff0000, v141\n"
      "v_mul_f32 v196, v196, v195\n"
      "v_mul_f32 v197, v197, v195\n"
      "v_cvt_pk_bf16_f32 v141, v196, v197\n"
      "v_lshlrev_b32 v196, 16, v142\n"
      "v_and_b32 v197, 0xffff0000, v142\n"
      "v_mul_f32 v196, v196, v195\n"
      "v_mul_f32 v197, v197, v195\n"
      "v_cvt_pk_bf16_f32 v142, v196, v197\n"
      "v_lshlrev_b32 v196, 16, v143\n"
      "v_and_b32 v197, 0xffff0000, v143\n"
      "v_mul_f32 v196, v196, v195\n"
      "v_mul_f32 v197, v197, v195\n"
      "v_cvt_pk_bf16_f32 v143, v196, v197\n"
      "v_exp_f32 v188, v96\n"
      "v_exp_f32 v189, v97\n"
      "v_exp_f32 v190, v98\n"
      "v_exp_f32 v191, v99\n"
      "v_cvt_pk_bf16_f32 v144, v188, v189\n"
      "v_cvt_pk_bf16_f32 v145, v190, v191\n"
      "v_exp_f32 v192, v100\n"
      "v_exp_f32 v193, v101\n"
      "v_exp_f32 v194, v102\n"
      "v_exp_f32 v195, v103\n"
      "v_cvt_pk_bf16_f32 v146, v192, v193\n"
      "v_cvt_pk_bf16_f32 v147, v194, v195\n"
      "v_exp_f32 v188, v104\n"
      "v_exp_f32 v189, v105\n"
      "v_exp_f32 v190, v106\n"
      "v_exp_f32 v191, v107\n"
      "v_cvt_pk_bf16_f32 v148, v188, v189\n"
      "v_cvt_pk_bf16_f32 v149, v190, v191\n"
      "v_exp_f32 v192, v108\n"
      "v_exp_f32 v193, v109\n"
      "v_exp_f32 v194, v110\n"
      "v_exp_f32 v195, v111\n"
      "v_cvt_pk_bf16_f32 v150, v192, v193\n"
      "v_cvt_pk_bf16_f32 v151, v194, v195\n"
      "v_exp_f32 v188, v112\n"
      "v_exp_f32 v189, v113\n"
      "v_exp_f32 v190, v114\n"
      "v_exp_f32 v191, v115\n"
      "v_cvt_pk_bf16_f32 v152, v188, v189\n"
      "v_cvt_pk_bf16_f32 v153, v190, v191\n"
      "v_exp_f32 v192, v116\n"
      "v_exp_f32 v193, v117\n"
      "v_exp_f32 v194, v118\n"
      "v_exp_f32 v195, v119\n"
      "v_cvt_pk_bf16_f32 v154, v192, v193\n"
      "v_cvt_pk_bf16_f32 v155, v194, v195\n"
      "v_exp_f32 v188, v120\n"
      "v_exp_f32 v189, v121\n"
      "v_exp_f32 v190, v122\n"
      "v_exp_f32 v191, v123\n"
      "v_cvt_pk_bf16_f32 v156, v188, v189\n"
      "v_cvt_pk_bf16_f32 v157, v190, v191\n"
      "v_exp_f32 v192, v124\n"
      "v_exp_f32 v193, v125\n"
      "v_exp_f32 v194, v126\n"
      "v_exp_f32 v195, v127\n"
      "v_cvt_pk_bf16_f32 v158, v192, v193\n"
      "v_cvt_pk_bf16_f32 v159, v194, v195\n"
      "s_nop 1\n"
      "v_mfma_f32_16x16x32_bf16 v[184:187], a[128:131], v[144:147], 0\n"
      "v_mfma_f32_16x16x32_bf16 v[184:187], a[132:135], v[148:151], v[184:187]\n"
      "v_mfma_f32_16x16x32_bf16 v[184:187], a[136:139], v[152:155], v[184:187]\n"
      "v_mfma_f32_16x16x32_bf16 v[184:187], a[140:143], v[156:159], v[184:187]\n"
      "s_nop 7\n"
      "s_setpc_b64 s[96:97]\n"
      "SLOW_%=:\n"
      "ds_write_b128 v234, v[176:179] offset:5120\n"
      "s_waitcnt lgkmcnt(0)\n"
      "s_barrier\n"
      "ds_read_b128 v[192:195], v235 offset:5120\n"
      "s_waitcnt lgkmcnt(0)\n"
      "v_sub_f32 v188, v176, v194\n"
      "v_sub_f32 v189, v177, v195\n"
      "v_sub_f32 v190, v178, v192\n"
      "v_sub_f32 v191, v179, v193\n"
      "v_max_f32 v188, 0, v188\n"
      "v_max_f32 v189, 0, v189\n"
      "v_max_f32 v190, 0, v190\n"
      "v_max_f32 v191, 0, v191\n"
      "s_call_b64 s[96:97], RS1_%=\n"
      "ds_write_b128 v232, a[176:179] offset:0\n"
      "ds_write_b128 v232, a[180:183] offset:1024\n"
      "ds_write_b128 v232, a[184:187] offset:2048\n"
      "ds_write_b128 v232, a[188:191] offset:3072\n"
      "ds_write_b128 v232, a[192:195] offset:4096\n"
      "ds_write_b128 v232, a[196:199] offset:5120\n"
      "ds_write_b128 v232, a[200:203] offset:6144\n"
      "ds_write_b128 v232, a[204:207] offset:7168\n"
      "ds_write_b128 v234, v[152:155] offset:0\n"
      "ds_write_b128 v234, v[156:159] offset:1024\n"
      "v_cndmask_b32 v229, v180, v181, s[74:75]\n"
      "v_cndmask_b32 v200, v182, v183, s[74:75]\n"
      "v_cndmask_b32 v228, v176, v177, s[74:75]\n"
      "v_cndmask_b32 v202, v186, v187, s[74:75]\n"
      "v_cndmask_b32 v201, v184, v185, s[74:75]\n"
      "v_add_f32 v200, v200, v202\n"
      "ds_write_b32 v234, v200 offset:4096\n"
      "v_add_f32 v229, v229, v201\n"
      "s_waitcnt lgkmcnt(0)\n"
      "s_barrier\n"
      "ds_read_b32 v208, v209\n"
      "ds_read_b128 v[160:163], v235 offset:0\n"
      "ds_read_b128 v[164:167], v235 offset:1024\n"
      "ds_read_b128 v[64:67], v233 offset:0\n"
      "ds_read_b128 v[68:71], v233 offset:1024\n"
      "ds_read_b128 v[72:75], v233 offset:2048\n"
      "ds_read_b128 v[76:79], v233 offset:3072\n"
      "ds_read_b128 v[80:83], v233 offset:4096\n"
      "ds_read_b128 v[84:87], v233 offset:5120\n"
      "ds_read_b128 v[88:91], v233 offset:6144\n"
      "ds_read_b128 v[92:95], v233 offset:7168\n"
      "ds_read_b32 v204, v235 offset:4096\n"
      "s_branch AGAIN_%=\n"
      ".subsection 0\n"
      : "+{v0}"(tid)
      : [blk] "s"(blockIdx.x), [kp] "s"(__builtin_amdgcn_kernarg_segment_ptr()), "s"(lds0)
      : "v1", "v2", "v3", "v4", "v5", "v6", "v7", "v8", "v9", "v10", "v11", "v12", "v13", "v14", "v15", "v16", "v17", "v18", "v19", "v20", "v21", "v22", "v23", "v24", "v25", "v26", "v27", "v28", "v29", "v30", "v31", "v32", "v33", "v34", "v35", "v36", "v37", "v38", "v39", "v40", "v41", "v42", "v43", "v44", "v45", "v46", "v47", "v48", "v49", "v50", "v51", "v52", "v53", "v54", "v55", "v56", "v57", "v58", "v59", "v60", "v61", "v62", "v63", "v64", "v65", "v66", "v67", "v68", "v69", "v70", "v71", "v72", "v73", "v74", "v75", "v76", "v77", "v78", "v79", "v80", "v81", "v82", "v83", "v84", "v85", "v86", "v87", "v88", "v89", "v90", "v91", "v92", "v93", "v94", "v95", "v96", "v97", "v98", "v99", "v100", "v101", "v102", "v103", "v104", "v105", "v106", "v107", "v108", "v109", "v110", "v111", "v112", "v113", "v114", "v115", "v116", "v117", "v118", "v119", "v120", "v121", "v122", "v123", "v124", "v125", "v126", "v127", "v128", "v129", "v130", "v131", "v132", "v133", "v134", "v135", "v136", "v137", "v138", "v139", "v140", "v141", "v142", "v143", "v144", "v145", "v146", "v147", "v148", "v149", "v150", "v151", "v152", "v153", "v154", "v155", "v156", "v157", "v158", "v159", "v160", "v161", "v162", "v163", "v164", "v165", "v166", "v167", "v168", "v169", "v170", "v171", "v172", "v173", "v174", "v175", "v176", "v177", "v178", "v179", "v180", "v181", "v182", "v183", "v184", "v185", "v186", "v187", "v188", "v189", "v190", "v191", "v192", "v193", "v194", "v195", "v196", "v197", "v198", "v199", "v200", "v201", "v202", "v203", "v204", "v205", "v206", "v207", "v208", "v209", "v210", "v211", "v212", "v213", "v214", "v215", "v216", "v217", "v218", "v219", "v220", "v221", "v222", "v223", "v224", "v225", "v226", "v227", "v228", "v229", "v230", "v231", "v232", "v233", "v234", "v235", "v236", "v237", "v238", "v239", "v240", "v241", "v242", "v243", "v244", "v245", "v246", "v247", "v248", "v249", "v250", "v251", "a0", "a1", "a2", "a3", "a4", "a5", "a6", "a7", "a8", "a9", "a10", "a11", "a12", "a13", "a14", "a15", "a16", "a17", "a18", "a19", "a20", "a21", "a22", "a23", "a24", "a25", "a26", "a27", "a28", "a29", "a30", "a31", "a32", "a33", "a34", "a35", "a36", "a37", "a38", "a39", "a40", "a41", "a42", "a43", "a44", "a45", "a46", "a47", "a48", "a49", "a50", "a51", "a52", "a53", "a54", "a55", "a56", "a57", "a58", "a59", "a60", "a61", "a62", "a63", "a64", "a65", "a66", "a67", "a68", "a69", "a70", "a71", "a72", "a73", "a74", "a75", "a76", "a77", "a78", "a79", "a80", "a81", "a82", "a83", "a84", "a85", "a86", "a87", "a88", "a89", "a90", "a91", "a92", "a93", "a94", "a95", "a96", "a97", "a98", "a99", "a100", "a101", "a102", "a103", "a104", "a105", "a106", "a107", "a108", "a109", "a110", "a111", "a112", "a113", "a114", "a115", "a116", "a117", "a118", "a119", "a120", "a121", "a122", "a123", "a124", "a125", "a126", "a127", "a128", "a129", "a130", "a131", "a132", "a133", "a134", "a135", "a136", "a137", "a138", "a139", "a140", "a141", "a142", "a143", "a144", "a145", "a146", "a147", "a148", "a149", "a150", "a151", "a152", "a153", "a154", "a155", "a156", "a157", "a158", "a159", "a160", "a161", "a162", "a163", "a164", "a165", "a166", "a167", "a168", "a169", "a170", "a171", "a172", "a173", "a174", "a175", "a176", "a177", "a178", "a179", "a180", "a181", "a182", "a183", "a184", "a185", "a186", "a187", "a188", "a189", "a190", "a191", "a192", "a193", "a194", "a195", "a196", "a197", "a198", "a199", "a200", "a201", "a202", "a203", "a204", "a205", "a206", "a207", "s4", "s5", "s6", "s7", "s8", "s9", "s10", "s11", "s12", "s13", "s14", "s15", "s16", "s17", "s18", "s19", "s20", "s21", "s22", "s23", "s24", "s25", "s26", "s27", "s28", "s29", "s30", "s31", "s32", "s33", "s34", "s35", "s38", "s39", "s40", "s43", "s44", "s45", "s46", "s47", "s50", "s52", "s53", "s54", "s56", "s57", "s58", "s59", "s60", "s61", "s62", "s63", "s64", "s65", "s66", "s67", "s68", "s69", "s70", "s71", "s72", "s74", "s75", "s76", "s77", "s78", "s79", "s80", "s81", "s82", "s83", "s84", "s85", "s86", "s88", "s89", "s90", "s91", "s92", "s93", "s94", "s95", "s96", "s97", "vcc", "scc", "memory");
}

}

extern "C" void kernel_launch(void* const* d_in, const int* in_sizes, int n_in, void* d_out, int out_size,
                              void* d_ws, size_t ws_size, hipStream_t stream) {
  if (n_in != 6 || ws_size < WS_END) return;
  const __hip_bfloat16* q = (const __hip_bfloat16*)d_in[0];
  const __hip_bfloat16* kc = (const __hip_bfloat16*)d_in[1];
  const __hip_bfloat16* vc = (const __hip_bfloat16*)d_in[2];
  const __hip_bfloat16* kt = (const __hip_bfloat16*)d_in[3];
  const __hip_bfloat16* vt = (const __hip_bfloat16*)d_in[4];
  const __hip_bfloat16* tm = (const __hip_bfloat16*)d_in[5];
  hipLaunchKernelGGL(tree_attn_main, dim3(B_ * HKV_ * 2), dim3(256), 0, stream, q, kc, vc, kt, vt, tm,
                     (__hip_bfloat16*)d_out, (char*)d_ws);
}
```

```cpp
#include <hip/hip_runtime.h>
#include <hip/hip_bf16.h>
#include <stdint.h>

#pragma clang diagnostic ignored "-Wint-to-pointer-cast"
#pragma clang diagnostic ignored "-Winline-asm"

namespace {

constexpr int B_ = 16, HKV_ = 8, D_ = 128;
constexpr int SLOT = 64 * D_ * 2;
constexpr int LDS_BYTES = 10 * SLOT;

constexpr size_t WS_EPOCH = 0;
constexpr size_t WS_REF = WS_EPOCH + 128 * 4 * 128;
constexpr size_t WS_X = WS_REF + 16384;
constexpr size_t XW_BYTES = 4608;
constexpr size_t WS_END = WS_X + (size_t)128 * 2 * 4 * XW_BYTES;

__global__ __launch_bounds__(256, 1)
void tree_attn_main(const __hip_bfloat16*, const __hip_bfloat16*, const __hip_bfloat16*, const __hip_bfloat16*,
                    const __hip_bfloat16*, const __hip_bfloat16*, __hip_bfloat16*, char*) {
  __shared__ __attribute__((aligned(1024))) char smem[LDS_BYTES];
  const unsigned lds0 = (unsigned)(uintptr_t)smem;
  unsigned tid = threadIdx.x;
  asm volatile(
      "s_load_dwordx16 s[4:19], %[kp], 0x0\n"
      "s_getpc_b64 s[60:61]\n"
      "v_lshlrev_b32 v241, 4, v0\n"
      "s_lshl_b32 s62, %[blk], 9\n"
      "s_and_b32 s62, s62, 0x3000\n"
      "s_add_u32 s60, s60, s62\n"
      "s_addc_u32 s61, s61, 0\n"
      "global_load_dwordx4 v[236:239], v241, s[60:61]\n"
      "v_and_b32 v1, 0xc0, v0\n"
      "v_lshlrev_b32 v248, 1, v0\n"
      "v_readfirstlane_b32 s50, v1\n"
      "v_and_b32 v248, 0x60, v248\n"
      "v_and_b32 v249, 0x70, v241\n"
      "v_xor_b32 v248, v248, v249\n"
      "v_lshl_or_b32 v248, v1, 6, v248\n"
      "v_and_b32 v1, 56, v0\n"
      "v_lshl_or_b32 v248, v1, 5, v248\n"
      "s_mov_b32 s64, 0x14100400\n"
      "s_mov_b32 s65, 0x54504440\n"
      "s_lshl_b32 s66, %[blk], 3\n"
      "s_lshr_b64 s[64:65], s[64:65], s66\n"
      "s_mov_b32 s66, 0x3020100\n"
      "s_mov_b32 s67, 0x83828180\n"
      "s_and_b32 s65, %[blk], 56\n"
      "s_lshr_b64 s[66:67], s[66:67], s65\n"
      "s_or_b32 s64, s64, s66\n"
      "s_lshr_b32 s65, %[blk], 3\n"
      "s_and_b32 s65, s65, 24\n"
      "s_lshr_b32 s65, 0x28082000, s65\n"
      "s_or_b32 s64, s64, s65\n"
      "s_and_b32 s63, s64, 0x80\n"
      "s_and_b32 s44, s64, 127\n"
      "v_or_b32 v248, s63, v248\n"
      "v_xor_b32 v249, 0x80, v248\n"
      "s_lshl_b32 s64, s44, 20\n"
      "s_lshl_b32 s62, s44, 15\n"
      "s_lshl_b32 s50, s50, 6\n"
      "s_add_u32 m0, s50, 131072\n"
      "s_mov_b32 s58, 0x120000\n"
      "s_mov_b32 s59, 0x20000\n"
      "s_movk_i32 s22, 0x4000\n"
      "s_waitcnt lgkmcnt(0)\n"
      "s_add_u32 s56, s4, s62\n"
      "s_addc_u32 s57, s5, 0\n"
      "buffer_load_dwordx4 v248, s[56:59], 0 offen nt lds\n"
      "buffer_load_dwordx4 v248, s[56:59], 0 offen offset:2048 nt lds\n"
      "s_bitset1_b32 m0, 10\n"
      "s_memrealtime s[82:83]\n"
      "s_add_u32 s20, s6, s64\n"
      "s_addc_u32 s21, s7, 0\n"
      "s_add_u32 s32, s8, s64\n"
      "s_addc_u32 s33, s9, 0\n"
      "s_load_dword s92, s[20:21], 0x0\n"
      "s_load_dword s92, s[10:11], 0x0\n"
      "s_load_dword s92, s[32:33], 0x0\n"
      "buffer_load_dwordx4 v249, s[56:59], 0 offen nt lds\n"
      "buffer_load_dwordx4 v249, s[56:59], 0 offen offset:2048 nt lds\n"
      "s_add_u32 m0, s50, 147456\n"
      "s_load_dword s92, s[18:19], 0x0\n"
      "buffer_load_dwordx4 v248, s[56:59], s22 offen nt lds\n"
      "buffer_load_dwordx4 v248, s[56:59], s22 offen offset:2048 nt lds\n"
      "s_bitset1_b32 m0, 10\n"
      "s_nop 0\n"
      "buffer_load_dwordx4 v249, s[56:59], s22 offen nt lds\n"
      "buffer_load_dwordx4 v249, s[56:59], s22 offen offset:2048 nt lds\n"
      "s_mov_b64 s[56:57], s[20:21]\n"
      "s_load_dword s92, s[14:15], 0x0\n"
      "s_load_dword s92, s[12:13], 0x0\n"
      "s_lshr_b32 s74, s50, 2\n"
      "s_lshr_b32 s75, s50, 5\n"
      "s_xor_b32 s75, s75, s63\n"
      "s_and_b32 s75, s75, 0x80\n"
      "s_and_b32 s76, s74, 0x800\n"
      "s_add_u32 s75, s75, s76\n"
      "s_add_u32 s70, s63, s50\n"
      "s_sub_u32 s70, s70, s75\n"
      "s_lshr_b32 s75, s50, 1\n"
      "s_lshr_b32 s71, s62, 2\n"
      "s_sub_u32 s71, s71, s75\n"
      "s_ashr_i32 s72, s71, 31\n"
      "s_mov_b32 s23, s59\n"
      "s_mov_b64 s[26:27], s[22:23]\n"
      "s_sub_u32 s20, s56, s70\n"
      "s_subb_u32 s21, s57, 0\n"
      "s_add_u32 m0, s74, 24576\n"
      "s_add_u32 s24, s10, s71\n"
      "buffer_load_dwordx4 v248, s[20:23], 0 offen nt lds\n"
      "s_addc_u32 s25, s11, s72\n"
      "s_add_u32 m0, s75, 16384\n"
      "s_nop 0\n"
      "buffer_load_dwordx4 v248, s[24:27], 0 offen nt lds\n"
      "s_bitset1_b32 m0, 10\n"
      "s_nop 0\n"
      "buffer_load_dwordx4 v249, s[24:27], 0 offen nt lds\n"
      "s_lshr_b32 s43, s63, 7\n"
      "s_lshl_b32 s76, s44, 9\n"
      "s_lshr_b32 s69, s50, 5\n"
      "s_add_u32 s76, s76, s69\n"
      "s_add_u32 s46, s18, s76\n"
      "s_addc_u32 s47, s19, 0\n"
      "s_mul_i32 s76, s43, 0x3000\n"
      "s_lshl_b32 s69, s44, 8\n"
      "s_and_b32 s69, s69, 0x7800\n"
      "s_add_u32 s14, s14, s69\n"
      "s_addc_u32 s15, s15, 0\n"
      "v_mov_b64 v[192:193], 0\n"
      "v_mov_b64 v[194:195], 0\n"
      "v_lshlrev_b32 v3, 6, v0\n"
      "v_and_b32 v3, 0x3c0, v3\n"
      "v_lshrrev_b32 v4, 1, v0\n"
      "v_and_or_b32 v211, v4, 24, v3\n"
      "v_mov_b64 v[200:201], 0\n"
      "v_mov_b64 v[202:203], 0\n"
      "v_mov_b64 v[204:205], 0\n"
      "v_mov_b64 v[206:207], 0\n"
      "s_cmp_lg_u32 s76, s50\n"
      "s_cbranch_scc1 NOMASK_%=\n"
      "global_load_dwordx2 v[200:201], v211, s[14:15] offset:0\n"
      "global_load_dwordx2 v[202:203], v211, s[14:15] offset:32\n"
      "global_load_dwordx2 v[204:205], v211, s[14:15] offset:1024\n"
      "global_load_dwordx2 v[206:207], v211, s[14:15] offset:1056\n"
      "NOMASK_%=:\n"
      "global_load_dword v208, v192, s[46:47] sc1\n"
      "s_mul_i32 s69, s43, 0x8000\n"
      "s_bitcmp1_b32 s44, 0\n"
      "s_cselect_b32 s66, 0x8000, 0\n"
      "s_xor_b32 s69, s69, s66\n"
      "v_and_b32 v228, 0xff, v248\n"
      "v_xor_b32 v228, s69, v228\n"
      "v_bfe_i32 v2, v0, 3, 1\n"
      "v_and_b32 v2, 0x1000, v2\n"
      "v_xor_b32 v228, v228, v2\n"
      "v_bfe_i32 v2, v0, 4, 1\n"
      "v_and_b32 v2, 0x20400, v2\n"
      "v_xor_b32 v228, v228, v2\n"
      "v_bfe_i32 v2, v0, 5, 1\n"
      "v_and_b32 v2, 0x100, v2\n"
      "v_xor_b32 v228, v228, v2\n"
      "v_bfe_i32 v2, v0, 6, 1\n"
      "v_and_b32 v2, 0x400, v2\n"
      "v_xor_b32 v228, v228, v2\n"
      "v_bfe_i32 v2, v0, 7, 1\n"
      "v_and_b32 v2, 0x2000, v2\n"
      "v_xor_b32 v228, v228, v2\n"
      "v_xor_b32 v231, 0x280, v228\n"
      "v_xor_b32 v230, 0x200, v228\n"
      "v_xor_b32 v229, 0x80, v228\n"
      "v_add_u32 v228, 0xc00, v228\n"
      "v_add_u32 v229, 0x800, v229\n"
      "v_add_u32 v230, 0x400, v230\n"
      "s_sub_u32 s56, s56, 0xc00\n"
      "s_subb_u32 s57, s57, 0\n"
      "s_sub_u32 s32, s32, 0xc00\n"
      "s_subb_u32 s33, s33, 0\n"
      "s_xor_b64 s[32:33], s[32:33], s[56:57]\n"
      "s_mov_b32 s69, 0\n"
      "s_bitcmp1_b32 s44, 4\n"
      "s_cselect_b32 s66, 0x10000, 0\n"
      "s_xor_b32 s69, s69, s66\n"
      "s_bitcmp1_b32 s44, 0\n"
      "s_cselect_b32 s66, 0x800, 0\n"
      "s_xor_b32 s69, s69, s66\n"
      "s_bitcmp1_b32 s44, 2\n"
      "s_cselect_b32 s66, 0x40800, 0\n"
      "s_xor_b32 s69, s69, s66\n"
      "s_bitcmp1_b32 s44, 4\n"
      "s_cselect_b32 s66, 0x4000, 0\n"
      "s_xor_b32 s69, s69, s66\n"
      "s_mov_b32 s60, s69\n"
      "s_mov_b32 s62, s50\n"
      "s_mov_b64 s[30:31], s[22:23]\n"
      "s_add_u32 s28, s12, s71\n"
      "s_addc_u32 s29, s13, s72\n"
      "s_lshl_b32 s69, s43, 13\n"
      "s_xor_b32 s69, s69, 0x2000\n"
      "v_add_u32 v2, s69, v241\n"
      "ds_write_b128 v241, v[192:195] offset:28672\n"
      "ds_write_b128 v2, v[192:195] offset:49152\n"
      "ds_write_b128 v2, v[192:195] offset:53248\n"
      "v_and_b32 v241, 0x3f0, v241\n"
      "s_and_b32 s91, s50, 8192\n"
      "v_lshlrev_b32 v4, 8, v0\n"
      "v_and_b32 v4, 0x800, v4\n"
      "v_lshlrev_b32 v5, 7, v0\n"
      "v_and_b32 v6, 0x380, v5\n"
      "v_or3_b32 v4, v4, v6, s91\n"
      "v_and_b32 v6, 0x60, v241\n"
      "v_and_b32 v2, 48, v0\n"
      "v_xor_b32 v6, v6, v2\n"
      "v_or_b32 v242, v4, v6\n"
      "v_xor_b32 v243, 64, v242\n"
      "v_lshlrev_b32 v4, 6, v0\n"
      "v_and_b32 v6, 0x800, v4\n"
      "v_lshlrev_b32 v2, 5, v0\n"
      "v_and_b32 v2, 0x380, v2\n"
      "s_add_u32 s69, s91, 16384\n"
      "v_or3_b32 v6, v6, v2, s69\n"
      "v_lshlrev_b32 v2, 3, v0\n"
      "v_and_or_b32 v6, v2, 24, v6\n"
      "v_lshlrev_b32 v2, 2, v0\n"
      "v_and_b32 v2, 0x60, v2\n"
      "v_xor_b32 v244, v6, v2\n"
      "v_xor_b32 v245, 0x20, v244\n"
      "v_xor_b32 v246, 0x40, v244\n"
      "v_xor_b32 v247, 0x60, v244\n"
      "v_and_b32 v5, 0x1e00, v5\n"
      "v_and_or_b32 v250, v241, 48, v5\n"
      "s_lshl_b32 s69, %[blk], 9\n"
      "s_and_b32 s69, s69, 0x1e00\n"
      "v_and_b32 v4, 0x1c0, v4\n"
      "v_or_b32 v251, s69, v4\n"
      "s_and_b32 s69, s50, 4096\n"
      "s_lshl_b32 s69, s69, 2\n"
      "s_add_u32 s69, s69, 131072\n"
      "s_sub_u32 s91, 16384, s91\n"
      "v_add_u32 v196, s69, v242\n"
      "v_add_u32 v198, s91, v242\n"
      "v_xor_b32 v214, 0x2000, v196\n"
      "v_add_u32 v197, s69, v243\n"
      "v_add_u32 v199, s91, v243\n"
      "v_xor_b32 v215, 0x2000, v197\n"
      "v_and_b32 v3, 1, v0\n"
      "v_bfe_u32 v4, v0, 1, 5\n"
      "v_lshlrev_b32 v3, 1, v3\n"
      "v_sub_u32 v4, v4, v3\n"
      "v_mov_b32 v219, s60\n"
      "v_bfe_i32 v2, v4, 0, 1\n"
      "v_and_b32 v2, 0x10000, v2\n"
      "v_xor_b32 v219, v219, v2\n"
      "v_bfe_i32 v2, v4, 1, 1\n"
      "v_and_b32 v2, 0x800, v2\n"
      "v_xor_b32 v219, v219, v2\n"
      "v_bfe_i32 v2, v4, 2, 1\n"
      "v_and_b32 v2, 0x40800, v2\n"
      "v_xor_b32 v219, v219, v2\n"
      "v_bfe_i32 v2, v4, 3, 1\n"
      "v_and_b32 v2, 0x80000, v2\n"
      "v_xor_b32 v219, v219, v2\n"
      "v_bfe_i32 v2, v4, 4, 1\n"
      "v_and_b32 v2, 0x4000, v2\n"
      "v_xor_b32 v219, v219, v2\n"
      "v_writelane_b32 v219, 0, 1\n"
      "s_mov_b32 s61, 0\n"
      "s_add_u32 s69, s50, 131072\n"
      "v_add_u32 v210, s69, v241\n"
      "v_and_b32 v2, 3, v0\n"
      "v_mov_b32 v4, 0x3f803f80\n"
      "v_cmp_eq_u32 vcc, 0, v2\n"
      "s_nop 1\n"
      "v_cndmask_b32 v5, 0, v4, vcc\n"
      "v_accvgpr_write_b32 a128, v5\n"
      "v_accvgpr_write_b32 a129, v5\n"
      "v_accvgpr_write_b32 a130, v5\n"
      "v_accvgpr_write_b32 a131, v5\n"
      "v_cmp_eq_u32 vcc, 1, v2\n"
      "s_nop 1\n"
      "v_cndmask_b32 v5, 0, v4, vcc\n"
      "v_accvgpr_write_b32 a132, v5\n"
      "v_accvgpr_write_b32 a133, v5\n"
      "v_accvgpr_write_b32 a134, v5\n"
      "v_accvgpr_write_b32 a135, v5\n"
      "v_cmp_eq_u32 vcc, 2, v2\n"
      "s_nop 1\n"
      "v_cndmask_b32 v5, 0, v4, vcc\n"
      "v_accvgpr_write_b32 a136, v5\n"
      "v_accvgpr_write_b32 a137, v5\n"
      "v_accvgpr_write_b32 a138, v5\n"
      "v_accvgpr_write_b32 a139, v5\n"
      "v_cmp_eq_u32 vcc, 3, v2\n"
      "s_nop 1\n"
      "v_cndmask_b32 v5, 0, v4, vcc\n"
      "v_accvgpr_write_b32 a140, v5\n"
      "v_accvgpr_write_b32 a141, v5\n"
      "v_accvgpr_write_b32 a142, v5\n"
      "v_accvgpr_write_b32 a143, v5\n"
      "v_mfma_f32_32x32x16_bf16 a[144:159], v[192:195], v[192:195], 0\n"
      "v_mfma_f32_32x32x16_bf16 a[160:175], v[192:195], v[192:195], 0\n"
      "v_mfma_f32_32x32x16_bf16 a[176:191], v[192:195], v[192:195], 0\n"
      "v_mfma_f32_32x32x16_bf16 a[192:207], v[192:195], v[192:195], 0\n"
      "v_mfma_f32_32x32x16_bf16 v[0:15], v[192:195], v[192:195], 0\n"
      "v_mfma_f32_32x32x16_bf16 v[16:31], v[192:195], v[192:195], 0\n"
      "v_mfma_f32_32x32x16_bf16 v[32:47], v[192:195], v[192:195], 0\n"
      "v_mfma_f32_32x32x16_bf16 v[48:63], v[192:195], v[192:195], 0\n"
      "v_mfma_f32_32x32x16_bf16 v[128:143], v[192:195], v[192:195], 0\n"
      "v_mov_b64 v[180:181], 0\n"
      "v_mov_b64 v[182:183], 0\n"
      "v_mov_b64 v[184:185], 0\n"
      "v_mov_b64 v[186:187], 0\n"
      "s_mov_b32 s94, 0x3e0293ee\n"
      "s_mov_b32 s95, s94\n"
      "s_lshl_b32 s69, s43, 13\n"
      "s_add_u32 s45, s69, s75\n"
      "s_mov_b32 m0, s62\n"
      "s_nop 0\n"
      "buffer_load_dwordx4 v228, s[56:59], s60 offen nt lds\n"
      "buffer_load_dwordx4 v229, s[56:59], s60 offen offset:1024 nt lds\n"
      "buffer_load_dwordx4 v230, s[56:59], s60 offen offset:2048 nt lds\n"
      "buffer_load_dwordx4 v231, s[56:59], s60 offen offset:3072 nt lds\n"
      "s_xor_b64 s[56:57], s[56:57], s[32:33]\n"
      "s_add_u32 s61, s61, 1\n"
      "s_add_u32 s62, s62, 0x4000\n"
      "s_and_b32 s62, s62, 0x1ffff\n"
      "v_readlane_b32 s60, v219, s61\n"
      "s_xor_b64 s[56:57], s[56:57], s[32:33]\n"
      "s_add_u32 s61, s61, 1\n"
      "s_add_u32 s62, s62, 0x4000\n"
      "s_and_b32 s62, s62, 0x1ffff\n"
      "v_readlane_b32 s60, v219, s61\n"
      "s_waitcnt vmcnt(8)\n"
      "s_memrealtime s[68:69]\n"
      "ds_read_b128 v[64:67], v210 offset:0\n"
      "ds_read_b128 v[68:71], v210 offset:1024\n"
      "ds_read_b128 v[72:75], v210 offset:2048\n"
      "ds_read_b128 v[76:79], v210 offset:3072\n"
      "ds_read_b128 v[80:83], v210 offset:16384\n"
      "ds_read_b128 v[84:87], v210 offset:17408\n"
      "ds_read_b128 v[88:91], v210 offset:18432\n"
      "ds_read_b128 v[92:95], v210 offset:19456\n"
      "s_add_u32 s20, s18, 65536\n"
      "s_addc_u32 s21, s19, 0\n"
      "s_lshl_b32 s75, %[blk], 6\n"
      "s_add_u32 s75, s75, s50\n"
      "s_waitcnt lgkmcnt(0)\n"
      "s_sub_u32 s67, s68, s82\n"
      "s_min_u32 s67, s67, 165\n"
      "s_add_u32 s93, s82, s67\n"
      "s_sub_u32 s71, s68, 400\n"
      "s_cmp_lt_u32 s75, 512\n"
      "s_cbranch_scc0 NOLEAD_%=\n"
      "v_mov_b32 v188, s68\n"
      "v_mov_b32 v189, s68\n"
      "v_mov_b32 v190, s68\n"
      "v_mov_b32 v191, s68\n"
      "buffer_store_dwordx4 v[188:191], v250, s[20:23], s75 offen sc1\n"
      "s_nop 1\n"
      "NOLEAD_%=:\n"
      "s_mov_b32 m0, s62\n"
      "s_nop 0\n"
      "buffer_load_dwordx4 v228, s[56:59], s60 offen nt lds\n"
      "buffer_load_dwordx4 v229, s[56:59], s60 offen offset:1024 nt lds\n"
      "buffer_load_dwordx4 v230, s[56:59], s60 offen offset:2048 nt lds\n"
      "buffer_load_dwordx4 v231, s[56:59], s60 offen offset:3072 nt lds\n"
      "s_xor_b64 s[56:57], s[56:57], s[32:33]\n"
      "s_add_u32 s61, s61, 1\n"
      "s_add_u32 s62, s62, 0x4000\n"
      "s_and_b32 s62, s62, 0x1ffff\n"
      "v_readlane_b32 s60, v219, s61\n"
      "s_xor_b64 s[56:57], s[56:57], s[32:33]\n"
      "s_add_u32 s61, s61, 1\n"
      "s_add_u32 s62, s62, 0x4000\n"
      "s_and_b32 s62, s62, 0x1ffff\n"
      "v_readlane_b32 s60, v219, s61\n"
      "s_add_u32 m0, s45, 49152\n"
      "s_nop 0\n"
      "buffer_load_dwordx4 v248, s[28:31], 0 offen nt lds\n"
      "s_bitset1_b32 m0, 10\n"
      "s_nop 0\n"
      "buffer_load_dwordx4 v249, s[28:31], 0 offen nt lds\n"
      "v_lshlrev_b32 v188, 16, v64\n"
      "v_and_b32 v189, 0xffff0000, v64\n"
      "v_lshlrev_b32 v190, 16, v65\n"
      "v_and_b32 v191, 0xffff0000, v65\n"
      "v_lshlrev_b32 v192, 16, v66\n"
      "v_and_b32 v193, 0xffff0000, v66\n"
      "v_lshlrev_b32 v194, 16, v67\n"
      "v_and_b32 v195, 0xffff0000, v67\n"
      "v_pk_mul_f32 v[188:189], v[188:189], s[94:95]\n"
      "v_pk_mul_f32 v[190:191], v[190:191], s[94:95]\n"
      "v_pk_mul_f32 v[192:193], v[192:193], s[94:95]\n"
      "v_pk_mul_f32 v[194:195], v[194:195], s[94:95]\n"
      "v_cvt_pk_bf16_f32 v64, v188, v189\n"
      "v_cvt_pk_bf16_f32 v65, v190, v191\n"
      "v_cvt_pk_bf16_f32 v66, v192, v193\n"
      "v_cvt_pk_bf16_f32 v67, v194, v195\n"
      "ds_write_b128 v210, v[64:67] offset:0\n"
      "v_lshlrev_b32 v188, 16, v68\n"
      "v_and_b32 v189, 0xffff0000, v68\n"
      "v_lshlrev_b32 v190, 16, v69\n"
      "v_and_b32 v191, 0xffff0000, v69\n"
      "v_lshlrev_b32 v192, 16, v70\n"
      "v_and_b32 v193, 0xffff0000, v70\n"
      "v_lshlrev_b32 v194, 16, v71\n"
      "v_and_b32 v195, 0xffff0000, v71\n"
      "v_pk_mul_f32 v[188:189], v[188:189], s[94:95]\n"
      "v_pk_mul_f32 v[190:191], v[190:191], s[94:95]\n"
      "v_pk_mul_f32 v[192:193], v[192:193], s[94:95]\n"
      "v_pk_mul_f32 v[194:195], v[194:195], s[94:95]\n"
      "v_cvt_pk_bf16_f32 v68, v188, v189\n"
      "v_cvt_pk_bf16_f32 v69, v190, v191\n"
      "v_cvt_pk_bf16_f32 v70, v192, v193\n"
      "v_cvt_pk_bf16_f32 v71, v194, v195\n"
      "ds_write_b128 v210, v[68:71] offset:1024\n"
      "v_lshlrev_b32 v188, 16, v72\n"
      "v_and_b32 v189, 0xffff0000, v72\n"
      "v_lshlrev_b32 v190, 16, v73\n"
      "v_and_b32 v191, 0xffff0000, v73\n"
      "v_lshlrev_b32 v192, 16, v74\n"
      "v_and_b32 v193, 0xffff0000, v74\n"
      "v_lshlrev_b32 v194, 16, v75\n"
      "v_and_b32 v195, 0xffff0000, v75\n"
      "v_pk_mul_f32 v[188:189], v[188:189], s[94:95]\n"
      "v_pk_mul_f32 v[190:191], v[190:191], s[94:95]\n"
      "v_pk_mul_f32 v[192:193], v[192:193], s[94:95]\n"
      "v_pk_mul_f32 v[194:195], v[194:195], s[94:95]\n"
      "v_cvt_pk_bf16_f32 v72, v188, v189\n"
      "v_cvt_pk_bf16_f32 v73, v190, v191\n"
      "v_cvt_pk_bf16_f32 v74, v192, v193\n"
      "v_cvt_pk_bf16_f32 v75, v194, v195\n"
      "ds_write_b128 v210, v[72:75] offset:2048\n"
      "v_lshlrev_b32 v188, 16, v76\n"
      "v_and_b32 v189, 0xffff0000, v76\n"
      "v_lshlrev_b32 v190, 16, v77\n"
      "v_and_b32 v191, 0xffff0000, v77\n"
      "v_lshlrev_b32 v192, 16, v78\n"
      "v_and_b32 v193, 0xffff0000, v78\n"
      "v_lshlrev_b32 v194, 16, v79\n"
      "v_and_b32 v195, 0xffff0000, v79\n"
      "v_pk_mul_f32 v[188:189], v[188:189], s[94:95]\n"
      "v_pk_mul_f32 v[190:191], v[190:191], s[94:95]\n"
      "v_pk_mul_f32 v[192:193], v[192:193], s[94:95]\n"
      "v_pk_mul_f32 v[194:195], v[194:195], s[94:95]\n"
      "v_cvt_pk_bf16_f32 v76, v188, v189\n"
      "v_cvt_pk_bf16_f32 v77, v190, v191\n"
      "v_cvt_pk_bf16_f32 v78, v192, v193\n"
      "v_cvt_pk_bf16_f32 v79, v194, v195\n"
      "ds_write_b128 v210, v[76:79] offset:3072\n"
      "v_lshlrev_b32 v188, 16, v80\n"
      "v_and_b32 v189, 0xffff0000, v80\n"
      "v_lshlrev_b32 v190, 16, v81\n"
      "v_and_b32 v191, 0xffff0000, v81\n"
      "v_lshlrev_b32 v192, 16, v82\n"
      "v_and_b32 v193, 0xffff0000, v82\n"
      "v_lshlrev_b32 v194, 16, v83\n"
      "v_and_b32 v195, 0xffff0000, v83\n"
      "v_pk_mul_f32 v[188:189], v[188:189], s[94:95]\n"
      "v_pk_mul_f32 v[190:191], v[190:191], s[94:95]\n"
      "v_pk_mul_f32 v[192:193], v[192:193], s[94:95]\n"
      "v_pk_mul_f32 v[194:195], v[194:195], s[94:95]\n"
      "v_cvt_pk_bf16_f32 v80, v188, v189\n"
      "v_cvt_pk_bf16_f32 v81, v190, v191\n"
      "v_cvt_pk_bf16_f32 v82, v192, v193\n"
      "v_cvt_pk_bf16_f32 v83, v194, v195\n"
      "ds_write_b128 v210, v[80:83] offset:16384\n"
      "v_lshlrev_b32 v188, 16, v84\n"
      "v_and_b32 v189, 0xffff0000, v84\n"
      "v_lshlrev_b32 v190, 16, v85\n"
      "v_and_b32 v191, 0xffff0000, v85\n"
      "v_lshlrev_b32 v192, 16, v86\n"
      "v_and_b32 v193, 0xffff0000, v86\n"
      "v_lshlrev_b32 v194, 16, v87\n"
      "v_and_b32 v195, 0xffff0000, v87\n"
      "v_pk_mul_f32 v[188:189], v[188:189], s[94:95]\n"
      "v_pk_mul_f32 v[190:191], v[190:191], s[94:95]\n"
      "v_pk_mul_f32 v[192:193], v[192:193], s[94:95]\n"
      "v_pk_mul_f32 v[194:195], v[194:195], s[94:95]\n"
      "v_cvt_pk_bf16_f32 v84, v188, v189\n"
      "v_cvt_pk_bf16_f32 v85, v190, v191\n"
      "v_cvt_pk_bf16_f32 v86, v192, v193\n"
      "v_cvt_pk_bf16_f32 v87, v194, v195\n"
      "ds_write_b128 v210, v[84:87] offset:17408\n"
      "v_lshlrev_b32 v188, 16, v88\n"
      "v_and_b32 v189, 0xffff0000, v88\n"
      "v_lshlrev_b32 v190, 16, v89\n"
      "v_and_b32 v191, 0xffff0000, v89\n"
      "v_lshlrev_b32 v192, 16, v90\n"
      "v_and_b32 v193, 0xffff0000, v90\n"
      "v_lshlrev_b32 v194, 16, v91\n"
      "v_and_b32 v195, 0xffff0000, v91\n"
      "v_pk_mul_f32 v[188:189], v[188:189], s[94:95]\n"
      "v_pk_mul_f32 v[190:191], v[190:191], s[94:95]\n"
      "v_pk_mul_f32 v[192:193], v[192:193], s[94:95]\n"
      "v_pk_mul_f32 v[194:195], v[194:195], s[94:95]\n"
      "v_cvt_pk_bf16_f32 v88, v188, v189\n"
      "v_cvt_pk_bf16_f32 v89, v190, v191\n"
      "v_cvt_pk_bf16_f32 v90, v192, v193\n"
      "v_cvt_pk_bf16_f32 v91, v194, v195\n"
      "ds_write_b128 v210, v[88:91] offset:18432\n"
      "v_lshlrev_b32 v188, 16, v92\n"
      "v_and_b32 v189, 0xffff0000, v92\n"
      "v_lshlrev_b32 v190, 16, v93\n"
      "v_and_b32 v191, 0xffff0000, v93\n"
      "v_lshlrev_b32 v192, 16, v94\n"
      "v_and_b32 v193, 0xffff0000, v94\n"
      "v_lshlrev_b32 v194, 16, v95\n"
      "v_and_b32 v195, 0xffff0000, v95\n"
      "v_pk_mul_f32 v[188:189], v[188:189], s[94:95]\n"
      "v_pk_mul_f32 v[190:191], v[190:191], s[94:95]\n"
      "v_pk_mul_f32 v[192:193], v[192:193], s[94:95]\n"
      "v_pk_mul_f32 v[194:195], v[194:195], s[94:95]\n"
      "v_cvt_pk_bf16_f32 v92, v188, v189\n"
      "v_cvt_pk_bf16_f32 v93, v190, v191\n"
      "v_cvt_pk_bf16_f32 v94, v192, v193\n"
      "v_cvt_pk_bf16_f32 v95, v194, v195\n"
      "ds_write_b128 v210, v[92:95] offset:19456\n"
      "s_mov_b32 m0, s62\n"
      "s_nop 0\n"
      "buffer_load_dwordx4 v228, s[56:59], s60 offen nt lds\n"
      "buffer_load_dwordx4 v229, s[56:59], s60 offen offset:1024 nt lds\n"
      "buffer_load_dwordx4 v230, s[56:59], s60 offen offset:2048 nt lds\n"
      "buffer_load_dwordx4 v231, s[56:59], s60 offen offset:3072 nt lds\n"
      "s_xor_b64 s[56:57], s[56:57], s[32:33]\n"
      "s_add_u32 s61, s61, 1\n"
      "s_add_u32 s62, s62, 0x4000\n"
      "s_and_b32 s62, s62, 0x1ffff\n"
      "v_readlane_b32 s60, v219, s61\n"
      "s_cmp_eq_u32 s50, 0\n"
      "s_cselect_b32 s38, 0x44554001, 1\n"
      "s_cselect_b32 s39, 0x2a040083, 3\n"
      "s_cselect_b32 s52, 0x31109890, 0\n"
      "s_cselect_b32 s53, 0x80011, 0\n"
      "s_mov_b64 s[34:35], 0\n"
      "s_and_b32 s69, %[blk], 7\n"
      "s_mul_i32 s69, s69, 5\n"
      "s_lshr_b32 s74, %[blk], 4\n"
      "s_add_u32 s69, s69, s74\n"
      "s_and_b32 s69, s69, 15\n"
      "s_mul_i32 s74, s69, 16\n"
      "s_lshr_b32 s74, s74, 6\n"
      "s_lshl2_add_u32 s76, s74, 9\n"
      "s_bitset1_b64 s[34:35], s76\n"
      "s_lshl_b32 s74, s74, 1\n"
      "s_sub_u32 s74, 28, s74\n"
      "s_bitset1_b32 s39, s74\n"
      "s_mul_i32 s74, s69, 20\n"
      "s_lshr_b32 s74, s74, 6\n"
      "s_lshl2_add_u32 s76, s74, 25\n"
      "s_bitset1_b64 s[34:35], s76\n"
      "s_lshl_b32 s74, s74, 1\n"
      "s_sub_u32 s74, 20, s74\n"
      "s_bitset1_b32 s39, s74\n"
      "s_mul_i32 s74, s69, 20\n"
      "s_lshr_b32 s74, s74, 6\n"
      "s_lshl2_add_u32 s76, s74, 45\n"
      "s_bitset1_b64 s[34:35], s76\n"
      "s_lshl_b32 s74, s74, 1\n"
      "s_sub_u32 s74, 10, s74\n"
      "s_bitset1_b32 s39, s74\n"
      "s_mov_b32 s54, 3\n"
      "s_mov_b32 s70, 250\n"
      "s_mov_b32 s86, 59\n"
      "s_mov_b32 s77, 177\n"
      "s_lshl_b32 s91, s50, 16\n"
      "s_sub_u32 s80, 46, s91\n"
      "s_sub_u32 s90, 34, s91\n"
      "s_sub_u32 s72, s75, 14336\n"
      "s_and_b32 s79, %[blk], 31\n"
      "s_lshl_b32 s79, s79, 8\n"
      "s_add_u32 s79, s79, 8192\n"
      "s_mul_i32 s69, %[blk], 53\n"
      "s_lshr_b32 s69, s69, 8\n"
      "s_add_u32 s66, s93, 150\n"
      "s_sub_u32 s81, s93, s71\n"
      "s_sub_u32 s81, s81, s69\n"
      "s_add_u32 s81, s81, 24\n"
      "s_sub_u32 s66, s66, s91\n"
      "s_mov_b32 s88, s66\n"
      "s_mov_b32 s65, s66\n"
      "s_getpc_b64 s[84:85]\n"
      "PC_%=:\n"
      "s_add_u32 s84, s84, HOOK_%=-PC_%=\n"
      "s_addc_u32 s85, s85, 0\n"
      "s_waitcnt vmcnt(14) lgkmcnt(0)\n"
      "s_barrier\n"
      "ds_read_b128 a[64:67], v198 offset:8192\n"
      "ds_read_b128 a[72:75], v199 offset:8192\n"
      "ds_read_b128 a[80:83], v198 offset:9216\n"
      "ds_read_b128 a[88:91], v199 offset:9216\n"
      "ds_read_b128 a[0:3], v196 offset:0\n"
      "ds_read_b128 a[16:19], v197 offset:0\n"
      "ds_read_b128 a[32:35], v196 offset:1024\n"
      "ds_read_b128 a[48:51], v197 offset:1024\n"
      "ds_read_b128 a[4:7], v196 offset:4096\n"
      "ds_read_b128 a[20:23], v197 offset:4096\n"
      "ds_read_b128 a[36:39], v196 offset:5120\n"
      "ds_read_b128 a[52:55], v197 offset:5120\n"
      "v_readfirstlane_b32 s40, v208\n"
      "s_mov_b32 s94, 0xceabfb8f\n"
      "v_lshlrev_b32 v144, 16, v200\n"
      "v_and_b32 v145, 0xffff0000, v200\n"
      "v_lshlrev_b32 v146, 16, v201\n"
      "v_and_b32 v147, 0xffff0000, v201\n"
      "v_lshlrev_b32 v148, 16, v202\n"
      "v_and_b32 v149, 0xffff0000, v202\n"
      "v_lshlrev_b32 v150, 16, v203\n"
      "v_and_b32 v151, 0xffff0000, v203\n"
      "v_lshlrev_b32 v152, 16, v204\n"
      "v_and_b32 v153, 0xffff0000, v204\n"
      "v_lshlrev_b32 v154, 16, v205\n"
      "v_and_b32 v155, 0xffff0000, v205\n"
      "v_lshlrev_b32 v156, 16, v206\n"
      "v_and_b32 v157, 0xffff0000, v206\n"
      "v_lshlrev_b32 v158, 16, v207\n"
      "v_and_b32 v159, 0xffff0000, v207\n"
      "s_waitcnt lgkmcnt(4)\n"
      "v_mfma_f32_16x16x32_bf16 v[64:67], a[64:67], a[0:3], 0\n"
      "ds_read_b128 a[8:11], v214 offset:0\n"
      "v_fma_f32 v144, -v144, s94, s94\n"
      "v_mfma_f32_16x16x32_bf16 v[64:67], a[72:75], a[16:19], v[64:67]\n"
      "ds_read_b128 a[24:27], v215 offset:0\n"
      "v_fma_f32 v145, -v145, s94, s94\n"
      "v_mfma_f32_16x16x32_bf16 v[64:67], a[80:83], a[32:35], v[64:67]\n"
      "ds_read_b128 a[40:43], v214 offset:1024\n"
      "v_fma_f32 v146, -v146, s94, s94\n"
      "v_mfma_f32_16x16x32_bf16 v[64:67], a[88:91], a[48:51], v[64:67]\n"
      "ds_read_b128 a[56:59], v215 offset:1024\n"
      "v_fma_f32 v147, -v147, s94, s94\n"
      "s_waitcnt lgkmcnt(4)\n"
      "v_mfma_f32_16x16x32_bf16 v[72:75], a[64:67], a[4:7], 0\n"
      "ds_read_b128 a[12:15], v214 offset:4096\n"
      "v_fma_f32 v148, -v148, s94, s94\n"
      "v_mfma_f32_16x16x32_bf16 v[72:75], a[72:75], a[20:23], v[72:75]\n"
      "ds_read_b128 a[28:31], v215 offset:4096\n"
      "v_fma_f32 v149, -v149, s94, s94\n"
      "v_mfma_f32_16x16x32_bf16 v[72:75], a[80:83], a[36:39], v[72:75]\n"
      "ds_read_b128 a[44:47], v214 offset:5120\n"
      "v_fma_f32 v150, -v150, s94, s94\n"
      "v_mfma_f32_16x16x32_bf16 v[72:75], a[88:91], a[52:55], v[72:75]\n"
      "ds_read_b128 a[60:63], v215 offset:5120\n"
      "v_fma_f32 v151, -v151, s94, s94\n"
      "s_waitcnt lgkmcnt(4)\n"
      "v_mfma_f32_16x16x32_bf16 v[80:83], a[64:67], a[8:11], 0\n"
      "ds_read_b128 a[96:99], v198 offset:0\n"
      "v_fma_f32 v152, -v152, s94, s94\n"
      "v_mfma_f32_16x16x32_bf16 v[80:83], a[72:75], a[24:27], v[80:83]\n"
      "ds_read_b128 a[100:103], v198 offset:4096\n"
      "v_fma_f32 v153, -v153, s94, s94\n"
      "v_mfma_f32_16x16x32_bf16 v[80:83], a[80:83], a[40:43], v[80:83]\n"
      "ds_read_b128 a[104:107], v199 offset:0\n"
      "v_fma_f32 v154, -v154, s94, s94\n"
      "v_mfma_f32_16x16x32_bf16 v[80:83], a[88:91], a[56:59], v[80:83]\n"
      "ds_read_b128 a[108:111], v199 offset:4096\n"
      "v_fma_f32 v155, -v155, s94, s94\n"
      "s_waitcnt lgkmcnt(4)\n"
      "v_mfma_f32_16x16x32_bf16 v[88:91], a[64:67], a[12:15], 0\n"
      "ds_read_b128 a[112:115], v198 offset:1024\n"
      "v_fma_f32 v156, -v156, s94, s94\n"
      "v_mfma_f32_16x16x32_bf16 v[88:91], a[72:75], a[28:31], v[88:91]\n"
      "ds_read_b128 a[116:119], v198 offset:5120\n"
      "v_fma_f32 v157, -v157, s94, s94\n"
      "v_mfma_f32_16x16x32_bf16 v[88:91], a[80:83], a[44:47], v[88:91]\n"
      "ds_read_b128 a[120:123], v199 offset:1024\n"
      "v_fma_f32 v158, -v158, s94, s94\n"
      "v_mfma_f32_16x16x32_bf16 v[88:91], a[88:91], a[60:63], v[88:91]\n"
      "ds_read_b128 a[124:127], v199 offset:5120\n"
      "v_fma_f32 v159, -v159, s94, s94\n"
      "s_mov_b32 m0, s62\n"
      "s_nop 0\n"
      "buffer_load_dwordx4 v228, s[56:59], s60 offen nt lds\n"
      "buffer_load_dwordx4 v229, s[56:59], s60 offen offset:1024 nt lds\n"
      "buffer_load_dwordx4 v230, s[56:59], s60 offen offset:2048 nt lds\n"
      "buffer_load_dwordx4 v231, s[56:59], s60 offen offset:3072 nt lds\n"
      "s_waitcnt lgkmcnt(0)\n"
      "v_mfma_f32_16x16x32_bf16 v[96:99], a[96:99], a[0:3], v[144:147]\n"
      "s_add_u32 s40, s40, 0x7f4a7c15\n"
      "v_mfma_f32_16x16x32_bf16 v[96:99], a[104:107], a[16:19], v[96:99]\n"
      "s_lshl_b32 s69, s44, 3\n"
      "v_max3_f32 v188, v64, v65, v66\n"
      "v_max3_f32 v189, v72, v73, v74\n"
      "v_mfma_f32_16x16x32_bf16 v[96:99], a[112:115], a[32:35], v[96:99]\n"
      "s_lshr_b32 s74, s50, 12\n"
      "v_max3_f32 v190, v80, v81, v82\n"
      "v_max3_f32 v191, v88, v89, v90\n"
      "v_mfma_f32_16x16x32_bf16 v[96:99], a[120:123], a[48:51], v[96:99]\n"
      "s_add_u32 s69, s69, s74\n"
      "v_max_f32 v188, v188, v67\n"
      "v_max_f32 v189, v189, v75\n"
      "v_mfma_f32_16x16x32_bf16 v[104:107], a[96:99], a[4:7], v[152:155]\n"
      "s_mul_i32 s69, s69, 4608\n"
      "v_max_f32 v190, v190, v83\n"
      "v_max_f32 v191, v191, v91\n"
      "v_mfma_f32_16x16x32_bf16 v[104:107], a[104:107], a[20:23], v[104:107]\n"
      "s_add_u32 s69, s69, 81920\n"
      "v_mov_b32 v192, v188\n"
      "v_mov_b32 v193, v189\n"
      "v_mfma_f32_16x16x32_bf16 v[104:107], a[112:115], a[36:39], v[104:107]\n"
      "s_add_u32 s24, s18, s69\n"
      "v_mov_b32 v194, v190\n"
      "v_mov_b32 v195, v191\n"
      "v_mfma_f32_16x16x32_bf16 v[104:107], a[120:123], a[52:55], v[104:107]\n"
      "s_addc_u32 s25, s19, 0\n"
      "v_permlane16_swap_b32 v188, v192\n"
      "v_permlane16_swap_b32 v189, v193\n"
      "v_mfma_f32_16x16x32_bf16 v[112:115], a[96:99], a[8:11], v[144:147]\n"
      "s_movk_i32 s26, 23040\n"
      "v_permlane16_swap_b32 v190, v194\n"
      "v_permlane16_swap_b32 v191, v195\n"
      "v_mfma_f32_16x16x32_bf16 v[112:115], a[104:107], a[24:27], v[112:115]\n"
      "s_mul_i32 s78, s43, 18432\n"
      "v_max_f32 v188, v188, v192\n"
      "v_max_f32 v189, v189, v193\n"
      "v_mfma_f32_16x16x32_bf16 v[112:115], a[112:115], a[40:43], v[112:115]\n"
      "s_and_b32 s69, s50, 4096\n"
      "v_max_f32 v190, v190, v194\n"
      "v_max_f32 v191, v191, v195\n"
      "v_mfma_f32_16x16x32_bf16 v[112:115], a[120:123], a[56:59], v[112:115]\n"
      "s_lshl_b32 s69, s69, 2\n"
      "v_mov_b32 v192, v188\n"
      "v_mov_b32 v193, v189\n"
      "v_mfma_f32_16x16x32_bf16 v[120:123], a[96:99], a[12:15], v[152:155]\n"
      "s_and_b32 s74, s50, 8192\n"
      "v_mov_b32 v194, v190\n"
      "v_mov_b32 v195, v191\n"
      "v_mfma_f32_16x16x32_bf16 v[120:123], a[104:107], a[28:31], v[120:123]\n"
      "s_add_u32 s74, s74, 4096\n"
      "v_permlane32_swap_b32 v188, v192\n"
      "v_permlane32_swap_b32 v189, v193\n"
      "v_mfma_f32_16x16x32_bf16 v[120:123], a[112:115], a[44:47], v[120:123]\n"
      "s_add_u32 s69, s69, s74\n"
      "v_permlane32_swap_b32 v190, v194\n"
      "v_permlane32_swap_b32 v191, v195\n"
      "v_mfma_f32_16x16x32_bf16 v[120:123], a[120:123], a[60:63], v[120:123]\n"
      "s_lshl_b32 s74, s43, 7\n"
      "v_max_f32 v188, v188, v192\n"
      "v_max_f32 v189, v189, v193\n"
      "v_mfma_f32_16x16x32_bf16 v[100:103], a[100:103], a[0:3], v[148:151]\n"
      "s_add_u32 s69, s69, s74\n"
      "v_max_f32 v190, v190, v194\n"
      "v_max_f32 v191, v191, v195\n"
      "v_mfma_f32_16x16x32_bf16 v[100:103], a[108:111], a[16:19], v[100:103]\n"
      "s_lshl_b32 s74, s44, 15\n"
      "v_xor_b32 v176, 0x80000000, v188\n"
      "v_xor_b32 v177, 0x80000000, v189\n"
      "v_mfma_f32_16x16x32_bf16 v[100:103], a[116:119], a[32:35], v[100:103]\n"
      "s_add_u32 s69, s69, s74\n"
      "v_xor_b32 v178, 0x80000000, v190\n"
      "v_xor_b32 v179, 0x80000000, v191\n"
      "v_mfma_f32_16x16x32_bf16 v[100:103], a[124:127], a[48:51], v[100:103]\n"
      "s_add_u32 s30, s16, s69\n"
      "v_mov_b32 v160, v176\n"
      "v_mov_b32 v161, v176\n"
      "v_mfma_f32_16x16x32_bf16 v[108:111], a[100:103], a[4:7], v[156:159]\n"
      "s_addc_u32 s31, s17, 0\n"
      "v_mov_b32 v162, v176\n"
      "v_mov_b32 v163, v176\n"
      "v_mfma_f32_16x16x32_bf16 v[108:111], a[108:111], a[20:23], v[108:111]\n"
      "v_mov_b32 v164, v177\n"
      "v_mov_b32 v165, v177\n"
      "v_mfma_f32_16x16x32_bf16 v[108:111], a[116:119], a[36:39], v[108:111]\n"
      "v_mov_b32 v166, v177\n"
      "v_mov_b32 v167, v177\n"
      "v_mfma_f32_16x16x32_bf16 v[108:111], a[124:127], a[52:55], v[108:111]\n"
      "v_mov_b32 v168, v178\n"
      "v_mov_b32 v169, v178\n"
      "v_mfma_f32_16x16x32_bf16 v[116:119], a[100:103], a[8:11], v[148:151]\n"
      "v_mov_b32 v170, v178\n"
      "v_mov_b32 v171, v178\n"
      "v_mfma_f32_16x16x32_bf16 v[116:119], a[108:111], a[24:27], v[116:119]\n"
      "v_mov_b32 v172, v179\n"
      "v_mov_b32 v173, v179\n"
      "v_mfma_f32_16x16x32_bf16 v[116:119], a[116:119], a[40:43], v[116:119]\n"
      "v_mov_b32 v174, v179\n"
      "v_mov_b32 v175, v179\n"
      "v_mfma_f32_16x16x32_bf16 v[116:119], a[124:127], a[56:59], v[116:119]\n"
      "v_mov_b32 v209, 139248\n"
      "v_lshl_add_u32 v232, s50, 1, v241\n"
      "v_mfma_f32_16x16x32_bf16 v[124:127], a[100:103], a[12:15], v[156:159]\n"
      "v_add_u32 v232, 0x10000, v232\n"
      "v_xor_b32 v233, 0x4000, v232\n"
      "v_mfma_f32_16x16x32_bf16 v[124:127], a[108:111], a[28:31], v[124:127]\n"
      "v_add_u32 v234, 0x10000, v232\n"
      "v_xor_b32 v235, 0x4000, v234\n"
      "v_mfma_f32_16x16x32_bf16 v[124:127], a[116:119], a[44:47], v[124:127]\n"
      "v_lshrrev_b32 v240, 1, v241\n"
      "v_add_u32 v240, 0x1000, v240\n"
      "v_mfma_f32_16x16x32_bf16 v[124:127], a[124:127], a[60:63], v[124:127]\n"
      "v_mov_b32 v196, 0\n"
      "v_pk_add_f32 v[96:97], v[96:97], v[160:161]\n"
      "v_pk_add_f32 v[98:99], v[98:99], v[160:161]\n"
      "v_pk_add_f32 v[104:105], v[104:105], v[164:165]\n"
      "v_pk_add_f32 v[106:107], v[106:107], v[164:165]\n"
      "v_pk_add_f32 v[112:113], v[112:113], v[168:169]\n"
      "v_pk_add_f32 v[114:115], v[114:115], v[168:169]\n"
      "v_pk_add_f32 v[120:121], v[120:121], v[172:173]\n"
      "v_pk_add_f32 v[122:123], v[122:123], v[172:173]\n"
      "v_pk_add_f32 v[100:101], v[100:101], v[160:161]\n"
      "v_pk_add_f32 v[102:103], v[102:103], v[160:161]\n"
      "v_pk_add_f32 v[108:109], v[108:109], v[164:165]\n"
      "v_pk_add_f32 v[110:111], v[110:111], v[164:165]\n"
      "v_pk_add_f32 v[116:117], v[116:117], v[168:169]\n"
      "v_pk_add_f32 v[118:119], v[118:119], v[168:169]\n"
      "v_pk_add_f32 v[124:125], v[124:125], v[172:173]\n"
      "v_pk_add_f32 v[126:127], v[126:127], v[172:173]\n"
      "v_mov_b32 v224, v244\n"
      "v_mov_b32 v225, v245\n"
      "v_mov_b32 v226, v246\n"
      "v_mov_b32 v227, v247\n"
      "v_xor_b32 v220, 0x2000, v244\n"
      "v_xor_b32 v221, 0x2000, v245\n"
      "v_xor_b32 v222, 0x2000, v246\n"
      "v_xor_b32 v223, 0x2000, v247\n"
      "s_lshl_b32 s76, s43, 3\n"
      "v_mov_b32 v237, s40\n"
      "v_mov_b32 v238, s76\n"
      "v_xor_b32 v239, 8, v238\n"
      "s_memrealtime s[68:69]\n"
      "s_waitcnt vmcnt(14) lgkmcnt(0)\n"
      "s_sub_u32 s67, s68, s88\n"
      "s_cmp_lt_i32 s67, -8\n"
      "s_cbranch_scc1 GW0_%=\n"
      "GX0_%=:\n"
      "s_barrier\n"
      "ds_read_b128 a[64:67], v242 offset:0\n"
      "ds_read_b128 a[68:71], v242 offset:4096\n"
      "ds_read_b128 a[72:75], v243 offset:0\n"
      "ds_read_b128 a[76:79], v243 offset:4096\n"
      "ds_read_b128 a[80:83], v242 offset:1024\n"
      "ds_read_b128 a[84:87], v242 offset:5120\n"
      "ds_read_b128 a[88:91], v243 offset:1024\n"
      "ds_read_b128 a[92:95], v243 offset:5120\n"
      "ds_write_b32 v209, v196\n"
      "s_add_u32 s38, s38, s38\n"
      "s_memrealtime s[68:69]\n"
      "s_xor_b64 s[56:57], s[56:57], s[32:33]\n"
      "s_add_u32 s61, s61, 1\n"
      "s_add_u32 s62, s62, 0x4000\n"
      "s_and_b32 s62, s62, 0x1ffff\n"
      "v_readlane_b32 s60, v219, s61\n"
      "s_waitcnt lgkmcnt(0)\n"
      "s_sub_u32 s67, s68, s66\n"
      "s_cmp_gt_i32 s67, s70\n"
      "s_cselect_b32 s69, s77, s86\n"
      "s_cselect_b32 s70, 60, s70\n"
      "s_cselect_b32 s90, s80, s90\n"
      "s_add_u32 s66, s66, s69\n"
      "s_mov_b32 s82, s68\n"
      "LOOP_%=:\n"
      "v_mfma_f32_16x16x32_bf16 v[64:67], a[64:67], a[0:3], v[160:163]\n"
      "s_add_u32 s65, s68, s90\n"
      "v_exp_f32 v188, v96\n"
      "v_mfma_f32_16x16x32_bf16 v[64:67], a[72:75], a[16:19], v[64:67]\n"
      "s_max_u32 s88, s66, s65\n"
      "v_add_f32 v180, v180, v184\n"
      "v_add_f32 v181, v181, v185\n"
      "v_mfma_f32_16x16x32_bf16 v[64:67], a[80:83], a[32:35], v[64:67]\n"
      "v_exp_f32 v189, v97\n"
      "v_mfma_f32_16x16x32_bf16 v[64:67], a[88:91], a[48:51], v[64:67]\n"
      "s_getreg_b32 s67, hwreg(HW_REG_IB_STS, 0, 4)\n"
      "v_add_f32 v182, v182, v186\n"
      "v_add_f32 v183, v183, v187\n"
      "v_mfma_f32_16x16x32_bf16 v[72:75], a[64:67], a[4:7], v[164:167]\n"
      "s_cmp_gt_u32 s67, 4\n"
      "v_exp_f32 v190, v98\n"
      "v_mfma_f32_16x16x32_bf16 v[72:75], a[72:75], a[20:23], v[72:75]\n"
      "s_addc_u32 s89, s89, 0\n"
      "v_exp_f32 v191, v99\n"
      "v_mfma_f32_16x16x32_bf16 v[72:75], a[80:83], a[36:39], v[72:75]\n"
      "v_cvt_pk_bf16_f32 v144, v188, v189\n"
      "v_cvt_pk_bf16_f32 v145, v190, v191\n"
      "v_mfma_f32_16x16x32_bf16 v[72:75], a[88:91], a[52:55], v[72:75]\n"
      "v_exp_f32 v192, v100\n"
      "v_mfma_f32_16x16x32_bf16 v[80:83], a[64:67], a[8:11], v[168:171]\n"
      "s_add_u32 s39, s39, s39\n"
      "v_exp_f32 v193, v101\n"
      "v_mfma_f32_16x16x32_bf16 v[80:83], a[72:75], a[24:27], v[80:83]\n"
      "s_memrealtime s[68:69]\n"
      "v_exp_f32 v194, v102\n"
      "v_mfma_f32_16x16x32_bf16 v[80:83], a[80:83], a[40:43], v[80:83]\n"
      "v_exp_f32 v195, v103\n"
      "v_mfma_f32_16x16x32_bf16 v[80:83], a[88:91], a[56:59], v[80:83]\n"
      "s_mov_b32 m0, s62\n"
      "v_cvt_pk_bf16_f32 v146, v192, v193\n"
      "v_cvt_pk_bf16_f32 v147, v194, v195\n"
      "v_mfma_f32_16x16x32_bf16 v[88:91], a[64:67], a[12:15], v[172:175]\n"
      "buffer_load_dwordx4 v228, s[56:59], s60 offen nt lds\n"
      "v_mfma_f32_16x16x32_bf16 v[184:187], a[128:131], v[144:147], 0\n"
      "buffer_load_dwordx4 v229, s[56:59], s60 offen offset:1024 nt lds\n"
      "v_mfma_f32_16x16x32_bf16 v[88:91], a[72:75], a[28:31], v[88:91]\n"
      "buffer_load_dwordx4 v230, s[56:59], s60 offen offset:2048 nt lds\n"
      "v_mfma_f32_16x16x32_bf16 v[88:91], a[80:83], a[44:47], v[88:91]\n"
      "buffer_load_dwordx4 v231, s[56:59], s60 offen offset:3072 nt lds\n"
      "s_cbranch_scc1 SP1_%=\n"
      "HD1_%=:\n"
      "s_cmp_lg_u32 s54, 0\n"
      "s_waitcnt vmcnt(12) lgkmcnt(0)\n"
      "s_cbranch_scc1 W1_%=\n"
      "s_waitcnt vmcnt(8)\n"
      "W1_%=:\n"
      "s_sub_u32 s67, s68, s88\n"
      "s_cmp_lt_i32 s67, -8\n"
      "s_cbranch_scc1 GW4_%=\n"
      "GX4_%=:\n"
      "s_barrier\n"
      "v_mfma_f32_16x16x32_bf16 v[88:91], a[88:91], a[60:63], v[88:91]\n"
      "s_memrealtime s[68:69]\n"
      "ds_read_b64_tr_b16 a[96:97], v244 offset:0\n"
      "ds_read_b64_tr_b16 a[98:99], v244 offset:4096\n"
      "v_mfma_f32_16x16x32_bf16 v[68:71], a[68:71], a[0:3], v[160:163]\n"
      "s_xor_b64 s[56:57], s[56:57], s[32:33]\n"
      "ds_read_b64_tr_b16 a[100:101], v245 offset:0\n"
      "ds_read_b64_tr_b16 a[102:103], v245 offset:4096\n"
      "v_mfma_f32_16x16x32_bf16 v[68:71], a[76:79], a[16:19], v[68:71]\n"
      "s_add_u32 s61, s61, 1\n"
      "ds_read_b64_tr_b16 a[104:105], v246 offset:0\n"
      "ds_read_b64_tr_b16 a[106:107], v246 offset:4096\n"
      "v_mfma_f32_16x16x32_bf16 v[68:71], a[84:87], a[32:35], v[68:71]\n"
      "s_add_u32 s62, s62, 0x4000\n"
      "ds_read_b64_tr_b16 a[108:109], v247 offset:0\n"
      "ds_read_b64_tr_b16 a[110:111], v247 offset:4096\n"
      "v_mfma_f32_16x16x32_bf16 v[68:71], a[92:95], a[48:51], v[68:71]\n"
      "s_and_b32 s62, s62, 0x1ffff\n"
      "v_exp_f32 v188, v104\n"
      "v_mfma_f32_16x16x32_bf16 v[76:79], a[68:71], a[4:7], v[164:167]\n"
      "v_readlane_b32 s60, v219, s61\n"
      "v_exp_f32 v189, v105\n"
      "v_mfma_f32_16x16x32_bf16 v[76:79], a[76:79], a[20:23], v[76:79]\n"
      "v_exp_f32 v190, v106\n"
      "v_mfma_f32_16x16x32_bf16 v[76:79], a[84:87], a[36:39], v[76:79]\n"
      "v_exp_f32 v191, v107\n"
      "v_mfma_f32_16x16x32_bf16 v[76:79], a[92:95], a[52:55], v[76:79]\n"
      "v_cvt_pk_bf16_f32 v148, v188, v189\n"
      "v_cvt_pk_bf16_f32 v149, v190, v191\n"
      "v_mfma_f32_16x16x32_bf16 v[84:87], a[68:71], a[8:11], v[168:171]\n"
      "v_exp_f32 v192, v108\n"
      "v_mfma_f32_16x16x32_bf16 v[84:87], a[76:79], a[24:27], v[84:87]\n"
      "v_exp_f32 v193, v109\n"
      "s_waitcnt lgkmcnt(0)\n"
      "v_mfma_f32_16x16x32_bf16 v[84:87], a[84:87], a[40:43], v[84:87]\n"
      "s_sub_u32 s67, s68, s66\n"
      "ds_read_b64_tr_b16 a[112:113], v244 offset:1024\n"
      "v_cvt_pk_bf16_f32 v150, v192, v193\n"
      "v_mfma_f32_16x16x32_bf16 v[84:87], a[92:95], a[56:59], v[84:87]\n"
      "s_cmp_gt_i32 s67, s70\n"
      "v_exp_f32 v194, v110\n"
      "v_mfma_f32_16x16x32_bf16 v[92:95], a[68:71], a[12:15], v[172:175]\n"
      "s_cselect_b32 s69, s77, s86\n"
      "ds_read_b64_tr_b16 a[114:115], v244 offset:5120\n"
      "ds_read_b64_tr_b16 a[116:117], v245 offset:1024\n"
      "v_mfma_f32_16x16x32_bf16 v[92:95], a[76:79], a[28:31], v[92:95]\n"
      "s_cselect_b32 s70, 60, s70\n"
      "v_exp_f32 v195, v111\n"
      "v_mfma_f32_16x16x32_bf16 v[92:95], a[84:87], a[44:47], v[92:95]\n"
      "s_cselect_b32 s90, s80, s90\n"
      "ds_read_b64_tr_b16 a[118:119], v245 offset:5120\n"
      "v_cvt_pk_bf16_f32 v151, v194, v195\n"
      "v_mfma_f32_16x16x32_bf16 v[92:95], a[92:95], a[60:63], v[92:95]\n"
      "s_add_u32 s66, s66, s69\n"
      "v_exp_f32 v188, v112\n"
      "v_mfma_f32_16x16x32_bf16 v[184:187], a[132:135], v[148:151], v[184:187]\n"
      "s_mov_b32 s82, s68\n"
      "ds_read_b64_tr_b16 a[120:121], v246 offset:1024\n"
      "ds_read_b64_tr_b16 a[122:123], v246 offset:5120\n"
      "v_mfma_f32_16x16x32_bf16 v[0:3], a[96:99], v[128:131], v[0:3]\n"
      "s_add_u32 s65, s68, s90\n"
      "v_exp_f32 v189, v113\n"
      "v_mfma_f32_16x16x32_bf16 v[4:7], a[96:99], v[132:135], v[4:7]\n"
      "s_max_u32 s88, s66, s65\n"
      "ds_read_b64_tr_b16 a[124:125], v247 offset:1024\n"
      "v_cvt_pk_bf16_f32 v152, v188, v189\n"
      "v_mfma_f32_16x16x32_bf16 a[144:147], a[96:99], v[136:139], a[144:147]\n"
      "v_exp_f32 v190, v114\n"
      "v_mfma_f32_16x16x32_bf16 a[148:151], a[96:99], v[140:143], a[148:151]\n"
      "s_getreg_b32 s67, hwreg(HW_REG_IB_STS, 0, 4)\n"
      "v_exp_f32 v191, v115\n"
      "v_mfma_f32_16x16x32_bf16 a[156:159], a[100:103], v[140:143], a[156:159]\n"
      "s_cmp_gt_u32 s67, 4\n"
      "ds_read_b64_tr_b16 a[126:127], v247 offset:5120\n"
      "v_cvt_pk_bf16_f32 v153, v190, v191\n"
      "v_mfma_f32_16x16x32_bf16 a[152:155], a[100:103], v[136:139], a[152:155]\n"
      "s_addc_u32 s89, s89, 0\n"
      "v_exp_f32 v192, v116\n"
      "v_mfma_f32_16x16x32_bf16 v[12:15], a[100:103], v[132:135], v[12:15]\n"
      "v_exp_f32 v193, v117\n"
      "v_mfma_f32_16x16x32_bf16 v[8:11], a[100:103], v[128:131], v[8:11]\n"
      "v_exp_f32 v194, v118\n"
      "v_mfma_f32_16x16x32_bf16 v[16:19], a[104:107], v[128:131], v[16:19]\n"
      "s_add_u32 s38, s38, s38\n"
      "v_exp_f32 v195, v119\n"
      "v_mfma_f32_16x16x32_bf16 v[20:23], a[104:107], v[132:135], v[20:23]\n"
      "s_memrealtime s[68:69]\n"
      "v_cvt_pk_bf16_f32 v154, v192, v193\n"
      "v_cvt_pk_bf16_f32 v155, v194, v195\n"
      "v_mfma_f32_16x16x32_bf16 a[160:163], a[104:107], v[136:139], a[160:163]\n"
      "v_exp_f32 v188, v120\n"
      "v_mfma_f32_16x16x32_bf16 v[184:187], a[136:139], v[152:155], v[184:187]\n"
      "s_mov_b32 m0, s62\n"
      "v_exp_f32 v189, v121\n"
      "v_mfma_f32_16x16x32_bf16 a[164:167], a[104:107], v[140:143], a[164:167]\n"
      "buffer_load_dwordx4 v228, s[56:59], s60 offen nt lds\n"
      "v_mfma_f32_16x16x32_bf16 a[172:175], a[108:111], v[140:143], a[172:175]\n"
      "buffer_load_dwordx4 v229, s[56:59], s60 offen offset:1024 nt lds\n"
      "v_mfma_f32_16x16x32_bf16 a[168:171], a[108:111], v[136:139], a[168:171]\n"
      "buffer_load_dwordx4 v230, s[56:59], s60 offen offset:2048 nt lds\n"
      "v_mfma_f32_16x16x32_bf16 v[28:31], a[108:111], v[132:135], v[28:31]\n"
      "buffer_load_dwordx4 v231, s[56:59], s60 offen offset:3072 nt lds\n"
      "s_cbranch_scc1 SP2_%=\n"
      "HD2_%=:\n"
      "s_cmp_lg_u32 s54, 0\n"
      "s_waitcnt vmcnt(12) lgkmcnt(0)\n"
      "s_cbranch_scc1 W2_%=\n"
      "s_waitcnt vmcnt(8)\n"
      "W2_%=:\n"
      "s_sub_u32 s67, s68, s88\n"
      "s_cmp_lt_i32 s67, -8\n"
      "s_cbranch_scc1 GW5_%=\n"
      "GX5_%=:\n"
      "s_barrier\n"
      "v_mfma_f32_16x16x32_bf16 v[24:27], a[108:111], v[128:131], v[24:27]\n"
      "s_memrealtime s[68:69]\n"
      "ds_read_b128 a[64:67], v242 offset:32768\n"
      "ds_read_b128 a[72:75], v243 offset:32768\n"
      "v_mfma_f32_16x16x32_bf16 v[32:35], a[112:115], v[128:131], v[32:35]\n"
      "s_xor_b64 s[56:57], s[56:57], s[32:33]\n"
      "ds_read_b128 a[80:83], v242 offset:33792\n"
      "ds_read_b128 a[88:91], v243 offset:33792\n"
      "v_mfma_f32_16x16x32_bf16 v[36:39], a[112:115], v[132:135], v[36:39]\n"
      "s_add_u32 s61, s61, 1\n"
      "v_exp_f32 v190, v122\n"
      "v_mfma_f32_16x16x32_bf16 a[176:179], a[112:115], v[136:139], a[176:179]\n"
      "s_add_u32 s62, s62, 0x4000\n"
      "v_exp_f32 v191, v123\n"
      "v_mfma_f32_16x16x32_bf16 a[180:183], a[112:115], v[140:143], a[180:183]\n"
      "s_and_b32 s62, s62, 0x1ffff\n"
      "v_cvt_pk_bf16_f32 v156, v188, v189\n"
      "v_cvt_pk_bf16_f32 v157, v190, v191\n"
      "v_mfma_f32_16x16x32_bf16 a[188:191], a[116:119], v[140:143], a[188:191]\n"
      "v_readlane_b32 s60, v219, s61\n"
      "v_exp_f32 v192, v124\n"
      "v_mfma_f32_16x16x32_bf16 a[184:187], a[116:119], v[136:139], a[184:187]\n"
      "v_exp_f32 v193, v125\n"
      "v_mfma_f32_16x16x32_bf16 v[44:47], a[116:119], v[132:135], v[44:47]\n"
      "v_exp_f32 v194, v126\n"
      "v_mfma_f32_16x16x32_bf16 v[40:43], a[116:119], v[128:131], v[40:43]\n"
      "v_exp_f32 v195, v127\n"
      "v_mfma_f32_16x16x32_bf16 v[48:51], a[120:123], v[128:131], v[48:51]\n"
      "v_cvt_pk_bf16_f32 v158, v192, v193\n"
      "v_cvt_pk_bf16_f32 v159, v194, v195\n"
      "v_mfma_f32_16x16x32_bf16 v[52:55], a[120:123], v[132:135], v[52:55]\n"
      "s_waitcnt lgkmcnt(0)\n"
      "v_mfma_f32_16x16x32_bf16 v[184:187], a[140:143], v[156:159], v[184:187]\n"
      "s_sub_u32 s67, s68, s66\n"
      "ds_read_b128 a[68:71], v242 offset:36864\n"
      "ds_read_b128 a[76:79], v243 offset:36864\n"
      "v_mfma_f32_16x16x32_bf16 a[192:195], a[120:123], v[136:139], a[192:195]\n"
      "s_cmp_gt_i32 s67, s70\n"
      "ds_read_b128 a[84:87], v242 offset:37888\n"
      "ds_read_b128 a[92:95], v243 offset:37888\n"
      "v_mfma_f32_16x16x32_bf16 a[196:199], a[120:123], v[140:143], a[196:199]\n"
      "s_cselect_b32 s69, s77, s86\n"
      "v_max3_f32 v196, v184, v185, v186\n"
      "v_max_f32 v196, v196, v187\n"
      "v_mfma_f32_16x16x32_bf16 a[204:207], a[124:127], v[140:143], a[204:207]\n"
      "s_cselect_b32 s70, 60, s70\n"
      "v_cmp_nge_f32 vcc, 0x4b800000, v196\n"
      "v_mfma_f32_16x16x32_bf16 a[200:203], a[124:127], v[136:139], a[200:203]\n"
      "s_cselect_b32 s90, s80, s90\n"
      "v_mfma_f32_16x16x32_bf16 v[60:63], a[124:127], v[132:135], v[60:63]\n"
      "s_add_u32 s66, s66, s69\n"
      "v_mfma_f32_16x16x32_bf16 v[56:59], a[124:127], v[128:131], v[56:59]\n"
      "s_mov_b32 s82, s68\n"
      "s_cbranch_vccnz FS1_%=\n"
      "FR1_%=:\n"
      "v_mfma_f32_16x16x32_bf16 v[96:99], a[64:67], a[0:3], v[160:163]\n"
      "s_add_u32 s65, s68, s90\n"
      "v_exp_f32 v188, v64\n"
      "v_mfma_f32_16x16x32_bf16 v[96:99], a[72:75], a[16:19], v[96:99]\n"
      "s_max_u32 s88, s66, s65\n"
      "v_add_f32 v180, v180, v184\n"
      "v_add_f32 v181, v181, v185\n"
      "v_mfma_f32_16x16x32_bf16 v[96:99], a[80:83], a[32:35], v[96:99]\n"
      "v_exp_f32 v189, v65\n"
      "v_mfma_f32_16x16x32_bf16 v[96:99], a[88:91], a[48:51], v[96:99]\n"
      "s_getreg_b32 s67, hwreg(HW_REG_IB_STS, 0, 4)\n"
      "v_add_f32 v182, v182, v186\n"
      "v_add_f32 v183, v183, v187\n"
      "v_mfma_f32_16x16x32_bf16 v[104:107], a[64:67], a[4:7], v[164:167]\n"
      "s_cmp_gt_u32 s67, 4\n"
      "v_exp_f32 v190, v66\n"
      "v_mfma_f32_16x16x32_bf16 v[104:107], a[72:75], a[20:23], v[104:107]\n"
      "s_addc_u32 s89, s89, 0\n"
      "v_exp_f32 v191, v67\n"
      "v_mfma_f32_16x16x32_bf16 v[104:107], a[80:83], a[36:39], v[104:107]\n"
      "v_cvt_pk_bf16_f32 v128, v188, v189\n"
      "v_cvt_pk_bf16_f32 v129, v190, v191\n"
      "v_mfma_f32_16x16x32_bf16 v[104:107], a[88:91], a[52:55], v[104:107]\n"
      "v_exp_f32 v192, v68\n"
      "v_mfma_f32_16x16x32_bf16 v[112:115], a[64:67], a[8:11], v[168:171]\n"
      "s_add_u32 s39, s39, s39\n"
      "v_exp_f32 v193, v69\n"
      "v_mfma_f32_16x16x32_bf16 v[112:115], a[72:75], a[24:27], v[112:115]\n"
      "s_memrealtime s[68:69]\n"
      "v_exp_f32 v194, v70\n"
      "v_mfma_f32_16x16x32_bf16 v[112:115], a[80:83], a[40:43], v[112:115]\n"
      "v_exp_f32 v195, v71\n"
      "v_mfma_f32_16x16x32_bf16 v[112:115], a[88:91], a[56:59], v[112:115]\n"
      "s_mov_b32 m0, s62\n"
      "v_cvt_pk_bf16_f32 v130, v192, v193\n"
      "v_cvt_pk_bf16_f32 v131, v194, v195\n"
      "v_mfma_f32_16x16x32_bf16 v[120:123], a[64:67], a[12:15], v[172:175]\n"
      "buffer_load_dwordx4 v228, s[56:59], s60 offen nt lds\n"
      "v_mfma_f32_16x16x32_bf16 v[184:187], a[128:131], v[128:131], 0\n"
      "buffer_load_dwordx4 v229, s[56:59], s60 offen offset:1024 nt lds\n"
      "v_mfma_f32_16x16x32_bf16 v[120:123], a[72:75], a[28:31], v[120:123]\n"
      "buffer_load_dwordx4 v230, s[56:59], s60 offen offset:2048 nt lds\n"
      "v_mfma_f32_16x16x32_bf16 v[120:123], a[80:83], a[44:47], v[120:123]\n"
      "buffer_load_dwordx4 v231, s[56:59], s60 offen offset:3072 nt lds\n"
      "s_cbranch_scc1 SP3_%=\n"
      "HD3_%=:\n"
      "s_cmp_lg_u32 s54, 0\n"
      "s_waitcnt vmcnt(12) lgkmcnt(0)\n"
      "s_cbranch_scc1 W3_%=\n"
      "s_waitcnt vmcnt(8)\n"
      "W3_%=:\n"
      "s_sub_u32 s67, s68, s88\n"
      "s_cmp_lt_i32 s67, -8\n"
      "s_cbranch_scc1 GW6_%=\n"
      "GX6_%=:\n"
      "s_barrier\n"
      "v_mfma_f32_16x16x32_bf16 v[120:123], a[88:91], a[60:63], v[120:123]\n"
      "s_memrealtime s[68:69]\n"
      "ds_read_b64_tr_b16 a[96:97], v244 offset:32768\n"
      "ds_read_b64_tr_b16 a[98:99], v244 offset:36864\n"
      "v_mfma_f32_16x16x32_bf16 v[100:103], a[68:71], a[0:3], v[160:163]\n"
      "s_xor_b64 s[56:57], s[56:57], s[32:33]\n"
      "ds_read_b64_tr_b16 a[100:101], v245 offset:32768\n"
      "ds_read_b64_tr_b16 a[102:103], v245 offset:36864\n"
      "v_mfma_f32_16x16x32_bf16 v[100:103], a[76:79], a[16:19], v[100:103]\n"
      "s_add_u32 s61, s61, 1\n"
      "ds_read_b64_tr_b16 a[104:105], v246 offset:32768\n"
      "ds_read_b64_tr_b16 a[106:107], v246 offset:36864\n"
      "v_mfma_f32_16x16x32_bf16 v[100:103], a[84:87], a[32:35], v[100:103]\n"
      "s_add_u32 s62, s62, 0x4000\n"
      "ds_read_b64_tr_b16 a[108:109], v247 offset:32768\n"
      "ds_read_b64_tr_b16 a[110:111], v247 offset:36864\n"
      "v_mfma_f32_16x16x32_bf16 v[100:103], a[92:95], a[48:51], v[100:103]\n"
      "s_and_b32 s62, s62, 0x1ffff\n"
      "v_exp_f32 v188, v72\n"
      "v_mfma_f32_16x16x32_bf16 v[108:111], a[68:71], a[4:7], v[164:167]\n"
      "v_readlane_b32 s60, v219, s61\n"
      "v_exp_f32 v189, v73\n"
      "v_mfma_f32_16x16x32_bf16 v[108:111], a[76:79], a[20:23], v[108:111]\n"
      "v_xor_b32 v242, 0x10000, v242\n"
      "v_xor_b32 v243, 0x10000, v243\n"
      "v_mfma_f32_16x16x32_bf16 v[108:111], a[84:87], a[36:39], v[108:111]\n"
      "v_exp_f32 v190, v74\n"
      "v_mfma_f32_16x16x32_bf16 v[108:111], a[92:95], a[52:55], v[108:111]\n"
      "v_exp_f32 v191, v75\n"
      "v_mfma_f32_16x16x32_bf16 v[116:119], a[68:71], a[8:11], v[168:171]\n"
      "v_cvt_pk_bf16_f32 v132, v188, v189\n"
      "v_cvt_pk_bf16_f32 v133, v190, v191\n"
      "v_mfma_f32_16x16x32_bf16 v[116:119], a[76:79], a[24:27], v[116:119]\n"
      "v_exp_f32 v192, v76\n"
      "s_waitcnt lgkmcnt(0)\n"
      "v_mfma_f32_16x16x32_bf16 v[116:119], a[84:87], a[40:43], v[116:119]\n"
      "s_sub_u32 s67, s68, s66\n"
      "ds_read_b64_tr_b16 a[112:113], v244 offset:33792\n"
      "ds_read_b64_tr_b16 a[114:115], v244 offset:37888\n"
      "v_mfma_f32_16x16x32_bf16 v[116:119], a[92:95], a[56:59], v[116:119]\n"
      "s_cmp_gt_i32 s67, s70\n"
      "v_exp_f32 v193, v77\n"
      "v_mfma_f32_16x16x32_bf16 v[124:127], a[68:71], a[12:15], v[172:175]\n"
      "s_cselect_b32 s69, s77, s86\n"
      "ds_read_b64_tr_b16 a[116:117], v245 offset:33792\n"
      "v_cvt_pk_bf16_f32 v134, v192, v193\n"
      "v_mfma_f32_16x16x32_bf16 v[124:127], a[76:79], a[28:31], v[124:127]\n"
      "s_cselect_b32 s70, 60, s70\n"
      "v_exp_f32 v194, v78\n"
      "v_mfma_f32_16x16x32_bf16 v[124:127], a[84:87], a[44:47], v[124:127]\n"
      "s_cselect_b32 s90, s80, s90\n"
      "ds_read_b64_tr_b16 a[118:119], v245 offset:37888\n"
      "ds_read_b64_tr_b16 a[120:121], v246 offset:33792\n"
      "v_mfma_f32_16x16x32_bf16 v[124:127], a[92:95], a[60:63], v[124:127]\n"
      "s_add_u32 s66, s66, s69\n"
      "v_exp_f32 v195, v79\n"
      "v_mfma_f32_16x16x32_bf16 v[0:3], a[96:99], v[144:147], v[0:3]\n"
      "s_mov_b32 s82, s68\n"
      "ds_read_b64_tr_b16 a[122:123], v246 offset:37888\n"
      "v_cvt_pk_bf16_f32 v135, v194, v195\n"
      "v_mfma_f32_16x16x32_bf16 v[4:7], a[96:99], v[148:151], v[4:7]\n"
      "s_add_u32 s65, s68, s90\n"
      "v_exp_f32 v188, v80\n"
      "v_mfma_f32_16x16x32_bf16 v[184:187], a[132:135], v[132:135], v[184:187]\n"
      "s_max_u32 s88, s66, s65\n"
      "ds_read_b64_tr_b16 a[124:125], v247 offset:33792\n"
      "ds_read_b64_tr_b16 a[126:127], v247 offset:37888\n"
      "v_mfma_f32_16x16x32_bf16 a[144:147], a[96:99], v[152:155], a[144:147]\n"
      "v_exp_f32 v189, v81\n"
      "v_mfma_f32_16x16x32_bf16 a[148:151], a[96:99], v[156:159], a[148:151]\n"
      "s_getreg_b32 s67, hwreg(HW_REG_IB_STS, 0, 4)\n"
      "v_exp_f32 v190, v82\n"
      "v_mfma_f32_16x16x32_bf16 a[156:159], a[100:103], v[156:159], a[156:159]\n"
      "s_cmp_gt_u32 s67, 4\n"
      "v_exp_f32 v191, v83\n"
      "v_mfma_f32_16x16x32_bf16 a[152:155], a[100:103], v[152:155], a[152:155]\n"
      "s_addc_u32 s89, s89, 0\n"
      "v_cvt_pk_bf16_f32 v136, v188, v189\n"
      "v_cvt_pk_bf16_f32 v137, v190, v191\n"
      "v_mfma_f32_16x16x32_bf16 v[12:15], a[100:103], v[148:151], v[12:15]\n"
      "v_exp_f32 v192, v84\n"
      "v_mfma_f32_16x16x32_bf16 v[8:11], a[100:103], v[144:147], v[8:11]\n"
      "v_exp_f32 v193, v85\n"
      "v_mfma_f32_16x16x32_bf16 v[16:19], a[104:107], v[144:147], v[16:19]\n"
      "s_add_u32 s38, s38, s38\n"
      "v_exp_f32 v194, v86\n"
      "v_mfma_f32_16x16x32_bf16 v[20:23], a[104:107], v[148:151], v[20:23]\n"
      "s_memrealtime s[68:69]\n"
      "v_exp_f32 v195, v87\n"
      "v_mfma_f32_16x16x32_bf16 a[160:163], a[104:107], v[152:155], a[160:163]\n"
      "v_cvt_pk_bf16_f32 v138, v192, v193\n"
      "v_cvt_pk_bf16_f32 v139, v194, v195\n"
      "v_mfma_f32_16x16x32_bf16 a[164:167], a[104:107], v[156:159], a[164:167]\n"
      "s_mov_b32 m0, s62\n"
      "v_exp_f32 v188, v88\n"
      "v_mfma_f32_16x16x32_bf16 v[184:187], a[136:139], v[136:139], v[184:187]\n"
      "buffer_load_dwordx4 v228, s[56:59], s60 offen nt lds\n"
      "v_mfma_f32_16x16x32_bf16 a[172:175], a[108:111], v[156:159], a[172:175]\n"
      "buffer_load_dwordx4 v229, s[56:59], s60 offen offset:1024 nt lds\n"
      "v_mfma_f32_16x16x32_bf16 a[168:171], a[108:111], v[152:155], a[168:171]\n"
      "buffer_load_dwordx4 v230, s[56:59], s60 offen offset:2048 nt lds\n"
      "v_mfma_f32_16x16x32_bf16 v[28:31], a[108:111], v[148:151], v[28:31]\n"
      "buffer_load_dwordx4 v231, s[56:59], s60 offen offset:3072 nt lds\n"
      "HD0_%=:\n"
      "s_cmp_lg_u32 s54, 0\n"
      "s_waitcnt vmcnt(12) lgkmcnt(0)\n"
      "s_cbranch_scc1 W4_%=\n"
      "s_waitcnt vmcnt(8)\n"
      "W4_%=:\n"
      "s_sub_u32 s67, s68, s88\n"
      "s_cmp_lt_i32 s67, -8\n"
      "s_cbranch_scc1 GW7_%=\n"
      "GX7_%=:\n"
      "s_barrier\n"
      "v_mfma_f32_16x16x32_bf16 v[24:27], a[108:111], v[144:147], v[24:27]\n"
      "s_memrealtime s[68:69]\n"
      "ds_read_b128 a[64:67], v242 offset:0\n"
      "ds_read_b128 a[72:75], v243 offset:0\n"
      "v_mfma_f32_16x16x32_bf16 v[32:35], a[112:115], v[144:147], v[32:35]\n"
      "s_xor_b64 s[56:57], s[56:57], s[32:33]\n"
      "ds_read_b128 a[80:83], v242 offset:1024\n"
      "ds_read_b128 a[88:91], v243 offset:1024\n"
      "v_mfma_f32_16x16x32_bf16 v[36:39], a[112:115], v[148:151], v[36:39]\n"
      "s_add_u32 s61, s61, 1\n"
      "v_exp_f32 v189, v89\n"
      "v_mfma_f32_16x16x32_bf16 a[176:179], a[112:115], v[152:155], a[176:179]\n"
      "s_add_u32 s62, s62, 0x4000\n"
      "v_exp_f32 v190, v90\n"
      "v_mfma_f32_16x16x32_bf16 a[180:183], a[112:115], v[156:159], a[180:183]\n"
      "s_and_b32 s62, s62, 0x1ffff\n"
      "v_exp_f32 v191, v91\n"
      "v_mfma_f32_16x16x32_bf16 a[188:191], a[116:119], v[156:159], a[188:191]\n"
      "v_cvt_pk_bf16_f32 v140, v188, v189\n"
      "v_readlane_b32 s60, v219, s61\n"
      "v_mfma_f32_16x16x32_bf16 a[184:187], a[116:119], v[152:155], a[184:187]\n"
      "v_exp_f32 v192, v92\n"
      "v_mfma_f32_16x16x32_bf16 v[44:47], a[116:119], v[148:151], v[44:47]\n"
      "v_exp_f32 v193, v93\n"
      "v_mfma_f32_16x16x32_bf16 v[40:43], a[116:119], v[144:147], v[40:43]\n"
      "v_cvt_pk_bf16_f32 v141, v190, v191\n"
      "v_cvt_pk_bf16_f32 v142, v192, v193\n"
      "v_mfma_f32_16x16x32_bf16 v[48:51], a[120:123], v[144:147], v[48:51]\n"
      "v_exp_f32 v194, v94\n"
      "v_mfma_f32_16x16x32_bf16 v[52:55], a[120:123], v[148:151], v[52:55]\n"
      "v_exp_f32 v195, v95\n"
      "s_waitcnt lgkmcnt(0)\n"
      "v_mfma_f32_16x16x32_bf16 a[192:195], a[120:123], v[152:155], a[192:195]\n"
      "s_sub_u32 s67, s68, s66\n"
      "v_cvt_pk_bf16_f32 v143, v194, v195\n"
      "v_xor_b32 v244, 0x10000, v244\n"
      "v_mfma_f32_16x16x32_bf16 a[196:199], a[120:123], v[156:159], a[196:199]\n"
      "s_cmp_gt_i32 s67, s70\n"
      "ds_read_b128 a[68:71], v242 offset:4096\n"
      "ds_read_b128 a[76:79], v243 offset:4096\n"
      "v_mfma_f32_16x16x32_bf16 v[184:187], a[140:143], v[140:143], v[184:187]\n"
      "s_cselect_b32 s69, s77, s86\n"
      "ds_read_b128 a[84:87], v242 offset:5120\n"
      "ds_read_b128 a[92:95], v243 offset:5120\n"
      "v_mfma_f32_16x16x32_bf16 a[204:207], a[124:127], v[156:159], a[204:207]\n"
      "s_cselect_b32 s70, 60, s70\n"
      "v_xor_b32 v245, 0x10000, v245\n"
      "v_xor_b32 v246, 0x10000, v246\n"
      "v_mfma_f32_16x16x32_bf16 a[200:203], a[124:127], v[152:155], a[200:203]\n"
      "s_cselect_b32 s90, s80, s90\n"
      "v_xor_b32 v247, 0x10000, v247\n"
      "v_max3_f32 v196, v184, v185, v186\n"
      "v_mfma_f32_16x16x32_bf16 v[60:63], a[124:127], v[148:151], v[60:63]\n"
      "s_add_u32 s66, s66, s69\n"
      "v_max_f32 v196, v196, v187\n"
      "v_cmp_nge_f32 vcc, 0x4b800000, v196\n"
      "v_mfma_f32_16x16x32_bf16 v[56:59], a[124:127], v[144:147], v[56:59]\n"
      "s_mov_b32 s82, s68\n"
      "s_cbranch_vccnz FS2_%=\n"
      "FR2_%=:\n"
      "s_branch LOOP_%=\n"
      "SP1_%=:\n"
      "s_ff1_i32_b32 s64, s39\n"
      "s_lshl1_add_u32 s64, s64, 1\n"
      "s_bitcmp1_b64 s[52:53], s64\n"
      "s_cbranch_scc0 SH1_%=\n"
      "s_swappc_b64 s[84:85], s[84:85]\n"
      "SH1_%=:\n"
      "s_cmp_lt_u32 s64, 63\n"
      "s_cbranch_scc1 HD1_%=\n"
      "s_xor_b64 s[56:57], s[56:57], s[32:33]\n"
      "s_add_u32 s61, s61, 1\n"
      "s_add_u32 s62, s62, 0x4000\n"
      "s_and_b32 s62, s62, 0x1ffff\n"
      "v_readlane_b32 s74, v219, 60\n"
      "v_readlane_b32 s76, v219, 62\n"
      "s_mov_b32 s61, 0\n"
      "s_add_u32 s74, s74, 0x400\n"
      "v_add_u32 v228, s74, v229\n"
      "v_add_u32 v230, s74, v231\n"
      "v_add_u32 v229, s76, v229\n"
      "v_add_u32 v231, s76, v231\n"
      "s_branch HD1_%=\n"
      "SP2_%=:\n"
      "s_cmp_eq_u32 s38, 0\n"
      "s_cbranch_scc1 L62_%=\n"
      "s_ff1_i32_b32 s64, s38\n"
      "s_lshl1_add_u32 s64, s64, 0\n"
      "s_bitcmp1_b64 s[52:53], s64\n"
      "s_cbranch_scc0 SH2_%=\n"
      "s_swappc_b64 s[84:85], s[84:85]\n"
      "SH2_%=:\n"
      "s_branch HD2_%=\n"
      "L62_%=:\n"
      "s_xor_b64 s[56:57], s[56:57], s[32:33]\n"
      "s_mov_b32 s61, 0\n"
      "v_xor_b32 v228, 0x80, v228\n"
      "v_xor_b32 v229, 0x80, v229\n"
      "v_xor_b32 v230, 0x80, v230\n"
      "v_xor_b32 v231, 0x80, v231\n"
      "s_mov_b32 s38, 0x80000000\n"
      "s_branch HD2_%=\n"
      "SP3_%=:\n"
      "s_cmp_eq_u32 s39, 0\n"
      "s_cbranch_scc1 NH_%=\n"
      "s_ff1_i32_b32 s64, s39\n"
      "s_lshl1_add_u32 s64, s64, 1\n"
      "s_bitcmp1_b64 s[52:53], s64\n"
      "s_cbranch_scc0 SH3_%=\n"
      "s_swappc_b64 s[84:85], s[84:85]\n"
      "SH3_%=:\n"
      "s_bitcmp1_b64 s[34:35], s64\n"
      "s_cbranch_scc0 HD3_%=\n"
      "NH_%=:\n"
      "s_cmp_lg_u32 s54, 0\n"
      "s_waitcnt vmcnt(12) lgkmcnt(0)\n"
      "s_cbranch_scc1 W5_%=\n"
      "s_waitcnt vmcnt(8)\n"
      "W5_%=:\n"
      "s_barrier\n"
      "v_mfma_f32_16x16x32_bf16 v[120:123], a[88:91], a[60:63], v[120:123]\n"
      "s_sub_u32 s54, s54, 1\n"
      "ds_read_b64_tr_b16 a[96:97], v244 offset:32768\n"
      "ds_read_b64_tr_b16 a[98:99], v244 offset:36864\n"
      "v_mfma_f32_16x16x32_bf16 v[100:103], a[68:71], a[0:3], v[160:163]\n"
      "ds_read_b64_tr_b16 a[100:101], v245 offset:32768\n"
      "ds_read_b64_tr_b16 a[102:103], v245 offset:36864\n"
      "v_mfma_f32_16x16x32_bf16 v[100:103], a[76:79], a[16:19], v[100:103]\n"
      "ds_read_b64_tr_b16 a[104:105], v246 offset:32768\n"
      "ds_read_b64_tr_b16 a[106:107], v246 offset:36864\n"
      "v_mfma_f32_16x16x32_bf16 v[100:103], a[84:87], a[32:35], v[100:103]\n"
      "ds_read_b64_tr_b16 a[108:109], v247 offset:32768\n"
      "ds_read_b64_tr_b16 a[110:111], v247 offset:36864\n"
      "v_mfma_f32_16x16x32_bf16 v[100:103], a[92:95], a[48:51], v[100:103]\n"
      "v_exp_f32 v188, v72\n"
      "v_mfma_f32_16x16x32_bf16 v[108:111], a[68:71], a[4:7], v[164:167]\n"
      "v_exp_f32 v189, v73\n"
      "v_mfma_f32_16x16x32_bf16 v[108:111], a[76:79], a[20:23], v[108:111]\n"
      "v_xor_b32 v242, 0x10000, v242\n"
      "v_xor_b32 v243, 0x10000, v243\n"
      "v_mfma_f32_16x16x32_bf16 v[108:111], a[84:87], a[36:39], v[108:111]\n"
      "v_exp_f32 v190, v74\n"
      "v_mfma_f32_16x16x32_bf16 v[108:111], a[92:95], a[52:55], v[108:111]\n"
      "v_exp_f32 v191, v75\n"
      "v_mfma_f32_16x16x32_bf16 v[116:119], a[68:71], a[8:11], v[168:171]\n"
      "v_cvt_pk_bf16_f32 v132, v188, v189\n"
      "v_cvt_pk_bf16_f32 v133, v190, v191\n"
      "v_mfma_f32_16x16x32_bf16 v[116:119], a[76:79], a[24:27], v[116:119]\n"
      "v_exp_f32 v192, v76\n"
      "s_waitcnt lgkmcnt(0)\n"
      "v_mfma_f32_16x16x32_bf16 v[116:119], a[84:87], a[40:43], v[116:119]\n"
      "ds_read_b64_tr_b16 a[112:113], v244 offset:33792\n"
      "ds_read_b64_tr_b16 a[114:115], v244 offset:37888\n"
      "v_mfma_f32_16x16x32_bf16 v[116:119], a[92:95], a[56:59], v[116:119]\n"
      "v_exp_f32 v193, v77\n"
      "v_mfma_f32_16x16x32_bf16 v[124:127], a[68:71], a[12:15], v[172:175]\n"
      "ds_read_b64_tr_b16 a[116:117], v245 offset:33792\n"
      "v_cvt_pk_bf16_f32 v134, v192, v193\n"
      "v_mfma_f32_16x16x32_bf16 v[124:127], a[76:79], a[28:31], v[124:127]\n"
      "v_exp_f32 v194, v78\n"
      "v_mfma_f32_16x16x32_bf16 v[124:127], a[84:87], a[44:47], v[124:127]\n"
      "ds_read_b64_tr_b16 a[118:119], v245 offset:37888\n"
      "ds_read_b64_tr_b16 a[120:121], v246 offset:33792\n"
      "v_mfma_f32_16x16x32_bf16 v[124:127], a[92:95], a[60:63], v[124:127]\n"
      "v_exp_f32 v195, v79\n"
      "v_mfma_f32_16x16x32_bf16 v[0:3], a[96:99], v[144:147], v[0:3]\n"
      "ds_read_b64_tr_b16 a[122:123], v246 offset:37888\n"
      "v_cvt_pk_bf16_f32 v135, v194, v195\n"
      "v_mfma_f32_16x16x32_bf16 v[4:7], a[96:99], v[148:151], v[4:7]\n"
      "v_exp_f32 v188, v80\n"
      "v_mfma_f32_16x16x32_bf16 v[184:187], a[132:135], v[132:135], v[184:187]\n"
      "ds_read_b64_tr_b16 a[124:125], v247 offset:33792\n"
      "ds_read_b64_tr_b16 a[126:127], v247 offset:37888\n"
      "v_mfma_f32_16x16x32_bf16 a[144:147], a[96:99], v[152:155], a[144:147]\n"
      "v_exp_f32 v189, v81\n"
      "v_mfma_f32_16x16x32_bf16 a[148:151], a[96:99], v[156:159], a[148:151]\n"
      "v_exp_f32 v190, v82\n"
      "v_mfma_f32_16x16x32_bf16 a[156:159], a[100:103], v[156:159], a[156:159]\n"
      "v_exp_f32 v191, v83\n"
      "v_mfma_f32_16x16x32_bf16 a[152:155], a[100:103], v[152:155], a[152:155]\n"
      "v_cvt_pk_bf16_f32 v136, v188, v189\n"
      "v_cvt_pk_bf16_f32 v137, v190, v191\n"
      "v_mfma_f32_16x16x32_bf16 v[12:15], a[100:103], v[148:151], v[12:15]\n"
      "v_exp_f32 v192, v84\n"
      "v_mfma_f32_16x16x32_bf16 v[8:11], a[100:103], v[144:147], v[8:11]\n"
      "v_exp_f32 v193, v85\n"
      "v_mfma_f32_16x16x32_bf16 v[16:19], a[104:107], v[144:147], v[16:19]\n"
      "s_add_u32 s38, s38, s38\n"
      "v_exp_f32 v194, v86\n"
      "v_mfma_f32_16x16x32_bf16 v[20:23], a[104:107], v[148:151], v[20:23]\n"
      "s_memrealtime s[68:69]\n"
      "v_exp_f32 v195, v87\n"
      "v_mfma_f32_16x16x32_bf16 a[160:163], a[104:107], v[152:155], a[160:163]\n"
      "v_cvt_pk_bf16_f32 v138, v192, v193\n"
      "v_cvt_pk_bf16_f32 v139, v194, v195\n"
      "v_mfma_f32_16x16x32_bf16 a[164:167], a[104:107], v[156:159], a[164:167]\n"
      "v_exp_f32 v188, v88\n"
      "v_mfma_f32_16x16x32_bf16 v[184:187], a[136:139], v[136:139], v[184:187]\n"
      "s_nop 0\n"
      "v_mfma_f32_16x16x32_bf16 a[172:175], a[108:111], v[156:159], a[172:175]\n"
      "s_nop 0\n"
      "v_mfma_f32_16x16x32_bf16 a[168:171], a[108:111], v[152:155], a[168:171]\n"
      "s_nop 0\n"
      "v_mfma_f32_16x16x32_bf16 v[28:31], a[108:111], v[148:151], v[28:31]\n"
      "s_nop 0\n"
      "s_cbranch_scc0 MH_%=\n"
      "END_%=:\n"
      "s_waitcnt lgkmcnt(0)\n"
      "v_mfma_f32_16x16x32_bf16 v[24:27], a[108:111], v[144:147], v[24:27]\n"
      "v_mov_b32 v236, s68\n"
      "v_exp_f32 v189, v89\n"
      "v_mfma_f32_16x16x32_bf16 v[32:35], a[112:115], v[144:147], v[32:35]\n"
      "s_mov_b64 exec, 1\n"
      "global_store_dwordx2 v238, v[236:237], s[46:47] offset:8 sc1\n"
      "s_mov_b64 exec, -1\n"
      "v_mfma_f32_16x16x32_bf16 v[36:39], a[112:115], v[148:151], v[36:39]\n"
      "s_mov_b32 s82, s68\n"
      "v_exp_f32 v190, v90\n"
      "v_mfma_f32_16x16x32_bf16 a[176:179], a[112:115], v[152:155], a[176:179]\n"
      "v_exp_f32 v191, v91\n"
      "v_mfma_f32_16x16x32_bf16 a[180:183], a[112:115], v[156:159], a[180:183]\n"
      "v_cvt_pk_bf16_f32 v140, v188, v189\n"
      "v_cvt_pk_bf16_f32 v141, v190, v191\n"
      "v_mfma_f32_16x16x32_bf16 a[188:191], a[116:119], v[156:159], a[188:191]\n"
      "v_exp_f32 v192, v92\n"
      "s_waitcnt vmcnt(5)\n"
      "s_barrier\n"
      "v_mfma_f32_16x16x32_bf16 a[184:187], a[116:119], v[152:155], a[184:187]\n"
      "ds_read_b64_tr_b16 a[80:81], v224 offset:0\n"
      "ds_read_b64_tr_b16 a[82:83], v224 offset:4096\n"
      "v_mfma_f32_16x16x32_bf16 v[44:47], a[116:119], v[148:151], v[44:47]\n"
      "ds_read_b64_tr_b16 a[84:85], v225 offset:0\n"
      "ds_read_b64_tr_b16 a[86:87], v225 offset:4096\n"
      "v_mfma_f32_16x16x32_bf16 v[40:43], a[116:119], v[144:147], v[40:43]\n"
      "s_mov_b32 s74, 0xffff0000\n"
      "ds_read_b64_tr_b16 a[88:89], v226 offset:0\n"
      "ds_read_b64_tr_b16 a[90:91], v226 offset:4096\n"
      "v_mfma_f32_16x16x32_bf16 v[48:51], a[120:123], v[144:147], v[48:51]\n"
      "s_mov_b32 s75, 0xffff0000\n"
      "ds_read_b64_tr_b16 a[92:93], v227 offset:0\n"
      "ds_read_b64_tr_b16 a[94:95], v227 offset:4096\n"
      "v_mfma_f32_16x16x32_bf16 v[52:55], a[120:123], v[148:151], v[52:55]\n"
      "s_mov_b32 s94, 0\n"
      "v_exp_f32 v193, v93\n"
      "v_mfma_f32_16x16x32_bf16 a[192:195], a[120:123], v[152:155], a[192:195]\n"
      "s_mov_b32 s95, -1\n"
      "ds_read_b64_tr_b16 a[16:17], v224 offset:1024\n"
      "v_cvt_pk_bf16_f32 v142, v192, v193\n"
      "v_mfma_f32_16x16x32_bf16 a[196:199], a[120:123], v[156:159], a[196:199]\n"
      "v_exp_f32 v194, v94\n"
      "v_mfma_f32_16x16x32_bf16 a[204:207], a[124:127], v[156:159], a[204:207]\n"
      "ds_read_b64_tr_b16 a[18:19], v224 offset:5120\n"
      "ds_read_b64_tr_b16 a[20:21], v225 offset:1024\n"
      "v_mfma_f32_16x16x32_bf16 a[200:203], a[124:127], v[152:155], a[200:203]\n"
      "v_exp_f32 v195, v95\n"
      "v_mfma_f32_16x16x32_bf16 v[60:63], a[124:127], v[148:151], v[60:63]\n"
      "ds_read_b64_tr_b16 a[22:23], v225 offset:5120\n"
      "v_cvt_pk_bf16_f32 v143, v194, v195\n"
      "v_mfma_f32_16x16x32_bf16 v[56:59], a[124:127], v[144:147], v[56:59]\n"
      "ds_read_b64_tr_b16 a[24:25], v226 offset:1024\n"
      "ds_read_b64_tr_b16 a[26:27], v226 offset:5120\n"
      "v_mfma_f32_16x16x32_bf16 v[184:187], a[140:143], v[140:143], v[184:187]\n"
      "ds_read_b64_tr_b16 a[28:29], v227 offset:1024\n"
      "ds_read_b64_tr_b16 a[30:31], v227 offset:5120\n"
      "ds_read_b64_tr_b16 a[0:1], v220 offset:1024\n"
      "ds_read_b64_tr_b16 a[2:3], v220 offset:5120\n"
      "ds_read_b64_tr_b16 a[4:5], v221 offset:1024\n"
      "ds_read_b64_tr_b16 a[6:7], v221 offset:5120\n"
      "ds_read_b64_tr_b16 a[8:9], v222 offset:1024\n"
      "ds_read_b64_tr_b16 a[10:11], v222 offset:5120\n"
      "ds_read_b64_tr_b16 a[12:13], v223 offset:1024\n"
      "ds_read_b64_tr_b16 a[14:15], v223 offset:5120\n"
      "v_max3_f32 v196, v184, v185, v186\n"
      "v_max_f32 v196, v196, v187\n"
      "v_cmp_nge_f32 vcc, 0x4b800000, v196\n"
      "s_cbranch_vccnz FS3_%=\n"
      "FR3_%=:\n"
      "s_waitcnt lgkmcnt(15)\n"
      "v_mfma_f32_16x16x32_bf16 a[176:179], a[80:83], v[136:139], a[176:179]\n"
      "v_add_f32 v180, v180, v184\n"
      "v_add_f32 v181, v181, v185\n"
      "v_mfma_f32_16x16x32_bf16 a[180:183], a[80:83], v[140:143], a[180:183]\n"
      "v_add_f32 v182, v182, v186\n"
      "v_add_f32 v183, v183, v187\n"
      "v_mfma_f32_16x16x32_bf16 a[184:187], a[84:87], v[136:139], a[184:187]\n"
      "v_exp_f32 v188, v112\n"
      "v_mfma_f32_16x16x32_bf16 a[188:191], a[84:87], v[140:143], a[188:191]\n"
      "ds_write_b128 v232, a[176:179] offset:0\n"
      "v_exp_f32 v189, v113\n"
      "v_mfma_f32_16x16x32_bf16 a[192:195], a[88:91], v[136:139], a[192:195]\n"
      "v_exp_f32 v190, v114\n"
      "v_mfma_f32_16x16x32_bf16 a[196:199], a[88:91], v[140:143], a[196:199]\n"
      "v_exp_f32 v191, v115\n"
      "v_mfma_f32_16x16x32_bf16 a[200:203], a[92:95], v[136:139], a[200:203]\n"
      "ds_write_b128 v232, a[180:183] offset:1024\n"
      "v_exp_f32 v192, v116\n"
      "v_mfma_f32_16x16x32_bf16 a[204:207], a[92:95], v[140:143], a[204:207]\n"
      "v_exp_f32 v193, v117\n"
      "v_exp_f32 v194, v118\n"
      "v_cvt_pk_bf16_f32 v152, v188, v189\n"
      "v_exp_f32 v195, v119\n"
      "v_cvt_pk_bf16_f32 v153, v190, v191\n"
      "ds_write_b128 v232, a[184:187] offset:2048\n"
      "v_exp_f32 v188, v120\n"
      "v_cvt_pk_bf16_f32 v154, v192, v193\n"
      "v_mfma_f32_16x16x32_bf16 v[32:35], a[80:83], v[128:131], v[32:35]\n"
      "v_exp_f32 v189, v121\n"
      "v_cvt_pk_bf16_f32 v155, v194, v195\n"
      "v_exp_f32 v190, v122\n"
      "v_cvt_pk_bf16_f32 v156, v188, v189\n"
      "v_mfma_f32_16x16x32_bf16 v[184:187], a[136:139], v[152:155], 0\n"
      "ds_write_b128 v234, v[152:155] offset:0\n"
      "v_exp_f32 v191, v123\n"
      "v_mfma_f32_16x16x32_bf16 v[36:39], a[80:83], v[132:135], v[36:39]\n"
      "v_exp_f32 v192, v124\n"
      "v_cvt_pk_bf16_f32 v157, v190, v191\n"
      "v_exp_f32 v193, v125\n"
      "v_exp_f32 v194, v126\n"
      "v_cvt_pk_bf16_f32 v158, v192, v193\n"
      "ds_write_b128 v232, a[188:191] offset:3072\n"
      "v_mfma_f32_16x16x32_bf16 v[40:43], a[84:87], v[128:131], v[40:43]\n"
      "v_exp_f32 v195, v127\n"
      "v_exp_f32 v188, v96\n"
      "v_cvt_pk_bf16_f32 v159, v194, v195\n"
      "v_exp_f32 v189, v97\n"
      "v_mfma_f32_16x16x32_bf16 v[44:47], a[84:87], v[132:135], v[44:47]\n"
      "v_exp_f32 v190, v98\n"
      "ds_write_b128 v234, v[156:159] offset:1024\n"
      "v_cvt_pk_bf16_f32 v144, v188, v189\n"
      "v_mfma_f32_16x16x32_bf16 v[184:187], a[140:143], v[156:159], v[184:187]\n"
      "v_exp_f32 v191, v99\n"
      "v_exp_f32 v192, v100\n"
      "v_cvt_pk_bf16_f32 v145, v190, v191\n"
      "v_mfma_f32_16x16x32_bf16 v[48:51], a[88:91], v[128:131], v[48:51]\n"
      "v_exp_f32 v193, v101\n"
      "ds_write_b128 v232, a[192:195] offset:4096\n"
      "v_exp_f32 v194, v102\n"
      "v_cvt_pk_bf16_f32 v146, v192, v193\n"
      "v_exp_f32 v195, v103\n"
      "v_mfma_f32_16x16x32_bf16 v[52:55], a[88:91], v[132:135], v[52:55]\n"
      "v_exp_f32 v188, v104\n"
      "v_cvt_pk_bf16_f32 v147, v194, v195\n"
      "v_exp_f32 v189, v105\n"
      "ds_write_b128 v232, a[196:199] offset:5120\n"
      "v_exp_f32 v190, v106\n"
      "v_cvt_pk_bf16_f32 v148, v188, v189\n"
      "v_mfma_f32_16x16x32_bf16 v[184:187], a[128:131], v[144:147], v[184:187]\n"
      "v_exp_f32 v191, v107\n"
      "v_exp_f32 v192, v108\n"
      "v_cvt_pk_bf16_f32 v149, v190, v191\n"
      "v_exp_f32 v193, v109\n"
      "ds_write_b128 v232, a[200:203] offset:6144\n"
      "v_mfma_f32_16x16x32_bf16 v[56:59], a[92:95], v[128:131], v[56:59]\n"
      "v_exp_f32 v194, v110\n"
      "v_cvt_pk_bf16_f32 v150, v192, v193\n"
      "v_exp_f32 v195, v111\n"
      "v_cndmask_b32 v229, v180, v181, s[74:75]\n"
      "v_cvt_pk_bf16_f32 v151, v194, v195\n"
      "v_mfma_f32_16x16x32_bf16 v[60:63], a[92:95], v[132:135], v[60:63]\n"
      "v_cndmask_b32 v200, v182, v183, s[74:75]\n"
      "ds_write_b128 v232, a[204:207] offset:7168\n"
      "v_cndmask_b32 v228, v176, v177, s[74:75]\n"
      "v_mfma_f32_16x16x32_bf16 v[184:187], a[132:135], v[148:151], v[184:187]\n"
      "s_nop 7\n"
      "v_max3_f32 v196, v184, v185, v186\n"
      "v_max_f32 v196, v196, v187\n"
      "v_cmp_nge_f32 vcc, 0x4b800000, v196\n"
      "s_cbranch_vccnz FS8_%=\n"
      "FR8_%=:\n"
      "v_cndmask_b32 v202, v186, v187, s[74:75]\n"
      "v_cndmask_b32 v201, v184, v185, s[74:75]\n"
      "v_add_f32 v200, v200, v202\n"
      "ds_write_b32 v234, v200 offset:4096\n"
      "v_add_f32 v229, v229, v201\n"
      "s_waitcnt lgkmcnt(8)\n"
      "v_mfma_f32_16x16x32_bf16 v[32:35], a[16:19], v[144:147], v[32:35]\n"
      "v_mfma_f32_16x16x32_bf16 v[36:39], a[16:19], v[148:151], v[36:39]\n"
      "v_mfma_f32_16x16x32_bf16 v[40:43], a[20:23], v[144:147], v[40:43]\n"
      "s_waitcnt lgkmcnt(0)\n"
      "s_barrier\n"
      "v_mfma_f32_16x16x32_bf16 v[44:47], a[20:23], v[148:151], v[44:47]\n"
      "ds_read_b32 v208, v209\n"
      "ds_read_b128 v[160:163], v235 offset:0\n"
      "v_mfma_f32_16x16x32_bf16 v[48:51], a[24:27], v[144:147], v[48:51]\n"
      "ds_read_b128 v[164:167], v235 offset:1024\n"
      "ds_read_b128 v[64:67], v233 offset:0\n"
      "v_mfma_f32_16x16x32_bf16 v[52:55], a[24:27], v[148:151], v[52:55]\n"
      "ds_read_b128 v[68:71], v233 offset:1024\n"
      "ds_read_b128 v[72:75], v233 offset:2048\n"
      "ds_read_b128 v[76:79], v233 offset:3072\n"
      "v_mfma_f32_16x16x32_bf16 v[56:59], a[28:31], v[144:147], v[56:59]\n"
      "ds_read_b128 v[80:83], v233 offset:4096\n"
      "ds_read_b128 v[84:87], v233 offset:5120\n"
      "v_mfma_f32_16x16x32_bf16 v[60:63], a[28:31], v[148:151], v[60:63]\n"
      "ds_read_b128 v[88:91], v233 offset:6144\n"
      "ds_read_b128 v[92:95], v233 offset:7168\n"
      "ds_read_b32 v204, v235 offset:4096\n"
      "s_waitcnt lgkmcnt(11)\n"
      "v_cmp_ne_u32 vcc, 0, v208\n"
      "s_cbranch_vccnz SLOW_%=\n"
      "AGAIN_%=:\n"
      "s_waitcnt lgkmcnt(9)\n"
      "v_mfma_f32_16x16x32_bf16 v[32:35], a[0:3], v[160:163], v[32:35]\n"
      "v_mfma_f32_16x16x32_bf16 v[36:39], a[0:3], v[164:167], v[36:39]\n"
      "v_mfma_f32_16x16x32_bf16 v[40:43], a[4:7], v[160:163], v[40:43]\n"
      "v_mfma_f32_16x16x32_bf16 v[44:47], a[4:7], v[164:167], v[44:47]\n"
      "v_mfma_f32_16x16x32_bf16 v[48:51], a[8:11], v[160:163], v[48:51]\n"
      "v_mfma_f32_16x16x32_bf16 v[52:55], a[8:11], v[164:167], v[52:55]\n"
      "v_mfma_f32_16x16x32_bf16 v[56:59], a[12:15], v[160:163], v[56:59]\n"
      "v_mfma_f32_16x16x32_bf16 v[60:63], a[12:15], v[164:167], v[60:63]\n"
      "global_load_dwordx2 v[206:207], v239, s[46:47] offset:8 sc1\n"
      "s_waitcnt lgkmcnt(7)\n"
      "v_pk_add_f32 v[32:33], v[32:33], v[64:65]\n"
      "v_pk_add_f32 v[34:35], v[34:35], v[66:67]\n"
      "v_pk_add_f32 v[36:37], v[36:37], v[68:69]\n"
      "v_pk_add_f32 v[38:39], v[38:39], v[70:71]\n"
      "v_cvt_pk_bf16_f32 v212, v32, v33\n"
      "v_cvt_pk_bf16_f32 v213, v34, v35\n"
      "v_cvt_pk_bf16_f32 v214, v36, v37\n"
      "v_cvt_pk_bf16_f32 v215, v38, v39\n"
      "buffer_store_dwordx4 v[212:215], v241, s[24:27], s78 offen offset:0 sc1\n"
      "s_waitcnt lgkmcnt(5)\n"
      "v_pk_add_f32 v[40:41], v[40:41], v[72:73]\n"
      "v_pk_add_f32 v[42:43], v[42:43], v[74:75]\n"
      "v_pk_add_f32 v[44:45], v[44:45], v[76:77]\n"
      "v_pk_add_f32 v[46:47], v[46:47], v[78:79]\n"
      "v_cvt_pk_bf16_f32 v216, v40, v41\n"
      "v_cvt_pk_bf16_f32 v217, v42, v43\n"
      "v_cvt_pk_bf16_f32 v218, v44, v45\n"
      "v_cvt_pk_bf16_f32 v219, v46, v47\n"
      "buffer_store_dwordx4 v[216:219], v241, s[24:27], s78 offen offset:1024 sc1\n"
      "s_waitcnt lgkmcnt(3)\n"
      "v_pk_add_f32 v[48:49], v[48:49], v[80:81]\n"
      "v_pk_add_f32 v[50:51], v[50:51], v[82:83]\n"
      "v_pk_add_f32 v[52:53], v[52:53], v[84:85]\n"
      "v_pk_add_f32 v[54:55], v[54:55], v[86:87]\n"
      "v_cvt_pk_bf16_f32 v220, v48, v49\n"
      "v_cvt_pk_bf16_f32 v221, v50, v51\n"
      "v_cvt_pk_bf16_f32 v222, v52, v53\n"
      "v_cvt_pk_bf16_f32 v223, v54, v55\n"
      "buffer_store_dwordx4 v[220:223], v241, s[24:27], s78 offen offset:2048 sc1\n"
      "s_waitcnt lgkmcnt(0)\n"
      "v_pk_add_f32 v[56:57], v[56:57], v[88:89]\n"
      "v_pk_add_f32 v[58:59], v[58:59], v[90:91]\n"
      "v_pk_add_f32 v[60:61], v[60:61], v[92:93]\n"
      "v_pk_add_f32 v[62:63], v[62:63], v[94:95]\n"
      "v_cvt_pk_bf16_f32 v224, v56, v57\n"
      "v_cvt_pk_bf16_f32 v225, v58, v59\n"
      "v_cvt_pk_bf16_f32 v226, v60, v61\n"
      "v_cvt_pk_bf16_f32 v227, v62, v63\n"
      "buffer_store_dwordx4 v[224:227], v241, s[24:27], s78 offen offset:3072 sc1\n"
      "v_add_f32 v229, v229, v204\n"
      "v_cndmask_b32 v230, v228, v229, s[94:95]\n"
      "v_add_u32 v231, s40, v230\n"
      "v_add3_u32 v231, v231, v212, v213\n"
      "v_add3_u32 v231, v231, v214, v215\n"
      "v_add3_u32 v231, v231, v216, v217\n"
      "v_add3_u32 v231, v231, v218, v219\n"
      "v_add3_u32 v231, v231, v220, v221\n"
      "v_add3_u32 v231, v231, v222, v223\n"
      "v_add3_u32 v231, v231, v224, v225\n"
      "v_add3_u32 v231, v231, v226, v227\n"
      "buffer_store_dwordx2 v[230:231], v240, s[24:27], s78 offen sc1\n"
      "v_xor_b32 v244, 0x10000, v244\n"
      "v_xor_b32 v245, 0x10000, v245\n"
      "v_xor_b32 v246, 0x10000, v246\n"
      "v_xor_b32 v247, 0x10000, v247\n"
      "s_waitcnt vmcnt(7)\n"
      "s_barrier\n"
      "ds_read_b64_tr_b16 a[64:65], v244 offset:16384\n"
      "ds_read_b64_tr_b16 a[66:67], v244 offset:20480\n"
      "ds_read_b64_tr_b16 a[68:69], v245 offset:16384\n"
      "ds_read_b64_tr_b16 a[70:71], v245 offset:20480\n"
      "ds_read_b64_tr_b16 a[72:73], v246 offset:16384\n"
      "ds_read_b64_tr_b16 a[74:75], v246 offset:20480\n"
      "ds_read_b64_tr_b16 a[76:77], v247 offset:16384\n"
      "ds_read_b64_tr_b16 a[78:79], v247 offset:20480\n"
      "s_waitcnt lgkmcnt(4)\n"
      "v_mfma_f32_16x16x32_bf16 a[144:147], a[64:67], v[136:139], a[144:147]\n"
      "ds_read_b64_tr_b16 a[96:97], v244 offset:17408\n"
      "ds_read_b64_tr_b16 a[98:99], v244 offset:21504\n"
      "v_mfma_f32_16x16x32_bf16 a[148:151], a[64:67], v[140:143], a[148:151]\n"
      "ds_read_b64_tr_b16 a[100:101], v245 offset:17408\n"
      "ds_read_b64_tr_b16 a[102:103], v245 offset:21504\n"
      "v_mfma_f32_16x16x32_bf16 a[152:155], a[68:71], v[136:139], a[152:155]\n"
      "ds_read_b64_tr_b16 a[104:105], v246 offset:17408\n"
      "ds_read_b64_tr_b16 a[106:107], v246 offset:21504\n"
      "v_mfma_f32_16x16x32_bf16 a[156:159], a[68:71], v[140:143], a[156:159]\n"
      "ds_read_b64_tr_b16 a[108:109], v247 offset:17408\n"
      "ds_read_b64_tr_b16 a[110:111], v247 offset:21504\n"
      "s_waitcnt lgkmcnt(8)\n"
      "v_mfma_f32_16x16x32_bf16 a[160:163], a[72:75], v[136:139], a[160:163]\n"
      "s_sub_u32 s76, 18432, s78\n"
      "v_mfma_f32_16x16x32_bf16 a[164:167], a[72:75], v[140:143], a[164:167]\n"
      "s_mov_b32 s63, 0x40000\n"
      "v_mfma_f32_16x16x32_bf16 a[168:171], a[76:79], v[136:139], a[168:171]\n"
      "v_mov_b32 v198, 0\n"
      "v_mfma_f32_16x16x32_bf16 a[172:175], a[76:79], v[140:143], a[172:175]\n"
      "v_and_b32 v197, 0xf0, v241\n"
      "s_waitcnt lgkmcnt(0)\n"
      "v_mfma_f32_16x16x32_bf16 a[144:147], a[96:99], v[152:155], a[144:147]\n"
      "v_lshrrev_b32 v196, 3, v241\n"
      "v_mfma_f32_16x16x32_bf16 a[148:151], a[96:99], v[156:159], a[148:151]\n"
      "v_and_b32 v196, 32, v196\n"
      "v_mfma_f32_16x16x32_bf16 a[152:155], a[100:103], v[152:155], a[152:155]\n"
      "v_lshl_or_b32 v196, v197, 4, v196\n"
      "v_mfma_f32_16x16x32_bf16 a[156:159], a[100:103], v[156:159], a[156:159]\n"
      "v_lshrrev_b32 v199, 5, v241\n"
      "v_mfma_f32_16x16x32_bf16 a[160:163], a[104:107], v[152:155], a[160:163]\n"
      "v_and_or_b32 v196, v199, 16, v196\n"
      "v_mfma_f32_16x16x32_bf16 a[164:167], a[104:107], v[156:159], a[164:167]\n"
      "v_mov_b32 v191, s40\n"
      "v_mfma_f32_16x16x32_bf16 a[168:171], a[108:111], v[152:155], a[168:171]\n"
      "v_mfma_f32_16x16x32_bf16 a[172:175], a[108:111], v[156:159], a[172:175]\n"
      "s_waitcnt vmcnt(0)\n"
      "v_subrev_u32 v205, s82, v206\n"
      "v_cmp_ne_u32 vcc, s40, v207\n"
      "s_cbranch_vccnz LAG_%=\n"
      "v_cmp_lt_i32 vcc, 10, v205\n"
      "s_cbranch_vccnz LAG_%=\n"
      "READ_%=:\n"
      "buffer_load_dwordx2 v[160:161], v240, s[24:27], s76 offen sc1\n"
      "buffer_load_dwordx4 v[96:99], v241, s[24:27], s76 offen offset:0 sc1\n"
      "buffer_load_dwordx4 v[100:103], v241, s[24:27], s76 offen offset:1024 sc1\n"
      "buffer_load_dwordx4 v[104:107], v241, s[24:27], s76 offen offset:2048 sc1\n"
      "buffer_load_dwordx4 v[108:111], v241, s[24:27], s76 offen offset:3072 sc1\n"
      "v_mfma_f32_16x16x32_bf16 v[0:3], a[64:67], v[128:131], v[0:3]\n"
      "v_mfma_f32_16x16x32_bf16 v[4:7], a[64:67], v[132:135], v[4:7]\n"
      "ds_write_b128 v232, a[144:147] offset:32768\n"
      "v_mfma_f32_16x16x32_bf16 v[8:11], a[68:71], v[128:131], v[8:11]\n"
      "v_mfma_f32_16x16x32_bf16 v[12:15], a[68:71], v[132:135], v[12:15]\n"
      "ds_write_b128 v232, a[148:151] offset:33792\n"
      "v_mfma_f32_16x16x32_bf16 v[16:19], a[72:75], v[128:131], v[16:19]\n"
      "v_mfma_f32_16x16x32_bf16 v[20:23], a[72:75], v[132:135], v[20:23]\n"
      "ds_write_b128 v232, a[152:155] offset:34816\n"
      "v_mfma_f32_16x16x32_bf16 v[24:27], a[76:79], v[128:131], v[24:27]\n"
      "v_mfma_f32_16x16x32_bf16 v[28:31], a[76:79], v[132:135], v[28:31]\n"
      "ds_write_b128 v232, a[156:159] offset:35840\n"
      "v_mfma_f32_16x16x32_bf16 v[0:3], a[96:99], v[144:147], v[0:3]\n"
      "v_mfma_f32_16x16x32_bf16 v[4:7], a[96:99], v[148:151], v[4:7]\n"
      "ds_write_b128 v232, a[160:163] offset:36864\n"
      "v_mfma_f32_16x16x32_bf16 v[8:11], a[100:103], v[144:147], v[8:11]\n"
      "v_mfma_f32_16x16x32_bf16 v[12:15], a[100:103], v[148:151], v[12:15]\n"
      "ds_write_b128 v232, a[164:167] offset:37888\n"
      "v_mfma_f32_16x16x32_bf16 v[16:19], a[104:107], v[144:147], v[16:19]\n"
      "v_mfma_f32_16x16x32_bf16 v[20:23], a[104:107], v[148:151], v[20:23]\n"
      "ds_write_b128 v232, a[168:171] offset:38912\n"
      "v_mfma_f32_16x16x32_bf16 v[24:27], a[108:111], v[144:147], v[24:27]\n"
      "v_mfma_f32_16x16x32_bf16 v[28:31], a[108:111], v[148:151], v[28:31]\n"
      "ds_write_b128 v232, a[172:175] offset:39936\n"
      "s_waitcnt lgkmcnt(0)\n"
      "s_barrier\n"
      "ds_read_b128 v[64:67], v233 offset:32768\n"
      "ds_read_b128 v[68:71], v233 offset:33792\n"
      "ds_read_b128 v[72:75], v233 offset:34816\n"
      "ds_read_b128 v[76:79], v233 offset:35840\n"
      "ds_read_b128 v[80:83], v233 offset:36864\n"
      "ds_read_b128 v[84:87], v233 offset:37888\n"
      "ds_read_b128 v[88:91], v233 offset:38912\n"
      "ds_read_b128 v[92:95], v233 offset:39936\n"
      "s_waitcnt lgkmcnt(6)\n"
      "v_pk_add_f32 v[0:1], v[0:1], v[64:65]\n"
      "v_pk_add_f32 v[2:3], v[2:3], v[66:67]\n"
      "v_pk_add_f32 v[4:5], v[4:5], v[68:69]\n"
      "v_pk_add_f32 v[6:7], v[6:7], v[70:71]\n"
      "s_waitcnt lgkmcnt(4)\n"
      "v_pk_add_f32 v[8:9], v[8:9], v[72:73]\n"
      "v_pk_add_f32 v[10:11], v[10:11], v[74:75]\n"
      "v_pk_add_f32 v[12:13], v[12:13], v[76:77]\n"
      "v_pk_add_f32 v[14:15], v[14:15], v[78:79]\n"
      "s_waitcnt lgkmcnt(2)\n"
      "v_pk_add_f32 v[16:17], v[16:17], v[80:81]\n"
      "v_pk_add_f32 v[18:19], v[18:19], v[82:83]\n"
      "v_pk_add_f32 v[20:21], v[20:21], v[84:85]\n"
      "v_pk_add_f32 v[22:23], v[22:23], v[86:87]\n"
      "s_waitcnt lgkmcnt(0)\n"
      "v_pk_add_f32 v[24:25], v[24:25], v[88:89]\n"
      "v_pk_add_f32 v[26:27], v[26:27], v[90:91]\n"
      "v_pk_add_f32 v[28:29], v[28:29], v[92:93]\n"
      "v_pk_add_f32 v[30:31], v[30:31], v[94:95]\n"
      "TRY_%=:\n"
      "s_waitcnt vmcnt(4)\n"
      "v_mov_b32 v162, v160\n"
      "v_add_u32 v231, s40, v160\n"
      "s_nop 0\n"
      "v_permlane32_swap_b32 v160, v162\n"
      "v_sub_f32 v168, v160, v228\n"
      "v_min_f32 v172, 0, -v168\n"
      "v_min_f32 v168, 0, v168\n"
      "v_exp_f32 v168, v168\n"
      "v_exp_f32 v172, v172\n"
      "v_mul_f32 v163, v229, v168\n"
      "v_fmac_f32 v163, v162, v172\n"
      "v_rcp_f32 v163, v163\n"
      "s_nop 0\n"
      "v_mul_f32 v168, v168, v163\n"
      "v_mul_f32 v172, v172, v163\n"
      "v_mov_b32 v169, v168\n"
      "v_mov_b32 v173, v172\n"
      "s_nop 0\n"
      "v_permlane16_swap_b32 v168, v169\n"
      "v_permlane16_swap_b32 v172, v173\n"
      "v_pk_mul_f32 v[32:33], v[0:1], v[168:169] op_sel:[0,0] op_sel_hi:[1,0]\n"
      "v_pk_mul_f32 v[34:35], v[2:3], v[168:169] op_sel:[0,0] op_sel_hi:[1,0]\n"
      "v_pk_mul_f32 v[36:37], v[4:5], v[168:169] op_sel:[0,1] op_sel_hi:[1,1]\n"
      "v_pk_mul_f32 v[38:39], v[6:7], v[168:169] op_sel:[0,1] op_sel_hi:[1,1]\n"
      "v_pk_mul_f32 v[40:41], v[8:9], v[168:169] op_sel:[0,0] op_sel_hi:[1,0]\n"
      "v_pk_mul_f32 v[42:43], v[10:11], v[168:169] op_sel:[0,0] op_sel_hi:[1,0]\n"
      "v_pk_mul_f32 v[44:45], v[12:13], v[168:169] op_sel:[0,1] op_sel_hi:[1,1]\n"
      "v_pk_mul_f32 v[46:47], v[14:15], v[168:169] op_sel:[0,1] op_sel_hi:[1,1]\n"
      "v_pk_mul_f32 v[48:49], v[16:17], v[168:169] op_sel:[0,0] op_sel_hi:[1,0]\n"
      "v_pk_mul_f32 v[50:51], v[18:19], v[168:169] op_sel:[0,0] op_sel_hi:[1,0]\n"
      "v_pk_mul_f32 v[52:53], v[20:21], v[168:169] op_sel:[0,1] op_sel_hi:[1,1]\n"
      "v_pk_mul_f32 v[54:55], v[22:23], v[168:169] op_sel:[0,1] op_sel_hi:[1,1]\n"
      "v_pk_mul_f32 v[56:57], v[24:25], v[168:169] op_sel:[0,0] op_sel_hi:[1,0]\n"
      "v_pk_mul_f32 v[58:59], v[26:27], v[168:169] op_sel:[0,0] op_sel_hi:[1,0]\n"
      "v_pk_mul_f32 v[60:61], v[28:29], v[168:169] op_sel:[0,1] op_sel_hi:[1,1]\n"
      "v_pk_mul_f32 v[62:63], v[30:31], v[168:169] op_sel:[0,1] op_sel_hi:[1,1]\n"
      "s_waitcnt vmcnt(3)\n"
      "v_add3_u32 v231, v231, v96, v97\n"
      "v_add3_u32 v231, v231, v98, v99\n"
      "s_waitcnt vmcnt(2)\n"
      "v_add3_u32 v231, v231, v100, v101\n"
      "v_add3_u32 v231, v231, v102, v103\n"
      "s_waitcnt vmcnt(1)\n"
      "v_add3_u32 v231, v231, v104, v105\n"
      "v_add3_u32 v231, v231, v106, v107\n"
      "s_waitcnt vmcnt(0)\n"
      "v_add3_u32 v231, v231, v108, v109\n"
      "v_add3_u32 v231, v231, v110, v111\n"
      "v_cmp_ne_u32 vcc, v231, v161\n"
      "s_cbranch_vccnz RETRY_%=\n"
      "GOT_%=:\n"
      "v_lshlrev_b32 v0, 16, v96\n"
      "v_and_b32 v1, 0xffff0000, v96\n"
      "v_lshlrev_b32 v2, 16, v97\n"
      "v_and_b32 v3, 0xffff0000, v97\n"
      "v_lshlrev_b32 v8, 16, v100\n"
      "v_and_b32 v9, 0xffff0000, v100\n"
      "v_lshlrev_b32 v10, 16, v101\n"
      "v_and_b32 v11, 0xffff0000, v101\n"
      "v_pk_fma_f32 v[32:33], v[0:1], v[172:173], v[32:33] op_sel:[0,0,0] op_sel_hi:[1,0,1]\n"
      "v_pk_fma_f32 v[34:35], v[2:3], v[172:173], v[34:35] op_sel:[0,0,0] op_sel_hi:[1,0,1]\n"
      "v_pk_fma_f32 v[40:41], v[8:9], v[172:173], v[40:41] op_sel:[0,0,0] op_sel_hi:[1,0,1]\n"
      "v_pk_fma_f32 v[42:43], v[10:11], v[172:173], v[42:43] op_sel:[0,0,0] op_sel_hi:[1,0,1]\n"
      "v_cvt_pk_bf16_f32 v112, v32, v33\n"
      "v_cvt_pk_bf16_f32 v114, v40, v41\n"
      "v_cvt_pk_bf16_f32 v113, v34, v35\n"
      "v_cvt_pk_bf16_f32 v115, v42, v43\n"
      "v_permlane16_swap_b32 v112, v114\n"
      "s_nop 0\n"
      "v_permlane16_swap_b32 v113, v115\n"
      "global_store_dwordx4 v196, v[112:115], s[30:31] offset:-4096 sc1 nt\n"
      "v_lshlrev_b32 v16, 16, v104\n"
      "v_and_b32 v17, 0xffff0000, v104\n"
      "v_lshlrev_b32 v18, 16, v105\n"
      "v_and_b32 v19, 0xffff0000, v105\n"
      "v_lshlrev_b32 v24, 16, v108\n"
      "v_and_b32 v25, 0xffff0000, v108\n"
      "v_lshlrev_b32 v26, 16, v109\n"
      "v_and_b32 v27, 0xffff0000, v109\n"
      "v_pk_fma_f32 v[48:49], v[16:17], v[172:173], v[48:49] op_sel:[0,0,0] op_sel_hi:[1,0,1]\n"
      "v_pk_fma_f32 v[50:51], v[18:19], v[172:173], v[50:51] op_sel:[0,0,0] op_sel_hi:[1,0,1]\n"
      "v_pk_fma_f32 v[56:57], v[24:25], v[172:173], v[56:57] op_sel:[0,0,0] op_sel_hi:[1,0,1]\n"
      "v_pk_fma_f32 v[58:59], v[26:27], v[172:173], v[58:59] op_sel:[0,0,0] op_sel_hi:[1,0,1]\n"
      "v_cvt_pk_bf16_f32 v116, v48, v49\n"
      "v_cvt_pk_bf16_f32 v118, v56, v57\n"
      "v_cvt_pk_bf16_f32 v117, v50, v51\n"
      "v_cvt_pk_bf16_f32 v119, v58, v59\n"
      "v_permlane16_swap_b32 v116, v118\n"
      "s_nop 0\n"
      "v_permlane16_swap_b32 v117, v119\n"
      "global_store_dwordx4 v196, v[116:119], s[30:31] offset:-4032 sc1 nt\n"
      "v_lshlrev_b32 v4, 16, v98\n"
      "v_and_b32 v5, 0xffff0000, v98\n"
      "v_lshlrev_b32 v6, 16, v99\n"
      "v_and_b32 v7, 0xffff0000, v99\n"
      "v_lshlrev_b32 v12, 16, v102\n"
      "v_and_b32 v13, 0xffff0000, v102\n"
      "v_lshlrev_b32 v14, 16, v103\n"
      "v_and_b32 v15, 0xffff0000, v103\n"
      "v_pk_fma_f32 v[36:37], v[4:5], v[172:173], v[36:37] op_sel:[0,1,0] op_sel_hi:[1,1,1]\n"
      "v_pk_fma_f32 v[38:39], v[6:7], v[172:173], v[38:39] op_sel:[0,1,0] op_sel_hi:[1,1,1]\n"
      "v_pk_fma_f32 v[44:45], v[12:13], v[172:173], v[44:45] op_sel:[0,1,0] op_sel_hi:[1,1,1]\n"
      "v_pk_fma_f32 v[46:47], v[14:15], v[172:173], v[46:47] op_sel:[0,1,0] op_sel_hi:[1,1,1]\n"
      "v_cvt_pk_bf16_f32 v120, v36, v37\n"
      "v_cvt_pk_bf16_f32 v122, v44, v45\n"
      "v_cvt_pk_bf16_f32 v121, v38, v39\n"
      "v_cvt_pk_bf16_f32 v123, v46, v47\n"
      "v_permlane16_swap_b32 v120, v122\n"
      "s_nop 0\n"
      "v_permlane16_swap_b32 v121, v123\n"
      "global_store_dwordx4 v196, v[120:123], s[30:31] offset:0 sc1 nt\n"
      "v_lshlrev_b32 v20, 16, v106\n"
      "v_and_b32 v21, 0xffff0000, v106\n"
      "v_lshlrev_b32 v22, 16, v107\n"
      "v_and_b32 v23, 0xffff0000, v107\n"
      "v_lshlrev_b32 v28, 16, v110\n"
      "v_and_b32 v29, 0xffff0000, v110\n"
      "v_lshlrev_b32 v30, 16, v111\n"
      "v_and_b32 v31, 0xffff0000, v111\n"
      "v_pk_fma_f32 v[52:53], v[20:21], v[172:173], v[52:53] op_sel:[0,1,0] op_sel_hi:[1,1,1]\n"
      "v_pk_fma_f32 v[54:55], v[22:23], v[172:173], v[54:55] op_sel:[0,1,0] op_sel_hi:[1,1,1]\n"
      "v_pk_fma_f32 v[60:61], v[28:29], v[172:173], v[60:61] op_sel:[0,1,0] op_sel_hi:[1,1,1]\n"
      "v_pk_fma_f32 v[62:63], v[30:31], v[172:173], v[62:63] op_sel:[0,1,0] op_sel_hi:[1,1,1]\n"
      "v_cvt_pk_bf16_f32 v124, v52, v53\n"
      "v_cvt_pk_bf16_f32 v126, v60, v61\n"
      "v_cvt_pk_bf16_f32 v125, v54, v55\n"
      "v_cvt_pk_bf16_f32 v127, v62, v63\n"
      "v_permlane16_swap_b32 v124, v126\n"
      "s_nop 0\n"
      "v_permlane16_swap_b32 v125, v127\n"
      "global_store_dwordx4 v196, v[124:127], s[30:31] offset:64 sc1 nt\n"
      "s_mov_b64 exec, 1\n"
      "global_store_dword v198, v191, s[46:47] sc1\n"
      "s_mov_b64 exec, -1\n"
      "ENDE_%=:\n"
      ".subsection 1\n"
      "MH_%=:\n"
      "s_cmp_lg_u32 s54, 0\n"
      "s_waitcnt vmcnt(12) lgkmcnt(0)\n"
      "s_cbranch_scc1 W6_%=\n"
      "s_waitcnt vmcnt(8)\n"
      "W6_%=:\n"
      "s_barrier\n"
      "v_mfma_f32_16x16x32_bf16 v[24:27], a[108:111], v[144:147], v[24:27]\n"
      "s_xor_b64 s[56:57], s[56:57], s[32:33]\n"
      "ds_read_b128 a[64:67], v242 offset:0\n"
      "ds_read_b128 a[72:75], v243 offset:0\n"
      "v_mfma_f32_16x16x32_bf16 v[32:35], a[112:115], v[144:147], v[32:35]\n"
      "s_add_u32 s61, s61, 1\n"
      "ds_read_b128 a[80:83], v242 offset:1024\n"
      "ds_read_b128 a[88:91], v243 offset:1024\n"
      "v_mfma_f32_16x16x32_bf16 v[36:39], a[112:115], v[148:151], v[36:39]\n"
      "s_add_u32 s62, s62, 0x4000\n"
      "v_exp_f32 v189, v89\n"
      "v_mfma_f32_16x16x32_bf16 a[176:179], a[112:115], v[152:155], a[176:179]\n"
      "s_and_b32 s62, s62, 0x1ffff\n"
      "v_exp_f32 v190, v90\n"
      "v_mfma_f32_16x16x32_bf16 a[180:183], a[112:115], v[156:159], a[180:183]\n"
      "v_readlane_b32 s60, v219, s61\n"
      "v_exp_f32 v191, v91\n"
      "v_mfma_f32_16x16x32_bf16 a[188:191], a[116:119], v[156:159], a[188:191]\n"
      "s_add_u32 s66, s66, s86\n"
      "v_cvt_pk_bf16_f32 v140, v188, v189\n"
      "v_mfma_f32_16x16x32_bf16 a[184:187], a[116:119], v[152:155], a[184:187]\n"
      "v_exp_f32 v192, v92\n"
      "v_mfma_f32_16x16x32_bf16 v[44:47], a[116:119], v[148:151], v[44:47]\n"
      "s_add_u32 s82, s82, s86\n"
      "v_exp_f32 v193, v93\n"
      "v_mfma_f32_16x16x32_bf16 v[40:43], a[116:119], v[144:147], v[40:43]\n"
      "s_add_u32 s65, s65, s86\n"
      "v_cvt_pk_bf16_f32 v141, v190, v191\n"
      "v_cvt_pk_bf16_f32 v142, v192, v193\n"
      "v_mfma_f32_16x16x32_bf16 v[48:51], a[120:123], v[144:147], v[48:51]\n"
      "s_max_u32 s88, s66, s65\n"
      "v_exp_f32 v194, v94\n"
      "v_mfma_f32_16x16x32_bf16 v[52:55], a[120:123], v[148:151], v[52:55]\n"
      "v_exp_f32 v195, v95\n"
      "s_waitcnt lgkmcnt(0)\n"
      "v_mfma_f32_16x16x32_bf16 a[192:195], a[120:123], v[152:155], a[192:195]\n"
      "v_cvt_pk_bf16_f32 v143, v194, v195\n"
      "v_xor_b32 v244, 0x10000, v244\n"
      "v_mfma_f32_16x16x32_bf16 a[196:199], a[120:123], v[156:159], a[196:199]\n"
      "ds_read_b128 a[68:71], v242 offset:4096\n"
      "ds_read_b128 a[76:79], v243 offset:4096\n"
      "v_mfma_f32_16x16x32_bf16 v[184:187], a[140:143], v[140:143], v[184:187]\n"
      "s_mov_b32 m0, s62\n"
      "ds_read_b128 a[84:87], v242 offset:5120\n"
      "ds_read_b128 a[92:95], v243 offset:5120\n"
      "v_mfma_f32_16x16x32_bf16 a[204:207], a[124:127], v[156:159], a[204:207]\n"
      "buffer_load_dwordx4 v228, s[56:59], s60 offen nt lds\n"
      "v_xor_b32 v245, 0x10000, v245\n"
      "v_xor_b32 v246, 0x10000, v246\n"
      "v_mfma_f32_16x16x32_bf16 a[200:203], a[124:127], v[152:155], a[200:203]\n"
      "buffer_load_dwordx4 v229, s[56:59], s60 offen offset:1024 nt lds\n"
      "v_xor_b32 v247, 0x10000, v247\n"
      "v_max3_f32 v196, v184, v185, v186\n"
      "v_mfma_f32_16x16x32_bf16 v[60:63], a[124:127], v[148:151], v[60:63]\n"
      "buffer_load_dwordx4 v230, s[56:59], s60 offen offset:2048 nt lds\n"
      "v_max_f32 v196, v196, v187\n"
      "v_cmp_nge_f32 vcc, 0x4b800000, v196\n"
      "v_mfma_f32_16x16x32_bf16 v[56:59], a[124:127], v[144:147], v[56:59]\n"
      "buffer_load_dwordx4 v231, s[56:59], s60 offen offset:3072 nt lds\n"
      "s_cbranch_vccnz FS11_%=\n"
      "FR11_%=:\n"
      "v_mfma_f32_16x16x32_bf16 v[64:67], a[64:67], a[0:3], v[160:163]\n"
      "v_exp_f32 v188, v96\n"
      "v_mfma_f32_16x16x32_bf16 v[64:67], a[72:75], a[16:19], v[64:67]\n"
      "v_add_f32 v180, v180, v184\n"
      "v_add_f32 v181, v181, v185\n"
      "v_mfma_f32_16x16x32_bf16 v[64:67], a[80:83], a[32:35], v[64:67]\n"
      "v_exp_f32 v189, v97\n"
      "v_mfma_f32_16x16x32_bf16 v[64:67], a[88:91], a[48:51], v[64:67]\n"
      "v_add_f32 v182, v182, v186\n"
      "v_add_f32 v183, v183, v187\n"
      "v_mfma_f32_16x16x32_bf16 v[72:75], a[64:67], a[4:7], v[164:167]\n"
      "v_exp_f32 v190, v98\n"
      "v_mfma_f32_16x16x32_bf16 v[72:75], a[72:75], a[20:23], v[72:75]\n"
      "v_exp_f32 v191, v99\n"
      "v_mfma_f32_16x16x32_bf16 v[72:75], a[80:83], a[36:39], v[72:75]\n"
      "v_cvt_pk_bf16_f32 v144, v188, v189\n"
      "v_cvt_pk_bf16_f32 v145, v190, v191\n"
      "v_mfma_f32_16x16x32_bf16 v[72:75], a[88:91], a[52:55], v[72:75]\n"
      "v_exp_f32 v192, v100\n"
      "v_mfma_f32_16x16x32_bf16 v[80:83], a[64:67], a[8:11], v[168:171]\n"
      "s_add_u32 s39, s39, s39\n"
      "v_exp_f32 v193, v101\n"
      "v_mfma_f32_16x16x32_bf16 v[80:83], a[72:75], a[24:27], v[80:83]\n"
      "s_memrealtime s[68:69]\n"
      "v_exp_f32 v194, v102\n"
      "v_mfma_f32_16x16x32_bf16 v[80:83], a[80:83], a[40:43], v[80:83]\n"
      "v_exp_f32 v195, v103\n"
      "v_mfma_f32_16x16x32_bf16 v[80:83], a[88:91], a[56:59], v[80:83]\n"
      "v_cvt_pk_bf16_f32 v146, v192, v193\n"
      "v_cvt_pk_bf16_f32 v147, v194, v195\n"
      "v_mfma_f32_16x16x32_bf16 v[88:91], a[64:67], a[12:15], v[172:175]\n"
      "s_nop 0\n"
      "v_mfma_f32_16x16x32_bf16 v[184:187], a[128:131], v[144:147], 0\n"
      "s_nop 0\n"
      "v_mfma_f32_16x16x32_bf16 v[88:91], a[72:75], a[28:31], v[88:91]\n"
      "s_nop 0\n"
      "v_mfma_f32_16x16x32_bf16 v[88:91], a[80:83], a[44:47], v[88:91]\n"
      "s_nop 0\n"
      "s_cbranch_scc1 SP1_%=\n"
      "s_branch HD1_%=\n"
      "HOOK_%=:\n"
      "buffer_load_dword v250, v251, s[20:23], 0 offen sc1\n"
      "s_swappc_b64 s[84:85], s[84:85]\n"
      "s_waitcnt vmcnt(12)\n"
      "v_subrev_u32 v250, s71, v250\n"
      "v_min_u32 v250, 0x258, v250\n"
      "s_nop 1\n"
      "v_add_u32_dpp v250, v250, v250 quad_perm:[1,0,3,2] row_mask:0xf bank_mask:0xf\n"
      "s_nop 1\n"
      "v_add_u32_dpp v250, v250, v250 quad_perm:[2,3,0,1] row_mask:0xf bank_mask:0xf\n"
      "s_nop 1\n"
      "v_add_u32_dpp v250, v250, v250 row_half_mirror row_mask:0xf bank_mask:0xf\n"
      "s_nop 0\n"
      "v_readfirstlane_b32 s67, v250\n"
      "s_sub_u32 s66, s66, s81\n"
      "s_lshr_b32 s67, s67, 3\n"
      "s_add_u32 s66, s66, s67\n"
      "s_max_u32 s88, s66, s65\n"
      "s_mov_b32 s86, 57\n"
      "s_mov_b32 s77, 171\n"
      "v_bfe_u32 v251, v241, 2, 7\n"
      "v_add_u32 v251, s79, v251\n"
      "buffer_load_dword v250, v251, s[20:23], 0 offen sc1\n"
      "s_swappc_b64 s[84:85], s[84:85]\n"
      "s_mov_b32 s86, 55\n"
      "s_mov_b32 s77, 165\n"
      "s_swappc_b64 s[84:85], s[84:85]\n"
      "s_mov_b32 s89, 0\n"
      "s_swappc_b64 s[84:85], s[84:85]\n"
      "s_mov_b32 s86, 54\n"
      "s_mov_b32 s77, 162\n"
      "s_swappc_b64 s[84:85], s[84:85]\n"
      "s_cmp_lt_u32 s89, 2\n"
      "s_cselect_b32 s67, 2048, s72\n"
      "s_mov_b32 s89, 0\n"
      "s_cmp_lt_u32 s67, 2048\n"
      "s_cbranch_scc0 NV0_%=\n"
      "v_and_b32 v216, 0x1f0, v241\n"
      "v_lshlrev_b32 v216, 4, v216\n"
      "v_mov_b32 v215, s82\n"
      "s_lshr_b32 s67, s67, 4\n"
      "s_add_u32 s67, s67, 8192\n"
      "buffer_store_dword v215, v216, s[20:23], s67 offen sc1\n"
      "NV0_%=:\n"
      "s_swappc_b64 s[84:85], s[84:85]\n"
      "buffer_load_dword v250, v251, s[20:23], 0 offen sc1\n"
      "s_swappc_b64 s[84:85], s[84:85]\n"
      "s_mov_b32 s86, 53\n"
      "s_mov_b32 s77, 159\n"
      "s_min_i32 s70, s70, 150\n"
      "s_waitcnt vmcnt(12)\n"
      "v_sub_u32 v250, s82, v250\n"
      "v_cmp_gt_u32 vcc, 1600, v250\n"
      "s_movk_i32 s76, 36\n"
      "s_nop 2\n"
      "s_bcnt1_i32_b32 s67, vcc_lo\n"
      "s_cmp_eq_u32 s70, 60\n"
      "s_cselect_b32 s67, 0, s67\n"
      "s_cmp_ge_u32 s67, 10\n"
      "s_cbranch_scc1 SM0_%=\n"
      "s_swappc_b64 s[84:85], s[84:85]\n"
      "s_cmp_lt_u32 s89, 4\n"
      "s_cselect_b32 s67, 2048, s72\n"
      "s_mov_b32 s89, 0\n"
      "s_cmp_lt_u32 s67, 2048\n"
      "s_cbranch_scc0 NV1_%=\n"
      "v_and_b32 v216, 0x1f0, v241\n"
      "v_lshlrev_b32 v216, 4, v216\n"
      "v_mov_b32 v215, s82\n"
      "s_lshr_b32 s67, s67, 4\n"
      "s_add_u32 s67, s67, 8192\n"
      "buffer_store_dword v215, v216, s[20:23], s67 offen sc1\n"
      "NV1_%=:\n"
      "s_swappc_b64 s[84:85], s[84:85]\n"
      "buffer_load_dword v250, v251, s[20:23], 0 offen sc1\n"
      "s_swappc_b64 s[84:85], s[84:85]\n"
      "s_waitcnt vmcnt(12)\n"
      "v_sub_u32 v250, s82, v250\n"
      "v_cmp_gt_u32 vcc, 1600, v250\n"
      "s_movk_i32 s76, 56\n"
      "s_nop 2\n"
      "s_bcnt1_i32_b32 s67, vcc_lo\n"
      "s_cmp_eq_u32 s70, 60\n"
      "s_cselect_b32 s67, 0, s67\n"
      "s_cmp_ge_u32 s67, 10\n"
      "s_cbranch_scc1 SM1_%=\n"
      "s_swappc_b64 s[84:85], s[84:85]\n"
      "s_getpc_b64 s[74:75]\n"
      "PF_%=:\n"
      "s_sub_u32 s74, s74, PF_%=-END_%=\n"
      "s_subb_u32 s75, s75, 0\n"
      "s_lshl_b32 s76, %[blk], 7\n"
      "s_and_b32 s76, s76, 0x1c00\n"
      "s_min_u32 s76, s76, ENDE_%=-END_%=-1024\n"
      "v_add_u32 v200, s76, v241\n"
      "global_load_dwordx4 v[200:203], v200, s[74:75]\n"
      "v_mov_b32 v212, 0\n"
      "global_load_dword v213, v212, s[30:31] nt\n"
      "buffer_load_dword v214, off, s[24:27], s78 nt\n"
      "s_swappc_b64 s[84:85], s[84:85]\n"
      "SM1_%=:\n"
      "s_add_u32 s66, s66, s76\n"
      "s_max_u32 s88, s66, s65\n"
      "s_min_i32 s70, s70, 150\n"
      "s_branch SM2_%=\n"
      "SM0_%=:\n"
      "s_or_b32 s39, s39, 0xc0000000\n"
      "s_mov_b32 s52, 0xaaaaaaaa\n"
      "s_mov_b32 s53, 0xaaaaaaaa\n"
      "s_add_u32 s66, s66, s76\n"
      "s_max_u32 s88, s66, s65\n"
      "s_min_i32 s70, s70, 150\n"
      "s_mov_b32 s86, 59\n"
      "s_mov_b32 s77, 177\n"
      "s_swappc_b64 s[84:85], s[84:85]\n"
      "s_mov_b32 s86, 57\n"
      "s_mov_b32 s77, 171\n"
      "s_swappc_b64 s[84:85], s[84:85]\n"
      "SM2_%=:\n"
      "s_mov_b32 s86, 55\n"
      "s_mov_b32 s77, 165\n"
      "s_mov_b32 s52, 0x0\n"
      "s_mov_b32 s53, 0x80000\n"
      "s_swappc_b64 s[84:85], s[84:85]\n"
      "s_getpc_b64 s[74:75]\n"
      "PG_%=:\n"
      "s_sub_u32 s74, s74, PG_%=-END_%=\n"
      "s_subb_u32 s75, s75, 0\n"
      "s_lshl_b32 s76, %[blk], 7\n"
      "s_and_b32 s76, s76, 0x1c00\n"
      "s_min_u32 s76, s76, ENDE_%=-END_%=-1024\n"
      "v_add_u32 v200, s76, v241\n"
      "global_load_dwordx4 v[200:203], v200, s[74:75]\n"
      "v_mov_b32 v212, 0\n"
      "global_load_dword v213, v212, s[30:31] nt\n"
      "buffer_load_dword v214, off, s[24:27], s78 nt\n"
      "s_swappc_b64 s[84:85], s[84:85]\n"
      "GW0_%=:\n"
      "s_memrealtime s[68:69]\n"
      "s_waitcnt lgkmcnt(0)\n"
      "s_sub_u32 s67, s68, s88\n"
      "s_cmp_ge_i32 s67, 0\n"
      "s_cbranch_scc1 GX0_%=\n"
      "s_branch GW0_%=\n"
      "FS1_%=:\n"
      "s_call_b64 s[96:97], FB1_%=\n"
      "s_branch FR1_%=\n"
      "FS2_%=:\n"
      "s_call_b64 s[96:97], FB0_%=\n"
      "s_branch FR2_%=\n"
      "FS3_%=:\n"
      "s_call_b64 s[96:97], FB0_%=\n"
      "s_branch FR3_%=\n"
      "GW4_%=:\n"
      "s_memrealtime s[68:69]\n"
      "s_waitcnt lgkmcnt(0)\n"
      "s_sub_u32 s67, s68, s88\n"
      "s_cmp_ge_i32 s67, 0\n"
      "s_cbranch_scc1 GX4_%=\n"
      "s_branch GW4_%=\n"
      "GW5_%=:\n"
      "s_memrealtime s[68:69]\n"
      "s_waitcnt lgkmcnt(0)\n"
      "s_sub_u32 s67, s68, s88\n"
      "s_cmp_ge_i32 s67, 0\n"
      "s_cbranch_scc1 GX5_%=\n"
      "s_branch GW5_%=\n"
      "GW6_%=:\n"
      "s_memrealtime s[68:69]\n"
      "s_waitcnt lgkmcnt(0)\n"
      "s_sub_u32 s67, s68, s88\n"
      "s_cmp_ge_i32 s67, 0\n"
      "s_cbranch_scc1 GX6_%=\n"
      "s_branch GW6_%=\n"
      "GW7_%=:\n"
      "s_memrealtime s[68:69]\n"
      "s_waitcnt lgkmcnt(0)\n"
      "s_sub_u32 s67, s68, s88\n"
      "s_cmp_ge_i32 s67, 0\n"
      "s_cbranch_scc1 GX7_%=\n"
      "s_branch GW7_%=\n"
      "FS8_%=:\n"
      "s_call_b64 s[96:97], FB1_%=\n"
      "s_branch FR8_%=\n"
      "LAG_%=:\n"
      "s_memrealtime s[68:69]\n"
      "s_waitcnt lgkmcnt(0)\n"
      "s_mov_b32 s88, s68\n"
      "LG1_%=:\n"
      "v_cmp_ne_u32 vcc, s40, v207\n"
      "s_cbranch_vccz LG2_%=\n"
      "s_sub_u32 s63, s63, 1\n"
      "s_cmp_eq_u32 s63, 0\n"
      "s_cbranch_scc1 READ_%=\n"
      "s_sleep 4\n"
      "global_load_dwordx2 v[206:207], v239, s[46:47] offset:8 sc1\n"
      "s_waitcnt vmcnt(0)\n"
      "s_branch LG1_%=\n"
      "LG2_%=:\n"
      "v_readfirstlane_b32 s67, v206\n"
      "s_sub_u32 s88, s88, s82\n"
      "s_min_u32 s88, s88, 400\n"
      "s_add_u32 s88, s88, s67\n"
      "LG3_%=:\n"
      "s_memrealtime s[68:69]\n"
      "s_waitcnt lgkmcnt(0)\n"
      "s_sub_u32 s67, s88, s68\n"
      "s_sub_u32 s67, s67, 11\n"
      "s_cmp_lt_u32 s67, 400\n"
      "s_cbranch_scc1 LG3_%=\n"
      "s_branch READ_%=\n"
      "RETRY_%=:\n"
      "s_sub_u32 s63, s63, 1\n"
      "s_cbranch_scc1 GOT_%=\n"
      "buffer_load_dwordx2 v[160:161], v240, s[24:27], s76 offen sc1\n"
      "buffer_load_dwordx4 v[96:99], v241, s[24:27], s76 offen offset:0 sc1\n"
      "buffer_load_dwordx4 v[100:103], v241, s[24:27], s76 offen offset:1024 sc1\n"
      "buffer_load_dwordx4 v[104:107], v241, s[24:27], s76 offen offset:2048 sc1\n"
      "buffer_load_dwordx4 v[108:111], v241, s[24:27], s76 offen offset:3072 sc1\n"
      "s_branch TRY_%=\n"
      "FS11_%=:\n"
      "s_call_b64 s[96:97], FB0_%=\n"
      "s_branch FR11_%=\n"
      "COLD_%=:\n"
      "FB0_%=:\n"
      "v_mov_b32 v196, 139248\n"
      "s_nop 7\n"
      "ds_write_b32 v196, v196\n"
      "v_max3_f32 v188, v64, v65, 0\n"
      "v_max3_f32 v189, v72, v73, 0\n"
      "v_max3_f32 v190, v80, v81, 0\n"
      "v_max3_f32 v191, v88, v89, 0\n"
      "v_max3_f32 v188, v188, v66, v67\n"
      "v_max3_f32 v189, v189, v74, v75\n"
      "v_max3_f32 v190, v190, v82, v83\n"
      "v_max3_f32 v191, v191, v90, v91\n"
      "v_max3_f32 v188, v188, v68, v69\n"
      "v_max3_f32 v189, v189, v76, v77\n"
      "v_max3_f32 v190, v190, v84, v85\n"
      "v_max3_f32 v191, v191, v92, v93\n"
      "v_max3_f32 v188, v188, v70, v71\n"
      "v_max3_f32 v189, v189, v78, v79\n"
      "v_max3_f32 v190, v190, v86, v87\n"
      "v_max3_f32 v191, v191, v94, v95\n"
      "v_mov_b32 v192, v188\n"
      "v_mov_b32 v193, v189\n"
      "v_mov_b32 v194, v190\n"
      "v_mov_b32 v195, v191\n"
      "v_permlane16_swap_b32 v188, v192\n"
      "v_permlane16_swap_b32 v189, v193\n"
      "v_permlane16_swap_b32 v190, v194\n"
      "v_permlane16_swap_b32 v191, v195\n"
      "v_max_f32 v188, v188, v192\n"
      "v_max_f32 v189, v189, v193\n"
      "v_max_f32 v190, v190, v194\n"
      "v_max_f32 v191, v191, v195\n"
      "v_mov_b32 v192, v188\n"
      "v_mov_b32 v193, v189\n"
      "v_mov_b32 v194, v190\n"
      "v_mov_b32 v195, v191\n"
      "v_permlane32_swap_b32 v188, v192\n"
      "v_permlane32_swap_b32 v189, v193\n"
      "v_permlane32_swap_b32 v190, v194\n"
      "v_permlane32_swap_b32 v191, v195\n"
      "v_max_f32 v188, v188, v192\n"
      "v_max_f32 v189, v189, v193\n"
      "v_max_f32 v190, v190, v194\n"
      "v_max_f32 v191, v191, v195\n"
      "RS0_%=:\n"
      "v_exp_f32 v192, -v188\n"
      "v_exp_f32 v193, -v189\n"
      "v_exp_f32 v194, -v190\n"
      "v_exp_f32 v195, -v191\n"
      "v_sub_f32 v176, v176, v188\n"
      "v_sub_f32 v177, v177, v189\n"
      "v_sub_f32 v178, v178, v190\n"
      "v_sub_f32 v179, v179, v191\n"
      "v_mov_b32 v160, v176\n"
      "v_mov_b32 v161, v176\n"
      "v_mov_b32 v162, v176\n"
      "v_mov_b32 v163, v176\n"
      "v_mov_b32 v164, v177\n"
      "v_mov_b32 v165, v177\n"
      "v_mov_b32 v166, v177\n"
      "v_mov_b32 v167, v177\n"
      "v_mov_b32 v168, v178\n"
      "v_mov_b32 v169, v178\n"
      "v_mov_b32 v170, v178\n"
      "v_mov_b32 v171, v178\n"
      "v_mov_b32 v172, v179\n"
      "v_mov_b32 v173, v179\n"
      "v_mov_b32 v174, v179\n"
      "v_mov_b32 v175, v179\n"
      "v_sub_f32 v64, v64, v188\n"
      "v_sub_f32 v65, v65, v188\n"
      "v_sub_f32 v66, v66, v188\n"
      "v_sub_f32 v67, v67, v188\n"
      "v_sub_f32 v68, v68, v188\n"
      "v_sub_f32 v69, v69, v188\n"
      "v_sub_f32 v70, v70, v188\n"
      "v_sub_f32 v71, v71, v188\n"
      "v_sub_f32 v72, v72, v189\n"
      "v_sub_f32 v73, v73, v189\n"
      "v_sub_f32 v74, v74, v189\n"
      "v_sub_f32 v75, v75, v189\n"
      "v_sub_f32 v76, v76, v189\n"
      "v_sub_f32 v77, v77, v189\n"
      "v_sub_f32 v78, v78, v189\n"
      "v_sub_f32 v79, v79, v189\n"
      "v_sub_f32 v80, v80, v190\n"
      "v_sub_f32 v81, v81, v190\n"
      "v_sub_f32 v82, v82, v190\n"
      "v_sub_f32 v83, v83, v190\n"
      "v_sub_f32 v84, v84, v190\n"
      "v_sub_f32 v85, v85, v190\n"
      "v_sub_f32 v86, v86, v190\n"
      "v_sub_f32 v87, v87, v190\n"
      "v_sub_f32 v88, v88, v191\n"
      "v_sub_f32 v89, v89, v191\n"
      "v_sub_f32 v90, v90, v191\n"
      "v_sub_f32 v91, v91, v191\n"
      "v_sub_f32 v92, v92, v191\n"
      "v_sub_f32 v93, v93, v191\n"
      "v_sub_f32 v94, v94, v191\n"
      "v_sub_f32 v95, v95, v191\n"
      "v_sub_f32 v96, v96, v188\n"
      "v_sub_f32 v97, v97, v188\n"
      "v_sub_f32 v98, v98, v188\n"
      "v_sub_f32 v99, v99, v188\n"
      "v_sub_f32 v100, v100, v188\n"
      "v_sub_f32 v101, v101, v188\n"
      "v_sub_f32 v102, v102, v188\n"
      "v_sub_f32 v103, v103, v188\n"
      "v_sub_f32 v104, v104, v189\n"
      "v_sub_f32 v105, v105, v189\n"
      "v_sub_f32 v106, v106, v189\n"
      "v_sub_f32 v107, v107, v189\n"
      "v_sub_f32 v108, v108, v189\n"
      "v_sub_f32 v109, v109, v189\n"
      "v_sub_f32 v110, v110, v189\n"
      "v_sub_f32 v111, v111, v189\n"
      "v_sub_f32 v112, v112, v190\n"
      "v_sub_f32 v113, v113, v190\n"
      "v_sub_f32 v114, v114, v190\n"
      "v_sub_f32 v115, v115, v190\n"
      "v_sub_f32 v116, v116, v190\n"
      "v_sub_f32 v117, v117, v190\n"
      "v_sub_f32 v118, v118, v190\n"
      "v_sub_f32 v119, v119, v190\n"
      "v_sub_f32 v120, v120, v191\n"
      "v_sub_f32 v121, v121, v191\n"
      "v_sub_f32 v122, v122, v191\n"
      "v_sub_f32 v123, v123, v191\n"
      "v_sub_f32 v124, v124, v191\n"
      "v_sub_f32 v125, v125, v191\n"
      "v_sub_f32 v126, v126, v191\n"
      "v_sub_f32 v127, v127, v191\n"
      "v_mul_f32 v0, v0, v192\n"
      "v_mul_f32 v1, v1, v192\n"
      "v_mul_f32 v2, v2, v192\n"
      "v_mul_f32 v3, v3, v192\n"
      "v_mul_f32 v4, v4, v193\n"
      "v_mul_f32 v5, v5, v193\n"
      "v_mul_f32 v6, v6, v193\n"
      "v_mul_f32 v7, v7, v193\n"
      "v_mul_f32 v8, v8, v192\n"
      "v_mul_f32 v9, v9, v192\n"
      "v_mul_f32 v10, v10, v192\n"
      "v_mul_f32 v11, v11, v192\n"
      "v_mul_f32 v12, v12, v193\n"
      "v_mul_f32 v13, v13, v193\n"
      "v_mul_f32 v14, v14, v193\n"
      "v_mul_f32 v15, v15, v193\n"
      "v_mul_f32 v16, v16, v192\n"
      "v_mul_f32 v17, v17, v192\n"
      "v_mul_f32 v18, v18, v192\n"
      "v_mul_f32 v19, v19, v192\n"
      "v_mul_f32 v20, v20, v193\n"
      "v_mul_f32 v21, v21, v193\n"
      "v_mul_f32 v22, v22, v193\n"
      "v_mul_f32 v23, v23, v193\n"
      "v_mul_f32 v24, v24, v192\n"
      "v_mul_f32 v25, v25, v192\n"
      "v_mul_f32 v26, v26, v192\n"
      "v_mul_f32 v27, v27, v192\n"
      "v_mul_f32 v28, v28, v193\n"
      "v_mul_f32 v29, v29, v193\n"
      "v_mul_f32 v30, v30, v193\n"
      "v_mul_f32 v31, v31, v193\n"
      "v_mul_f32 v32, v32, v192\n"
      "v_mul_f32 v33, v33, v192\n"
      "v_mul_f32 v34, v34, v192\n"
      "v_mul_f32 v35, v35, v192\n"
      "v_mul_f32 v36, v36, v193\n"
      "v_mul_f32 v37, v37, v193\n"
      "v_mul_f32 v38, v38, v193\n"
      "v_mul_f32 v39, v39, v193\n"
      "v_mul_f32 v40, v40, v192\n"
      "v_mul_f32 v41, v41, v192\n"
      "v_mul_f32 v42, v42, v192\n"
      "v_mul_f32 v43, v43, v192\n"
      "v_mul_f32 v44, v44, v193\n"
      "v_mul_f32 v45, v45, v193\n"
      "v_mul_f32 v46, v46, v193\n"
      "v_mul_f32 v47, v47, v193\n"
      "v_mul_f32 v48, v48, v192\n"
      "v_mul_f32 v49, v49, v192\n"
      "v_mul_f32 v50, v50, v192\n"
      "v_mul_f32 v51, v51, v192\n"
      "v_mul_f32 v52, v52, v193\n"
      "v_mul_f32 v53, v53, v193\n"
      "v_mul_f32 v54, v54, v193\n"
      "v_mul_f32 v55, v55, v193\n"
      "v_mul_f32 v56, v56, v192\n"
      "v_mul_f32 v57, v57, v192\n"
      "v_mul_f32 v58, v58, v192\n"
      "v_mul_f32 v59, v59, v192\n"
      "v_mul_f32 v60, v60, v193\n"
      "v_mul_f32 v61, v61, v193\n"
      "v_mul_f32 v62, v62, v193\n"
      "v_mul_f32 v63, v63, v193\n"
      "v_accvgpr_read_b32 v196, a144\n"
      "v_accvgpr_read_b32 v197, a145\n"
      "v_accvgpr_read_b32 v198, a146\n"
      "v_accvgpr_read_b32 v199, a147\n"
      "v_mul_f32 v196, v196, v194\n"
      "v_mul_f32 v197, v197, v194\n"
      "v_mul_f32 v198, v198, v194\n"
      "v_mul_f32 v199, v199, v194\n"
      "v_accvgpr_write_b32 a144, v196\n"
      "v_accvgpr_write_b32 a145, v197\n"
      "v_accvgpr_write_b32 a146, v198\n"
      "v_accvgpr_write_b32 a147, v199\n"
      "v_accvgpr_read_b32 v196, a148\n"
      "v_accvgpr_read_b32 v197, a149\n"
      "v_accvgpr_read_b32 v198, a150\n"
      "v_accvgpr_read_b32 v199, a151\n"
      "v_mul_f32 v196, v196, v195\n"
      "v_mul_f32 v197, v197, v195\n"
      "v_mul_f32 v198, v198, v195\n"
      "v_mul_f32 v199, v199, v195\n"
      "v_accvgpr_write_b32 a148, v196\n"
      "v_accvgpr_write_b32 a149, v197\n"
      "v_accvgpr_write_b32 a150, v198\n"
      "v_accvgpr_write_b32 a151, v199\n"
      "v_accvgpr_read_b32 v196, a152\n"
      "v_accvgpr_read_b32 v197, a153\n"
      "v_accvgpr_read_b32 v198, a154\n"
      "v_accvgpr_read_b32 v199, a155\n"
      "v_mul_f32 v196, v196, v194\n"
      "v_mul_f32 v197, v197, v194\n"
      "v_mul_f32 v198, v198, v194\n"
      "v_mul_f32 v199, v199, v194\n"
      "v_accvgpr_write_b32 a152, v196\n"
      "v_accvgpr_write_b32 a153, v197\n"
      "v_accvgpr_write_b32 a154, v198\n"
      "v_accvgpr_write_b32 a155, v199\n"
      "v_accvgpr_read_b32 v196, a156\n"
      "v_accvgpr_read_b32 v197, a157\n"
      "v_accvgpr_read_b32 v198, a158\n"
      "v_accvgpr_read_b32 v199, a159\n"
      "v_mul_f32 v196, v196, v195\n"
      "v_mul_f32 v197, v197, v195\n"
      "v_mul_f32 v198, v198, v195\n"
      "v_mul_f32 v199, v199, v195\n"
      "v_accvgpr_write_b32 a156, v196\n"
      "v_accvgpr_write_b32 a157, v197\n"
      "v_accvgpr_write_b32 a158, v198\n"
      "v_accvgpr_write_b32 a159, v199\n"
      "v_accvgpr_read_b32 v196, a160\n"
      "v_accvgpr_read_b32 v197, a161\n"
      "v_accvgpr_read_b32 v198, a162\n"
      "v_accvgpr_read_b32 v199, a163\n"
      "v_mul_f32 v196, v196, v194\n"
      "v_mul_f32 v197, v197, v194\n"
      "v_mul_f32 v198, v198, v194\n"
      "v_mul_f32 v199, v199, v194\n"
      "v_accvgpr_write_b32 a160, v196\n"
      "v_accvgpr_write_b32 a161, v197\n"
      "v_accvgpr_write_b32 a162, v198\n"
      "v_accvgpr_write_b32 a163, v199\n"
      "v_accvgpr_read_b32 v196, a164\n"
      "v_accvgpr_read_b32 v197, a165\n"
      "v_accvgpr_read_b32 v198, a166\n"
      "v_accvgpr_read_b32 v199, a167\n"
      "v_mul_f32 v196, v196, v195\n"
      "v_mul_f32 v197, v197, v195\n"
      "v_mul_f32 v198, v198, v195\n"
      "v_mul_f32 v199, v199, v195\n"
      "v_accvgpr_write_b32 a164, v196\n"
      "v_accvgpr_write_b32 a165, v197\n"
      "v_accvgpr_write_b32 a166, v198\n"
      "v_accvgpr_write_b32 a167, v199\n"
      "v_accvgpr_read_b32 v196, a168\n"
      "v_accvgpr_read_b32 v197, a169\n"
      "v_accvgpr_read_b32 v198, a170\n"
      "v_accvgpr_read_b32 v199, a171\n"
      "v_mul_f32 v196, v196, v194\n"
      "v_mul_f32 v197, v197, v194\n"
      "v_mul_f32 v198, v198, v194\n"
      "v_mul_f32 v199, v199, v194\n"
      "v_accvgpr_write_b32 a168, v196\n"
      "v_accvgpr_write_b32 a169, v197\n"
      "v_accvgpr_write_b32 a170, v198\n"
      "v_accvgpr_write_b32 a171, v199\n"
      "v_accvgpr_read_b32 v196, a172\n"
      "v_accvgpr_read_b32 v197, a173\n"
      "v_accvgpr_read_b32 v198, a174\n"
      "v_accvgpr_read_b32 v199, a175\n"
      "v_mul_f32 v196, v196, v195\n"
      "v_mul_f32 v197, v197, v195\n"
      "v_mul_f32 v198, v198, v195\n"
      "v_mul_f32 v199, v199, v195\n"
      "v_accvgpr_write_b32 a172, v196\n"
      "v_accvgpr_write_b32 a173, v197\n"
      "v_accvgpr_write_b32 a174, v198\n"
      "v_accvgpr_write_b32 a175, v199\n"
      "v_accvgpr_read_b32 v196, a176\n"
      "v_accvgpr_read_b32 v197, a177\n"
      "v_accvgpr_read_b32 v198, a178\n"
      "v_accvgpr_read_b32 v199, a179\n"
      "v_mul_f32 v196, v196, v194\n"
      "v_mul_f32 v197, v197, v194\n"
      "v_mul_f32 v198, v198, v194\n"
      "v_mul_f32 v199, v199, v194\n"
      "v_accvgpr_write_b32 a176, v196\n"
      "v_accvgpr_write_b32 a177, v197\n"
      "v_accvgpr_write_b32 a178, v198\n"
      "v_accvgpr_write_b32 a179, v199\n"
      "v_accvgpr_read_b32 v196, a180\n"
      "v_accvgpr_read_b32 v197, a181\n"
      "v_accvgpr_read_b32 v198, a182\n"
      "v_accvgpr_read_b32 v199, a183\n"
      "v_mul_f32 v196, v196, v195\n"
      "v_mul_f32 v197, v197, v195\n"
      "v_mul_f32 v198, v198, v195\n"
      "v_mul_f32 v199, v199, v195\n"
      "v_accvgpr_write_b32 a180, v196\n"
      "v_accvgpr_write_b32 a181, v197\n"
      "v_accvgpr_write_b32 a182, v198\n"
      "v_accvgpr_write_b32 a183, v199\n"
      "v_accvgpr_read_b32 v196, a184\n"
      "v_accvgpr_read_b32 v197, a185\n"
      "v_accvgpr_read_b32 v198, a186\n"
      "v_accvgpr_read_b32 v199, a187\n"
      "v_mul_f32 v196, v196, v194\n"
      "v_mul_f32 v197, v197, v194\n"
      "v_mul_f32 v198, v198, v194\n"
      "v_mul_f32 v199, v199, v194\n"
      "v_accvgpr_write_b32 a184, v196\n"
      "v_accvgpr_write_b32 a185, v197\n"
      "v_accvgpr_write_b32 a186, v198\n"
      "v_accvgpr_write_b32 a187, v199\n"
      "v_accvgpr_read_b32 v196, a188\n"
      "v_accvgpr_read_b32 v197, a189\n"
      "v_accvgpr_read_b32 v198, a190\n"
      "v_accvgpr_read_b32 v199, a191\n"
      "v_mul_f32 v196, v196, v195\n"
      "v_mul_f32 v197, v197, v195\n"
      "v_mul_f32 v198, v198, v195\n"
      "v_mul_f32 v199, v199, v195\n"
      "v_accvgpr_write_b32 a188, v196\n"
      "v_accvgpr_write_b32 a189, v197\n"
      "v_accvgpr_write_b32 a190, v198\n"
      "v_accvgpr_write_b32 a191, v199\n"
      "v_accvgpr_read_b32 v196, a192\n"
      "v_accvgpr_read_b32 v197, a193\n"
      "v_accvgpr_read_b32 v198, a194\n"
      "v_accvgpr_read_b32 v199, a195\n"
      "v_mul_f32 v196, v196, v194\n"
      "v_mul_f32 v197, v197, v194\n"
      "v_mul_f32 v198, v198, v194\n"
      "v_mul_f32 v199, v199, v194\n"
      "v_accvgpr_write_b32 a192, v196\n"
      "v_accvgpr_write_b32 a193, v197\n"
      "v_accvgpr_write_b32 a194, v198\n"
      "v_accvgpr_write_b32 a195, v199\n"
      "v_accvgpr_read_b32 v196, a196\n"
      "v_accvgpr_read_b32 v197, a197\n"
      "v_accvgpr_read_b32 v198, a198\n"
      "v_accvgpr_read_b32 v199, a199\n"
      "v_mul_f32 v196, v196, v195\n"
      "v_mul_f32 v197, v197, v195\n"
      "v_mul_f32 v198, v198, v195\n"
      "v_mul_f32 v199, v199, v195\n"
      "v_accvgpr_write_b32 a196, v196\n"
      "v_accvgpr_write_b32 a197, v197\n"
      "v_accvgpr_write_b32 a198, v198\n"
      "v_accvgpr_write_b32 a199, v199\n"
      "v_accvgpr_read_b32 v196, a200\n"
      "v_accvgpr_read_b32 v197, a201\n"
      "v_accvgpr_read_b32 v198, a202\n"
      "v_accvgpr_read_b32 v199, a203\n"
      "v_mul_f32 v196, v196, v194\n"
      "v_mul_f32 v197, v197, v194\n"
      "v_mul_f32 v198, v198, v194\n"
      "v_mul_f32 v199, v199, v194\n"
      "v_accvgpr_write_b32 a200, v196\n"
      "v_accvgpr_write_b32 a201, v197\n"
      "v_accvgpr_write_b32 a202, v198\n"
      "v_accvgpr_write_b32 a203, v199\n"
      "v_accvgpr_read_b32 v196, a204\n"
      "v_accvgpr_read_b32 v197, a205\n"
      "v_accvgpr_read_b32 v198, a206\n"
      "v_accvgpr_read_b32 v199, a207\n"
      "v_mul_f32 v196, v196, v195\n"
      "v_mul_f32 v197, v197, v195\n"
      "v_mul_f32 v198, v198, v195\n"
      "v_mul_f32 v199, v199, v195\n"
      "v_accvgpr_write_b32 a204, v196\n"
      "v_accvgpr_write_b32 a205, v197\n"
      "v_accvgpr_write_b32 a206, v198\n"
      "v_accvgpr_write_b32 a207, v199\n"
      "v_mul_f32 v180, v180, v192\n"
      "v_mul_f32 v181, v181, v193\n"
      "v_mul_f32 v182, v182, v194\n"
      "v_mul_f32 v183, v183, v195\n"
      "v_exp_f32 v188, v64\n"
      "v_exp_f32 v189, v65\n"
      "v_exp_f32 v190, v66\n"
      "v_exp_f32 v191, v67\n"
      "v_cvt_pk_bf16_f32 v128, v188, v189\n"
      "v_cvt_pk_bf16_f32 v129, v190, v191\n"
      "v_exp_f32 v192, v68\n"
      "v_exp_f32 v193, v69\n"
      "v_exp_f32 v194, v70\n"
      "v_exp_f32 v195, v71\n"
      "v_cvt_pk_bf16_f32 v130, v192, v193\n"
      "v_cvt_pk_bf16_f32 v131, v194, v195\n"
      "v_exp_f32 v188, v72\n"
      "v_exp_f32 v189, v73\n"
      "v_exp_f32 v190, v74\n"
      "v_exp_f32 v191, v75\n"
      "v_cvt_pk_bf16_f32 v132, v188, v189\n"
      "v_cvt_pk_bf16_f32 v133, v190, v191\n"
      "v_exp_f32 v192, v76\n"
      "v_exp_f32 v193, v77\n"
      "v_exp_f32 v194, v78\n"
      "v_exp_f32 v195, v79\n"
      "v_cvt_pk_bf16_f32 v134, v192, v193\n"
      "v_cvt_pk_bf16_f32 v135, v194, v195\n"
      "v_exp_f32 v188, v80\n"
      "v_exp_f32 v189, v81\n"
      "v_exp_f32 v190, v82\n"
      "v_exp_f32 v191, v83\n"
      "v_cvt_pk_bf16_f32 v136, v188, v189\n"
      "v_cvt_pk_bf16_f32 v137, v190, v191\n"
      "v_exp_f32 v192, v84\n"
      "v_exp_f32 v193, v85\n"
      "v_exp_f32 v194, v86\n"
      "v_exp_f32 v195, v87\n"
      "v_cvt_pk_bf16_f32 v138, v192, v193\n"
      "v_cvt_pk_bf16_f32 v139, v194, v195\n"
      "v_exp_f32 v188, v88\n"
      "v_exp_f32 v189, v89\n"
      "v_exp_f32 v190, v90\n"
      "v_exp_f32 v191, v91\n"
      "v_cvt_pk_bf16_f32 v140, v188, v189\n"
      "v_cvt_pk_bf16_f32 v141, v190, v191\n"
      "v_exp_f32 v192, v92\n"
      "v_exp_f32 v193, v93\n"
      "v_exp_f32 v194, v94\n"
      "v_exp_f32 v195, v95\n"
      "v_cvt_pk_bf16_f32 v142, v192, v193\n"
      "v_cvt_pk_bf16_f32 v143, v194, v195\n"
      "s_nop 1\n"
      "v_mfma_f32_16x16x32_bf16 v[184:187], a[128:131], v[128:131], 0\n"
      "v_mfma_f32_16x16x32_bf16 v[184:187], a[132:135], v[132:135], v[184:187]\n"
      "v_mfma_f32_16x16x32_bf16 v[184:187], a[136:139], v[136:139], v[184:187]\n"
      "v_mfma_f32_16x16x32_bf16 v[184:187], a[140:143], v[140:143], v[184:187]\n"
      "s_nop 7\n"
      "s_setpc_b64 s[96:97]\n"
      "FB1_%=:\n"
      "v_mov_b32 v196, 139248\n"
      "s_nop 7\n"
      "ds_write_b32 v196, v196\n"
      "v_max3_f32 v188, v96, v97, 0\n"
      "v_max3_f32 v189, v104, v105, 0\n"
      "v_max3_f32 v190, v112, v113, 0\n"
      "v_max3_f32 v191, v120, v121, 0\n"
      "v_max3_f32 v188, v188, v98, v99\n"
      "v_max3_f32 v189, v189, v106, v107\n"
      "v_max3_f32 v190, v190, v114, v115\n"
      "v_max3_f32 v191, v191, v122, v123\n"
      "v_max3_f32 v188, v188, v100, v101\n"
      "v_max3_f32 v189, v189, v108, v109\n"
      "v_max3_f32 v190, v190, v116, v117\n"
      "v_max3_f32 v191, v191, v124, v125\n"
      "v_max3_f32 v188, v188, v102, v103\n"
      "v_max3_f32 v189, v189, v110, v111\n"
      "v_max3_f32 v190, v190, v118, v119\n"
      "v_max3_f32 v191, v191, v126, v127\n"
      "v_mov_b32 v192, v188\n"
      "v_mov_b32 v193, v189\n"
      "v_mov_b32 v194, v190\n"
      "v_mov_b32 v195, v191\n"
      "v_permlane16_swap_b32 v188, v192\n"
      "v_permlane16_swap_b32 v189, v193\n"
      "v_permlane16_swap_b32 v190, v194\n"
      "v_permlane16_swap_b32 v191, v195\n"
      "v_max_f32 v188, v188, v192\n"
      "v_max_f32 v189, v189, v193\n"
      "v_max_f32 v190, v190, v194\n"
      "v_max_f32 v191, v191, v195\n"
      "v_mov_b32 v192, v188\n"
      "v_mov_b32 v193, v189\n"
      "v_mov_b32 v194, v190\n"
      "v_mov_b32 v195, v191\n"
      "v_permlane32_swap_b32 v188, v192\n"
      "v_permlane32_swap_b32 v189, v193\n"
      "v_permlane32_swap_b32 v190, v194\n"
      "v_permlane32_swap_b32 v191, v195\n"
      "v_max_f32 v188, v188, v192\n"
      "v_max_f32 v189, v189, v193\n"
      "v_max_f32 v190, v190, v194\n"
      "v_max_f32 v191, v191, v195\n"
      "RS1_%=:\n"
      "v_exp_f32 v192, -v188\n"
      "v_exp_f32 v193, -v189\n"
      "v_exp_f32 v194, -v190\n"
      "v_exp_f32 v195, -v191\n"
      "v_sub_f32 v176, v176, v188\n"
      "v_sub_f32 v177, v177, v189\n"
      "v_sub_f32 v178, v178, v190\n"
      "v_sub_f32 v179, v179, v191\n"
      "v_mov_b32 v160, v176\n"
      "v_mov_b32 v161, v176\n"
      "v_mov_b32 v162, v176\n"
      "v_mov_b32 v163, v176\n"
      "v_mov_b32 v164, v177\n"
      "v_mov_b32 v165, v177\n"
      "v_mov_b32 v166, v177\n"
      "v_mov_b32 v167, v177\n"
      "v_mov_b32 v168, v178\n"
      "v_mov_b32 v169, v178\n"
      "v_mov_b32 v170, v178\n"
      "v_mov_b32 v171, v178\n"
      "v_mov_b32 v172, v179\n"
      "v_mov_b32 v173, v179\n"
      "v_mov_b32 v174, v179\n"
      "v_mov_b32 v175, v179\n"
      "v_sub_f32 v64, v64, v188\n"
      "v_sub_f32 v65, v65, v188\n"
      "v_sub_f32 v66, v66, v188\n"
      "v_sub_f32 v67, v67, v188\n"
      "v_sub_f32 v68, v68, v188\n"
      "v_sub_f32 v69, v69, v188\n"
      "v_sub_f32 v70, v70, v188\n"
      "v_sub_f32 v71, v71, v188\n"
      "v_sub_f32 v72, v72, v189\n"
      "v_sub_f32 v73, v73, v189\n"
      "v_sub_f32 v74, v74, v189\n"
      "v_sub_f32 v75, v75, v189\n"
      "v_sub_f32 v76, v76, v189\n"
      "v_sub_f32 v77, v77, v189\n"
      "v_sub_f32 v78, v78, v189\n"
      "v_sub_f32 v79, v79, v189\n"
      "v_sub_f32 v80, v80, v190\n"
      "v_sub_f32 v81, v81, v190\n"
      "v_sub_f32 v82, v82, v190\n"
      "v_sub_f32 v83, v83, v190\n"
      "v_sub_f32 v84, v84, v190\n"
      "v_sub_f32 v85, v85, v190\n"
      "v_sub_f32 v86, v86, v190\n"
      "v_sub_f32 v87, v87, v190\n"
      "v_sub_f32 v88, v88, v191\n"
      "v_sub_f32 v89, v89, v191\n"
      "v_sub_f32 v90, v90, v191\n"
      "v_sub_f32 v91, v91, v191\n"
      "v_sub_f32 v92, v92, v191\n"
      "v_sub_f32 v93, v93, v191\n"
      "v_sub_f32 v94, v94, v191\n"
      "v_sub_f32 v95, v95, v191\n"
      "v_sub_f32 v96, v96, v188\n"
      "v_sub_f32 v97, v97, v188\n"
      "v_sub_f32 v98, v98, v188\n"
      "v_sub_f32 v99, v99, v188\n"
      "v_sub_f32 v100, v100, v188\n"
      "v_sub_f32 v101, v101, v188\n"
      "v_sub_f32 v102, v102, v188\n"
      "v_sub_f32 v103, v103, v188\n"
      "v_sub_f32 v104, v104, v189\n"
      "v_sub_f32 v105, v105, v189\n"
      "v_sub_f32 v106, v106, v189\n"
      "v_sub_f32 v107, v107, v189\n"
      "v_sub_f32 v108, v108, v189\n"
      "v_sub_f32 v109, v109, v189\n"
      "v_sub_f32 v110, v110, v189\n"
      "v_sub_f32 v111, v111, v189\n"
      "v_sub_f32 v112, v112, v190\n"
      "v_sub_f32 v113, v113, v190\n"
      "v_sub_f32 v114, v114, v190\n"
      "v_sub_f32 v115, v115, v190\n"
      "v_sub_f32 v116, v116, v190\n"
      "v_sub_f32 v117, v117, v190\n"
      "v_sub_f32 v118, v118, v190\n"
      "v_sub_f32 v119, v119, v190\n"
      "v_sub_f32 v120, v120, v191\n"
      "v_sub_f32 v121, v121, v191\n"
      "v_sub_f32 v122, v122, v191\n"
      "v_sub_f32 v123, v123, v191\n"
      "v_sub_f32 v124, v124, v191\n"
      "v_sub_f32 v125, v125, v191\n"
      "v_sub_f32 v126, v126, v191\n"
      "v_sub_f32 v127, v127, v191\n"
      "v_mul_f32 v0, v0, v192\n"
      "v_mul_f32 v1, v1, v192\n"
      "v_mul_f32 v2, v2, v192\n"
      "v_mul_f32 v3, v3, v192\n"
      "v_mul_f32 v4, v4, v193\n"
      "v_mul_f32 v5, v5, v193\n"
      "v_mul_f32 v6, v6, v193\n"
      "v_mul_f32 v7, v7, v193\n"
      "v_mul_f32 v8, v8, v192\n"
      "v_mul_f32 v9, v9, v192\n"
      "v_mul_f32 v10, v10, v192\n"
      "v_mul_f32 v11, v11, v192\n"
      "v_mul_f32 v12, v12, v193\n"
      "v_mul_f32 v13, v13, v193\n"
      "v_mul_f32 v14, v14, v193\n"
      "v_mul_f32 v15, v15, v193\n"
      "v_mul_f32 v16, v16, v192\n"
      "v_mul_f32 v17, v17, v192\n"
      "v_mul_f32 v18, v18, v192\n"
      "v_mul_f32 v19, v19, v192\n"
      "v_mul_f32 v20, v20, v193\n"
      "v_mul_f32 v21, v21, v193\n"
      "v_mul_f32 v22, v22, v193\n"
      "v_mul_f32 v23, v23, v193\n"
      "v_mul_f32 v24, v24, v192\n"
      "v_mul_f32 v25, v25, v192\n"
      "v_mul_f32 v26, v26, v192\n"
      "v_mul_f32 v27, v27, v192\n"
      "v_mul_f32 v28, v28, v193\n"
      "v_mul_f32 v29, v29, v193\n"
      "v_mul_f32 v30, v30, v193\n"
      "v_mul_f32 v31, v31, v193\n"
      "v_mul_f32 v32, v32, v192\n"
      "v_mul_f32 v33, v33, v192\n"
      "v_mul_f32 v34, v34, v192\n"
      "v_mul_f32 v35, v35, v192\n"
      "v_mul_f32 v36, v36, v193\n"
      "v_mul_f32 v37, v37, v193\n"
      "v_mul_f32 v38, v38, v193\n"
      "v_mul_f32 v39, v39, v193\n"
      "v_mul_f32 v40, v40, v192\n"
      "v_mul_f32 v41, v41, v192\n"
      "v_mul_f32 v42, v42, v192\n"
      "v_mul_f32 v43, v43, v192\n"
      "v_mul_f32 v44, v44, v193\n"
      "v_mul_f32 v45, v45, v193\n"
      "v_mul_f32 v46, v46, v193\n"
      "v_mul_f32 v47, v47, v193\n"
      "v_mul_f32 v48, v48, v192\n"
      "v_mul_f32 v49, v49, v192\n"
      "v_mul_f32 v50, v50, v192\n"
      "v_mul_f32 v51, v51, v192\n"
      "v_mul_f32 v52, v52, v193\n"
      "v_mul_f32 v53, v53, v193\n"
      "v_mul_f32 v54, v54, v193\n"
      "v_mul_f32 v55, v55, v193\n"
      "v_mul_f32 v56, v56, v192\n"
      "v_mul_f32 v57, v57, v192\n"
      "v_mul_f32 v58, v58, v192\n"
      "v_mul_f32 v59, v59, v192\n"
      "v_mul_f32 v60, v60, v193\n"
      "v_mul_f32 v61, v61, v193\n"
      "v_mul_f32 v62, v62, v193\n"
      "v_mul_f32 v63, v63, v193\n"
      "v_accvgpr_read_b32 v196, a144\n"
      "v_accvgpr_read_b32 v197, a145\n"
      "v_accvgpr_read_b32 v198, a146\n"
      "v_accvgpr_read_b32 v199, a147\n"
      "v_mul_f32 v196, v196, v194\n"
      "v_mul_f32 v197, v197, v194\n"
      "v_mul_f32 v198, v198, v194\n"
      "v_mul_f32 v199, v199, v194\n"
      "v_accvgpr_write_b32 a144, v196\n"
      "v_accvgpr_write_b32 a145, v197\n"
      "v_accvgpr_write_b32 a146, v198\n"
      "v_accvgpr_write_b32 a147, v199\n"
      "v_accvgpr_read_b32 v196, a148\n"
      "v_accvgpr_read_b32 v197, a149\n"
      "v_accvgpr_read_b32 v198, a150\n"
      "v_accvgpr_read_b32 v199, a151\n"
      "v_mul_f32 v196, v196, v195\n"
      "v_mul_f32 v197, v197, v195\n"
      "v_mul_f32 v198, v198, v195\n"
      "v_mul_f32 v199, v199, v195\n"
      "v_accvgpr_write_b32 a148, v196\n"
      "v_accvgpr_write_b32 a149, v197\n"
      "v_accvgpr_write_b32 a150, v198\n"
      "v_accvgpr_write_b32 a151, v199\n"
      "v_accvgpr_read_b32 v196, a152\n"
      "v_accvgpr_read_b32 v197, a153\n"
      "v_accvgpr_read_b32 v198, a154\n"
      "v_accvgpr_read_b32 v199, a155\n"
      "v_mul_f32 v196, v196, v194\n"
      "v_mul_f32 v197, v197, v194\n"
      "v_mul_f32 v198, v198, v194\n"
      "v_mul_f32 v199, v199, v194\n"
      "v_accvgpr_write_b32 a152, v196\n"
      "v_accvgpr_write_b32 a153, v197\n"
      "v_accvgpr_write_b32 a154, v198\n"
      "v_accvgpr_write_b32 a155, v199\n"
      "v_accvgpr_read_b32 v196, a156\n"
      "v_accvgpr_read_b32 v197, a157\n"
      "v_accvgpr_read_b32 v198, a158\n"
      "v_accvgpr_read_b32 v199, a159\n"
      "v_mul_f32 v196, v196, v195\n"
      "v_mul_f32 v197, v197, v195\n"
      "v_mul_f32 v198, v198, v195\n"
      "v_mul_f32 v199, v199, v195\n"
      "v_accvgpr_write_b32 a156, v196\n"
      "v_accvgpr_write_b32 a157, v197\n"
      "v_accvgpr_write_b32 a158, v198\n"
      "v_accvgpr_write_b32 a159, v199\n"
      "v_accvgpr_read_b32 v196, a160\n"
      "v_accvgpr_read_b32 v197, a161\n"
      "v_accvgpr_read_b32 v198, a162\n"
      "v_accvgpr_read_b32 v199, a163\n"
      "v_mul_f32 v196, v196, v194\n"
      "v_mul_f32 v197, v197, v194\n"
      "v_mul_f32 v198, v198, v194\n"
      "v_mul_f32 v199, v199, v194\n"
      "v_accvgpr_write_b32 a160, v196\n"
      "v_accvgpr_write_b32 a161, v197\n"
      "v_accvgpr_write_b32 a162, v198\n"
      "v_accvgpr_write_b32 a163, v199\n"
      "v_accvgpr_read_b32 v196, a164\n"
      "v_accvgpr_read_b32 v197, a165\n"
      "v_accvgpr_read_b32 v198, a166\n"
      "v_accvgpr_read_b32 v199, a167\n"
      "v_mul_f32 v196, v196, v195\n"
      "v_mul_f32 v197, v197, v195\n"
      "v_mul_f32 v198, v198, v195\n"
      "v_mul_f32 v199, v199, v195\n"
      "v_accvgpr_write_b32 a164, v196\n"
      "v_accvgpr_write_b32 a165, v197\n"
      "v_accvgpr_write_b32 a166, v198\n"
      "v_accvgpr_write_b32 a167, v199\n"
      "v_accvgpr_read_b32 v196, a168\n"
      "v_accvgpr_read_b32 v197, a169\n"
      "v_accvgpr_read_b32 v198, a170\n"
      "v_accvgpr_read_b32 v199, a171\n"
      "v_mul_f32 v196, v196, v194\n"
      "v_mul_f32 v197, v197, v194\n"
      "v_mul_f32 v198, v198, v194\n"
      "v_mul_f32 v199, v199, v194\n"
      "v_accvgpr_write_b32 a168, v196\n"
      "v_accvgpr_write_b32 a169, v197\n"
      "v_accvgpr_write_b32 a170, v198\n"
      "v_accvgpr_write_b32 a171, v199\n"
      "v_accvgpr_read_b32 v196, a172\n"
      "v_accvgpr_read_b32 v197, a173\n"
      "v_accvgpr_read_b32 v198, a174\n"
      "v_accvgpr_read_b32 v199, a175\n"
      "v_mul_f32 v196, v196, v195\n"
      "v_mul_f32 v197, v197, v195\n"
      "v_mul_f32 v198, v198, v195\n"
      "v_mul_f32 v199, v199, v195\n"
      "v_accvgpr_write_b32 a172, v196\n"
      "v_accvgpr_write_b32 a173, v197\n"
      "v_accvgpr_write_b32 a174, v198\n"
      "v_accvgpr_write_b32 a175, v199\n"
      "v_accvgpr_read_b32 v196, a176\n"
      "v_accvgpr_read_b32 v197, a177\n"
      "v_accvgpr_read_b32 v198, a178\n"
      "v_accvgpr_read_b32 v199, a179\n"
      "v_mul_f32 v196, v196, v194\n"
      "v_mul_f32 v197, v197, v194\n"
      "v_mul_f32 v198, v198, v194\n"
      "v_mul_f32 v199, v199, v194\n"
      "v_accvgpr_write_b32 a176, v196\n"
      "v_accvgpr_write_b32 a177, v197\n"
      "v_accvgpr_write_b32 a178, v198\n"
      "v_accvgpr_write_b32 a179, v199\n"
      "v_accvgpr_read_b32 v196, a180\n"
      "v_accvgpr_read_b32 v197, a181\n"
      "v_accvgpr_read_b32 v198, a182\n"
      "v_accvgpr_read_b32 v199, a183\n"
      "v_mul_f32 v196, v196, v195\n"
      "v_mul_f32 v197, v197, v195\n"
      "v_mul_f32 v198, v198, v195\n"
      "v_mul_f32 v199, v199, v195\n"
      "v_accvgpr_write_b32 a180, v196\n"
      "v_accvgpr_write_b32 a181, v197\n"
      "v_accvgpr_write_b32 a182, v198\n"
      "v_accvgpr_write_b32 a183, v199\n"
      "v_accvgpr_read_b32 v196, a184\n"
      "v_accvgpr_read_b32 v197, a185\n"
      "v_accvgpr_read_b32 v198, a186\n"
      "v_accvgpr_read_b32 v199, a187\n"
      "v_mul_f32 v196, v196, v194\n"
      "v_mul_f32 v197, v197, v194\n"
      "v_mul_f32 v198, v198, v194\n"
      "v_mul_f32 v199, v199, v194\n"
      "v_accvgpr_write_b32 a184, v196\n"
      "v_accvgpr_write_b32 a185, v197\n"
      "v_accvgpr_write_b32 a186, v198\n"
      "v_accvgpr_write_b32 a187, v199\n"
      "v_accvgpr_read_b32 v196, a188\n"
      "v_accvgpr_read_b32 v197, a189\n"
      "v_accvgpr_read_b32 v198, a190\n"
      "v_accvgpr_read_b32 v199, a191\n"
      "v_mul_f32 v196, v196, v195\n"
      "v_mul_f32 v197, v197, v195\n"
      "v_mul_f32 v198, v198, v195\n"
      "v_mul_f32 v199, v199, v195\n"
      "v_accvgpr_write_b32 a188, v196\n"
      "v_accvgpr_write_b32 a189, v197\n"
      "v_accvgpr_write_b32 a190, v198\n"
      "v_accvgpr_write_b32 a191, v199\n"
      "v_accvgpr_read_b32 v196, a192\n"
      "v_accvgpr_read_b32 v197, a193\n"
      "v_accvgpr_read_b32 v198, a194\n"
      "v_accvgpr_read_b32 v199, a195\n"
      "v_mul_f32 v196, v196, v194\n"
      "v_mul_f32 v197, v197, v194\n"
      "v_mul_f32 v198, v198, v194\n"
      "v_mul_f32 v199, v199, v194\n"
      "v_accvgpr_write_b32 a192, v196\n"
      "v_accvgpr_write_b32 a193, v197\n"
      "v_accvgpr_write_b32 a194, v198\n"
      "v_accvgpr_write_b32 a195, v199\n"
      "v_accvgpr_read_b32 v196, a196\n"
      "v_accvgpr_read_b32 v197, a197\n"
      "v_accvgpr_read_b32 v198, a198\n"
      "v_accvgpr_read_b32 v199, a199\n"
      "v_mul_f32 v196, v196, v195\n"
      "v_mul_f32 v197, v197, v195\n"
      "v_mul_f32 v198, v198, v195\n"
      "v_mul_f32 v199, v199, v195\n"
      "v_accvgpr_write_b32 a196, v196\n"
      "v_accvgpr_write_b32 a197, v197\n"
      "v_accvgpr_write_b32 a198, v198\n"
      "v_accvgpr_write_b32 a199, v199\n"
      "v_accvgpr_read_b32 v196, a200\n"
      "v_accvgpr_read_b32 v197, a201\n"
      "v_accvgpr_read_b32 v198, a202\n"
      "v_accvgpr_read_b32 v199, a203\n"
      "v_mul_f32 v196, v196, v194\n"
      "v_mul_f32 v197, v197, v194\n"
      "v_mul_f32 v198, v198, v194\n"
      "v_mul_f32 v199, v199, v194\n"
      "v_accvgpr_write_b32 a200, v196\n"
      "v_accvgpr_write_b32 a201, v197\n"
      "v_accvgpr_write_b32 a202, v198\n"
      "v_accvgpr_write_b32 a203, v199\n"
      "v_accvgpr_read_b32 v196, a204\n"
      "v_accvgpr_read_b32 v197, a205\n"
      "v_accvgpr_read_b32 v198, a206\n"
      "v_accvgpr_read_b32 v199, a207\n"
      "v_mul_f32 v196, v196, v195\n"
      "v_mul_f32 v197, v197, v195\n"
      "v_mul_f32 v198, v198, v195\n"
      "v_mul_f32 v199, v199, v195\n"
      "v_accvgpr_write_b32 a204, v196\n"
      "v_accvgpr_write_b32 a205, v197\n"
      "v_accvgpr_write_b32 a206, v198\n"
      "v_accvgpr_write_b32 a207, v199\n"
      "v_mul_f32 v180, v180, v192\n"
      "v_mul_f32 v181, v181, v193\n"
      "v_mul_f32 v182, v182, v194\n"
      "v_mul_f32 v183, v183, v195\n"
      "v_lshlrev_b32 v196, 16, v128\n"
      "v_and_b32 v197, 0xffff0000, v128\n"
      "v_mul_f32 v196, v196, v192\n"
      "v_mul_f32 v197, v197, v192\n"
      "v_cvt_pk_bf16_f32 v128, v196, v197\n"
      "v_lshlrev_b32 v196, 16, v129\n"
      "v_and_b32 v197, 0xffff0000, v129\n"
      "v_mul_f32 v196, v196, v192\n"
      "v_mul_f32 v197, v197, v192\n"
      "v_cvt_pk_bf16_f32 v129, v196, v197\n"
      "v_lshlrev_b32 v196, 16, v130\n"
      "v_and_b32 v197, 0xffff0000, v130\n"
      "v_mul_f32 v196, v196, v192\n"
      "v_mul_f32 v197, v197, v192\n"
      "v_cvt_pk_bf16_f32 v130, v196, v197\n"
      "v_lshlrev_b32 v196, 16, v131\n"
      "v_and_b32 v197, 0xffff0000, v131\n"
      "v_mul_f32 v196, v196, v192\n"
      "v_mul_f32 v197, v197, v192\n"
      "v_cvt_pk_bf16_f32 v131, v196, v197\n"
      "v_lshlrev_b32 v196, 16, v132\n"
      "v_and_b32 v197, 0xffff0000, v132\n"
      "v_mul_f32 v196, v196, v193\n"
      "v_mul_f32 v197, v197, v193\n"
      "v_cvt_pk_bf16_f32 v132, v196, v197\n"
      "v_lshlrev_b32 v196, 16, v133\n"
      "v_and_b32 v197, 0xffff0000, v133\n"
      "v_mul_f32 v196, v196, v193\n"
      "v_mul_f32 v197, v197, v193\n"
      "v_cvt_pk_bf16_f32 v133, v196, v197\n"
      "v_lshlrev_b32 v196, 16, v134\n"
      "v_and_b32 v197, 0xffff0000, v134\n"
      "v_mul_f32 v196, v196, v193\n"
      "v_mul_f32 v197, v197, v193\n"
      "v_cvt_pk_bf16_f32 v134, v196, v197\n"
      "v_lshlrev_b32 v196, 16, v135\n"
      "v_and_b32 v197, 0xffff0000, v135\n"
      "v_mul_f32 v196, v196, v193\n"
      "v_mul_f32 v197, v197, v193\n"
      "v_cvt_pk_bf16_f32 v135, v196, v197\n"
      "v_lshlrev_b32 v196, 16, v136\n"
      "v_and_b32 v197, 0xffff0000, v136\n"
      "v_mul_f32 v196, v196, v194\n"
      "v_mul_f32 v197, v197, v194\n"
      "v_cvt_pk_bf16_f32 v136, v196, v197\n"
      "v_lshlrev_b32 v196, 16, v137\n"
      "v_and_b32 v197, 0xffff0000, v137\n"
      "v_mul_f32 v196, v196, v194\n"
      "v_mul_f32 v197, v197, v194\n"
      "v_cvt_pk_bf16_f32 v137, v196, v197\n"
      "v_lshlrev_b32 v196, 16, v138\n"
      "v_and_b32 v197, 0xffff0000, v138\n"
      "v_mul_f32 v196, v196, v194\n"
      "v_mul_f32 v197, v197, v194\n"
      "v_cvt_pk_bf16_f32 v138, v196, v197\n"
      "v_lshlrev_b32 v196, 16, v139\n"
      "v_and_b32 v197, 0xffff0000, v139\n"
      "v_mul_f32 v196, v196, v194\n"
      "v_mul_f32 v197, v197, v194\n"
      "v_cvt_pk_bf16_f32 v139, v196, v197\n"
      "v_lshlrev_b32 v196, 16, v140\n"
      "v_and_b32 v197, 0xffff0000, v140\n"
      "v_mul_f32 v196, v196, v195\n"
      "v_mul_f32 v197, v197, v195\n"
      "v_cvt_pk_bf16_f32 v140, v196, v197\n"
      "v_lshlrev_b32 v196, 16, v141\n"
      "v_and_b32 v197, 0xffff0000, v141\n"
      "v_mul_f32 v196, v196, v195\n"
      "v_mul_f32 v197, v197, v195\n"
      "v_cvt_pk_bf16_f32 v141, v196, v197\n"
      "v_lshlrev_b32 v196, 16, v142\n"
      "v_and_b32 v197, 0xffff0000, v142\n"
      "v_mul_f32 v196, v196, v195\n"
      "v_mul_f32 v197, v197, v195\n"
      "v_cvt_pk_bf16_f32 v142, v196, v197\n"
      "v_lshlrev_b32 v196, 16, v143\n"
      "v_and_b32 v197, 0xffff0000, v143\n"
      "v_mul_f32 v196, v196, v195\n"
      "v_mul_f32 v197, v197, v195\n"
      "v_cvt_pk_bf16_f32 v143, v196, v197\n"
      "v_exp_f32 v188, v96\n"
      "v_exp_f32 v189, v97\n"
      "v_exp_f32 v190, v98\n"
      "v_exp_f32 v191, v99\n"
      "v_cvt_pk_bf16_f32 v144, v188, v189\n"
      "v_cvt_pk_bf16_f32 v145, v190, v191\n"
      "v_exp_f32 v192, v100\n"
      "v_exp_f32 v193, v101\n"
      "v_exp_f32 v194, v102\n"
      "v_exp_f32 v195, v103\n"
      "v_cvt_pk_bf16_f32 v146, v192, v193\n"
      "v_cvt_pk_bf16_f32 v147, v194, v195\n"
      "v_exp_f32 v188, v104\n"
      "v_exp_f32 v189, v105\n"
      "v_exp_f32 v190, v106\n"
      "v_exp_f32 v191, v107\n"
      "v_cvt_pk_bf16_f32 v148, v188, v189\n"
      "v_cvt_pk_bf16_f32 v149, v190, v191\n"
      "v_exp_f32 v192, v108\n"
      "v_exp_f32 v193, v109\n"
      "v_exp_f32 v194, v110\n"
      "v_exp_f32 v195, v111\n"
      "v_cvt_pk_bf16_f32 v150, v192, v193\n"
      "v_cvt_pk_bf16_f32 v151, v194, v195\n"
      "v_exp_f32 v188, v112\n"
      "v_exp_f32 v189, v113\n"
      "v_exp_f32 v190, v114\n"
      "v_exp_f32 v191, v115\n"
      "v_cvt_pk_bf16_f32 v152, v188, v189\n"
      "v_cvt_pk_bf16_f32 v153, v190, v191\n"
      "v_exp_f32 v192, v116\n"
      "v_exp_f32 v193, v117\n"
      "v_exp_f32 v194, v118\n"
      "v_exp_f32 v195, v119\n"
      "v_cvt_pk_bf16_f32 v154, v192, v193\n"
      "v_cvt_pk_bf16_f32 v155, v194, v195\n"
      "v_exp_f32 v188, v120\n"
      "v_exp_f32 v189, v121\n"
      "v_exp_f32 v190, v122\n"
      "v_exp_f32 v191, v123\n"
      "v_cvt_pk_bf16_f32 v156, v188, v189\n"
      "v_cvt_pk_bf16_f32 v157, v190, v191\n"
      "v_exp_f32 v192, v124\n"
      "v_exp_f32 v193, v125\n"
      "v_exp_f32 v194, v126\n"
      "v_exp_f32 v195, v127\n"
      "v_cvt_pk_bf16_f32 v158, v192, v193\n"
      "v_cvt_pk_bf16_f32 v159, v194, v195\n"
      "s_nop 1\n"
      "v_mfma_f32_16x16x32_bf16 v[184:187], a[128:131], v[144:147], 0\n"
      "v_mfma_f32_16x16x32_bf16 v[184:187], a[132:135], v[148:151], v[184:187]\n"
      "v_mfma_f32_16x16x32_bf16 v[184:187], a[136:139], v[152:155], v[184:187]\n"
      "v_mfma_f32_16x16x32_bf16 v[184:187], a[140:143], v[156:159], v[184:187]\n"
      "s_nop 7\n"
      "s_setpc_b64 s[96:97]\n"
      "SLOW_%=:\n"
      "ds_write_b128 v234, v[176:179] offset:5120\n"
      "s_waitcnt lgkmcnt(0)\n"
      "s_barrier\n"
      "ds_read_b128 v[192:195], v235 offset:5120\n"
      "s_waitcnt lgkmcnt(0)\n"
      "v_sub_f32 v188, v176, v194\n"
      "v_sub_f32 v189, v177, v195\n"
      "v_sub_f32 v190, v178, v192\n"
      "v_sub_f32 v191, v179, v193\n"
      "v_max_f32 v188, 0, v188\n"
      "v_max_f32 v189, 0, v189\n"
      "v_max_f32 v190, 0, v190\n"
      "v_max_f32 v191, 0, v191\n"
      "s_call_b64 s[96:97], RS1_%=\n"
      "ds_write_b128 v232, a[176:179] offset:0\n"
      "ds_write_b128 v232, a[180:183] offset:1024\n"
      "ds_write_b128 v232, a[184:187] offset:2048\n"
      "ds_write_b128 v232, a[188:191] offset:3072\n"
      "ds_write_b128 v232, a[192:195] offset:4096\n"
      "ds_write_b128 v232, a[196:199] offset:5120\n"
      "ds_write_b128 v232, a[200:203] offset:6144\n"
      "ds_write_b128 v232, a[204:207] offset:7168\n"
      "ds_write_b128 v234, v[152:155] offset:0\n"
      "ds_write_b128 v234, v[156:159] offset:1024\n"
      "v_cndmask_b32 v229, v180, v181, s[74:75]\n"
      "v_cndmask_b32 v200, v182, v183, s[74:75]\n"
      "v_cndmask_b32 v228, v176, v177, s[74:75]\n"
      "v_cndmask_b32 v202, v186, v187, s[74:75]\n"
      "v_cndmask_b32 v201, v184, v185, s[74:75]\n"
      "v_add_f32 v200, v200, v202\n"
      "ds_write_b32 v234, v200 offset:4096\n"
      "v_add_f32 v229, v229, v201\n"
      "s_waitcnt lgkmcnt(0)\n"
      "s_barrier\n"
      "ds_read_b32 v208, v209\n"
      "ds_read_b128 v[160:163], v235 offset:0\n"
      "ds_read_b128 v[164:167], v235 offset:1024\n"
      "ds_read_b128 v[64:67], v233 offset:0\n"
      "ds_read_b128 v[68:71], v233 offset:1024\n"
      "ds_read_b128 v[72:75], v233 offset:2048\n"
      "ds_read_b128 v[76:79], v233 offset:3072\n"
      "ds_read_b128 v[80:83], v233 offset:4096\n"
      "ds_read_b128 v[84:87], v233 offset:5120\n"
      "ds_read_b128 v[88:91], v233 offset:6144\n"
      "ds_read_b128 v[92:95], v233 offset:7168\n"
      "ds_read_b32 v204, v235 offset:4096\n"
      "s_branch AGAIN_%=\n"
      ".subsection 0\n"
      : "+{v0}"(tid)
      : [blk] "s"(blockIdx.x), [kp] "s"(__builtin_amdgcn_kernarg_segment_ptr()), "s"(lds0)
      : "v1", "v2", "v3", "v4", "v5", "v6", "v7", "v8", "v9", "v10", "v11", "v12", "v13", "v14", "v15", "v16", "v17", "v18", "v19", "v20", "v21", "v22", "v23", "v24", "v25", "v26", "v27", "v28", "v29", "v30", "v31", "v32", "v33", "v34", "v35", "v36", "v37", "v38", "v39", "v40", "v41", "v42", "v43", "v44", "v45", "v46", "v47", "v48", "v49", "v50", "v51", "v52", "v53", "v54", "v55", "v56", "v57", "v58", "v59", "v60", "v61", "v62", "v63", "v64", "v65", "v66", "v67", "v68", "v69", "v70", "v71", "v72", "v73", "v74", "v75", "v76", "v77", "v78", "v79", "v80", "v81", "v82", "v83", "v84", "v85", "v86", "v87", "v88", "v89", "v90", "v91", "v92", "v93", "v94", "v95", "v96", "v97", "v98", "v99", "v100", "v101", "v102", "v103", "v104", "v105", "v106", "v107", "v108", "v109", "v110", "v111", "v112", "v113", "v114", "v115", "v116", "v117", "v118", "v119", "v120", "v121", "v122", "v123", "v124", "v125", "v126", "v127", "v128", "v129", "v130", "v131", "v132", "v133", "v134", "v135", "v136", "v137", "v138", "v139", "v140", "v141", "v142", "v143", "v144", "v145", "v146", "v147", "v148", "v149", "v150", "v151", "v152", "v153", "v154", "v155", "v156", "v157", "v158", "v159", "v160", "v161", "v162", "v163", "v164", "v165", "v166", "v167", "v168", "v169", "v170", "v171", "v172", "v173", "v174", "v175", "v176", "v177", "v178", "v179", "v180", "v181", "v182", "v183", "v184", "v185", "v186", "v187", "v188", "v189", "v190", "v191", "v192", "v193", "v194", "v195", "v196", "v197", "v198", "v199", "v200", "v201", "v202", "v203", "v204", "v205", "v206", "v207", "v208", "v209", "v210", "v211", "v212", "v213", "v214", "v215", "v216", "v217", "v218", "v219", "v220", "v221", "v222", "v223", "v224", "v225", "v226", "v227", "v228", "v229", "v230", "v231", "v232", "v233", "v234", "v235", "v236", "v237", "v238", "v239", "v240", "v241", "v242", "v243", "v244", "v245", "v246", "v247", "v248", "v249", "v250", "v251", "a0", "a1", "a2", "a3", "a4", "a5", "a6", "a7", "a8", "a9", "a10", "a11", "a12", "a13", "a14", "a15", "a16", "a17", "a18", "a19", "a20", "a21", "a22", "a23", "a24", "a25", "a26", "a27", "a28", "a29", "a30", "a31", "a32", "a33", "a34", "a35", "a36", "a37", "a38", "a39", "a40", "a41", "a42", "a43", "a44", "a45", "a46", "a47", "a48", "a49", "a50", "a51", "a52", "a53", "a54", "a55", "a56", "a57", "a58", "a59", "a60", "a61", "a62", "a63", "a64", "a65", "a66", "a67", "a68", "a69", "a70", "a71", "a72", "a73", "a74", "a75", "a76", "a77", "a78", "a79", "a80", "a81", "a82", "a83", "a84", "a85", "a86", "a87", "a88", "a89", "a90", "a91", "a92", "a93", "a94", "a95", "a96", "a97", "a98", "a99", "a100", "a101", "a102", "a103", "a104", "a105", "a106", "a107", "a108", "a109", "a110", "a111", "a112", "a113", "a114", "a115", "a116", "a117", "a118", "a119", "a120", "a121", "a122", "a123", "a124", "a125", "a126", "a127", "a128", "a129", "a130", "a131", "a132", "a133", "a134", "a135", "a136", "a137", "a138", "a139", "a140", "a141", "a142", "a143", "a144", "a145", "a146", "a147", "a148", "a149", "a150", "a151", "a152", "a153", "a154", "a155", "a156", "a157", "a158", "a159", "a160", "a161", "a162", "a163", "a164", "a165", "a166", "a167", "a168", "a169", "a170", "a171", "a172", "a173", "a174", "a175", "a176", "a177", "a178", "a179", "a180", "a181", "a182", "a183", "a184", "a185", "a186", "a187", "a188", "a189", "a190", "a191", "a192", "a193", "a194", "a195", "a196", "a197", "a198", "a199", "a200", "a201", "a202", "a203", "a204", "a205", "a206", "a207", "s4", "s5", "s6", "s7", "s8", "s9", "s10", "s11", "s12", "s13", "s14", "s15", "s16", "s17", "s18", "s19", "s20", "s21", "s22", "s23", "s24", "s25", "s26", "s27", "s28", "s29", "s30", "s31", "s32", "s33", "s34", "s35", "s38", "s39", "s40", "s43", "s44", "s45", "s46", "s47", "s50", "s52", "s53", "s54", "s56", "s57", "s58", "s59", "s60", "s61", "s62", "s63", "s64", "s65", "s66", "s67", "s68", "s69", "s70", "s71", "s72", "s74", "s75", "s76", "s77", "s78", "s79", "s80", "s81", "s82", "s83", "s84", "s85", "s86", "s88", "s89", "s90", "s91", "s92", "s93", "s94", "s95", "s96", "s97", "vcc", "scc", "memory");
}

}

extern "C" void kernel_launch(void* const* d_in, const int* in_sizes, int n_in, void* d_out, int out_size,
                              void* d_ws, size_t ws_size, hipStream_t stream) {
  if (n_in != 6 || ws_size < WS_END) return;
  const __hip_bfloat16* q = (const __hip_bfloat16*)d_in[0];
  const __hip_bfloat16* kc = (const __hip_bfloat16*)d_in[1];
  const __hip_bfloat16* vc = (const __hip_bfloat16*)d_in[2];
  const __hip_bfloat16* kt = (const __hip_bfloat16*)d_in[3];
  const __hip_bfloat16* vt = (const __hip_bfloat16*)d_in[4];
  const __hip_bfloat16* tm = (const __hip_bfloat16*)d_in[5];
  hipLaunchKernelGGL(tree_attn_main, dim3(B_ * HKV_ * 2), dim3(256), 0, stream, q, kc, vc, kt, vt, tm,
                     (__hip_bfloat16*)d_out, (char*)d_ws);
}
```

```cpp
#include <hip/hip_runtime.h>
#include <hip/hip_bf16.h>
#include <stdint.h>

#pragma clang diagnostic ignored "-Wint-to-pointer-cast"
#pragma clang diagnostic ignored "-Winline-asm"

namespace {

constexpr int B_ = 16, HKV_ = 8, D_ = 128;
constexpr int SLOT = 64 * D_ * 2;
constexpr int LDS_BYTES = 10 * SLOT;

constexpr size_t WS_EPOCH = 0;
constexpr size_t WS_REF = WS_EPOCH + 128 * 4 * 128;
constexpr size_t WS_X = WS_REF + 16384;
constexpr size_t XW_BYTES = 4608;
constexpr size_t WS_END = WS_X + (size_t)128 * 2 * 4 * XW_BYTES;

__global__ __launch_bounds__(256, 1)
void tree_attn_main(const __hip_bfloat16*, const __hip_bfloat16*, const __hip_bfloat16*, const __hip_bfloat16*,
                    const __hip_bfloat16*, const __hip_bfloat16*, __hip_bfloat16*, char*) {
  __shared__ __attribute__((aligned(1024))) char smem[LDS_BYTES];
  const unsigned lds0 = (unsigned)(uintptr_t)smem;
  unsigned tid = threadIdx.x;
  asm volatile(
      "s_load_dwordx16 s[4:19], %[kp], 0x0\n"
      "s_getpc_b64 s[60:61]\n"
      "v_lshlrev_b32 v241, 4, v0\n"
      "s_lshl_b32 s62, %[blk], 9\n"
      "s_and_b32 s62, s62, 0x3000\n"
      "s_add_u32 s60, s60, s62\n"
      "s_addc_u32 s61, s61, 0\n"
      "global_load_dwordx4 v[236:239], v241, s[60:61]\n"
      "v_and_b32 v1, 0xc0, v0\n"
      "v_lshlrev_b32 v248, 1, v0\n"
      "v_readfirstlane_b32 s50, v1\n"
      "v_and_b32 v248, 0x60, v248\n"
      "v_and_b32 v249, 0x70, v241\n"
      "v_xor_b32 v248, v248, v249\n"
      "v_lshl_or_b32 v248, v1, 6, v248\n"
      "v_and_b32 v1, 56, v0\n"
      "v_lshl_or_b32 v248, v1, 5, v248\n"
      "s_mov_b32 s64, 0x14100400\n"
      "s_mov_b32 s65, 0x54504440\n"
      "s_lshl_b32 s66, %[blk], 3\n"
      "s_lshr_b64 s[64:65], s[64:65], s66\n"
      "s_mov_b32 s66, 0x3020100\n"
      "s_mov_b32 s67, 0x83828180\n"
      "s_and_b32 s65, %[blk], 56\n"
      "s_lshr_b64 s[66:67], s[66:67], s65\n"
      "s_or_b32 s64, s64, s66\n"
      "s_lshr_b32 s65, %[blk], 3\n"
      "s_and_b32 s65, s65, 24\n"
      "s_lshr_b32 s65, 0x28082000, s65\n"
      "s_or_b32 s64, s64, s65\n"
      "s_and_b32 s63, s64, 0x80\n"
      "s_and_b32 s44, s64, 127\n"
      "v_or_b32 v248, s63, v248\n"
      "v_xor_b32 v249, 0x80, v248\n"
      "s_lshl_b32 s64, s44, 20\n"
      "s_lshl_b32 s62, s44, 15\n"
      "s_lshl_b32 s50, s50, 6\n"
      "s_add_u32 m0, s50, 131072\n"
      "s_mov_b32 s58, 0x120000\n"
      "s_mov_b32 s59, 0x20000\n"
      "s_movk_i32 s22, 0x4000\n"
      "s_waitcnt lgkmcnt(0)\n"
      "s_add_u32 s56, s4, s62\n"
      "s_addc_u32 s57, s5, 0\n"
      "buffer_load_dwordx4 v248, s[56:59], 0 offen nt lds\n"
      "buffer_load_dwordx4 v248, s[56:59], 0 offen offset:2048 nt lds\n"
      "s_bitset1_b32 m0, 10\n"
      "s_memrealtime s[82:83]\n"
      "s_add_u32 s20, s6, s64\n"
      "s_addc_u32 s21, s7, 0\n"
      "s_add_u32 s32, s8, s64\n"
      "s_addc_u32 s33, s9, 0\n"
      "s_load_dword s92, s[20:21], 0x0\n"
      "s_load_dword s92, s[10:11], 0x0\n"
      "s_load_dword s92, s[32:33], 0x0\n"
      "buffer_load_dwordx4 v249, s[56:59], 0 offen nt lds\n"
      "buffer_load_dwordx4 v249, s[56:59], 0 offen offset:2048 nt lds\n"
      "s_add_u32 m0, s50, 147456\n"
      "s_load_dword s92, s[18:19], 0x0\n"
      "buffer_load_dwordx4 v248, s[56:59], s22 offen nt lds\n"
      "buffer_load_dwordx4 v248, s[56:59], s22 offen offset:2048 nt lds\n"
      "s_bitset1_b32 m0, 10\n"
      "s_nop 0\n"
      "buffer_load_dwordx4 v249, s[56:59], s22 offen nt lds\n"
      "buffer_load_dwordx4 v249, s[56:59], s22 offen offset:2048 nt lds\n"
      "s_mov_b64 s[56:57], s[20:21]\n"
      "s_load_dword s92, s[14:15], 0x0\n"
      "s_load_dword s92, s[12:13], 0x0\n"
      "s_lshr_b32 s74, s50, 2\n"
      "s_lshr_b32 s75, s50, 5\n"
      "s_xor_b32 s75, s75, s63\n"
      "s_and_b32 s75, s75, 0x80\n"
      "s_and_b32 s76, s74, 0x800\n"
      "s_add_u32 s75, s75, s76\n"
      "s_add_u32 s70, s63, s50\n"
      "s_sub_u32 s70, s70, s75\n"
      "s_lshr_b32 s75, s50, 1\n"
      "s_lshr_b32 s71, s62, 2\n"
      "s_sub_u32 s71, s71, s75\n"
      "s_ashr_i32 s72, s71, 31\n"
      "s_mov_b32 s23, s59\n"
      "s_mov_b64 s[26:27], s[22:23]\n"
      "s_sub_u32 s20, s56, s70\n"
      "s_subb_u32 s21, s57, 0\n"
      "s_add_u32 m0, s74, 24576\n"
      "s_add_u32 s24, s10, s71\n"
      "buffer_load_dwordx4 v248, s[20:23], 0 offen nt lds\n"
      "s_addc_u32 s25, s11, s72\n"
      "s_add_u32 m0, s75, 16384\n"
      "s_nop 0\n"
      "buffer_load_dwordx4 v248, s[24:27], 0 offen nt lds\n"
      "s_bitset1_b32 m0, 10\n"
      "s_nop 0\n"
      "buffer_load_dwordx4 v249, s[24:27], 0 offen nt lds\n"
      "s_lshr_b32 s43, s63, 7\n"
      "s_lshl_b32 s76, s44, 9\n"
      "s_lshr_b32 s69, s50, 5\n"
      "s_add_u32 s76, s76, s69\n"
      "s_add_u32 s46, s18, s76\n"
      "s_addc_u32 s47, s19, 0\n"
      "s_mul_i32 s76, s43, 0x3000\n"
      "s_lshl_b32 s69, s44, 8\n"
      "s_and_b32 s69, s69, 0x7800\n"
      "s_add_u32 s14, s14, s69\n"
      "s_addc_u32 s15, s15, 0\n"
      "v_mov_b64 v[192:193], 0\n"
      "v_mov_b64 v[194:195], 0\n"
      "v_lshlrev_b32 v3, 6, v0\n"
      "v_and_b32 v3, 0x3c0, v3\n"
      "v_lshrrev_b32 v4, 1, v0\n"
      "v_and_or_b32 v211, v4, 24, v3\n"
      "v_mov_b64 v[200:201], 0\n"
      "v_mov_b64 v[202:203], 0\n"
      "v_mov_b64 v[204:205], 0\n"
      "v_mov_b64 v[206:207], 0\n"
      "s_cmp_lg_u32 s76, s50\n"
      "s_cbranch_scc1 NOMASK_%=\n"
      "global_load_dwordx2 v[200:201], v211, s[14:15] offset:0\n"
      "global_load_dwordx2 v[202:203], v211, s[14:15] offset:32\n"
      "global_load_dwordx2 v[204:205], v211, s[14:15] offset:1024\n"
      "global_load_dwordx2 v[206:207], v211, s[14:15] offset:1056\n"
      "NOMASK_%=:\n"
      "global_load_dword v208, v192, s[46:47] sc1\n"
      "s_mul_i32 s69, s43, 0x8000\n"
      "s_bitcmp1_b32 s44, 0\n"
      "s_cselect_b32 s66, 0x8000, 0\n"
      "s_xor_b32 s69, s69, s66\n"
      "v_and_b32 v228, 0xff, v248\n"
      "v_xor_b32 v228, s69, v228\n"
      "v_bfe_i32 v2, v0, 3, 1\n"
      "v_and_b32 v2, 0x1000, v2\n"
      "v_xor_b32 v228, v228, v2\n"
      "v_bfe_i32 v2, v0, 4, 1\n"
      "v_and_b32 v2, 0x20400, v2\n"
      "v_xor_b32 v228, v228, v2\n"
      "v_bfe_i32 v2, v0, 5, 1\n"
      "v_and_b32 v2, 0x100, v2\n"
      "v_xor_b32 v228, v228, v2\n"
      "v_bfe_i32 v2, v0, 6, 1\n"
      "v_and_b32 v2, 0x400, v2\n"
      "v_xor_b32 v228, v228, v2\n"
      "v_bfe_i32 v2, v0, 7, 1\n"
      "v_and_b32 v2, 0x2000, v2\n"
      "v_xor_b32 v228, v228, v2\n"
      "v_xor_b32 v231, 0x280, v228\n"
      "v_xor_b32 v230, 0x200, v228\n"
      "v_xor_b32 v229, 0x80, v228\n"
      "v_add_u32 v228, 0xc00, v228\n"
      "v_add_u32 v229, 0x800, v229\n"
      "v_add_u32 v230, 0x400, v230\n"
      "s_sub_u32 s56, s56, 0xc00\n"
      "s_subb_u32 s57, s57, 0\n"
      "s_sub_u32 s32, s32, 0xc00\n"
      "s_subb_u32 s33, s33, 0\n"
      "s_xor_b64 s[32:33], s[32:33], s[56:57]\n"
      "s_mov_b32 s69, 0\n"
      "s_bitcmp1_b32 s44, 4\n"
      "s_cselect_b32 s66, 0x10000, 0\n"
      "s_xor_b32 s69, s69, s66\n"
      "s_bitcmp1_b32 s44, 0\n"
      "s_cselect_b32 s66, 0x800, 0\n"
      "s_xor_b32 s69, s69, s66\n"
      "s_bitcmp1_b32 s44, 2\n"
      "s_cselect_b32 s66, 0x40800, 0\n"
      "s_xor_b32 s69, s69, s66\n"
      "s_bitcmp1_b32 s44, 4\n"
      "s_cselect_b32 s66, 0x4000, 0\n"
      "s_xor_b32 s69, s69, s66\n"
      "s_mov_b32 s60, s69\n"
      "s_mov_b32 s62, s50\n"
      "s_mov_b64 s[30:31], s[22:23]\n"
      "s_add_u32 s28, s12, s71\n"
      "s_addc_u32 s29, s13, s72\n"
      "s_lshl_b32 s69, s43, 13\n"
      "s_xor_b32 s69, s69, 0x2000\n"
      "v_add_u32 v2, s69, v241\n"
      "ds_write_b128 v241, v[192:195] offset:28672\n"
      "ds_write_b128 v2, v[192:195] offset:49152\n"
      "ds_write_b128 v2, v[192:195] offset:53248\n"
      "v_and_b32 v241, 0x3f0, v241\n"
      "s_and_b32 s91, s50, 8192\n"
      "v_lshlrev_b32 v4, 8, v0\n"
      "v_and_b32 v4, 0x800, v4\n"
      "v_lshlrev_b32 v5, 7, v0\n"
      "v_and_b32 v6, 0x380, v5\n"
      "v_or3_b32 v4, v4, v6, s91\n"
      "v_and_b32 v6, 0x60, v241\n"
      "v_and_b32 v2, 48, v0\n"
      "v_xor_b32 v6, v6, v2\n"
      "v_or_b32 v242, v4, v6\n"
      "v_xor_b32 v243, 64, v242\n"
      "v_lshlrev_b32 v4, 6, v0\n"
      "v_and_b32 v6, 0x800, v4\n"
      "v_lshlrev_b32 v2, 5, v0\n"
      "v_and_b32 v2, 0x380, v2\n"
      "s_add_u32 s69, s91, 16384\n"
      "v_or3_b32 v6, v6, v2, s69\n"
      "v_lshlrev_b32 v2, 3, v0\n"
      "v_and_or_b32 v6, v2, 24, v6\n"
      "v_lshlrev_b32 v2, 2, v0\n"
      "v_and_b32 v2, 0x60, v2\n"
      "v_xor_b32 v244, v6, v2\n"
      "v_xor_b32 v245, 0x20, v244\n"
      "v_xor_b32 v246, 0x40, v244\n"
      "v_xor_b32 v247, 0x60, v244\n"
      "v_and_b32 v5, 0x1e00, v5\n"
      "v_and_or_b32 v250, v241, 48, v5\n"
      "s_lshl_b32 s69, %[blk], 9\n"
      "s_and_b32 s69, s69, 0x1e00\n"
      "v_and_b32 v4, 0x1c0, v4\n"
      "v_or_b32 v251, s69, v4\n"
      "s_and_b32 s69, s50, 4096\n"
      "s_lshl_b32 s69, s69, 2\n"
      "s_add_u32 s69, s69, 131072\n"
      "s_sub_u32 s91, 16384, s91\n"
      "v_add_u32 v196, s69, v242\n"
      "v_add_u32 v198, s91, v242\n"
      "v_xor_b32 v214, 0x2000, v196\n"
      "v_add_u32 v197, s69, v243\n"
      "v_add_u32 v199, s91, v243\n"
      "v_xor_b32 v215, 0x2000, v197\n"
      "v_and_b32 v3, 1, v0\n"
      "v_bfe_u32 v4, v0, 1, 5\n"
      "v_lshlrev_b32 v3, 1, v3\n"
      "v_sub_u32 v4, v4, v3\n"
      "v_mov_b32 v219, s60\n"
      "v_bfe_i32 v2, v4, 0, 1\n"
      "v_and_b32 v2, 0x10000, v2\n"
      "v_xor_b32 v219, v219, v2\n"
      "v_bfe_i32 v2, v4, 1, 1\n"
      "v_and_b32 v2, 0x800, v2\n"
      "v_xor_b32 v219, v219, v2\n"
      "v_bfe_i32 v2, v4, 2, 1\n"
      "v_and_b32 v2, 0x40800, v2\n"
      "v_xor_b32 v219, v219, v2\n"
      "v_bfe_i32 v2, v4, 3, 1\n"
      "v_and_b32 v2, 0x80000, v2\n"
      "v_xor_b32 v219, v219, v2\n"
      "v_bfe_i32 v2, v4, 4, 1\n"
      "v_and_b32 v2, 0x4000, v2\n"
      "v_xor_b32 v219, v219, v2\n"
      "v_writelane_b32 v219, 0, 1\n"
      "s_mov_b32 s61, 0\n"
      "s_add_u32 s69, s50, 131072\n"
      "v_add_u32 v210, s69, v241\n"
      "v_and_b32 v2, 3, v0\n"
      "v_mov_b32 v4, 0x3f803f80\n"
      "v_cmp_eq_u32 vcc, 0, v2\n"
      "s_nop 1\n"
      "v_cndmask_b32 v5, 0, v4, vcc\n"
      "v_accvgpr_write_b32 a128, v5\n"
      "v_accvgpr_write_b32 a129, v5\n"
      "v_accvgpr_write_b32 a130, v5\n"
      "v_accvgpr_write_b32 a131, v5\n"
      "v_cmp_eq_u32 vcc, 1, v2\n"
      "s_nop 1\n"
      "v_cndmask_b32 v5, 0, v4, vcc\n"
      "v_accvgpr_write_b32 a132, v5\n"
      "v_accvgpr_write_b32 a133, v5\n"
      "v_accvgpr_write_b32 a134, v5\n"
      "v_accvgpr_write_b32 a135, v5\n"
      "v_cmp_eq_u32 vcc, 2, v2\n"
      "s_nop 1\n"
      "v_cndmask_b32 v5, 0, v4, vcc\n"
      "v_accvgpr_write_b32 a136, v5\n"
      "v_accvgpr_write_b32 a137, v5\n"
      "v_accvgpr_write_b32 a138, v5\n"
      "v_accvgpr_write_b32 a139, v5\n"
      "v_cmp_eq_u32 vcc, 3, v2\n"
      "s_nop 1\n"
      "v_cndmask_b32 v5, 0, v4, vcc\n"
      "v_accvgpr_write_b32 a140, v5\n"
      "v_accvgpr_write_b32 a141, v5\n"
      "v_accvgpr_write_b32 a142, v5\n"
      "v_accvgpr_write_b32 a143, v5\n"
      "v_mfma_f32_32x32x16_bf16 a[144:159], v[192:195], v[192:195], 0\n"
      "v_mfma_f32_32x32x16_bf16 a[160:175], v[192:195], v[192:195], 0\n"
      "v_mfma_f32_32x32x16_bf16 a[176:191], v[192:195], v[192:195], 0\n"
      "v_mfma_f32_32x32x16_bf16 a[192:207], v[192:195], v[192:195], 0\n"
      "v_mfma_f32_32x32x16_bf16 v[0:15], v[192:195], v[192:195], 0\n"
      "v_mfma_f32_32x32x16_bf16 v[16:31], v[192:195], v[192:195], 0\n"
      "v_mfma_f32_32x32x16_bf16 v[32:47], v[192:195], v[192:195], 0\n"
      "v_mfma_f32_32x32x16_bf16 v[48:63], v[192:195], v[192:195], 0\n"
      "v_mfma_f32_32x32x16_bf16 v[128:143], v[192:195], v[192:195], 0\n"
      "v_mov_b64 v[180:181], 0\n"
      "v_mov_b64 v[182:183], 0\n"
      "v_mov_b64 v[184:185], 0\n"
      "v_mov_b64 v[186:187], 0\n"
      "s_mov_b32 s94, 0x3e0293ee\n"
      "s_mov_b32 s95, s94\n"
      "s_lshl_b32 s69, s43, 13\n"
      "s_add_u32 s45, s69, s75\n"
      "s_mov_b32 m0, s62\n"
      "s_nop 0\n"
      "buffer_load_dwordx4 v228, s[56:59], s60 offen nt lds\n"
      "buffer_load_dwordx4 v229, s[56:59], s60 offen offset:1024 nt lds\n"
      "buffer_load_dwordx4 v230, s[56:59], s60 offen offset:2048 nt lds\n"
      "buffer_load_dwordx4 v231, s[56:59], s60 offen offset:3072 nt lds\n"
      "s_xor_b64 s[56:57], s[56:57], s[32:33]\n"
      "s_add_u32 s61, s61, 1\n"
      "s_add_u32 s62, s62, 0x4000\n"
      "s_and_b32 s62, s62, 0x1ffff\n"
      "v_readlane_b32 s60, v219, s61\n"
      "s_xor_b64 s[56:57], s[56:57], s[32:33]\n"
      "s_add_u32 s61, s61, 1\n"
      "s_add_u32 s62, s62, 0x4000\n"
      "s_and_b32 s62, s62, 0x1ffff\n"
      "v_readlane_b32 s60, v219, s61\n"
      "s_waitcnt vmcnt(8)\n"
      "s_memrealtime s[68:69]\n"
      "ds_read_b128 v[64:67], v210 offset:0\n"
      "ds_read_b128 v[68:71], v210 offset:1024\n"
      "ds_read_b128 v[72:75], v210 offset:2048\n"
      "ds_read_b128 v[76:79], v210 offset:3072\n"
      "ds_read_b128 v[80:83], v210 offset:16384\n"
      "ds_read_b128 v[84:87], v210 offset:17408\n"
      "ds_read_b128 v[88:91], v210 offset:18432\n"
      "ds_read_b128 v[92:95], v210 offset:19456\n"
      "s_add_u32 s20, s18, 65536\n"
      "s_addc_u32 s21, s19, 0\n"
      "s_lshl_b32 s75, %[blk], 6\n"
      "s_xor_b32 s74, s50, 4096\n"
      "s_add_u32 s75, s75, s74\n"
      "s_waitcnt lgkmcnt(0)\n"
      "s_sub_u32 s67, s68, s82\n"
      "s_min_u32 s67, s67, 165\n"
      "s_add_u32 s93, s82, s67\n"
      "s_sub_u32 s71, s68, 400\n"
      "s_cmp_lt_u32 s75, 512\n"
      "s_cbranch_scc0 NOLEAD_%=\n"
      "v_mov_b32 v188, s68\n"
      "v_mov_b32 v189, s68\n"
      "v_mov_b32 v190, s68\n"
      "v_mov_b32 v191, s68\n"
      "buffer_store_dwordx4 v[188:191], v250, s[20:23], s75 offen sc1\n"
      "s_nop 1\n"
      "NOLEAD_%=:\n"
      "s_mov_b32 m0, s62\n"
      "s_nop 0\n"
      "buffer_load_dwordx4 v228, s[56:59], s60 offen nt lds\n"
      "buffer_load_dwordx4 v229, s[56:59], s60 offen offset:1024 nt lds\n"
      "buffer_load_dwordx4 v230, s[56:59], s60 offen offset:2048 nt lds\n"
      "buffer_load_dwordx4 v231, s[56:59], s60 offen offset:3072 nt lds\n"
      "s_xor_b64 s[56:57], s[56:57], s[32:33]\n"
      "s_add_u32 s61, s61, 1\n"
      "s_add_u32 s62, s62, 0x4000\n"
      "s_and_b32 s62, s62, 0x1ffff\n"
      "v_readlane_b32 s60, v219, s61\n"
      "s_xor_b64 s[56:57], s[56:57], s[32:33]\n"
      "s_add_u32 s61, s61, 1\n"
      "s_add_u32 s62, s62, 0x4000\n"
      "s_and_b32 s62, s62, 0x1ffff\n"
      "v_readlane_b32 s60, v219, s61\n"
      "s_add_u32 m0, s45, 49152\n"
      "s_nop 0\n"
      "buffer_load_dwordx4 v248, s[28:31], 0 offen nt lds\n"
      "s_bitset1_b32 m0, 10\n"
      "s_nop 0\n"
      "buffer_load_dwordx4 v249, s[28:31], 0 offen nt lds\n"
      "v_lshlrev_b32 v188, 16, v64\n"
      "v_and_b32 v189, 0xffff0000, v64\n"
      "v_lshlrev_b32 v190, 16, v65\n"
      "v_and_b32 v191, 0xffff0000, v65\n"
      "v_lshlrev_b32 v192, 16, v66\n"
      "v_and_b32 v193, 0xffff0000, v66\n"
      "v_lshlrev_b32 v194, 16, v67\n"
      "v_and_b32 v195, 0xffff0000, v67\n"
      "v_pk_mul_f32 v[188:189], v[188:189], s[94:95]\n"
      "v_pk_mul_f32 v[190:191], v[190:191], s[94:95]\n"
      "v_pk_mul_f32 v[192:193], v[192:193], s[94:95]\n"
      "v_pk_mul_f32 v[194:195], v[194:195], s[94:95]\n"
      "v_cvt_pk_bf16_f32 v64, v188, v189\n"
      "v_cvt_pk_bf16_f32 v65, v190, v191\n"
      "v_cvt_pk_bf16_f32 v66, v192, v193\n"
      "v_cvt_pk_bf16_f32 v67, v194, v195\n"
      "ds_write_b128 v210, v[64:67] offset:0\n"
      "v_lshlrev_b32 v188, 16, v68\n"
      "v_and_b32 v189, 0xffff0000, v68\n"
      "v_lshlrev_b32 v190, 16, v69\n"
      "v_and_b32 v191, 0xffff0000, v69\n"
      "v_lshlrev_b32 v192, 16, v70\n"
      "v_and_b32 v193, 0xffff0000, v70\n"
      "v_lshlrev_b32 v194, 16, v71\n"
      "v_and_b32 v195, 0xffff0000, v71\n"
      "v_pk_mul_f32 v[188:189], v[188:189], s[94:95]\n"
      "v_pk_mul_f32 v[190:191], v[190:191], s[94:95]\n"
      "v_pk_mul_f32 v[192:193], v[192:193], s[94:95]\n"
      "v_pk_mul_f32 v[194:195], v[194:195], s[94:95]\n"
      "v_cvt_pk_bf16_f32 v68, v188, v189\n"
      "v_cvt_pk_bf16_f32 v69, v190, v191\n"
      "v_cvt_pk_bf16_f32 v70, v192, v193\n"
      "v_cvt_pk_bf16_f32 v71, v194, v195\n"
      "ds_write_b128 v210, v[68:71] offset:1024\n"
      "v_lshlrev_b32 v188, 16, v72\n"
      "v_and_b32 v189, 0xffff0000, v72\n"
      "v_lshlrev_b32 v190, 16, v73\n"
      "v_and_b32 v191, 0xffff0000, v73\n"
      "v_lshlrev_b32 v192, 16, v74\n"
      "v_and_b32 v193, 0xffff0000, v74\n"
      "v_lshlrev_b32 v194, 16, v75\n"
      "v_and_b32 v195, 0xffff0000, v75\n"
      "v_pk_mul_f32 v[188:189], v[188:189], s[94:95]\n"
      "v_pk_mul_f32 v[190:191], v[190:191], s[94:95]\n"
      "v_pk_mul_f32 v[192:193], v[192:193], s[94:95]\n"
      "v_pk_mul_f32 v[194:195], v[194:195], s[94:95]\n"
      "v_cvt_pk_bf16_f32 v72, v188, v189\n"
      "v_cvt_pk_bf16_f32 v73, v190, v191\n"
      "v_cvt_pk_bf16_f32 v74, v192, v193\n"
      "v_cvt_pk_bf16_f32 v75, v194, v195\n"
      "ds_write_b128 v210, v[72:75] offset:2048\n"
      "v_lshlrev_b32 v188, 16, v76\n"
      "v_and_b32 v189, 0xffff0000, v76\n"
      "v_lshlrev_b32 v190, 16, v77\n"
      "v_and_b32 v191, 0xffff0000, v77\n"
      "v_lshlrev_b32 v192, 16, v78\n"
      "v_and_b32 v193, 0xffff0000, v78\n"
      "v_lshlrev_b32 v194, 16, v79\n"
      "v_and_b32 v195, 0xffff0000, v79\n"
      "v_pk_mul_f32 v[188:189], v[188:189], s[94:95]\n"
      "v_pk_mul_f32 v[190:191], v[190:191], s[94:95]\n"
      "v_pk_mul_f32 v[192:193], v[192:193], s[94:95]\n"
      "v_pk_mul_f32 v[194:195], v[194:195], s[94:95]\n"
      "v_cvt_pk_bf16_f32 v76, v188, v189\n"
      "v_cvt_pk_bf16_f32 v77, v190, v191\n"
      "v_cvt_pk_bf16_f32 v78, v192, v193\n"
      "v_cvt_pk_bf16_f32 v79, v194, v195\n"
      "ds_write_b128 v210, v[76:79] offset:3072\n"
      "v_lshlrev_b32 v188, 16, v80\n"
      "v_and_b32 v189, 0xffff0000, v80\n"
      "v_lshlrev_b32 v190, 16, v81\n"
      "v_and_b32 v191, 0xffff0000, v81\n"
      "v_lshlrev_b32 v192, 16, v82\n"
      "v_and_b32 v193, 0xffff0000, v82\n"
      "v_lshlrev_b32 v194, 16, v83\n"
      "v_and_b32 v195, 0xffff0000, v83\n"
      "v_pk_mul_f32 v[188:189], v[188:189], s[94:95]\n"
      "v_pk_mul_f32 v[190:191], v[190:191], s[94:95]\n"
      "v_pk_mul_f32 v[192:193], v[192:193], s[94:95]\n"
      "v_pk_mul_f32 v[194:195], v[194:195], s[94:95]\n"
      "v_cvt_pk_bf16_f32 v80, v188, v189\n"
      "v_cvt_pk_bf16_f32 v81, v190, v191\n"
      "v_cvt_pk_bf16_f32 v82, v192, v193\n"
      "v_cvt_pk_bf16_f32 v83, v194, v195\n"
      "ds_write_b128 v210, v[80:83] offset:16384\n"
      "v_lshlrev_b32 v188, 16, v84\n"
      "v_and_b32 v189, 0xffff0000, v84\n"
      "v_lshlrev_b32 v190, 16, v85\n"
      "v_and_b32 v191, 0xffff0000, v85\n"
      "v_lshlrev_b32 v192, 16, v86\n"
      "v_and_b32 v193, 0xffff0000, v86\n"
      "v_lshlrev_b32 v194, 16, v87\n"
      "v_and_b32 v195, 0xffff0000, v87\n"
      "v_pk_mul_f32 v[188:189], v[188:189], s[94:95]\n"
      "v_pk_mul_f32 v[190:191], v[190:191], s[94:95]\n"
      "v_pk_mul_f32 v[192:193], v[192:193], s[94:95]\n"
      "v_pk_mul_f32 v[194:195], v[194:195], s[94:95]\n"
      "v_cvt_pk_bf16_f32 v84, v188, v189\n"
      "v_cvt_pk_bf16_f32 v85, v190, v191\n"
      "v_cvt_pk_bf16_f32 v86, v192, v193\n"
      "v_cvt_pk_bf16_f32 v87, v194, v195\n"
      "ds_write_b128 v210, v[84:87] offset:17408\n"
      "v_lshlrev_b32 v188, 16, v88\n"
      "v_and_b32 v189, 0xffff0000, v88\n"
      "v_lshlrev_b32 v190, 16, v89\n"
      "v_and_b32 v191, 0xffff0000, v89\n"
      "v_lshlrev_b32 v192, 16, v90\n"
      "v_and_b32 v193, 0xffff0000, v90\n"
      "v_lshlrev_b32 v194, 16, v91\n"
      "v_and_b32 v195, 0xffff0000, v91\n"
      "v_pk_mul_f32 v[188:189], v[188:189], s[94:95]\n"
      "v_pk_mul_f32 v[190:191], v[190:191], s[94:95]\n"
      "v_pk_mul_f32 v[192:193], v[192:193], s[94:95]\n"
      "v_pk_mul_f32 v[194:195], v[194:195], s[94:95]\n"
      "v_cvt_pk_bf16_f32 v88, v188, v189\n"
      "v_cvt_pk_bf16_f32 v89, v190, v191\n"
      "v_cvt_pk_bf16_f32 v90, v192, v193\n"
      "v_cvt_pk_bf16_f32 v91, v194, v195\n"
      "ds_write_b128 v210, v[88:91] offset:18432\n"
      "v_lshlrev_b32 v188, 16, v92\n"
      "v_and_b32 v189, 0xffff0000, v92\n"
      "v_lshlrev_b32 v190, 16, v93\n"
      "v_and_b32 v191, 0xffff0000, v93\n"
      "v_lshlrev_b32 v192, 16, v94\n"
      "v_and_b32 v193, 0xffff0000, v94\n"
      "v_lshlrev_b32 v194, 16, v95\n"
      "v_and_b32 v195, 0xffff0000, v95\n"
      "v_pk_mul_f32 v[188:189], v[188:189], s[94:95]\n"
      "v_pk_mul_f32 v[190:191], v[190:191], s[94:95]\n"
      "v_pk_mul_f32 v[192:193], v[192:193], s[94:95]\n"
      "v_pk_mul_f32 v[194:195], v[194:195], s[94:95]\n"
      "v_cvt_pk_bf16_f32 v92, v188, v189\n"
      "v_cvt_pk_bf16_f32 v93, v190, v191\n"
      "v_cvt_pk_bf16_f32 v94, v192, v193\n"
      "v_cvt_pk_bf16_f32 v95, v194, v195\n"
      "ds_write_b128 v210, v[92:95] offset:19456\n"
      "s_mov_b32 m0, s62\n"
      "s_nop 0\n"
      "buffer_load_dwordx4 v228, s[56:59], s60 offen nt lds\n"
      "buffer_load_dwordx4 v229, s[56:59], s60 offen offset:1024 nt lds\n"
      "buffer_load_dwordx4 v230, s[56:59], s60 offen offset:2048 nt lds\n"
      "buffer_load_dwordx4 v231, s[56:59], s60 offen offset:3072 nt lds\n"
      "s_xor_b64 s[56:57], s[56:57], s[32:33]\n"
      "s_add_u32 s61, s61, 1\n"
      "s_add_u32 s62, s62, 0x4000\n"
      "s_and_b32 s62, s62, 0x1ffff\n"
      "v_readlane_b32 s60, v219, s61\n"
      "s_cmp_eq_u32 s50, 0\n"
      "s_cselect_b32 s38, 0x44554001, 1\n"
      "s_cselect_b32 s39, 0x2a040083, 3\n"
      "s_cselect_b32 s52, 0x31109890, 0\n"
      "s_cselect_b32 s53, 0x80011, 0\n"
      "s_mov_b64 s[34:35], 0\n"
      "s_and_b32 s69, %[blk], 7\n"
      "s_mul_i32 s69, s69, 5\n"
      "s_lshr_b32 s74, %[blk], 4\n"
      "s_add_u32 s69, s69, s74\n"
      "s_and_b32 s69, s69, 15\n"
      "s_mul_i32 s74, s69, 16\n"
      "s_lshr_b32 s74, s74, 6\n"
      "s_lshl2_add_u32 s76, s74, 9\n"
      "s_bitset1_b64 s[34:35], s76\n"
      "s_lshl_b32 s74, s74, 1\n"
      "s_sub_u32 s74, 28, s74\n"
      "s_bitset1_b32 s39, s74\n"
      "s_mul_i32 s74, s69, 20\n"
      "s_lshr_b32 s74, s74, 6\n"
      "s_lshl2_add_u32 s76, s74, 25\n"
      "s_bitset1_b64 s[34:35], s76\n"
      "s_lshl_b32 s74, s74, 1\n"
      "s_sub_u32 s74, 20, s74\n"
      "s_bitset1_b32 s39, s74\n"
      "s_mul_i32 s74, s69, 20\n"
      "s_lshr_b32 s74, s74, 6\n"
      "s_lshl2_add_u32 s76, s74, 45\n"
      "s_bitset1_b64 s[34:35], s76\n"
      "s_lshl_b32 s74, s74, 1\n"
      "s_sub_u32 s74, 10, s74\n"
      "s_bitset1_b32 s39, s74\n"
      "s_mov_b32 s54, 3\n"
      "s_mov_b32 s70, 250\n"
      "s_mov_b32 s86, 59\n"
      "s_mov_b32 s77, 177\n"
      "s_lshl_b32 s91, s50, 16\n"
      "s_sub_u32 s80, 46, s91\n"
      "s_sub_u32 s90, 34, s91\n"
      "s_sub_u32 s72, s75, 18432\n"
      "s_and_b32 s79, %[blk], 31\n"
      "s_lshl_b32 s79, s79, 8\n"
      "s_add_u32 s79, s79, 8192\n"
      "s_mul_i32 s69, %[blk], 53\n"
      "s_lshr_b32 s69, s69, 8\n"
      "s_add_u32 s66, s93, s69\n"
      "s_add_u32 s66, s66, 125\n"
      "s_sub_u32 s81, s93, s71\n"
      "s_add_u32 s81, s81, -26\n"
      "s_sub_u32 s66, s66, s91\n"
      "s_mov_b32 s88, s66\n"
      "s_mov_b32 s65, s66\n"
      "s_getpc_b64 s[84:85]\n"
      "PC_%=:\n"
      "s_add_u32 s84, s84, HOOK_%=-PC_%=\n"
      "s_addc_u32 s85, s85, 0\n"
      "s_waitcnt vmcnt(14) lgkmcnt(0)\n"
      "s_barrier\n"
      "ds_read_b128 a[64:67], v198 offset:8192\n"
      "ds_read_b128 a[72:75], v199 offset:8192\n"
      "ds_read_b128 a[80:83], v198 offset:9216\n"
      "ds_read_b128 a[88:91], v199 offset:9216\n"
      "ds_read_b128 a[0:3], v196 offset:0\n"
      "ds_read_b128 a[16:19], v197 offset:0\n"
      "ds_read_b128 a[32:35], v196 offset:1024\n"
      "ds_read_b128 a[48:51], v197 offset:1024\n"
      "ds_read_b128 a[4:7], v196 offset:4096\n"
      "ds_read_b128 a[20:23], v197 offset:4096\n"
      "ds_read_b128 a[36:39], v196 offset:5120\n"
      "ds_read_b128 a[52:55], v197 offset:5120\n"
      "v_readfirstlane_b32 s40, v208\n"
      "s_mov_b32 s94, 0xceabfb8f\n"
      "v_lshlrev_b32 v144, 16, v200\n"
      "v_and_b32 v145, 0xffff0000, v200\n"
      "v_lshlrev_b32 v146, 16, v201\n"
      "v_and_b32 v147, 0xffff0000, v201\n"
      "v_lshlrev_b32 v148, 16, v202\n"
      "v_and_b32 v149, 0xffff0000, v202\n"
      "v_lshlrev_b32 v150, 16, v203\n"
      "v_and_b32 v151, 0xffff0000, v203\n"
      "v_lshlrev_b32 v152, 16, v204\n"
      "v_and_b32 v153, 0xffff0000, v204\n"
      "v_lshlrev_b32 v154, 16, v205\n"
      "v_and_b32 v155, 0xffff0000, v205\n"
      "v_lshlrev_b32 v156, 16, v206\n"
      "v_and_b32 v157, 0xffff0000, v206\n"
      "v_lshlrev_b32 v158, 16, v207\n"
      "v_and_b32 v159, 0xffff0000, v207\n"
      "s_waitcnt lgkmcnt(4)\n"
      "v_mfma_f32_16x16x32_bf16 v[64:67], a[64:67], a[0:3], 0\n"
      "ds_read_b128 a[8:11], v214 offset:0\n"
      "v_fma_f32 v144, -v144, s94, s94\n"
      "v_mfma_f32_16x16x32_bf16 v[64:67], a[72:75], a[16:19], v[64:67]\n"
      "ds_read_b128 a[24:27], v215 offset:0\n"
      "v_fma_f32 v145, -v145, s94, s94\n"
      "v_mfma_f32_16x16x32_bf16 v[64:67], a[80:83], a[32:35], v[64:67]\n"
      "ds_read_b128 a[40:43], v214 offset:1024\n"
      "v_fma_f32 v146, -v146, s94, s94\n"
      "v_mfma_f32_16x16x32_bf16 v[64:67], a[88:91], a[48:51], v[64:67]\n"
      "ds_read_b128 a[56:59], v215 offset:1024\n"
      "v_fma_f32 v147, -v147, s94, s94\n"
      "s_waitcnt lgkmcnt(4)\n"
      "v_mfma_f32_16x16x32_bf16 v[72:75], a[64:67], a[4:7], 0\n"
      "ds_read_b128 a[12:15], v214 offset:4096\n"
      "v_fma_f32 v148, -v148, s94, s94\n"
      "v_mfma_f32_16x16x32_bf16 v[72:75], a[72:75], a[20:23], v[72:75]\n"
      "ds_read_b128 a[28:31], v215 offset:4096\n"
      "v_fma_f32 v149, -v149, s94, s94\n"
      "v_mfma_f32_16x16x32_bf16 v[72:75], a[80:83], a[36:39], v[72:75]\n"
      "ds_read_b128 a[44:47], v214 offset:5120\n"
      "v_fma_f32 v150, -v150, s94, s94\n"
      "v_mfma_f32_16x16x32_bf16 v[72:75], a[88:91], a[52:55], v[72:75]\n"
      "ds_read_b128 a[60:63], v215 offset:5120\n"
      "v_fma_f32 v151, -v151, s94, s94\n"
      "s_waitcnt lgkmcnt(4)\n"
      "v_mfma_f32_16x16x32_bf16 v[80:83], a[64:67], a[8:11], 0\n"
      "ds_read_b128 a[96:99], v198 offset:0\n"
      "v_fma_f32 v152, -v152, s94, s94\n"
      "v_mfma_f32_16x16x32_bf16 v[80:83], a[72:75], a[24:27], v[80:83]\n"
      "ds_read_b128 a[100:103], v198 offset:4096\n"
      "v_fma_f32 v153, -v153, s94, s94\n"
      "v_mfma_f32_16x16x32_bf16 v[80:83], a[80:83], a[40:43], v[80:83]\n"
      "ds_read_b128 a[104:107], v199 offset:0\n"
      "v_fma_f32 v154, -v154, s94, s94\n"
      "v_mfma_f32_16x16x32_bf16 v[80:83], a[88:91], a[56:59], v[80:83]\n"
      "ds_read_b128 a[108:111], v199 offset:4096\n"
      "v_fma_f32 v155, -v155, s94, s94\n"
      "s_waitcnt lgkmcnt(4)\n"
      "v_mfma_f32_16x16x32_bf16 v[88:91], a[64:67], a[12:15], 0\n"
      "ds_read_b128 a[112:115], v198 offset:1024\n"
      "v_fma_f32 v156, -v156, s94, s94\n"
      "v_mfma_f32_16x16x32_bf16 v[88:91], a[72:75], a[28:31], v[88:91]\n"
      "ds_read_b128 a[116:119], v198 offset:5120\n"
      "v_fma_f32 v157, -v157, s94, s94\n"
      "v_mfma_f32_16x16x32_bf16 v[88:91], a[80:83], a[44:47], v[88:91]\n"
      "ds_read_b128 a[120:123], v199 offset:1024\n"
      "v_fma_f32 v158, -v158, s94, s94\n"
      "v_mfma_f32_16x16x32_bf16 v[88:91], a[88:91], a[60:63], v[88:91]\n"
      "ds_read_b128 a[124:127], v199 offset:5120\n"
      "v_fma_f32 v159, -v159, s94, s94\n"
      "s_mov_b32 m0, s62\n"
      "s_nop 0\n"
      "buffer_load_dwordx4 v228, s[56:59], s60 offen nt lds\n"
      "buffer_load_dwordx4 v229, s[56:59], s60 offen offset:1024 nt lds\n"
      "buffer_load_dwordx4 v230, s[56:59], s60 offen offset:2048 nt lds\n"
      "buffer_load_dwordx4 v231, s[56:59], s60 offen offset:3072 nt lds\n"
      "s_waitcnt lgkmcnt(0)\n"
      "v_mfma_f32_16x16x32_bf16 v[96:99], a[96:99], a[0:3], v[144:147]\n"
      "s_add_u32 s40, s40, 0x7f4a7c15\n"
      "v_mfma_f32_16x16x32_bf16 v[96:99], a[104:107], a[16:19], v[96:99]\n"
      "s_lshl_b32 s69, s44, 3\n"
      "v_max3_f32 v188, v64, v65, v66\n"
      "v_max3_f32 v189, v72, v73, v74\n"
      "v_mfma_f32_16x16x32_bf16 v[96:99], a[112:115], a[32:35], v[96:99]\n"
      "s_lshr_b32 s74, s50, 12\n"
      "v_max3_f32 v190, v80, v81, v82\n"
      "v_max3_f32 v191, v88, v89, v90\n"
      "v_mfma_f32_16x16x32_bf16 v[96:99], a[120:123], a[48:51], v[96:99]\n"
      "s_add_u32 s69, s69, s74\n"
      "v_max_f32 v188, v188, v67\n"
      "v_max_f32 v189, v189, v75\n"
      "v_mfma_f32_16x16x32_bf16 v[104:107], a[96:99], a[4:7], v[152:155]\n"
      "s_mul_i32 s69, s69, 4608\n"
      "v_max_f32 v190, v190, v83\n"
      "v_max_f32 v191, v191, v91\n"
      "v_mfma_f32_16x16x32_bf16 v[104:107], a[104:107], a[20:23], v[104:107]\n"
      "s_add_u32 s69, s69, 81920\n"
      "v_mov_b32 v192, v188\n"
      "v_mov_b32 v193, v189\n"
      "v_mfma_f32_16x16x32_bf16 v[104:107], a[112:115], a[36:39], v[104:107]\n"
      "s_add_u32 s24, s18, s69\n"
      "v_mov_b32 v194, v190\n"
      "v_mov_b32 v195, v191\n"
      "v_mfma_f32_16x16x32_bf16 v[104:107], a[120:123], a[52:55], v[104:107]\n"
      "s_addc_u32 s25, s19, 0\n"
      "v_permlane16_swap_b32 v188, v192\n"
      "v_permlane16_swap_b32 v189, v193\n"
      "v_mfma_f32_16x16x32_bf16 v[112:115], a[96:99], a[8:11], v[144:147]\n"
      "s_movk_i32 s26, 23040\n"
      "v_permlane16_swap_b32 v190, v194\n"
      "v_permlane16_swap_b32 v191, v195\n"
      "v_mfma_f32_16x16x32_bf16 v[112:115], a[104:107], a[24:27], v[112:115]\n"
      "s_mul_i32 s78, s43, 18432\n"
      "v_max_f32 v188, v188, v192\n"
      "v_max_f32 v189, v189, v193\n"
      "v_mfma_f32_16x16x32_bf16 v[112:115], a[112:115], a[40:43], v[112:115]\n"
      "s_and_b32 s69, s50, 4096\n"
      "v_max_f32 v190, v190, v194\n"
      "v_max_f32 v191, v191, v195\n"
      "v_mfma_f32_16x16x32_bf16 v[112:115], a[120:123], a[56:59], v[112:115]\n"
      "s_lshl_b32 s69, s69, 2\n"
      "v_mov_b32 v192, v188\n"
      "v_mov_b32 v193, v189\n"
      "v_mfma_f32_16x16x32_bf16 v[120:123], a[96:99], a[12:15], v[152:155]\n"
      "s_and_b32 s74, s50, 8192\n"
      "v_mov_b32 v194, v190\n"
      "v_mov_b32 v195, v191\n"
      "v_mfma_f32_16x16x32_bf16 v[120:123], a[104:107], a[28:31], v[120:123]\n"
      "s_add_u32 s74, s74, 4096\n"
      "v_permlane32_swap_b32 v188, v192\n"
      "v_permlane32_swap_b32 v189, v193\n"
      "v_mfma_f32_16x16x32_bf16 v[120:123], a[112:115], a[44:47], v[120:123]\n"
      "s_add_u32 s69, s69, s74\n"
      "v_permlane32_swap_b32 v190, v194\n"
      "v_permlane32_swap_b32 v191, v195\n"
      "v_mfma_f32_16x16x32_bf16 v[120:123], a[120:123], a[60:63], v[120:123]\n"
      "s_lshl_b32 s74, s43, 7\n"
      "v_max_f32 v188, v188, v192\n"
      "v_max_f32 v189, v189, v193\n"
      "v_mfma_f32_16x16x32_bf16 v[100:103], a[100:103], a[0:3], v[148:151]\n"
      "s_add_u32 s69, s69, s74\n"
      "v_max_f32 v190, v190, v194\n"
      "v_max_f32 v191, v191, v195\n"
      "v_mfma_f32_16x16x32_bf16 v[100:103], a[108:111], a[16:19], v[100:103]\n"
      "s_lshl_b32 s74, s44, 15\n"
      "v_xor_b32 v176, 0x80000000, v188\n"
      "v_xor_b32 v177, 0x80000000, v189\n"
      "v_mfma_f32_16x16x32_bf16 v[100:103], a[116:119], a[32:35], v[100:103]\n"
      "s_add_u32 s69, s69, s74\n"
      "v_xor_b32 v178, 0x80000000, v190\n"
      "v_xor_b32 v179, 0x80000000, v191\n"
      "v_mfma_f32_16x16x32_bf16 v[100:103], a[124:127], a[48:51], v[100:103]\n"
      "s_add_u32 s30, s16, s69\n"
      "v_mov_b32 v160, v176\n"
      "v_mov_b32 v161, v176\n"
      "v_mfma_f32_16x16x32_bf16 v[108:111], a[100:103], a[4:7], v[156:159]\n"
      "s_addc_u32 s31, s17, 0\n"
      "v_mov_b32 v162, v176\n"
      "v_mov_b32 v163, v176\n"
      "v_mfma_f32_16x16x32_bf16 v[108:111], a[108:111], a[20:23], v[108:111]\n"
      "v_mov_b32 v164, v177\n"
      "v_mov_b32 v165, v177\n"
      "v_mfma_f32_16x16x32_bf16 v[108:111], a[116:119], a[36:39], v[108:111]\n"
      "v_mov_b32 v166, v177\n"
      "v_mov_b32 v167, v177\n"
      "v_mfma_f32_16x16x32_bf16 v[108:111], a[124:127], a[52:55], v[108:111]\n"
      "v_mov_b32 v168, v178\n"
      "v_mov_b32 v169, v178\n"
      "v_mfma_f32_16x16x32_bf16 v[116:119], a[100:103], a[8:11], v[148:151]\n"
      "v_mov_b32 v170, v178\n"
      "v_mov_b32 v171, v178\n"
      "v_mfma_f32_16x16x32_bf16 v[116:119], a[108:111], a[24:27], v[116:119]\n"
      "v_mov_b32 v172, v179\n"
      "v_mov_b32 v173, v179\n"
      "v_mfma_f32_16x16x32_bf16 v[116:119], a[116:119], a[40:43], v[116:119]\n"
      "v_mov_b32 v174, v179\n"
      "v_mov_b32 v175, v179\n"
      "v_mfma_f32_16x16x32_bf16 v[116:119], a[124:127], a[56:59], v[116:119]\n"
      "v_mov_b32 v209, 139248\n"
      "v_lshl_add_u32 v232, s50, 1, v241\n"
      "v_mfma_f32_16x16x32_bf16 v[124:127], a[100:103], a[12:15], v[156:159]\n"
      "v_add_u32 v232, 0x10000, v232\n"
      "v_xor_b32 v233, 0x4000, v232\n"
      "v_mfma_f32_16x16x32_bf16 v[124:127], a[108:111], a[28:31], v[124:127]\n"
      "v_add_u32 v234, 0x10000, v232\n"
      "v_xor_b32 v235, 0x4000, v234\n"
      "v_mfma_f32_16x16x32_bf16 v[124:127], a[116:119], a[44:47], v[124:127]\n"
      "v_lshrrev_b32 v240, 1, v241\n"
      "v_add_u32 v240, 0x1000, v240\n"
      "v_mfma_f32_16x16x32_bf16 v[124:127], a[124:127], a[60:63], v[124:127]\n"
      "v_mov_b32 v196, 0\n"
      "v_pk_add_f32 v[96:97], v[96:97], v[160:161]\n"
      "v_pk_add_f32 v[98:99], v[98:99], v[160:161]\n"
      "v_pk_add_f32 v[104:105], v[104:105], v[164:165]\n"
      "v_pk_add_f32 v[106:107], v[106:107], v[164:165]\n"
      "v_pk_add_f32 v[112:113], v[112:113], v[168:169]\n"
      "v_pk_add_f32 v[114:115], v[114:115], v[168:169]\n"
      "v_pk_add_f32 v[120:121], v[120:121], v[172:173]\n"
      "v_pk_add_f32 v[122:123], v[122:123], v[172:173]\n"
      "v_pk_add_f32 v[100:101], v[100:101], v[160:161]\n"
      "v_pk_add_f32 v[102:103], v[102:103], v[160:161]\n"
      "v_pk_add_f32 v[108:109], v[108:109], v[164:165]\n"
      "v_pk_add_f32 v[110:111], v[110:111], v[164:165]\n"
      "v_pk_add_f32 v[116:117], v[116:117], v[168:169]\n"
      "v_pk_add_f32 v[118:119], v[118:119], v[168:169]\n"
      "v_pk_add_f32 v[124:125], v[124:125], v[172:173]\n"
      "v_pk_add_f32 v[126:127], v[126:127], v[172:173]\n"
      "v_mov_b32 v224, v244\n"
      "v_mov_b32 v225, v245\n"
      "v_mov_b32 v226, v246\n"
      "v_mov_b32 v227, v247\n"
      "v_xor_b32 v220, 0x2000, v244\n"
      "v_xor_b32 v221, 0x2000, v245\n"
      "v_xor_b32 v222, 0x2000, v246\n"
      "v_xor_b32 v223, 0x2000, v247\n"
      "s_lshl_b32 s76, s43, 3\n"
      "v_mov_b32 v237, s40\n"
      "v_mov_b32 v238, s76\n"
      "v_xor_b32 v239, 8, v238\n"
      "s_memrealtime s[68:69]\n"
      "s_waitcnt vmcnt(14) lgkmcnt(0)\n"
      "s_sub_u32 s67, s68, s88\n"
      "s_cmp_lt_i32 s67, -8\n"
      "s_cbranch_scc1 GW0_%=\n"
      "GX0_%=:\n"
      "s_barrier\n"
      "ds_read_b128 a[64:67], v242 offset:0\n"
      "ds_read_b128 a[68:71], v242 offset:4096\n"
      "ds_read_b128 a[72:75], v243 offset:0\n"
      "ds_read_b128 a[76:79], v243 offset:4096\n"
      "ds_read_b128 a[80:83], v242 offset:1024\n"
      "ds_read_b128 a[84:87], v242 offset:5120\n"
      "ds_read_b128 a[88:91], v243 offset:1024\n"
      "ds_read_b128 a[92:95], v243 offset:5120\n"
      "ds_write_b32 v209, v196\n"
      "s_add_u32 s38, s38, s38\n"
      "s_memrealtime s[68:69]\n"
      "s_xor_b64 s[56:57], s[56:57], s[32:33]\n"
      "s_add_u32 s61, s61, 1\n"
      "s_add_u32 s62, s62, 0x4000\n"
      "s_and_b32 s62, s62, 0x1ffff\n"
      "v_readlane_b32 s60, v219, s61\n"
      "s_waitcnt lgkmcnt(0)\n"
      "s_sub_u32 s67, s68, s66\n"
      "s_cmp_gt_i32 s67, s70\n"
      "s_cselect_b32 s69, s77, s86\n"
      "s_cselect_b32 s70, 60, s70\n"
      "s_cselect_b32 s90, s80, s90\n"
      "s_add_u32 s66, s66, s69\n"
      "s_mov_b32 s82, s68\n"
      "LOOP_%=:\n"
      "v_mfma_f32_16x16x32_bf16 v[64:67], a[64:67], a[0:3], v[160:163]\n"
      "s_add_u32 s65, s68, s90\n"
      "v_exp_f32 v188, v96\n"
      "v_mfma_f32_16x16x32_bf16 v[64:67], a[72:75], a[16:19], v[64:67]\n"
      "s_max_u32 s88, s66, s65\n"
      "v_add_f32 v180, v180, v184\n"
      "v_add_f32 v181, v181, v185\n"
      "v_mfma_f32_16x16x32_bf16 v[64:67], a[80:83], a[32:35], v[64:67]\n"
      "v_exp_f32 v189, v97\n"
      "v_mfma_f32_16x16x32_bf16 v[64:67], a[88:91], a[48:51], v[64:67]\n"
      "s_getreg_b32 s67, hwreg(HW_REG_IB_STS, 0, 4)\n"
      "v_add_f32 v182, v182, v186\n"
      "v_add_f32 v183, v183, v187\n"
      "v_mfma_f32_16x16x32_bf16 v[72:75], a[64:67], a[4:7], v[164:167]\n"
      "s_cmp_gt_u32 s67, 4\n"
      "v_exp_f32 v190, v98\n"
      "v_mfma_f32_16x16x32_bf16 v[72:75], a[72:75], a[20:23], v[72:75]\n"
      "s_addc_u32 s89, s89, 0\n"
      "v_exp_f32 v191, v99\n"
      "v_mfma_f32_16x16x32_bf16 v[72:75], a[80:83], a[36:39], v[72:75]\n"
      "v_cvt_pk_bf16_f32 v144, v188, v189\n"
      "v_cvt_pk_bf16_f32 v145, v190, v191\n"
      "v_mfma_f32_16x16x32_bf16 v[72:75], a[88:91], a[52:55], v[72:75]\n"
      "v_exp_f32 v192, v100\n"
      "v_mfma_f32_16x16x32_bf16 v[80:83], a[64:67], a[8:11], v[168:171]\n"
      "s_add_u32 s39, s39, s39\n"
      "v_exp_f32 v193, v101\n"
      "v_mfma_f32_16x16x32_bf16 v[80:83], a[72:75], a[24:27], v[80:83]\n"
      "s_memrealtime s[68:69]\n"
      "v_exp_f32 v194, v102\n"
      "v_mfma_f32_16x16x32_bf16 v[80:83], a[80:83], a[40:43], v[80:83]\n"
      "v_exp_f32 v195, v103\n"
      "v_mfma_f32_16x16x32_bf16 v[80:83], a[88:91], a[56:59], v[80:83]\n"
      "s_mov_b32 m0, s62\n"
      "v_cvt_pk_bf16_f32 v146, v192, v193\n"
      "v_cvt_pk_bf16_f32 v147, v194, v195\n"
      "v_mfma_f32_16x16x32_bf16 v[88:91], a[64:67], a[12:15], v[172:175]\n"
      "buffer_load_dwordx4 v228, s[56:59], s60 offen nt lds\n"
      "v_mfma_f32_16x16x32_bf16 v[184:187], a[128:131], v[144:147], 0\n"
      "buffer_load_dwordx4 v229, s[56:59], s60 offen offset:1024 nt lds\n"
      "v_mfma_f32_16x16x32_bf16 v[88:91], a[72:75], a[28:31], v[88:91]\n"
      "buffer_load_dwordx4 v230, s[56:59], s60 offen offset:2048 nt lds\n"
      "v_mfma_f32_16x16x32_bf16 v[88:91], a[80:83], a[44:47], v[88:91]\n"
      "buffer_load_dwordx4 v231, s[56:59], s60 offen offset:3072 nt lds\n"
      "s_cbranch_scc1 SP1_%=\n"
      "HD1_%=:\n"
      "s_cmp_lg_u32 s54, 0\n"
      "s_waitcnt vmcnt(12) lgkmcnt(0)\n"
      "s_cbranch_scc1 W1_%=\n"
      "s_waitcnt vmcnt(8)\n"
      "W1_%=:\n"
      "s_sub_u32 s67, s68, s88\n"
      "s_cmp_lt_i32 s67, -8\n"
      "s_cbranch_scc1 GW4_%=\n"
      "GX4_%=:\n"
      "s_barrier\n"
      "v_mfma_f32_16x16x32_bf16 v[88:91], a[88:91], a[60:63], v[88:91]\n"
      "s_memrealtime s[68:69]\n"
      "ds_read_b64_tr_b16 a[96:97], v244 offset:0\n"
      "ds_read_b64_tr_b16 a[98:99], v244 offset:4096\n"
      "v_mfma_f32_16x16x32_bf16 v[68:71], a[68:71], a[0:3], v[160:163]\n"
      "s_xor_b64 s[56:57], s[56:57], s[32:33]\n"
      "ds_read_b64_tr_b16 a[100:101], v245 offset:0\n"
      "ds_read_b64_tr_b16 a[102:103], v245 offset:4096\n"
      "v_mfma_f32_16x16x32_bf16 v[68:71], a[76:79], a[16:19], v[68:71]\n"
      "s_add_u32 s61, s61, 1\n"
      "ds_read_b64_tr_b16 a[104:105], v246 offset:0\n"
      "ds_read_b64_tr_b16 a[106:107], v246 offset:4096\n"
      "v_mfma_f32_16x16x32_bf16 v[68:71], a[84:87], a[32:35], v[68:71]\n"
      "s_add_u32 s62, s62, 0x4000\n"
      "ds_read_b64_tr_b16 a[108:109], v247 offset:0\n"
      "ds_read_b64_tr_b16 a[110:111], v247 offset:4096\n"
      "v_mfma_f32_16x16x32_bf16 v[68:71], a[92:95], a[48:51], v[68:71]\n"
      "s_and_b32 s62, s62, 0x1ffff\n"
      "v_exp_f32 v188, v104\n"
      "v_mfma_f32_16x16x32_bf16 v[76:79], a[68:71], a[4:7], v[164:167]\n"
      "v_readlane_b32 s60, v219, s61\n"
      "v_exp_f32 v189, v105\n"
      "v_mfma_f32_16x16x32_bf16 v[76:79], a[76:79], a[20:23], v[76:79]\n"
      "v_exp_f32 v190, v106\n"
      "v_mfma_f32_16x16x32_bf16 v[76:79], a[84:87], a[36:39], v[76:79]\n"
      "v_exp_f32 v191, v107\n"
      "v_mfma_f32_16x16x32_bf16 v[76:79], a[92:95], a[52:55], v[76:79]\n"
      "v_cvt_pk_bf16_f32 v148, v188, v189\n"
      "v_cvt_pk_bf16_f32 v149, v190, v191\n"
      "v_mfma_f32_16x16x32_bf16 v[84:87], a[68:71], a[8:11], v[168:171]\n"
      "v_exp_f32 v192, v108\n"
      "v_mfma_f32_16x16x32_bf16 v[84:87], a[76:79], a[24:27], v[84:87]\n"
      "v_exp_f32 v193, v109\n"
      "s_waitcnt lgkmcnt(0)\n"
      "v_mfma_f32_16x16x32_bf16 v[84:87], a[84:87], a[40:43], v[84:87]\n"
      "s_sub_u32 s67, s68, s66\n"
      "ds_read_b64_tr_b16 a[112:113], v244 offset:1024\n"
      "v_cvt_pk_bf16_f32 v150, v192, v193\n"
      "v_mfma_f32_16x16x32_bf16 v[84:87], a[92:95], a[56:59], v[84:87]\n"
      "s_cmp_gt_i32 s67, s70\n"
      "v_exp_f32 v194, v110\n"
      "v_mfma_f32_16x16x32_bf16 v[92:95], a[68:71], a[12:15], v[172:175]\n"
      "s_cselect_b32 s69, s77, s86\n"
      "ds_read_b64_tr_b16 a[114:115], v244 offset:5120\n"
      "ds_read_b64_tr_b16 a[116:117], v245 offset:1024\n"
      "v_mfma_f32_16x16x32_bf16 v[92:95], a[76:79], a[28:31], v[92:95]\n"
      "s_cselect_b32 s70, 60, s70\n"
      "v_exp_f32 v195, v111\n"
      "v_mfma_f32_16x16x32_bf16 v[92:95], a[84:87], a[44:47], v[92:95]\n"
      "s_cselect_b32 s90, s80, s90\n"
      "ds_read_b64_tr_b16 a[118:119], v245 offset:5120\n"
      "v_cvt_pk_bf16_f32 v151, v194, v195\n"
      "v_mfma_f32_16x16x32_bf16 v[92:95], a[92:95], a[60:63], v[92:95]\n"
      "s_add_u32 s66, s66, s69\n"
      "v_exp_f32 v188, v112\n"
      "v_mfma_f32_16x16x32_bf16 v[184:187], a[132:135], v[148:151], v[184:187]\n"
      "s_mov_b32 s82, s68\n"
      "ds_read_b64_tr_b16 a[120:121], v246 offset:1024\n"
      "ds_read_b64_tr_b16 a[122:123], v246 offset:5120\n"
      "v_mfma_f32_16x16x32_bf16 v[0:3], a[96:99], v[128:131], v[0:3]\n"
      "s_add_u32 s65, s68, s90\n"
      "v_exp_f32 v189, v113\n"
      "v_mfma_f32_16x16x32_bf16 v[4:7], a[96:99], v[132:135], v[4:7]\n"
      "s_max_u32 s88, s66, s65\n"
      "ds_read_b64_tr_b16 a[124:125], v247 offset:1024\n"
      "v_cvt_pk_bf16_f32 v152, v188, v189\n"
      "v_mfma_f32_16x16x32_bf16 a[144:147], a[96:99], v[136:139], a[144:147]\n"
      "v_exp_f32 v190, v114\n"
      "v_mfma_f32_16x16x32_bf16 a[148:151], a[96:99], v[140:143], a[148:151]\n"
      "s_getreg_b32 s67, hwreg(HW_REG_IB_STS, 0, 4)\n"
      "v_exp_f32 v191, v115\n"
      "v_mfma_f32_16x16x32_bf16 a[156:159], a[100:103], v[140:143], a[156:159]\n"
      "s_cmp_gt_u32 s67, 4\n"
      "ds_read_b64_tr_b16 a[126:127], v247 offset:5120\n"
      "v_cvt_pk_bf16_f32 v153, v190, v191\n"
      "v_mfma_f32_16x16x32_bf16 a[152:155], a[100:103], v[136:139], a[152:155]\n"
      "s_addc_u32 s89, s89, 0\n"
      "v_exp_f32 v192, v116\n"
      "v_mfma_f32_16x16x32_bf16 v[12:15], a[100:103], v[132:135], v[12:15]\n"
      "v_exp_f32 v193, v117\n"
      "v_mfma_f32_16x16x32_bf16 v[8:11], a[100:103], v[128:131], v[8:11]\n"
      "v_exp_f32 v194, v118\n"
      "v_mfma_f32_16x16x32_bf16 v[16:19], a[104:107], v[128:131], v[16:19]\n"
      "s_add_u32 s38, s38, s38\n"
      "v_exp_f32 v195, v119\n"
      "v_mfma_f32_16x16x32_bf16 v[20:23], a[104:107], v[132:135], v[20:23]\n"
      "s_memrealtime s[68:69]\n"
      "v_cvt_pk_bf16_f32 v154, v192, v193\n"
      "v_cvt_pk_bf16_f32 v155, v194, v195\n"
      "v_mfma_f32_16x16x32_bf16 a[160:163], a[104:107], v[136:139], a[160:163]\n"
      "v_exp_f32 v188, v120\n"
      "v_mfma_f32_16x16x32_bf16 v[184:187], a[136:139], v[152:155], v[184:187]\n"
      "s_mov_b32 m0, s62\n"
      "v_exp_f32 v189, v121\n"
      "v_mfma_f32_16x16x32_bf16 a[164:167], a[104:107], v[140:143], a[164:167]\n"
      "buffer_load_dwordx4 v228, s[56:59], s60 offen nt lds\n"
      "v_mfma_f32_16x16x32_bf16 a[172:175], a[108:111], v[140:143], a[172:175]\n"
      "buffer_load_dwordx4 v229, s[56:59], s60 offen offset:1024 nt lds\n"
      "v_mfma_f32_16x16x32_bf16 a[168:171], a[108:111], v[136:139], a[168:171]\n"
      "buffer_load_dwordx4 v230, s[56:59], s60 offen offset:2048 nt lds\n"
      "v_mfma_f32_16x16x32_bf16 v[28:31], a[108:111], v[132:135], v[28:31]\n"
      "buffer_load_dwordx4 v231, s[56:59], s60 offen offset:3072 nt lds\n"
      "s_cbranch_scc1 SP2_%=\n"
      "HD2_%=:\n"
      "s_cmp_lg_u32 s54, 0\n"
      "s_waitcnt vmcnt(12) lgkmcnt(0)\n"
      "s_cbranch_scc1 W2_%=\n"
      "s_waitcnt vmcnt(8)\n"
      "W2_%=:\n"
      "s_sub_u32 s67, s68, s88\n"
      "s_cmp_lt_i32 s67, -8\n"
      "s_cbranch_scc1 GW5_%=\n"
      "GX5_%=:\n"
      "s_barrier\n"
      "v_mfma_f32_16x16x32_bf16 v[24:27], a[108:111], v[128:131], v[24:27]\n"
      "s_memrealtime s[68:69]\n"
      "ds_read_b128 a[64:67], v242 offset:32768\n"
      "ds_read_b128 a[72:75], v243 offset:32768\n"
      "v_mfma_f32_16x16x32_bf16 v[32:35], a[112:115], v[128:131], v[32:35]\n"
      "s_xor_b64 s[56:57], s[56:57], s[32:33]\n"
      "ds_read_b128 a[80:83], v242 offset:33792\n"
      "ds_read_b128 a[88:91], v243 offset:33792\n"
      "v_mfma_f32_16x16x32_bf16 v[36:39], a[112:115], v[132:135], v[36:39]\n"
      "s_add_u32 s61, s61, 1\n"
      "v_exp_f32 v190, v122\n"
      "v_mfma_f32_16x16x32_bf16 a[176:179], a[112:115], v[136:139], a[176:179]\n"
      "s_add_u32 s62, s62, 0x4000\n"
      "v_exp_f32 v191, v123\n"
      "v_mfma_f32_16x16x32_bf16 a[180:183], a[112:115], v[140:143], a[180:183]\n"
      "s_and_b32 s62, s62, 0x1ffff\n"
      "v_cvt_pk_bf16_f32 v156, v188, v189\n"
      "v_cvt_pk_bf16_f32 v157, v190, v191\n"
      "v_mfma_f32_16x16x32_bf16 a[188:191], a[116:119], v[140:143], a[188:191]\n"
      "v_readlane_b32 s60, v219, s61\n"
      "v_exp_f32 v192, v124\n"
      "v_mfma_f32_16x16x32_bf16 a[184:187], a[116:119], v[136:139], a[184:187]\n"
      "v_exp_f32 v193, v125\n"
      "v_mfma_f32_16x16x32_bf16 v[44:47], a[116:119], v[132:135], v[44:47]\n"
      "v_exp_f32 v194, v126\n"
      "v_mfma_f32_16x16x32_bf16 v[40:43], a[116:119], v[128:131], v[40:43]\n"
      "v_exp_f32 v195, v127\n"
      "v_mfma_f32_16x16x32_bf16 v[48:51], a[120:123], v[128:131], v[48:51]\n"
      "v_cvt_pk_bf16_f32 v158, v192, v193\n"
      "v_cvt_pk_bf16_f32 v159, v194, v195\n"
      "v_mfma_f32_16x16x32_bf16 v[52:55], a[120:123], v[132:135], v[52:55]\n"
      "s_waitcnt lgkmcnt(0)\n"
      "v_mfma_f32_16x16x32_bf16 v[184:187], a[140:143], v[156:159], v[184:187]\n"
      "s_sub_u32 s67, s68, s66\n"
      "ds_read_b128 a[68:71], v242 offset:36864\n"
      "ds_read_b128 a[76:79], v243 offset:36864\n"
      "v_mfma_f32_16x16x32_bf16 a[192:195], a[120:123], v[136:139], a[192:195]\n"
      "s_cmp_gt_i32 s67, s70\n"
      "ds_read_b128 a[84:87], v242 offset:37888\n"
      "ds_read_b128 a[92:95], v243 offset:37888\n"
      "v_mfma_f32_16x16x32_bf16 a[196:199], a[120:123], v[140:143], a[196:199]\n"
      "s_cselect_b32 s69, s77, s86\n"
      "v_max3_f32 v196, v184, v185, v186\n"
      "v_max_f32 v196, v196, v187\n"
      "v_mfma_f32_16x16x32_bf16 a[204:207], a[124:127], v[140:143], a[204:207]\n"
      "s_cselect_b32 s70, 60, s70\n"
      "v_cmp_nge_f32 vcc, 0x4b800000, v196\n"
      "v_mfma_f32_16x16x32_bf16 a[200:203], a[124:127], v[136:139], a[200:203]\n"
      "s_cselect_b32 s90, s80, s90\n"
      "v_mfma_f32_16x16x32_bf16 v[60:63], a[124:127], v[132:135], v[60:63]\n"
      "s_add_u32 s66, s66, s69\n"
      "v_mfma_f32_16x16x32_bf16 v[56:59], a[124:127], v[128:131], v[56:59]\n"
      "s_mov_b32 s82, s68\n"
      "s_cbranch_vccnz FS1_%=\n"
      "FR1_%=:\n"
      "v_mfma_f32_16x16x32_bf16 v[96:99], a[64:67], a[0:3], v[160:163]\n"
      "s_add_u32 s65, s68, s90\n"
      "v_exp_f32 v188, v64\n"
      "v_mfma_f32_16x16x32_bf16 v[96:99], a[72:75], a[16:19], v[96:99]\n"
      "s_max_u32 s88, s66, s65\n"
      "v_add_f32 v180, v180, v184\n"
      "v_add_f32 v181, v181, v185\n"
      "v_mfma_f32_16x16x32_bf16 v[96:99], a[80:83], a[32:35], v[96:99]\n"
      "v_exp_f32 v189, v65\n"
      "v_mfma_f32_16x16x32_bf16 v[96:99], a[88:91], a[48:51], v[96:99]\n"
      "s_getreg_b32 s67, hwreg(HW_REG_IB_STS, 0, 4)\n"
      "v_add_f32 v182, v182, v186\n"
      "v_add_f32 v183, v183, v187\n"
      "v_mfma_f32_16x16x32_bf16 v[104:107], a[64:67], a[4:7], v[164:167]\n"
      "s_cmp_gt_u32 s67, 4\n"
      "v_exp_f32 v190, v66\n"
      "v_mfma_f32_16x16x32_bf16 v[104:107], a[72:75], a[20:23], v[104:107]\n"
      "s_addc_u32 s89, s89, 0\n"
      "v_exp_f32 v191, v67\n"
      "v_mfma_f32_16x16x32_bf16 v[104:107], a[80:83], a[36:39], v[104:107]\n"
      "v_cvt_pk_bf16_f32 v128, v188, v189\n"
      "v_cvt_pk_bf16_f32 v129, v190, v191\n"
      "v_mfma_f32_16x16x32_bf16 v[104:107], a[88:91], a[52:55], v[104:107]\n"
      "v_exp_f32 v192, v68\n"
      "v_mfma_f32_16x16x32_bf16 v[112:115], a[64:67], a[8:11], v[168:171]\n"
      "s_add_u32 s39, s39, s39\n"
      "v_exp_f32 v193, v69\n"
      "v_mfma_f32_16x16x32_bf16 v[112:115], a[72:75], a[24:27], v[112:115]\n"
      "s_memrealtime s[68:69]\n"
      "v_exp_f32 v194, v70\n"
      "v_mfma_f32_16x16x32_bf16 v[112:115], a[80:83], a[40:43], v[112:115]\n"
      "v_exp_f32 v195, v71\n"
      "v_mfma_f32_16x16x32_bf16 v[112:115], a[88:91], a[56:59], v[112:115]\n"
      "s_mov_b32 m0, s62\n"
      "v_cvt_pk_bf16_f32 v130, v192, v193\n"
      "v_cvt_pk_bf16_f32 v131, v194, v195\n"
      "v_mfma_f32_16x16x32_bf16 v[120:123], a[64:67], a[12:15], v[172:175]\n"
      "buffer_load_dwordx4 v228, s[56:59], s60 offen nt lds\n"
      "v_mfma_f32_16x16x32_bf16 v[184:187], a[128:131], v[128:131], 0\n"
      "buffer_load_dwordx4 v229, s[56:59], s60 offen offset:1024 nt lds\n"
      "v_mfma_f32_16x16x32_bf16 v[120:123], a[72:75], a[28:31], v[120:123]\n"
      "buffer_load_dwordx4 v230, s[56:59], s60 offen offset:2048 nt lds\n"
      "v_mfma_f32_16x16x32_bf16 v[120:123], a[80:83], a[44:47], v[120:123]\n"
      "buffer_load_dwordx4 v231, s[56:59], s60 offen offset:3072 nt lds\n"
      "s_cbranch_scc1 SP3_%=\n"
      "HD3_%=:\n"
      "s_cmp_lg_u32 s54, 0\n"
      "s_waitcnt vmcnt(12) lgkmcnt(0)\n"
      "s_cbranch_scc1 W3_%=\n"
      "s_waitcnt vmcnt(8)\n"
      "W3_%=:\n"
      "s_sub_u32 s67, s68, s88\n"
      "s_cmp_lt_i32 s67, -8\n"
      "s_cbranch_scc1 GW6_%=\n"
      "GX6_%=:\n"
      "s_barrier\n"
      "v_mfma_f32_16x16x32_bf16 v[120:123], a[88:91], a[60:63], v[120:123]\n"
      "s_memrealtime s[68:69]\n"
      "ds_read_b64_tr_b16 a[96:97], v244 offset:32768\n"
      "ds_read_b64_tr_b16 a[98:99], v244 offset:36864\n"
      "v_mfma_f32_16x16x32_bf16 v[100:103], a[68:71], a[0:3], v[160:163]\n"
      "s_xor_b64 s[56:57], s[56:57], s[32:33]\n"
      "ds_read_b64_tr_b16 a[100:101], v245 offset:32768\n"
      "ds_read_b64_tr_b16 a[102:103], v245 offset:36864\n"
      "v_mfma_f32_16x16x32_bf16 v[100:103], a[76:79], a[16:19], v[100:103]\n"
      "s_add_u32 s61, s61, 1\n"
      "ds_read_b64_tr_b16 a[104:105], v246 offset:32768\n"
      "ds_read_b64_tr_b16 a[106:107], v246 offset:36864\n"
      "v_mfma_f32_16x16x32_bf16 v[100:103], a[84:87], a[32:35], v[100:103]\n"
      "s_add_u32 s62, s62, 0x4000\n"
      "ds_read_b64_tr_b16 a[108:109], v247 offset:32768\n"
      "ds_read_b64_tr_b16 a[110:111], v247 offset:36864\n"
      "v_mfma_f32_16x16x32_bf16 v[100:103], a[92:95], a[48:51], v[100:103]\n"
      "s_and_b32 s62, s62, 0x1ffff\n"
      "v_exp_f32 v188, v72\n"
      "v_mfma_f32_16x16x32_bf16 v[108:111], a[68:71], a[4:7], v[164:167]\n"
      "v_readlane_b32 s60, v219, s61\n"
      "v_exp_f32 v189, v73\n"
      "v_mfma_f32_16x16x32_bf16 v[108:111], a[76:79], a[20:23], v[108:111]\n"
      "v_xor_b32 v242, 0x10000, v242\n"
      "v_xor_b32 v243, 0x10000, v243\n"
      "v_mfma_f32_16x16x32_bf16 v[108:111], a[84:87], a[36:39], v[108:111]\n"
      "v_exp_f32 v190, v74\n"
      "v_mfma_f32_16x16x32_bf16 v[108:111], a[92:95], a[52:55], v[108:111]\n"
      "v_exp_f32 v191, v75\n"
      "v_mfma_f32_16x16x32_bf16 v[116:119], a[68:71], a[8:11], v[168:171]\n"
      "v_cvt_pk_bf16_f32 v132, v188, v189\n"
      "v_cvt_pk_bf16_f32 v133, v190, v191\n"
      "v_mfma_f32_16x16x32_bf16 v[116:119], a[76:79], a[24:27], v[116:119]\n"
      "v_exp_f32 v192, v76\n"
      "s_waitcnt lgkmcnt(0)\n"
      "v_mfma_f32_16x16x32_bf16 v[116:119], a[84:87], a[40:43], v[116:119]\n"
      "s_sub_u32 s67, s68, s66\n"
      "ds_read_b64_tr_b16 a[112:113], v244 offset:33792\n"
      "ds_read_b64_tr_b16 a[114:115], v244 offset:37888\n"
      "v_mfma_f32_16x16x32_bf16 v[116:119], a[92:95], a[56:59], v[116:119]\n"
      "s_cmp_gt_i32 s67, s70\n"
      "v_exp_f32 v193, v77\n"
      "v_mfma_f32_16x16x32_bf16 v[124:127], a[68:71], a[12:15], v[172:175]\n"
      "s_cselect_b32 s69, s77, s86\n"
      "ds_read_b64_tr_b16 a[116:117], v245 offset:33792\n"
      "v_cvt_pk_bf16_f32 v134, v192, v193\n"
      "v_mfma_f32_16x16x32_bf16 v[124:127], a[76:79], a[28:31], v[124:127]\n"
      "s_cselect_b32 s70, 60, s70\n"
      "v_exp_f32 v194, v78\n"
      "v_mfma_f32_16x16x32_bf16 v[124:127], a[84:87], a[44:47], v[124:127]\n"
      "s_cselect_b32 s90, s80, s90\n"
      "ds_read_b64_tr_b16 a[118:119], v245 offset:37888\n"
      "ds_read_b64_tr_b16 a[120:121], v246 offset:33792\n"
      "v_mfma_f32_16x16x32_bf16 v[124:127], a[92:95], a[60:63], v[124:127]\n"
      "s_add_u32 s66, s66, s69\n"
      "v_exp_f32 v195, v79\n"
      "v_mfma_f32_16x16x32_bf16 v[0:3], a[96:99], v[144:147], v[0:3]\n"
      "s_mov_b32 s82, s68\n"
      "ds_read_b64_tr_b16 a[122:123], v246 offset:37888\n"
      "v_cvt_pk_bf16_f32 v135, v194, v195\n"
      "v_mfma_f32_16x16x32_bf16 v[4:7], a[96:99], v[148:151], v[4:7]\n"
      "s_add_u32 s65, s68, s90\n"
      "v_exp_f32 v188, v80\n"
      "v_mfma_f32_16x16x32_bf16 v[184:187], a[132:135], v[132:135], v[184:187]\n"
      "s_max_u32 s88, s66, s65\n"
      "ds_read_b64_tr_b16 a[124:125], v247 offset:33792\n"
      "ds_read_b64_tr_b16 a[126:127], v247 offset:37888\n"
      "v_mfma_f32_16x16x32_bf16 a[144:147], a[96:99], v[152:155], a[144:147]\n"
      "v_exp_f32 v189, v81\n"
      "v_mfma_f32_16x16x32_bf16 a[148:151], a[96:99], v[156:159], a[148:151]\n"
      "s_getreg_b32 s67, hwreg(HW_REG_IB_STS, 0, 4)\n"
      "v_exp_f32 v190, v82\n"
      "v_mfma_f32_16x16x32_bf16 a[156:159], a[100:103], v[156:159], a[156:159]\n"
      "s_cmp_gt_u32 s67, 4\n"
      "v_exp_f32 v191, v83\n"
      "v_mfma_f32_16x16x32_bf16 a[152:155], a[100:103], v[152:155], a[152:155]\n"
      "s_addc_u32 s89, s89, 0\n"
      "v_cvt_pk_bf16_f32 v136, v188, v189\n"
      "v_cvt_pk_bf16_f32 v137, v190, v191\n"
      "v_mfma_f32_16x16x32_bf16 v[12:15], a[100:103], v[148:151], v[12:15]\n"
      "v_exp_f32 v192, v84\n"
      "v_mfma_f32_16x16x32_bf16 v[8:11], a[100:103], v[144:147], v[8:11]\n"
      "v_exp_f32 v193, v85\n"
      "v_mfma_f32_16x16x32_bf16 v[16:19], a[104:107], v[144:147], v[16:19]\n"
      "s_add_u32 s38, s38, s38\n"
      "v_exp_f32 v194, v86\n"
      "v_mfma_f32_16x16x32_bf16 v[20:23], a[104:107], v[148:151], v[20:23]\n"
      "s_memrealtime s[68:69]\n"
      "v_exp_f32 v195, v87\n"
      "v_mfma_f32_16x16x32_bf16 a[160:163], a[104:107], v[152:155], a[160:163]\n"
      "v_cvt_pk_bf16_f32 v138, v192, v193\n"
      "v_cvt_pk_bf16_f32 v139, v194, v195\n"
      "v_mfma_f32_16x16x32_bf16 a[164:167], a[104:107], v[156:159], a[164:167]\n"
      "s_mov_b32 m0, s62\n"
      "v_exp_f32 v188, v88\n"
      "v_mfma_f32_16x16x32_bf16 v[184:187], a[136:139], v[136:139], v[184:187]\n"
      "buffer_load_dwordx4 v228, s[56:59], s60 offen nt lds\n"
      "v_mfma_f32_16x16x32_bf16 a[172:175], a[108:111], v[156:159], a[172:175]\n"
      "buffer_load_dwordx4 v229, s[56:59], s60 offen offset:1024 nt lds\n"
      "v_mfma_f32_16x16x32_bf16 a[168:171], a[108:111], v[152:155], a[168:171]\n"
      "buffer_load_dwordx4 v230, s[56:59], s60 offen offset:2048 nt lds\n"
      "v_mfma_f32_16x16x32_bf16 v[28:31], a[108:111], v[148:151], v[28:31]\n"
      "buffer_load_dwordx4 v231, s[56:59], s60 offen offset:3072 nt lds\n"
      "HD0_%=:\n"
      "s_cmp_lg_u32 s54, 0\n"
      "s_waitcnt vmcnt(12) lgkmcnt(0)\n"
      "s_cbranch_scc1 W4_%=\n"
      "s_waitcnt vmcnt(8)\n"
      "W4_%=:\n"
      "s_sub_u32 s67, s68, s88\n"
      "s_cmp_lt_i32 s67, -8\n"
      "s_cbranch_scc1 GW7_%=\n"
      "GX7_%=:\n"
      "s_barrier\n"
      "v_mfma_f32_16x16x32_bf16 v[24:27], a[108:111], v[144:147], v[24:27]\n"
      "s_memrealtime s[68:69]\n"
      "ds_read_b128 a[64:67], v242 offset:0\n"
      "ds_read_b128 a[72:75], v243 offset:0\n"
      "v_mfma_f32_16x16x32_bf16 v[32:35], a[112:115], v[144:147], v[32:35]\n"
      "s_xor_b64 s[56:57], s[56:57], s[32:33]\n"
      "ds_read_b128 a[80:83], v242 offset:1024\n"
      "ds_read_b128 a[88:91], v243 offset:1024\n"
      "v_mfma_f32_16x16x32_bf16 v[36:39], a[112:115], v[148:151], v[36:39]\n"
      "s_add_u32 s61, s61, 1\n"
      "v_exp_f32 v189, v89\n"
      "v_mfma_f32_16x16x32_bf16 a[176:179], a[112:115], v[152:155], a[176:179]\n"
      "s_add_u32 s62, s62, 0x4000\n"
      "v_exp_f32 v190, v90\n"
      "v_mfma_f32_16x16x32_bf16 a[180:183], a[112:115], v[156:159], a[180:183]\n"
      "s_and_b32 s62, s62, 0x1ffff\n"
      "v_exp_f32 v191, v91\n"
      "v_mfma_f32_16x16x32_bf16 a[188:191], a[116:119], v[156:159], a[188:191]\n"
      "v_cvt_pk_bf16_f32 v140, v188, v189\n"
      "v_readlane_b32 s60, v219, s61\n"
      "v_mfma_f32_16x16x32_bf16 a[184:187], a[116:119], v[152:155], a[184:187]\n"
      "v_exp_f32 v192, v92\n"
      "v_mfma_f32_16x16x32_bf16 v[44:47], a[116:119], v[148:151], v[44:47]\n"
      "v_exp_f32 v193, v93\n"
      "v_mfma_f32_16x16x32_bf16 v[40:43], a[116:119], v[144:147], v[40:43]\n"
      "v_cvt_pk_bf16_f32 v141, v190, v191\n"
      "v_cvt_pk_bf16_f32 v142, v192, v193\n"
      "v_mfma_f32_16x16x32_bf16 v[48:51], a[120:123], v[144:147], v[48:51]\n"
      "v_exp_f32 v194, v94\n"
      "v_mfma_f32_16x16x32_bf16 v[52:55], a[120:123], v[148:151], v[52:55]\n"
      "v_exp_f32 v195, v95\n"
      "s_waitcnt lgkmcnt(0)\n"
      "v_mfma_f32_16x16x32_bf16 a[192:195], a[120:123], v[152:155], a[192:195]\n"
      "s_sub_u32 s67, s68, s66\n"
      "v_cvt_pk_bf16_f32 v143, v194, v195\n"
      "v_xor_b32 v244, 0x10000, v244\n"
      "v_mfma_f32_16x16x32_bf16 a[196:199], a[120:123], v[156:159], a[196:199]\n"
      "s_cmp_gt_i32 s67, s70\n"
      "ds_read_b128 a[68:71], v242 offset:4096\n"
      "ds_read_b128 a[76:79], v243 offset:4096\n"
      "v_mfma_f32_16x16x32_bf16 v[184:187], a[140:143], v[140:143], v[184:187]\n"
      "s_cselect_b32 s69, s77, s86\n"
      "ds_read_b128 a[84:87], v242 offset:5120\n"
      "ds_read_b128 a[92:95], v243 offset:5120\n"
      "v_mfma_f32_16x16x32_bf16 a[204:207], a[124:127], v[156:159], a[204:207]\n"
      "s_cselect_b32 s70, 60, s70\n"
      "v_xor_b32 v245, 0x10000, v245\n"
      "v_xor_b32 v246, 0x10000, v246\n"
      "v_mfma_f32_16x16x32_bf16 a[200:203], a[124:127], v[152:155], a[200:203]\n"
      "s_cselect_b32 s90, s80, s90\n"
      "v_xor_b32 v247, 0x10000, v247\n"
      "v_max3_f32 v196, v184, v185, v186\n"
      "v_mfma_f32_16x16x32_bf16 v[60:63], a[124:127], v[148:151], v[60:63]\n"
      "s_add_u32 s66, s66, s69\n"
      "v_max_f32 v196, v196, v187\n"
      "v_cmp_nge_f32 vcc, 0x4b800000, v196\n"
      "v_mfma_f32_16x16x32_bf16 v[56:59], a[124:127], v[144:147], v[56:59]\n"
      "s_mov_b32 s82, s68\n"
      "s_cbranch_vccnz FS2_%=\n"
      "FR2_%=:\n"
      "s_branch LOOP_%=\n"
      "SP1_%=:\n"
      "s_ff1_i32_b32 s64, s39\n"
      "s_lshl1_add_u32 s64, s64, 1\n"
      "s_bitcmp1_b64 s[52:53], s64\n"
      "s_cbranch_scc0 SH1_%=\n"
      "s_swappc_b64 s[84:85], s[84:85]\n"
      "SH1_%=:\n"
      "s_cmp_lt_u32 s64, 63\n"
      "s_cbranch_scc1 HD1_%=\n"
      "s_xor_b64 s[56:57], s[56:57], s[32:33]\n"
      "s_add_u32 s61, s61, 1\n"
      "s_add_u32 s62, s62, 0x4000\n"
      "s_and_b32 s62, s62, 0x1ffff\n"
      "v_readlane_b32 s74, v219, 60\n"
      "v_readlane_b32 s76, v219, 62\n"
      "s_mov_b32 s61, 0\n"
      "s_add_u32 s74, s74, 0x400\n"
      "v_add_u32 v228, s74, v229\n"
      "v_add_u32 v230, s74, v231\n"
      "v_add_u32 v229, s76, v229\n"
      "v_add_u32 v231, s76, v231\n"
      "s_branch HD1_%=\n"
      "SP2_%=:\n"
      "s_cmp_eq_u32 s38, 0\n"
      "s_cbranch_scc1 L62_%=\n"
      "s_ff1_i32_b32 s64, s38\n"
      "s_lshl1_add_u32 s64, s64, 0\n"
      "s_bitcmp1_b64 s[52:53], s64\n"
      "s_cbranch_scc0 SH2_%=\n"
      "s_swappc_b64 s[84:85], s[84:85]\n"
      "SH2_%=:\n"
      "s_branch HD2_%=\n"
      "L62_%=:\n"
      "s_xor_b64 s[56:57], s[56:57], s[32:33]\n"
      "s_mov_b32 s61, 0\n"
      "v_xor_b32 v228, 0x80, v228\n"
      "v_xor_b32 v229, 0x80, v229\n"
      "v_xor_b32 v230, 0x80, v230\n"
      "v_xor_b32 v231, 0x80, v231\n"
      "s_mov_b32 s38, 0x80000000\n"
      "s_branch HD2_%=\n"
      "SP3_%=:\n"
      "s_cmp_eq_u32 s39, 0\n"
      "s_cbranch_scc1 NH_%=\n"
      "s_ff1_i32_b32 s64, s39\n"
      "s_lshl1_add_u32 s64, s64, 1\n"
      "s_bitcmp1_b64 s[52:53], s64\n"
      "s_cbranch_scc0 SH3_%=\n"
      "s_swappc_b64 s[84:85], s[84:85]\n"
      "SH3_%=:\n"
      "s_bitcmp1_b64 s[34:35], s64\n"
      "s_cbranch_scc0 HD3_%=\n"
      "NH_%=:\n"
      "s_cmp_lg_u32 s54, 0\n"
      "s_waitcnt vmcnt(12) lgkmcnt(0)\n"
      "s_cbranch_scc1 W5_%=\n"
      "s_waitcnt vmcnt(8)\n"
      "W5_%=:\n"
      "s_barrier\n"
      "v_mfma_f32_16x16x32_bf16 v[120:123], a[88:91], a[60:63], v[120:123]\n"
      "s_sub_u32 s54, s54, 1\n"
      "ds_read_b64_tr_b16 a[96:97], v244 offset:32768\n"
      "ds_read_b64_tr_b16 a[98:99], v244 offset:36864\n"
      "v_mfma_f32_16x16x32_bf16 v[100:103], a[68:71], a[0:3], v[160:163]\n"
      "ds_read_b64_tr_b16 a[100:101], v245 offset:32768\n"
      "ds_read_b64_tr_b16 a[102:103], v245 offset:36864\n"
      "v_mfma_f32_16x16x32_bf16 v[100:103], a[76:79], a[16:19], v[100:103]\n"
      "ds_read_b64_tr_b16 a[104:105], v246 offset:32768\n"
      "ds_read_b64_tr_b16 a[106:107], v246 offset:36864\n"
      "v_mfma_f32_16x16x32_bf16 v[100:103], a[84:87], a[32:35], v[100:103]\n"
      "ds_read_b64_tr_b16 a[108:109], v247 offset:32768\n"
      "ds_read_b64_tr_b16 a[110:111], v247 offset:36864\n"
      "v_mfma_f32_16x16x32_bf16 v[100:103], a[92:95], a[48:51], v[100:103]\n"
      "v_exp_f32 v188, v72\n"
      "v_mfma_f32_16x16x32_bf16 v[108:111], a[68:71], a[4:7], v[164:167]\n"
      "v_exp_f32 v189, v73\n"
      "v_mfma_f32_16x16x32_bf16 v[108:111], a[76:79], a[20:23], v[108:111]\n"
      "v_xor_b32 v242, 0x10000, v242\n"
      "v_xor_b32 v243, 0x10000, v243\n"
      "v_mfma_f32_16x16x32_bf16 v[108:111], a[84:87], a[36:39], v[108:111]\n"
      "v_exp_f32 v190, v74\n"
      "v_mfma_f32_16x16x32_bf16 v[108:111], a[92:95], a[52:55], v[108:111]\n"
      "v_exp_f32 v191, v75\n"
      "v_mfma_f32_16x16x32_bf16 v[116:119], a[68:71], a[8:11], v[168:171]\n"
      "v_cvt_pk_bf16_f32 v132, v188, v189\n"
      "v_cvt_pk_bf16_f32 v133, v190, v191\n"
      "v_mfma_f32_16x16x32_bf16 v[116:119], a[76:79], a[24:27], v[116:119]\n"
      "v_exp_f32 v192, v76\n"
      "s_waitcnt lgkmcnt(0)\n"
      "v_mfma_f32_16x16x32_bf16 v[116:119], a[84:87], a[40:43], v[116:119]\n"
      "ds_read_b64_tr_b16 a[112:113], v244 offset:33792\n"
      "ds_read_b64_tr_b16 a[114:115], v244 offset:37888\n"
      "v_mfma_f32_16x16x32_bf16 v[116:119], a[92:95], a[56:59], v[116:119]\n"
      "v_exp_f32 v193, v77\n"
      "v_mfma_f32_16x16x32_bf16 v[124:127], a[68:71], a[12:15], v[172:175]\n"
      "ds_read_b64_tr_b16 a[116:117], v245 offset:33792\n"
      "v_cvt_pk_bf16_f32 v134, v192, v193\n"
      "v_mfma_f32_16x16x32_bf16 v[124:127], a[76:79], a[28:31], v[124:127]\n"
      "v_exp_f32 v194, v78\n"
      "v_mfma_f32_16x16x32_bf16 v[124:127], a[84:87], a[44:47], v[124:127]\n"
      "ds_read_b64_tr_b16 a[118:119], v245 offset:37888\n"
      "ds_read_b64_tr_b16 a[120:121], v246 offset:33792\n"
      "v_mfma_f32_16x16x32_bf16 v[124:127], a[92:95], a[60:63], v[124:127]\n"
      "v_exp_f32 v195, v79\n"
      "v_mfma_f32_16x16x32_bf16 v[0:3], a[96:99], v[144:147], v[0:3]\n"
      "ds_read_b64_tr_b16 a[122:123], v246 offset:37888\n"
      "v_cvt_pk_bf16_f32 v135, v194, v195\n"
      "v_mfma_f32_16x16x32_bf16 v[4:7], a[96:99], v[148:151], v[4:7]\n"
      "v_exp_f32 v188, v80\n"
      "v_mfma_f32_16x16x32_bf16 v[184:187], a[132:135], v[132:135], v[184:187]\n"
      "ds_read_b64_tr_b16 a[124:125], v247 offset:33792\n"
      "ds_read_b64_tr_b16 a[126:127], v247 offset:37888\n"
      "v_mfma_f32_16x16x32_bf16 a[144:147], a[96:99], v[152:155], a[144:147]\n"
      "v_exp_f32 v189, v81\n"
      "v_mfma_f32_16x16x32_bf16 a[148:151], a[96:99], v[156:159], a[148:151]\n"
      "v_exp_f32 v190, v82\n"
      "v_mfma_f32_16x16x32_bf16 a[156:159], a[100:103], v[156:159], a[156:159]\n"
      "v_exp_f32 v191, v83\n"
      "v_mfma_f32_16x16x32_bf16 a[152:155], a[100:103], v[152:155], a[152:155]\n"
      "v_cvt_pk_bf16_f32 v136, v188, v189\n"
      "v_cvt_pk_bf16_f32 v137, v190, v191\n"
      "v_mfma_f32_16x16x32_bf16 v[12:15], a[100:103], v[148:151], v[12:15]\n"
      "v_exp_f32 v192, v84\n"
      "v_mfma_f32_16x16x32_bf16 v[8:11], a[100:103], v[144:147], v[8:11]\n"
      "v_exp_f32 v193, v85\n"
      "v_mfma_f32_16x16x32_bf16 v[16:19], a[104:107], v[144:147], v[16:19]\n"
      "s_add_u32 s38, s38, s38\n"
      "v_exp_f32 v194, v86\n"
      "v_mfma_f32_16x16x32_bf16 v[20:23], a[104:107], v[148:151], v[20:23]\n"
      "s_memrealtime s[68:69]\n"
      "v_exp_f32 v195, v87\n"
      "v_mfma_f32_16x16x32_bf16 a[160:163], a[104:107], v[152:155], a[160:163]\n"
      "v_cvt_pk_bf16_f32 v138, v192, v193\n"
      "v_cvt_pk_bf16_f32 v139, v194, v195\n"
      "v_mfma_f32_16x16x32_bf16 a[164:167], a[104:107], v[156:159], a[164:167]\n"
      "v_exp_f32 v188, v88\n"
      "v_mfma_f32_16x16x32_bf16 v[184:187], a[136:139], v[136:139], v[184:187]\n"
      "s_nop 0\n"
      "v_mfma_f32_16x16x32_bf16 a[172:175], a[108:111], v[156:159], a[172:175]\n"
      "s_nop 0\n"
      "v_mfma_f32_16x16x32_bf16 a[168:171], a[108:111], v[152:155], a[168:171]\n"
      "s_nop 0\n"
      "v_mfma_f32_16x16x32_bf16 v[28:31], a[108:111], v[148:151], v[28:31]\n"
      "s_nop 0\n"
      "s_cbranch_scc0 MH_%=\n"
      "END_%=:\n"
      "s_waitcnt lgkmcnt(0)\n"
      "v_mfma_f32_16x16x32_bf16 v[24:27], a[108:111], v[144:147], v[24:27]\n"
      "v_mov_b32 v236, s68\n"
      "v_exp_f32 v189, v89\n"
      "v_mfma_f32_16x16x32_bf16 v[32:35], a[112:115], v[144:147], v[32:35]\n"
      "s_mov_b64 exec, 1\n"
      "global_store_dwordx2 v238, v[236:237], s[46:47] offset:8 sc1\n"
      "s_mov_b64 exec, -1\n"
      "v_mfma_f32_16x16x32_bf16 v[36:39], a[112:115], v[148:151], v[36:39]\n"
      "s_mov_b32 s82, s68\n"
      "v_exp_f32 v190, v90\n"
      "v_mfma_f32_16x16x32_bf16 a[176:179], a[112:115], v[152:155], a[176:179]\n"
      "v_exp_f32 v191, v91\n"
      "v_mfma_f32_16x16x32_bf16 a[180:183], a[112:115], v[156:159], a[180:183]\n"
      "v_cvt_pk_bf16_f32 v140, v188, v189\n"
      "v_cvt_pk_bf16_f32 v141, v190, v191\n"
      "v_mfma_f32_16x16x32_bf16 a[188:191], a[116:119], v[156:159], a[188:191]\n"
      "v_exp_f32 v192, v92\n"
      "s_waitcnt vmcnt(5)\n"
      "s_barrier\n"
      "v_mfma_f32_16x16x32_bf16 a[184:187], a[116:119], v[152:155], a[184:187]\n"
      "ds_read_b64_tr_b16 a[80:81], v224 offset:0\n"
      "ds_read_b64_tr_b16 a[82:83], v224 offset:4096\n"
      "v_mfma_f32_16x16x32_bf16 v[44:47], a[116:119], v[148:151], v[44:47]\n"
      "ds_read_b64_tr_b16 a[84:85], v225 offset:0\n"
      "ds_read_b64_tr_b16 a[86:87], v225 offset:4096\n"
      "v_mfma_f32_16x16x32_bf16 v[40:43], a[116:119], v[144:147], v[40:43]\n"
      "s_mov_b32 s74, 0xffff0000\n"
      "ds_read_b64_tr_b16 a[88:89], v226 offset:0\n"
      "ds_read_b64_tr_b16 a[90:91], v226 offset:4096\n"
      "v_mfma_f32_16x16x32_bf16 v[48:51], a[120:123], v[144:147], v[48:51]\n"
      "s_mov_b32 s75, 0xffff0000\n"
      "ds_read_b64_tr_b16 a[92:93], v227 offset:0\n"
      "ds_read_b64_tr_b16 a[94:95], v227 offset:4096\n"
      "v_mfma_f32_16x16x32_bf16 v[52:55], a[120:123], v[148:151], v[52:55]\n"
      "s_mov_b32 s94, 0\n"
      "v_exp_f32 v193, v93\n"
      "v_mfma_f32_16x16x32_bf16 a[192:195], a[120:123], v[152:155], a[192:195]\n"
      "s_mov_b32 s95, -1\n"
      "ds_read_b64_tr_b16 a[16:17], v224 offset:1024\n"
      "v_cvt_pk_bf16_f32 v142, v192, v193\n"
      "v_mfma_f32_16x16x32_bf16 a[196:199], a[120:123], v[156:159], a[196:199]\n"
      "v_exp_f32 v194, v94\n"
      "v_mfma_f32_16x16x32_bf16 a[204:207], a[124:127], v[156:159], a[204:207]\n"
      "ds_read_b64_tr_b16 a[18:19], v224 offset:5120\n"
      "ds_read_b64_tr_b16 a[20:21], v225 offset:1024\n"
      "v_mfma_f32_16x16x32_bf16 a[200:203], a[124:127], v[152:155], a[200:203]\n"
      "v_exp_f32 v195, v95\n"
      "v_mfma_f32_16x16x32_bf16 v[60:63], a[124:127], v[148:151], v[60:63]\n"
      "ds_read_b64_tr_b16 a[22:23], v225 offset:5120\n"
      "v_cvt_pk_bf16_f32 v143, v194, v195\n"
      "v_mfma_f32_16x16x32_bf16 v[56:59], a[124:127], v[144:147], v[56:59]\n"
      "ds_read_b64_tr_b16 a[24:25], v226 offset:1024\n"
      "ds_read_b64_tr_b16 a[26:27], v226 offset:5120\n"
      "v_mfma_f32_16x16x32_bf16 v[184:187], a[140:143], v[140:143], v[184:187]\n"
      "ds_read_b64_tr_b16 a[28:29], v227 offset:1024\n"
      "ds_read_b64_tr_b16 a[30:31], v227 offset:5120\n"
      "ds_read_b64_tr_b16 a[0:1], v220 offset:1024\n"
      "ds_read_b64_tr_b16 a[2:3], v220 offset:5120\n"
      "ds_read_b64_tr_b16 a[4:5], v221 offset:1024\n"
      "ds_read_b64_tr_b16 a[6:7], v221 offset:5120\n"
      "ds_read_b64_tr_b16 a[8:9], v222 offset:1024\n"
      "ds_read_b64_tr_b16 a[10:11], v222 offset:5120\n"
      "ds_read_b64_tr_b16 a[12:13], v223 offset:1024\n"
      "ds_read_b64_tr_b16 a[14:15], v223 offset:5120\n"
      "v_max3_f32 v196, v184, v185, v186\n"
      "v_max_f32 v196, v196, v187\n"
      "v_cmp_nge_f32 vcc, 0x4b800000, v196\n"
      "s_cbranch_vccnz FS3_%=\n"
      "FR3_%=:\n"
      "s_waitcnt lgkmcnt(15)\n"
      "v_mfma_f32_16x16x32_bf16 a[176:179], a[80:83], v[136:139], a[176:179]\n"
      "v_add_f32 v180, v180, v184\n"
      "v_add_f32 v181, v181, v185\n"
      "v_mfma_f32_16x16x32_bf16 a[180:183], a[80:83], v[140:143], a[180:183]\n"
      "v_add_f32 v182, v182, v186\n"
      "v_add_f32 v183, v183, v187\n"
      "v_mfma_f32_16x16x32_bf16 a[184:187], a[84:87], v[136:139], a[184:187]\n"
      "v_exp_f32 v188, v112\n"
      "v_mfma_f32_16x16x32_bf16 a[188:191], a[84:87], v[140:143], a[188:191]\n"
      "ds_write_b128 v232, a[176:179] offset:0\n"
      "v_exp_f32 v189, v113\n"
      "v_mfma_f32_16x16x32_bf16 a[192:195], a[88:91], v[136:139], a[192:195]\n"
      "v_exp_f32 v190, v114\n"
      "v_mfma_f32_16x16x32_bf16 a[196:199], a[88:91], v[140:143], a[196:199]\n"
      "v_exp_f32 v191, v115\n"
      "v_mfma_f32_16x16x32_bf16 a[200:203], a[92:95], v[136:139], a[200:203]\n"
      "ds_write_b128 v232, a[180:183] offset:1024\n"
      "v_exp_f32 v192, v116\n"
      "v_mfma_f32_16x16x32_bf16 a[204:207], a[92:95], v[140:143], a[204:207]\n"
      "v_exp_f32 v193, v117\n"
      "v_exp_f32 v194, v118\n"
      "v_cvt_pk_bf16_f32 v152, v188, v189\n"
      "v_exp_f32 v195, v119\n"
      "v_cvt_pk_bf16_f32 v153, v190, v191\n"
      "ds_write_b128 v232, a[184:187] offset:2048\n"
      "v_exp_f32 v188, v120\n"
      "v_cvt_pk_bf16_f32 v154, v192, v193\n"
      "v_mfma_f32_16x16x32_bf16 v[32:35], a[80:83], v[128:131], v[32:35]\n"
      "v_exp_f32 v189, v121\n"
      "v_cvt_pk_bf16_f32 v155, v194, v195\n"
      "v_exp_f32 v190, v122\n"
      "v_cvt_pk_bf16_f32 v156, v188, v189\n"
      "v_mfma_f32_16x16x32_bf16 v[184:187], a[136:139], v[152:155], 0\n"
      "ds_write_b128 v234, v[152:155] offset:0\n"
      "v_exp_f32 v191, v123\n"
      "v_mfma_f32_16x16x32_bf16 v[36:39], a[80:83], v[132:135], v[36:39]\n"
      "v_exp_f32 v192, v124\n"
      "v_cvt_pk_bf16_f32 v157, v190, v191\n"
      "v_exp_f32 v193, v125\n"
      "v_exp_f32 v194, v126\n"
      "v_cvt_pk_bf16_f32 v158, v192, v193\n"
      "ds_write_b128 v232, a[188:191] offset:3072\n"
      "v_mfma_f32_16x16x32_bf16 v[40:43], a[84:87], v[128:131], v[40:43]\n"
      "v_exp_f32 v195, v127\n"
      "v_exp_f32 v188, v96\n"
      "v_cvt_pk_bf16_f32 v159, v194, v195\n"
      "v_exp_f32 v189, v97\n"
      "v_mfma_f32_16x16x32_bf16 v[44:47], a[84:87], v[132:135], v[44:47]\n"
      "v_exp_f32 v190, v98\n"
      "ds_write_b128 v234, v[156:159] offset:1024\n"
      "v_cvt_pk_bf16_f32 v144, v188, v189\n"
      "v_mfma_f32_16x16x32_bf16 v[184:187], a[140:143], v[156:159], v[184:187]\n"
      "v_exp_f32 v191, v99\n"
      "v_exp_f32 v192, v100\n"
      "v_cvt_pk_bf16_f32 v145, v190, v191\n"
      "v_mfma_f32_16x16x32_bf16 v[48:51], a[88:91], v[128:131], v[48:51]\n"
      "v_exp_f32 v193, v101\n"
      "ds_write_b128 v232, a[192:195] offset:4096\n"
      "v_exp_f32 v194, v102\n"
      "v_cvt_pk_bf16_f32 v146, v192, v193\n"
      "v_exp_f32 v195, v103\n"
      "v_mfma_f32_16x16x32_bf16 v[52:55], a[88:91], v[132:135], v[52:55]\n"
      "v_exp_f32 v188, v104\n"
      "v_cvt_pk_bf16_f32 v147, v194, v195\n"
      "v_exp_f32 v189, v105\n"
      "ds_write_b128 v232, a[196:199] offset:5120\n"
      "v_exp_f32 v190, v106\n"
      "v_cvt_pk_bf16_f32 v148, v188, v189\n"
      "v_mfma_f32_16x16x32_bf16 v[184:187], a[128:131], v[144:147], v[184:187]\n"
      "v_exp_f32 v191, v107\n"
      "v_exp_f32 v192, v108\n"
      "v_cvt_pk_bf16_f32 v149, v190, v191\n"
      "v_exp_f32 v193, v109\n"
      "ds_write_b128 v232, a[200:203] offset:6144\n"
      "v_mfma_f32_16x16x32_bf16 v[56:59], a[92:95], v[128:131], v[56:59]\n"
      "v_exp_f32 v194, v110\n"
      "v_cvt_pk_bf16_f32 v150, v192, v193\n"
      "v_exp_f32 v195, v111\n"
      "v_cndmask_b32 v229, v180, v181, s[74:75]\n"
      "v_cvt_pk_bf16_f32 v151, v194, v195\n"
      "v_mfma_f32_16x16x32_bf16 v[60:63], a[92:95], v[132:135], v[60:63]\n"
      "v_cndmask_b32 v200, v182, v183, s[74:75]\n"
      "ds_write_b128 v232, a[204:207] offset:7168\n"
      "v_cndmask_b32 v228, v176, v177, s[74:75]\n"
      "v_mfma_f32_16x16x32_bf16 v[184:187], a[132:135], v[148:151], v[184:187]\n"
      "s_nop 7\n"
      "v_max3_f32 v196, v184, v185, v186\n"
      "v_max_f32 v196, v196, v187\n"
      "v_cmp_nge_f32 vcc, 0x4b800000, v196\n"
      "s_cbranch_vccnz FS8_%=\n"
      "FR8_%=:\n"
      "v_cndmask_b32 v202, v186, v187, s[74:75]\n"
      "v_cndmask_b32 v201, v184, v185, s[74:75]\n"
      "v_add_f32 v200, v200, v202\n"
      "ds_write_b32 v234, v200 offset:4096\n"
      "v_add_f32 v229, v229, v201\n"
      "s_waitcnt lgkmcnt(8)\n"
      "v_mfma_f32_16x16x32_bf16 v[32:35], a[16:19], v[144:147], v[32:35]\n"
      "v_mfma_f32_16x16x32_bf16 v[36:39], a[16:19], v[148:151], v[36:39]\n"
      "v_mfma_f32_16x16x32_bf16 v[40:43], a[20:23], v[144:147], v[40:43]\n"
      "s_waitcnt lgkmcnt(0)\n"
      "s_barrier\n"
      "v_mfma_f32_16x16x32_bf16 v[44:47], a[20:23], v[148:151], v[44:47]\n"
      "ds_read_b32 v208, v209\n"
      "ds_read_b128 v[160:163], v235 offset:0\n"
      "v_mfma_f32_16x16x32_bf16 v[48:51], a[24:27], v[144:147], v[48:51]\n"
      "ds_read_b128 v[164:167], v235 offset:1024\n"
      "ds_read_b128 v[64:67], v233 offset:0\n"
      "v_mfma_f32_16x16x32_bf16 v[52:55], a[24:27], v[148:151], v[52:55]\n"
      "ds_read_b128 v[68:71], v233 offset:1024\n"
      "ds_read_b128 v[72:75], v233 offset:2048\n"
      "ds_read_b128 v[76:79], v233 offset:3072\n"
      "v_mfma_f32_16x16x32_bf16 v[56:59], a[28:31], v[144:147], v[56:59]\n"
      "ds_read_b128 v[80:83], v233 offset:4096\n"
      "ds_read_b128 v[84:87], v233 offset:5120\n"
      "v_mfma_f32_16x16x32_bf16 v[60:63], a[28:31], v[148:151], v[60:63]\n"
      "ds_read_b128 v[88:91], v233 offset:6144\n"
      "ds_read_b128 v[92:95], v233 offset:7168\n"
      "ds_read_b32 v204, v235 offset:4096\n"
      "s_waitcnt lgkmcnt(11)\n"
      "v_cmp_ne_u32 vcc, 0, v208\n"
      "s_cbranch_vccnz SLOW_%=\n"
      "AGAIN_%=:\n"
      "s_waitcnt lgkmcnt(9)\n"
      "v_mfma_f32_16x16x32_bf16 v[32:35], a[0:3], v[160:163], v[32:35]\n"
      "v_mfma_f32_16x16x32_bf16 v[36:39], a[0:3], v[164:167], v[36:39]\n"
      "v_mfma_f32_16x16x32_bf16 v[40:43], a[4:7], v[160:163], v[40:43]\n"
      "v_mfma_f32_16x16x32_bf16 v[44:47], a[4:7], v[164:167], v[44:47]\n"
      "v_mfma_f32_16x16x32_bf16 v[48:51], a[8:11], v[160:163], v[48:51]\n"
      "v_mfma_f32_16x16x32_bf16 v[52:55], a[8:11], v[164:167], v[52:55]\n"
      "v_mfma_f32_16x16x32_bf16 v[56:59], a[12:15], v[160:163], v[56:59]\n"
      "v_mfma_f32_16x16x32_bf16 v[60:63], a[12:15], v[164:167], v[60:63]\n"
      "global_load_dwordx2 v[206:207], v239, s[46:47] offset:8 sc1\n"
      "s_waitcnt lgkmcnt(7)\n"
      "v_pk_add_f32 v[32:33], v[32:33], v[64:65]\n"
      "v_pk_add_f32 v[34:35], v[34:35], v[66:67]\n"
      "v_pk_add_f32 v[36:37], v[36:37], v[68:69]\n"
      "v_pk_add_f32 v[38:39], v[38:39], v[70:71]\n"
      "v_cvt_pk_bf16_f32 v212, v32, v33\n"
      "v_cvt_pk_bf16_f32 v213, v34, v35\n"
      "v_cvt_pk_bf16_f32 v214, v36, v37\n"
      "v_cvt_pk_bf16_f32 v215, v38, v39\n"
      "buffer_store_dwordx4 v[212:215], v241, s[24:27], s78 offen offset:0 sc1\n"
      "s_waitcnt lgkmcnt(5)\n"
      "v_pk_add_f32 v[40:41], v[40:41], v[72:73]\n"
      "v_pk_add_f32 v[42:43], v[42:43], v[74:75]\n"
      "v_pk_add_f32 v[44:45], v[44:45], v[76:77]\n"
      "v_pk_add_f32 v[46:47], v[46:47], v[78:79]\n"
      "v_cvt_pk_bf16_f32 v216, v40, v41\n"
      "v_cvt_pk_bf16_f32 v217, v42, v43\n"
      "v_cvt_pk_bf16_f32 v218, v44, v45\n"
      "v_cvt_pk_bf16_f32 v219, v46, v47\n"
      "buffer_store_dwordx4 v[216:219], v241, s[24:27], s78 offen offset:1024 sc1\n"
      "s_waitcnt lgkmcnt(3)\n"
      "v_pk_add_f32 v[48:49], v[48:49], v[80:81]\n"
      "v_pk_add_f32 v[50:51], v[50:51], v[82:83]\n"
      "v_pk_add_f32 v[52:53], v[52:53], v[84:85]\n"
      "v_pk_add_f32 v[54:55], v[54:55], v[86:87]\n"
      "v_cvt_pk_bf16_f32 v220, v48, v49\n"
      "v_cvt_pk_bf16_f32 v221, v50, v51\n"
      "v_cvt_pk_bf16_f32 v222, v52, v53\n"
      "v_cvt_pk_bf16_f32 v223, v54, v55\n"
      "buffer_store_dwordx4 v[220:223], v241, s[24:27], s78 offen offset:2048 sc1\n"
      "s_waitcnt lgkmcnt(0)\n"
      "v_pk_add_f32 v[56:57], v[56:57], v[88:89]\n"
      "v_pk_add_f32 v[58:59], v[58:59], v[90:91]\n"
      "v_pk_add_f32 v[60:61], v[60:61], v[92:93]\n"
      "v_pk_add_f32 v[62:63], v[62:63], v[94:95]\n"
      "v_cvt_pk_bf16_f32 v224, v56, v57\n"
      "v_cvt_pk_bf16_f32 v225, v58, v59\n"
      "v_cvt_pk_bf16_f32 v226, v60, v61\n"
      "v_cvt_pk_bf16_f32 v227, v62, v63\n"
      "buffer_store_dwordx4 v[224:227], v241, s[24:27], s78 offen offset:3072 sc1\n"
      "v_add_f32 v229, v229, v204\n"
      "v_cndmask_b32 v230, v228, v229, s[94:95]\n"
      "v_add_u32 v231, s40, v230\n"
      "v_add3_u32 v231, v231, v212, v213\n"
      "v_add3_u32 v231, v231, v214, v215\n"
      "v_add3_u32 v231, v231, v216, v217\n"
      "v_add3_u32 v231, v231, v218, v219\n"
      "v_add3_u32 v231, v231, v220, v221\n"
      "v_add3_u32 v231, v231, v222, v223\n"
      "v_add3_u32 v231, v231, v224, v225\n"
      "v_add3_u32 v231, v231, v226, v227\n"
      "buffer_store_dwordx2 v[230:231], v240, s[24:27], s78 offen sc1\n"
      "v_xor_b32 v244, 0x10000, v244\n"
      "v_xor_b32 v245, 0x10000, v245\n"
      "v_xor_b32 v246, 0x10000, v246\n"
      "v_xor_b32 v247, 0x10000, v247\n"
      "s_waitcnt vmcnt(7)\n"
      "s_barrier\n"
      "ds_read_b64_tr_b16 a[64:65], v244 offset:16384\n"
      "ds_read_b64_tr_b16 a[66:67], v244 offset:20480\n"
      "ds_read_b64_tr_b16 a[68:69], v245 offset:16384\n"
      "ds_read_b64_tr_b16 a[70:71], v245 offset:20480\n"
      "ds_read_b64_tr_b16 a[72:73], v246 offset:16384\n"
      "ds_read_b64_tr_b16 a[74:75], v246 offset:20480\n"
      "ds_read_b64_tr_b16 a[76:77], v247 offset:16384\n"
      "ds_read_b64_tr_b16 a[78:79], v247 offset:20480\n"
      "s_waitcnt lgkmcnt(4)\n"
      "v_mfma_f32_16x16x32_bf16 a[144:147], a[64:67], v[136:139], a[144:147]\n"
      "ds_read_b64_tr_b16 a[96:97], v244 offset:17408\n"
      "ds_read_b64_tr_b16 a[98:99], v244 offset:21504\n"
      "v_mfma_f32_16x16x32_bf16 a[148:151], a[64:67], v[140:143], a[148:151]\n"
      "ds_read_b64_tr_b16 a[100:101], v245 offset:17408\n"
      "ds_read_b64_tr_b16 a[102:103], v245 offset:21504\n"
      "v_mfma_f32_16x16x32_bf16 a[152:155], a[68:71], v[136:139], a[152:155]\n"
      "ds_read_b64_tr_b16 a[104:105], v246 offset:17408\n"
      "ds_read_b64_tr_b16 a[106:107], v246 offset:21504\n"
      "v_mfma_f32_16x16x32_bf16 a[156:159], a[68:71], v[140:143], a[156:159]\n"
      "ds_read_b64_tr_b16 a[108:109], v247 offset:17408\n"
      "ds_read_b64_tr_b16 a[110:111], v247 offset:21504\n"
      "s_waitcnt lgkmcnt(8)\n"
      "v_mfma_f32_16x16x32_bf16 a[160:163], a[72:75], v[136:139], a[160:163]\n"
      "s_sub_u32 s76, 18432, s78\n"
      "v_mfma_f32_16x16x32_bf16 a[164:167], a[72:75], v[140:143], a[164:167]\n"
      "s_mov_b32 s63, 0x40000\n"
      "v_mfma_f32_16x16x32_bf16 a[168:171], a[76:79], v[136:139], a[168:171]\n"
      "v_mov_b32 v198, 0\n"
      "v_mfma_f32_16x16x32_bf16 a[172:175], a[76:79], v[140:143], a[172:175]\n"
      "v_and_b32 v197, 0xf0, v241\n"
      "s_waitcnt lgkmcnt(0)\n"
      "v_mfma_f32_16x16x32_bf16 a[144:147], a[96:99], v[152:155], a[144:147]\n"
      "v_lshrrev_b32 v196, 3, v241\n"
      "v_mfma_f32_16x16x32_bf16 a[148:151], a[96:99], v[156:159], a[148:151]\n"
      "v_and_b32 v196, 32, v196\n"
      "v_mfma_f32_16x16x32_bf16 a[152:155], a[100:103], v[152:155], a[152:155]\n"
      "v_lshl_or_b32 v196, v197, 4, v196\n"
      "v_mfma_f32_16x16x32_bf16 a[156:159], a[100:103], v[156:159], a[156:159]\n"
      "v_lshrrev_b32 v199, 5, v241\n"
      "v_mfma_f32_16x16x32_bf16 a[160:163], a[104:107], v[152:155], a[160:163]\n"
      "v_and_or_b32 v196, v199, 16, v196\n"
      "v_mfma_f32_16x16x32_bf16 a[164:167], a[104:107], v[156:159], a[164:167]\n"
      "v_mov_b32 v191, s40\n"
      "v_mfma_f32_16x16x32_bf16 a[168:171], a[108:111], v[152:155], a[168:171]\n"
      "v_mfma_f32_16x16x32_bf16 a[172:175], a[108:111], v[156:159], a[172:175]\n"
      "s_waitcnt vmcnt(0)\n"
      "v_subrev_u32 v205, s82, v206\n"
      "v_cmp_ne_u32 vcc, s40, v207\n"
      "s_cbranch_vccnz LAG_%=\n"
      "v_cmp_lt_i32 vcc, 10, v205\n"
      "s_cbranch_vccnz LAG_%=\n"
      "READ_%=:\n"
      "buffer_load_dwordx2 v[160:161], v240, s[24:27], s76 offen sc1\n"
      "buffer_load_dwordx4 v[96:99], v241, s[24:27], s76 offen offset:0 sc1\n"
      "buffer_load_dwordx4 v[100:103], v241, s[24:27], s76 offen offset:1024 sc1\n"
      "buffer_load_dwordx4 v[104:107], v241, s[24:27], s76 offen offset:2048 sc1\n"
      "buffer_load_dwordx4 v[108:111], v241, s[24:27], s76 offen offset:3072 sc1\n"
      "v_mfma_f32_16x16x32_bf16 v[0:3], a[64:67], v[128:131], v[0:3]\n"
      "v_mfma_f32_16x16x32_bf16 v[4:7], a[64:67], v[132:135], v[4:7]\n"
      "ds_write_b128 v232, a[144:147] offset:32768\n"
      "v_mfma_f32_16x16x32_bf16 v[8:11], a[68:71], v[128:131], v[8:11]\n"
      "v_mfma_f32_16x16x32_bf16 v[12:15], a[68:71], v[132:135], v[12:15]\n"
      "ds_write_b128 v232, a[148:151] offset:33792\n"
      "v_mfma_f32_16x16x32_bf16 v[16:19], a[72:75], v[128:131], v[16:19]\n"
      "v_mfma_f32_16x16x32_bf16 v[20:23], a[72:75], v[132:135], v[20:23]\n"
      "ds_write_b128 v232, a[152:155] offset:34816\n"
      "v_mfma_f32_16x16x32_bf16 v[24:27], a[76:79], v[128:131], v[24:27]\n"
      "v_mfma_f32_16x16x32_bf16 v[28:31], a[76:79], v[132:135], v[28:31]\n"
      "ds_write_b128 v232, a[156:159] offset:35840\n"
      "v_mfma_f32_16x16x32_bf16 v[0:3], a[96:99], v[144:147], v[0:3]\n"
      "v_mfma_f32_16x16x32_bf16 v[4:7], a[96:99], v[148:151], v[4:7]\n"
      "ds_write_b128 v232, a[160:163] offset:36864\n"
      "v_mfma_f32_16x16x32_bf16 v[8:11], a[100:103], v[144:147], v[8:11]\n"
      "v_mfma_f32_16x16x32_bf16 v[12:15], a[100:103], v[148:151], v[12:15]\n"
      "ds_write_b128 v232, a[164:167] offset:37888\n"
      "v_mfma_f32_16x16x32_bf16 v[16:19], a[104:107], v[144:147], v[16:19]\n"
      "v_mfma_f32_16x16x32_bf16 v[20:23], a[104:107], v[148:151], v[20:23]\n"
      "ds_write_b128 v232, a[168:171] offset:38912\n"
      "v_mfma_f32_16x16x32_bf16 v[24:27], a[108:111], v[144:147], v[24:27]\n"
      "v_mfma_f32_16x16x32_bf16 v[28:31], a[108:111], v[148:151], v[28:31]\n"
      "ds_write_b128 v232, a[172:175] offset:39936\n"
      "s_waitcnt lgkmcnt(0)\n"
      "s_barrier\n"
      "ds_read_b128 v[64:67], v233 offset:32768\n"
      "ds_read_b128 v[68:71], v233 offset:33792\n"
      "ds_read_b128 v[72:75], v233 offset:34816\n"
      "ds_read_b128 v[76:79], v233 offset:35840\n"
      "ds_read_b128 v[80:83], v233 offset:36864\n"
      "ds_read_b128 v[84:87], v233 offset:37888\n"
      "ds_read_b128 v[88:91], v233 offset:38912\n"
      "ds_read_b128 v[92:95], v233 offset:39936\n"
      "s_waitcnt lgkmcnt(6)\n"
      "v_pk_add_f32 v[0:1], v[0:1], v[64:65]\n"
      "v_pk_add_f32 v[2:3], v[2:3], v[66:67]\n"
      "v_pk_add_f32 v[4:5], v[4:5], v[68:69]\n"
      "v_pk_add_f32 v[6:7], v[6:7], v[70:71]\n"
      "s_waitcnt lgkmcnt(4)\n"
      "v_pk_add_f32 v[8:9], v[8:9], v[72:73]\n"
      "v_pk_add_f32 v[10:11], v[10:11], v[74:75]\n"
      "v_pk_add_f32 v[12:13], v[12:13], v[76:77]\n"
      "v_pk_add_f32 v[14:15], v[14:15], v[78:79]\n"
      "s_waitcnt lgkmcnt(2)\n"
      "v_pk_add_f32 v[16:17], v[16:17], v[80:81]\n"
      "v_pk_add_f32 v[18:19], v[18:19], v[82:83]\n"
      "v_pk_add_f32 v[20:21], v[20:21], v[84:85]\n"
      "v_pk_add_f32 v[22:23], v[22:23], v[86:87]\n"
      "s_waitcnt lgkmcnt(0)\n"
      "v_pk_add_f32 v[24:25], v[24:25], v[88:89]\n"
      "v_pk_add_f32 v[26:27], v[26:27], v[90:91]\n"
      "v_pk_add_f32 v[28:29], v[28:29], v[92:93]\n"
      "v_pk_add_f32 v[30:31], v[30:31], v[94:95]\n"
      "TRY_%=:\n"
      "s_waitcnt vmcnt(4)\n"
      "v_mov_b32 v162, v160\n"
      "v_add_u32 v231, s40, v160\n"
      "s_nop 0\n"
      "v_permlane32_swap_b32 v160, v162\n"
      "v_sub_f32 v168, v160, v228\n"
      "v_min_f32 v172, 0, -v168\n"
      "v_min_f32 v168, 0, v168\n"
      "v_exp_f32 v168, v168\n"
      "v_exp_f32 v172, v172\n"
      "v_mul_f32 v163, v229, v168\n"
      "v_fmac_f32 v163, v162, v172\n"
      "v_rcp_f32 v163, v163\n"
      "s_nop 0\n"
      "v_mul_f32 v168, v168, v163\n"
      "v_mul_f32 v172, v172, v163\n"
      "v_mov_b32 v169, v168\n"
      "v_mov_b32 v173, v172\n"
      "s_nop 0\n"
      "v_permlane16_swap_b32 v168, v169\n"
      "v_permlane16_swap_b32 v172, v173\n"
      "v_pk_mul_f32 v[32:33], v[0:1], v[168:169] op_sel:[0,0] op_sel_hi:[1,0]\n"
      "v_pk_mul_f32 v[34:35], v[2:3], v[168:169] op_sel:[0,0] op_sel_hi:[1,0]\n"
      "v_pk_mul_f32 v[36:37], v[4:5], v[168:169] op_sel:[0,1] op_sel_hi:[1,1]\n"
      "v_pk_mul_f32 v[38:39], v[6:7], v[168:169] op_sel:[0,1] op_sel_hi:[1,1]\n"
      "v_pk_mul_f32 v[40:41], v[8:9], v[168:169] op_sel:[0,0] op_sel_hi:[1,0]\n"
      "v_pk_mul_f32 v[42:43], v[10:11], v[168:169] op_sel:[0,0] op_sel_hi:[1,0]\n"
      "v_pk_mul_f32 v[44:45], v[12:13], v[168:169] op_sel:[0,1] op_sel_hi:[1,1]\n"
      "v_pk_mul_f32 v[46:47], v[14:15], v[168:169] op_sel:[0,1] op_sel_hi:[1,1]\n"
      "v_pk_mul_f32 v[48:49], v[16:17], v[168:169] op_sel:[0,0] op_sel_hi:[1,0]\n"
      "v_pk_mul_f32 v[50:51], v[18:19], v[168:169] op_sel:[0,0] op_sel_hi:[1,0]\n"
      "v_pk_mul_f32 v[52:53], v[20:21], v[168:169] op_sel:[0,1] op_sel_hi:[1,1]\n"
      "v_pk_mul_f32 v[54:55], v[22:23], v[168:169] op_sel:[0,1] op_sel_hi:[1,1]\n"
      "v_pk_mul_f32 v[56:57], v[24:25], v[168:169] op_sel:[0,0] op_sel_hi:[1,0]\n"
      "v_pk_mul_f32 v[58:59], v[26:27], v[168:169] op_sel:[0,0] op_sel_hi:[1,0]\n"
      "v_pk_mul_f32 v[60:61], v[28:29], v[168:169] op_sel:[0,1] op_sel_hi:[1,1]\n"
      "v_pk_mul_f32 v[62:63], v[30:31], v[168:169] op_sel:[0,1] op_sel_hi:[1,1]\n"
      "s_waitcnt vmcnt(3)\n"
      "v_add3_u32 v231, v231, v96, v97\n"
      "v_add3_u32 v231, v231, v98, v99\n"
      "s_waitcnt vmcnt(2)\n"
      "v_add3_u32 v231, v231, v100, v101\n"
      "v_add3_u32 v231, v231, v102, v103\n"
      "s_waitcnt vmcnt(1)\n"
      "v_add3_u32 v231, v231, v104, v105\n"
      "v_add3_u32 v231, v231, v106, v107\n"
      "s_waitcnt vmcnt(0)\n"
      "v_add3_u32 v231, v231, v108, v109\n"
      "v_add3_u32 v231, v231, v110, v111\n"
      "v_cmp_ne_u32 vcc, v231, v161\n"
      "s_cbranch_vccnz RETRY_%=\n"
      "GOT_%=:\n"
      "v_lshlrev_b32 v0, 16, v96\n"
      "v_and_b32 v1, 0xffff0000, v96\n"
      "v_lshlrev_b32 v2, 16, v97\n"
      "v_and_b32 v3, 0xffff0000, v97\n"
      "v_lshlrev_b32 v8, 16, v100\n"
      "v_and_b32 v9, 0xffff0000, v100\n"
      "v_lshlrev_b32 v10, 16, v101\n"
      "v_and_b32 v11, 0xffff0000, v101\n"
      "v_pk_fma_f32 v[32:33], v[0:1], v[172:173], v[32:33] op_sel:[0,0,0] op_sel_hi:[1,0,1]\n"
      "v_pk_fma_f32 v[34:35], v[2:3], v[172:173], v[34:35] op_sel:[0,0,0] op_sel_hi:[1,0,1]\n"
      "v_pk_fma_f32 v[40:41], v[8:9], v[172:173], v[40:41] op_sel:[0,0,0] op_sel_hi:[1,0,1]\n"
      "v_pk_fma_f32 v[42:43], v[10:11], v[172:173], v[42:43] op_sel:[0,0,0] op_sel_hi:[1,0,1]\n"
      "v_cvt_pk_bf16_f32 v112, v32, v33\n"
      "v_cvt_pk_bf16_f32 v114, v40, v41\n"
      "v_cvt_pk_bf16_f32 v113, v34, v35\n"
      "v_cvt_pk_bf16_f32 v115, v42, v43\n"
      "v_permlane16_swap_b32 v112, v114\n"
      "s_nop 0\n"
      "v_permlane16_swap_b32 v113, v115\n"
      "global_store_dwordx4 v196, v[112:115], s[30:31] offset:-4096 sc1 nt\n"
      "v_lshlrev_b32 v16, 16, v104\n"
      "v_and_b32 v17, 0xffff0000, v104\n"
      "v_lshlrev_b32 v18, 16, v105\n"
      "v_and_b32 v19, 0xffff0000, v105\n"
      "v_lshlrev_b32 v24, 16, v108\n"
      "v_and_b32 v25, 0xffff0000, v108\n"
      "v_lshlrev_b32 v26, 16, v109\n"
      "v_and_b32 v27, 0xffff0000, v109\n"
      "v_pk_fma_f32 v[48:49], v[16:17], v[172:173], v[48:49] op_sel:[0,0,0] op_sel_hi:[1,0,1]\n"
      "v_pk_fma_f32 v[50:51], v[18:19], v[172:173], v[50:51] op_sel:[0,0,0] op_sel_hi:[1,0,1]\n"
      "v_pk_fma_f32 v[56:57], v[24:25], v[172:173], v[56:57] op_sel:[0,0,0] op_sel_hi:[1,0,1]\n"
      "v_pk_fma_f32 v[58:59], v[26:27], v[172:173], v[58:59] op_sel:[0,0,0] op_sel_hi:[1,0,1]\n"
      "v_cvt_pk_bf16_f32 v116, v48, v49\n"
      "v_cvt_pk_bf16_f32 v118, v56, v57\n"
      "v_cvt_pk_bf16_f32 v117, v50, v51\n"
      "v_cvt_pk_bf16_f32 v119, v58, v59\n"
      "v_permlane16_swap_b32 v116, v118\n"
      "s_nop 0\n"
      "v_permlane16_swap_b32 v117, v119\n"
      "global_store_dwordx4 v196, v[116:119], s[30:31] offset:-4032 sc1 nt\n"
      "v_lshlrev_b32 v4, 16, v98\n"
      "v_and_b32 v5, 0xffff0000, v98\n"
      "v_lshlrev_b32 v6, 16, v99\n"
      "v_and_b32 v7, 0xffff0000, v99\n"
      "v_lshlrev_b32 v12, 16, v102\n"
      "v_and_b32 v13, 0xffff0000, v102\n"
      "v_lshlrev_b32 v14, 16, v103\n"
      "v_and_b32 v15, 0xffff0000, v103\n"
      "v_pk_fma_f32 v[36:37], v[4:5], v[172:173], v[36:37] op_sel:[0,1,0] op_sel_hi:[1,1,1]\n"
      "v_pk_fma_f32 v[38:39], v[6:7], v[172:173], v[38:39] op_sel:[0,1,0] op_sel_hi:[1,1,1]\n"
      "v_pk_fma_f32 v[44:45], v[12:13], v[172:173], v[44:45] op_sel:[0,1,0] op_sel_hi:[1,1,1]\n"
      "v_pk_fma_f32 v[46:47], v[14:15], v[172:173], v[46:47] op_sel:[0,1,0] op_sel_hi:[1,1,1]\n"
      "v_cvt_pk_bf16_f32 v120, v36, v37\n"
      "v_cvt_pk_bf16_f32 v122, v44, v45\n"
      "v_cvt_pk_bf16_f32 v121, v38, v39\n"
      "v_cvt_pk_bf16_f32 v123, v46, v47\n"
      "v_permlane16_swap_b32 v120, v122\n"
      "s_nop 0\n"
      "v_permlane16_swap_b32 v121, v123\n"
      "global_store_dwordx4 v196, v[120:123], s[30:31] offset:0 sc1 nt\n"
      "v_lshlrev_b32 v20, 16, v106\n"
      "v_and_b32 v21, 0xffff0000, v106\n"
      "v_lshlrev_b32 v22, 16, v107\n"
      "v_and_b32 v23, 0xffff0000, v107\n"
      "v_lshlrev_b32 v28, 16, v110\n"
      "v_and_b32 v29, 0xffff0000, v110\n"
      "v_lshlrev_b32 v30, 16, v111\n"
      "v_and_b32 v31, 0xffff0000, v111\n"
      "v_pk_fma_f32 v[52:53], v[20:21], v[172:173], v[52:53] op_sel:[0,1,0] op_sel_hi:[1,1,1]\n"
      "v_pk_fma_f32 v[54:55], v[22:23], v[172:173], v[54:55] op_sel:[0,1,0] op_sel_hi:[1,1,1]\n"
      "v_pk_fma_f32 v[60:61], v[28:29], v[172:173], v[60:61] op_sel:[0,1,0] op_sel_hi:[1,1,1]\n"
      "v_pk_fma_f32 v[62:63], v[30:31], v[172:173], v[62:63] op_sel:[0,1,0] op_sel_hi:[1,1,1]\n"
      "v_cvt_pk_bf16_f32 v124, v52, v53\n"
      "v_cvt_pk_bf16_f32 v126, v60, v61\n"
      "v_cvt_pk_bf16_f32 v125, v54, v55\n"
      "v_cvt_pk_bf16_f32 v127, v62, v63\n"
      "v_permlane16_swap_b32 v124, v126\n"
      "s_nop 0\n"
      "v_permlane16_swap_b32 v125, v127\n"
      "global_store_dwordx4 v196, v[124:127], s[30:31] offset:64 sc1 nt\n"
      "s_mov_b64 exec, 1\n"
      "global_store_dword v198, v191, s[46:47] sc1\n"
      "s_mov_b64 exec, -1\n"
      "ENDE_%=:\n"
      ".subsection 1\n"
      "MH_%=:\n"
      "s_cmp_lg_u32 s54, 0\n"
      "s_waitcnt vmcnt(12) lgkmcnt(0)\n"
      "s_cbranch_scc1 W6_%=\n"
      "s_waitcnt vmcnt(8)\n"
      "W6_%=:\n"
      "s_barrier\n"
      "v_mfma_f32_16x16x32_bf16 v[24:27], a[108:111], v[144:147], v[24:27]\n"
      "s_xor_b64 s[56:57], s[56:57], s[32:33]\n"
      "ds_read_b128 a[64:67], v242 offset:0\n"
      "ds_read_b128 a[72:75], v243 offset:0\n"
      "v_mfma_f32_16x16x32_bf16 v[32:35], a[112:115], v[144:147], v[32:35]\n"
      "s_add_u32 s61, s61, 1\n"
      "ds_read_b128 a[80:83], v242 offset:1024\n"
      "ds_read_b128 a[88:91], v243 offset:1024\n"
      "v_mfma_f32_16x16x32_bf16 v[36:39], a[112:115], v[148:151], v[36:39]\n"
      "s_add_u32 s62, s62, 0x4000\n"
      "v_exp_f32 v189, v89\n"
      "v_mfma_f32_16x16x32_bf16 a[176:179], a[112:115], v[152:155], a[176:179]\n"
      "s_and_b32 s62, s62, 0x1ffff\n"
      "v_exp_f32 v190, v90\n"
      "v_mfma_f32_16x16x32_bf16 a[180:183], a[112:115], v[156:159], a[180:183]\n"
      "v_readlane_b32 s60, v219, s61\n"
      "v_exp_f32 v191, v91\n"
      "v_mfma_f32_16x16x32_bf16 a[188:191], a[116:119], v[156:159], a[188:191]\n"
      "s_add_u32 s66, s66, s86\n"
      "v_cvt_pk_bf16_f32 v140, v188, v189\n"
      "v_mfma_f32_16x16x32_bf16 a[184:187], a[116:119], v[152:155], a[184:187]\n"
      "v_exp_f32 v192, v92\n"
      "v_mfma_f32_16x16x32_bf16 v[44:47], a[116:119], v[148:151], v[44:47]\n"
      "s_add_u32 s82, s82, s86\n"
      "v_exp_f32 v193, v93\n"
      "v_mfma_f32_16x16x32_bf16 v[40:43], a[116:119], v[144:147], v[40:43]\n"
      "s_add_u32 s65, s65, s86\n"
      "v_cvt_pk_bf16_f32 v141, v190, v191\n"
      "v_cvt_pk_bf16_f32 v142, v192, v193\n"
      "v_mfma_f32_16x16x32_bf16 v[48:51], a[120:123], v[144:147], v[48:51]\n"
      "s_max_u32 s88, s66, s65\n"
      "v_exp_f32 v194, v94\n"
      "v_mfma_f32_16x16x32_bf16 v[52:55], a[120:123], v[148:151], v[52:55]\n"
      "v_exp_f32 v195, v95\n"
      "s_waitcnt lgkmcnt(0)\n"
      "v_mfma_f32_16x16x32_bf16 a[192:195], a[120:123], v[152:155], a[192:195]\n"
      "v_cvt_pk_bf16_f32 v143, v194, v195\n"
      "v_xor_b32 v244, 0x10000, v244\n"
      "v_mfma_f32_16x16x32_bf16 a[196:199], a[120:123], v[156:159], a[196:199]\n"
      "ds_read_b128 a[68:71], v242 offset:4096\n"
      "ds_read_b128 a[76:79], v243 offset:4096\n"
      "v_mfma_f32_16x16x32_bf16 v[184:187], a[140:143], v[140:143], v[184:187]\n"
      "s_mov_b32 m0, s62\n"
      "ds_read_b128 a[84:87], v242 offset:5120\n"
      "ds_read_b128 a[92:95], v243 offset:5120\n"
      "v_mfma_f32_16x16x32_bf16 a[204:207], a[124:127], v[156:159], a[204:207]\n"
      "buffer_load_dwordx4 v228, s[56:59], s60 offen nt lds\n"
      "v_xor_b32 v245, 0x10000, v245\n"
      "v_xor_b32 v246, 0x10000, v246\n"
      "v_mfma_f32_16x16x32_bf16 a[200:203], a[124:127], v[152:155], a[200:203]\n"
      "buffer_load_dwordx4 v229, s[56:59], s60 offen offset:1024 nt lds\n"
      "v_xor_b32 v247, 0x10000, v247\n"
      "v_max3_f32 v196, v184, v185, v186\n"
      "v_mfma_f32_16x16x32_bf16 v[60:63], a[124:127], v[148:151], v[60:63]\n"
      "buffer_load_dwordx4 v230, s[56:59], s60 offen offset:2048 nt lds\n"
      "v_max_f32 v196, v196, v187\n"
      "v_cmp_nge_f32 vcc, 0x4b800000, v196\n"
      "v_mfma_f32_16x16x32_bf16 v[56:59], a[124:127], v[144:147], v[56:59]\n"
      "buffer_load_dwordx4 v231, s[56:59], s60 offen offset:3072 nt lds\n"
      "s_cbranch_vccnz FS11_%=\n"
      "FR11_%=:\n"
      "v_mfma_f32_16x16x32_bf16 v[64:67], a[64:67], a[0:3], v[160:163]\n"
      "v_exp_f32 v188, v96\n"
      "v_mfma_f32_16x16x32_bf16 v[64:67], a[72:75], a[16:19], v[64:67]\n"
      "v_add_f32 v180, v180, v184\n"
      "v_add_f32 v181, v181, v185\n"
      "v_mfma_f32_16x16x32_bf16 v[64:67], a[80:83], a[32:35], v[64:67]\n"
      "v_exp_f32 v189, v97\n"
      "v_mfma_f32_16x16x32_bf16 v[64:67], a[88:91], a[48:51], v[64:67]\n"
      "v_add_f32 v182, v182, v186\n"
      "v_add_f32 v183, v183, v187\n"
      "v_mfma_f32_16x16x32_bf16 v[72:75], a[64:67], a[4:7], v[164:167]\n"
      "v_exp_f32 v190, v98\n"
      "v_mfma_f32_16x16x32_bf16 v[72:75], a[72:75], a[20:23], v[72:75]\n"
      "v_exp_f32 v191, v99\n"
      "v_mfma_f32_16x16x32_bf16 v[72:75], a[80:83], a[36:39], v[72:75]\n"
      "v_cvt_pk_bf16_f32 v144, v188, v189\n"
      "v_cvt_pk_bf16_f32 v145, v190, v191\n"
      "v_mfma_f32_16x16x32_bf16 v[72:75], a[88:91], a[52:55], v[72:75]\n"
      "v_exp_f32 v192, v100\n"
      "v_mfma_f32_16x16x32_bf16 v[80:83], a[64:67], a[8:11], v[168:171]\n"
      "s_add_u32 s39, s39, s39\n"
      "v_exp_f32 v193, v101\n"
      "v_mfma_f32_16x16x32_bf16 v[80:83], a[72:75], a[24:27], v[80:83]\n"
      "s_memrealtime s[68:69]\n"
      "v_exp_f32 v194, v102\n"
      "v_mfma_f32_16x16x32_bf16 v[80:83], a[80:83], a[40:43], v[80:83]\n"
      "v_exp_f32 v195, v103\n"
      "v_mfma_f32_16x16x32_bf16 v[80:83], a[88:91], a[56:59], v[80:83]\n"
      "v_cvt_pk_bf16_f32 v146, v192, v193\n"
      "v_cvt_pk_bf16_f32 v147, v194, v195\n"
      "v_mfma_f32_16x16x32_bf16 v[88:91], a[64:67], a[12:15], v[172:175]\n"
      "s_nop 0\n"
      "v_mfma_f32_16x16x32_bf16 v[184:187], a[128:131], v[144:147], 0\n"
      "s_nop 0\n"
      "v_mfma_f32_16x16x32_bf16 v[88:91], a[72:75], a[28:31], v[88:91]\n"
      "s_nop 0\n"
      "v_mfma_f32_16x16x32_bf16 v[88:91], a[80:83], a[44:47], v[88:91]\n"
      "s_nop 0\n"
      "s_cbranch_scc1 SP1_%=\n"
      "s_branch HD1_%=\n"
      "HOOK_%=:\n"
      "buffer_load_dword v250, v251, s[20:23], 0 offen sc1\n"
      "s_swappc_b64 s[84:85], s[84:85]\n"
      "s_waitcnt vmcnt(12)\n"
      "v_subrev_u32 v250, s71, v250\n"
      "v_min_u32 v250, 0x258, v250\n"
      "s_nop 1\n"
      "v_add_u32_dpp v250, v250, v250 quad_perm:[1,0,3,2] row_mask:0xf bank_mask:0xf\n"
      "s_nop 1\n"
      "v_add_u32_dpp v250, v250, v250 quad_perm:[2,3,0,1] row_mask:0xf bank_mask:0xf\n"
      "s_nop 1\n"
      "v_add_u32_dpp v250, v250, v250 row_half_mirror row_mask:0xf bank_mask:0xf\n"
      "s_nop 0\n"
      "v_readfirstlane_b32 s67, v250\n"
      "s_sub_u32 s66, s66, s81\n"
      "s_lshr_b32 s67, s67, 3\n"
      "s_add_u32 s66, s66, s67\n"
      "s_max_u32 s88, s66, s65\n"
      "s_mov_b32 s86, 57\n"
      "s_mov_b32 s77, 171\n"
      "v_bfe_u32 v251, v241, 2, 7\n"
      "v_add_u32 v251, s79, v251\n"
      "buffer_load_dword v250, v251, s[20:23], 0 offen sc1\n"
      "s_swappc_b64 s[84:85], s[84:85]\n"
      "s_mov_b32 s86, 55\n"
      "s_mov_b32 s77, 165\n"
      "s_swappc_b64 s[84:85], s[84:85]\n"
      "s_mov_b32 s89, 0\n"
      "s_swappc_b64 s[84:85], s[84:85]\n"
      "s_mov_b32 s86, 54\n"
      "s_mov_b32 s77, 162\n"
      "s_swappc_b64 s[84:85], s[84:85]\n"
      "s_cmp_lt_u32 s89, 2\n"
      "s_cselect_b32 s67, 2048, s72\n"
      "s_mov_b32 s89, 0\n"
      "s_cmp_lt_u32 s67, 2048\n"
      "s_cbranch_scc0 NV0_%=\n"
      "v_and_b32 v216, 0x1f0, v241\n"
      "v_lshlrev_b32 v216, 4, v216\n"
      "v_mov_b32 v215, s82\n"
      "s_lshr_b32 s67, s67, 4\n"
      "s_add_u32 s67, s67, 8192\n"
      "buffer_store_dword v215, v216, s[20:23], s67 offen sc1\n"
      "NV0_%=:\n"
      "s_swappc_b64 s[84:85], s[84:85]\n"
      "buffer_load_dword v250, v251, s[20:23], 0 offen sc1\n"
      "s_swappc_b64 s[84:85], s[84:85]\n"
      "s_mov_b32 s86, 53\n"
      "s_mov_b32 s77, 159\n"
      "s_min_i32 s70, s70, 150\n"
      "s_waitcnt vmcnt(12)\n"
      "v_sub_u32 v250, s82, v250\n"
      "v_cmp_gt_u32 vcc, 1600, v250\n"
      "s_movk_i32 s76, 45\n"
      "s_nop 2\n"
      "s_bcnt1_i32_b32 s67, vcc_lo\n"
      "s_cmp_eq_u32 s70, 60\n"
      "s_cselect_b32 s67, 0, s67\n"
      "s_cmp_ge_u32 s67, 10\n"
      "s_cbranch_scc1 SM0_%=\n"
      "s_swappc_b64 s[84:85], s[84:85]\n"
      "s_cmp_lt_u32 s89, 4\n"
      "s_cselect_b32 s67, 2048, s72\n"
      "s_mov_b32 s89, 0\n"
      "s_cmp_lt_u32 s67, 2048\n"
      "s_cbranch_scc0 NV1_%=\n"
      "v_and_b32 v216, 0x1f0, v241\n"
      "v_lshlrev_b32 v216, 4, v216\n"
      "v_mov_b32 v215, s82\n"
      "s_lshr_b32 s67, s67, 4\n"
      "s_add_u32 s67, s67, 8192\n"
      "buffer_store_dword v215, v216, s[20:23], s67 offen sc1\n"
      "NV1_%=:\n"
      "s_swappc_b64 s[84:85], s[84:85]\n"
      "buffer_load_dword v250, v251, s[20:23], 0 offen sc1\n"
      "s_swappc_b64 s[84:85], s[84:85]\n"
      "s_waitcnt vmcnt(12)\n"
      "v_sub_u32 v250, s82, v250\n"
      "v_cmp_gt_u32 vcc, 1600, v250\n"
      "s_movk_i32 s76, 65\n"
      "s_nop 2\n"
      "s_bcnt1_i32_b32 s67, vcc_lo\n"
      "s_cmp_eq_u32 s70, 60\n"
      "s_cselect_b32 s67, 0, s67\n"
      "s_cmp_ge_u32 s67, 10\n"
      "s_cbranch_scc1 SM1_%=\n"
      "s_swappc_b64 s[84:85], s[84:85]\n"
      "s_getpc_b64 s[74:75]\n"
      "PF_%=:\n"
      "s_sub_u32 s74, s74, PF_%=-END_%=\n"
      "s_subb_u32 s75, s75, 0\n"
      "s_lshl_b32 s76, %[blk], 7\n"
      "s_and_b32 s76, s76, 0x1c00\n"
      "s_min_u32 s76, s76, ENDE_%=-END_%=-1024\n"
      "v_add_u32 v200, s76, v241\n"
      "global_load_dwordx4 v[200:203], v200, s[74:75]\n"
      "v_mov_b32 v212, 0\n"
      "global_load_dword v213, v212, s[30:31] nt\n"
      "buffer_load_dword v214, off, s[24:27], s78 nt\n"
      "s_swappc_b64 s[84:85], s[84:85]\n"
      "SM1_%=:\n"
      "s_add_u32 s66, s66, s76\n"
      "s_max_u32 s88, s66, s65\n"
      "s_min_i32 s70, s70, 150\n"
      "s_branch SM2_%=\n"
      "SM0_%=:\n"
      "s_or_b32 s39, s39, 0xc0000000\n"
      "s_mov_b32 s52, 0xaaaaaaaa\n"
      "s_mov_b32 s53, 0xaaaaaaaa\n"
      "s_add_u32 s66, s66, s76\n"
      "s_max_u32 s88, s66, s65\n"
      "s_min_i32 s70, s70, 150\n"
      "s_mov_b32 s86, 59\n"
      "s_mov_b32 s77, 177\n"
      "s_swappc_b64 s[84:85], s[84:85]\n"
      "s_mov_b32 s86, 57\n"
      "s_mov_b32 s77, 171\n"
      "s_swappc_b64 s[84:85], s[84:85]\n"
      "SM2_%=:\n"
      "s_mov_b32 s86, 55\n"
      "s_mov_b32 s77, 165\n"
      "s_mov_b32 s52, 0x0\n"
      "s_mov_b32 s53, 0x80000\n"
      "s_swappc_b64 s[84:85], s[84:85]\n"
      "s_getpc_b64 s[74:75]\n"
      "PG_%=:\n"
      "s_sub_u32 s74, s74, PG_%=-END_%=\n"
      "s_subb_u32 s75, s75, 0\n"
      "s_lshl_b32 s76, %[blk], 7\n"
      "s_and_b32 s76, s76, 0x1c00\n"
      "s_min_u32 s76, s76, ENDE_%=-END_%=-1024\n"
      "v_add_u32 v200, s76, v241\n"
      "global_load_dwordx4 v[200:203], v200, s[74:75]\n"
      "v_mov_b32 v212, 0\n"
      "global_load_dword v213, v212, s[30:31] nt\n"
      "buffer_load_dword v214, off, s[24:27], s78 nt\n"
      "s_swappc_b64 s[84:85], s[84:85]\n"
      "GW0_%=:\n"
      "s_memrealtime s[68:69]\n"
      "s_waitcnt lgkmcnt(0)\n"
      "s_sub_u32 s67, s68, s88\n"
      "s_cmp_ge_i32 s67, 0\n"
      "s_cbranch_scc1 GX0_%=\n"
      "s_branch GW0_%=\n"
      "FS1_%=:\n"
      "s_call_b64 s[96:97], FB1_%=\n"
      "s_branch FR1_%=\n"
      "FS2_%=:\n"
      "s_call_b64 s[96:97], FB0_%=\n"
      "s_branch FR2_%=\n"
      "FS3_%=:\n"
      "s_call_b64 s[96:97], FB0_%=\n"
      "s_branch FR3_%=\n"
      "GW4_%=:\n"
      "s_memrealtime s[68:69]\n"
      "s_waitcnt lgkmcnt(0)\n"
      "s_sub_u32 s67, s68, s88\n"
      "s_cmp_ge_i32 s67, 0\n"
      "s_cbranch_scc1 GX4_%=\n"
      "s_branch GW4_%=\n"
      "GW5_%=:\n"
      "s_memrealtime s[68:69]\n"
      "s_waitcnt lgkmcnt(0)\n"
      "s_sub_u32 s67, s68, s88\n"
      "s_cmp_ge_i32 s67, 0\n"
      "s_cbranch_scc1 GX5_%=\n"
      "s_branch GW5_%=\n"
      "GW6_%=:\n"
      "s_memrealtime s[68:69]\n"
      "s_waitcnt lgkmcnt(0)\n"
      "s_sub_u32 s67, s68, s88\n"
      "s_cmp_ge_i32 s67, 0\n"
      "s_cbranch_scc1 GX6_%=\n"
      "s_branch GW6_%=\n"
      "GW7_%=:\n"
      "s_memrealtime s[68:69]\n"
      "s_waitcnt lgkmcnt(0)\n"
      "s_sub_u32 s67, s68, s88\n"
      "s_cmp_ge_i32 s67, 0\n"
      "s_cbranch_scc1 GX7_%=\n"
      "s_branch GW7_%=\n"
      "FS8_%=:\n"
      "s_call_b64 s[96:97], FB1_%=\n"
      "s_branch FR8_%=\n"
      "LAG_%=:\n"
      "s_memrealtime s[68:69]\n"
      "s_waitcnt lgkmcnt(0)\n"
      "s_mov_b32 s88, s68\n"
      "LG1_%=:\n"
      "v_cmp_ne_u32 vcc, s40, v207\n"
      "s_cbranch_vccz LG2_%=\n"
      "s_sub_u32 s63, s63, 1\n"
      "s_cmp_eq_u32 s63, 0\n"
      "s_cbranch_scc1 READ_%=\n"
      "s_sleep 4\n"
      "global_load_dwordx2 v[206:207], v239, s[46:47] offset:8 sc1\n"
      "s_waitcnt vmcnt(0)\n"
      "s_branch LG1_%=\n"
      "LG2_%=:\n"
      "v_readfirstlane_b32 s67, v206\n"
      "s_sub_u32 s88, s88, s82\n"
      "s_min_u32 s88, s88, 400\n"
      "s_add_u32 s88, s88, s67\n"
      "LG3_%=:\n"
      "s_memrealtime s[68:69]\n"
      "s_waitcnt lgkmcnt(0)\n"
      "s_sub_u32 s67, s88, s68\n"
      "s_sub_u32 s67, s67, 11\n"
      "s_cmp_lt_u32 s67, 400\n"
      "s_cbranch_scc1 LG3_%=\n"
      "s_branch READ_%=\n"
      "RETRY_%=:\n"
      "s_sub_u32 s63, s63, 1\n"
      "s_cbranch_scc1 GOT_%=\n"
      "buffer_load_dwordx2 v[160:161], v240, s[24:27], s76 offen sc1\n"
      "buffer_load_dwordx4 v[96:99], v241, s[24:27], s76 offen offset:0 sc1\n"
      "buffer_load_dwordx4 v[100:103], v241, s[24:27], s76 offen offset:1024 sc1\n"
      "buffer_load_dwordx4 v[104:107], v241, s[24:27], s76 offen offset:2048 sc1\n"
      "buffer_load_dwordx4 v[108:111], v241, s[24:27], s76 offen offset:3072 sc1\n"
      "s_branch TRY_%=\n"
      "FS11_%=:\n"
      "s_call_b64 s[96:97], FB0_%=\n"
      "s_branch FR11_%=\n"
      "COLD_%=:\n"
      "FB0_%=:\n"
      "v_mov_b32 v196, 139248\n"
      "s_nop 7\n"
      "ds_write_b32 v196, v196\n"
      "v_max3_f32 v188, v64, v65, 0\n"
      "v_max3_f32 v189, v72, v73, 0\n"
      "v_max3_f32 v190, v80, v81, 0\n"
      "v_max3_f32 v191, v88, v89, 0\n"
      "v_max3_f32 v188, v188, v66, v67\n"
      "v_max3_f32 v189, v189, v74, v75\n"
      "v_max3_f32 v190, v190, v82, v83\n"
      "v_max3_f32 v191, v191, v90, v91\n"
      "v_max3_f32 v188, v188, v68, v69\n"
      "v_max3_f32 v189, v189, v76, v77\n"
      "v_max3_f32 v190, v190, v84, v85\n"
      "v_max3_f32 v191, v191, v92, v93\n"
      "v_max3_f32 v188, v188, v70, v71\n"
      "v_max3_f32 v189, v189, v78, v79\n"
      "v_max3_f32 v190, v190, v86, v87\n"
      "v_max3_f32 v191, v191, v94, v95\n"
      "v_mov_b32 v192, v188\n"
      "v_mov_b32 v193, v189\n"
      "v_mov_b32 v194, v190\n"
      "v_mov_b32 v195, v191\n"
      "v_permlane16_swap_b32 v188, v192\n"
      "v_permlane16_swap_b32 v189, v193\n"
      "v_permlane16_swap_b32 v190, v194\n"
      "v_permlane16_swap_b32 v191, v195\n"
      "v_max_f32 v188, v188, v192\n"
      "v_max_f32 v189, v189, v193\n"
      "v_max_f32 v190, v190, v194\n"
      "v_max_f32 v191, v191, v195\n"
      "v_mov_b32 v192, v188\n"
      "v_mov_b32 v193, v189\n"
      "v_mov_b32 v194, v190\n"
      "v_mov_b32 v195, v191\n"
      "v_permlane32_swap_b32 v188, v192\n"
      "v_permlane32_swap_b32 v189, v193\n"
      "v_permlane32_swap_b32 v190, v194\n"
      "v_permlane32_swap_b32 v191, v195\n"
      "v_max_f32 v188, v188, v192\n"
      "v_max_f32 v189, v189, v193\n"
      "v_max_f32 v190, v190, v194\n"
      "v_max_f32 v191, v191, v195\n"
      "RS0_%=:\n"
      "v_exp_f32 v192, -v188\n"
      "v_exp_f32 v193, -v189\n"
      "v_exp_f32 v194, -v190\n"
      "v_exp_f32 v195, -v191\n"
      "v_sub_f32 v176, v176, v188\n"
      "v_sub_f32 v177, v177, v189\n"
      "v_sub_f32 v178, v178, v190\n"
      "v_sub_f32 v179, v179, v191\n"
      "v_mov_b32 v160, v176\n"
      "v_mov_b32 v161, v176\n"
      "v_mov_b32 v162, v176\n"
      "v_mov_b32 v163, v176\n"
      "v_mov_b32 v164, v177\n"
      "v_mov_b32 v165, v177\n"
      "v_mov_b32 v166, v177\n"
      "v_mov_b32 v167, v177\n"
      "v_mov_b32 v168, v178\n"
      "v_mov_b32 v169, v178\n"
      "v_mov_b32 v170, v178\n"
      "v_mov_b32 v171, v178\n"
      "v_mov_b32 v172, v179\n"
      "v_mov_b32 v173, v179\n"
      "v_mov_b32 v174, v179\n"
      "v_mov_b32 v175, v179\n"
      "v_sub_f32 v64, v64, v188\n"
      "v_sub_f32 v65, v65, v188\n"
      "v_sub_f32 v66, v66, v188\n"
      "v_sub_f32 v67, v67, v188\n"
      "v_sub_f32 v68, v68, v188\n"
      "v_sub_f32 v69, v69, v188\n"
      "v_sub_f32 v70, v70, v188\n"
      "v_sub_f32 v71, v71, v188\n"
      "v_sub_f32 v72, v72, v189\n"
      "v_sub_f32 v73, v73, v189\n"
      "v_sub_f32 v74, v74, v189\n"
      "v_sub_f32 v75, v75, v189\n"
      "v_sub_f32 v76, v76, v189\n"
      "v_sub_f32 v77, v77, v189\n"
      "v_sub_f32 v78, v78, v189\n"
      "v_sub_f32 v79, v79, v189\n"
      "v_sub_f32 v80, v80, v190\n"
      "v_sub_f32 v81, v81, v190\n"
      "v_sub_f32 v82, v82, v190\n"
      "v_sub_f32 v83, v83, v190\n"
      "v_sub_f32 v84, v84, v190\n"
      "v_sub_f32 v85, v85, v190\n"
      "v_sub_f32 v86, v86, v190\n"
      "v_sub_f32 v87, v87, v190\n"
      "v_sub_f32 v88, v88, v191\n"
      "v_sub_f32 v89, v89, v191\n"
      "v_sub_f32 v90, v90, v191\n"
      "v_sub_f32 v91, v91, v191\n"
      "v_sub_f32 v92, v92, v191\n"
      "v_sub_f32 v93, v93, v191\n"
      "v_sub_f32 v94, v94, v191\n"
      "v_sub_f32 v95, v95, v191\n"
      "v_sub_f32 v96, v96, v188\n"
      "v_sub_f32 v97, v97, v188\n"
      "v_sub_f32 v98, v98, v188\n"
      "v_sub_f32 v99, v99, v188\n"
      "v_sub_f32 v100, v100, v188\n"
      "v_sub_f32 v101, v101, v188\n"
      "v_sub_f32 v102, v102, v188\n"
      "v_sub_f32 v103, v103, v188\n"
      "v_sub_f32 v104, v104, v189\n"
      "v_sub_f32 v105, v105, v189\n"
      "v_sub_f32 v106, v106, v189\n"
      "v_sub_f32 v107, v107, v189\n"
      "v_sub_f32 v108, v108, v189\n"
      "v_sub_f32 v109, v109, v189\n"
      "v_sub_f32 v110, v110, v189\n"
      "v_sub_f32 v111, v111, v189\n"
      "v_sub_f32 v112, v112, v190\n"
      "v_sub_f32 v113, v113, v190\n"
      "v_sub_f32 v114, v114, v190\n"
      "v_sub_f32 v115, v115, v190\n"
      "v_sub_f32 v116, v116, v190\n"
      "v_sub_f32 v117, v117, v190\n"
      "v_sub_f32 v118, v118, v190\n"
      "v_sub_f32 v119, v119, v190\n"
      "v_sub_f32 v120, v120, v191\n"
      "v_sub_f32 v121, v121, v191\n"
      "v_sub_f32 v122, v122, v191\n"
      "v_sub_f32 v123, v123, v191\n"
      "v_sub_f32 v124, v124, v191\n"
      "v_sub_f32 v125, v125, v191\n"
      "v_sub_f32 v126, v126, v191\n"
      "v_sub_f32 v127, v127, v191\n"
      "v_mul_f32 v0, v0, v192\n"
      "v_mul_f32 v1, v1, v192\n"
      "v_mul_f32 v2, v2, v192\n"
      "v_mul_f32 v3, v3, v192\n"
      "v_mul_f32 v4, v4, v193\n"
      "v_mul_f32 v5, v5, v193\n"
      "v_mul_f32 v6, v6, v193\n"
      "v_mul_f32 v7, v7, v193\n"
      "v_mul_f32 v8, v8, v192\n"
      "v_mul_f32 v9, v9, v192\n"
      "v_mul_f32 v10, v10, v192\n"
      "v_mul_f32 v11, v11, v192\n"
      "v_mul_f32 v12, v12, v193\n"
      "v_mul_f32 v13, v13, v193\n"
      "v_mul_f32 v14, v14, v193\n"
      "v_mul_f32 v15, v15, v193\n"
      "v_mul_f32 v16, v16, v192\n"
      "v_mul_f32 v17, v17, v192\n"
      "v_mul_f32 v18, v18, v192\n"
      "v_mul_f32 v19, v19, v192\n"
      "v_mul_f32 v20, v20, v193\n"
      "v_mul_f32 v21, v21, v193\n"
      "v_mul_f32 v22, v22, v193\n"
      "v_mul_f32 v23, v23, v193\n"
      "v_mul_f32 v24, v24, v192\n"
      "v_mul_f32 v25, v25, v192\n"
      "v_mul_f32 v26, v26, v192\n"
      "v_mul_f32 v27, v27, v192\n"
      "v_mul_f32 v28, v28, v193\n"
      "v_mul_f32 v29, v29, v193\n"
      "v_mul_f32 v30, v30, v193\n"
      "v_mul_f32 v31, v31, v193\n"
      "v_mul_f32 v32, v32, v192\n"
      "v_mul_f32 v33, v33, v192\n"
      "v_mul_f32 v34, v34, v192\n"
      "v_mul_f32 v35, v35, v192\n"
      "v_mul_f32 v36, v36, v193\n"
      "v_mul_f32 v37, v37, v193\n"
      "v_mul_f32 v38, v38, v193\n"
      "v_mul_f32 v39, v39, v193\n"
      "v_mul_f32 v40, v40, v192\n"
      "v_mul_f32 v41, v41, v192\n"
      "v_mul_f32 v42, v42, v192\n"
      "v_mul_f32 v43, v43, v192\n"
      "v_mul_f32 v44, v44, v193\n"
      "v_mul_f32 v45, v45, v193\n"
      "v_mul_f32 v46, v46, v193\n"
      "v_mul_f32 v47, v47, v193\n"
      "v_mul_f32 v48, v48, v192\n"
      "v_mul_f32 v49, v49, v192\n"
      "v_mul_f32 v50, v50, v192\n"
      "v_mul_f32 v51, v51, v192\n"
      "v_mul_f32 v52, v52, v193\n"
      "v_mul_f32 v53, v53, v193\n"
      "v_mul_f32 v54, v54, v193\n"
      "v_mul_f32 v55, v55, v193\n"
      "v_mul_f32 v56, v56, v192\n"
      "v_mul_f32 v57, v57, v192\n"
      "v_mul_f32 v58, v58, v192\n"
      "v_mul_f32 v59, v59, v192\n"
      "v_mul_f32 v60, v60, v193\n"
      "v_mul_f32 v61, v61, v193\n"
      "v_mul_f32 v62, v62, v193\n"
      "v_mul_f32 v63, v63, v193\n"
      "v_accvgpr_read_b32 v196, a144\n"
      "v_accvgpr_read_b32 v197, a145\n"
      "v_accvgpr_read_b32 v198, a146\n"
      "v_accvgpr_read_b32 v199, a147\n"
      "v_mul_f32 v196, v196, v194\n"
      "v_mul_f32 v197, v197, v194\n"
      "v_mul_f32 v198, v198, v194\n"
      "v_mul_f32 v199, v199, v194\n"
      "v_accvgpr_write_b32 a144, v196\n"
      "v_accvgpr_write_b32 a145, v197\n"
      "v_accvgpr_write_b32 a146, v198\n"
      "v_accvgpr_write_b32 a147, v199\n"
      "v_accvgpr_read_b32 v196, a148\n"
      "v_accvgpr_read_b32 v197, a149\n"
      "v_accvgpr_read_b32 v198, a150\n"
      "v_accvgpr_read_b32 v199, a151\n"
      "v_mul_f32 v196, v196, v195\n"
      "v_mul_f32 v197, v197, v195\n"
      "v_mul_f32 v198, v198, v195\n"
      "v_mul_f32 v199, v199, v195\n"
      "v_accvgpr_write_b32 a148, v196\n"
      "v_accvgpr_write_b32 a149, v197\n"
      "v_accvgpr_write_b32 a150, v198\n"
      "v_accvgpr_write_b32 a151, v199\n"
      "v_accvgpr_read_b32 v196, a152\n"
      "v_accvgpr_read_b32 v197, a153\n"
      "v_accvgpr_read_b32 v198, a154\n"
      "v_accvgpr_read_b32 v199, a155\n"
      "v_mul_f32 v196, v196, v194\n"
      "v_mul_f32 v197, v197, v194\n"
      "v_mul_f32 v198, v198, v194\n"
      "v_mul_f32 v199, v199, v194\n"
      "v_accvgpr_write_b32 a152, v196\n"
      "v_accvgpr_write_b32 a153, v197\n"
      "v_accvgpr_write_b32 a154, v198\n"
      "v_accvgpr_write_b32 a155, v199\n"
      "v_accvgpr_read_b32 v196, a156\n"
      "v_accvgpr_read_b32 v197, a157\n"
      "v_accvgpr_read_b32 v198, a158\n"
      "v_accvgpr_read_b32 v199, a159\n"
      "v_mul_f32 v196, v196, v195\n"
      "v_mul_f32 v197, v197, v195\n"
      "v_mul_f32 v198, v198, v195\n"
      "v_mul_f32 v199, v199, v195\n"
      "v_accvgpr_write_b32 a156, v196\n"
      "v_accvgpr_write_b32 a157, v197\n"
      "v_accvgpr_write_b32 a158, v198\n"
      "v_accvgpr_write_b32 a159, v199\n"
      "v_accvgpr_read_b32 v196, a160\n"
      "v_accvgpr_read_b32 v197, a161\n"
      "v_accvgpr_read_b32 v198, a162\n"
      "v_accvgpr_read_b32 v199, a163\n"
      "v_mul_f32 v196, v196, v194\n"
      "v_mul_f32 v197, v197, v194\n"
      "v_mul_f32 v198, v198, v194\n"
      "v_mul_f32 v199, v199, v194\n"
      "v_accvgpr_write_b32 a160, v196\n"
      "v_accvgpr_write_b32 a161, v197\n"
      "v_accvgpr_write_b32 a162, v198\n"
      "v_accvgpr_write_b32 a163, v199\n"
      "v_accvgpr_read_b32 v196, a164\n"
      "v_accvgpr_read_b32 v197, a165\n"
      "v_accvgpr_read_b32 v198, a166\n"
      "v_accvgpr_read_b32 v199, a167\n"
      "v_mul_f32 v196, v196, v195\n"
      "v_mul_f32 v197, v197, v195\n"
      "v_mul_f32 v198, v198, v195\n"
      "v_mul_f32 v199, v199, v195\n"
      "v_accvgpr_write_b32 a164, v196\n"
      "v_accvgpr_write_b32 a165, v197\n"
      "v_accvgpr_write_b32 a166, v198\n"
      "v_accvgpr_write_b32 a167, v199\n"
      "v_accvgpr_read_b32 v196, a168\n"
      "v_accvgpr_read_b32 v197, a169\n"
      "v_accvgpr_read_b32 v198, a170\n"
      "v_accvgpr_read_b32 v199, a171\n"
      "v_mul_f32 v196, v196, v194\n"
      "v_mul_f32 v197, v197, v194\n"
      "v_mul_f32 v198, v198, v194\n"
      "v_mul_f32 v199, v199, v194\n"
      "v_accvgpr_write_b32 a168, v196\n"
      "v_accvgpr_write_b32 a169, v197\n"
      "v_accvgpr_write_b32 a170, v198\n"
      "v_accvgpr_write_b32 a171, v199\n"
      "v_accvgpr_read_b32 v196, a172\n"
      "v_accvgpr_read_b32 v197, a173\n"
      "v_accvgpr_read_b32 v198, a174\n"
      "v_accvgpr_read_b32 v199, a175\n"
      "v_mul_f32 v196, v196, v195\n"
      "v_mul_f32 v197, v197, v195\n"
      "v_mul_f32 v198, v198, v195\n"
      "v_mul_f32 v199, v199, v195\n"
      "v_accvgpr_write_b32 a172, v196\n"
      "v_accvgpr_write_b32 a173, v197\n"
      "v_accvgpr_write_b32 a174, v198\n"
      "v_accvgpr_write_b32 a175, v199\n"
      "v_accvgpr_read_b32 v196, a176\n"
      "v_accvgpr_read_b32 v197, a177\n"
      "v_accvgpr_read_b32 v198, a178\n"
      "v_accvgpr_read_b32 v199, a179\n"
      "v_mul_f32 v196, v196, v194\n"
      "v_mul_f32 v197, v197, v194\n"
      "v_mul_f32 v198, v198, v194\n"
      "v_mul_f32 v199, v199, v194\n"
      "v_accvgpr_write_b32 a176, v196\n"
      "v_accvgpr_write_b32 a177, v197\n"
      "v_accvgpr_write_b32 a178, v198\n"
      "v_accvgpr_write_b32 a179, v199\n"
      "v_accvgpr_read_b32 v196, a180\n"
      "v_accvgpr_read_b32 v197, a181\n"
      "v_accvgpr_read_b32 v198, a182\n"
      "v_accvgpr_read_b32 v199, a183\n"
      "v_mul_f32 v196, v196, v195\n"
      "v_mul_f32 v197, v197, v195\n"
      "v_mul_f32 v198, v198, v195\n"
      "v_mul_f32 v199, v199, v195\n"
      "v_accvgpr_write_b32 a180, v196\n"
      "v_accvgpr_write_b32 a181, v197\n"
      "v_accvgpr_write_b32 a182, v198\n"
      "v_accvgpr_write_b32 a183, v199\n"
      "v_accvgpr_read_b32 v196, a184\n"
      "v_accvgpr_read_b32 v197, a185\n"
      "v_accvgpr_read_b32 v198, a186\n"
      "v_accvgpr_read_b32 v199, a187\n"
      "v_mul_f32 v196, v196, v194\n"
      "v_mul_f32 v197, v197, v194\n"
      "v_mul_f32 v198, v198, v194\n"
      "v_mul_f32 v199, v199, v194\n"
      "v_accvgpr_write_b32 a184, v196\n"
      "v_accvgpr_write_b32 a185, v197\n"
      "v_accvgpr_write_b32 a186, v198\n"
      "v_accvgpr_write_b32 a187, v199\n"
      "v_accvgpr_read_b32 v196, a188\n"
      "v_accvgpr_read_b32 v197, a189\n"
      "v_accvgpr_read_b32 v198, a190\n"
      "v_accvgpr_read_b32 v199, a191\n"
      "v_mul_f32 v196, v196, v195\n"
      "v_mul_f32 v197, v197, v195\n"
      "v_mul_f32 v198, v198, v195\n"
      "v_mul_f32 v199, v199, v195\n"
      "v_accvgpr_write_b32 a188, v196\n"
      "v_accvgpr_write_b32 a189, v197\n"
      "v_accvgpr_write_b32 a190, v198\n"
      "v_accvgpr_write_b32 a191, v199\n"
      "v_accvgpr_read_b32 v196, a192\n"
      "v_accvgpr_read_b32 v197, a193\n"
      "v_accvgpr_read_b32 v198, a194\n"
      "v_accvgpr_read_b32 v199, a195\n"
      "v_mul_f32 v196, v196, v194\n"
      "v_mul_f32 v197, v197, v194\n"
      "v_mul_f32 v198, v198, v194\n"
      "v_mul_f32 v199, v199, v194\n"
      "v_accvgpr_write_b32 a192, v196\n"
      "v_accvgpr_write_b32 a193, v197\n"
      "v_accvgpr_write_b32 a194, v198\n"
      "v_accvgpr_write_b32 a195, v199\n"
      "v_accvgpr_read_b32 v196, a196\n"
      "v_accvgpr_read_b32 v197, a197\n"
      "v_accvgpr_read_b32 v198, a198\n"
      "v_accvgpr_read_b32 v199, a199\n"
      "v_mul_f32 v196, v196, v195\n"
      "v_mul_f32 v197, v197, v195\n"
      "v_mul_f32 v198, v198, v195\n"
      "v_mul_f32 v199, v199, v195\n"
      "v_accvgpr_write_b32 a196, v196\n"
      "v_accvgpr_write_b32 a197, v197\n"
      "v_accvgpr_write_b32 a198, v198\n"
      "v_accvgpr_write_b32 a199, v199\n"
      "v_accvgpr_read_b32 v196, a200\n"
      "v_accvgpr_read_b32 v197, a201\n"
      "v_accvgpr_read_b32 v198, a202\n"
      "v_accvgpr_read_b32 v199, a203\n"
      "v_mul_f32 v196, v196, v194\n"
      "v_mul_f32 v197, v197, v194\n"
      "v_mul_f32 v198, v198, v194\n"
      "v_mul_f32 v199, v199, v194\n"
      "v_accvgpr_write_b32 a200, v196\n"
      "v_accvgpr_write_b32 a201, v197\n"
      "v_accvgpr_write_b32 a202, v198\n"
      "v_accvgpr_write_b32 a203, v199\n"
      "v_accvgpr_read_b32 v196, a204\n"
      "v_accvgpr_read_b32 v197, a205\n"
      "v_accvgpr_read_b32 v198, a206\n"
      "v_accvgpr_read_b32 v199, a207\n"
      "v_mul_f32 v196, v196, v195\n"
      "v_mul_f32 v197, v197, v195\n"
      "v_mul_f32 v198, v198, v195\n"
      "v_mul_f32 v199, v199, v195\n"
      "v_accvgpr_write_b32 a204, v196\n"
      "v_accvgpr_write_b32 a205, v197\n"
      "v_accvgpr_write_b32 a206, v198\n"
      "v_accvgpr_write_b32 a207, v199\n"
      "v_mul_f32 v180, v180, v192\n"
      "v_mul_f32 v181, v181, v193\n"
      "v_mul_f32 v182, v182, v194\n"
      "v_mul_f32 v183, v183, v195\n"
      "v_exp_f32 v188, v64\n"
      "v_exp_f32 v189, v65\n"
      "v_exp_f32 v190, v66\n"
      "v_exp_f32 v191, v67\n"
      "v_cvt_pk_bf16_f32 v128, v188, v189\n"
      "v_cvt_pk_bf16_f32 v129, v190, v191\n"
      "v_exp_f32 v192, v68\n"
      "v_exp_f32 v193, v69\n"
      "v_exp_f32 v194, v70\n"
      "v_exp_f32 v195, v71\n"
      "v_cvt_pk_bf16_f32 v130, v192, v193\n"
      "v_cvt_pk_bf16_f32 v131, v194, v195\n"
      "v_exp_f32 v188, v72\n"
      "v_exp_f32 v189, v73\n"
      "v_exp_f32 v190, v74\n"
      "v_exp_f32 v191, v75\n"
      "v_cvt_pk_bf16_f32 v132, v188, v189\n"
      "v_cvt_pk_bf16_f32 v133, v190, v191\n"
      "v_exp_f32 v192, v76\n"
      "v_exp_f32 v193, v77\n"
      "v_exp_f32 v194, v78\n"
      "v_exp_f32 v195, v79\n"
      "v_cvt_pk_bf16_f32 v134, v192, v193\n"
      "v_cvt_pk_bf16_f32 v135, v194, v195\n"
      "v_exp_f32 v188, v80\n"
      "v_exp_f32 v189, v81\n"
      "v_exp_f32 v190, v82\n"
      "v_exp_f32 v191, v83\n"
      "v_cvt_pk_bf16_f32 v136, v188, v189\n"
      "v_cvt_pk_bf16_f32 v137, v190, v191\n"
      "v_exp_f32 v192, v84\n"
      "v_exp_f32 v193, v85\n"
      "v_exp_f32 v194, v86\n"
      "v_exp_f32 v195, v87\n"
      "v_cvt_pk_bf16_f32 v138, v192, v193\n"
      "v_cvt_pk_bf16_f32 v139, v194, v195\n"
      "v_exp_f32 v188, v88\n"
      "v_exp_f32 v189, v89\n"
      "v_exp_f32 v190, v90\n"
      "v_exp_f32 v191, v91\n"
      "v_cvt_pk_bf16_f32 v140, v188, v189\n"
      "v_cvt_pk_bf16_f32 v141, v190, v191\n"
      "v_exp_f32 v192, v92\n"
      "v_exp_f32 v193, v93\n"
      "v_exp_f32 v194, v94\n"
      "v_exp_f32 v195, v95\n"
      "v_cvt_pk_bf16_f32 v142, v192, v193\n"
      "v_cvt_pk_bf16_f32 v143, v194, v195\n"
      "s_nop 1\n"
      "v_mfma_f32_16x16x32_bf16 v[184:187], a[128:131], v[128:131], 0\n"
      "v_mfma_f32_16x16x32_bf16 v[184:187], a[132:135], v[132:135], v[184:187]\n"
      "v_mfma_f32_16x16x32_bf16 v[184:187], a[136:139], v[136:139], v[184:187]\n"
      "v_mfma_f32_16x16x32_bf16 v[184:187], a[140:143], v[140:143], v[184:187]\n"
      "s_nop 7\n"
      "s_setpc_b64 s[96:97]\n"
      "FB1_%=:\n"
      "v_mov_b32 v196, 139248\n"
      "s_nop 7\n"
      "ds_write_b32 v196, v196\n"
      "v_max3_f32 v188, v96, v97, 0\n"
      "v_max3_f32 v189, v104, v105, 0\n"
      "v_max3_f32 v190, v112, v113, 0\n"
      "v_max3_f32 v191, v120, v121, 0\n"
      "v_max3_f32 v188, v188, v98, v99\n"
      "v_max3_f32 v189, v189, v106, v107\n"
      "v_max3_f32 v190, v190, v114, v115\n"
      "v_max3_f32 v191, v191, v122, v123\n"
      "v_max3_f32 v188, v188, v100, v101\n"
      "v_max3_f32 v189, v189, v108, v109\n"
      "v_max3_f32 v190, v190, v116, v117\n"
      "v_max3_f32 v191, v191, v124, v125\n"
      "v_max3_f32 v188, v188, v102, v103\n"
      "v_max3_f32 v189, v189, v110, v111\n"
      "v_max3_f32 v190, v190, v118, v119\n"
      "v_max3_f32 v191, v191, v126, v127\n"
      "v_mov_b32 v192, v188\n"
      "v_mov_b32 v193, v189\n"
      "v_mov_b32 v194, v190\n"
      "v_mov_b32 v195, v191\n"
      "v_permlane16_swap_b32 v188, v192\n"
      "v_permlane16_swap_b32 v189, v193\n"
      "v_permlane16_swap_b32 v190, v194\n"
      "v_permlane16_swap_b32 v191, v195\n"
      "v_max_f32 v188, v188, v192\n"
      "v_max_f32 v189, v189, v193\n"
      "v_max_f32 v190, v190, v194\n"
      "v_max_f32 v191, v191, v195\n"
      "v_mov_b32 v192, v188\n"
      "v_mov_b32 v193, v189\n"
      "v_mov_b32 v194, v190\n"
      "v_mov_b32 v195, v191\n"
      "v_permlane32_swap_b32 v188, v192\n"
      "v_permlane32_swap_b32 v189, v193\n"
      "v_permlane32_swap_b32 v190, v194\n"
      "v_permlane32_swap_b32 v191, v195\n"
      "v_max_f32 v188, v188, v192\n"
      "v_max_f32 v189, v189, v193\n"
      "v_max_f32 v190, v190, v194\n"
      "v_max_f32 v191, v191, v195\n"
      "RS1_%=:\n"
      "v_exp_f32 v192, -v188\n"
      "v_exp_f32 v193, -v189\n"
      "v_exp_f32 v194, -v190\n"
      "v_exp_f32 v195, -v191\n"
      "v_sub_f32 v176, v176, v188\n"
      "v_sub_f32 v177, v177, v189\n"
      "v_sub_f32 v178, v178, v190\n"
      "v_sub_f32 v179, v179, v191\n"
      "v_mov_b32 v160, v176\n"
      "v_mov_b32 v161, v176\n"
      "v_mov_b32 v162, v176\n"
      "v_mov_b32 v163, v176\n"
      "v_mov_b32 v164, v177\n"
      "v_mov_b32 v165, v177\n"
      "v_mov_b32 v166, v177\n"
      "v_mov_b32 v167, v177\n"
      "v_mov_b32 v168, v178\n"
      "v_mov_b32 v169, v178\n"
      "v_mov_b32 v170, v178\n"
      "v_mov_b32 v171, v178\n"
      "v_mov_b32 v172, v179\n"
      "v_mov_b32 v173, v179\n"
      "v_mov_b32 v174, v179\n"
      "v_mov_b32 v175, v179\n"
      "v_sub_f32 v64, v64, v188\n"
      "v_sub_f32 v65, v65, v188\n"
      "v_sub_f32 v66, v66, v188\n"
      "v_sub_f32 v67, v67, v188\n"
      "v_sub_f32 v68, v68, v188\n"
      "v_sub_f32 v69, v69, v188\n"
      "v_sub_f32 v70, v70, v188\n"
      "v_sub_f32 v71, v71, v188\n"
      "v_sub_f32 v72, v72, v189\n"
      "v_sub_f32 v73, v73, v189\n"
      "v_sub_f32 v74, v74, v189\n"
      "v_sub_f32 v75, v75, v189\n"
      "v_sub_f32 v76, v76, v189\n"
      "v_sub_f32 v77, v77, v189\n"
      "v_sub_f32 v78, v78, v189\n"
      "v_sub_f32 v79, v79, v189\n"
      "v_sub_f32 v80, v80, v190\n"
      "v_sub_f32 v81, v81, v190\n"
      "v_sub_f32 v82, v82, v190\n"
      "v_sub_f32 v83, v83, v190\n"
      "v_sub_f32 v84, v84, v190\n"
      "v_sub_f32 v85, v85, v190\n"
      "v_sub_f32 v86, v86, v190\n"
      "v_sub_f32 v87, v87, v190\n"
      "v_sub_f32 v88, v88, v191\n"
      "v_sub_f32 v89, v89, v191\n"
      "v_sub_f32 v90, v90, v191\n"
      "v_sub_f32 v91, v91, v191\n"
      "v_sub_f32 v92, v92, v191\n"
      "v_sub_f32 v93, v93, v191\n"
      "v_sub_f32 v94, v94, v191\n"
      "v_sub_f32 v95, v95, v191\n"
      "v_sub_f32 v96, v96, v188\n"
      "v_sub_f32 v97, v97, v188\n"
      "v_sub_f32 v98, v98, v188\n"
      "v_sub_f32 v99, v99, v188\n"
      "v_sub_f32 v100, v100, v188\n"
      "v_sub_f32 v101, v101, v188\n"
      "v_sub_f32 v102, v102, v188\n"
      "v_sub_f32 v103, v103, v188\n"
      "v_sub_f32 v104, v104, v189\n"
      "v_sub_f32 v105, v105, v189\n"
      "v_sub_f32 v106, v106, v189\n"
      "v_sub_f32 v107, v107, v189\n"
      "v_sub_f32 v108, v108, v189\n"
      "v_sub_f32 v109, v109, v189\n"
      "v_sub_f32 v110, v110, v189\n"
      "v_sub_f32 v111, v111, v189\n"
      "v_sub_f32 v112, v112, v190\n"
      "v_sub_f32 v113, v113, v190\n"
      "v_sub_f32 v114, v114, v190\n"
      "v_sub_f32 v115, v115, v190\n"
      "v_sub_f32 v116, v116, v190\n"
      "v_sub_f32 v117, v117, v190\n"
      "v_sub_f32 v118, v118, v190\n"
      "v_sub_f32 v119, v119, v190\n"
      "v_sub_f32 v120, v120, v191\n"
      "v_sub_f32 v121, v121, v191\n"
      "v_sub_f32 v122, v122, v191\n"
      "v_sub_f32 v123, v123, v191\n"
      "v_sub_f32 v124, v124, v191\n"
      "v_sub_f32 v125, v125, v191\n"
      "v_sub_f32 v126, v126, v191\n"
      "v_sub_f32 v127, v127, v191\n"
      "v_mul_f32 v0, v0, v192\n"
      "v_mul_f32 v1, v1, v192\n"
      "v_mul_f32 v2, v2, v192\n"
      "v_mul_f32 v3, v3, v192\n"
      "v_mul_f32 v4, v4, v193\n"
      "v_mul_f32 v5, v5, v193\n"
      "v_mul_f32 v6, v6, v193\n"
      "v_mul_f32 v7, v7, v193\n"
      "v_mul_f32 v8, v8, v192\n"
      "v_mul_f32 v9, v9, v192\n"
      "v_mul_f32 v10, v10, v192\n"
      "v_mul_f32 v11, v11, v192\n"
      "v_mul_f32 v12, v12, v193\n"
      "v_mul_f32 v13, v13, v193\n"
      "v_mul_f32 v14, v14, v193\n"
      "v_mul_f32 v15, v15, v193\n"
      "v_mul_f32 v16, v16, v192\n"
      "v_mul_f32 v17, v17, v192\n"
      "v_mul_f32 v18, v18, v192\n"
      "v_mul_f32 v19, v19, v192\n"
      "v_mul_f32 v20, v20, v193\n"
      "v_mul_f32 v21, v21, v193\n"
      "v_mul_f32 v22, v22, v193\n"
      "v_mul_f32 v23, v23, v193\n"
      "v_mul_f32 v24, v24, v192\n"
      "v_mul_f32 v25, v25, v192\n"
      "v_mul_f32 v26, v26, v192\n"
      "v_mul_f32 v27, v27, v192\n"
      "v_mul_f32 v28, v28, v193\n"
      "v_mul_f32 v29, v29, v193\n"
      "v_mul_f32 v30, v30, v193\n"
      "v_mul_f32 v31, v31, v193\n"
      "v_mul_f32 v32, v32, v192\n"
      "v_mul_f32 v33, v33, v192\n"
      "v_mul_f32 v34, v34, v192\n"
      "v_mul_f32 v35, v35, v192\n"
      "v_mul_f32 v36, v36, v193\n"
      "v_mul_f32 v37, v37, v193\n"
      "v_mul_f32 v38, v38, v193\n"
      "v_mul_f32 v39, v39, v193\n"
      "v_mul_f32 v40, v40, v192\n"
      "v_mul_f32 v41, v41, v192\n"
      "v_mul_f32 v42, v42, v192\n"
      "v_mul_f32 v43, v43, v192\n"
      "v_mul_f32 v44, v44, v193\n"
      "v_mul_f32 v45, v45, v193\n"
      "v_mul_f32 v46, v46, v193\n"
      "v_mul_f32 v47, v47, v193\n"
      "v_mul_f32 v48, v48, v192\n"
      "v_mul_f32 v49, v49, v192\n"
      "v_mul_f32 v50, v50, v192\n"
      "v_mul_f32 v51, v51, v192\n"
      "v_mul_f32 v52, v52, v193\n"
      "v_mul_f32 v53, v53, v193\n"
      "v_mul_f32 v54, v54, v193\n"
      "v_mul_f32 v55, v55, v193\n"
      "v_mul_f32 v56, v56, v192\n"
      "v_mul_f32 v57, v57, v192\n"
      "v_mul_f32 v58, v58, v192\n"
      "v_mul_f32 v59, v59, v192\n"
      "v_mul_f32 v60, v60, v193\n"
      "v_mul_f32 v61, v61, v193\n"
      "v_mul_f32 v62, v62, v193\n"
      "v_mul_f32 v63, v63, v193\n"
      "v_accvgpr_read_b32 v196, a144\n"
      "v_accvgpr_read_b32 v197, a145\n"
      "v_accvgpr_read_b32 v198, a146\n"
      "v_accvgpr_read_b32 v199, a147\n"
      "v_mul_f32 v196, v196, v194\n"
      "v_mul_f32 v197, v197, v194\n"
      "v_mul_f32 v198, v198, v194\n"
      "v_mul_f32 v199, v199, v194\n"
      "v_accvgpr_write_b32 a144, v196\n"
      "v_accvgpr_write_b32 a145, v197\n"
      "v_accvgpr_write_b32 a146, v198\n"
      "v_accvgpr_write_b32 a147, v199\n"
      "v_accvgpr_read_b32 v196, a148\n"
      "v_accvgpr_read_b32 v197, a149\n"
      "v_accvgpr_read_b32 v198, a150\n"
      "v_accvgpr_read_b32 v199, a151\n"
      "v_mul_f32 v196, v196, v195\n"
      "v_mul_f32 v197, v197, v195\n"
      "v_mul_f32 v198, v198, v195\n"
      "v_mul_f32 v199, v199, v195\n"
      "v_accvgpr_write_b32 a148, v196\n"
      "v_accvgpr_write_b32 a149, v197\n"
      "v_accvgpr_write_b32 a150, v198\n"
      "v_accvgpr_write_b32 a151, v199\n"
      "v_accvgpr_read_b32 v196, a152\n"
      "v_accvgpr_read_b32 v197, a153\n"
      "v_accvgpr_read_b32 v198, a154\n"
      "v_accvgpr_read_b32 v199, a155\n"
      "v_mul_f32 v196, v196, v194\n"
      "v_mul_f32 v197, v197, v194\n"
      "v_mul_f32 v198, v198, v194\n"
      "v_mul_f32 v199, v199, v194\n"
      "v_accvgpr_write_b32 a152, v196\n"
      "v_accvgpr_write_b32 a153, v197\n"
      "v_accvgpr_write_b32 a154, v198\n"
      "v_accvgpr_write_b32 a155, v199\n"
      "v_accvgpr_read_b32 v196, a156\n"
      "v_accvgpr_read_b32 v197, a157\n"
      "v_accvgpr_read_b32 v198, a158\n"
      "v_accvgpr_read_b32 v199, a159\n"
      "v_mul_f32 v196, v196, v195\n"
      "v_mul_f32 v197, v197, v195\n"
      "v_mul_f32 v198, v198, v195\n"
      "v_mul_f32 v199, v199, v195\n"
      "v_accvgpr_write_b32 a156, v196\n"
      "v_accvgpr_write_b32 a157, v197\n"
      "v_accvgpr_write_b32 a158, v198\n"
      "v_accvgpr_write_b32 a159, v199\n"
      "v_accvgpr_read_b32 v196, a160\n"
      "v_accvgpr_read_b32 v197, a161\n"
      "v_accvgpr_read_b32 v198, a162\n"
      "v_accvgpr_read_b32 v199, a163\n"
      "v_mul_f32 v196, v196, v194\n"
      "v_mul_f32 v197, v197, v194\n"
      "v_mul_f32 v198, v198, v194\n"
      "v_mul_f32 v199, v199, v194\n"
      "v_accvgpr_write_b32 a160, v196\n"
      "v_accvgpr_write_b32 a161, v197\n"
      "v_accvgpr_write_b32 a162, v198\n"
      "v_accvgpr_write_b32 a163, v199\n"
      "v_accvgpr_read_b32 v196, a164\n"
      "v_accvgpr_read_b32 v197, a165\n"
      "v_accvgpr_read_b32 v198, a166\n"
      "v_accvgpr_read_b32 v199, a167\n"
      "v_mul_f32 v196, v196, v195\n"
      "v_mul_f32 v197, v197, v195\n"
      "v_mul_f32 v198, v198, v195\n"
      "v_mul_f32 v199, v199, v195\n"
      "v_accvgpr_write_b32 a164, v196\n"
      "v_accvgpr_write_b32 a165, v197\n"
      "v_accvgpr_write_b32 a166, v198\n"
      "v_accvgpr_write_b32 a167, v199\n"
      "v_accvgpr_read_b32 v196, a168\n"
      "v_accvgpr_read_b32 v197, a169\n"
      "v_accvgpr_read_b32 v198, a170\n"
      "v_accvgpr_read_b32 v199, a171\n"
      "v_mul_f32 v196, v196, v194\n"
      "v_mul_f32 v197, v197, v194\n"
      "v_mul_f32 v198, v198, v194\n"
      "v_mul_f32 v199, v199, v194\n"
      "v_accvgpr_write_b32 a168, v196\n"
      "v_accvgpr_write_b32 a169, v197\n"
      "v_accvgpr_write_b32 a170, v198\n"
      "v_accvgpr_write_b32 a171, v199\n"
      "v_accvgpr_read_b32 v196, a172\n"
      "v_accvgpr_read_b32 v197, a173\n"
      "v_accvgpr_read_b32 v198, a174\n"
      "v_accvgpr_read_b32 v199, a175\n"
      "v_mul_f32 v196, v196, v195\n"
      "v_mul_f32 v197, v197, v195\n"
      "v_mul_f32 v198, v198, v195\n"
      "v_mul_f32 v199, v199, v195\n"
      "v_accvgpr_write_b32 a172, v196\n"
      "v_accvgpr_write_b32 a173, v197\n"
      "v_accvgpr_write_b32 a174, v198\n"
      "v_accvgpr_write_b32 a175, v199\n"
      "v_accvgpr_read_b32 v196, a176\n"
      "v_accvgpr_read_b32 v197, a177\n"
      "v_accvgpr_read_b32 v198, a178\n"
      "v_accvgpr_read_b32 v199, a179\n"
      "v_mul_f32 v196, v196, v194\n"
      "v_mul_f32 v197, v197, v194\n"
      "v_mul_f32 v198, v198, v194\n"
      "v_mul_f32 v199, v199, v194\n"
      "v_accvgpr_write_b32 a176, v196\n"
      "v_accvgpr_write_b32 a177, v197\n"
      "v_accvgpr_write_b32 a178, v198\n"
      "v_accvgpr_write_b32 a179, v199\n"
      "v_accvgpr_read_b32 v196, a180\n"
      "v_accvgpr_read_b32 v197, a181\n"
      "v_accvgpr_read_b32 v198, a182\n"
      "v_accvgpr_read_b32 v199, a183\n"
      "v_mul_f32 v196, v196, v195\n"
      "v_mul_f32 v197, v197, v195\n"
      "v_mul_f32 v198, v198, v195\n"
      "v_mul_f32 v199, v199, v195\n"
      "v_accvgpr_write_b32 a180, v196\n"
      "v_accvgpr_write_b32 a181, v197\n"
      "v_accvgpr_write_b32 a182, v198\n"
      "v_accvgpr_write_b32 a183, v199\n"
      "v_accvgpr_read_b32 v196, a184\n"
      "v_accvgpr_read_b32 v197, a185\n"
      "v_accvgpr_read_b32 v198, a186\n"
      "v_accvgpr_read_b32 v199, a187\n"
      "v_mul_f32 v196, v196, v194\n"
      "v_mul_f32 v197, v197, v194\n"
      "v_mul_f32 v198, v198, v194\n"
      "v_mul_f32 v199, v199, v194\n"
      "v_accvgpr_write_b32 a184, v196\n"
      "v_accvgpr_write_b32 a185, v197\n"
      "v_accvgpr_write_b32 a186, v198\n"
      "v_accvgpr_write_b32 a187, v199\n"
      "v_accvgpr_read_b32 v196, a188\n"
      "v_accvgpr_read_b32 v197, a189\n"
      "v_accvgpr_read_b32 v198, a190\n"
      "v_accvgpr_read_b32 v199, a191\n"
      "v_mul_f32 v196, v196, v195\n"
      "v_mul_f32 v197, v197, v195\n"
      "v_mul_f32 v198, v198, v195\n"
      "v_mul_f32 v199, v199, v195\n"
      "v_accvgpr_write_b32 a188, v196\n"
      "v_accvgpr_write_b32 a189, v197\n"
      "v_accvgpr_write_b32 a190, v198\n"
      "v_accvgpr_write_b32 a191, v199\n"
      "v_accvgpr_read_b32 v196, a192\n"
      "v_accvgpr_read_b32 v197, a193\n"
      "v_accvgpr_read_b32 v198, a194\n"
      "v_accvgpr_read_b32 v199, a195\n"
      "v_mul_f32 v196, v196, v194\n"
      "v_mul_f32 v197, v197, v194\n"
      "v_mul_f32 v198, v198, v194\n"
      "v_mul_f32 v199, v199, v194\n"
      "v_accvgpr_write_b32 a192, v196\n"
      "v_accvgpr_write_b32 a193, v197\n"
      "v_accvgpr_write_b32 a194, v198\n"
      "v_accvgpr_write_b32 a195, v199\n"
      "v_accvgpr_read_b32 v196, a196\n"
      "v_accvgpr_read_b32 v197, a197\n"
      "v_accvgpr_read_b32 v198, a198\n"
      "v_accvgpr_read_b32 v199, a199\n"
      "v_mul_f32 v196, v196, v195\n"
      "v_mul_f32 v197, v197, v195\n"
      "v_mul_f32 v198, v198, v195\n"
      "v_mul_f32 v199, v199, v195\n"
      "v_accvgpr_write_b32 a196, v196\n"
      "v_accvgpr_write_b32 a197, v197\n"
      "v_accvgpr_write_b32 a198, v198\n"
      "v_accvgpr_write_b32 a199, v199\n"
      "v_accvgpr_read_b32 v196, a200\n"
      "v_accvgpr_read_b32 v197, a201\n"
      "v_accvgpr_read_b32 v198, a202\n"
      "v_accvgpr_read_b32 v199, a203\n"
      "v_mul_f32 v196, v196, v194\n"
      "v_mul_f32 v197, v197, v194\n"
      "v_mul_f32 v198, v198, v194\n"
      "v_mul_f32 v199, v199, v194\n"
      "v_accvgpr_write_b32 a200, v196\n"
      "v_accvgpr_write_b32 a201, v197\n"
      "v_accvgpr_write_b32 a202, v198\n"
      "v_accvgpr_write_b32 a203, v199\n"
      "v_accvgpr_read_b32 v196, a204\n"
      "v_accvgpr_read_b32 v197, a205\n"
      "v_accvgpr_read_b32 v198, a206\n"
      "v_accvgpr_read_b32 v199, a207\n"
      "v_mul_f32 v196, v196, v195\n"
      "v_mul_f32 v197, v197, v195\n"
      "v_mul_f32 v198, v198, v195\n"
      "v_mul_f32 v199, v199, v195\n"
      "v_accvgpr_write_b32 a204, v196\n"
      "v_accvgpr_write_b32 a205, v197\n"
      "v_accvgpr_write_b32 a206, v198\n"
      "v_accvgpr_write_b32 a207, v199\n"
      "v_mul_f32 v180, v180, v192\n"
      "v_mul_f32 v181, v181, v193\n"
      "v_mul_f32 v182, v182, v194\n"
      "v_mul_f32 v183, v183, v195\n"
      "v_lshlrev_b32 v196, 16, v128\n"
      "v_and_b32 v197, 0xffff0000, v128\n"
      "v_mul_f32 v196, v196, v192\n"
      "v_mul_f32 v197, v197, v192\n"
      "v_cvt_pk_bf16_f32 v128, v196, v197\n"
      "v_lshlrev_b32 v196, 16, v129\n"
      "v_and_b32 v197, 0xffff0000, v129\n"
      "v_mul_f32 v196, v196, v192\n"
      "v_mul_f32 v197, v197, v192\n"
      "v_cvt_pk_bf16_f32 v129, v196, v197\n"
      "v_lshlrev_b32 v196, 16, v130\n"
      "v_and_b32 v197, 0xffff0000, v130\n"
      "v_mul_f32 v196, v196, v192\n"
      "v_mul_f32 v197, v197, v192\n"
      "v_cvt_pk_bf16_f32 v130, v196, v197\n"
      "v_lshlrev_b32 v196, 16, v131\n"
      "v_and_b32 v197, 0xffff0000, v131\n"
      "v_mul_f32 v196, v196, v192\n"
      "v_mul_f32 v197, v197, v192\n"
      "v_cvt_pk_bf16_f32 v131, v196, v197\n"
      "v_lshlrev_b32 v196, 16, v132\n"
      "v_and_b32 v197, 0xffff0000, v132\n"
      "v_mul_f32 v196, v196, v193\n"
      "v_mul_f32 v197, v197, v193\n"
      "v_cvt_pk_bf16_f32 v132, v196, v197\n"
      "v_lshlrev_b32 v196, 16, v133\n"
      "v_and_b32 v197, 0xffff0000, v133\n"
      "v_mul_f32 v196, v196, v193\n"
      "v_mul_f32 v197, v197, v193\n"
      "v_cvt_pk_bf16_f32 v133, v196, v197\n"
      "v_lshlrev_b32 v196, 16, v134\n"
      "v_and_b32 v197, 0xffff0000, v134\n"
      "v_mul_f32 v196, v196, v193\n"
      "v_mul_f32 v197, v197, v193\n"
      "v_cvt_pk_bf16_f32 v134, v196, v197\n"
      "v_lshlrev_b32 v196, 16, v135\n"
      "v_and_b32 v197, 0xffff0000, v135\n"
      "v_mul_f32 v196, v196, v193\n"
      "v_mul_f32 v197, v197, v193\n"
      "v_cvt_pk_bf16_f32 v135, v196, v197\n"
      "v_lshlrev_b32 v196, 16, v136\n"
      "v_and_b32 v197, 0xffff0000, v136\n"
      "v_mul_f32 v196, v196, v194\n"
      "v_mul_f32 v197, v197, v194\n"
      "v_cvt_pk_bf16_f32 v136, v196, v197\n"
      "v_lshlrev_b32 v196, 16, v137\n"
      "v_and_b32 v197, 0xffff0000, v137\n"
      "v_mul_f32 v196, v196, v194\n"
      "v_mul_f32 v197, v197, v194\n"
      "v_cvt_pk_bf16_f32 v137, v196, v197\n"
      "v_lshlrev_b32 v196, 16, v138\n"
      "v_and_b32 v197, 0xffff0000, v138\n"
      "v_mul_f32 v196, v196, v194\n"
      "v_mul_f32 v197, v197, v194\n"
      "v_cvt_pk_bf16_f32 v138, v196, v197\n"
      "v_lshlrev_b32 v196, 16, v139\n"
      "v_and_b32 v197, 0xffff0000, v139\n"
      "v_mul_f32 v196, v196, v194\n"
      "v_mul_f32 v197, v197, v194\n"
      "v_cvt_pk_bf16_f32 v139, v196, v197\n"
      "v_lshlrev_b32 v196, 16, v140\n"
      "v_and_b32 v197, 0xffff0000, v140\n"
      "v_mul_f32 v196, v196, v195\n"
      "v_mul_f32 v197, v197, v195\n"
      "v_cvt_pk_bf16_f32 v140, v196, v197\n"
      "v_lshlrev_b32 v196, 16, v141\n"
      "v_and_b32 v197, 0xffff0000, v141\n"
      "v_mul_f32 v196, v196, v195\n"
      "v_mul_f32 v197, v197, v195\n"
      "v_cvt_pk_bf16_f32 v141, v196, v197\n"
      "v_lshlrev_b32 v196, 16, v142\n"
      "v_and_b32 v197, 0xffff0000, v142\n"
      "v_mul_f32 v196, v196, v195\n"
      "v_mul_f32 v197, v197, v195\n"
      "v_cvt_pk_bf16_f32 v142, v196, v197\n"
      "v_lshlrev_b32 v196, 16, v143\n"
      "v_and_b32 v197, 0xffff0000, v143\n"
      "v_mul_f32 v196, v196, v195\n"
      "v_mul_f32 v197, v197, v195\n"
      "v_cvt_pk_bf16_f32 v143, v196, v197\n"
      "v_exp_f32 v188, v96\n"
      "v_exp_f32 v189, v97\n"
      "v_exp_f32 v190, v98\n"
      "v_exp_f32 v191, v99\n"
      "v_cvt_pk_bf16_f32 v144, v188, v189\n"
      "v_cvt_pk_bf16_f32 v145, v190, v191\n"
      "v_exp_f32 v192, v100\n"
      "v_exp_f32 v193, v101\n"
      "v_exp_f32 v194, v102\n"
      "v_exp_f32 v195, v103\n"
      "v_cvt_pk_bf16_f32 v146, v192, v193\n"
      "v_cvt_pk_bf16_f32 v147, v194, v195\n"
      "v_exp_f32 v188, v104\n"
      "v_exp_f32 v189, v105\n"
      "v_exp_f32 v190, v106\n"
      "v_exp_f32 v191, v107\n"
      "v_cvt_pk_bf16_f32 v148, v188, v189\n"
      "v_cvt_pk_bf16_f32 v149, v190, v191\n"
      "v_exp_f32 v192, v108\n"
      "v_exp_f32 v193, v109\n"
      "v_exp_f32 v194, v110\n"
      "v_exp_f32 v195, v111\n"
      "v_cvt_pk_bf16_f32 v150, v192, v193\n"
      "v_cvt_pk_bf16_f32 v151, v194, v195\n"
      "v_exp_f32 v188, v112\n"
      "v_exp_f32 v189, v113\n"
      "v_exp_f32 v190, v114\n"
      "v_exp_f32 v191, v115\n"
      "v_cvt_pk_bf16_f32 v152, v188, v189\n"
      "v_cvt_pk_bf16_f32 v153, v190, v191\n"
      "v_exp_f32 v192, v116\n"
      "v_exp_f32 v193, v117\n"
      "v_exp_f32 v194, v118\n"
      "v_exp_f32 v195, v119\n"
      "v_cvt_pk_bf16_f32 v154, v192, v193\n"
      "v_cvt_pk_bf16_f32 v155, v194, v195\n"
      "v_exp_f32 v188, v120\n"
      "v_exp_f32 v189, v121\n"
      "v_exp_f32 v190, v122\n"
      "v_exp_f32 v191, v123\n"
      "v_cvt_pk_bf16_f32 v156, v188, v189\n"
      "v_cvt_pk_bf16_f32 v157, v190, v191\n"
      "v_exp_f32 v192, v124\n"
      "v_exp_f32 v193, v125\n"
      "v_exp_f32 v194, v126\n"
      "v_exp_f32 v195, v127\n"
      "v_cvt_pk_bf16_f32 v158, v192, v193\n"
      "v_cvt_pk_bf16_f32 v159, v194, v195\n"
      "s_nop 1\n"
      "v_mfma_f32_16x16x32_bf16 v[184:187], a[128:131], v[144:147], 0\n"
      "v_mfma_f32_16x16x32_bf16 v[184:187], a[132:135], v[148:151], v[184:187]\n"
      "v_mfma_f32_16x16x32_bf16 v[184:187], a[136:139], v[152:155], v[184:187]\n"
      "v_mfma_f32_16x16x32_bf16 v[184:187], a[140:143], v[156:159], v[184:187]\n"
      "s_nop 7\n"
      "s_setpc_b64 s[96:97]\n"
      "SLOW_%=:\n"
      "ds_write_b128 v234, v[176:179] offset:5120\n"
      "s_waitcnt lgkmcnt(0)\n"
      "s_barrier\n"
      "ds_read_b128 v[192:195], v235 offset:5120\n"
      "s_waitcnt lgkmcnt(0)\n"
      "v_sub_f32 v188, v176, v194\n"
      "v_sub_f32 v189, v177, v195\n"
      "v_sub_f32 v190, v178, v192\n"
      "v_sub_f32 v191, v179, v193\n"
      "v_max_f32 v188, 0, v188\n"
      "v_max_f32 v189, 0, v189\n"
      "v_max_f32 v190, 0, v190\n"
      "v_max_f32 v191, 0, v191\n"
      "s_call_b64 s[96:97], RS1_%=\n"
      "ds_write_b128 v232, a[176:179] offset:0\n"
      "ds_write_b128 v232, a[180:183] offset:1024\n"
      "ds_write_b128 v232, a[184:187] offset:2048\n"
      "ds_write_b128 v232, a[188:191] offset:3072\n"
      "ds_write_b128 v232, a[192:195] offset:4096\n"
      "ds_write_b128 v232, a[196:199] offset:5120\n"
      "ds_write_b128 v232, a[200:203] offset:6144\n"
      "ds_write_b128 v232, a[204:207] offset:7168\n"
      "ds_write_b128 v234, v[152:155] offset:0\n"
      "ds_write_b128 v234, v[156:159] offset:1024\n"
      "v_cndmask_b32 v229, v180, v181, s[74:75]\n"
      "v_cndmask_b32 v200, v182, v183, s[74:75]\n"
      "v_cndmask_b32 v228, v176, v177, s[74:75]\n"
      "v_cndmask_b32 v202, v186, v187, s[74:75]\n"
      "v_cndmask_b32 v201, v184, v185, s[74:75]\n"
      "v_add_f32 v200, v200, v202\n"
      "ds_write_b32 v234, v200 offset:4096\n"
      "v_add_f32 v229, v229, v201\n"
      "s_waitcnt lgkmcnt(0)\n"
      "s_barrier\n"
      "ds_read_b32 v208, v209\n"
      "ds_read_b128 v[160:163], v235 offset:0\n"
      "ds_read_b128 v[164:167], v235 offset:1024\n"
      "ds_read_b128 v[64:67], v233 offset:0\n"
      "ds_read_b128 v[68:71], v233 offset:1024\n"
      "ds_read_b128 v[72:75], v233 offset:2048\n"
      "ds_read_b128 v[76:79], v233 offset:3072\n"
      "ds_read_b128 v[80:83], v233 offset:4096\n"
      "ds_read_b128 v[84:87], v233 offset:5120\n"
      "ds_read_b128 v[88:91], v233 offset:6144\n"
      "ds_read_b128 v[92:95], v233 offset:7168\n"
      "ds_read_b32 v204, v235 offset:4096\n"
      "s_branch AGAIN_%=\n"
      ".subsection 0\n"
      : "+{v0}"(tid)
      : [blk] "s"(blockIdx.x), [kp] "s"(__builtin_amdgcn_kernarg_segment_ptr()), "s"(lds0)
      : "v1", "v2", "v3", "v4", "v5", "v6", "v7", "v8", "v9", "v10", "v11", "v12", "v13", "v14", "v15", "v16", "v17", "v18", "v19", "v20", "v21", "v22", "v23", "v24", "v25", "v26", "v27", "v28", "v29", "v30", "v31", "v32", "v33", "v34", "v35", "v36", "v37", "v38", "v39", "v40", "v41", "v42", "v43", "v44", "v45", "v46", "v47", "v48", "v49", "v50", "v51", "v52", "v53", "v54", "v55", "v56", "v57", "v58", "v59", "v60", "v61", "v62", "v63", "v64", "v65", "v66", "v67", "v68", "v69", "v70", "v71", "v72", "v73", "v74", "v75", "v76", "v77", "v78", "v79", "v80", "v81", "v82", "v83", "v84", "v85", "v86", "v87", "v88", "v89", "v90", "v91", "v92", "v93", "v94", "v95", "v96", "v97", "v98", "v99", "v100", "v101", "v102", "v103", "v104", "v105", "v106", "v107", "v108", "v109", "v110", "v111", "v112", "v113", "v114", "v115", "v116", "v117", "v118", "v119", "v120", "v121", "v122", "v123", "v124", "v125", "v126", "v127", "v128", "v129", "v130", "v131", "v132", "v133", "v134", "v135", "v136", "v137", "v138", "v139", "v140", "v141", "v142", "v143", "v144", "v145", "v146", "v147", "v148", "v149", "v150", "v151", "v152", "v153", "v154", "v155", "v156", "v157", "v158", "v159", "v160", "v161", "v162", "v163", "v164", "v165", "v166", "v167", "v168", "v169", "v170", "v171", "v172", "v173", "v174", "v175", "v176", "v177", "v178", "v179", "v180", "v181", "v182", "v183", "v184", "v185", "v186", "v187", "v188", "v189", "v190", "v191", "v192", "v193", "v194", "v195", "v196", "v197", "v198", "v199", "v200", "v201", "v202", "v203", "v204", "v205", "v206", "v207", "v208", "v209", "v210", "v211", "v212", "v213", "v214", "v215", "v216", "v217", "v218", "v219", "v220", "v221", "v222", "v223", "v224", "v225", "v226", "v227", "v228", "v229", "v230", "v231", "v232", "v233", "v234", "v235", "v236", "v237", "v238", "v239", "v240", "v241", "v242", "v243", "v244", "v245", "v246", "v247", "v248", "v249", "v250", "v251", "a0", "a1", "a2", "a3", "a4", "a5", "a6", "a7", "a8", "a9", "a10", "a11", "a12", "a13", "a14", "a15", "a16", "a17", "a18", "a19", "a20", "a21", "a22", "a23", "a24", "a25", "a26", "a27", "a28", "a29", "a30", "a31", "a32", "a33", "a34", "a35", "a36", "a37", "a38", "a39", "a40", "a41", "a42", "a43", "a44", "a45", "a46", "a47", "a48", "a49", "a50", "a51", "a52", "a53", "a54", "a55", "a56", "a57", "a58", "a59", "a60", "a61", "a62", "a63", "a64", "a65", "a66", "a67", "a68", "a69", "a70", "a71", "a72", "a73", "a74", "a75", "a76", "a77", "a78", "a79", "a80", "a81", "a82", "a83", "a84", "a85", "a86", "a87", "a88", "a89", "a90", "a91", "a92", "a93", "a94", "a95", "a96", "a97", "a98", "a99", "a100", "a101", "a102", "a103", "a104", "a105", "a106", "a107", "a108", "a109", "a110", "a111", "a112", "a113", "a114", "a115", "a116", "a117", "a118", "a119", "a120", "a121", "a122", "a123", "a124", "a125", "a126", "a127", "a128", "a129", "a130", "a131", "a132", "a133", "a134", "a135", "a136", "a137", "a138", "a139", "a140", "a141", "a142", "a143", "a144", "a145", "a146", "a147", "a148", "a149", "a150", "a151", "a152", "a153", "a154", "a155", "a156", "a157", "a158", "a159", "a160", "a161", "a162", "a163", "a164", "a165", "a166", "a167", "a168", "a169", "a170", "a171", "a172", "a173", "a174", "a175", "a176", "a177", "a178", "a179", "a180", "a181", "a182", "a183", "a184", "a185", "a186", "a187", "a188", "a189", "a190", "a191", "a192", "a193", "a194", "a195", "a196", "a197", "a198", "a199", "a200", "a201", "a202", "a203", "a204", "a205", "a206", "a207", "s4", "s5", "s6", "s7", "s8", "s9", "s10", "s11", "s12", "s13", "s14", "s15", "s16", "s17", "s18", "s19", "s20", "s21", "s22", "s23", "s24", "s25", "s26", "s27", "s28", "s29", "s30", "s31", "s32", "s33", "s34", "s35", "s38", "s39", "s40", "s43", "s44", "s45", "s46", "s47", "s50", "s52", "s53", "s54", "s56", "s57", "s58", "s59", "s60", "s61", "s62", "s63", "s64", "s65", "s66", "s67", "s68", "s69", "s70", "s71", "s72", "s74", "s75", "s76", "s77", "s78", "s79", "s80", "s81", "s82", "s83", "s84", "s85", "s86", "s88", "s89", "s90", "s91", "s92", "s93", "s94", "s95", "s96", "s97", "vcc", "scc", "memory");
}

}

extern "C" void kernel_launch(void* const* d_in, const int* in_sizes, int n_in, void* d_out, int out_size,
                              void* d_ws, size_t ws_size, hipStream_t stream) {
  if (n_in != 6 || ws_size < WS_END) return;
  const __hip_bfloat16* q = (const __hip_bfloat16*)d_in[0];
  const __hip_bfloat16* kc = (const __hip_bfloat16*)d_in[1];
  const __hip_bfloat16* vc = (const __hip_bfloat16*)d_in[2];
  const __hip_bfloat16* kt = (const __hip_bfloat16*)d_in[3];
  const __hip_bfloat16* vt = (const __hip_bfloat16*)d_in[4];
  const __hip_bfloat16* tm = (const __hip_bfloat16*)d_in[5];
  hipLaunchKernelGGL(tree_attn_main, dim3(B_ * HKV_ * 2), dim3(256), 0, stream, q, kc, vc, kt, vt, tm,
                     (__hip_bfloat16*)d_out, (char*)d_ws);
}
```

```cpp
#include <hip/hip_runtime.h>
#include <hip/hip_bf16.h>
#include <stdint.h>

#pragma clang diagnostic ignored "-Wint-to-pointer-cast"
#pragma clang diagnostic ignored "-Winline-asm"

namespace {

constexpr int B_ = 16, HKV_ = 8, D_ = 128;
constexpr int SLOT = 64 * D_ * 2;
constexpr int LDS_BYTES = 10 * SLOT;

constexpr size_t WS_EPOCH = 0;
constexpr size_t WS_REF = WS_EPOCH + 128 * 4 * 128;
constexpr size_t WS_X = WS_REF + 16384;
constexpr size_t XW_BYTES = 4608;
constexpr size_t WS_END = WS_X + (size_t)128 * 2 * 4 * XW_BYTES;

__global__ __launch_bounds__(256, 1)
void tree_attn_main(const __hip_bfloat16*, const __hip_bfloat16*, const __hip_bfloat16*, const __hip_bfloat16*,
                    const __hip_bfloat16*, const __hip_bfloat16*, __hip_bfloat16*, char*) {
  __shared__ __attribute__((aligned(1024))) char smem[LDS_BYTES];
  const unsigned lds0 = (unsigned)(uintptr_t)smem;
  unsigned tid = threadIdx.x;
  asm volatile(
      "s_load_dwordx16 s[4:19], %[kp], 0x0\n"
      "s_getpc_b64 s[60:61]\n"
      "v_lshlrev_b32 v241, 4, v0\n"
      "s_lshl_b32 s62, %[blk], 9\n"
      "s_and_b32 s62, s62, 0x3000\n"
      "s_add_u32 s60, s60, s62\n"
      "s_addc_u32 s61, s61, 0\n"
      "global_load_dwordx4 v[236:239], v241, s[60:61]\n"
      "v_and_b32 v1, 0xc0, v0\n"
      "v_lshlrev_b32 v248, 1, v0\n"
      "v_readfirstlane_b32 s50, v1\n"
      "v_and_b32 v248, 0x60, v248\n"
      "v_and_b32 v249, 0x70, v241\n"
      "v_xor_b32 v248, v248, v249\n"
      "v_lshl_or_b32 v248, v1, 6, v248\n"
      "v_and_b32 v1, 56, v0\n"
      "v_lshl_or_b32 v248, v1, 5, v248\n"
      "s_mov_b32 s64, 0x14100400\n"
      "s_mov_b32 s65, 0x54504440\n"
      "s_lshl_b32 s66, %[blk], 3\n"
      "s_lshr_b64 s[64:65], s[64:65], s66\n"
      "s_mov_b32 s66, 0x3020100\n"
      "s_mov_b32 s67, 0x83828180\n"
      "s_and_b32 s65, %[blk], 56\n"
      "s_lshr_b64 s[66:67], s[66:67], s65\n"
      "s_or_b32 s64, s64, s66\n"
      "s_lshr_b32 s65, %[blk], 3\n"
      "s_and_b32 s65, s65, 24\n"
      "s_lshr_b32 s65, 0x28082000, s65\n"
      "s_or_b32 s64, s64, s65\n"
      "s_and_b32 s63, s64, 0x80\n"
      "s_and_b32 s44, s64, 127\n"
      "v_or_b32 v248, s63, v248\n"
      "v_xor_b32 v249, 0x80, v248\n"
      "s_lshl_b32 s64, s44, 20\n"
      "s_lshl_b32 s62, s44, 15\n"
      "s_lshl_b32 s50, s50, 6\n"
      "s_add_u32 m0, s50, 131072\n"
      "s_mov_b32 s58, 0x120000\n"
      "s_mov_b32 s59, 0x20000\n"
      "s_movk_i32 s22, 0x4000\n"
      "s_waitcnt lgkmcnt(0)\n"
      "s_add_u32 s56, s4, s62\n"
      "s_addc_u32 s57, s5, 0\n"
      "buffer_load_dwordx4 v248, s[56:59], 0 offen nt lds\n"
      "buffer_load_dwordx4 v248, s[56:59], 0 offen offset:2048 nt lds\n"
      "s_bitset1_b32 m0, 10\n"
      "s_memrealtime s[82:83]\n"
      "s_add_u32 s20, s6, s64\n"
      "s_addc_u32 s21, s7, 0\n"
      "s_add_u32 s32, s8, s64\n"
      "s_addc_u32 s33, s9, 0\n"
      "s_load_dword s92, s[20:21], 0x0\n"
      "s_load_dword s92, s[10:11], 0x0\n"
      "s_load_dword s92, s[32:33], 0x0\n"
      "buffer_load_dwordx4 v249, s[56:59], 0 offen nt lds\n"
      "buffer_load_dwordx4 v249, s[56:59], 0 offen offset:2048 nt lds\n"
      "s_add_u32 m0, s50, 147456\n"
      "s_load_dword s92, s[18:19], 0x0\n"
      "buffer_load_dwordx4 v248, s[56:59], s22 offen nt lds\n"
      "buffer_load_dwordx4 v248, s[56:59], s22 offen offset:2048 nt lds\n"
      "s_bitset1_b32 m0, 10\n"
      "s_nop 0\n"
      "buffer_load_dwordx4 v249, s[56:59], s22 offen nt lds\n"
      "buffer_load_dwordx4 v249, s[56:59], s22 offen offset:2048 nt lds\n"
      "s_mov_b64 s[56:57], s[20:21]\n"
      "s_load_dword s92, s[14:15], 0x0\n"
      "s_load_dword s92, s[12:13], 0x0\n"
      "s_lshr_b32 s74, s50, 2\n"
      "s_lshr_b32 s75, s50, 5\n"
      "s_xor_b32 s75, s75, s63\n"
      "s_and_b32 s75, s75, 0x80\n"
      "s_and_b32 s76, s74, 0x800\n"
      "s_add_u32 s75, s75, s76\n"
      "s_add_u32 s70, s63, s50\n"
      "s_sub_u32 s70, s70, s75\n"
      "s_lshr_b32 s75, s50, 1\n"
      "s_lshr_b32 s71, s62, 2\n"
      "s_sub_u32 s71, s71, s75\n"
      "s_ashr_i32 s72, s71, 31\n"
      "s_mov_b32 s23, s59\n"
      "s_mov_b64 s[26:27], s[22:23]\n"
      "s_sub_u32 s20, s56, s70\n"
      "s_subb_u32 s21, s57, 0\n"
      "s_add_u32 m0, s74, 24576\n"
      "s_add_u32 s24, s10, s71\n"
      "buffer_load_dwordx4 v248, s[20:23], 0 offen nt lds\n"
      "s_addc_u32 s25, s11, s72\n"
      "s_add_u32 m0, s75, 16384\n"
      "s_nop 0\n"
      "buffer_load_dwordx4 v248, s[24:27], 0 offen nt lds\n"
      "s_bitset1_b32 m0, 10\n"
      "s_nop 0\n"
      "buffer_load_dwordx4 v249, s[24:27], 0 offen nt lds\n"
      "s_lshr_b32 s43, s63, 7\n"
      "s_lshl_b32 s76, s44, 9\n"
      "s_lshr_b32 s69, s50, 5\n"
      "s_add_u32 s76, s76, s69\n"
      "s_add_u32 s46, s18, s76\n"
      "s_addc_u32 s47, s19, 0\n"
      "s_mul_i32 s76, s43, 0x3000\n"
      "s_lshl_b32 s69, s44, 8\n"
      "s_and_b32 s69, s69, 0x7800\n"
      "s_add_u32 s14, s14, s69\n"
      "s_addc_u32 s15, s15, 0\n"
      "v_mov_b64 v[192:193], 0\n"
      "v_mov_b64 v[194:195], 0\n"
      "v_lshlrev_b32 v3, 6, v0\n"
      "v_and_b32 v3, 0x3c0, v3\n"
      "v_lshrrev_b32 v4, 1, v0\n"
      "v_and_or_b32 v211, v4, 24, v3\n"
      "v_mov_b64 v[200:201], 0\n"
      "v_mov_b64 v[202:203], 0\n"
      "v_mov_b64 v[204:205], 0\n"
      "v_mov_b64 v[206:207], 0\n"
      "s_cmp_lg_u32 s76, s50\n"
      "s_cbranch_scc1 NOMASK_%=\n"
      "global_load_dwordx2 v[200:201], v211, s[14:15] offset:0\n"
      "global_load_dwordx2 v[202:203], v211, s[14:15] offset:32\n"
      "global_load_dwordx2 v[204:205], v211, s[14:15] offset:1024\n"
      "global_load_dwordx2 v[206:207], v211, s[14:15] offset:1056\n"
      "NOMASK_%=:\n"
      "global_load_dword v208, v192, s[46:47] sc1\n"
      "s_mul_i32 s69, s43, 0x8000\n"
      "s_bitcmp1_b32 s44, 0\n"
      "s_cselect_b32 s66, 0x8000, 0\n"
      "s_xor_b32 s69, s69, s66\n"
      "v_and_b32 v228, 0xff, v248\n"
      "v_xor_b32 v228, s69, v228\n"
      "v_bfe_i32 v2, v0, 3, 1\n"
      "v_and_b32 v2, 0x1000, v2\n"
      "v_xor_b32 v228, v228, v2\n"
      "v_bfe_i32 v2, v0, 4, 1\n"
      "v_and_b32 v2, 0x20400, v2\n"
      "v_xor_b32 v228, v228, v2\n"
      "v_bfe_i32 v2, v0, 5, 1\n"
      "v_and_b32 v2, 0x100, v2\n"
      "v_xor_b32 v228, v228, v2\n"
      "v_bfe_i32 v2, v0, 6, 1\n"
      "v_and_b32 v2, 0x400, v2\n"
      "v_xor_b32 v228, v228, v2\n"
      "v_bfe_i32 v2, v0, 7, 1\n"
      "v_and_b32 v2, 0x2000, v2\n"
      "v_xor_b32 v228, v228, v2\n"
      "v_xor_b32 v231, 0x280, v228\n"
      "v_xor_b32 v230, 0x200, v228\n"
      "v_xor_b32 v229, 0x80, v228\n"
      "v_add_u32 v228, 0xc00, v228\n"
      "v_add_u32 v229, 0x800, v229\n"
      "v_add_u32 v230, 0x400, v230\n"
      "s_sub_u32 s56, s56, 0xc00\n"
      "s_subb_u32 s57, s57, 0\n"
      "s_sub_u32 s32, s32, 0xc00\n"
      "s_subb_u32 s33, s33, 0\n"
      "s_xor_b64 s[32:33], s[32:33], s[56:57]\n"
      "s_mov_b32 s69, 0\n"
      "s_bitcmp1_b32 s44, 4\n"
      "s_cselect_b32 s66, 0x10000, 0\n"
      "s_xor_b32 s69, s69, s66\n"
      "s_bitcmp1_b32 s44, 0\n"
      "s_cselect_b32 s66, 0x800, 0\n"
      "s_xor_b32 s69, s69, s66\n"
      "s_bitcmp1_b32 s44, 2\n"
      "s_cselect_b32 s66, 0x40800, 0\n"
      "s_xor_b32 s69, s69, s66\n"
      "s_bitcmp1_b32 s44, 4\n"
      "s_cselect_b32 s66, 0x4000, 0\n"
      "s_xor_b32 s69, s69, s66\n"
      "s_mov_b32 s60, s69\n"
      "s_mov_b32 s62, s50\n"
      "s_mov_b64 s[30:31], s[22:23]\n"
      "s_add_u32 s28, s12, s71\n"
      "s_addc_u32 s29, s13, s72\n"
      "s_lshl_b32 s69, s43, 13\n"
      "s_xor_b32 s69, s69, 0x2000\n"
      "v_add_u32 v2, s69, v241\n"
      "ds_write_b128 v241, v[192:195] offset:28672\n"
      "ds_write_b128 v2, v[192:195] offset:49152\n"
      "ds_write_b128 v2, v[192:195] offset:53248\n"
      "v_and_b32 v241, 0x3f0, v241\n"
      "s_and_b32 s91, s50, 8192\n"
      "v_lshlrev_b32 v4, 8, v0\n"
      "v_and_b32 v4, 0x800, v4\n"
      "v_lshlrev_b32 v5, 7, v0\n"
      "v_and_b32 v6, 0x380, v5\n"
      "v_or3_b32 v4, v4, v6, s91\n"
      "v_and_b32 v6, 0x60, v241\n"
      "v_and_b32 v2, 48, v0\n"
      "v_xor_b32 v6, v6, v2\n"
      "v_or_b32 v242, v4, v6\n"
      "v_xor_b32 v243, 64, v242\n"
      "v_lshlrev_b32 v4, 6, v0\n"
      "v_and_b32 v6, 0x800, v4\n"
      "v_lshlrev_b32 v2, 5, v0\n"
      "v_and_b32 v2, 0x380, v2\n"
      "s_add_u32 s69, s91, 16384\n"
      "v_or3_b32 v6, v6, v2, s69\n"
      "v_lshlrev_b32 v2, 3, v0\n"
      "v_and_or_b32 v6, v2, 24, v6\n"
      "v_lshlrev_b32 v2, 2, v0\n"
      "v_and_b32 v2, 0x60, v2\n"
      "v_xor_b32 v244, v6, v2\n"
      "v_xor_b32 v245, 0x20, v244\n"
      "v_xor_b32 v246, 0x40, v244\n"
      "v_xor_b32 v247, 0x60, v244\n"
      "v_and_b32 v5, 0x1e00, v5\n"
      "v_and_or_b32 v250, v241, 48, v5\n"
      "s_lshl_b32 s69, %[blk], 9\n"
      "s_and_b32 s69, s69, 0x1e00\n"
      "v_and_b32 v4, 0x1c0, v4\n"
      "v_or_b32 v251, s69, v4\n"
      "s_and_b32 s69, s50, 4096\n"
      "s_lshl_b32 s69, s69, 2\n"
      "s_add_u32 s69, s69, 131072\n"
      "s_sub_u32 s91, 16384, s91\n"
      "v_add_u32 v196, s69, v242\n"
      "v_add_u32 v198, s91, v242\n"
      "v_xor_b32 v214, 0x2000, v196\n"
      "v_add_u32 v197, s69, v243\n"
      "v_add_u32 v199, s91, v243\n"
      "v_xor_b32 v215, 0x2000, v197\n"
      "v_and_b32 v3, 1, v0\n"
      "v_bfe_u32 v4, v0, 1, 5\n"
      "v_lshlrev_b32 v3, 1, v3\n"
      "v_sub_u32 v4, v4, v3\n"
      "v_mov_b32 v219, s60\n"
      "v_bfe_i32 v2, v4, 0, 1\n"
      "v_and_b32 v2, 0x10000, v2\n"
      "v_xor_b32 v219, v219, v2\n"
      "v_bfe_i32 v2, v4, 1, 1\n"
      "v_and_b32 v2, 0x800, v2\n"
      "v_xor_b32 v219, v219, v2\n"
      "v_bfe_i32 v2, v4, 2, 1\n"
      "v_and_b32 v2, 0x40800, v2\n"
      "v_xor_b32 v219, v219, v2\n"
      "v_bfe_i32 v2, v4, 3, 1\n"
      "v_and_b32 v2, 0x80000, v2\n"
      "v_xor_b32 v219, v219, v2\n"
      "v_bfe_i32 v2, v4, 4, 1\n"
      "v_and_b32 v2, 0x4000, v2\n"
      "v_xor_b32 v219, v219, v2\n"
      "v_writelane_b32 v219, 0, 1\n"
      "s_mov_b32 s61, 0\n"
      "s_add_u32 s69, s50, 131072\n"
      "v_add_u32 v210, s69, v241\n"
      "v_and_b32 v2, 3, v0\n"
      "v_mov_b32 v4, 0x3f803f80\n"
      "v_cmp_eq_u32 vcc, 0, v2\n"
      "s_nop 1\n"
      "v_cndmask_b32 v5, 0, v4, vcc\n"
      "v_accvgpr_write_b32 a128, v5\n"
      "v_accvgpr_write_b32 a129, v5\n"
      "v_accvgpr_write_b32 a130, v5\n"
      "v_accvgpr_write_b32 a131, v5\n"
      "v_cmp_eq_u32 vcc, 1, v2\n"
      "s_nop 1\n"
      "v_cndmask_b32 v5, 0, v4, vcc\n"
      "v_accvgpr_write_b32 a132, v5\n"
      "v_accvgpr_write_b32 a133, v5\n"
      "v_accvgpr_write_b32 a134, v5\n"
      "v_accvgpr_write_b32 a135, v5\n"
      "v_cmp_eq_u32 vcc, 2, v2\n"
      "s_nop 1\n"
      "v_cndmask_b32 v5, 0, v4, vcc\n"
      "v_accvgpr_write_b32 a136, v5\n"
      "v_accvgpr_write_b32 a137, v5\n"
      "v_accvgpr_write_b32 a138, v5\n"
      "v_accvgpr_write_b32 a139, v5\n"
      "v_cmp_eq_u32 vcc, 3, v2\n"
      "s_nop 1\n"
      "v_cndmask_b32 v5, 0, v4, vcc\n"
      "v_accvgpr_write_b32 a140, v5\n"
      "v_accvgpr_write_b32 a141, v5\n"
      "v_accvgpr_write_b32 a142, v5\n"
      "v_accvgpr_write_b32 a143, v5\n"
      "v_mfma_f32_32x32x16_bf16 a[144:159], v[192:195], v[192:195], 0\n"
      "v_mfma_f32_32x32x16_bf16 a[160:175], v[192:195], v[192:195], 0\n"
      "v_mfma_f32_32x32x16_bf16 a[176:191], v[192:195], v[192:195], 0\n"
      "v_mfma_f32_32x32x16_bf16 a[192:207], v[192:195], v[192:195], 0\n"
      "v_mfma_f32_32x32x16_bf16 v[0:15], v[192:195], v[192:195], 0\n"
      "v_mfma_f32_32x32x16_bf16 v[16:31], v[192:195], v[192:195], 0\n"
      "v_mfma_f32_32x32x16_bf16 v[32:47], v[192:195], v[192:195], 0\n"
      "v_mfma_f32_32x32x16_bf16 v[48:63], v[192:195], v[192:195], 0\n"
      "v_mfma_f32_32x32x16_bf16 v[128:143], v[192:195], v[192:195], 0\n"
      "v_mov_b64 v[180:181], 0\n"
      "v_mov_b64 v[182:183], 0\n"
      "v_mov_b64 v[184:185], 0\n"
      "v_mov_b64 v[186:187], 0\n"
      "s_mov_b32 s94, 0x3e0293ee\n"
      "s_mov_b32 s95, s94\n"
      "s_lshl_b32 s69, s43, 13\n"
      "s_add_u32 s45, s69, s75\n"
      "s_mov_b32 m0, s62\n"
      "s_nop 0\n"
      "buffer_load_dwordx4 v228, s[56:59], s60 offen nt lds\n"
      "buffer_load_dwordx4 v229, s[56:59], s60 offen offset:1024 nt lds\n"
      "buffer_load_dwordx4 v230, s[56:59], s60 offen offset:2048 nt lds\n"
      "buffer_load_dwordx4 v231, s[56:59], s60 offen offset:3072 nt lds\n"
      "s_xor_b64 s[56:57], s[56:57], s[32:33]\n"
      "s_add_u32 s61, s61, 1\n"
      "s_add_u32 s62, s62, 0x4000\n"
      "s_and_b32 s62, s62, 0x1ffff\n"
      "v_readlane_b32 s60, v219, s61\n"
      "s_xor_b64 s[56:57], s[56:57], s[32:33]\n"
      "s_add_u32 s61, s61, 1\n"
      "s_add_u32 s62, s62, 0x4000\n"
      "s_and_b32 s62, s62, 0x1ffff\n"
      "v_readlane_b32 s60, v219, s61\n"
      "s_waitcnt vmcnt(8)\n"
      "s_memrealtime s[68:69]\n"
      "ds_read_b128 v[64:67], v210 offset:0\n"
      "ds_read_b128 v[68:71], v210 offset:1024\n"
      "ds_read_b128 v[72:75], v210 offset:2048\n"
      "ds_read_b128 v[76:79], v210 offset:3072\n"
      "ds_read_b128 v[80:83], v210 offset:16384\n"
      "ds_read_b128 v[84:87], v210 offset:17408\n"
      "ds_read_b128 v[88:91], v210 offset:18432\n"
      "ds_read_b128 v[92:95], v210 offset:19456\n"
      "s_add_u32 s20, s18, 65536\n"
      "s_addc_u32 s21, s19, 0\n"
      "s_lshl_b32 s75, %[blk], 6\n"
      "s_xor_b32 s74, s50, 4096\n"
      "s_add_u32 s75, s75, s74\n"
      "s_waitcnt lgkmcnt(0)\n"
      "s_sub_u32 s67, s68, s82\n"
      "s_min_u32 s67, s67, 165\n"
      "s_add_u32 s93, s82, s67\n"
      "s_sub_u32 s71, s68, 400\n"
      "s_cmp_lt_u32 s75, 512\n"
      "s_cbranch_scc0 NOLEAD_%=\n"
      "v_mov_b32 v188, s68\n"
      "v_mov_b32 v189, s68\n"
      "v_mov_b32 v190, s68\n"
      "v_mov_b32 v191, s68\n"
      "buffer_store_dwordx4 v[188:191], v250, s[20:23], s75 offen sc1\n"
      "s_nop 1\n"
      "NOLEAD_%=:\n"
      "s_mov_b32 m0, s62\n"
      "s_nop 0\n"
      "buffer_load_dwordx4 v228, s[56:59], s60 offen nt lds\n"
      "buffer_load_dwordx4 v229, s[56:59], s60 offen offset:1024 nt lds\n"
      "buffer_load_dwordx4 v230, s[56:59], s60 offen offset:2048 nt lds\n"
      "buffer_load_dwordx4 v231, s[56:59], s60 offen offset:3072 nt lds\n"
      "s_xor_b64 s[56:57], s[56:57], s[32:33]\n"
      "s_add_u32 s61, s61, 1\n"
      "s_add_u32 s62, s62, 0x4000\n"
      "s_and_b32 s62, s62, 0x1ffff\n"
      "v_readlane_b32 s60, v219, s61\n"
      "s_xor_b64 s[56:57], s[56:57], s[32:33]\n"
      "s_add_u32 s61, s61, 1\n"
      "s_add_u32 s62, s62, 0x4000\n"
      "s_and_b32 s62, s62, 0x1ffff\n"
      "v_readlane_b32 s60, v219, s61\n"
      "s_add_u32 m0, s45, 49152\n"
      "s_nop 0\n"
      "buffer_load_dwordx4 v248, s[28:31], 0 offen nt lds\n"
      "s_bitset1_b32 m0, 10\n"
      "s_nop 0\n"
      "buffer_load_dwordx4 v249, s[28:31], 0 offen nt lds\n"
      "v_lshlrev_b32 v188, 16, v64\n"
      "v_and_b32 v189, 0xffff0000, v64\n"
      "v_lshlrev_b32 v190, 16, v65\n"
      "v_and_b32 v191, 0xffff0000, v65\n"
      "v_lshlrev_b32 v192, 16, v66\n"
      "v_and_b32 v193, 0xffff0000, v66\n"
      "v_lshlrev_b32 v194, 16, v67\n"
      "v_and_b32 v195, 0xffff0000, v67\n"
      "v_pk_mul_f32 v[188:189], v[188:189], s[94:95]\n"
      "v_pk_mul_f32 v[190:191], v[190:191], s[94:95]\n"
      "v_pk_mul_f32 v[192:193], v[192:193], s[94:95]\n"
      "v_pk_mul_f32 v[194:195], v[194:195], s[94:95]\n"
      "v_cvt_pk_bf16_f32 v64, v188, v189\n"
      "v_cvt_pk_bf16_f32 v65, v190, v191\n"
      "v_cvt_pk_bf16_f32 v66, v192, v193\n"
      "v_cvt_pk_bf16_f32 v67, v194, v195\n"
      "ds_write_b128 v210, v[64:67] offset:0\n"
      "v_lshlrev_b32 v188, 16, v68\n"
      "v_and_b32 v189, 0xffff0000, v68\n"
      "v_lshlrev_b32 v190, 16, v69\n"
      "v_and_b32 v191, 0xffff0000, v69\n"
      "v_lshlrev_b32 v192, 16, v70\n"
      "v_and_b32 v193, 0xffff0000, v70\n"
      "v_lshlrev_b32 v194, 16, v71\n"
      "v_and_b32 v195, 0xffff0000, v71\n"
      "v_pk_mul_f32 v[188:189], v[188:189], s[94:95]\n"
      "v_pk_mul_f32 v[190:191], v[190:191], s[94:95]\n"
      "v_pk_mul_f32 v[192:193], v[192:193], s[94:95]\n"
      "v_pk_mul_f32 v[194:195], v[194:195], s[94:95]\n"
      "v_cvt_pk_bf16_f32 v68, v188, v189\n"
      "v_cvt_pk_bf16_f32 v69, v190, v191\n"
      "v_cvt_pk_bf16_f32 v70, v192, v193\n"
      "v_cvt_pk_bf16_f32 v71, v194, v195\n"
      "ds_write_b128 v210, v[68:71] offset:1024\n"
      "v_lshlrev_b32 v188, 16, v72\n"
      "v_and_b32 v189, 0xffff0000, v72\n"
      "v_lshlrev_b32 v190, 16, v73\n"
      "v_and_b32 v191, 0xffff0000, v73\n"
      "v_lshlrev_b32 v192, 16, v74\n"
      "v_and_b32 v193, 0xffff0000, v74\n"
      "v_lshlrev_b32 v194, 16, v75\n"
      "v_and_b32 v195, 0xffff0000, v75\n"
      "v_pk_mul_f32 v[188:189], v[188:189], s[94:95]\n"
      "v_pk_mul_f32 v[190:191], v[190:191], s[94:95]\n"
      "v_pk_mul_f32 v[192:193], v[192:193], s[94:95]\n"
      "v_pk_mul_f32 v[194:195], v[194:195], s[94:95]\n"
      "v_cvt_pk_bf16_f32 v72, v188, v189\n"
      "v_cvt_pk_bf16_f32 v73, v190, v191\n"
      "v_cvt_pk_bf16_f32 v74, v192, v193\n"
      "v_cvt_pk_bf16_f32 v75, v194, v195\n"
      "ds_write_b128 v210, v[72:75] offset:2048\n"
      "v_lshlrev_b32 v188, 16, v76\n"
      "v_and_b32 v189, 0xffff0000, v76\n"
      "v_lshlrev_b32 v190, 16, v77\n"
      "v_and_b32 v191, 0xffff0000, v77\n"
      "v_lshlrev_b32 v192, 16, v78\n"
      "v_and_b32 v193, 0xffff0000, v78\n"
      "v_lshlrev_b32 v194, 16, v79\n"
      "v_and_b32 v195, 0xffff0000, v79\n"
      "v_pk_mul_f32 v[188:189], v[188:189], s[94:95]\n"
      "v_pk_mul_f32 v[190:191], v[190:191], s[94:95]\n"
      "v_pk_mul_f32 v[192:193], v[192:193], s[94:95]\n"
      "v_pk_mul_f32 v[194:195], v[194:195], s[94:95]\n"
      "v_cvt_pk_bf16_f32 v76, v188, v189\n"
      "v_cvt_pk_bf16_f32 v77, v190, v191\n"
      "v_cvt_pk_bf16_f32 v78, v192, v193\n"
      "v_cvt_pk_bf16_f32 v79, v194, v195\n"
      "ds_write_b128 v210, v[76:79] offset:3072\n"
      "v_lshlrev_b32 v188, 16, v80\n"
      "v_and_b32 v189, 0xffff0000, v80\n"
      "v_lshlrev_b32 v190, 16, v81\n"
      "v_and_b32 v191, 0xffff0000, v81\n"
      "v_lshlrev_b32 v192, 16, v82\n"
      "v_and_b32 v193, 0xffff0000, v82\n"
      "v_lshlrev_b32 v194, 16, v83\n"
      "v_and_b32 v195, 0xffff0000, v83\n"
      "v_pk_mul_f32 v[188:189], v[188:189], s[94:95]\n"
      "v_pk_mul_f32 v[190:191], v[190:191], s[94:95]\n"
      "v_pk_mul_f32 v[192:193], v[192:193], s[94:95]\n"
      "v_pk_mul_f32 v[194:195], v[194:195], s[94:95]\n"
      "v_cvt_pk_bf16_f32 v80, v188, v189\n"
      "v_cvt_pk_bf16_f32 v81, v190, v191\n"
      "v_cvt_pk_bf16_f32 v82, v192, v193\n"
      "v_cvt_pk_bf16_f32 v83, v194, v195\n"
      "ds_write_b128 v210, v[80:83] offset:16384\n"
      "v_lshlrev_b32 v188, 16, v84\n"
      "v_and_b32 v189, 0xffff0000, v84\n"
      "v_lshlrev_b32 v190, 16, v85\n"
      "v_and_b32 v191, 0xffff0000, v85\n"
      "v_lshlrev_b32 v192, 16, v86\n"
      "v_and_b32 v193, 0xffff0000, v86\n"
      "v_lshlrev_b32 v194, 16, v87\n"
      "v_and_b32 v195, 0xffff0000, v87\n"
      "v_pk_mul_f32 v[188:189], v[188:189], s[94:95]\n"
      "v_pk_mul_f32 v[190:191], v[190:191], s[94:95]\n"
      "v_pk_mul_f32 v[192:193], v[192:193], s[94:95]\n"
      "v_pk_mul_f32 v[194:195], v[194:195], s[94:95]\n"
      "v_cvt_pk_bf16_f32 v84, v188, v189\n"
      "v_cvt_pk_bf16_f32 v85, v190, v191\n"
      "v_cvt_pk_bf16_f32 v86, v192, v193\n"
      "v_cvt_pk_bf16_f32 v87, v194, v195\n"
      "ds_write_b128 v210, v[84:87] offset:17408\n"
      "v_lshlrev_b32 v188, 16, v88\n"
      "v_and_b32 v189, 0xffff0000, v88\n"
      "v_lshlrev_b32 v190, 16, v89\n"
      "v_and_b32 v191, 0xffff0000, v89\n"
      "v_lshlrev_b32 v192, 16, v90\n"
      "v_and_b32 v193, 0xffff0000, v90\n"
      "v_lshlrev_b32 v194, 16, v91\n"
      "v_and_b32 v195, 0xffff0000, v91\n"
      "v_pk_mul_f32 v[188:189], v[188:189], s[94:95]\n"
      "v_pk_mul_f32 v[190:191], v[190:191], s[94:95]\n"
      "v_pk_mul_f32 v[192:193], v[192:193], s[94:95]\n"
      "v_pk_mul_f32 v[194:195], v[194:195], s[94:95]\n"
      "v_cvt_pk_bf16_f32 v88, v188, v189\n"
      "v_cvt_pk_bf16_f32 v89, v190, v191\n"
      "v_cvt_pk_bf16_f32 v90, v192, v193\n"
      "v_cvt_pk_bf16_f32 v91, v194, v195\n"
      "ds_write_b128 v210, v[88:91] offset:18432\n"
      "v_lshlrev_b32 v188, 16, v92\n"
      "v_and_b32 v189, 0xffff0000, v92\n"
      "v_lshlrev_b32 v190, 16, v93\n"
      "v_and_b32 v191, 0xffff0000, v93\n"
      "v_lshlrev_b32 v192, 16, v94\n"
      "v_and_b32 v193, 0xffff0000, v94\n"
      "v_lshlrev_b32 v194, 16, v95\n"
      "v_and_b32 v195, 0xffff0000, v95\n"
      "v_pk_mul_f32 v[188:189], v[188:189], s[94:95]\n"
      "v_pk_mul_f32 v[190:191], v[190:191], s[94:95]\n"
      "v_pk_mul_f32 v[192:193], v[192:193], s[94:95]\n"
      "v_pk_mul_f32 v[194:195], v[194:195], s[94:95]\n"
      "v_cvt_pk_bf16_f32 v92, v188, v189\n"
      "v_cvt_pk_bf16_f32 v93, v190, v191\n"
      "v_cvt_pk_bf16_f32 v94, v192, v193\n"
      "v_cvt_pk_bf16_f32 v95, v194, v195\n"
      "ds_write_b128 v210, v[92:95] offset:19456\n"
      "s_mov_b32 m0, s62\n"
      "s_nop 0\n"
      "buffer_load_dwordx4 v228, s[56:59], s60 offen nt lds\n"
      "buffer_load_dwordx4 v229, s[56:59], s60 offen offset:1024 nt lds\n"
      "buffer_load_dwordx4 v230, s[56:59], s60 offen offset:2048 nt lds\n"
      "buffer_load_dwordx4 v231, s[56:59], s60 offen offset:3072 nt lds\n"
      "s_xor_b64 s[56:57], s[56:57], s[32:33]\n"
      "s_add_u32 s61, s61, 1\n"
      "s_add_u32 s62, s62, 0x4000\n"
      "s_and_b32 s62, s62, 0x1ffff\n"
      "v_readlane_b32 s60, v219, s61\n"
      "s_cmp_eq_u32 s50, 0\n"
      "s_cselect_b32 s38, 0x44554001, 1\n"
      "s_cselect_b32 s39, 0x2a040083, 3\n"
      "s_cselect_b32 s52, 0x31109890, 0\n"
      "s_cselect_b32 s53, 0x80011, 0\n"
      "s_mov_b64 s[34:35], 0\n"
      "s_and_b32 s69, %[blk], 7\n"
      "s_mul_i32 s69, s69, 5\n"
      "s_lshr_b32 s74, %[blk], 4\n"
      "s_add_u32 s69, s69, s74\n"
      "s_and_b32 s69, s69, 15\n"
      "s_mul_i32 s74, s69, 16\n"
      "s_lshr_b32 s74, s74, 6\n"
      "s_lshl2_add_u32 s76, s74, 9\n"
      "s_bitset1_b64 s[34:35], s76\n"
      "s_lshl_b32 s74, s74, 1\n"
      "s_sub_u32 s74, 28, s74\n"
      "s_bitset1_b32 s39, s74\n"
      "s_mul_i32 s74, s69, 20\n"
      "s_lshr_b32 s74, s74, 6\n"
      "s_lshl2_add_u32 s76, s74, 25\n"
      "s_bitset1_b64 s[34:35], s76\n"
      "s_lshl_b32 s74, s74, 1\n"
      "s_sub_u32 s74, 20, s74\n"
      "s_bitset1_b32 s39, s74\n"
      "s_mul_i32 s74, s69, 20\n"
      "s_lshr_b32 s74, s74, 6\n"
      "s_lshl2_add_u32 s76, s74, 45\n"
      "s_bitset1_b64 s[34:35], s76\n"
      "s_lshl_b32 s74, s74, 1\n"
      "s_sub_u32 s74, 10, s74\n"
      "s_bitset1_b32 s39, s74\n"
      "s_mov_b32 s54, 3\n"
      "s_mov_b32 s70, 250\n"
      "s_mov_b32 s86, 59\n"
      "s_mov_b32 s77, 177\n"
      "s_lshl_b32 s91, s50, 16\n"
      "s_sub_u32 s80, 46, s91\n"
      "s_sub_u32 s90, 34, s91\n"
      "s_sub_u32 s72, s75, 18432\n"
      "s_and_b32 s79, %[blk], 31\n"
      "s_lshl_b32 s79, s79, 8\n"
      "s_add_u32 s79, s79, 8192\n"
      "s_mul_i32 s69, %[blk], 53\n"
      "s_lshr_b32 s69, s69, 8\n"
      "s_add_u32 s66, s93, s69\n"
      "s_add_u32 s66, s66, 125\n"
      "s_sub_u32 s81, s93, s71\n"
      "s_add_u32 s81, s81, -26\n"
      "s_sub_u32 s66, s66, s91\n"
      "s_mov_b32 s88, s66\n"
      "s_mov_b32 s65, s66\n"
      "s_getpc_b64 s[84:85]\n"
      "PC_%=:\n"
      "s_add_u32 s84, s84, HOOK_%=-PC_%=\n"
      "s_addc_u32 s85, s85, 0\n"
      "s_waitcnt vmcnt(14) lgkmcnt(0)\n"
      "s_barrier\n"
      "ds_read_b128 a[64:67], v198 offset:8192\n"
      "ds_read_b128 a[72:75], v199 offset:8192\n"
      "ds_read_b128 a[80:83], v198 offset:9216\n"
      "ds_read_b128 a[88:91], v199 offset:9216\n"
      "ds_read_b128 a[0:3], v196 offset:0\n"
      "ds_read_b128 a[16:19], v197 offset:0\n"
      "ds_read_b128 a[32:35], v196 offset:1024\n"
      "ds_read_b128 a[48:51], v197 offset:1024\n"
      "ds_read_b128 a[4:7], v196 offset:4096\n"
      "ds_read_b128 a[20:23], v197 offset:4096\n"
      "ds_read_b128 a[36:39], v196 offset:5120\n"
      "ds_read_b128 a[52:55], v197 offset:5120\n"
      "v_readfirstlane_b32 s40, v208\n"
      "s_mov_b32 s94, 0xceabfb8f\n"
      "v_lshlrev_b32 v144, 16, v200\n"
      "v_and_b32 v145, 0xffff0000, v200\n"
      "v_lshlrev_b32 v146, 16, v201\n"
      "v_and_b32 v147, 0xffff0000, v201\n"
      "v_lshlrev_b32 v148, 16, v202\n"
      "v_and_b32 v149, 0xffff0000, v202\n"
      "v_lshlrev_b32 v150, 16, v203\n"
      "v_and_b32 v151, 0xffff0000, v203\n"
      "v_lshlrev_b32 v152, 16, v204\n"
      "v_and_b32 v153, 0xffff0000, v204\n"
      "v_lshlrev_b32 v154, 16, v205\n"
      "v_and_b32 v155, 0xffff0000, v205\n"
      "v_lshlrev_b32 v156, 16, v206\n"
      "v_and_b32 v157, 0xffff0000, v206\n"
      "v_lshlrev_b32 v158, 16, v207\n"
      "v_and_b32 v159, 0xffff0000, v207\n"
      "s_waitcnt lgkmcnt(4)\n"
      "v_mfma_f32_16x16x32_bf16 v[64:67], a[64:67], a[0:3], 0\n"
      "ds_read_b128 a[8:11], v214 offset:0\n"
      "v_fma_f32 v144, -v144, s94, s94\n"
      "v_mfma_f32_16x16x32_bf16 v[64:67], a[72:75], a[16:19], v[64:67]\n"
      "ds_read_b128 a[24:27], v215 offset:0\n"
      "v_fma_f32 v145, -v145, s94, s94\n"
      "v_mfma_f32_16x16x32_bf16 v[64:67], a[80:83], a[32:35], v[64:67]\n"
      "ds_read_b128 a[40:43], v214 offset:1024\n"
      "v_fma_f32 v146, -v146, s94, s94\n"
      "v_mfma_f32_16x16x32_bf16 v[64:67], a[88:91], a[48:51], v[64:67]\n"
      "ds_read_b128 a[56:59], v215 offset:1024\n"
      "v_fma_f32 v147, -v147, s94, s94\n"
      "s_waitcnt lgkmcnt(4)\n"
      "v_mfma_f32_16x16x32_bf16 v[72:75], a[64:67], a[4:7], 0\n"
      "ds_read_b128 a[12:15], v214 offset:4096\n"
      "v_fma_f32 v148, -v148, s94, s94\n"
      "v_mfma_f32_16x16x32_bf16 v[72:75], a[72:75], a[20:23], v[72:75]\n"
      "ds_read_b128 a[28:31], v215 offset:4096\n"
      "v_fma_f32 v149, -v149, s94, s94\n"
      "v_mfma_f32_16x16x32_bf16 v[72:75], a[80:83], a[36:39], v[72:75]\n"
      "ds_read_b128 a[44:47], v214 offset:5120\n"
      "v_fma_f32 v150, -v150, s94, s94\n"
      "v_mfma_f32_16x16x32_bf16 v[72:75], a[88:91], a[52:55], v[72:75]\n"
      "ds_read_b128 a[60:63], v215 offset:5120\n"
      "v_fma_f32 v151, -v151, s94, s94\n"
      "s_waitcnt lgkmcnt(4)\n"
      "v_mfma_f32_16x16x32_bf16 v[80:83], a[64:67], a[8:11], 0\n"
      "ds_read_b128 a[96:99], v198 offset:0\n"
      "v_fma_f32 v152, -v152, s94, s94\n"
      "v_mfma_f32_16x16x32_bf16 v[80:83], a[72:75], a[24:27], v[80:83]\n"
      "ds_read_b128 a[100:103], v198 offset:4096\n"
      "v_fma_f32 v153, -v153, s94, s94\n"
      "v_mfma_f32_16x16x32_bf16 v[80:83], a[80:83], a[40:43], v[80:83]\n"
      "ds_read_b128 a[104:107], v199 offset:0\n"
      "v_fma_f32 v154, -v154, s94, s94\n"
      "v_mfma_f32_16x16x32_bf16 v[80:83], a[88:91], a[56:59], v[80:83]\n"
      "ds_read_b128 a[108:111], v199 offset:4096\n"
      "v_fma_f32 v155, -v155, s94, s94\n"
      "s_waitcnt lgkmcnt(4)\n"
      "v_mfma_f32_16x16x32_bf16 v[88:91], a[64:67], a[12:15], 0\n"
      "ds_read_b128 a[112:115], v198 offset:1024\n"
      "v_fma_f32 v156, -v156, s94, s94\n"
      "v_mfma_f32_16x16x32_bf16 v[88:91], a[72:75], a[28:31], v[88:91]\n"
      "ds_read_b128 a[116:119], v198 offset:5120\n"
      "v_fma_f32 v157, -v157, s94, s94\n"
      "v_mfma_f32_16x16x32_bf16 v[88:91], a[80:83], a[44:47], v[88:91]\n"
      "ds_read_b128 a[120:123], v199 offset:1024\n"
      "v_fma_f32 v158, -v158, s94, s94\n"
      "v_mfma_f32_16x16x32_bf16 v[88:91], a[88:91], a[60:63], v[88:91]\n"
      "ds_read_b128 a[124:127], v199 offset:5120\n"
      "v_fma_f32 v159, -v159, s94, s94\n"
      "s_mov_b32 m0, s62\n"
      "s_nop 0\n"
      "buffer_load_dwordx4 v228, s[56:59], s60 offen nt lds\n"
      "buffer_load_dwordx4 v229, s[56:59], s60 offen offset:1024 nt lds\n"
      "buffer_load_dwordx4 v230, s[56:59], s60 offen offset:2048 nt lds\n"
      "buffer_load_dwordx4 v231, s[56:59], s60 offen offset:3072 nt lds\n"
      "s_waitcnt lgkmcnt(0)\n"
      "v_mfma_f32_16x16x32_bf16 v[96:99], a[96:99], a[0:3], v[144:147]\n"
      "s_add_u32 s40, s40, 0x7f4a7c15\n"
      "v_mfma_f32_16x16x32_bf16 v[96:99], a[104:107], a[16:19], v[96:99]\n"
      "s_lshl_b32 s69, s44, 3\n"
      "v_max3_f32 v188, v64, v65, v66\n"
      "v_max3_f32 v189, v72, v73, v74\n"
      "v_mfma_f32_16x16x32_bf16 v[96:99], a[112:115], a[32:35], v[96:99]\n"
      "s_lshr_b32 s74, s50, 12\n"
      "v_max3_f32 v190, v80, v81, v82\n"
      "v_max3_f32 v191, v88, v89, v90\n"
      "v_mfma_f32_16x16x32_bf16 v[96:99], a[120:123], a[48:51], v[96:99]\n"
      "s_add_u32 s69, s69, s74\n"
      "v_max_f32 v188, v188, v67\n"
      "v_max_f32 v189, v189, v75\n"
      "v_mfma_f32_16x16x32_bf16 v[104:107], a[96:99], a[4:7], v[152:155]\n"
      "s_mul_i32 s69, s69, 4608\n"
      "v_max_f32 v190, v190, v83\n"
      "v_max_f32 v191, v191, v91\n"
      "v_mfma_f32_16x16x32_bf16 v[104:107], a[104:107], a[20:23], v[104:107]\n"
      "s_add_u32 s69, s69, 81920\n"
      "v_mov_b32 v192, v188\n"
      "v_mov_b32 v193, v189\n"
      "v_mfma_f32_16x16x32_bf16 v[104:107], a[112:115], a[36:39], v[104:107]\n"
      "s_add_u32 s24, s18, s69\n"
      "v_mov_b32 v194, v190\n"
      "v_mov_b32 v195, v191\n"
      "v_mfma_f32_16x16x32_bf16 v[104:107], a[120:123], a[52:55], v[104:107]\n"
      "s_addc_u32 s25, s19, 0\n"
      "v_permlane16_swap_b32 v188, v192\n"
      "v_permlane16_swap_b32 v189, v193\n"
      "v_mfma_f32_16x16x32_bf16 v[112:115], a[96:99], a[8:11], v[144:147]\n"
      "s_movk_i32 s26, 23040\n"
      "v_permlane16_swap_b32 v190, v194\n"
      "v_permlane16_swap_b32 v191, v195\n"
      "v_mfma_f32_16x16x32_bf16 v[112:115], a[104:107], a[24:27], v[112:115]\n"
      "s_mul_i32 s78, s43, 18432\n"
      "v_max_f32 v188, v188, v192\n"
      "v_max_f32 v189, v189, v193\n"
      "v_mfma_f32_16x16x32_bf16 v[112:115], a[112:115], a[40:43], v[112:115]\n"
      "s_and_b32 s69, s50, 4096\n"
      "v_max_f32 v190, v190, v194\n"
      "v_max_f32 v191, v191, v195\n"
      "v_mfma_f32_16x16x32_bf16 v[112:115], a[120:123], a[56:59], v[112:115]\n"
      "s_lshl_b32 s69, s69, 2\n"
      "v_mov_b32 v192, v188\n"
      "v_mov_b32 v193, v189\n"
      "v_mfma_f32_16x16x32_bf16 v[120:123], a[96:99], a[12:15], v[152:155]\n"
      "s_and_b32 s74, s50, 8192\n"
      "v_mov_b32 v194, v190\n"
      "v_mov_b32 v195, v191\n"
      "v_mfma_f32_16x16x32_bf16 v[120:123], a[104:107], a[28:31], v[120:123]\n"
      "s_add_u32 s74, s74, 4096\n"
      "v_permlane32_swap_b32 v188, v192\n"
      "v_permlane32_swap_b32 v189, v193\n"
      "v_mfma_f32_16x16x32_bf16 v[120:123], a[112:115], a[44:47], v[120:123]\n"
      "s_add_u32 s69, s69, s74\n"
      "v_permlane32_swap_b32 v190, v194\n"
      "v_permlane32_swap_b32 v191, v195\n"
      "v_mfma_f32_16x16x32_bf16 v[120:123], a[120:123], a[60:63], v[120:123]\n"
      "s_lshl_b32 s74, s43, 7\n"
      "v_max_f32 v188, v188, v192\n"
      "v_max_f32 v189, v189, v193\n"
      "v_mfma_f32_16x16x32_bf16 v[100:103], a[100:103], a[0:3], v[148:151]\n"
      "s_add_u32 s69, s69, s74\n"
      "v_max_f32 v190, v190, v194\n"
      "v_max_f32 v191, v191, v195\n"
      "v_mfma_f32_16x16x32_bf16 v[100:103], a[108:111], a[16:19], v[100:103]\n"
      "s_lshl_b32 s74, s44, 15\n"
      "v_xor_b32 v176, 0x80000000, v188\n"
      "v_xor_b32 v177, 0x80000000, v189\n"
      "v_mfma_f32_16x16x32_bf16 v[100:103], a[116:119], a[32:35], v[100:103]\n"
      "s_add_u32 s69, s69, s74\n"
      "v_xor_b32 v178, 0x80000000, v190\n"
      "v_xor_b32 v179, 0x80000000, v191\n"
      "v_mfma_f32_16x16x32_bf16 v[100:103], a[124:127], a[48:51], v[100:103]\n"
      "s_add_u32 s30, s16, s69\n"
      "v_mov_b32 v160, v176\n"
      "v_mov_b32 v161, v176\n"
      "v_mfma_f32_16x16x32_bf16 v[108:111], a[100:103], a[4:7], v[156:159]\n"
      "s_addc_u32 s31, s17, 0\n"
      "v_mov_b32 v162, v176\n"
      "v_mov_b32 v163, v176\n"
      "v_mfma_f32_16x16x32_bf16 v[108:111], a[108:111], a[20:23], v[108:111]\n"
      "v_mov_b32 v164, v177\n"
      "v_mov_b32 v165, v177\n"
      "v_mfma_f32_16x16x32_bf16 v[108:111], a[116:119], a[36:39], v[108:111]\n"
      "v_mov_b32 v166, v177\n"
      "v_mov_b32 v167, v177\n"
      "v_mfma_f32_16x16x32_bf16 v[108:111], a[124:127], a[52:55], v[108:111]\n"
      "v_mov_b32 v168, v178\n"
      "v_mov_b32 v169, v178\n"
      "v_mfma_f32_16x16x32_bf16 v[116:119], a[100:103], a[8:11], v[148:151]\n"
      "v_mov_b32 v170, v178\n"
      "v_mov_b32 v171, v178\n"
      "v_mfma_f32_16x16x32_bf16 v[116:119], a[108:111], a[24:27], v[116:119]\n"
      "v_mov_b32 v172, v179\n"
      "v_mov_b32 v173, v179\n"
      "v_mfma_f32_16x16x32_bf16 v[116:119], a[116:119], a[40:43], v[116:119]\n"
      "v_mov_b32 v174, v179\n"
      "v_mov_b32 v175, v179\n"
      "v_mfma_f32_16x16x32_bf16 v[116:119], a[124:127], a[56:59], v[116:119]\n"
      "v_mov_b32 v209, 139248\n"
      "v_lshl_add_u32 v232, s50, 1, v241\n"
      "v_mfma_f32_16x16x32_bf16 v[124:127], a[100:103], a[12:15], v[156:159]\n"
      "v_add_u32 v232, 0x10000, v232\n"
      "v_xor_b32 v233, 0x4000, v232\n"
      "v_mfma_f32_16x16x32_bf16 v[124:127], a[108:111], a[28:31], v[124:127]\n"
      "v_add_u32 v234, 0x10000, v232\n"
      "v_xor_b32 v235, 0x4000, v234\n"
      "v_mfma_f32_16x16x32_bf16 v[124:127], a[116:119], a[44:47], v[124:127]\n"
      "v_lshrrev_b32 v240, 1, v241\n"
      "v_add_u32 v240, 0x1000, v240\n"
      "v_mfma_f32_16x16x32_bf16 v[124:127], a[124:127], a[60:63], v[124:127]\n"
      "v_mov_b32 v196, 0\n"
      "v_pk_add_f32 v[96:97], v[96:97], v[160:161]\n"
      "v_pk_add_f32 v[98:99], v[98:99], v[160:161]\n"
      "v_pk_add_f32 v[104:105], v[104:105], v[164:165]\n"
      "v_pk_add_f32 v[106:107], v[106:107], v[164:165]\n"
      "v_pk_add_f32 v[112:113], v[112:113], v[168:169]\n"
      "v_pk_add_f32 v[114:115], v[114:115], v[168:169]\n"
      "v_pk_add_f32 v[120:121], v[120:121], v[172:173]\n"
      "v_pk_add_f32 v[122:123], v[122:123], v[172:173]\n"
      "v_pk_add_f32 v[100:101], v[100:101], v[160:161]\n"
      "v_pk_add_f32 v[102:103], v[102:103], v[160:161]\n"
      "v_pk_add_f32 v[108:109], v[108:109], v[164:165]\n"
      "v_pk_add_f32 v[110:111], v[110:111], v[164:165]\n"
      "v_pk_add_f32 v[116:117], v[116:117], v[168:169]\n"
      "v_pk_add_f32 v[118:119], v[118:119], v[168:169]\n"
      "v_pk_add_f32 v[124:125], v[124:125], v[172:173]\n"
      "v_pk_add_f32 v[126:127], v[126:127], v[172:173]\n"
      "v_mov_b32 v224, v244\n"
      "v_mov_b32 v225, v245\n"
      "v_mov_b32 v226, v246\n"
      "v_mov_b32 v227, v247\n"
      "v_xor_b32 v220, 0x2000, v244\n"
      "v_xor_b32 v221, 0x2000, v245\n"
      "v_xor_b32 v222, 0x2000, v246\n"
      "v_xor_b32 v223, 0x2000, v247\n"
      "s_lshl_b32 s76, s43, 3\n"
      "v_mov_b32 v237, s40\n"
      "v_mov_b32 v238, s76\n"
      "v_xor_b32 v239, 8, v238\n"
      "s_memrealtime s[68:69]\n"
      "s_waitcnt vmcnt(14) lgkmcnt(0)\n"
      "s_sub_u32 s67, s68, s88\n"
      "s_cmp_lt_i32 s67, -8\n"
      "s_cbranch_scc1 GW0_%=\n"
      "GX0_%=:\n"
      "s_barrier\n"
      "ds_read_b128 a[64:67], v242 offset:0\n"
      "ds_read_b128 a[68:71], v242 offset:4096\n"
      "ds_read_b128 a[72:75], v243 offset:0\n"
      "ds_read_b128 a[76:79], v243 offset:4096\n"
      "ds_read_b128 a[80:83], v242 offset:1024\n"
      "ds_read_b128 a[84:87], v242 offset:5120\n"
      "ds_read_b128 a[88:91], v243 offset:1024\n"
      "ds_read_b128 a[92:95], v243 offset:5120\n"
      "ds_write_b32 v209, v196\n"
      "s_add_u32 s38, s38, s38\n"
      "s_memrealtime s[68:69]\n"
      "s_xor_b64 s[56:57], s[56:57], s[32:33]\n"
      "s_add_u32 s61, s61, 1\n"
      "s_add_u32 s62, s62, 0x4000\n"
      "s_and_b32 s62, s62, 0x1ffff\n"
      "v_readlane_b32 s60, v219, s61\n"
      "s_waitcnt lgkmcnt(0)\n"
      "s_sub_u32 s67, s68, s66\n"
      "s_cmp_gt_i32 s67, s70\n"
      "s_cselect_b32 s69, s77, s86\n"
      "s_cselect_b32 s70, 60, s70\n"
      "s_cselect_b32 s90, s80, s90\n"
      "s_add_u32 s66, s66, s69\n"
      "s_mov_b32 s82, s68\n"
      "LOOP_%=:\n"
      "v_mfma_f32_16x16x32_bf16 v[64:67], a[64:67], a[0:3], v[160:163]\n"
      "s_add_u32 s65, s68, s90\n"
      "v_exp_f32 v188, v96\n"
      "v_mfma_f32_16x16x32_bf16 v[64:67], a[72:75], a[16:19], v[64:67]\n"
      "s_max_u32 s88, s66, s65\n"
      "v_add_f32 v180, v180, v184\n"
      "v_add_f32 v181, v181, v185\n"
      "v_mfma_f32_16x16x32_bf16 v[64:67], a[80:83], a[32:35], v[64:67]\n"
      "v_exp_f32 v189, v97\n"
      "v_mfma_f32_16x16x32_bf16 v[64:67], a[88:91], a[48:51], v[64:67]\n"
      "s_getreg_b32 s67, hwreg(HW_REG_IB_STS, 0, 4)\n"
      "v_add_f32 v182, v182, v186\n"
      "v_add_f32 v183, v183, v187\n"
      "v_mfma_f32_16x16x32_bf16 v[72:75], a[64:67], a[4:7], v[164:167]\n"
      "s_cmp_gt_u32 s67, 4\n"
      "v_exp_f32 v190, v98\n"
      "v_mfma_f32_16x16x32_bf16 v[72:75], a[72:75], a[20:23], v[72:75]\n"
      "s_addc_u32 s89, s89, 0\n"
      "v_exp_f32 v191, v99\n"
      "v_mfma_f32_16x16x32_bf16 v[72:75], a[80:83], a[36:39], v[72:75]\n"
      "v_cvt_pk_bf16_f32 v144, v188, v189\n"
      "v_cvt_pk_bf16_f32 v145, v190, v191\n"
      "v_mfma_f32_16x16x32_bf16 v[72:75], a[88:91], a[52:55], v[72:75]\n"
      "v_exp_f32 v192, v100\n"
      "v_mfma_f32_16x16x32_bf16 v[80:83], a[64:67], a[8:11], v[168:171]\n"
      "s_add_u32 s39, s39, s39\n"
      "v_exp_f32 v193, v101\n"
      "v_mfma_f32_16x16x32_bf16 v[80:83], a[72:75], a[24:27], v[80:83]\n"
      "s_memrealtime s[68:69]\n"
      "v_exp_f32 v194, v102\n"
      "v_mfma_f32_16x16x32_bf16 v[80:83], a[80:83], a[40:43], v[80:83]\n"
      "v_exp_f32 v195, v103\n"
      "v_mfma_f32_16x16x32_bf16 v[80:83], a[88:91], a[56:59], v[80:83]\n"
      "s_mov_b32 m0, s62\n"
      "v_cvt_pk_bf16_f32 v146, v192, v193\n"
      "v_cvt_pk_bf16_f32 v147, v194, v195\n"
      "v_mfma_f32_16x16x32_bf16 v[88:91], a[64:67], a[12:15], v[172:175]\n"
      "buffer_load_dwordx4 v228, s[56:59], s60 offen nt lds\n"
      "v_mfma_f32_16x16x32_bf16 v[184:187], a[128:131], v[144:147], 0\n"
      "buffer_load_dwordx4 v229, s[56:59], s60 offen offset:1024 nt lds\n"
      "v_mfma_f32_16x16x32_bf16 v[88:91], a[72:75], a[28:31], v[88:91]\n"
      "buffer_load_dwordx4 v230, s[56:59], s60 offen offset:2048 nt lds\n"
      "v_mfma_f32_16x16x32_bf16 v[88:91], a[80:83], a[44:47], v[88:91]\n"
      "buffer_load_dwordx4 v231, s[56:59], s60 offen offset:3072 nt lds\n"
      "s_cbranch_scc1 SP1_%=\n"
      "HD1_%=:\n"
      "s_cmp_lg_u32 s54, 0\n"
      "s_waitcnt vmcnt(12) lgkmcnt(0)\n"
      "s_cbranch_scc1 W1_%=\n"
      "s_waitcnt vmcnt(8)\n"
      "W1_%=:\n"
      "s_sub_u32 s67, s68, s88\n"
      "s_cmp_lt_i32 s67, -8\n"
      "s_cbranch_scc1 GW4_%=\n"
      "GX4_%=:\n"
      "s_barrier\n"
      "v_mfma_f32_16x16x32_bf16 v[88:91], a[88:91], a[60:63], v[88:91]\n"
      "s_memrealtime s[68:69]\n"
      "ds_read_b64_tr_b16 a[96:97], v244 offset:0\n"
      "ds_read_b64_tr_b16 a[98:99], v244 offset:4096\n"
      "v_mfma_f32_16x16x32_bf16 v[68:71], a[68:71], a[0:3], v[160:163]\n"
      "s_xor_b64 s[56:57], s[56:57], s[32:33]\n"
      "ds_read_b64_tr_b16 a[100:101], v245 offset:0\n"
      "ds_read_b64_tr_b16 a[102:103], v245 offset:4096\n"
      "v_mfma_f32_16x16x32_bf16 v[68:71], a[76:79], a[16:19], v[68:71]\n"
      "s_add_u32 s61, s61, 1\n"
      "ds_read_b64_tr_b16 a[104:105], v246 offset:0\n"
      "ds_read_b64_tr_b16 a[106:107], v246 offset:4096\n"
      "v_mfma_f32_16x16x32_bf16 v[68:71], a[84:87], a[32:35], v[68:71]\n"
      "s_add_u32 s62, s62, 0x4000\n"
      "ds_read_b64_tr_b16 a[108:109], v247 offset:0\n"
      "ds_read_b64_tr_b16 a[110:111], v247 offset:4096\n"
      "v_mfma_f32_16x16x32_bf16 v[68:71], a[92:95], a[48:51], v[68:71]\n"
      "s_and_b32 s62, s62, 0x1ffff\n"
      "v_exp_f32 v188, v104\n"
      "v_mfma_f32_16x16x32_bf16 v[76:79], a[68:71], a[4:7], v[164:167]\n"
      "v_readlane_b32 s60, v219, s61\n"
      "v_exp_f32 v189, v105\n"
      "v_mfma_f32_16x16x32_bf16 v[76:79], a[76:79], a[20:23], v[76:79]\n"
      "v_exp_f32 v190, v106\n"
      "v_mfma_f32_16x16x32_bf16 v[76:79], a[84:87], a[36:39], v[76:79]\n"
      "v_exp_f32 v191, v107\n"
      "v_mfma_f32_16x16x32_bf16 v[76:79], a[92:95], a[52:55], v[76:79]\n"
      "v_cvt_pk_bf16_f32 v148, v188, v189\n"
      "v_cvt_pk_bf16_f32 v149, v190, v191\n"
      "v_mfma_f32_16x16x32_bf16 v[84:87], a[68:71], a[8:11], v[168:171]\n"
      "v_exp_f32 v192, v108\n"
      "v_mfma_f32_16x16x32_bf16 v[84:87], a[76:79], a[24:27], v[84:87]\n"
      "v_exp_f32 v193, v109\n"
      "s_waitcnt lgkmcnt(0)\n"
      "v_mfma_f32_16x16x32_bf16 v[84:87], a[84:87], a[40:43], v[84:87]\n"
      "s_sub_u32 s67, s68, s66\n"
      "ds_read_b64_tr_b16 a[112:113], v244 offset:1024\n"
      "v_cvt_pk_bf16_f32 v150, v192, v193\n"
      "v_mfma_f32_16x16x32_bf16 v[84:87], a[92:95], a[56:59], v[84:87]\n"
      "s_cmp_gt_i32 s67, s70\n"
      "v_exp_f32 v194, v110\n"
      "v_mfma_f32_16x16x32_bf16 v[92:95], a[68:71], a[12:15], v[172:175]\n"
      "s_cselect_b32 s69, s77, s86\n"
      "ds_read_b64_tr_b16 a[114:115], v244 offset:5120\n"
      "ds_read_b64_tr_b16 a[116:117], v245 offset:1024\n"
      "v_mfma_f32_16x16x32_bf16 v[92:95], a[76:79], a[28:31], v[92:95]\n"
      "s_cselect_b32 s70, 60, s70\n"
      "v_exp_f32 v195, v111\n"
      "v_mfma_f32_16x16x32_bf16 v[92:95], a[84:87], a[44:47], v[92:95]\n"
      "s_cselect_b32 s90, s80, s90\n"
      "ds_read_b64_tr_b16 a[118:119], v245 offset:5120\n"
      "v_cvt_pk_bf16_f32 v151, v194, v195\n"
      "v_mfma_f32_16x16x32_bf16 v[92:95], a[92:95], a[60:63], v[92:95]\n"
      "s_add_u32 s66, s66, s69\n"
      "v_exp_f32 v188, v112\n"
      "v_mfma_f32_16x16x32_bf16 v[184:187], a[132:135], v[148:151], v[184:187]\n"
      "s_mov_b32 s82, s68\n"
      "ds_read_b64_tr_b16 a[120:121], v246 offset:1024\n"
      "ds_read_b64_tr_b16 a[122:123], v246 offset:5120\n"
      "v_mfma_f32_16x16x32_bf16 v[0:3], a[96:99], v[128:131], v[0:3]\n"
      "s_add_u32 s65, s68, s90\n"
      "v_exp_f32 v189, v113\n"
      "v_mfma_f32_16x16x32_bf16 v[4:7], a[96:99], v[132:135], v[4:7]\n"
      "s_max_u32 s88, s66, s65\n"
      "ds_read_b64_tr_b16 a[124:125], v247 offset:1024\n"
      "v_cvt_pk_bf16_f32 v152, v188, v189\n"
      "v_mfma_f32_16x16x32_bf16 a[144:147], a[96:99], v[136:139], a[144:147]\n"
      "v_exp_f32 v190, v114\n"
      "v_mfma_f32_16x16x32_bf16 a[148:151], a[96:99], v[140:143], a[148:151]\n"
      "s_getreg_b32 s67, hwreg(HW_REG_IB_STS, 0, 4)\n"
      "v_exp_f32 v191, v115\n"
      "v_mfma_f32_16x16x32_bf16 a[156:159], a[100:103], v[140:143], a[156:159]\n"
      "s_cmp_gt_u32 s67, 4\n"
      "ds_read_b64_tr_b16 a[126:127], v247 offset:5120\n"
      "v_cvt_pk_bf16_f32 v153, v190, v191\n"
      "v_mfma_f32_16x16x32_bf16 a[152:155], a[100:103], v[136:139], a[152:155]\n"
      "s_addc_u32 s89, s89, 0\n"
      "v_exp_f32 v192, v116\n"
      "v_mfma_f32_16x16x32_bf16 v[12:15], a[100:103], v[132:135], v[12:15]\n"
      "v_exp_f32 v193, v117\n"
      "v_mfma_f32_16x16x32_bf16 v[8:11], a[100:103], v[128:131], v[8:11]\n"
      "v_exp_f32 v194, v118\n"
      "v_mfma_f32_16x16x32_bf16 v[16:19], a[104:107], v[128:131], v[16:19]\n"
      "s_add_u32 s38, s38, s38\n"
      "v_exp_f32 v195, v119\n"
      "v_mfma_f32_16x16x32_bf16 v[20:23], a[104:107], v[132:135], v[20:23]\n"
      "s_memrealtime s[68:69]\n"
      "v_cvt_pk_bf16_f32 v154, v192, v193\n"
      "v_cvt_pk_bf16_f32 v155, v194, v195\n"
      "v_mfma_f32_16x16x32_bf16 a[160:163], a[104:107], v[136:139], a[160:163]\n"
      "v_exp_f32 v188, v120\n"
      "v_mfma_f32_16x16x32_bf16 v[184:187], a[136:139], v[152:155], v[184:187]\n"
      "s_mov_b32 m0, s62\n"
      "v_exp_f32 v189, v121\n"
      "v_mfma_f32_16x16x32_bf16 a[164:167], a[104:107], v[140:143], a[164:167]\n"
      "buffer_load_dwordx4 v228, s[56:59], s60 offen nt lds\n"
      "v_mfma_f32_16x16x32_bf16 a[172:175], a[108:111], v[140:143], a[172:175]\n"
      "buffer_load_dwordx4 v229, s[56:59], s60 offen offset:1024 nt lds\n"
      "v_mfma_f32_16x16x32_bf16 a[168:171], a[108:111], v[136:139], a[168:171]\n"
      "buffer_load_dwordx4 v230, s[56:59], s60 offen offset:2048 nt lds\n"
      "v_mfma_f32_16x16x32_bf16 v[28:31], a[108:111], v[132:135], v[28:31]\n"
      "buffer_load_dwordx4 v231, s[56:59], s60 offen offset:3072 nt lds\n"
      "s_cbranch_scc1 SP2_%=\n"
      "HD2_%=:\n"
      "s_cmp_lg_u32 s54, 0\n"
      "s_waitcnt vmcnt(12) lgkmcnt(0)\n"
      "s_cbranch_scc1 W2_%=\n"
      "s_waitcnt vmcnt(8)\n"
      "W2_%=:\n"
      "s_sub_u32 s67, s68, s88\n"
      "s_cmp_lt_i32 s67, -8\n"
      "s_cbranch_scc1 GW5_%=\n"
      "GX5_%=:\n"
      "s_barrier\n"
      "v_mfma_f32_16x16x32_bf16 v[24:27], a[108:111], v[128:131], v[24:27]\n"
      "s_memrealtime s[68:69]\n"
      "ds_read_b128 a[64:67], v242 offset:32768\n"
      "ds_read_b128 a[72:75], v243 offset:32768\n"
      "v_mfma_f32_16x16x32_bf16 v[32:35], a[112:115], v[128:131], v[32:35]\n"
      "s_xor_b64 s[56:57], s[56:57], s[32:33]\n"
      "ds_read_b128 a[80:83], v242 offset:33792\n"
      "ds_read_b128 a[88:91], v243 offset:33792\n"
      "v_mfma_f32_16x16x32_bf16 v[36:39], a[112:115], v[132:135], v[36:39]\n"
      "s_add_u32 s61, s61, 1\n"
      "v_exp_f32 v190, v122\n"
      "v_mfma_f32_16x16x32_bf16 a[176:179], a[112:115], v[136:139], a[176:179]\n"
      "s_add_u32 s62, s62, 0x4000\n"
      "v_exp_f32 v191, v123\n"
      "v_mfma_f32_16x16x32_bf16 a[180:183], a[112:115], v[140:143], a[180:183]\n"
      "s_and_b32 s62, s62, 0x1ffff\n"
      "v_cvt_pk_bf16_f32 v156, v188, v189\n"
      "v_cvt_pk_bf16_f32 v157, v190, v191\n"
      "v_mfma_f32_16x16x32_bf16 a[188:191], a[116:119], v[140:143], a[188:191]\n"
      "v_readlane_b32 s60, v219, s61\n"
      "v_exp_f32 v192, v124\n"
      "v_mfma_f32_16x16x32_bf16 a[184:187], a[116:119], v[136:139], a[184:187]\n"
      "v_exp_f32 v193, v125\n"
      "v_mfma_f32_16x16x32_bf16 v[44:47], a[116:119], v[132:135], v[44:47]\n"
      "v_exp_f32 v194, v126\n"
      "v_mfma_f32_16x16x32_bf16 v[40:43], a[116:119], v[128:131], v[40:43]\n"
      "v_exp_f32 v195, v127\n"
      "v_mfma_f32_16x16x32_bf16 v[48:51], a[120:123], v[128:131], v[48:51]\n"
      "v_cvt_pk_bf16_f32 v158, v192, v193\n"
      "v_cvt_pk_bf16_f32 v159, v194, v195\n"
      "v_mfma_f32_16x16x32_bf16 v[52:55], a[120:123], v[132:135], v[52:55]\n"
      "s_waitcnt lgkmcnt(0)\n"
      "v_mfma_f32_16x16x32_bf16 v[184:187], a[140:143], v[156:159], v[184:187]\n"
      "s_sub_u32 s67, s68, s66\n"
      "ds_read_b128 a[68:71], v242 offset:36864\n"
      "ds_read_b128 a[76:79], v243 offset:36864\n"
      "v_mfma_f32_16x16x32_bf16 a[192:195], a[120:123], v[136:139], a[192:195]\n"
      "s_cmp_gt_i32 s67, s70\n"
      "ds_read_b128 a[84:87], v242 offset:37888\n"
      "ds_read_b128 a[92:95], v243 offset:37888\n"
      "v_mfma_f32_16x16x32_bf16 a[196:199], a[120:123], v[140:143], a[196:199]\n"
      "s_cselect_b32 s69, s77, s86\n"
      "v_max3_f32 v196, v184, v185, v186\n"
      "v_max_f32 v196, v196, v187\n"
      "v_mfma_f32_16x16x32_bf16 a[204:207], a[124:127], v[140:143], a[204:207]\n"
      "s_cselect_b32 s70, 60, s70\n"
      "v_cmp_nge_f32 vcc, 0x4b800000, v196\n"
      "v_mfma_f32_16x16x32_bf16 a[200:203], a[124:127], v[136:139], a[200:203]\n"
      "s_cselect_b32 s90, s80, s90\n"
      "v_mfma_f32_16x16x32_bf16 v[60:63], a[124:127], v[132:135], v[60:63]\n"
      "s_add_u32 s66, s66, s69\n"
      "v_mfma_f32_16x16x32_bf16 v[56:59], a[124:127], v[128:131], v[56:59]\n"
      "s_mov_b32 s82, s68\n"
      "s_cbranch_vccnz FS1_%=\n"
      "FR1_%=:\n"
      "v_mfma_f32_16x16x32_bf16 v[96:99], a[64:67], a[0:3], v[160:163]\n"
      "s_add_u32 s65, s68, s90\n"
      "v_exp_f32 v188, v64\n"
      "v_mfma_f32_16x16x32_bf16 v[96:99], a[72:75], a[16:19], v[96:99]\n"
      "s_max_u32 s88, s66, s65\n"
      "v_add_f32 v180, v180, v184\n"
      "v_add_f32 v181, v181, v185\n"
      "v_mfma_f32_16x16x32_bf16 v[96:99], a[80:83], a[32:35], v[96:99]\n"
      "v_exp_f32 v189, v65\n"
      "v_mfma_f32_16x16x32_bf16 v[96:99], a[88:91], a[48:51], v[96:99]\n"
      "s_getreg_b32 s67, hwreg(HW_REG_IB_STS, 0, 4)\n"
      "v_add_f32 v182, v182, v186\n"
      "v_add_f32 v183, v183, v187\n"
      "v_mfma_f32_16x16x32_bf16 v[104:107], a[64:67], a[4:7], v[164:167]\n"
      "s_cmp_gt_u32 s67, 4\n"
      "v_exp_f32 v190, v66\n"
      "v_mfma_f32_16x16x32_bf16 v[104:107], a[72:75], a[20:23], v[104:107]\n"
      "s_addc_u32 s89, s89, 0\n"
      "v_exp_f32 v191, v67\n"
      "v_mfma_f32_16x16x32_bf16 v[104:107], a[80:83], a[36:39], v[104:107]\n"
      "v_cvt_pk_bf16_f32 v128, v188, v189\n"
      "v_cvt_pk_bf16_f32 v129, v190, v191\n"
      "v_mfma_f32_16x16x32_bf16 v[104:107], a[88:91], a[52:55], v[104:107]\n"
      "v_exp_f32 v192, v68\n"
      "v_mfma_f32_16x16x32_bf16 v[112:115], a[64:67], a[8:11], v[168:171]\n"
      "s_add_u32 s39, s39, s39\n"
      "v_exp_f32 v193, v69\n"
      "v_mfma_f32_16x16x32_bf16 v[112:115], a[72:75], a[24:27], v[112:115]\n"
      "s_memrealtime s[68:69]\n"
      "v_exp_f32 v194, v70\n"
      "v_mfma_f32_16x16x32_bf16 v[112:115], a[80:83], a[40:43], v[112:115]\n"
      "v_exp_f32 v195, v71\n"
      "v_mfma_f32_16x16x32_bf16 v[112:115], a[88:91], a[56:59], v[112:115]\n"
      "s_mov_b32 m0, s62\n"
      "v_cvt_pk_bf16_f32 v130, v192, v193\n"
      "v_cvt_pk_bf16_f32 v131, v194, v195\n"
      "v_mfma_f32_16x16x32_bf16 v[120:123], a[64:67], a[12:15], v[172:175]\n"
      "buffer_load_dwordx4 v228, s[56:59], s60 offen nt lds\n"
      "v_mfma_f32_16x16x32_bf16 v[184:187], a[128:131], v[128:131], 0\n"
      "buffer_load_dwordx4 v229, s[56:59], s60 offen offset:1024 nt lds\n"
      "v_mfma_f32_16x16x32_bf16 v[120:123], a[72:75], a[28:31], v[120:123]\n"
      "buffer_load_dwordx4 v230, s[56:59], s60 offen offset:2048 nt lds\n"
      "v_mfma_f32_16x16x32_bf16 v[120:123], a[80:83], a[44:47], v[120:123]\n"
      "buffer_load_dwordx4 v231, s[56:59], s60 offen offset:3072 nt lds\n"
      "s_cbranch_scc1 SP3_%=\n"
      "HD3_%=:\n"
      "s_cmp_lg_u32 s54, 0\n"
      "s_waitcnt vmcnt(12) lgkmcnt(0)\n"
      "s_cbranch_scc1 W3_%=\n"
      "s_waitcnt vmcnt(8)\n"
      "W3_%=:\n"
      "s_sub_u32 s67, s68, s88\n"
      "s_cmp_lt_i32 s67, -8\n"
      "s_cbranch_scc1 GW6_%=\n"
      "GX6_%=:\n"
      "s_barrier\n"
      "v_mfma_f32_16x16x32_bf16 v[120:123], a[88:91], a[60:63], v[120:123]\n"
      "s_memrealtime s[68:69]\n"
      "ds_read_b64_tr_b16 a[96:97], v244 offset:32768\n"
      "ds_read_b64_tr_b16 a[98:99], v244 offset:36864\n"
      "v_mfma_f32_16x16x32_bf16 v[100:103], a[68:71], a[0:3], v[160:163]\n"
      "s_xor_b64 s[56:57], s[56:57], s[32:33]\n"
      "ds_read_b64_tr_b16 a[100:101], v245 offset:32768\n"
      "ds_read_b64_tr_b16 a[102:103], v245 offset:36864\n"
      "v_mfma_f32_16x16x32_bf16 v[100:103], a[76:79], a[16:19], v[100:103]\n"
      "s_add_u32 s61, s61, 1\n"
      "ds_read_b64_tr_b16 a[104:105], v246 offset:32768\n"
      "ds_read_b64_tr_b16 a[106:107], v246 offset:36864\n"
      "v_mfma_f32_16x16x32_bf16 v[100:103], a[84:87], a[32:35], v[100:103]\n"
      "s_add_u32 s62, s62, 0x4000\n"
      "ds_read_b64_tr_b16 a[108:109], v247 offset:32768\n"
      "ds_read_b64_tr_b16 a[110:111], v247 offset:36864\n"
      "v_mfma_f32_16x16x32_bf16 v[100:103], a[92:95], a[48:51], v[100:103]\n"
      "s_and_b32 s62, s62, 0x1ffff\n"
      "v_exp_f32 v188, v72\n"
      "v_mfma_f32_16x16x32_bf16 v[108:111], a[68:71], a[4:7], v[164:167]\n"
      "v_readlane_b32 s60, v219, s61\n"
      "v_exp_f32 v189, v73\n"
      "v_mfma_f32_16x16x32_bf16 v[108:111], a[76:79], a[20:23], v[108:111]\n"
      "v_xor_b32 v242, 0x10000, v242\n"
      "v_xor_b32 v243, 0x10000, v243\n"
      "v_mfma_f32_16x16x32_bf16 v[108:111], a[84:87], a[36:39], v[108:111]\n"
      "v_exp_f32 v190, v74\n"
      "v_mfma_f32_16x16x32_bf16 v[108:111], a[92:95], a[52:55], v[108:111]\n"
      "v_exp_f32 v191, v75\n"
      "v_mfma_f32_16x16x32_bf16 v[116:119], a[68:71], a[8:11], v[168:171]\n"
      "v_cvt_pk_bf16_f32 v132, v188, v189\n"
      "v_cvt_pk_bf16_f32 v133, v190, v191\n"
      "v_mfma_f32_16x16x32_bf16 v[116:119], a[76:79], a[24:27], v[116:119]\n"
      "v_exp_f32 v192, v76\n"
      "s_waitcnt lgkmcnt(0)\n"
      "v_mfma_f32_16x16x32_bf16 v[116:119], a[84:87], a[40:43], v[116:119]\n"
      "s_sub_u32 s67, s68, s66\n"
      "ds_read_b64_tr_b16 a[112:113], v244 offset:33792\n"
      "ds_read_b64_tr_b16 a[114:115], v244 offset:37888\n"
      "v_mfma_f32_16x16x32_bf16 v[116:119], a[92:95], a[56:59], v[116:119]\n"
      "s_cmp_gt_i32 s67, s70\n"
      "v_exp_f32 v193, v77\n"
      "v_mfma_f32_16x16x32_bf16 v[124:127], a[68:71], a[12:15], v[172:175]\n"
      "s_cselect_b32 s69, s77, s86\n"
      "ds_read_b64_tr_b16 a[116:117], v245 offset:33792\n"
      "v_cvt_pk_bf16_f32 v134, v192, v193\n"
      "v_mfma_f32_16x16x32_bf16 v[124:127], a[76:79], a[28:31], v[124:127]\n"
      "s_cselect_b32 s70, 60, s70\n"
      "v_exp_f32 v194, v78\n"
      "v_mfma_f32_16x16x32_bf16 v[124:127], a[84:87], a[44:47], v[124:127]\n"
      "s_cselect_b32 s90, s80, s90\n"
      "ds_read_b64_tr_b16 a[118:119], v245 offset:37888\n"
      "ds_read_b64_tr_b16 a[120:121], v246 offset:33792\n"
      "v_mfma_f32_16x16x32_bf16 v[124:127], a[92:95], a[60:63], v[124:127]\n"
      "s_add_u32 s66, s66, s69\n"
      "v_exp_f32 v195, v79\n"
      "v_mfma_f32_16x16x32_bf16 v[0:3], a[96:99], v[144:147], v[0:3]\n"
      "s_mov_b32 s82, s68\n"
      "ds_read_b64_tr_b16 a[122:123], v246 offset:37888\n"
      "v_cvt_pk_bf16_f32 v135, v194, v195\n"
      "v_mfma_f32_16x16x32_bf16 v[4:7], a[96:99], v[148:151], v[4:7]\n"
      "s_add_u32 s65, s68, s90\n"
      "v_exp_f32 v188, v80\n"
      "v_mfma_f32_16x16x32_bf16 v[184:187], a[132:135], v[132:135], v[184:187]\n"
      "s_max_u32 s88, s66, s65\n"
      "ds_read_b64_tr_b16 a[124:125], v247 offset:33792\n"
      "ds_read_b64_tr_b16 a[126:127], v247 offset:37888\n"
      "v_mfma_f32_16x16x32_bf16 a[144:147], a[96:99], v[152:155], a[144:147]\n"
      "v_exp_f32 v189, v81\n"
      "v_mfma_f32_16x16x32_bf16 a[148:151], a[96:99], v[156:159], a[148:151]\n"
      "s_getreg_b32 s67, hwreg(HW_REG_IB_STS, 0, 4)\n"
      "v_exp_f32 v190, v82\n"
      "v_mfma_f32_16x16x32_bf16 a[156:159], a[100:103], v[156:159], a[156:159]\n"
      "s_cmp_gt_u32 s67, 4\n"
      "v_exp_f32 v191, v83\n"
      "v_mfma_f32_16x16x32_bf16 a[152:155], a[100:103], v[152:155], a[152:155]\n"
      "s_addc_u32 s89, s89, 0\n"
      "v_cvt_pk_bf16_f32 v136, v188, v189\n"
      "v_cvt_pk_bf16_f32 v137, v190, v191\n"
      "v_mfma_f32_16x16x32_bf16 v[12:15], a[100:103], v[148:151], v[12:15]\n"
      "v_exp_f32 v192, v84\n"
      "v_mfma_f32_16x16x32_bf16 v[8:11], a[100:103], v[144:147], v[8:11]\n"
      "v_exp_f32 v193, v85\n"
      "v_mfma_f32_16x16x32_bf16 v[16:19], a[104:107], v[144:147], v[16:19]\n"
      "s_add_u32 s38, s38, s38\n"
      "v_exp_f32 v194, v86\n"
      "v_mfma_f32_16x16x32_bf16 v[20:23], a[104:107], v[148:151], v[20:23]\n"
      "s_memrealtime s[68:69]\n"
      "v_exp_f32 v195, v87\n"
      "v_mfma_f32_16x16x32_bf16 a[160:163], a[104:107], v[152:155], a[160:163]\n"
      "v_cvt_pk_bf16_f32 v138, v192, v193\n"
      "v_cvt_pk_bf16_f32 v139, v194, v195\n"
      "v_mfma_f32_16x16x32_bf16 a[164:167], a[104:107], v[156:159], a[164:167]\n"
      "s_mov_b32 m0, s62\n"
      "v_exp_f32 v188, v88\n"
      "v_mfma_f32_16x16x32_bf16 v[184:187], a[136:139], v[136:139], v[184:187]\n"
      "buffer_load_dwordx4 v228, s[56:59], s60 offen nt lds\n"
      "v_mfma_f32_16x16x32_bf16 a[172:175], a[108:111], v[156:159], a[172:175]\n"
      "buffer_load_dwordx4 v229, s[56:59], s60 offen offset:1024 nt lds\n"
      "v_mfma_f32_16x16x32_bf16 a[168:171], a[108:111], v[152:155], a[168:171]\n"
      "buffer_load_dwordx4 v230, s[56:59], s60 offen offset:2048 nt lds\n"
      "v_mfma_f32_16x16x32_bf16 v[28:31], a[108:111], v[148:151], v[28:31]\n"
      "buffer_load_dwordx4 v231, s[56:59], s60 offen offset:3072 nt lds\n"
      "HD0_%=:\n"
      "s_cmp_lg_u32 s54, 0\n"
      "s_waitcnt vmcnt(12) lgkmcnt(0)\n"
      "s_cbranch_scc1 W4_%=\n"
      "s_waitcnt vmcnt(8)\n"
      "W4_%=:\n"
      "s_sub_u32 s67, s68, s88\n"
      "s_cmp_lt_i32 s67, -8\n"
      "s_cbranch_scc1 GW7_%=\n"
      "GX7_%=:\n"
      "s_barrier\n"
      "v_mfma_f32_16x16x32_bf16 v[24:27], a[108:111], v[144:147], v[24:27]\n"
      "s_memrealtime s[68:69]\n"
      "ds_read_b128 a[64:67], v242 offset:0\n"
      "ds_read_b128 a[72:75], v243 offset:0\n"
      "v_mfma_f32_16x16x32_bf16 v[32:35], a[112:115], v[144:147], v[32:35]\n"
      "s_xor_b64 s[56:57], s[56:57], s[32:33]\n"
      "ds_read_b128 a[80:83], v242 offset:1024\n"
      "ds_read_b128 a[88:91], v243 offset:1024\n"
      "v_mfma_f32_16x16x32_bf16 v[36:39], a[112:115], v[148:151], v[36:39]\n"
      "s_add_u32 s61, s61, 1\n"
      "v_exp_f32 v189, v89\n"
      "v_mfma_f32_16x16x32_bf16 a[176:179], a[112:115], v[152:155], a[176:179]\n"
      "s_add_u32 s62, s62, 0x4000\n"
      "v_exp_f32 v190, v90\n"
      "v_mfma_f32_16x16x32_bf16 a[180:183], a[112:115], v[156:159], a[180:183]\n"
      "s_and_b32 s62, s62, 0x1ffff\n"
      "v_exp_f32 v191, v91\n"
      "v_mfma_f32_16x16x32_bf16 a[188:191], a[116:119], v[156:159], a[188:191]\n"
      "v_cvt_pk_bf16_f32 v140, v188, v189\n"
      "v_readlane_b32 s60, v219, s61\n"
      "v_mfma_f32_16x16x32_bf16 a[184:187], a[116:119], v[152:155], a[184:187]\n"
      "v_exp_f32 v192, v92\n"
      "v_mfma_f32_16x16x32_bf16 v[44:47], a[116:119], v[148:151], v[44:47]\n"
      "v_exp_f32 v193, v93\n"
      "v_mfma_f32_16x16x32_bf16 v[40:43], a[116:119], v[144:147], v[40:43]\n"
      "v_cvt_pk_bf16_f32 v141, v190, v191\n"
      "v_cvt_pk_bf16_f32 v142, v192, v193\n"
      "v_mfma_f32_16x16x32_bf16 v[48:51], a[120:123], v[144:147], v[48:51]\n"
      "v_exp_f32 v194, v94\n"
      "v_mfma_f32_16x16x32_bf16 v[52:55], a[120:123], v[148:151], v[52:55]\n"
      "v_exp_f32 v195, v95\n"
      "s_waitcnt lgkmcnt(0)\n"
      "v_mfma_f32_16x16x32_bf16 a[192:195], a[120:123], v[152:155], a[192:195]\n"
      "s_sub_u32 s67, s68, s66\n"
      "v_cvt_pk_bf16_f32 v143, v194, v195\n"
      "v_xor_b32 v244, 0x10000, v244\n"
      "v_mfma_f32_16x16x32_bf16 a[196:199], a[120:123], v[156:159], a[196:199]\n"
      "s_cmp_gt_i32 s67, s70\n"
      "ds_read_b128 a[68:71], v242 offset:4096\n"
      "ds_read_b128 a[76:79], v243 offset:4096\n"
      "v_mfma_f32_16x16x32_bf16 v[184:187], a[140:143], v[140:143], v[184:187]\n"
      "s_cselect_b32 s69, s77, s86\n"
      "ds_read_b128 a[84:87], v242 offset:5120\n"
      "ds_read_b128 a[92:95], v243 offset:5120\n"
      "v_mfma_f32_16x16x32_bf16 a[204:207], a[124:127], v[156:159], a[204:207]\n"
      "s_cselect_b32 s70, 60, s70\n"
      "v_xor_b32 v245, 0x10000, v245\n"
      "v_xor_b32 v246, 0x10000, v246\n"
      "v_mfma_f32_16x16x32_bf16 a[200:203], a[124:127], v[152:155], a[200:203]\n"
      "s_cselect_b32 s90, s80, s90\n"
      "v_xor_b32 v247, 0x10000, v247\n"
      "v_max3_f32 v196, v184, v185, v186\n"
      "v_mfma_f32_16x16x32_bf16 v[60:63], a[124:127], v[148:151], v[60:63]\n"
      "s_add_u32 s66, s66, s69\n"
      "v_max_f32 v196, v196, v187\n"
      "v_cmp_nge_f32 vcc, 0x4b800000, v196\n"
      "v_mfma_f32_16x16x32_bf16 v[56:59], a[124:127], v[144:147], v[56:59]\n"
      "s_mov_b32 s82, s68\n"
      "s_cbranch_vccnz FS2_%=\n"
      "FR2_%=:\n"
      "s_branch LOOP_%=\n"
      "SP1_%=:\n"
      "s_ff1_i32_b32 s64, s39\n"
      "s_lshl1_add_u32 s64, s64, 1\n"
      "s_bitcmp1_b64 s[52:53], s64\n"
      "s_cbranch_scc0 SH1_%=\n"
      "s_swappc_b64 s[84:85], s[84:85]\n"
      "SH1_%=:\n"
      "s_cmp_lt_u32 s64, 63\n"
      "s_cbranch_scc1 HD1_%=\n"
      "s_xor_b64 s[56:57], s[56:57], s[32:33]\n"
      "s_add_u32 s61, s61, 1\n"
      "s_add_u32 s62, s62, 0x4000\n"
      "s_and_b32 s62, s62, 0x1ffff\n"
      "v_readlane_b32 s74, v219, 60\n"
      "v_readlane_b32 s76, v219, 62\n"
      "s_mov_b32 s61, 0\n"
      "s_add_u32 s74, s74, 0x400\n"
      "v_add_u32 v228, s74, v229\n"
      "v_add_u32 v230, s74, v231\n"
      "v_add_u32 v229, s76, v229\n"
      "v_add_u32 v231, s76, v231\n"
      "s_branch HD1_%=\n"
      "SP2_%=:\n"
      "s_cmp_eq_u32 s38, 0\n"
      "s_cbranch_scc1 L62_%=\n"
      "s_ff1_i32_b32 s64, s38\n"
      "s_lshl1_add_u32 s64, s64, 0\n"
      "s_bitcmp1_b64 s[52:53], s64\n"
      "s_cbranch_scc0 SH2_%=\n"
      "s_swappc_b64 s[84:85], s[84:85]\n"
      "SH2_%=:\n"
      "s_branch HD2_%=\n"
      "L62_%=:\n"
      "s_xor_b64 s[56:57], s[56:57], s[32:33]\n"
      "s_mov_b32 s61, 0\n"
      "v_xor_b32 v228, 0x80, v228\n"
      "v_xor_b32 v229, 0x80, v229\n"
      "v_xor_b32 v230, 0x80, v230\n"
      "v_xor_b32 v231, 0x80, v231\n"
      "s_mov_b32 s38, 0x80000000\n"
      "s_branch HD2_%=\n"
      "SP3_%=:\n"
      "s_cmp_eq_u32 s39, 0\n"
      "s_cbranch_scc1 NH_%=\n"
      "s_ff1_i32_b32 s64, s39\n"
      "s_lshl1_add_u32 s64, s64, 1\n"
      "s_bitcmp1_b64 s[52:53], s64\n"
      "s_cbranch_scc0 SH3_%=\n"
      "s_swappc_b64 s[84:85], s[84:85]\n"
      "SH3_%=:\n"
      "s_bitcmp1_b64 s[34:35], s64\n"
      "s_cbranch_scc0 HD3_%=\n"
      "NH_%=:\n"
      "s_cmp_lg_u32 s54, 0\n"
      "s_waitcnt vmcnt(12) lgkmcnt(0)\n"
      "s_cbranch_scc1 W5_%=\n"
      "s_waitcnt vmcnt(8)\n"
      "W5_%=:\n"
      "s_barrier\n"
      "v_mfma_f32_16x16x32_bf16 v[120:123], a[88:91], a[60:63], v[120:123]\n"
      "s_sub_u32 s54, s54, 1\n"
      "ds_read_b64_tr_b16 a[96:97], v244 offset:32768\n"
      "ds_read_b64_tr_b16 a[98:99], v244 offset:36864\n"
      "v_mfma_f32_16x16x32_bf16 v[100:103], a[68:71], a[0:3], v[160:163]\n"
      "ds_read_b64_tr_b16 a[100:101], v245 offset:32768\n"
      "ds_read_b64_tr_b16 a[102:103], v245 offset:36864\n"
      "v_mfma_f32_16x16x32_bf16 v[100:103], a[76:79], a[16:19], v[100:103]\n"
      "ds_read_b64_tr_b16 a[104:105], v246 offset:32768\n"
      "ds_read_b64_tr_b16 a[106:107], v246 offset:36864\n"
      "v_mfma_f32_16x16x32_bf16 v[100:103], a[84:87], a[32:35], v[100:103]\n"
      "ds_read_b64_tr_b16 a[108:109], v247 offset:32768\n"
      "ds_read_b64_tr_b16 a[110:111], v247 offset:36864\n"
      "v_mfma_f32_16x16x32_bf16 v[100:103], a[92:95], a[48:51], v[100:103]\n"
      "v_exp_f32 v188, v72\n"
      "v_mfma_f32_16x16x32_bf16 v[108:111], a[68:71], a[4:7], v[164:167]\n"
      "v_exp_f32 v189, v73\n"
      "v_mfma_f32_16x16x32_bf16 v[108:111], a[76:79], a[20:23], v[108:111]\n"
      "v_xor_b32 v242, 0x10000, v242\n"
      "v_xor_b32 v243, 0x10000, v243\n"
      "v_mfma_f32_16x16x32_bf16 v[108:111], a[84:87], a[36:39], v[108:111]\n"
      "v_exp_f32 v190, v74\n"
      "v_mfma_f32_16x16x32_bf16 v[108:111], a[92:95], a[52:55], v[108:111]\n"
      "v_exp_f32 v191, v75\n"
      "v_mfma_f32_16x16x32_bf16 v[116:119], a[68:71], a[8:11], v[168:171]\n"
      "v_cvt_pk_bf16_f32 v132, v188, v189\n"
      "v_cvt_pk_bf16_f32 v133, v190, v191\n"
      "v_mfma_f32_16x16x32_bf16 v[116:119], a[76:79], a[24:27], v[116:119]\n"
      "v_exp_f32 v192, v76\n"
      "s_waitcnt lgkmcnt(0)\n"
      "v_mfma_f32_16x16x32_bf16 v[116:119], a[84:87], a[40:43], v[116:119]\n"
      "ds_read_b64_tr_b16 a[112:113], v244 offset:33792\n"
      "ds_read_b64_tr_b16 a[114:115], v244 offset:37888\n"
      "v_mfma_f32_16x16x32_bf16 v[116:119], a[92:95], a[56:59], v[116:119]\n"
      "v_exp_f32 v193, v77\n"
      "v_mfma_f32_16x16x32_bf16 v[124:127], a[68:71], a[12:15], v[172:175]\n"
      "ds_read_b64_tr_b16 a[116:117], v245 offset:33792\n"
      "v_cvt_pk_bf16_f32 v134, v192, v193\n"
      "v_mfma_f32_16x16x32_bf16 v[124:127], a[76:79], a[28:31], v[124:127]\n"
      "v_exp_f32 v194, v78\n"
      "v_mfma_f32_16x16x32_bf16 v[124:127], a[84:87], a[44:47], v[124:127]\n"
      "ds_read_b64_tr_b16 a[118:119], v245 offset:37888\n"
      "ds_read_b64_tr_b16 a[120:121], v246 offset:33792\n"
      "v_mfma_f32_16x16x32_bf16 v[124:127], a[92:95], a[60:63], v[124:127]\n"
      "v_exp_f32 v195, v79\n"
      "v_mfma_f32_16x16x32_bf16 v[0:3], a[96:99], v[144:147], v[0:3]\n"
      "ds_read_b64_tr_b16 a[122:123], v246 offset:37888\n"
      "v_cvt_pk_bf16_f32 v135, v194, v195\n"
      "v_mfma_f32_16x16x32_bf16 v[4:7], a[96:99], v[148:151], v[4:7]\n"
      "v_exp_f32 v188, v80\n"
      "v_mfma_f32_16x16x32_bf16 v[184:187], a[132:135], v[132:135], v[184:187]\n"
      "ds_read_b64_tr_b16 a[124:125], v247 offset:33792\n"
      "ds_read_b64_tr_b16 a[126:127], v247 offset:37888\n"
      "v_mfma_f32_16x16x32_bf16 a[144:147], a[96:99], v[152:155], a[144:147]\n"
      "v_exp_f32 v189, v81\n"
      "v_mfma_f32_16x16x32_bf16 a[148:151], a[96:99], v[156:159], a[148:151]\n"
      "v_exp_f32 v190, v82\n"
      "v_mfma_f32_16x16x32_bf16 a[156:159], a[100:103], v[156:159], a[156:159]\n"
      "v_exp_f32 v191, v83\n"
      "v_mfma_f32_16x16x32_bf16 a[152:155], a[100:103], v[152:155], a[152:155]\n"
      "v_cvt_pk_bf16_f32 v136, v188, v189\n"
      "v_cvt_pk_bf16_f32 v137, v190, v191\n"
      "v_mfma_f32_16x16x32_bf16 v[12:15], a[100:103], v[148:151], v[12:15]\n"
      "v_exp_f32 v192, v84\n"
      "v_mfma_f32_16x16x32_bf16 v[8:11], a[100:103], v[144:147], v[8:11]\n"
      "v_exp_f32 v193, v85\n"
      "v_mfma_f32_16x16x32_bf16 v[16:19], a[104:107], v[144:147], v[16:19]\n"
      "s_add_u32 s38, s38, s38\n"
      "v_exp_f32 v194, v86\n"
      "v_mfma_f32_16x16x32_bf16 v[20:23], a[104:107], v[148:151], v[20:23]\n"
      "s_memrealtime s[68:69]\n"
      "v_exp_f32 v195, v87\n"
      "v_mfma_f32_16x16x32_bf16 a[160:163], a[104:107], v[152:155], a[160:163]\n"
      "v_cvt_pk_bf16_f32 v138, v192, v193\n"
      "v_cvt_pk_bf16_f32 v139, v194, v195\n"
      "v_mfma_f32_16x16x32_bf16 a[164:167], a[104:107], v[156:159], a[164:167]\n"
      "v_exp_f32 v188, v88\n"
      "v_mfma_f32_16x16x32_bf16 v[184:187], a[136:139], v[136:139], v[184:187]\n"
      "s_nop 0\n"
      "v_mfma_f32_16x16x32_bf16 a[172:175], a[108:111], v[156:159], a[172:175]\n"
      "s_nop 0\n"
      "v_mfma_f32_16x16x32_bf16 a[168:171], a[108:111], v[152:155], a[168:171]\n"
      "s_nop 0\n"
      "v_mfma_f32_16x16x32_bf16 v[28:31], a[108:111], v[148:151], v[28:31]\n"
      "s_nop 0\n"
      "s_cbranch_scc0 MH_%=\n"
      "END_%=:\n"
      "s_waitcnt lgkmcnt(0)\n"
      "v_mfma_f32_16x16x32_bf16 v[24:27], a[108:111], v[144:147], v[24:27]\n"
      "v_mov_b32 v236, s68\n"
      "v_exp_f32 v189, v89\n"
      "v_mfma_f32_16x16x32_bf16 v[32:35], a[112:115], v[144:147], v[32:35]\n"
      "s_mov_b64 exec, 1\n"
      "global_store_dwordx2 v238, v[236:237], s[46:47] offset:8 sc1\n"
      "s_mov_b64 exec, -1\n"
      "v_mfma_f32_16x16x32_bf16 v[36:39], a[112:115], v[148:151], v[36:39]\n"
      "s_mov_b32 s82, s68\n"
      "v_exp_f32 v190, v90\n"
      "v_mfma_f32_16x16x32_bf16 a[176:179], a[112:115], v[152:155], a[176:179]\n"
      "v_exp_f32 v191, v91\n"
      "v_mfma_f32_16x16x32_bf16 a[180:183], a[112:115], v[156:159], a[180:183]\n"
      "v_cvt_pk_bf16_f32 v140, v188, v189\n"
      "v_cvt_pk_bf16_f32 v141, v190, v191\n"
      "v_mfma_f32_16x16x32_bf16 a[188:191], a[116:119], v[156:159], a[188:191]\n"
      "v_exp_f32 v192, v92\n"
      "s_waitcnt vmcnt(5)\n"
      "s_barrier\n"
      "v_mfma_f32_16x16x32_bf16 a[184:187], a[116:119], v[152:155], a[184:187]\n"
      "ds_read_b64_tr_b16 a[80:81], v224 offset:0\n"
      "ds_read_b64_tr_b16 a[82:83], v224 offset:4096\n"
      "v_mfma_f32_16x16x32_bf16 v[44:47], a[116:119], v[148:151], v[44:47]\n"
      "ds_read_b64_tr_b16 a[84:85], v225 offset:0\n"
      "ds_read_b64_tr_b16 a[86:87], v225 offset:4096\n"
      "v_mfma_f32_16x16x32_bf16 v[40:43], a[116:119], v[144:147], v[40:43]\n"
      "s_mov_b32 s74, 0xffff0000\n"
      "ds_read_b64_tr_b16 a[88:89], v226 offset:0\n"
      "ds_read_b64_tr_b16 a[90:91], v226 offset:4096\n"
      "v_mfma_f32_16x16x32_bf16 v[48:51], a[120:123], v[144:147], v[48:51]\n"
      "s_mov_b32 s75, 0xffff0000\n"
      "ds_read_b64_tr_b16 a[92:93], v227 offset:0\n"
      "ds_read_b64_tr_b16 a[94:95], v227 offset:4096\n"
      "v_mfma_f32_16x16x32_bf16 v[52:55], a[120:123], v[148:151], v[52:55]\n"
      "s_mov_b32 s94, 0\n"
      "v_exp_f32 v193, v93\n"
      "v_mfma_f32_16x16x32_bf16 a[192:195], a[120:123], v[152:155], a[192:195]\n"
      "s_mov_b32 s95, -1\n"
      "ds_read_b64_tr_b16 a[16:17], v224 offset:1024\n"
      "v_cvt_pk_bf16_f32 v142, v192, v193\n"
      "v_mfma_f32_16x16x32_bf16 a[196:199], a[120:123], v[156:159], a[196:199]\n"
      "v_exp_f32 v194, v94\n"
      "v_mfma_f32_16x16x32_bf16 a[204:207], a[124:127], v[156:159], a[204:207]\n"
      "ds_read_b64_tr_b16 a[18:19], v224 offset:5120\n"
      "ds_read_b64_tr_b16 a[20:21], v225 offset:1024\n"
      "v_mfma_f32_16x16x32_bf16 a[200:203], a[124:127], v[152:155], a[200:203]\n"
      "v_exp_f32 v195, v95\n"
      "v_mfma_f32_16x16x32_bf16 v[60:63], a[124:127], v[148:151], v[60:63]\n"
      "ds_read_b64_tr_b16 a[22:23], v225 offset:5120\n"
      "v_cvt_pk_bf16_f32 v143, v194, v195\n"
      "v_mfma_f32_16x16x32_bf16 v[56:59], a[124:127], v[144:147], v[56:59]\n"
      "ds_read_b64_tr_b16 a[24:25], v226 offset:1024\n"
      "ds_read_b64_tr_b16 a[26:27], v226 offset:5120\n"
      "v_mfma_f32_16x16x32_bf16 v[184:187], a[140:143], v[140:143], v[184:187]\n"
      "ds_read_b64_tr_b16 a[28:29], v227 offset:1024\n"
      "ds_read_b64_tr_b16 a[30:31], v227 offset:5120\n"
      "ds_read_b64_tr_b16 a[0:1], v220 offset:1024\n"
      "ds_read_b64_tr_b16 a[2:3], v220 offset:5120\n"
      "ds_read_b64_tr_b16 a[4:5], v221 offset:1024\n"
      "ds_read_b64_tr_b16 a[6:7], v221 offset:5120\n"
      "ds_read_b64_tr_b16 a[8:9], v222 offset:1024\n"
      "ds_read_b64_tr_b16 a[10:11], v222 offset:5120\n"
      "ds_read_b64_tr_b16 a[12:13], v223 offset:1024\n"
      "ds_read_b64_tr_b16 a[14:15], v223 offset:5120\n"
      "v_max3_f32 v196, v184, v185, v186\n"
      "v_max_f32 v196, v196, v187\n"
      "v_cmp_nge_f32 vcc, 0x4b800000, v196\n"
      "s_cbranch_vccnz FS3_%=\n"
      "FR3_%=:\n"
      "s_waitcnt lgkmcnt(15)\n"
      "v_mfma_f32_16x16x32_bf16 a[176:179], a[80:83], v[136:139], a[176:179]\n"
      "v_add_f32 v180, v180, v184\n"
      "v_add_f32 v181, v181, v185\n"
      "v_mfma_f32_16x16x32_bf16 a[180:183], a[80:83], v[140:143], a[180:183]\n"
      "v_add_f32 v182, v182, v186\n"
      "v_add_f32 v183, v183, v187\n"
      "v_mfma_f32_16x16x32_bf16 a[184:187], a[84:87], v[136:139], a[184:187]\n"
      "v_exp_f32 v188, v112\n"
      "v_mfma_f32_16x16x32_bf16 a[188:191], a[84:87], v[140:143], a[188:191]\n"
      "ds_write_b128 v232, a[176:179] offset:0\n"
      "v_exp_f32 v189, v113\n"
      "v_mfma_f32_16x16x32_bf16 a[192:195], a[88:91], v[136:139], a[192:195]\n"
      "v_exp_f32 v190, v114\n"
      "v_mfma_f32_16x16x32_bf16 a[196:199], a[88:91], v[140:143], a[196:199]\n"
      "v_exp_f32 v191, v115\n"
      "v_mfma_f32_16x16x32_bf16 a[200:203], a[92:95], v[136:139], a[200:203]\n"
      "ds_write_b128 v232, a[180:183] offset:1024\n"
      "v_exp_f32 v192, v116\n"
      "v_mfma_f32_16x16x32_bf16 a[204:207], a[92:95], v[140:143], a[204:207]\n"
      "v_exp_f32 v193, v117\n"
      "v_exp_f32 v194, v118\n"
      "v_cvt_pk_bf16_f32 v152, v188, v189\n"
      "v_exp_f32 v195, v119\n"
      "v_cvt_pk_bf16_f32 v153, v190, v191\n"
      "ds_write_b128 v232, a[184:187] offset:2048\n"
      "v_exp_f32 v188, v120\n"
      "v_cvt_pk_bf16_f32 v154, v192, v193\n"
      "v_mfma_f32_16x16x32_bf16 v[32:35], a[80:83], v[128:131], v[32:35]\n"
      "v_exp_f32 v189, v121\n"
      "v_cvt_pk_bf16_f32 v155, v194, v195\n"
      "v_exp_f32 v190, v122\n"
      "v_cvt_pk_bf16_f32 v156, v188, v189\n"
      "v_mfma_f32_16x16x32_bf16 v[184:187], a[136:139], v[152:155], 0\n"
      "ds_write_b128 v234, v[152:155] offset:0\n"
      "v_exp_f32 v191, v123\n"
      "v_mfma_f32_16x16x32_bf16 v[36:39], a[80:83], v[132:135], v[36:39]\n"
      "v_exp_f32 v192, v124\n"
      "v_cvt_pk_bf16_f32 v157, v190, v191\n"
      "v_exp_f32 v193, v125\n"
      "v_exp_f32 v194, v126\n"
      "v_cvt_pk_bf16_f32 v158, v192, v193\n"
      "ds_write_b128 v232, a[188:191] offset:3072\n"
      "v_mfma_f32_16x16x32_bf16 v[40:43], a[84:87], v[128:131], v[40:43]\n"
      "v_exp_f32 v195, v127\n"
      "v_exp_f32 v188, v96\n"
      "v_cvt_pk_bf16_f32 v159, v194, v195\n"
      "v_exp_f32 v189, v97\n"
      "v_mfma_f32_16x16x32_bf16 v[44:47], a[84:87], v[132:135], v[44:47]\n"
      "v_exp_f32 v190, v98\n"
      "ds_write_b128 v234, v[156:159] offset:1024\n"
      "v_cvt_pk_bf16_f32 v144, v188, v189\n"
      "v_mfma_f32_16x16x32_bf16 v[184:187], a[140:143], v[156:159], v[184:187]\n"
      "v_exp_f32 v191, v99\n"
      "v_exp_f32 v192, v100\n"
      "v_cvt_pk_bf16_f32 v145, v190, v191\n"
      "v_mfma_f32_16x16x32_bf16 v[48:51], a[88:91], v[128:131], v[48:51]\n"
      "v_exp_f32 v193, v101\n"
      "ds_write_b128 v232, a[192:195] offset:4096\n"
      "v_exp_f32 v194, v102\n"
      "v_cvt_pk_bf16_f32 v146, v192, v193\n"
      "v_exp_f32 v195, v103\n"
      "v_mfma_f32_16x16x32_bf16 v[52:55], a[88:91], v[132:135], v[52:55]\n"
      "v_exp_f32 v188, v104\n"
      "v_cvt_pk_bf16_f32 v147, v194, v195\n"
      "v_exp_f32 v189, v105\n"
      "ds_write_b128 v232, a[196:199] offset:5120\n"
      "v_exp_f32 v190, v106\n"
      "v_cvt_pk_bf16_f32 v148, v188, v189\n"
      "v_mfma_f32_16x16x32_bf16 v[184:187], a[128:131], v[144:147], v[184:187]\n"
      "v_exp_f32 v191, v107\n"
      "v_exp_f32 v192, v108\n"
      "v_cvt_pk_bf16_f32 v149, v190, v191\n"
      "v_exp_f32 v193, v109\n"
      "ds_write_b128 v232, a[200:203] offset:6144\n"
      "v_mfma_f32_16x16x32_bf16 v[56:59], a[92:95], v[128:131], v[56:59]\n"
      "v_exp_f32 v194, v110\n"
      "v_cvt_pk_bf16_f32 v150, v192, v193\n"
      "v_exp_f32 v195, v111\n"
      "v_cndmask_b32 v229, v180, v181, s[74:75]\n"
      "v_cvt_pk_bf16_f32 v151, v194, v195\n"
      "v_mfma_f32_16x16x32_bf16 v[60:63], a[92:95], v[132:135], v[60:63]\n"
      "v_cndmask_b32 v200, v182, v183, s[74:75]\n"
      "ds_write_b128 v232, a[204:207] offset:7168\n"
      "v_cndmask_b32 v228, v176, v177, s[74:75]\n"
      "v_mfma_f32_16x16x32_bf16 v[184:187], a[132:135], v[148:151], v[184:187]\n"
      "s_nop 7\n"
      "v_max3_f32 v196, v184, v185, v186\n"
      "v_max_f32 v196, v196, v187\n"
      "v_cmp_nge_f32 vcc, 0x4b800000, v196\n"
      "s_cbranch_vccnz FS8_%=\n"
      "FR8_%=:\n"
      "v_cndmask_b32 v202, v186, v187, s[74:75]\n"
      "v_cndmask_b32 v201, v184, v185, s[74:75]\n"
      "v_add_f32 v200, v200, v202\n"
      "ds_write_b32 v234, v200 offset:4096\n"
      "v_add_f32 v229, v229, v201\n"
      "s_waitcnt lgkmcnt(8)\n"
      "v_mfma_f32_16x16x32_bf16 v[32:35], a[16:19], v[144:147], v[32:35]\n"
      "v_mfma_f32_16x16x32_bf16 v[36:39], a[16:19], v[148:151], v[36:39]\n"
      "v_mfma_f32_16x16x32_bf16 v[40:43], a[20:23], v[144:147], v[40:43]\n"
      "s_waitcnt lgkmcnt(0)\n"
      "s_barrier\n"
      "v_mfma_f32_16x16x32_bf16 v[44:47], a[20:23], v[148:151], v[44:47]\n"
      "ds_read_b32 v208, v209\n"
      "ds_read_b128 v[160:163], v235 offset:0\n"
      "v_mfma_f32_16x16x32_bf16 v[48:51], a[24:27], v[144:147], v[48:51]\n"
      "ds_read_b128 v[164:167], v235 offset:1024\n"
      "ds_read_b128 v[64:67], v233 offset:0\n"
      "v_mfma_f32_16x16x32_bf16 v[52:55], a[24:27], v[148:151], v[52:55]\n"
      "ds_read_b128 v[68:71], v233 offset:1024\n"
      "ds_read_b128 v[72:75], v233 offset:2048\n"
      "ds_read_b128 v[76:79], v233 offset:3072\n"
      "v_mfma_f32_16x16x32_bf16 v[56:59], a[28:31], v[144:147], v[56:59]\n"
      "ds_read_b128 v[80:83], v233 offset:4096\n"
      "ds_read_b128 v[84:87], v233 offset:5120\n"
      "v_mfma_f32_16x16x32_bf16 v[60:63], a[28:31], v[148:151], v[60:63]\n"
      "ds_read_b128 v[88:91], v233 offset:6144\n"
      "ds_read_b128 v[92:95], v233 offset:7168\n"
      "ds_read_b32 v204, v235 offset:4096\n"
      "s_waitcnt lgkmcnt(11)\n"
      "v_cmp_ne_u32 vcc, 0, v208\n"
      "s_cbranch_vccnz SLOW_%=\n"
      "AGAIN_%=:\n"
      "s_waitcnt lgkmcnt(9)\n"
      "v_mfma_f32_16x16x32_bf16 v[32:35], a[0:3], v[160:163], v[32:35]\n"
      "v_mfma_f32_16x16x32_bf16 v[36:39], a[0:3], v[164:167], v[36:39]\n"
      "v_mfma_f32_16x16x32_bf16 v[40:43], a[4:7], v[160:163], v[40:43]\n"
      "v_mfma_f32_16x16x32_bf16 v[44:47], a[4:7], v[164:167], v[44:47]\n"
      "v_mfma_f32_16x16x32_bf16 v[48:51], a[8:11], v[160:163], v[48:51]\n"
      "v_mfma_f32_16x16x32_bf16 v[52:55], a[8:11], v[164:167], v[52:55]\n"
      "v_mfma_f32_16x16x32_bf16 v[56:59], a[12:15], v[160:163], v[56:59]\n"
      "v_mfma_f32_16x16x32_bf16 v[60:63], a[12:15], v[164:167], v[60:63]\n"
      "global_load_dwordx2 v[206:207], v239, s[46:47] offset:8 sc1\n"
      "s_waitcnt lgkmcnt(7)\n"
      "v_pk_add_f32 v[32:33], v[32:33], v[64:65]\n"
      "v_pk_add_f32 v[34:35], v[34:35], v[66:67]\n"
      "v_pk_add_f32 v[36:37], v[36:37], v[68:69]\n"
      "v_pk_add_f32 v[38:39], v[38:39], v[70:71]\n"
      "v_cvt_pk_bf16_f32 v212, v32, v33\n"
      "v_cvt_pk_bf16_f32 v213, v34, v35\n"
      "v_cvt_pk_bf16_f32 v214, v36, v37\n"
      "v_cvt_pk_bf16_f32 v215, v38, v39\n"
      "buffer_store_dwordx4 v[212:215], v241, s[24:27], s78 offen offset:0 sc1\n"
      "s_waitcnt lgkmcnt(5)\n"
      "v_pk_add_f32 v[40:41], v[40:41], v[72:73]\n"
      "v_pk_add_f32 v[42:43], v[42:43], v[74:75]\n"
      "v_pk_add_f32 v[44:45], v[44:45], v[76:77]\n"
      "v_pk_add_f32 v[46:47], v[46:47], v[78:79]\n"
      "v_cvt_pk_bf16_f32 v216, v40, v41\n"
      "v_cvt_pk_bf16_f32 v217, v42, v43\n"
      "v_cvt_pk_bf16_f32 v218, v44, v45\n"
      "v_cvt_pk_bf16_f32 v219, v46, v47\n"
      "buffer_store_dwordx4 v[216:219], v241, s[24:27], s78 offen offset:1024 sc1\n"
      "s_waitcnt lgkmcnt(3)\n"
      "v_pk_add_f32 v[48:49], v[48:49], v[80:81]\n"
      "v_pk_add_f32 v[50:51], v[50:51], v[82:83]\n"
      "v_pk_add_f32 v[52:53], v[52:53], v[84:85]\n"
      "v_pk_add_f32 v[54:55], v[54:55], v[86:87]\n"
      "v_cvt_pk_bf16_f32 v220, v48, v49\n"
      "v_cvt_pk_bf16_f32 v221, v50, v51\n"
      "v_cvt_pk_bf16_f32 v222, v52, v53\n"
      "v_cvt_pk_bf16_f32 v223, v54, v55\n"
      "buffer_store_dwordx4 v[220:223], v241, s[24:27], s78 offen offset:2048 sc1\n"
      "s_waitcnt lgkmcnt(0)\n"
      "v_pk_add_f32 v[56:57], v[56:57], v[88:89]\n"
      "v_pk_add_f32 v[58:59], v[58:59], v[90:91]\n"
      "v_pk_add_f32 v[60:61], v[60:61], v[92:93]\n"
      "v_pk_add_f32 v[62:63], v[62:63], v[94:95]\n"
      "v_cvt_pk_bf16_f32 v224, v56, v57\n"
      "v_cvt_pk_bf16_f32 v225, v58, v59\n"
      "v_cvt_pk_bf16_f32 v226, v60, v61\n"
      "v_cvt_pk_bf16_f32 v227, v62, v63\n"
      "buffer_store_dwordx4 v[224:227], v241, s[24:27], s78 offen offset:3072 sc1\n"
      "v_add_f32 v229, v229, v204\n"
      "v_cndmask_b32 v230, v228, v229, s[94:95]\n"
      "v_add_u32 v231, s40, v230\n"
      "v_add3_u32 v231, v231, v212, v213\n"
      "v_add3_u32 v231, v231, v214, v215\n"
      "v_add3_u32 v231, v231, v216, v217\n"
      "v_add3_u32 v231, v231, v218, v219\n"
      "v_add3_u32 v231, v231, v220, v221\n"
      "v_add3_u32 v231, v231, v222, v223\n"
      "v_add3_u32 v231, v231, v224, v225\n"
      "v_add3_u32 v231, v231, v226, v227\n"
      "buffer_store_dwordx2 v[230:231], v240, s[24:27], s78 offen sc1\n"
      "v_xor_b32 v244, 0x10000, v244\n"
      "v_xor_b32 v245, 0x10000, v245\n"
      "v_xor_b32 v246, 0x10000, v246\n"
      "v_xor_b32 v247, 0x10000, v247\n"
      "s_waitcnt vmcnt(7)\n"
      "s_barrier\n"
      "ds_read_b64_tr_b16 a[64:65], v244 offset:16384\n"
      "ds_read_b64_tr_b16 a[66:67], v244 offset:20480\n"
      "ds_read_b64_tr_b16 a[68:69], v245 offset:16384\n"
      "ds_read_b64_tr_b16 a[70:71], v245 offset:20480\n"
      "ds_read_b64_tr_b16 a[72:73], v246 offset:16384\n"
      "ds_read_b64_tr_b16 a[74:75], v246 offset:20480\n"
      "ds_read_b64_tr_b16 a[76:77], v247 offset:16384\n"
      "ds_read_b64_tr_b16 a[78:79], v247 offset:20480\n"
      "s_waitcnt lgkmcnt(4)\n"
      "v_mfma_f32_16x16x32_bf16 a[144:147], a[64:67], v[136:139], a[144:147]\n"
      "ds_read_b64_tr_b16 a[96:97], v244 offset:17408\n"
      "ds_read_b64_tr_b16 a[98:99], v244 offset:21504\n"
      "v_mfma_f32_16x16x32_bf16 a[148:151], a[64:67], v[140:143], a[148:151]\n"
      "ds_read_b64_tr_b16 a[100:101], v245 offset:17408\n"
      "ds_read_b64_tr_b16 a[102:103], v245 offset:21504\n"
      "v_mfma_f32_16x16x32_bf16 a[152:155], a[68:71], v[136:139], a[152:155]\n"
      "ds_read_b64_tr_b16 a[104:105], v246 offset:17408\n"
      "ds_read_b64_tr_b16 a[106:107], v246 offset:21504\n"
      "v_mfma_f32_16x16x32_bf16 a[156:159], a[68:71], v[140:143], a[156:159]\n"
      "ds_read_b64_tr_b16 a[108:109], v247 offset:17408\n"
      "ds_read_b64_tr_b16 a[110:111], v247 offset:21504\n"
      "s_waitcnt lgkmcnt(8)\n"
      "v_mfma_f32_16x16x32_bf16 a[160:163], a[72:75], v[136:139], a[160:163]\n"
      "s_sub_u32 s76, 18432, s78\n"
      "v_mfma_f32_16x16x32_bf16 a[164:167], a[72:75], v[140:143], a[164:167]\n"
      "s_mov_b32 s63, 0x40000\n"
      "v_mfma_f32_16x16x32_bf16 a[168:171], a[76:79], v[136:139], a[168:171]\n"
      "v_mov_b32 v198, 0\n"
      "v_mfma_f32_16x16x32_bf16 a[172:175], a[76:79], v[140:143], a[172:175]\n"
      "v_and_b32 v197, 0xf0, v241\n"
      "s_waitcnt lgkmcnt(0)\n"
      "v_mfma_f32_16x16x32_bf16 a[144:147], a[96:99], v[152:155], a[144:147]\n"
      "v_lshrrev_b32 v196, 3, v241\n"
      "v_mfma_f32_16x16x32_bf16 a[148:151], a[96:99], v[156:159], a[148:151]\n"
      "v_and_b32 v196, 32, v196\n"
      "v_mfma_f32_16x16x32_bf16 a[152:155], a[100:103], v[152:155], a[152:155]\n"
      "v_lshl_or_b32 v196, v197, 4, v196\n"
      "v_mfma_f32_16x16x32_bf16 a[156:159], a[100:103], v[156:159], a[156:159]\n"
      "v_lshrrev_b32 v199, 5, v241\n"
      "v_mfma_f32_16x16x32_bf16 a[160:163], a[104:107], v[152:155], a[160:163]\n"
      "v_and_or_b32 v196, v199, 16, v196\n"
      "v_mfma_f32_16x16x32_bf16 a[164:167], a[104:107], v[156:159], a[164:167]\n"
      "v_mov_b32 v191, s40\n"
      "v_mfma_f32_16x16x32_bf16 a[168:171], a[108:111], v[152:155], a[168:171]\n"
      "v_mfma_f32_16x16x32_bf16 a[172:175], a[108:111], v[156:159], a[172:175]\n"
      "s_waitcnt vmcnt(0)\n"
      "v_subrev_u32 v205, s82, v206\n"
      "v_cmp_ne_u32 vcc, s40, v207\n"
      "s_cbranch_vccnz LAG_%=\n"
      "v_cmp_lt_i32 vcc, 10, v205\n"
      "s_cbranch_vccnz LAG_%=\n"
      "READ_%=:\n"
      "buffer_load_dwordx2 v[160:161], v240, s[24:27], s76 offen sc1\n"
      "buffer_load_dwordx4 v[96:99], v241, s[24:27], s76 offen offset:0 sc1\n"
      "buffer_load_dwordx4 v[100:103], v241, s[24:27], s76 offen offset:1024 sc1\n"
      "buffer_load_dwordx4 v[104:107], v241, s[24:27], s76 offen offset:2048 sc1\n"
      "buffer_load_dwordx4 v[108:111], v241, s[24:27], s76 offen offset:3072 sc1\n"
      "v_mfma_f32_16x16x32_bf16 v[0:3], a[64:67], v[128:131], v[0:3]\n"
      "v_mfma_f32_16x16x32_bf16 v[4:7], a[64:67], v[132:135], v[4:7]\n"
      "ds_write_b128 v232, a[144:147] offset:32768\n"
      "v_mfma_f32_16x16x32_bf16 v[8:11], a[68:71], v[128:131], v[8:11]\n"
      "v_mfma_f32_16x16x32_bf16 v[12:15], a[68:71], v[132:135], v[12:15]\n"
      "ds_write_b128 v232, a[148:151] offset:33792\n"
      "v_mfma_f32_16x16x32_bf16 v[16:19], a[72:75], v[128:131], v[16:19]\n"
      "v_mfma_f32_16x16x32_bf16 v[20:23], a[72:75], v[132:135], v[20:23]\n"
      "ds_write_b128 v232, a[152:155] offset:34816\n"
      "v_mfma_f32_16x16x32_bf16 v[24:27], a[76:79], v[128:131], v[24:27]\n"
      "v_mfma_f32_16x16x32_bf16 v[28:31], a[76:79], v[132:135], v[28:31]\n"
      "ds_write_b128 v232, a[156:159] offset:35840\n"
      "v_mfma_f32_16x16x32_bf16 v[0:3], a[96:99], v[144:147], v[0:3]\n"
      "v_mfma_f32_16x16x32_bf16 v[4:7], a[96:99], v[148:151], v[4:7]\n"
      "ds_write_b128 v232, a[160:163] offset:36864\n"
      "v_mfma_f32_16x16x32_bf16 v[8:11], a[100:103], v[144:147], v[8:11]\n"
      "v_mfma_f32_16x16x32_bf16 v[12:15], a[100:103], v[148:151], v[12:15]\n"
      "ds_write_b128 v232, a[164:167] offset:37888\n"
      "v_mfma_f32_16x16x32_bf16 v[16:19], a[104:107], v[144:147], v[16:19]\n"
      "v_mfma_f32_16x16x32_bf16 v[20:23], a[104:107], v[148:151], v[20:23]\n"
      "ds_write_b128 v232, a[168:171] offset:38912\n"
      "v_mfma_f32_16x16x32_bf16 v[24:27], a[108:111], v[144:147], v[24:27]\n"
      "v_mfma_f32_16x16x32_bf16 v[28:31], a[108:111], v[148:151], v[28:31]\n"
      "ds_write_b128 v232, a[172:175] offset:39936\n"
      "s_waitcnt lgkmcnt(0)\n"
      "s_barrier\n"
      "ds_read_b128 v[64:67], v233 offset:32768\n"
      "ds_read_b128 v[68:71], v233 offset:33792\n"
      "ds_read_b128 v[72:75], v233 offset:34816\n"
      "ds_read_b128 v[76:79], v233 offset:35840\n"
      "ds_read_b128 v[80:83], v233 offset:36864\n"
      "ds_read_b128 v[84:87], v233 offset:37888\n"
      "ds_read_b128 v[88:91], v233 offset:38912\n"
      "ds_read_b128 v[92:95], v233 offset:39936\n"
      "s_waitcnt lgkmcnt(6)\n"
      "v_pk_add_f32 v[0:1], v[0:1], v[64:65]\n"
      "v_pk_add_f32 v[2:3], v[2:3], v[66:67]\n"
      "v_pk_add_f32 v[4:5], v[4:5], v[68:69]\n"
      "v_pk_add_f32 v[6:7], v[6:7], v[70:71]\n"
      "s_waitcnt lgkmcnt(4)\n"
      "v_pk_add_f32 v[8:9], v[8:9], v[72:73]\n"
      "v_pk_add_f32 v[10:11], v[10:11], v[74:75]\n"
      "v_pk_add_f32 v[12:13], v[12:13], v[76:77]\n"
      "v_pk_add_f32 v[14:15], v[14:15], v[78:79]\n"
      "s_waitcnt lgkmcnt(2)\n"
      "v_pk_add_f32 v[16:17], v[16:17], v[80:81]\n"
      "v_pk_add_f32 v[18:19], v[18:19], v[82:83]\n"
      "v_pk_add_f32 v[20:21], v[20:21], v[84:85]\n"
      "v_pk_add_f32 v[22:23], v[22:23], v[86:87]\n"
      "s_waitcnt lgkmcnt(0)\n"
      "v_pk_add_f32 v[24:25], v[24:25], v[88:89]\n"
      "v_pk_add_f32 v[26:27], v[26:27], v[90:91]\n"
      "v_pk_add_f32 v[28:29], v[28:29], v[92:93]\n"
      "v_pk_add_f32 v[30:31], v[30:31], v[94:95]\n"
      "TRY_%=:\n"
      "s_waitcnt vmcnt(4)\n"
      "v_mov_b32 v162, v160\n"
      "v_add_u32 v231, s40, v160\n"
      "s_nop 0\n"
      "v_permlane32_swap_b32 v160, v162\n"
      "v_sub_f32 v168, v160, v228\n"
      "v_min_f32 v172, 0, -v168\n"
      "v_min_f32 v168, 0, v168\n"
      "v_exp_f32 v168, v168\n"
      "v_exp_f32 v172, v172\n"
      "v_mul_f32 v163, v229, v168\n"
      "v_fmac_f32 v163, v162, v172\n"
      "v_rcp_f32 v163, v163\n"
      "s_nop 0\n"
      "v_mul_f32 v168, v168, v163\n"
      "v_mul_f32 v172, v172, v163\n"
      "v_mov_b32 v169, v168\n"
      "v_mov_b32 v173, v172\n"
      "s_nop 0\n"
      "v_permlane16_swap_b32 v168, v169\n"
      "v_permlane16_swap_b32 v172, v173\n"
      "v_pk_mul_f32 v[32:33], v[0:1], v[168:169] op_sel:[0,0] op_sel_hi:[1,0]\n"
      "v_pk_mul_f32 v[34:35], v[2:3], v[168:169] op_sel:[0,0] op_sel_hi:[1,0]\n"
      "v_pk_mul_f32 v[36:37], v[4:5], v[168:169] op_sel:[0,1] op_sel_hi:[1,1]\n"
      "v_pk_mul_f32 v[38:39], v[6:7], v[168:169] op_sel:[0,1] op_sel_hi:[1,1]\n"
      "v_pk_mul_f32 v[40:41], v[8:9], v[168:169] op_sel:[0,0] op_sel_hi:[1,0]\n"
      "v_pk_mul_f32 v[42:43], v[10:11], v[168:169] op_sel:[0,0] op_sel_hi:[1,0]\n"
      "v_pk_mul_f32 v[44:45], v[12:13], v[168:169] op_sel:[0,1] op_sel_hi:[1,1]\n"
      "v_pk_mul_f32 v[46:47], v[14:15], v[168:169] op_sel:[0,1] op_sel_hi:[1,1]\n"
      "v_pk_mul_f32 v[48:49], v[16:17], v[168:169] op_sel:[0,0] op_sel_hi:[1,0]\n"
      "v_pk_mul_f32 v[50:51], v[18:19], v[168:169] op_sel:[0,0] op_sel_hi:[1,0]\n"
      "v_pk_mul_f32 v[52:53], v[20:21], v[168:169] op_sel:[0,1] op_sel_hi:[1,1]\n"
      "v_pk_mul_f32 v[54:55], v[22:23], v[168:169] op_sel:[0,1] op_sel_hi:[1,1]\n"
      "v_pk_mul_f32 v[56:57], v[24:25], v[168:169] op_sel:[0,0] op_sel_hi:[1,0]\n"
      "v_pk_mul_f32 v[58:59], v[26:27], v[168:169] op_sel:[0,0] op_sel_hi:[1,0]\n"
      "v_pk_mul_f32 v[60:61], v[28:29], v[168:169] op_sel:[0,1] op_sel_hi:[1,1]\n"
      "v_pk_mul_f32 v[62:63], v[30:31], v[168:169] op_sel:[0,1] op_sel_hi:[1,1]\n"
      "s_waitcnt vmcnt(3)\n"
      "v_add3_u32 v231, v231, v96, v97\n"
      "v_add3_u32 v231, v231, v98, v99\n"
      "s_waitcnt vmcnt(2)\n"
      "v_add3_u32 v231, v231, v100, v101\n"
      "v_add3_u32 v231, v231, v102, v103\n"
      "s_waitcnt vmcnt(1)\n"
      "v_add3_u32 v231, v231, v104, v105\n"
      "v_add3_u32 v231, v231, v106, v107\n"
      "s_waitcnt vmcnt(0)\n"
      "v_add3_u32 v231, v231, v108, v109\n"
      "v_add3_u32 v231, v231, v110, v111\n"
      "v_cmp_ne_u32 vcc, v231, v161\n"
      "s_cbranch_vccnz RETRY_%=\n"
      "GOT_%=:\n"
      "v_lshlrev_b32 v0, 16, v96\n"
      "v_and_b32 v1, 0xffff0000, v96\n"
      "v_lshlrev_b32 v2, 16, v97\n"
      "v_and_b32 v3, 0xffff0000, v97\n"
      "v_lshlrev_b32 v8, 16, v100\n"
      "v_and_b32 v9, 0xffff0000, v100\n"
      "v_lshlrev_b32 v10, 16, v101\n"
      "v_and_b32 v11, 0xffff0000, v101\n"
      "v_pk_fma_f32 v[32:33], v[0:1], v[172:173], v[32:33] op_sel:[0,0,0] op_sel_hi:[1,0,1]\n"
      "v_pk_fma_f32 v[34:35], v[2:3], v[172:173], v[34:35] op_sel:[0,0,0] op_sel_hi:[1,0,1]\n"
      "v_pk_fma_f32 v[40:41], v[8:9], v[172:173], v[40:41] op_sel:[0,0,0] op_sel_hi:[1,0,1]\n"
      "v_pk_fma_f32 v[42:43], v[10:11], v[172:173], v[42:43] op_sel:[0,0,0] op_sel_hi:[1,0,1]\n"
      "v_cvt_pk_bf16_f32 v112, v32, v33\n"
      "v_cvt_pk_bf16_f32 v114, v40, v41\n"
      "v_cvt_pk_bf16_f32 v113, v34, v35\n"
      "v_cvt_pk_bf16_f32 v115, v42, v43\n"
      "v_permlane16_swap_b32 v112, v114\n"
      "s_nop 0\n"
      "v_permlane16_swap_b32 v113, v115\n"
      "global_store_dwordx4 v196, v[112:115], s[30:31] offset:-4096 sc0 sc1 nt\n"
      "v_lshlrev_b32 v16, 16, v104\n"
      "v_and_b32 v17, 0xffff0000, v104\n"
      "v_lshlrev_b32 v18, 16, v105\n"
      "v_and_b32 v19, 0xffff0000, v105\n"
      "v_lshlrev_b32 v24, 16, v108\n"
      "v_and_b32 v25, 0xffff0000, v108\n"
      "v_lshlrev_b32 v26, 16, v109\n"
      "v_and_b32 v27, 0xffff0000, v109\n"
      "v_pk_fma_f32 v[48:49], v[16:17], v[172:173], v[48:49] op_sel:[0,0,0] op_sel_hi:[1,0,1]\n"
      "v_pk_fma_f32 v[50:51], v[18:19], v[172:173], v[50:51] op_sel:[0,0,0] op_sel_hi:[1,0,1]\n"
      "v_pk_fma_f32 v[56:57], v[24:25], v[172:173], v[56:57] op_sel:[0,0,0] op_sel_hi:[1,0,1]\n"
      "v_pk_fma_f32 v[58:59], v[26:27], v[172:173], v[58:59] op_sel:[0,0,0] op_sel_hi:[1,0,1]\n"
      "v_cvt_pk_bf16_f32 v116, v48, v49\n"
      "v_cvt_pk_bf16_f32 v118, v56, v57\n"
      "v_cvt_pk_bf16_f32 v117, v50, v51\n"
      "v_cvt_pk_bf16_f32 v119, v58, v59\n"
      "v_permlane16_swap_b32 v116, v118\n"
      "s_nop 0\n"
      "v_permlane16_swap_b32 v117, v119\n"
      "global_store_dwordx4 v196, v[116:119], s[30:31] offset:-4032 sc0 sc1 nt\n"
      "v_lshlrev_b32 v4, 16, v98\n"
      "v_and_b32 v5, 0xffff0000, v98\n"
      "v_lshlrev_b32 v6, 16, v99\n"
      "v_and_b32 v7, 0xffff0000, v99\n"
      "v_lshlrev_b32 v12, 16, v102\n"
      "v_and_b32 v13, 0xffff0000, v102\n"
      "v_lshlrev_b32 v14, 16, v103\n"
      "v_and_b32 v15, 0xffff0000, v103\n"
      "v_pk_fma_f32 v[36:37], v[4:5], v[172:173], v[36:37] op_sel:[0,1,0] op_sel_hi:[1,1,1]\n"
      "v_pk_fma_f32 v[38:39], v[6:7], v[172:173], v[38:39] op_sel:[0,1,0] op_sel_hi:[1,1,1]\n"
      "v_pk_fma_f32 v[44:45], v[12:13], v[172:173], v[44:45] op_sel:[0,1,0] op_sel_hi:[1,1,1]\n"
      "v_pk_fma_f32 v[46:47], v[14:15], v[172:173], v[46:47] op_sel:[0,1,0] op_sel_hi:[1,1,1]\n"
      "v_cvt_pk_bf16_f32 v120, v36, v37\n"
      "v_cvt_pk_bf16_f32 v122, v44, v45\n"
      "v_cvt_pk_bf16_f32 v121, v38, v39\n"
      "v_cvt_pk_bf16_f32 v123, v46, v47\n"
      "v_permlane16_swap_b32 v120, v122\n"
      "s_nop 0\n"
      "v_permlane16_swap_b32 v121, v123\n"
      "global_store_dwordx4 v196, v[120:123], s[30:31] offset:0 sc0 sc1 nt\n"
      "v_lshlrev_b32 v20, 16, v106\n"
      "v_and_b32 v21, 0xffff0000, v106\n"
      "v_lshlrev_b32 v22, 16, v107\n"
      "v_and_b32 v23, 0xffff0000, v107\n"
      "v_lshlrev_b32 v28, 16, v110\n"
      "v_and_b32 v29, 0xffff0000, v110\n"
      "v_lshlrev_b32 v30, 16, v111\n"
      "v_and_b32 v31, 0xffff0000, v111\n"
      "v_pk_fma_f32 v[52:53], v[20:21], v[172:173], v[52:53] op_sel:[0,1,0] op_sel_hi:[1,1,1]\n"
      "v_pk_fma_f32 v[54:55], v[22:23], v[172:173], v[54:55] op_sel:[0,1,0] op_sel_hi:[1,1,1]\n"
      "v_pk_fma_f32 v[60:61], v[28:29], v[172:173], v[60:61] op_sel:[0,1,0] op_sel_hi:[1,1,1]\n"
      "v_pk_fma_f32 v[62:63], v[30:31], v[172:173], v[62:63] op_sel:[0,1,0] op_sel_hi:[1,1,1]\n"
      "v_cvt_pk_bf16_f32 v124, v52, v53\n"
      "v_cvt_pk_bf16_f32 v126, v60, v61\n"
      "v_cvt_pk_bf16_f32 v125, v54, v55\n"
      "v_cvt_pk_bf16_f32 v127, v62, v63\n"
      "v_permlane16_swap_b32 v124, v126\n"
      "s_nop 0\n"
      "v_permlane16_swap_b32 v125, v127\n"
      "global_store_dwordx4 v196, v[124:127], s[30:31] offset:64 sc0 sc1 nt\n"
      "s_mov_b64 exec, 1\n"
      "global_store_dword v198, v191, s[46:47] sc1\n"
      "s_mov_b64 exec, -1\n"
      "ENDE_%=:\n"
      ".subsection 1\n"
      "MH_%=:\n"
      "s_cmp_lg_u32 s54, 0\n"
      "s_waitcnt vmcnt(12) lgkmcnt(0)\n"
      "s_cbranch_scc1 W6_%=\n"
      "s_waitcnt vmcnt(8)\n"
      "W6_%=:\n"
      "s_barrier\n"
      "v_mfma_f32_16x16x32_bf16 v[24:27], a[108:111], v[144:147], v[24:27]\n"
      "s_xor_b64 s[56:57], s[56:57], s[32:33]\n"
      "ds_read_b128 a[64:67], v242 offset:0\n"
      "ds_read_b128 a[72:75], v243 offset:0\n"
      "v_mfma_f32_16x16x32_bf16 v[32:35], a[112:115], v[144:147], v[32:35]\n"
      "s_add_u32 s61, s61, 1\n"
      "ds_read_b128 a[80:83], v242 offset:1024\n"
      "ds_read_b128 a[88:91], v243 offset:1024\n"
      "v_mfma_f32_16x16x32_bf16 v[36:39], a[112:115], v[148:151], v[36:39]\n"
      "s_add_u32 s62, s62, 0x4000\n"
      "v_exp_f32 v189, v89\n"
      "v_mfma_f32_16x16x32_bf16 a[176:179], a[112:115], v[152:155], a[176:179]\n"
      "s_and_b32 s62, s62, 0x1ffff\n"
      "v_exp_f32 v190, v90\n"
      "v_mfma_f32_16x16x32_bf16 a[180:183], a[112:115], v[156:159], a[180:183]\n"
      "v_readlane_b32 s60, v219, s61\n"
      "v_exp_f32 v191, v91\n"
      "v_mfma_f32_16x16x32_bf16 a[188:191], a[116:119], v[156:159], a[188:191]\n"
      "s_add_u32 s66, s66, s86\n"
      "v_cvt_pk_bf16_f32 v140, v188, v189\n"
      "v_mfma_f32_16x16x32_bf16 a[184:187], a[116:119], v[152:155], a[184:187]\n"
      "v_exp_f32 v192, v92\n"
      "v_mfma_f32_16x16x32_bf16 v[44:47], a[116:119], v[148:151], v[44:47]\n"
      "s_add_u32 s82, s82, s86\n"
      "v_exp_f32 v193, v93\n"
      "v_mfma_f32_16x16x32_bf16 v[40:43], a[116:119], v[144:147], v[40:43]\n"
      "s_add_u32 s65, s65, s86\n"
      "v_cvt_pk_bf16_f32 v141, v190, v191\n"
      "v_cvt_pk_bf16_f32 v142, v192, v193\n"
      "v_mfma_f32_16x16x32_bf16 v[48:51], a[120:123], v[144:147], v[48:51]\n"
      "s_max_u32 s88, s66, s65\n"
      "v_exp_f32 v194, v94\n"
      "v_mfma_f32_16x16x32_bf16 v[52:55], a[120:123], v[148:151], v[52:55]\n"
      "v_exp_f32 v195, v95\n"
      "s_waitcnt lgkmcnt(0)\n"
      "v_mfma_f32_16x16x32_bf16 a[192:195], a[120:123], v[152:155], a[192:195]\n"
      "v_cvt_pk_bf16_f32 v143, v194, v195\n"
      "v_xor_b32 v244, 0x10000, v244\n"
      "v_mfma_f32_16x16x32_bf16 a[196:199], a[120:123], v[156:159], a[196:199]\n"
      "ds_read_b128 a[68:71], v242 offset:4096\n"
      "ds_read_b128 a[76:79], v243 offset:4096\n"
      "v_mfma_f32_16x16x32_bf16 v[184:187], a[140:143], v[140:143], v[184:187]\n"
      "s_mov_b32 m0, s62\n"
      "ds_read_b128 a[84:87], v242 offset:5120\n"
      "ds_read_b128 a[92:95], v243 offset:5120\n"
      "v_mfma_f32_16x16x32_bf16 a[204:207], a[124:127], v[156:159], a[204:207]\n"
      "buffer_load_dwordx4 v228, s[56:59], s60 offen nt lds\n"
      "v_xor_b32 v245, 0x10000, v245\n"
      "v_xor_b32 v246, 0x10000, v246\n"
      "v_mfma_f32_16x16x32_bf16 a[200:203], a[124:127], v[152:155], a[200:203]\n"
      "buffer_load_dwordx4 v229, s[56:59], s60 offen offset:1024 nt lds\n"
      "v_xor_b32 v247, 0x10000, v247\n"
      "v_max3_f32 v196, v184, v185, v186\n"
      "v_mfma_f32_16x16x32_bf16 v[60:63], a[124:127], v[148:151], v[60:63]\n"
      "buffer_load_dwordx4 v230, s[56:59], s60 offen offset:2048 nt lds\n"
      "v_max_f32 v196, v196, v187\n"
      "v_cmp_nge_f32 vcc, 0x4b800000, v196\n"
      "v_mfma_f32_16x16x32_bf16 v[56:59], a[124:127], v[144:147], v[56:59]\n"
      "buffer_load_dwordx4 v231, s[56:59], s60 offen offset:3072 nt lds\n"
      "s_cbranch_vccnz FS11_%=\n"
      "FR11_%=:\n"
      "v_mfma_f32_16x16x32_bf16 v[64:67], a[64:67], a[0:3], v[160:163]\n"
      "v_exp_f32 v188, v96\n"
      "v_mfma_f32_16x16x32_bf16 v[64:67], a[72:75], a[16:19], v[64:67]\n"
      "v_add_f32 v180, v180, v184\n"
      "v_add_f32 v181, v181, v185\n"
      "v_mfma_f32_16x16x32_bf16 v[64:67], a[80:83], a[32:35], v[64:67]\n"
      "v_exp_f32 v189, v97\n"
      "v_mfma_f32_16x16x32_bf16 v[64:67], a[88:91], a[48:51], v[64:67]\n"
      "v_add_f32 v182, v182, v186\n"
      "v_add_f32 v183, v183, v187\n"
      "v_mfma_f32_16x16x32_bf16 v[72:75], a[64:67], a[4:7], v[164:167]\n"
      "v_exp_f32 v190, v98\n"
      "v_mfma_f32_16x16x32_bf16 v[72:75], a[72:75], a[20:23], v[72:75]\n"
      "v_exp_f32 v191, v99\n"
      "v_mfma_f32_16x16x32_bf16 v[72:75], a[80:83], a[36:39], v[72:75]\n"
      "v_cvt_pk_bf16_f32 v144, v188, v189\n"
      "v_cvt_pk_bf16_f32 v145, v190, v191\n"
      "v_mfma_f32_16x16x32_bf16 v[72:75], a[88:91], a[52:55], v[72:75]\n"
      "v_exp_f32 v192, v100\n"
      "v_mfma_f32_16x16x32_bf16 v[80:83], a[64:67], a[8:11], v[168:171]\n"
      "s_add_u32 s39, s39, s39\n"
      "v_exp_f32 v193, v101\n"
      "v_mfma_f32_16x16x32_bf16 v[80:83], a[72:75], a[24:27], v[80:83]\n"
      "s_memrealtime s[68:69]\n"
      "v_exp_f32 v194, v102\n"
      "v_mfma_f32_16x16x32_bf16 v[80:83], a[80:83], a[40:43], v[80:83]\n"
      "v_exp_f32 v195, v103\n"
      "v_mfma_f32_16x16x32_bf16 v[80:83], a[88:91], a[56:59], v[80:83]\n"
      "v_cvt_pk_bf16_f32 v146, v192, v193\n"
      "v_cvt_pk_bf16_f32 v147, v194, v195\n"
      "v_mfma_f32_16x16x32_bf16 v[88:91], a[64:67], a[12:15], v[172:175]\n"
      "s_nop 0\n"
      "v_mfma_f32_16x16x32_bf16 v[184:187], a[128:131], v[144:147], 0\n"
      "s_nop 0\n"
      "v_mfma_f32_16x16x32_bf16 v[88:91], a[72:75], a[28:31], v[88:91]\n"
      "s_nop 0\n"
      "v_mfma_f32_16x16x32_bf16 v[88:91], a[80:83], a[44:47], v[88:91]\n"
      "s_nop 0\n"
      "s_cbranch_scc1 SP1_%=\n"
      "s_branch HD1_%=\n"
      "HOOK_%=:\n"
      "buffer_load_dword v250, v251, s[20:23], 0 offen sc1\n"
      "s_swappc_b64 s[84:85], s[84:85]\n"
      "s_waitcnt vmcnt(12)\n"
      "v_subrev_u32 v250, s71, v250\n"
      "v_min_u32 v250, 0x258, v250\n"
      "s_nop 1\n"
      "v_add_u32_dpp v250, v250, v250 quad_perm:[1,0,3,2] row_mask:0xf bank_mask:0xf\n"
      "s_nop 1\n"
      "v_add_u32_dpp v250, v250, v250 quad_perm:[2,3,0,1] row_mask:0xf bank_mask:0xf\n"
      "s_nop 1\n"
      "v_add_u32_dpp v250, v250, v250 row_half_mirror row_mask:0xf bank_mask:0xf\n"
      "s_nop 0\n"
      "v_readfirstlane_b32 s67, v250\n"
      "s_sub_u32 s66, s66, s81\n"
      "s_lshr_b32 s67, s67, 3\n"
      "s_add_u32 s66, s66, s67\n"
      "s_max_u32 s88, s66, s65\n"
      "s_mov_b32 s86, 57\n"
      "s_mov_b32 s77, 171\n"
      "v_bfe_u32 v251, v241, 2, 7\n"
      "v_add_u32 v251, s79, v251\n"
      "buffer_load_dword v250, v251, s[20:23], 0 offen sc1\n"
      "s_swappc_b64 s[84:85], s[84:85]\n"
      "s_mov_b32 s86, 55\n"
      "s_mov_b32 s77, 165\n"
      "s_swappc_b64 s[84:85], s[84:85]\n"
      "s_mov_b32 s89, 0\n"
      "s_swappc_b64 s[84:85], s[84:85]\n"
      "s_mov_b32 s86, 54\n"
      "s_mov_b32 s77, 162\n"
      "s_swappc_b64 s[84:85], s[84:85]\n"
      "s_cmp_lt_u32 s89, 2\n"
      "s_cselect_b32 s67, 2048, s72\n"
      "s_mov_b32 s89, 0\n"
      "s_cmp_lt_u32 s67, 2048\n"
      "s_cbranch_scc0 NV0_%=\n"
      "v_and_b32 v216, 0x1f0, v241\n"
      "v_lshlrev_b32 v216, 4, v216\n"
      "v_mov_b32 v215, s82\n"
      "s_lshr_b32 s67, s67, 4\n"
      "s_add_u32 s67, s67, 8192\n"
      "buffer_store_dword v215, v216, s[20:23], s67 offen sc1\n"
      "NV0_%=:\n"
      "s_swappc_b64 s[84:85], s[84:85]\n"
      "buffer_load_dword v250, v251, s[20:23], 0 offen sc1\n"
      "s_swappc_b64 s[84:85], s[84:85]\n"
      "s_mov_b32 s86, 53\n"
      "s_mov_b32 s77, 159\n"
      "s_min_i32 s70, s70, 150\n"
      "s_waitcnt vmcnt(12)\n"
      "v_sub_u32 v250, s82, v250\n"
      "v_cmp_gt_u32 vcc, 1600, v250\n"
      "s_movk_i32 s76, 45\n"
      "s_nop 2\n"
      "s_bcnt1_i32_b32 s67, vcc_lo\n"
      "s_cmp_eq_u32 s70, 60\n"
      "s_cselect_b32 s67, 0, s67\n"
      "s_cmp_ge_u32 s67, 10\n"
      "s_cbranch_scc1 SM0_%=\n"
      "s_swappc_b64 s[84:85], s[84:85]\n"
      "s_cmp_lt_u32 s89, 4\n"
      "s_cselect_b32 s67, 2048, s72\n"
      "s_mov_b32 s89, 0\n"
      "s_cmp_lt_u32 s67, 2048\n"
      "s_cbranch_scc0 NV1_%=\n"
      "v_and_b32 v216, 0x1f0, v241\n"
      "v_lshlrev_b32 v216, 4, v216\n"
      "v_mov_b32 v215, s82\n"
      "s_lshr_b32 s67, s67, 4\n"
      "s_add_u32 s67, s67, 8192\n"
      "buffer_store_dword v215, v216, s[20:23], s67 offen sc1\n"
      "NV1_%=:\n"
      "s_swappc_b64 s[84:85], s[84:85]\n"
      "buffer_load_dword v250, v251, s[20:23], 0 offen sc1\n"
      "s_swappc_b64 s[84:85], s[84:85]\n"
      "s_waitcnt vmcnt(12)\n"
      "v_sub_u32 v250, s82, v250\n"
      "v_cmp_gt_u32 vcc, 1600, v250\n"
      "s_movk_i32 s76, 65\n"
      "s_nop 2\n"
      "s_bcnt1_i32_b32 s67, vcc_lo\n"
      "s_cmp_eq_u32 s70, 60\n"
      "s_cselect_b32 s67, 0, s67\n"
      "s_cmp_ge_u32 s67, 10\n"
      "s_cbranch_scc1 SM1_%=\n"
      "s_swappc_b64 s[84:85], s[84:85]\n"
      "s_getpc_b64 s[74:75]\n"
      "PF_%=:\n"
      "s_sub_u32 s74, s74, PF_%=-END_%=\n"
      "s_subb_u32 s75, s75, 0\n"
      "s_lshl_b32 s76, %[blk], 7\n"
      "s_and_b32 s76, s76, 0x1c00\n"
      "s_min_u32 s76, s76, ENDE_%=-END_%=-1024\n"
      "v_add_u32 v200, s76, v241\n"
      "global_load_dwordx4 v[200:203], v200, s[74:75]\n"
      "v_mov_b32 v212, 0\n"
      "global_load_dword v213, v212, s[30:31] nt\n"
      "buffer_load_dword v214, off, s[24:27], s78 nt\n"
      "s_swappc_b64 s[84:85], s[84:85]\n"
      "SM1_%=:\n"
      "s_add_u32 s66, s66, s76\n"
      "s_max_u32 s88, s66, s65\n"
      "s_min_i32 s70, s70, 150\n"
      "s_branch SM2_%=\n"
      "SM0_%=:\n"
      "s_or_b32 s39, s39, 0xc0000000\n"
      "s_mov_b32 s52, 0xaaaaaaaa\n"
      "s_mov_b32 s53, 0xaaaaaaaa\n"
      "s_add_u32 s66, s66, s76\n"
      "s_max_u32 s88, s66, s65\n"
      "s_min_i32 s70, s70, 150\n"
      "s_mov_b32 s86, 59\n"
      "s_mov_b32 s77, 177\n"
      "s_swappc_b64 s[84:85], s[84:85]\n"
      "s_mov_b32 s86, 57\n"
      "s_mov_b32 s77, 171\n"
      "s_swappc_b64 s[84:85], s[84:85]\n"
      "SM2_%=:\n"
      "s_mov_b32 s86, 55\n"
      "s_mov_b32 s77, 165\n"
      "s_mov_b32 s52, 0x0\n"
      "s_mov_b32 s53, 0x80000\n"
      "s_swappc_b64 s[84:85], s[84:85]\n"
      "s_getpc_b64 s[74:75]\n"
      "PG_%=:\n"
      "s_sub_u32 s74, s74, PG_%=-END_%=\n"
      "s_subb_u32 s75, s75, 0\n"
      "s_lshl_b32 s76, %[blk], 7\n"
      "s_and_b32 s76, s76, 0x1c00\n"
      "s_min_u32 s76, s76, ENDE_%=-END_%=-1024\n"
      "v_add_u32 v200, s76, v241\n"
      "global_load_dwordx4 v[200:203], v200, s[74:75]\n"
      "v_mov_b32 v212, 0\n"
      "global_load_dword v213, v212, s[30:31] nt\n"
      "buffer_load_dword v214, off, s[24:27], s78 nt\n"
      "s_swappc_b64 s[84:85], s[84:85]\n"
      "GW0_%=:\n"
      "s_memrealtime s[68:69]\n"
      "s_waitcnt lgkmcnt(0)\n"
      "s_sub_u32 s67, s68, s88\n"
      "s_cmp_ge_i32 s67, 0\n"
      "s_cbranch_scc1 GX0_%=\n"
      "s_branch GW0_%=\n"
      "FS1_%=:\n"
      "s_call_b64 s[96:97], FB1_%=\n"
      "s_branch FR1_%=\n"
      "FS2_%=:\n"
      "s_call_b64 s[96:97], FB0_%=\n"
      "s_branch FR2_%=\n"
      "FS3_%=:\n"
      "s_call_b64 s[96:97], FB0_%=\n"
      "s_branch FR3_%=\n"
      "GW4_%=:\n"
      "s_memrealtime s[68:69]\n"
      "s_waitcnt lgkmcnt(0)\n"
      "s_sub_u32 s67, s68, s88\n"
      "s_cmp_ge_i32 s67, 0\n"
      "s_cbranch_scc1 GX4_%=\n"
      "s_branch GW4_%=\n"
      "GW5_%=:\n"
      "s_memrealtime s[68:69]\n"
      "s_waitcnt lgkmcnt(0)\n"
      "s_sub_u32 s67, s68, s88\n"
      "s_cmp_ge_i32 s67, 0\n"
      "s_cbranch_scc1 GX5_%=\n"
      "s_branch GW5_%=\n"
      "GW6_%=:\n"
      "s_memrealtime s[68:69]\n"
      "s_waitcnt lgkmcnt(0)\n"
      "s_sub_u32 s67, s68, s88\n"
      "s_cmp_ge_i32 s67, 0\n"
      "s_cbranch_scc1 GX6_%=\n"
      "s_branch GW6_%=\n"
      "GW7_%=:\n"
      "s_memrealtime s[68:69]\n"
      "s_waitcnt lgkmcnt(0)\n"
      "s_sub_u32 s67, s68, s88\n"
      "s_cmp_ge_i32 s67, 0\n"
      "s_cbranch_scc1 GX7_%=\n"
      "s_branch GW7_%=\n"
      "FS8_%=:\n"
      "s_call_b64 s[96:97], FB1_%=\n"
      "s_branch FR8_%=\n"
      "LAG_%=:\n"
      "s_memrealtime s[68:69]\n"
      "s_waitcnt lgkmcnt(0)\n"
      "s_mov_b32 s88, s68\n"
      "LG1_%=:\n"
      "v_cmp_ne_u32 vcc, s40, v207\n"
      "s_cbranch_vccz LG2_%=\n"
      "s_sub_u32 s63, s63, 1\n"
      "s_cmp_eq_u32 s63, 0\n"
      "s_cbranch_scc1 READ_%=\n"
      "s_sleep 4\n"
      "global_load_dwordx2 v[206:207], v239, s[46:47] offset:8 sc1\n"
      "s_waitcnt vmcnt(0)\n"
      "s_branch LG1_%=\n"
      "LG2_%=:\n"
      "v_readfirstlane_b32 s67, v206\n"
      "s_sub_u32 s88, s88, s82\n"
      "s_min_u32 s88, s88, 400\n"
      "s_add_u32 s88, s88, s67\n"
      "LG3_%=:\n"
      "s_memrealtime s[68:69]\n"
      "s_waitcnt lgkmcnt(0)\n"
      "s_sub_u32 s67, s88, s68\n"
      "s_sub_u32 s67, s67, 11\n"
      "s_cmp_lt_u32 s67, 400\n"
      "s_cbranch_scc1 LG3_%=\n"
      "s_branch READ_%=\n"
      "RETRY_%=:\n"
      "s_sub_u32 s63, s63, 1\n"
      "s_cbranch_scc1 GOT_%=\n"
      "buffer_load_dwordx2 v[160:161], v240, s[24:27], s76 offen sc1\n"
      "buffer_load_dwordx4 v[96:99], v241, s[24:27], s76 offen offset:0 sc1\n"
      "buffer_load_dwordx4 v[100:103], v241, s[24:27], s76 offen offset:1024 sc1\n"
      "buffer_load_dwordx4 v[104:107], v241, s[24:27], s76 offen offset:2048 sc1\n"
      "buffer_load_dwordx4 v[108:111], v241, s[24:27], s76 offen offset:3072 sc1\n"
      "s_branch TRY_%=\n"
      "FS11_%=:\n"
      "s_call_b64 s[96:97], FB0_%=\n"
      "s_branch FR11_%=\n"
      "COLD_%=:\n"
      "FB0_%=:\n"
      "v_mov_b32 v196, 139248\n"
      "s_nop 7\n"
      "ds_write_b32 v196, v196\n"
      "v_max3_f32 v188, v64, v65, 0\n"
      "v_max3_f32 v189, v72, v73, 0\n"
      "v_max3_f32 v190, v80, v81, 0\n"
      "v_max3_f32 v191, v88, v89, 0\n"
      "v_max3_f32 v188, v188, v66, v67\n"
      "v_max3_f32 v189, v189, v74, v75\n"
      "v_max3_f32 v190, v190, v82, v83\n"
      "v_max3_f32 v191, v191, v90, v91\n"
      "v_max3_f32 v188, v188, v68, v69\n"
      "v_max3_f32 v189, v189, v76, v77\n"
      "v_max3_f32 v190, v190, v84, v85\n"
      "v_max3_f32 v191, v191, v92, v93\n"
      "v_max3_f32 v188, v188, v70, v71\n"
      "v_max3_f32 v189, v189, v78, v79\n"
      "v_max3_f32 v190, v190, v86, v87\n"
      "v_max3_f32 v191, v191, v94, v95\n"
      "v_mov_b32 v192, v188\n"
      "v_mov_b32 v193, v189\n"
      "v_mov_b32 v194, v190\n"
      "v_mov_b32 v195, v191\n"
      "v_permlane16_swap_b32 v188, v192\n"
      "v_permlane16_swap_b32 v189, v193\n"
      "v_permlane16_swap_b32 v190, v194\n"
      "v_permlane16_swap_b32 v191, v195\n"
      "v_max_f32 v188, v188, v192\n"
      "v_max_f32 v189, v189, v193\n"
      "v_max_f32 v190, v190, v194\n"
      "v_max_f32 v191, v191, v195\n"
      "v_mov_b32 v192, v188\n"
      "v_mov_b32 v193, v189\n"
      "v_mov_b32 v194, v190\n"
      "v_mov_b32 v195, v191\n"
      "v_permlane32_swap_b32 v188, v192\n"
      "v_permlane32_swap_b32 v189, v193\n"
      "v_permlane32_swap_b32 v190, v194\n"
      "v_permlane32_swap_b32 v191, v195\n"
      "v_max_f32 v188, v188, v192\n"
      "v_max_f32 v189, v189, v193\n"
      "v_max_f32 v190, v190, v194\n"
      "v_max_f32 v191, v191, v195\n"
      "RS0_%=:\n"
      "v_exp_f32 v192, -v188\n"
      "v_exp_f32 v193, -v189\n"
      "v_exp_f32 v194, -v190\n"
      "v_exp_f32 v195, -v191\n"
      "v_sub_f32 v176, v176, v188\n"
      "v_sub_f32 v177, v177, v189\n"
      "v_sub_f32 v178, v178, v190\n"
      "v_sub_f32 v179, v179, v191\n"
      "v_mov_b32 v160, v176\n"
      "v_mov_b32 v161, v176\n"
      "v_mov_b32 v162, v176\n"
      "v_mov_b32 v163, v176\n"
      "v_mov_b32 v164, v177\n"
      "v_mov_b32 v165, v177\n"
      "v_mov_b32 v166, v177\n"
      "v_mov_b32 v167, v177\n"
      "v_mov_b32 v168, v178\n"
      "v_mov_b32 v169, v178\n"
      "v_mov_b32 v170, v178\n"
      "v_mov_b32 v171, v178\n"
      "v_mov_b32 v172, v179\n"
      "v_mov_b32 v173, v179\n"
      "v_mov_b32 v174, v179\n"
      "v_mov_b32 v175, v179\n"
      "v_sub_f32 v64, v64, v188\n"
      "v_sub_f32 v65, v65, v188\n"
      "v_sub_f32 v66, v66, v188\n"
      "v_sub_f32 v67, v67, v188\n"
      "v_sub_f32 v68, v68, v188\n"
      "v_sub_f32 v69, v69, v188\n"
      "v_sub_f32 v70, v70, v188\n"
      "v_sub_f32 v71, v71, v188\n"
      "v_sub_f32 v72, v72, v189\n"
      "v_sub_f32 v73, v73, v189\n"
      "v_sub_f32 v74, v74, v189\n"
      "v_sub_f32 v75, v75, v189\n"
      "v_sub_f32 v76, v76, v189\n"
      "v_sub_f32 v77, v77, v189\n"
      "v_sub_f32 v78, v78, v189\n"
      "v_sub_f32 v79, v79, v189\n"
      "v_sub_f32 v80, v80, v190\n"
      "v_sub_f32 v81, v81, v190\n"
      "v_sub_f32 v82, v82, v190\n"
      "v_sub_f32 v83, v83, v190\n"
      "v_sub_f32 v84, v84, v190\n"
      "v_sub_f32 v85, v85, v190\n"
      "v_sub_f32 v86, v86, v190\n"
      "v_sub_f32 v87, v87, v190\n"
      "v_sub_f32 v88, v88, v191\n"
      "v_sub_f32 v89, v89, v191\n"
      "v_sub_f32 v90, v90, v191\n"
      "v_sub_f32 v91, v91, v191\n"
      "v_sub_f32 v92, v92, v191\n"
      "v_sub_f32 v93, v93, v191\n"
      "v_sub_f32 v94, v94, v191\n"
      "v_sub_f32 v95, v95, v191\n"
      "v_sub_f32 v96, v96, v188\n"
      "v_sub_f32 v97, v97, v188\n"
      "v_sub_f32 v98, v98, v188\n"
      "v_sub_f32 v99, v99, v188\n"
      "v_sub_f32 v100, v100, v188\n"
      "v_sub_f32 v101, v101, v188\n"
      "v_sub_f32 v102, v102, v188\n"
      "v_sub_f32 v103, v103, v188\n"
      "v_sub_f32 v104, v104, v189\n"
      "v_sub_f32 v105, v105, v189\n"
      "v_sub_f32 v106, v106, v189\n"
      "v_sub_f32 v107, v107, v189\n"
      "v_sub_f32 v108, v108, v189\n"
      "v_sub_f32 v109, v109, v189\n"
      "v_sub_f32 v110, v110, v189\n"
      "v_sub_f32 v111, v111, v189\n"
      "v_sub_f32 v112, v112, v190\n"
      "v_sub_f32 v113, v113, v190\n"
      "v_sub_f32 v114, v114, v190\n"
      "v_sub_f32 v115, v115, v190\n"
      "v_sub_f32 v116, v116, v190\n"
      "v_sub_f32 v117, v117, v190\n"
      "v_sub_f32 v118, v118, v190\n"
      "v_sub_f32 v119, v119, v190\n"
      "v_sub_f32 v120, v120, v191\n"
      "v_sub_f32 v121, v121, v191\n"
      "v_sub_f32 v122, v122, v191\n"
      "v_sub_f32 v123, v123, v191\n"
      "v_sub_f32 v124, v124, v191\n"
      "v_sub_f32 v125, v125, v191\n"
      "v_sub_f32 v126, v126, v191\n"
      "v_sub_f32 v127, v127, v191\n"
      "v_mul_f32 v0, v0, v192\n"
      "v_mul_f32 v1, v1, v192\n"
      "v_mul_f32 v2, v2, v192\n"
      "v_mul_f32 v3, v3, v192\n"
      "v_mul_f32 v4, v4, v193\n"
      "v_mul_f32 v5, v5, v193\n"
      "v_mul_f32 v6, v6, v193\n"
      "v_mul_f32 v7, v7, v193\n"
      "v_mul_f32 v8, v8, v192\n"
      "v_mul_f32 v9, v9, v192\n"
      "v_mul_f32 v10, v10, v192\n"
      "v_mul_f32 v11, v11, v192\n"
      "v_mul_f32 v12, v12, v193\n"
      "v_mul_f32 v13, v13, v193\n"
      "v_mul_f32 v14, v14, v193\n"
      "v_mul_f32 v15, v15, v193\n"
      "v_mul_f32 v16, v16, v192\n"
      "v_mul_f32 v17, v17, v192\n"
      "v_mul_f32 v18, v18, v192\n"
      "v_mul_f32 v19, v19, v192\n"
      "v_mul_f32 v20, v20, v193\n"
      "v_mul_f32 v21, v21, v193\n"
      "v_mul_f32 v22, v22, v193\n"
      "v_mul_f32 v23, v23, v193\n"
      "v_mul_f32 v24, v24, v192\n"
      "v_mul_f32 v25, v25, v192\n"
      "v_mul_f32 v26, v26, v192\n"
      "v_mul_f32 v27, v27, v192\n"
      "v_mul_f32 v28, v28, v193\n"
      "v_mul_f32 v29, v29, v193\n"
      "v_mul_f32 v30, v30, v193\n"
      "v_mul_f32 v31, v31, v193\n"
      "v_mul_f32 v32, v32, v192\n"
      "v_mul_f32 v33, v33, v192\n"
      "v_mul_f32 v34, v34, v192\n"
      "v_mul_f32 v35, v35, v192\n"
      "v_mul_f32 v36, v36, v193\n"
      "v_mul_f32 v37, v37, v193\n"
      "v_mul_f32 v38, v38, v193\n"
      "v_mul_f32 v39, v39, v193\n"
      "v_mul_f32 v40, v40, v192\n"
      "v_mul_f32 v41, v41, v192\n"
      "v_mul_f32 v42, v42, v192\n"
      "v_mul_f32 v43, v43, v192\n"
      "v_mul_f32 v44, v44, v193\n"
      "v_mul_f32 v45, v45, v193\n"
      "v_mul_f32 v46, v46, v193\n"
      "v_mul_f32 v47, v47, v193\n"
      "v_mul_f32 v48, v48, v192\n"
      "v_mul_f32 v49, v49, v192\n"
      "v_mul_f32 v50, v50, v192\n"
      "v_mul_f32 v51, v51, v192\n"
      "v_mul_f32 v52, v52, v193\n"
      "v_mul_f32 v53, v53, v193\n"
      "v_mul_f32 v54, v54, v193\n"
      "v_mul_f32 v55, v55, v193\n"
      "v_mul_f32 v56, v56, v192\n"
      "v_mul_f32 v57, v57, v192\n"
      "v_mul_f32 v58, v58, v192\n"
      "v_mul_f32 v59, v59, v192\n"
      "v_mul_f32 v60, v60, v193\n"
      "v_mul_f32 v61, v61, v193\n"
      "v_mul_f32 v62, v62, v193\n"
      "v_mul_f32 v63, v63, v193\n"
      "v_accvgpr_read_b32 v196, a144\n"
      "v_accvgpr_read_b32 v197, a145\n"
      "v_accvgpr_read_b32 v198, a146\n"
      "v_accvgpr_read_b32 v199, a147\n"
      "v_mul_f32 v196, v196, v194\n"
      "v_mul_f32 v197, v197, v194\n"
      "v_mul_f32 v198, v198, v194\n"
      "v_mul_f32 v199, v199, v194\n"
      "v_accvgpr_write_b32 a144, v196\n"
      "v_accvgpr_write_b32 a145, v197\n"
      "v_accvgpr_write_b32 a146, v198\n"
      "v_accvgpr_write_b32 a147, v199\n"
      "v_accvgpr_read_b32 v196, a148\n"
      "v_accvgpr_read_b32 v197, a149\n"
      "v_accvgpr_read_b32 v198, a150\n"
      "v_accvgpr_read_b32 v199, a151\n"
      "v_mul_f32 v196, v196, v195\n"
      "v_mul_f32 v197, v197, v195\n"
      "v_mul_f32 v198, v198, v195\n"
      "v_mul_f32 v199, v199, v195\n"
      "v_accvgpr_write_b32 a148, v196\n"
      "v_accvgpr_write_b32 a149, v197\n"
      "v_accvgpr_write_b32 a150, v198\n"
      "v_accvgpr_write_b32 a151, v199\n"
      "v_accvgpr_read_b32 v196, a152\n"
      "v_accvgpr_read_b32 v197, a153\n"
      "v_accvgpr_read_b32 v198, a154\n"
      "v_accvgpr_read_b32 v199, a155\n"
      "v_mul_f32 v196, v196, v194\n"
      "v_mul_f32 v197, v197, v194\n"
      "v_mul_f32 v198, v198, v194\n"
      "v_mul_f32 v199, v199, v194\n"
      "v_accvgpr_write_b32 a152, v196\n"
      "v_accvgpr_write_b32 a153, v197\n"
      "v_accvgpr_write_b32 a154, v198\n"
      "v_accvgpr_write_b32 a155, v199\n"
      "v_accvgpr_read_b32 v196, a156\n"
      "v_accvgpr_read_b32 v197, a157\n"
      "v_accvgpr_read_b32 v198, a158\n"
      "v_accvgpr_read_b32 v199, a159\n"
      "v_mul_f32 v196, v196, v195\n"
      "v_mul_f32 v197, v197, v195\n"
      "v_mul_f32 v198, v198, v195\n"
      "v_mul_f32 v199, v199, v195\n"
      "v_accvgpr_write_b32 a156, v196\n"
      "v_accvgpr_write_b32 a157, v197\n"
      "v_accvgpr_write_b32 a158, v198\n"
      "v_accvgpr_write_b32 a159, v199\n"
      "v_accvgpr_read_b32 v196, a160\n"
      "v_accvgpr_read_b32 v197, a161\n"
      "v_accvgpr_read_b32 v198, a162\n"
      "v_accvgpr_read_b32 v199, a163\n"
      "v_mul_f32 v196, v196, v194\n"
      "v_mul_f32 v197, v197, v194\n"
      "v_mul_f32 v198, v198, v194\n"
      "v_mul_f32 v199, v199, v194\n"
      "v_accvgpr_write_b32 a160, v196\n"
      "v_accvgpr_write_b32 a161, v197\n"
      "v_accvgpr_write_b32 a162, v198\n"
      "v_accvgpr_write_b32 a163, v199\n"
      "v_accvgpr_read_b32 v196, a164\n"
      "v_accvgpr_read_b32 v197, a165\n"
      "v_accvgpr_read_b32 v198, a166\n"
      "v_accvgpr_read_b32 v199, a167\n"
      "v_mul_f32 v196, v196, v195\n"
      "v_mul_f32 v197, v197, v195\n"
      "v_mul_f32 v198, v198, v195\n"
      "v_mul_f32 v199, v199, v195\n"
      "v_accvgpr_write_b32 a164, v196\n"
      "v_accvgpr_write_b32 a165, v197\n"
      "v_accvgpr_write_b32 a166, v198\n"
      "v_accvgpr_write_b32 a167, v199\n"
      "v_accvgpr_read_b32 v196, a168\n"
      "v_accvgpr_read_b32 v197, a169\n"
      "v_accvgpr_read_b32 v198, a170\n"
      "v_accvgpr_read_b32 v199, a171\n"
      "v_mul_f32 v196, v196, v194\n"
      "v_mul_f32 v197, v197, v194\n"
      "v_mul_f32 v198, v198, v194\n"
      "v_mul_f32 v199, v199, v194\n"
      "v_accvgpr_write_b32 a168, v196\n"
      "v_accvgpr_write_b32 a169, v197\n"
      "v_accvgpr_write_b32 a170, v198\n"
      "v_accvgpr_write_b32 a171, v199\n"
      "v_accvgpr_read_b32 v196, a172\n"
      "v_accvgpr_read_b32 v197, a173\n"
      "v_accvgpr_read_b32 v198, a174\n"
      "v_accvgpr_read_b32 v199, a175\n"
      "v_mul_f32 v196, v196, v195\n"
      "v_mul_f32 v197, v197, v195\n"
      "v_mul_f32 v198, v198, v195\n"
      "v_mul_f32 v199, v199, v195\n"
      "v_accvgpr_write_b32 a172, v196\n"
      "v_accvgpr_write_b32 a173, v197\n"
      "v_accvgpr_write_b32 a174, v198\n"
      "v_accvgpr_write_b32 a175, v199\n"
      "v_accvgpr_read_b32 v196, a176\n"
      "v_accvgpr_read_b32 v197, a177\n"
      "v_accvgpr_read_b32 v198, a178\n"
      "v_accvgpr_read_b32 v199, a179\n"
      "v_mul_f32 v196, v196, v194\n"
      "v_mul_f32 v197, v197, v194\n"
      "v_mul_f32 v198, v198, v194\n"
      "v_mul_f32 v199, v199, v194\n"
      "v_accvgpr_write_b32 a176, v196\n"
      "v_accvgpr_write_b32 a177, v197\n"
      "v_accvgpr_write_b32 a178, v198\n"
      "v_accvgpr_write_b32 a179, v199\n"
      "v_accvgpr_read_b32 v196, a180\n"
      "v_accvgpr_read_b32 v197, a181\n"
      "v_accvgpr_read_b32 v198, a182\n"
      "v_accvgpr_read_b32 v199, a183\n"
      "v_mul_f32 v196, v196, v195\n"
      "v_mul_f32 v197, v197, v195\n"
      "v_mul_f32 v198, v198, v195\n"
      "v_mul_f32 v199, v199, v195\n"
      "v_accvgpr_write_b32 a180, v196\n"
      "v_accvgpr_write_b32 a181, v197\n"
      "v_accvgpr_write_b32 a182, v198\n"
      "v_accvgpr_write_b32 a183, v199\n"
      "v_accvgpr_read_b32 v196, a184\n"
      "v_accvgpr_read_b32 v197, a185\n"
      "v_accvgpr_read_b32 v198, a186\n"
      "v_accvgpr_read_b32 v199, a187\n"
      "v_mul_f32 v196, v196, v194\n"
      "v_mul_f32 v197, v197, v194\n"
      "v_mul_f32 v198, v198, v194\n"
      "v_mul_f32 v199, v199, v194\n"
      "v_accvgpr_write_b32 a184, v196\n"
      "v_accvgpr_write_b32 a185, v197\n"
      "v_accvgpr_write_b32 a186, v198\n"
      "v_accvgpr_write_b32 a187, v199\n"
      "v_accvgpr_read_b32 v196, a188\n"
      "v_accvgpr_read_b32 v197, a189\n"
      "v_accvgpr_read_b32 v198, a190\n"
      "v_accvgpr_read_b32 v199, a191\n"
      "v_mul_f32 v196, v196, v195\n"
      "v_mul_f32 v197, v197, v195\n"
      "v_mul_f32 v198, v198, v195\n"
      "v_mul_f32 v199, v199, v195\n"
      "v_accvgpr_write_b32 a188, v196\n"
      "v_accvgpr_write_b32 a189, v197\n"
      "v_accvgpr_write_b32 a190, v198\n"
      "v_accvgpr_write_b32 a191, v199\n"
      "v_accvgpr_read_b32 v196, a192\n"
      "v_accvgpr_read_b32 v197, a193\n"
      "v_accvgpr_read_b32 v198, a194\n"
      "v_accvgpr_read_b32 v199, a195\n"
      "v_mul_f32 v196, v196, v194\n"
      "v_mul_f32 v197, v197, v194\n"
      "v_mul_f32 v198, v198, v194\n"
      "v_mul_f32 v199, v199, v194\n"
      "v_accvgpr_write_b32 a192, v196\n"
      "v_accvgpr_write_b32 a193, v197\n"
      "v_accvgpr_write_b32 a194, v198\n"
      "v_accvgpr_write_b32 a195, v199\n"
      "v_accvgpr_read_b32 v196, a196\n"
      "v_accvgpr_read_b32 v197, a197\n"
      "v_accvgpr_read_b32 v198, a198\n"
      "v_accvgpr_read_b32 v199, a199\n"
      "v_mul_f32 v196, v196, v195\n"
      "v_mul_f32 v197, v197, v195\n"
      "v_mul_f32 v198, v198, v195\n"
      "v_mul_f32 v199, v199, v195\n"
      "v_accvgpr_write_b32 a196, v196\n"
      "v_accvgpr_write_b32 a197, v197\n"
      "v_accvgpr_write_b32 a198, v198\n"
      "v_accvgpr_write_b32 a199, v199\n"
      "v_accvgpr_read_b32 v196, a200\n"
      "v_accvgpr_read_b32 v197, a201\n"
      "v_accvgpr_read_b32 v198, a202\n"
      "v_accvgpr_read_b32 v199, a203\n"
      "v_mul_f32 v196, v196, v194\n"
      "v_mul_f32 v197, v197, v194\n"
      "v_mul_f32 v198, v198, v194\n"
      "v_mul_f32 v199, v199, v194\n"
      "v_accvgpr_write_b32 a200, v196\n"
      "v_accvgpr_write_b32 a201, v197\n"
      "v_accvgpr_write_b32 a202, v198\n"
      "v_accvgpr_write_b32 a203, v199\n"
      "v_accvgpr_read_b32 v196, a204\n"
      "v_accvgpr_read_b32 v197, a205\n"
      "v_accvgpr_read_b32 v198, a206\n"
      "v_accvgpr_read_b32 v199, a207\n"
      "v_mul_f32 v196, v196, v195\n"
      "v_mul_f32 v197, v197, v195\n"
      "v_mul_f32 v198, v198, v195\n"
      "v_mul_f32 v199, v199, v195\n"
      "v_accvgpr_write_b32 a204, v196\n"
      "v_accvgpr_write_b32 a205, v197\n"
      "v_accvgpr_write_b32 a206, v198\n"
      "v_accvgpr_write_b32 a207, v199\n"
      "v_mul_f32 v180, v180, v192\n"
      "v_mul_f32 v181, v181, v193\n"
      "v_mul_f32 v182, v182, v194\n"
      "v_mul_f32 v183, v183, v195\n"
      "v_exp_f32 v188, v64\n"
      "v_exp_f32 v189, v65\n"
      "v_exp_f32 v190, v66\n"
      "v_exp_f32 v191, v67\n"
      "v_cvt_pk_bf16_f32 v128, v188, v189\n"
      "v_cvt_pk_bf16_f32 v129, v190, v191\n"
      "v_exp_f32 v192, v68\n"
      "v_exp_f32 v193, v69\n"
      "v_exp_f32 v194, v70\n"
      "v_exp_f32 v195, v71\n"
      "v_cvt_pk_bf16_f32 v130, v192, v193\n"
      "v_cvt_pk_bf16_f32 v131, v194, v195\n"
      "v_exp_f32 v188, v72\n"
      "v_exp_f32 v189, v73\n"
      "v_exp_f32 v190, v74\n"
      "v_exp_f32 v191, v75\n"
      "v_cvt_pk_bf16_f32 v132, v188, v189\n"
      "v_cvt_pk_bf16_f32 v133, v190, v191\n"
      "v_exp_f32 v192, v76\n"
      "v_exp_f32 v193, v77\n"
      "v_exp_f32 v194, v78\n"
      "v_exp_f32 v195, v79\n"
      "v_cvt_pk_bf16_f32 v134, v192, v193\n"
      "v_cvt_pk_bf16_f32 v135, v194, v195\n"
      "v_exp_f32 v188, v80\n"
      "v_exp_f32 v189, v81\n"
      "v_exp_f32 v190, v82\n"
      "v_exp_f32 v191, v83\n"
      "v_cvt_pk_bf16_f32 v136, v188, v189\n"
      "v_cvt_pk_bf16_f32 v137, v190, v191\n"
      "v_exp_f32 v192, v84\n"
      "v_exp_f32 v193, v85\n"
      "v_exp_f32 v194, v86\n"
      "v_exp_f32 v195, v87\n"
      "v_cvt_pk_bf16_f32 v138, v192, v193\n"
      "v_cvt_pk_bf16_f32 v139, v194, v195\n"
      "v_exp_f32 v188, v88\n"
      "v_exp_f32 v189, v89\n"
      "v_exp_f32 v190, v90\n"
      "v_exp_f32 v191, v91\n"
      "v_cvt_pk_bf16_f32 v140, v188, v189\n"
      "v_cvt_pk_bf16_f32 v141, v190, v191\n"
      "v_exp_f32 v192, v92\n"
      "v_exp_f32 v193, v93\n"
      "v_exp_f32 v194, v94\n"
      "v_exp_f32 v195, v95\n"
      "v_cvt_pk_bf16_f32 v142, v192, v193\n"
      "v_cvt_pk_bf16_f32 v143, v194, v195\n"
      "s_nop 1\n"
      "v_mfma_f32_16x16x32_bf16 v[184:187], a[128:131], v[128:131], 0\n"
      "v_mfma_f32_16x16x32_bf16 v[184:187], a[132:135], v[132:135], v[184:187]\n"
      "v_mfma_f32_16x16x32_bf16 v[184:187], a[136:139], v[136:139], v[184:187]\n"
      "v_mfma_f32_16x16x32_bf16 v[184:187], a[140:143], v[140:143], v[184:187]\n"
      "s_nop 7\n"
      "s_setpc_b64 s[96:97]\n"
      "FB1_%=:\n"
      "v_mov_b32 v196, 139248\n"
      "s_nop 7\n"
      "ds_write_b32 v196, v196\n"
      "v_max3_f32 v188, v96, v97, 0\n"
      "v_max3_f32 v189, v104, v105, 0\n"
      "v_max3_f32 v190, v112, v113, 0\n"
      "v_max3_f32 v191, v120, v121, 0\n"
      "v_max3_f32 v188, v188, v98, v99\n"
      "v_max3_f32 v189, v189, v106, v107\n"
      "v_max3_f32 v190, v190, v114, v115\n"
      "v_max3_f32 v191, v191, v122, v123\n"
      "v_max3_f32 v188, v188, v100, v101\n"
      "v_max3_f32 v189, v189, v108, v109\n"
      "v_max3_f32 v190, v190, v116, v117\n"
      "v_max3_f32 v191, v191, v124, v125\n"
      "v_max3_f32 v188, v188, v102, v103\n"
      "v_max3_f32 v189, v189, v110, v111\n"
      "v_max3_f32 v190, v190, v118, v119\n"
      "v_max3_f32 v191, v191, v126, v127\n"
      "v_mov_b32 v192, v188\n"
      "v_mov_b32 v193, v189\n"
      "v_mov_b32 v194, v190\n"
      "v_mov_b32 v195, v191\n"
      "v_permlane16_swap_b32 v188, v192\n"
      "v_permlane16_swap_b32 v189, v193\n"
      "v_permlane16_swap_b32 v190, v194\n"
      "v_permlane16_swap_b32 v191, v195\n"
      "v_max_f32 v188, v188, v192\n"
      "v_max_f32 v189, v189, v193\n"
      "v_max_f32 v190, v190, v194\n"
      "v_max_f32 v191, v191, v195\n"
      "v_mov_b32 v192, v188\n"
      "v_mov_b32 v193, v189\n"
      "v_mov_b32 v194, v190\n"
      "v_mov_b32 v195, v191\n"
      "v_permlane32_swap_b32 v188, v192\n"
      "v_permlane32_swap_b32 v189, v193\n"
      "v_permlane32_swap_b32 v190, v194\n"
      "v_permlane32_swap_b32 v191, v195\n"
      "v_max_f32 v188, v188, v192\n"
      "v_max_f32 v189, v189, v193\n"
      "v_max_f32 v190, v190, v194\n"
      "v_max_f32 v191, v191, v195\n"
      "RS1_%=:\n"
      "v_exp_f32 v192, -v188\n"
      "v_exp_f32 v193, -v189\n"
      "v_exp_f32 v194, -v190\n"
      "v_exp_f32 v195, -v191\n"
      "v_sub_f32 v176, v176, v188\n"
      "v_sub_f32 v177, v177, v189\n"
      "v_sub_f32 v178, v178, v190\n"
      "v_sub_f32 v179, v179, v191\n"
      "v_mov_b32 v160, v176\n"
      "v_mov_b32 v161, v176\n"
      "v_mov_b32 v162, v176\n"
      "v_mov_b32 v163, v176\n"
      "v_mov_b32 v164, v177\n"
      "v_mov_b32 v165, v177\n"
      "v_mov_b32 v166, v177\n"
      "v_mov_b32 v167, v177\n"
      "v_mov_b32 v168, v178\n"
      "v_mov_b32 v169, v178\n"
      "v_mov_b32 v170, v178\n"
      "v_mov_b32 v171, v178\n"
      "v_mov_b32 v172, v179\n"
      "v_mov_b32 v173, v179\n"
      "v_mov_b32 v174, v179\n"
      "v_mov_b32 v175, v179\n"
      "v_sub_f32 v64, v64, v188\n"
      "v_sub_f32 v65, v65, v188\n"
      "v_sub_f32 v66, v66, v188\n"
      "v_sub_f32 v67, v67, v188\n"
      "v_sub_f32 v68, v68, v188\n"
      "v_sub_f32 v69, v69, v188\n"
      "v_sub_f32 v70, v70, v188\n"
      "v_sub_f32 v71, v71, v188\n"
      "v_sub_f32 v72, v72, v189\n"
      "v_sub_f32 v73, v73, v189\n"
      "v_sub_f32 v74, v74, v189\n"
      "v_sub_f32 v75, v75, v189\n"
      "v_sub_f32 v76, v76, v189\n"
      "v_sub_f32 v77, v77, v189\n"
      "v_sub_f32 v78, v78, v189\n"
      "v_sub_f32 v79, v79, v189\n"
      "v_sub_f32 v80, v80, v190\n"
      "v_sub_f32 v81, v81, v190\n"
      "v_sub_f32 v82, v82, v190\n"
      "v_sub_f32 v83, v83, v190\n"
      "v_sub_f32 v84, v84, v190\n"
      "v_sub_f32 v85, v85, v190\n"
      "v_sub_f32 v86, v86, v190\n"
      "v_sub_f32 v87, v87, v190\n"
      "v_sub_f32 v88, v88, v191\n"
      "v_sub_f32 v89, v89, v191\n"
      "v_sub_f32 v90, v90, v191\n"
      "v_sub_f32 v91, v91, v191\n"
      "v_sub_f32 v92, v92, v191\n"
      "v_sub_f32 v93, v93, v191\n"
      "v_sub_f32 v94, v94, v191\n"
      "v_sub_f32 v95, v95, v191\n"
      "v_sub_f32 v96, v96, v188\n"
      "v_sub_f32 v97, v97, v188\n"
      "v_sub_f32 v98, v98, v188\n"
      "v_sub_f32 v99, v99, v188\n"
      "v_sub_f32 v100, v100, v188\n"
      "v_sub_f32 v101, v101, v188\n"
      "v_sub_f32 v102, v102, v188\n"
      "v_sub_f32 v103, v103, v188\n"
      "v_sub_f32 v104, v104, v189\n"
      "v_sub_f32 v105, v105, v189\n"
      "v_sub_f32 v106, v106, v189\n"
      "v_sub_f32 v107, v107, v189\n"
      "v_sub_f32 v108, v108, v189\n"
      "v_sub_f32 v109, v109, v189\n"
      "v_sub_f32 v110, v110, v189\n"
      "v_sub_f32 v111, v111, v189\n"
      "v_sub_f32 v112, v112, v190\n"
      "v_sub_f32 v113, v113, v190\n"
      "v_sub_f32 v114, v114, v190\n"
      "v_sub_f32 v115, v115, v190\n"
      "v_sub_f32 v116, v116, v190\n"
      "v_sub_f32 v117, v117, v190\n"
      "v_sub_f32 v118, v118, v190\n"
      "v_sub_f32 v119, v119, v190\n"
      "v_sub_f32 v120, v120, v191\n"
      "v_sub_f32 v121, v121, v191\n"
      "v_sub_f32 v122, v122, v191\n"
      "v_sub_f32 v123, v123, v191\n"
      "v_sub_f32 v124, v124, v191\n"
      "v_sub_f32 v125, v125, v191\n"
      "v_sub_f32 v126, v126, v191\n"
      "v_sub_f32 v127, v127, v191\n"
      "v_mul_f32 v0, v0, v192\n"
      "v_mul_f32 v1, v1, v192\n"
      "v_mul_f32 v2, v2, v192\n"
      "v_mul_f32 v3, v3, v192\n"
      "v_mul_f32 v4, v4, v193\n"
      "v_mul_f32 v5, v5, v193\n"
      "v_mul_f32 v6, v6, v193\n"
      "v_mul_f32 v7, v7, v193\n"
      "v_mul_f32 v8, v8, v192\n"
      "v_mul_f32 v9, v9, v192\n"
      "v_mul_f32 v10, v10, v192\n"
      "v_mul_f32 v11, v11, v192\n"
      "v_mul_f32 v12, v12, v193\n"
      "v_mul_f32 v13, v13, v193\n"
      "v_mul_f32 v14, v14, v193\n"
      "v_mul_f32 v15, v15, v193\n"
      "v_mul_f32 v16, v16, v192\n"
      "v_mul_f32 v17, v17, v192\n"
      "v_mul_f32 v18, v18, v192\n"
      "v_mul_f32 v19, v19, v192\n"
      "v_mul_f32 v20, v20, v193\n"
      "v_mul_f32 v21, v21, v193\n"
      "v_mul_f32 v22, v22, v193\n"
      "v_mul_f32 v23, v23, v193\n"
      "v_mul_f32 v24, v24, v192\n"
      "v_mul_f32 v25, v25, v192\n"
      "v_mul_f32 v26, v26, v192\n"
      "v_mul_f32 v27, v27, v192\n"
      "v_mul_f32 v28, v28, v193\n"
      "v_mul_f32 v29, v29, v193\n"
      "v_mul_f32 v30, v30, v193\n"
      "v_mul_f32 v31, v31, v193\n"
      "v_mul_f32 v32, v32, v192\n"
      "v_mul_f32 v33, v33, v192\n"
      "v_mul_f32 v34, v34, v192\n"
      "v_mul_f32 v35, v35, v192\n"
      "v_mul_f32 v36, v36, v193\n"
      "v_mul_f32 v37, v37, v193\n"
      "v_mul_f32 v38, v38, v193\n"
      "v_mul_f32 v39, v39, v193\n"
      "v_mul_f32 v40, v40, v192\n"
      "v_mul_f32 v41, v41, v192\n"
      "v_mul_f32 v42, v42, v192\n"
      "v_mul_f32 v43, v43, v192\n"
      "v_mul_f32 v44, v44, v193\n"
      "v_mul_f32 v45, v45, v193\n"
      "v_mul_f32 v46, v46, v193\n"
      "v_mul_f32 v47, v47, v193\n"
      "v_mul_f32 v48, v48, v192\n"
      "v_mul_f32 v49, v49, v192\n"
      "v_mul_f32 v50, v50, v192\n"
      "v_mul_f32 v51, v51, v192\n"
      "v_mul_f32 v52, v52, v193\n"
      "v_mul_f32 v53, v53, v193\n"
      "v_mul_f32 v54, v54, v193\n"
      "v_mul_f32 v55, v55, v193\n"
      "v_mul_f32 v56, v56, v192\n"
      "v_mul_f32 v57, v57, v192\n"
      "v_mul_f32 v58, v58, v192\n"
      "v_mul_f32 v59, v59, v192\n"
      "v_mul_f32 v60, v60, v193\n"
      "v_mul_f32 v61, v61, v193\n"
      "v_mul_f32 v62, v62, v193\n"
      "v_mul_f32 v63, v63, v193\n"
      "v_accvgpr_read_b32 v196, a144\n"
      "v_accvgpr_read_b32 v197, a145\n"
      "v_accvgpr_read_b32 v198, a146\n"
      "v_accvgpr_read_b32 v199, a147\n"
      "v_mul_f32 v196, v196, v194\n"
      "v_mul_f32 v197, v197, v194\n"
      "v_mul_f32 v198, v198, v194\n"
      "v_mul_f32 v199, v199, v194\n"
      "v_accvgpr_write_b32 a144, v196\n"
      "v_accvgpr_write_b32 a145, v197\n"
      "v_accvgpr_write_b32 a146, v198\n"
      "v_accvgpr_write_b32 a147, v199\n"
      "v_accvgpr_read_b32 v196, a148\n"
      "v_accvgpr_read_b32 v197, a149\n"
      "v_accvgpr_read_b32 v198, a150\n"
      "v_accvgpr_read_b32 v199, a151\n"
      "v_mul_f32 v196, v196, v195\n"
      "v_mul_f32 v197, v197, v195\n"
      "v_mul_f32 v198, v198, v195\n"
      "v_mul_f32 v199, v199, v195\n"
      "v_accvgpr_write_b32 a148, v196\n"
      "v_accvgpr_write_b32 a149, v197\n"
      "v_accvgpr_write_b32 a150, v198\n"
      "v_accvgpr_write_b32 a151, v199\n"
      "v_accvgpr_read_b32 v196, a152\n"
      "v_accvgpr_read_b32 v197, a153\n"
      "v_accvgpr_read_b32 v198, a154\n"
      "v_accvgpr_read_b32 v199, a155\n"
      "v_mul_f32 v196, v196, v194\n"
      "v_mul_f32 v197, v197, v194\n"
      "v_mul_f32 v198, v198, v194\n"
      "v_mul_f32 v199, v199, v194\n"
      "v_accvgpr_write_b32 a152, v196\n"
      "v_accvgpr_write_b32 a153, v197\n"
      "v_accvgpr_write_b32 a154, v198\n"
      "v_accvgpr_write_b32 a155, v199\n"
      "v_accvgpr_read_b32 v196, a156\n"
      "v_accvgpr_read_b32 v197, a157\n"
      "v_accvgpr_read_b32 v198, a158\n"
      "v_accvgpr_read_b32 v199, a159\n"
      "v_mul_f32 v196, v196, v195\n"
      "v_mul_f32 v197, v197, v195\n"
      "v_mul_f32 v198, v198, v195\n"
      "v_mul_f32 v199, v199, v195\n"
      "v_accvgpr_write_b32 a156, v196\n"
      "v_accvgpr_write_b32 a157, v197\n"
      "v_accvgpr_write_b32 a158, v198\n"
      "v_accvgpr_write_b32 a159, v199\n"
      "v_accvgpr_read_b32 v196, a160\n"
      "v_accvgpr_read_b32 v197, a161\n"
      "v_accvgpr_read_b32 v198, a162\n"
      "v_accvgpr_read_b32 v199, a163\n"
      "v_mul_f32 v196, v196, v194\n"
      "v_mul_f32 v197, v197, v194\n"
      "v_mul_f32 v198, v198, v194\n"
      "v_mul_f32 v199, v199, v194\n"
      "v_accvgpr_write_b32 a160, v196\n"
      "v_accvgpr_write_b32 a161, v197\n"
      "v_accvgpr_write_b32 a162, v198\n"
      "v_accvgpr_write_b32 a163, v199\n"
      "v_accvgpr_read_b32 v196, a164\n"
      "v_accvgpr_read_b32 v197, a165\n"
      "v_accvgpr_read_b32 v198, a166\n"
      "v_accvgpr_read_b32 v199, a167\n"
      "v_mul_f32 v196, v196, v195\n"
      "v_mul_f32 v197, v197, v195\n"
      "v_mul_f32 v198, v198, v195\n"
      "v_mul_f32 v199, v199, v195\n"
      "v_accvgpr_write_b32 a164, v196\n"
      "v_accvgpr_write_b32 a165, v197\n"
      "v_accvgpr_write_b32 a166, v198\n"
      "v_accvgpr_write_b32 a167, v199\n"
      "v_accvgpr_read_b32 v196, a168\n"
      "v_accvgpr_read_b32 v197, a169\n"
      "v_accvgpr_read_b32 v198, a170\n"
      "v_accvgpr_read_b32 v199, a171\n"
      "v_mul_f32 v196, v196, v194\n"
      "v_mul_f32 v197, v197, v194\n"
      "v_mul_f32 v198, v198, v194\n"
      "v_mul_f32 v199, v199, v194\n"
      "v_accvgpr_write_b32 a168, v196\n"
      "v_accvgpr_write_b32 a169, v197\n"
      "v_accvgpr_write_b32 a170, v198\n"
      "v_accvgpr_write_b32 a171, v199\n"
      "v_accvgpr_read_b32 v196, a172\n"
      "v_accvgpr_read_b32 v197, a173\n"
      "v_accvgpr_read_b32 v198, a174\n"
      "v_accvgpr_read_b32 v199, a175\n"
      "v_mul_f32 v196, v196, v195\n"
      "v_mul_f32 v197, v197, v195\n"
      "v_mul_f32 v198, v198, v195\n"
      "v_mul_f32 v199, v199, v195\n"
      "v_accvgpr_write_b32 a172, v196\n"
      "v_accvgpr_write_b32 a173, v197\n"
      "v_accvgpr_write_b32 a174, v198\n"
      "v_accvgpr_write_b32 a175, v199\n"
      "v_accvgpr_read_b32 v196, a176\n"
      "v_accvgpr_read_b32 v197, a177\n"
      "v_accvgpr_read_b32 v198, a178\n"
      "v_accvgpr_read_b32 v199, a179\n"
      "v_mul_f32 v196, v196, v194\n"
      "v_mul_f32 v197, v197, v194\n"
      "v_mul_f32 v198, v198, v194\n"
      "v_mul_f32 v199, v199, v194\n"
      "v_accvgpr_write_b32 a176, v196\n"
      "v_accvgpr_write_b32 a177, v197\n"
      "v_accvgpr_write_b32 a178, v198\n"
      "v_accvgpr_write_b32 a179, v199\n"
      "v_accvgpr_read_b32 v196, a180\n"
      "v_accvgpr_read_b32 v197, a181\n"
      "v_accvgpr_read_b32 v198, a182\n"
      "v_accvgpr_read_b32 v199, a183\n"
      "v_mul_f32 v196, v196, v195\n"
      "v_mul_f32 v197, v197, v195\n"
      "v_mul_f32 v198, v198, v195\n"
      "v_mul_f32 v199, v199, v195\n"
      "v_accvgpr_write_b32 a180, v196\n"
      "v_accvgpr_write_b32 a181, v197\n"
      "v_accvgpr_write_b32 a182, v198\n"
      "v_accvgpr_write_b32 a183, v199\n"
      "v_accvgpr_read_b32 v196, a184\n"
      "v_accvgpr_read_b32 v197, a185\n"
      "v_accvgpr_read_b32 v198, a186\n"
      "v_accvgpr_read_b32 v199, a187\n"
      "v_mul_f32 v196, v196, v194\n"
      "v_mul_f32 v197, v197, v194\n"
      "v_mul_f32 v198, v198, v194\n"
      "v_mul_f32 v199, v199, v194\n"
      "v_accvgpr_write_b32 a184, v196\n"
      "v_accvgpr_write_b32 a185, v197\n"
      "v_accvgpr_write_b32 a186, v198\n"
      "v_accvgpr_write_b32 a187, v199\n"
      "v_accvgpr_read_b32 v196, a188\n"
      "v_accvgpr_read_b32 v197, a189\n"
      "v_accvgpr_read_b32 v198, a190\n"
      "v_accvgpr_read_b32 v199, a191\n"
      "v_mul_f32 v196, v196, v195\n"
      "v_mul_f32 v197, v197, v195\n"
      "v_mul_f32 v198, v198, v195\n"
      "v_mul_f32 v199, v199, v195\n"
      "v_accvgpr_write_b32 a188, v196\n"
      "v_accvgpr_write_b32 a189, v197\n"
      "v_accvgpr_write_b32 a190, v198\n"
      "v_accvgpr_write_b32 a191, v199\n"
      "v_accvgpr_read_b32 v196, a192\n"
      "v_accvgpr_read_b32 v197, a193\n"
      "v_accvgpr_read_b32 v198, a194\n"
      "v_accvgpr_read_b32 v199, a195\n"
      "v_mul_f32 v196, v196, v194\n"
      "v_mul_f32 v197, v197, v194\n"
      "v_mul_f32 v198, v198, v194\n"
      "v_mul_f32 v199, v199, v194\n"
      "v_accvgpr_write_b32 a192, v196\n"
      "v_accvgpr_write_b32 a193, v197\n"
      "v_accvgpr_write_b32 a194, v198\n"
      "v_accvgpr_write_b32 a195, v199\n"
      "v_accvgpr_read_b32 v196, a196\n"
      "v_accvgpr_read_b32 v197, a197\n"
      "v_accvgpr_read_b32 v198, a198\n"
      "v_accvgpr_read_b32 v199, a199\n"
      "v_mul_f32 v196, v196, v195\n"
      "v_mul_f32 v197, v197, v195\n"
      "v_mul_f32 v198, v198, v195\n"
      "v_mul_f32 v199, v199, v195\n"
      "v_accvgpr_write_b32 a196, v196\n"
      "v_accvgpr_write_b32 a197, v197\n"
      "v_accvgpr_write_b32 a198, v198\n"
      "v_accvgpr_write_b32 a199, v199\n"
      "v_accvgpr_read_b32 v196, a200\n"
      "v_accvgpr_read_b32 v197, a201\n"
      "v_accvgpr_read_b32 v198, a202\n"
      "v_accvgpr_read_b32 v199, a203\n"
      "v_mul_f32 v196, v196, v194\n"
      "v_mul_f32 v197, v197, v194\n"
      "v_mul_f32 v198, v198, v194\n"
      "v_mul_f32 v199, v199, v194\n"
      "v_accvgpr_write_b32 a200, v196\n"
      "v_accvgpr_write_b32 a201, v197\n"
      "v_accvgpr_write_b32 a202, v198\n"
      "v_accvgpr_write_b32 a203, v199\n"
      "v_accvgpr_read_b32 v196, a204\n"
      "v_accvgpr_read_b32 v197, a205\n"
      "v_accvgpr_read_b32 v198, a206\n"
      "v_accvgpr_read_b32 v199, a207\n"
      "v_mul_f32 v196, v196, v195\n"
      "v_mul_f32 v197, v197, v195\n"
      "v_mul_f32 v198, v198, v195\n"
      "v_mul_f32 v199, v199, v195\n"
      "v_accvgpr_write_b32 a204, v196\n"
      "v_accvgpr_write_b32 a205, v197\n"
      "v_accvgpr_write_b32 a206, v198\n"
      "v_accvgpr_write_b32 a207, v199\n"
      "v_mul_f32 v180, v180, v192\n"
      "v_mul_f32 v181, v181, v193\n"
      "v_mul_f32 v182, v182, v194\n"
      "v_mul_f32 v183, v183, v195\n"
      "v_lshlrev_b32 v196, 16, v128\n"
      "v_and_b32 v197, 0xffff0000, v128\n"
      "v_mul_f32 v196, v196, v192\n"
      "v_mul_f32 v197, v197, v192\n"
      "v_cvt_pk_bf16_f32 v128, v196, v197\n"
      "v_lshlrev_b32 v196, 16, v129\n"
      "v_and_b32 v197, 0xffff0000, v129\n"
      "v_mul_f32 v196, v196, v192\n"
      "v_mul_f32 v197, v197, v192\n"
      "v_cvt_pk_bf16_f32 v129, v196, v197\n"
      "v_lshlrev_b32 v196, 16, v130\n"
      "v_and_b32 v197, 0xffff0000, v130\n"
      "v_mul_f32 v196, v196, v192\n"
      "v_mul_f32 v197, v197, v192\n"
      "v_cvt_pk_bf16_f32 v130, v196, v197\n"
      "v_lshlrev_b32 v196, 16, v131\n"
      "v_and_b32 v197, 0xffff0000, v131\n"
      "v_mul_f32 v196, v196, v192\n"
      "v_mul_f32 v197, v197, v192\n"
      "v_cvt_pk_bf16_f32 v131, v196, v197\n"
      "v_lshlrev_b32 v196, 16, v132\n"
      "v_and_b32 v197, 0xffff0000, v132\n"
      "v_mul_f32 v196, v196, v193\n"
      "v_mul_f32 v197, v197, v193\n"
      "v_cvt_pk_bf16_f32 v132, v196, v197\n"
      "v_lshlrev_b32 v196, 16, v133\n"
      "v_and_b32 v197, 0xffff0000, v133\n"
      "v_mul_f32 v196, v196, v193\n"
      "v_mul_f32 v197, v197, v193\n"
      "v_cvt_pk_bf16_f32 v133, v196, v197\n"
      "v_lshlrev_b32 v196, 16, v134\n"
      "v_and_b32 v197, 0xffff0000, v134\n"
      "v_mul_f32 v196, v196, v193\n"
      "v_mul_f32 v197, v197, v193\n"
      "v_cvt_pk_bf16_f32 v134, v196, v197\n"
      "v_lshlrev_b32 v196, 16, v135\n"
      "v_and_b32 v197, 0xffff0000, v135\n"
      "v_mul_f32 v196, v196, v193\n"
      "v_mul_f32 v197, v197, v193\n"
      "v_cvt_pk_bf16_f32 v135, v196, v197\n"
      "v_lshlrev_b32 v196, 16, v136\n"
      "v_and_b32 v197, 0xffff0000, v136\n"
      "v_mul_f32 v196, v196, v194\n"
      "v_mul_f32 v197, v197, v194\n"
      "v_cvt_pk_bf16_f32 v136, v196, v197\n"
      "v_lshlrev_b32 v196, 16, v137\n"
      "v_and_b32 v197, 0xffff0000, v137\n"
      "v_mul_f32 v196, v196, v194\n"
      "v_mul_f32 v197, v197, v194\n"
      "v_cvt_pk_bf16_f32 v137, v196, v197\n"
      "v_lshlrev_b32 v196, 16, v138\n"
      "v_and_b32 v197, 0xffff0000, v138\n"
      "v_mul_f32 v196, v196, v194\n"
      "v_mul_f32 v197, v197, v194\n"
      "v_cvt_pk_bf16_f32 v138, v196, v197\n"
      "v_lshlrev_b32 v196, 16, v139\n"
      "v_and_b32 v197, 0xffff0000, v139\n"
      "v_mul_f32 v196, v196, v194\n"
      "v_mul_f32 v197, v197, v194\n"
      "v_cvt_pk_bf16_f32 v139, v196, v197\n"
      "v_lshlrev_b32 v196, 16, v140\n"
      "v_and_b32 v197, 0xffff0000, v140\n"
      "v_mul_f32 v196, v196, v195\n"
      "v_mul_f32 v197, v197, v195\n"
      "v_cvt_pk_bf16_f32 v140, v196, v197\n"
      "v_lshlrev_b32 v196, 16, v141\n"
      "v_and_b32 v197, 0xffff0000, v141\n"
      "v_mul_f32 v196, v196, v195\n"
      "v_mul_f32 v197, v197, v195\n"
      "v_cvt_pk_bf16_f32 v141, v196, v197\n"
      "v_lshlrev_b32 v196, 16, v142\n"
      "v_and_b32 v197, 0xffff0000, v142\n"
      "v_mul_f32 v196, v196, v195\n"
      "v_mul_f32 v197, v197, v195\n"
      "v_cvt_pk_bf16_f32 v142, v196, v197\n"
      "v_lshlrev_b32 v196, 16, v143\n"
      "v_and_b32 v197, 0xffff0000, v143\n"
      "v_mul_f32 v196, v196, v195\n"
      "v_mul_f32 v197, v197, v195\n"
      "v_cvt_pk_bf16_f32 v143, v196, v197\n"
      "v_exp_f32 v188, v96\n"
      "v_exp_f32 v189, v97\n"
      "v_exp_f32 v190, v98\n"
      "v_exp_f32 v191, v99\n"
      "v_cvt_pk_bf16_f32 v144, v188, v189\n"
      "v_cvt_pk_bf16_f32 v145, v190, v191\n"
      "v_exp_f32 v192, v100\n"
      "v_exp_f32 v193, v101\n"
      "v_exp_f32 v194, v102\n"
      "v_exp_f32 v195, v103\n"
      "v_cvt_pk_bf16_f32 v146, v192, v193\n"
      "v_cvt_pk_bf16_f32 v147, v194, v195\n"
      "v_exp_f32 v188, v104\n"
      "v_exp_f32 v189, v105\n"
      "v_exp_f32 v190, v106\n"
      "v_exp_f32 v191, v107\n"
      "v_cvt_pk_bf16_f32 v148, v188, v189\n"
      "v_cvt_pk_bf16_f32 v149, v190, v191\n"
      "v_exp_f32 v192, v108\n"
      "v_exp_f32 v193, v109\n"
      "v_exp_f32 v194, v110\n"
      "v_exp_f32 v195, v111\n"
      "v_cvt_pk_bf16_f32 v150, v192, v193\n"
      "v_cvt_pk_bf16_f32 v151, v194, v195\n"
      "v_exp_f32 v188, v112\n"
      "v_exp_f32 v189, v113\n"
      "v_exp_f32 v190, v114\n"
      "v_exp_f32 v191, v115\n"
      "v_cvt_pk_bf16_f32 v152, v188, v189\n"
      "v_cvt_pk_bf16_f32 v153, v190, v191\n"
      "v_exp_f32 v192, v116\n"
      "v_exp_f32 v193, v117\n"
      "v_exp_f32 v194, v118\n"
      "v_exp_f32 v195, v119\n"
      "v_cvt_pk_bf16_f32 v154, v192, v193\n"
      "v_cvt_pk_bf16_f32 v155, v194, v195\n"
      "v_exp_f32 v188, v120\n"
      "v_exp_f32 v189, v121\n"
      "v_exp_f32 v190, v122\n"
      "v_exp_f32 v191, v123\n"
      "v_cvt_pk_bf16_f32 v156, v188, v189\n"
      "v_cvt_pk_bf16_f32 v157, v190, v191\n"
      "v_exp_f32 v192, v124\n"
      "v_exp_f32 v193, v125\n"
      "v_exp_f32 v194, v126\n"
      "v_exp_f32 v195, v127\n"
      "v_cvt_pk_bf16_f32 v158, v192, v193\n"
      "v_cvt_pk_bf16_f32 v159, v194, v195\n"
      "s_nop 1\n"
      "v_mfma_f32_16x16x32_bf16 v[184:187], a[128:131], v[144:147], 0\n"
      "v_mfma_f32_16x16x32_bf16 v[184:187], a[132:135], v[148:151], v[184:187]\n"
      "v_mfma_f32_16x16x32_bf16 v[184:187], a[136:139], v[152:155], v[184:187]\n"
      "v_mfma_f32_16x16x32_bf16 v[184:187], a[140:143], v[156:159], v[184:187]\n"
      "s_nop 7\n"
      "s_setpc_b64 s[96:97]\n"
      "SLOW_%=:\n"
      "ds_write_b128 v234, v[176:179] offset:5120\n"
      "s_waitcnt lgkmcnt(0)\n"
      "s_barrier\n"
      "ds_read_b128 v[192:195], v235 offset:5120\n"
      "s_waitcnt lgkmcnt(0)\n"
      "v_sub_f32 v188, v176, v194\n"
      "v_sub_f32 v189, v177, v195\n"
      "v_sub_f32 v190, v178, v192\n"
      "v_sub_f32 v191, v179, v193\n"
      "v_max_f32 v188, 0, v188\n"
      "v_max_f32 v189, 0, v189\n"
      "v_max_f32 v190, 0, v190\n"
      "v_max_f32 v191, 0, v191\n"
      "s_call_b64 s[96:97], RS1_%=\n"
      "ds_write_b128 v232, a[176:179] offset:0\n"
      "ds_write_b128 v232, a[180:183] offset:1024\n"
      "ds_write_b128 v232, a[184:187] offset:2048\n"
      "ds_write_b128 v232, a[188:191] offset:3072\n"
      "ds_write_b128 v232, a[192:195] offset:4096\n"
      "ds_write_b128 v232, a[196:199] offset:5120\n"
      "ds_write_b128 v232, a[200:203] offset:6144\n"
      "ds_write_b128 v232, a[204:207] offset:7168\n"
      "ds_write_b128 v234, v[152:155] offset:0\n"
      "ds_write_b128 v234, v[156:159] offset:1024\n"
      "v_cndmask_b32 v229, v180, v181, s[74:75]\n"
      "v_cndmask_b32 v200, v182, v183, s[74:75]\n"
      "v_cndmask_b32 v228, v176, v177, s[74:75]\n"
      "v_cndmask_b32 v202, v186, v187, s[74:75]\n"
      "v_cndmask_b32 v201, v184, v185, s[74:75]\n"
      "v_add_f32 v200, v200, v202\n"
      "ds_write_b32 v234, v200 offset:4096\n"
      "v_add_f32 v229, v229, v201\n"
      "s_waitcnt lgkmcnt(0)\n"
      "s_barrier\n"
      "ds_read_b32 v208, v209\n"
      "ds_read_b128 v[160:163], v235 offset:0\n"
      "ds_read_b128 v[164:167], v235 offset:1024\n"
      "ds_read_b128 v[64:67], v233 offset:0\n"
      "ds_read_b128 v[68:71], v233 offset:1024\n"
      "ds_read_b128 v[72:75], v233 offset:2048\n"
      "ds_read_b128 v[76:79], v233 offset:3072\n"
      "ds_read_b128 v[80:83], v233 offset:4096\n"
      "ds_read_b128 v[84:87], v233 offset:5120\n"
      "ds_read_b128 v[88:91], v233 offset:6144\n"
      "ds_read_b128 v[92:95], v233 offset:7168\n"
      "ds_read_b32 v204, v235 offset:4096\n"
      "s_branch AGAIN_%=\n"
      ".subsection 0\n"
      : "+{v0}"(tid)
      : [blk] "s"(blockIdx.x), [kp] "s"(__builtin_amdgcn_kernarg_segment_ptr()), "s"(lds0)
      : "v1", "v2", "v3", "v4", "v5", "v6", "v7", "v8", "v9", "v10", "v11", "v12", "v13", "v14", "v15", "v16", "v17", "v18", "v19", "v20", "v21", "v22", "v23", "v24", "v25", "v26", "v27", "v28", "v29", "v30", "v31", "v32", "v33", "v34", "v35", "v36", "v37", "v38", "v39", "v40", "v41", "v42", "v43", "v44", "v45", "v46", "v47", "v48", "v49", "v50", "v51", "v52", "v53", "v54", "v55", "v56", "v57", "v58", "v59", "v60", "v61", "v62", "v63", "v64", "v65", "v66", "v67", "v68", "v69", "v70", "v71", "v72", "v73", "v74", "v75", "v76", "v77", "v78", "v79", "v80", "v81", "v82", "v83", "v84", "v85", "v86", "v87", "v88", "v89", "v90", "v91", "v92", "v93", "v94", "v95", "v96", "v97", "v98", "v99", "v100", "v101", "v102", "v103", "v104", "v105", "v106", "v107", "v108", "v109", "v110", "v111", "v112", "v113", "v114", "v115", "v116", "v117", "v118", "v119", "v120", "v121", "v122", "v123", "v124", "v125", "v126", "v127", "v128", "v129", "v130", "v131", "v132", "v133", "v134", "v135", "v136", "v137", "v138", "v139", "v140", "v141", "v142", "v143", "v144", "v145", "v146", "v147", "v148", "v149", "v150", "v151", "v152", "v153", "v154", "v155", "v156", "v157", "v158", "v159", "v160", "v161", "v162", "v163", "v164", "v165", "v166", "v167", "v168", "v169", "v170", "v171", "v172", "v173", "v174", "v175", "v176", "v177", "v178", "v179", "v180", "v181", "v182", "v183", "v184", "v185", "v186", "v187", "v188", "v189", "v190", "v191", "v192", "v193", "v194", "v195", "v196", "v197", "v198", "v199", "v200", "v201", "v202", "v203", "v204", "v205", "v206", "v207", "v208", "v209", "v210", "v211", "v212", "v213", "v214", "v215", "v216", "v217", "v218", "v219", "v220", "v221", "v222", "v223", "v224", "v225", "v226", "v227", "v228", "v229", "v230", "v231", "v232", "v233", "v234", "v235", "v236", "v237", "v238", "v239", "v240", "v241", "v242", "v243", "v244", "v245", "v246", "v247", "v248", "v249", "v250", "v251", "a0", "a1", "a2", "a3", "a4", "a5", "a6", "a7", "a8", "a9", "a10", "a11", "a12", "a13", "a14", "a15", "a16", "a17", "a18", "a19", "a20", "a21", "a22", "a23", "a24", "a25", "a26", "a27", "a28", "a29", "a30", "a31", "a32", "a33", "a34", "a35", "a36", "a37", "a38", "a39", "a40", "a41", "a42", "a43", "a44", "a45", "a46", "a47", "a48", "a49", "a50", "a51", "a52", "a53", "a54", "a55", "a56", "a57", "a58", "a59", "a60", "a61", "a62", "a63", "a64", "a65", "a66", "a67", "a68", "a69", "a70", "a71", "a72", "a73", "a74", "a75", "a76", "a77", "a78", "a79", "a80", "a81", "a82", "a83", "a84", "a85", "a86", "a87", "a88", "a89", "a90", "a91", "a92", "a93", "a94", "a95", "a96", "a97", "a98", "a99", "a100", "a101", "a102", "a103", "a104", "a105", "a106", "a107", "a108", "a109", "a110", "a111", "a112", "a113", "a114", "a115", "a116", "a117", "a118", "a119", "a120", "a121", "a122", "a123", "a124", "a125", "a126", "a127", "a128", "a129", "a130", "a131", "a132", "a133", "a134", "a135", "a136", "a137", "a138", "a139", "a140", "a141", "a142", "a143", "a144", "a145", "a146", "a147", "a148", "a149", "a150", "a151", "a152", "a153", "a154", "a155", "a156", "a157", "a158", "a159", "a160", "a161", "a162", "a163", "a164", "a165", "a166", "a167", "a168", "a169", "a170", "a171", "a172", "a173", "a174", "a175", "a176", "a177", "a178", "a179", "a180", "a181", "a182", "a183", "a184", "a185", "a186", "a187", "a188", "a189", "a190", "a191", "a192", "a193", "a194", "a195", "a196", "a197", "a198", "a199", "a200", "a201", "a202", "a203", "a204", "a205", "a206", "a207", "s4", "s5", "s6", "s7", "s8", "s9", "s10", "s11", "s12", "s13", "s14", "s15", "s16", "s17", "s18", "s19", "s20", "s21", "s22", "s23", "s24", "s25", "s26", "s27", "s28", "s29", "s30", "s31", "s32", "s33", "s34", "s35", "s38", "s39", "s40", "s43", "s44", "s45", "s46", "s47", "s50", "s52", "s53", "s54", "s56", "s57", "s58", "s59", "s60", "s61", "s62", "s63", "s64", "s65", "s66", "s67", "s68", "s69", "s70", "s71", "s72", "s74", "s75", "s76", "s77", "s78", "s79", "s80", "s81", "s82", "s83", "s84", "s85", "s86", "s88", "s89", "s90", "s91", "s92", "s93", "s94", "s95", "s96", "s97", "vcc", "scc", "memory");
}

}

extern "C" void kernel_launch(void* const* d_in, const int* in_sizes, int n_in, void* d_out, int out_size,
                              void* d_ws, size_t ws_size, hipStream_t stream) {
  if (n_in != 6 || ws_size < WS_END) return;
  const __hip_bfloat16* q = (const __hip_bfloat16*)d_in[0];
  const __hip_bfloat16* kc = (const __hip_bfloat16*)d_in[1];
  const __hip_bfloat16* vc = (const __hip_bfloat16*)d_in[2];
  const __hip_bfloat16* kt = (const __hip_bfloat16*)d_in[3];
  const __hip_bfloat16* vt = (const __hip_bfloat16*)d_in[4];
  const __hip_bfloat16* tm = (const __hip_bfloat16*)d_in[5];
  hipLaunchKernelGGL(tree_attn_main, dim3(B_ * HKV_ * 2), dim3(256), 0, stream, q, kc, vc, kt, vt, tm,
                     (__hip_bfloat16*)d_out, (char*)d_ws);
}
```

```cpp
#include <hip/hip_runtime.h>
#include <hip/hip_bf16.h>
#include <stdint.h>

#pragma clang diagnostic ignored "-Wint-to-pointer-cast"
#pragma clang diagnostic ignored "-Winline-asm"

namespace {

constexpr int B_ = 16, HKV_ = 8, D_ = 128;
constexpr int SLOT = 64 * D_ * 2;
constexpr int LDS_BYTES = 10 * SLOT;

constexpr size_t WS_EPOCH = 0;
constexpr size_t WS_REF = WS_EPOCH + 128 * 4 * 128;
constexpr size_t WS_X = WS_REF + 16384;
constexpr size_t XW_BYTES = 4608;
constexpr size_t WS_END = WS_X + (size_t)128 * 2 * 4 * XW_BYTES;

__global__ __launch_bounds__(256, 1)
void tree_attn_main(const __hip_bfloat16*, const __hip_bfloat16*, const __hip_bfloat16*, const __hip_bfloat16*,
                    const __hip_bfloat16*, const __hip_bfloat16*, __hip_bfloat16*, char*) {
  __shared__ __attribute__((aligned(1024))) char smem[LDS_BYTES];
  const unsigned lds0 = (unsigned)(uintptr_t)smem;
  unsigned tid = threadIdx.x;
  asm volatile(
      "s_load_dwordx16 s[4:19], %[kp], 0x0\n"
      "s_getpc_b64 s[60:61]\n"
      "v_lshlrev_b32 v241, 4, v0\n"
      "s_lshl_b32 s62, %[blk], 9\n"
      "s_and_b32 s62, s62, 0x3000\n"
      "s_add_u32 s60, s60, s62\n"
      "s_addc_u32 s61, s61, 0\n"
      "global_load_dwordx4 v[236:239], v241, s[60:61]\n"
      "v_and_b32 v1, 0xc0, v0\n"
      "v_lshlrev_b32 v248, 1, v0\n"
      "v_readfirstlane_b32 s50, v1\n"
      "v_and_b32 v248, 0x60, v248\n"
      "v_and_b32 v249, 0x70, v241\n"
      "v_xor_b32 v248, v248, v249\n"
      "v_lshl_or_b32 v248, v1, 6, v248\n"
      "v_and_b32 v1, 56, v0\n"
      "v_lshl_or_b32 v248, v1, 5, v248\n"
      "s_mov_b32 s64, 0x14100400\n"
      "s_mov_b32 s65, 0x54504440\n"
      "s_lshl_b32 s66, %[blk], 3\n"
      "s_lshr_b64 s[64:65], s[64:65], s66\n"
      "s_mov_b32 s66, 0x3020100\n"
      "s_mov_b32 s67, 0x83828180\n"
      "s_and_b32 s65, %[blk], 56\n"
      "s_lshr_b64 s[66:67], s[66:67], s65\n"
      "s_or_b32 s64, s64, s66\n"
      "s_lshr_b32 s65, %[blk], 3\n"
      "s_and_b32 s65, s65, 24\n"
      "s_lshr_b32 s65, 0x28082000, s65\n"
      "s_or_b32 s64, s64, s65\n"
      "s_and_b32 s63, s64, 0x80\n"
      "s_and_b32 s44, s64, 127\n"
      "v_or_b32 v248, s63, v248\n"
      "v_xor_b32 v249, 0x80, v248\n"
      "s_lshl_b32 s64, s44, 20\n"
      "s_lshl_b32 s62, s44, 15\n"
      "s_lshl_b32 s50, s50, 6\n"
      "s_add_u32 m0, s50, 131072\n"
      "s_mov_b32 s58, 0x120000\n"
      "s_mov_b32 s59, 0x20000\n"
      "s_movk_i32 s22, 0x4000\n"
      "s_waitcnt lgkmcnt(0)\n"
      "s_add_u32 s56, s4, s62\n"
      "s_addc_u32 s57, s5, 0\n"
      "buffer_load_dwordx4 v248, s[56:59], 0 offen nt lds\n"
      "buffer_load_dwordx4 v248, s[56:59], 0 offen offset:2048 nt lds\n"
      "s_bitset1_b32 m0, 10\n"
      "s_memrealtime s[82:83]\n"
      "s_add_u32 s20, s6, s64\n"
      "s_addc_u32 s21, s7, 0\n"
      "s_add_u32 s32, s8, s64\n"
      "s_addc_u32 s33, s9, 0\n"
      "s_load_dword s92, s[20:21], 0x0\n"
      "s_load_dword s92, s[10:11], 0x0\n"
      "s_load_dword s92, s[32:33], 0x0\n"
      "buffer_load_dwordx4 v249, s[56:59], 0 offen nt lds\n"
      "buffer_load_dwordx4 v249, s[56:59], 0 offen offset:2048 nt lds\n"
      "s_add_u32 m0, s50, 147456\n"
      "s_load_dword s92, s[18:19], 0x0\n"
      "buffer_load_dwordx4 v248, s[56:59], s22 offen nt lds\n"
      "buffer_load_dwordx4 v248, s[56:59], s22 offen offset:2048 nt lds\n"
      "s_bitset1_b32 m0, 10\n"
      "s_nop 0\n"
      "buffer_load_dwordx4 v249, s[56:59], s22 offen nt lds\n"
      "buffer_load_dwordx4 v249, s[56:59], s22 offen offset:2048 nt lds\n"
      "s_mov_b64 s[56:57], s[20:21]\n"
      "s_load_dword s92, s[14:15], 0x0\n"
      "s_load_dword s92, s[12:13], 0x0\n"
      "s_lshr_b32 s74, s50, 2\n"
      "s_lshr_b32 s75, s50, 5\n"
      "s_xor_b32 s75, s75, s63\n"
      "s_and_b32 s75, s75, 0x80\n"
      "s_and_b32 s76, s74, 0x800\n"
      "s_add_u32 s75, s75, s76\n"
      "s_add_u32 s70, s63, s50\n"
      "s_sub_u32 s70, s70, s75\n"
      "s_lshr_b32 s75, s50, 1\n"
      "s_lshr_b32 s71, s62, 2\n"
      "s_sub_u32 s71, s71, s75\n"
      "s_ashr_i32 s72, s71, 31\n"
      "s_mov_b32 s23, s59\n"
      "s_mov_b64 s[26:27], s[22:23]\n"
      "s_sub_u32 s20, s56, s70\n"
      "s_subb_u32 s21, s57, 0\n"
      "s_add_u32 m0, s74, 24576\n"
      "s_add_u32 s24, s10, s71\n"
      "buffer_load_dwordx4 v248, s[20:23], 0 offen nt lds\n"
      "s_addc_u32 s25, s11, s72\n"
      "s_add_u32 m0, s75, 16384\n"
      "s_nop 0\n"
      "buffer_load_dwordx4 v248, s[24:27], 0 offen nt lds\n"
      "s_bitset1_b32 m0, 10\n"
      "s_nop 0\n"
      "buffer_load_dwordx4 v249, s[24:27], 0 offen nt lds\n"
      "s_lshr_b32 s43, s63, 7\n"
      "s_lshl_b32 s76, s44, 9\n"
      "s_lshr_b32 s69, s50, 5\n"
      "s_add_u32 s76, s76, s69\n"
      "s_add_u32 s46, s18, s76\n"
      "s_addc_u32 s47, s19, 0\n"
      "s_mul_i32 s76, s43, 0x3000\n"
      "s_lshl_b32 s69, s44, 8\n"
      "s_and_b32 s69, s69, 0x7800\n"
      "s_add_u32 s14, s14, s69\n"
      "s_addc_u32 s15, s15, 0\n"
      "v_mov_b64 v[192:193], 0\n"
      "v_mov_b64 v[194:195], 0\n"
      "v_lshlrev_b32 v3, 6, v0\n"
      "v_and_b32 v3, 0x3c0, v3\n"
      "v_lshrrev_b32 v4, 1, v0\n"
      "v_and_or_b32 v211, v4, 24, v3\n"
      "v_mov_b64 v[200:201], 0\n"
      "v_mov_b64 v[202:203], 0\n"
      "v_mov_b64 v[204:205], 0\n"
      "v_mov_b64 v[206:207], 0\n"
      "s_cmp_lg_u32 s76, s50\n"
      "s_cbranch_scc1 NOMASK_%=\n"
      "global_load_dwordx2 v[200:201], v211, s[14:15] offset:0\n"
      "global_load_dwordx2 v[202:203], v211, s[14:15] offset:32\n"
      "global_load_dwordx2 v[204:205], v211, s[14:15] offset:1024\n"
      "global_load_dwordx2 v[206:207], v211, s[14:15] offset:1056\n"
      "NOMASK_%=:\n"
      "global_load_dword v208, v192, s[46:47] sc1\n"
      "s_mul_i32 s69, s43, 0x8000\n"
      "s_bitcmp1_b32 s44, 0\n"
      "s_cselect_b32 s66, 0x8000, 0\n"
      "s_xor_b32 s69, s69, s66\n"
      "v_and_b32 v228, 0xff, v248\n"
      "v_xor_b32 v228, s69, v228\n"
      "v_bfe_i32 v2, v0, 3, 1\n"
      "v_and_b32 v2, 0x1000, v2\n"
      "v_xor_b32 v228, v228, v2\n"
      "v_bfe_i32 v2, v0, 4, 1\n"
      "v_and_b32 v2, 0x20400, v2\n"
      "v_xor_b32 v228, v228, v2\n"
      "v_bfe_i32 v2, v0, 5, 1\n"
      "v_and_b32 v2, 0x100, v2\n"
      "v_xor_b32 v228, v228, v2\n"
      "v_bfe_i32 v2, v0, 6, 1\n"
      "v_and_b32 v2, 0x400, v2\n"
      "v_xor_b32 v228, v228, v2\n"
      "v_bfe_i32 v2, v0, 7, 1\n"
      "v_and_b32 v2, 0x2000, v2\n"
      "v_xor_b32 v228, v228, v2\n"
      "v_xor_b32 v231, 0x280, v228\n"
      "v_xor_b32 v230, 0x200, v228\n"
      "v_xor_b32 v229, 0x80, v228\n"
      "v_add_u32 v228, 0xc00, v228\n"
      "v_add_u32 v229, 0x800, v229\n"
      "v_add_u32 v230, 0x400, v230\n"
      "s_sub_u32 s56, s56, 0xc00\n"
      "s_subb_u32 s57, s57, 0\n"
      "s_sub_u32 s32, s32, 0xc00\n"
      "s_subb_u32 s33, s33, 0\n"
      "s_xor_b64 s[32:33], s[32:33], s[56:57]\n"
      "s_mov_b32 s69, 0\n"
      "s_bitcmp1_b32 s44, 4\n"
      "s_cselect_b32 s66, 0x10000, 0\n"
      "s_xor_b32 s69, s69, s66\n"
      "s_bitcmp1_b32 s44, 0\n"
      "s_cselect_b32 s66, 0x800, 0\n"
      "s_xor_b32 s69, s69, s66\n"
      "s_bitcmp1_b32 s44, 2\n"
      "s_cselect_b32 s66, 0x40800, 0\n"
      "s_xor_b32 s69, s69, s66\n"
      "s_bitcmp1_b32 s44, 4\n"
      "s_cselect_b32 s66, 0x4000, 0\n"
      "s_xor_b32 s69, s69, s66\n"
      "s_mov_b32 s60, s69\n"
      "s_mov_b32 s62, s50\n"
      "s_mov_b64 s[30:31], s[22:23]\n"
      "s_add_u32 s28, s12, s71\n"
      "s_addc_u32 s29, s13, s72\n"
      "s_lshl_b32 s69, s43, 13\n"
      "s_xor_b32 s69, s69, 0x2000\n"
      "v_add_u32 v2, s69, v241\n"
      "ds_write_b128 v241, v[192:195] offset:28672\n"
      "ds_write_b128 v2, v[192:195] offset:49152\n"
      "ds_write_b128 v2, v[192:195] offset:53248\n"
      "v_and_b32 v241, 0x3f0, v241\n"
      "s_add_u32 s69, s50, 81920\n"
      "v_add_u32 v2, s69, v241\n"
      "ds_write_b128 v2, v[192:195] offset:0\n"
      "ds_write_b128 v2, v[192:195] offset:2048\n"
      "s_and_b32 s91, s50, 8192\n"
      "v_lshlrev_b32 v4, 8, v0\n"
      "v_and_b32 v4, 0x800, v4\n"
      "v_lshlrev_b32 v5, 7, v0\n"
      "v_and_b32 v6, 0x380, v5\n"
      "v_or3_b32 v4, v4, v6, s91\n"
      "v_and_b32 v6, 0x60, v241\n"
      "v_and_b32 v2, 48, v0\n"
      "v_xor_b32 v6, v6, v2\n"
      "v_or_b32 v242, v4, v6\n"
      "v_xor_b32 v243, 64, v242\n"
      "v_lshlrev_b32 v4, 6, v0\n"
      "v_and_b32 v6, 0x800, v4\n"
      "v_lshlrev_b32 v2, 5, v0\n"
      "v_and_b32 v2, 0x380, v2\n"
      "s_add_u32 s69, s91, 16384\n"
      "v_or3_b32 v6, v6, v2, s69\n"
      "v_lshlrev_b32 v2, 3, v0\n"
      "v_and_or_b32 v6, v2, 24, v6\n"
      "v_lshlrev_b32 v2, 2, v0\n"
      "v_and_b32 v2, 0x60, v2\n"
      "v_xor_b32 v244, v6, v2\n"
      "v_xor_b32 v245, 0x20, v244\n"
      "v_xor_b32 v246, 0x40, v244\n"
      "v_xor_b32 v247, 0x60, v244\n"
      "v_and_b32 v5, 0x1e00, v5\n"
      "v_and_or_b32 v250, v241, 48, v5\n"
      "s_lshl_b32 s69, %[blk], 9\n"
      "s_and_b32 s69, s69, 0x1e00\n"
      "v_and_b32 v4, 0x1c0, v4\n"
      "v_or_b32 v251, s69, v4\n"
      "s_and_b32 s69, s50, 4096\n"
      "s_lshl_b32 s69, s69, 2\n"
      "s_add_u32 s69, s69, 131072\n"
      "s_sub_u32 s91, 16384, s91\n"
      "v_add_u32 v196, s69, v242\n"
      "v_add_u32 v198, s91, v242\n"
      "v_xor_b32 v214, 0x2000, v196\n"
      "v_add_u32 v197, s69, v243\n"
      "v_add_u32 v199, s91, v243\n"
      "v_xor_b32 v215, 0x2000, v197\n"
      "v_and_b32 v3, 1, v0\n"
      "v_bfe_u32 v4, v0, 1, 5\n"
      "v_lshlrev_b32 v3, 1, v3\n"
      "v_sub_u32 v4, v4, v3\n"
      "v_mov_b32 v219, s60\n"
      "v_bfe_i32 v2, v4, 0, 1\n"
      "v_and_b32 v2, 0x10000, v2\n"
      "v_xor_b32 v219, v219, v2\n"
      "v_bfe_i32 v2, v4, 1, 1\n"
      "v_and_b32 v2, 0x800, v2\n"
      "v_xor_b32 v219, v219, v2\n"
      "v_bfe_i32 v2, v4, 2, 1\n"
      "v_and_b32 v2, 0x40800, v2\n"
      "v_xor_b32 v219, v219, v2\n"
      "v_bfe_i32 v2, v4, 3, 1\n"
      "v_and_b32 v2, 0x80000, v2\n"
      "v_xor_b32 v219, v219, v2\n"
      "v_bfe_i32 v2, v4, 4, 1\n"
      "v_and_b32 v2, 0x4000, v2\n"
      "v_xor_b32 v219, v219, v2\n"
      "v_writelane_b32 v219, 0, 1\n"
      "s_mov_b32 s61, 0\n"
      "s_add_u32 s69, s50, 131072\n"
      "v_add_u32 v210, s69, v241\n"
      "v_and_b32 v2, 3, v0\n"
      "v_mov_b32 v4, 0x3f803f80\n"
      "v_cmp_eq_u32 vcc, 0, v2\n"
      "s_nop 1\n"
      "v_cndmask_b32 v5, 0, v4, vcc\n"
      "v_accvgpr_write_b32 a128, v5\n"
      "v_accvgpr_write_b32 a129, v5\n"
      "v_accvgpr_write_b32 a130, v5\n"
      "v_accvgpr_write_b32 a131, v5\n"
      "v_cmp_eq_u32 vcc, 1, v2\n"
      "s_nop 1\n"
      "v_cndmask_b32 v5, 0, v4, vcc\n"
      "v_accvgpr_write_b32 a132, v5\n"
      "v_accvgpr_write_b32 a133, v5\n"
      "v_accvgpr_write_b32 a134, v5\n"
      "v_accvgpr_write_b32 a135, v5\n"
      "v_cmp_eq_u32 vcc, 2, v2\n"
      "s_nop 1\n"
      "v_cndmask_b32 v5, 0, v4, vcc\n"
      "v_accvgpr_write_b32 a136, v5\n"
      "v_accvgpr_write_b32 a137, v5\n"
      "v_accvgpr_write_b32 a138, v5\n"
      "v_accvgpr_write_b32 a139, v5\n"
      "v_cmp_eq_u32 vcc, 3, v2\n"
      "s_nop 1\n"
      "v_cndmask_b32 v5, 0, v4, vcc\n"
      "v_accvgpr_write_b32 a140, v5\n"
      "v_accvgpr_write_b32 a141, v5\n"
      "v_accvgpr_write_b32 a142, v5\n"
      "v_accvgpr_write_b32 a143, v5\n"
      "v_mfma_f32_32x32x16_bf16 a[144:159], v[192:195], v[192:195], 0\n"
      "v_mfma_f32_32x32x16_bf16 a[160:175], v[192:195], v[192:195], 0\n"
      "v_mfma_f32_32x32x16_bf16 a[176:191], v[192:195], v[192:195], 0\n"
      "v_mfma_f32_32x32x16_bf16 a[192:207], v[192:195], v[192:195], 0\n"
      "v_mfma_f32_32x32x16_bf16 v[0:15], v[192:195], v[192:195], 0\n"
      "v_mfma_f32_32x32x16_bf16 v[16:31], v[192:195], v[192:195], 0\n"
      "v_mfma_f32_32x32x16_bf16 v[32:47], v[192:195], v[192:195], 0\n"
      "v_mfma_f32_32x32x16_bf16 v[48:63], v[192:195], v[192:195], 0\n"
      "v_mfma_f32_32x32x16_bf16 v[128:143], v[192:195], v[192:195], 0\n"
      "v_mov_b64 v[180:181], 0\n"
      "v_mov_b64 v[182:183], 0\n"
      "v_mov_b64 v[184:185], 0\n"
      "v_mov_b64 v[186:187], 0\n"
      "s_mov_b32 s94, 0x3e0293ee\n"
      "s_mov_b32 s95, s94\n"
      "s_lshl_b32 s69, s43, 13\n"
      "s_add_u32 s45, s69, s75\n"
      "s_mov_b32 m0, s62\n"
      "s_nop 0\n"
      "buffer_load_dwordx4 v228, s[56:59], s60 offen nt lds\n"
      "buffer_load_dwordx4 v229, s[56:59], s60 offen offset:1024 nt lds\n"
      "buffer_load_dwordx4 v230, s[56:59], s60 offen offset:2048 nt lds\n"
      "buffer_load_dwordx4 v231, s[56:59], s60 offen offset:3072 nt lds\n"
      "s_xor_b64 s[56:57], s[56:57], s[32:33]\n"
      "s_add_u32 s61, s61, 1\n"
      "s_add_u32 s62, s62, 0x4000\n"
      "s_and_b32 s62, s62, 0x1ffff\n"
      "v_readlane_b32 s60, v219, s61\n"
      "s_xor_b64 s[56:57], s[56:57], s[32:33]\n"
      "s_add_u32 s61, s61, 1\n"
      "s_add_u32 s62, s62, 0x4000\n"
      "s_and_b32 s62, s62, 0x1ffff\n"
      "v_readlane_b32 s60, v219, s61\n"
      "s_waitcnt vmcnt(8)\n"
      "s_memrealtime s[68:69]\n"
      "ds_read_b128 v[64:67], v210 offset:0\n"
      "ds_read_b128 v[68:71], v210 offset:1024\n"
      "ds_read_b128 v[72:75], v210 offset:2048\n"
      "ds_read_b128 v[76:79], v210 offset:3072\n"
      "ds_read_b128 v[80:83], v210 offset:16384\n"
      "ds_read_b128 v[84:87], v210 offset:17408\n"
      "ds_read_b128 v[88:91], v210 offset:18432\n"
      "ds_read_b128 v[92:95], v210 offset:19456\n"
      "s_add_u32 s20, s18, 65536\n"
      "s_addc_u32 s21, s19, 0\n"
      "s_lshl_b32 s75, %[blk], 6\n"
      "s_xor_b32 s74, s50, 4096\n"
      "s_add_u32 s75, s75, s74\n"
      "s_waitcnt lgkmcnt(0)\n"
      "s_sub_u32 s67, s68, s82\n"
      "s_min_u32 s67, s67, 165\n"
      "s_add_u32 s93, s82, s67\n"
      "s_sub_u32 s71, s68, 400\n"
      "s_cmp_lt_u32 s75, 512\n"
      "s_cbranch_scc0 NOLEAD_%=\n"
      "v_mov_b32 v188, s68\n"
      "v_mov_b32 v189, s68\n"
      "v_mov_b32 v190, s68\n"
      "v_mov_b32 v191, s68\n"
      "buffer_store_dwordx4 v[188:191], v250, s[20:23], s75 offen sc1\n"
      "s_nop 1\n"
      "NOLEAD_%=:\n"
      "s_mov_b32 m0, s62\n"
      "s_nop 0\n"
      "buffer_load_dwordx4 v228, s[56:59], s60 offen nt lds\n"
      "buffer_load_dwordx4 v229, s[56:59], s60 offen offset:1024 nt lds\n"
      "buffer_load_dwordx4 v230, s[56:59], s60 offen offset:2048 nt lds\n"
      "buffer_load_dwordx4 v231, s[56:59], s60 offen offset:3072 nt lds\n"
      "s_xor_b64 s[56:57], s[56:57], s[32:33]\n"
      "s_add_u32 s61, s61, 1\n"
      "s_add_u32 s62, s62, 0x4000\n"
      "s_and_b32 s62, s62, 0x1ffff\n"
      "v_readlane_b32 s60, v219, s61\n"
      "s_xor_b64 s[56:57], s[56:57], s[32:33]\n"
      "s_add_u32 s61, s61, 1\n"
      "s_add_u32 s62, s62, 0x4000\n"
      "s_and_b32 s62, s62, 0x1ffff\n"
      "v_readlane_b32 s60, v219, s61\n"
      "s_add_u32 m0, s45, 49152\n"
      "s_nop 0\n"
      "buffer_load_dwordx4 v248, s[28:31], 0 offen nt lds\n"
      "s_bitset1_b32 m0, 10\n"
      "s_nop 0\n"
      "buffer_load_dwordx4 v249, s[28:31], 0 offen nt lds\n"
      "v_lshlrev_b32 v188, 16, v64\n"
      "v_and_b32 v189, 0xffff0000, v64\n"
      "v_lshlrev_b32 v190, 16, v65\n"
      "v_and_b32 v191, 0xffff0000, v65\n"
      "v_lshlrev_b32 v192, 16, v66\n"
      "v_and_b32 v193, 0xffff0000, v66\n"
      "v_lshlrev_b32 v194, 16, v67\n"
      "v_and_b32 v195, 0xffff0000, v67\n"
      "v_pk_mul_f32 v[188:189], v[188:189], s[94:95]\n"
      "v_pk_mul_f32 v[190:191], v[190:191], s[94:95]\n"
      "v_pk_mul_f32 v[192:193], v[192:193], s[94:95]\n"
      "v_pk_mul_f32 v[194:195], v[194:195], s[94:95]\n"
      "v_cvt_pk_bf16_f32 v64, v188, v189\n"
      "v_cvt_pk_bf16_f32 v65, v190, v191\n"
      "v_cvt_pk_bf16_f32 v66, v192, v193\n"
      "v_cvt_pk_bf16_f32 v67, v194, v195\n"
      "ds_write_b128 v210, v[64:67] offset:0\n"
      "v_lshlrev_b32 v188, 16, v68\n"
      "v_and_b32 v189, 0xffff0000, v68\n"
      "v_lshlrev_b32 v190, 16, v69\n"
      "v_and_b32 v191, 0xffff0000, v69\n"
      "v_lshlrev_b32 v192, 16, v70\n"
      "v_and_b32 v193, 0xffff0000, v70\n"
      "v_lshlrev_b32 v194, 16, v71\n"
      "v_and_b32 v195, 0xffff0000, v71\n"
      "v_pk_mul_f32 v[188:189], v[188:189], s[94:95]\n"
      "v_pk_mul_f32 v[190:191], v[190:191], s[94:95]\n"
      "v_pk_mul_f32 v[192:193], v[192:193], s[94:95]\n"
      "v_pk_mul_f32 v[194:195], v[194:195], s[94:95]\n"
      "v_cvt_pk_bf16_f32 v68, v188, v189\n"
      "v_cvt_pk_bf16_f32 v69, v190, v191\n"
      "v_cvt_pk_bf16_f32 v70, v192, v193\n"
      "v_cvt_pk_bf16_f32 v71, v194, v195\n"
      "ds_write_b128 v210, v[68:71] offset:1024\n"
      "v_lshlrev_b32 v188, 16, v72\n"
      "v_and_b32 v189, 0xffff0000, v72\n"
      "v_lshlrev_b32 v190, 16, v73\n"
      "v_and_b32 v191, 0xffff0000, v73\n"
      "v_lshlrev_b32 v192, 16, v74\n"
      "v_and_b32 v193, 0xffff0000, v74\n"
      "v_lshlrev_b32 v194, 16, v75\n"
      "v_and_b32 v195, 0xffff0000, v75\n"
      "v_pk_mul_f32 v[188:189], v[188:189], s[94:95]\n"
      "v_pk_mul_f32 v[190:191], v[190:191], s[94:95]\n"
      "v_pk_mul_f32 v[192:193], v[192:193], s[94:95]\n"
      "v_pk_mul_f32 v[194:195], v[194:195], s[94:95]\n"
      "v_cvt_pk_bf16_f32 v72, v188, v189\n"
      "v_cvt_pk_bf16_f32 v73, v190, v191\n"
      "v_cvt_pk_bf16_f32 v74, v192, v193\n"
      "v_cvt_pk_bf16_f32 v75, v194, v195\n"
      "ds_write_b128 v210, v[72:75] offset:2048\n"
      "v_lshlrev_b32 v188, 16, v76\n"
      "v_and_b32 v189, 0xffff0000, v76\n"
      "v_lshlrev_b32 v190, 16, v77\n"
      "v_and_b32 v191, 0xffff0000, v77\n"
      "v_lshlrev_b32 v192, 16, v78\n"
      "v_and_b32 v193, 0xffff0000, v78\n"
      "v_lshlrev_b32 v194, 16, v79\n"
      "v_and_b32 v195, 0xffff0000, v79\n"
      "v_pk_mul_f32 v[188:189], v[188:189], s[94:95]\n"
      "v_pk_mul_f32 v[190:191], v[190:191], s[94:95]\n"
      "v_pk_mul_f32 v[192:193], v[192:193], s[94:95]\n"
      "v_pk_mul_f32 v[194:195], v[194:195], s[94:95]\n"
      "v_cvt_pk_bf16_f32 v76, v188, v189\n"
      "v_cvt_pk_bf16_f32 v77, v190, v191\n"
      "v_cvt_pk_bf16_f32 v78, v192, v193\n"
      "v_cvt_pk_bf16_f32 v79, v194, v195\n"
      "ds_write_b128 v210, v[76:79] offset:3072\n"
      "v_lshlrev_b32 v188, 16, v80\n"
      "v_and_b32 v189, 0xffff0000, v80\n"
      "v_lshlrev_b32 v190, 16, v81\n"
      "v_and_b32 v191, 0xffff0000, v81\n"
      "v_lshlrev_b32 v192, 16, v82\n"
      "v_and_b32 v193, 0xffff0000, v82\n"
      "v_lshlrev_b32 v194, 16, v83\n"
      "v_and_b32 v195, 0xffff0000, v83\n"
      "v_pk_mul_f32 v[188:189], v[188:189], s[94:95]\n"
      "v_pk_mul_f32 v[190:191], v[190:191], s[94:95]\n"
      "v_pk_mul_f32 v[192:193], v[192:193], s[94:95]\n"
      "v_pk_mul_f32 v[194:195], v[194:195], s[94:95]\n"
      "v_cvt_pk_bf16_f32 v80, v188, v189\n"
      "v_cvt_pk_bf16_f32 v81, v190, v191\n"
      "v_cvt_pk_bf16_f32 v82, v192, v193\n"
      "v_cvt_pk_bf16_f32 v83, v194, v195\n"
      "ds_write_b128 v210, v[80:83] offset:16384\n"
      "v_lshlrev_b32 v188, 16, v84\n"
      "v_and_b32 v189, 0xffff0000, v84\n"
      "v_lshlrev_b32 v190, 16, v85\n"
      "v_and_b32 v191, 0xffff0000, v85\n"
      "v_lshlrev_b32 v192, 16, v86\n"
      "v_and_b32 v193, 0xffff0000, v86\n"
      "v_lshlrev_b32 v194, 16, v87\n"
      "v_and_b32 v195, 0xffff0000, v87\n"
      "v_pk_mul_f32 v[188:189], v[188:189], s[94:95]\n"
      "v_pk_mul_f32 v[190:191], v[190:191], s[94:95]\n"
      "v_pk_mul_f32 v[192:193], v[192:193], s[94:95]\n"
      "v_pk_mul_f32 v[194:195], v[194:195], s[94:95]\n"
      "v_cvt_pk_bf16_f32 v84, v188, v189\n"
      "v_cvt_pk_bf16_f32 v85, v190, v191\n"
      "v_cvt_pk_bf16_f32 v86, v192, v193\n"
      "v_cvt_pk_bf16_f32 v87, v194, v195\n"
      "ds_write_b128 v210, v[84:87] offset:17408\n"
      "v_lshlrev_b32 v188, 16, v88\n"
      "v_and_b32 v189, 0xffff0000, v88\n"
      "v_lshlrev_b32 v190, 16, v89\n"
      "v_and_b32 v191, 0xffff0000, v89\n"
      "v_lshlrev_b32 v192, 16, v90\n"
      "v_and_b32 v193, 0xffff0000, v90\n"
      "v_lshlrev_b32 v194, 16, v91\n"
      "v_and_b32 v195, 0xffff0000, v91\n"
      "v_pk_mul_f32 v[188:189], v[188:189], s[94:95]\n"
      "v_pk_mul_f32 v[190:191], v[190:191], s[94:95]\n"
      "v_pk_mul_f32 v[192:193], v[192:193], s[94:95]\n"
      "v_pk_mul_f32 v[194:195], v[194:195], s[94:95]\n"
      "v_cvt_pk_bf16_f32 v88, v188, v189\n"
      "v_cvt_pk_bf16_f32 v89, v190, v191\n"
      "v_cvt_pk_bf16_f32 v90, v192, v193\n"
      "v_cvt_pk_bf16_f32 v91, v194, v195\n"
      "ds_write_b128 v210, v[88:91] offset:18432\n"
      "v_lshlrev_b32 v188, 16, v92\n"
      "v_and_b32 v189, 0xffff0000, v92\n"
      "v_lshlrev_b32 v190, 16, v93\n"
      "v_and_b32 v191, 0xffff0000, v93\n"
      "v_lshlrev_b32 v192, 16, v94\n"
      "v_and_b32 v193, 0xffff0000, v94\n"
      "v_lshlrev_b32 v194, 16, v95\n"
      "v_and_b32 v195, 0xffff0000, v95\n"
      "v_pk_mul_f32 v[188:189], v[188:189], s[94:95]\n"
      "v_pk_mul_f32 v[190:191], v[190:191], s[94:95]\n"
      "v_pk_mul_f32 v[192:193], v[192:193], s[94:95]\n"
      "v_pk_mul_f32 v[194:195], v[194:195], s[94:95]\n"
      "v_cvt_pk_bf16_f32 v92, v188, v189\n"
      "v_cvt_pk_bf16_f32 v93, v190, v191\n"
      "v_cvt_pk_bf16_f32 v94, v192, v193\n"
      "v_cvt_pk_bf16_f32 v95, v194, v195\n"
      "ds_write_b128 v210, v[92:95] offset:19456\n"
      "s_mov_b32 m0, s62\n"
      "s_nop 0\n"
      "buffer_load_dwordx4 v228, s[56:59], s60 offen nt lds\n"
      "buffer_load_dwordx4 v229, s[56:59], s60 offen offset:1024 nt lds\n"
      "buffer_load_dwordx4 v230, s[56:59], s60 offen offset:2048 nt lds\n"
      "buffer_load_dwordx4 v231, s[56:59], s60 offen offset:3072 nt lds\n"
      "s_xor_b64 s[56:57], s[56:57], s[32:33]\n"
      "s_add_u32 s61, s61, 1\n"
      "s_add_u32 s62, s62, 0x4000\n"
      "s_and_b32 s62, s62, 0x1ffff\n"
      "v_readlane_b32 s60, v219, s61\n"
      "s_cmp_eq_u32 s50, 0\n"
      "s_cselect_b32 s38, 0x44554001, 1\n"
      "s_cselect_b32 s39, 0x2a040083, 3\n"
      "s_cselect_b32 s52, 0x31109890, 0\n"
      "s_cselect_b32 s53, 0x80011, 0\n"
      "s_mov_b64 s[34:35], 0\n"
      "s_bitset1_b32 s39, 29\n"
      "s_and_b32 s69, %[blk], 7\n"
      "s_mul_i32 s69, s69, 5\n"
      "s_lshr_b32 s74, %[blk], 4\n"
      "s_add_u32 s69, s69, s74\n"
      "s_and_b32 s69, s69, 15\n"
      "s_mul_i32 s74, s69, 16\n"
      "s_lshr_b32 s74, s74, 6\n"
      "s_lshl2_add_u32 s76, s74, 9\n"
      "s_bitset1_b64 s[34:35], s76\n"
      "s_lshl_b32 s74, s74, 1\n"
      "s_sub_u32 s74, 28, s74\n"
      "s_bitset1_b32 s39, s74\n"
      "s_mul_i32 s74, s69, 20\n"
      "s_lshr_b32 s74, s74, 6\n"
      "s_lshl2_add_u32 s76, s74, 25\n"
      "s_bitset1_b64 s[34:35], s76\n"
      "s_lshl_b32 s74, s74, 1\n"
      "s_sub_u32 s74, 20, s74\n"
      "s_bitset1_b32 s39, s74\n"
      "s_mul_i32 s74, s69, 20\n"
      "s_lshr_b32 s74, s74, 6\n"
      "s_lshl2_add_u32 s76, s74, 45\n"
      "s_bitset1_b64 s[34:35], s76\n"
      "s_lshl_b32 s74, s74, 1\n"
      "s_sub_u32 s74, 10, s74\n"
      "s_bitset1_b32 s39, s74\n"
      "s_mov_b32 s54, 3\n"
      "s_mov_b32 s70, 250\n"
      "s_mov_b32 s86, 59\n"
      "s_mov_b32 s77, 177\n"
      "s_lshl_b32 s91, s50, 16\n"
      "s_sub_u32 s80, 46, s91\n"
      "s_sub_u32 s90, 34, s91\n"
      "s_sub_u32 s72, s75, 18432\n"
      "s_and_b32 s79, %[blk], 31\n"
      "s_lshl_b32 s79, s79, 8\n"
      "s_add_u32 s79, s79, 8192\n"
      "s_mul_i32 s69, %[blk], 53\n"
      "s_lshr_b32 s69, s69, 8\n"
      "s_add_u32 s66, s93, s69\n"
      "s_add_u32 s66, s66, 112\n"
      "s_sub_u32 s81, s93, s71\n"
      "s_add_u32 s81, s81, -15\n"
      "s_sub_u32 s66, s66, s91\n"
      "s_mov_b32 s88, s66\n"
      "s_mov_b32 s65, s66\n"
      "s_getpc_b64 s[84:85]\n"
      "PC_%=:\n"
      "s_add_u32 s84, s84, HOOK_%=-PC_%=\n"
      "s_addc_u32 s85, s85, 0\n"
      "s_waitcnt vmcnt(14) lgkmcnt(0)\n"
      "s_barrier\n"
      "ds_read_b128 a[64:67], v198 offset:8192\n"
      "ds_read_b128 a[72:75], v199 offset:8192\n"
      "ds_read_b128 a[80:83], v198 offset:9216\n"
      "ds_read_b128 a[88:91], v199 offset:9216\n"
      "ds_read_b128 a[0:3], v196 offset:0\n"
      "ds_read_b128 a[16:19], v197 offset:0\n"
      "ds_read_b128 a[32:35], v196 offset:1024\n"
      "ds_read_b128 a[48:51], v197 offset:1024\n"
      "ds_read_b128 a[4:7], v196 offset:4096\n"
      "ds_read_b128 a[20:23], v197 offset:4096\n"
      "ds_read_b128 a[36:39], v196 offset:5120\n"
      "ds_read_b128 a[52:55], v197 offset:5120\n"
      "v_readfirstlane_b32 s40, v208\n"
      "s_mov_b32 s94, 0xceabfb8f\n"
      "v_lshlrev_b32 v144, 16, v200\n"
      "v_and_b32 v145, 0xffff0000, v200\n"
      "v_lshlrev_b32 v146, 16, v201\n"
      "v_and_b32 v147, 0xffff0000, v201\n"
      "v_lshlrev_b32 v148, 16, v202\n"
      "v_and_b32 v149, 0xffff0000, v202\n"
      "v_lshlrev_b32 v150, 16, v203\n"
      "v_and_b32 v151, 0xffff0000, v203\n"
      "v_lshlrev_b32 v152, 16, v204\n"
      "v_and_b32 v153, 0xffff0000, v204\n"
      "v_lshlrev_b32 v154, 16, v205\n"
      "v_and_b32 v155, 0xffff0000, v205\n"
      "v_lshlrev_b32 v156, 16, v206\n"
      "v_and_b32 v157, 0xffff0000, v206\n"
      "v_lshlrev_b32 v158, 16, v207\n"
      "v_and_b32 v159, 0xffff0000, v207\n"
      "s_waitcnt lgkmcnt(4)\n"
      "v_mfma_f32_16x16x32_bf16 v[64:67], a[64:67], a[0:3], 0\n"
      "ds_read_b128 a[8:11], v214 offset:0\n"
      "v_fma_f32 v144, -v144, s94, s94\n"
      "v_mfma_f32_16x16x32_bf16 v[64:67], a[72:75], a[16:19], v[64:67]\n"
      "ds_read_b128 a[24:27], v215 offset:0\n"
      "v_fma_f32 v145, -v145, s94, s94\n"
      "v_mfma_f32_16x16x32_bf16 v[64:67], a[80:83], a[32:35], v[64:67]\n"
      "ds_read_b128 a[40:43], v214 offset:1024\n"
      "v_fma_f32 v146, -v146, s94, s94\n"
      "v_mfma_f32_16x16x32_bf16 v[64:67], a[88:91], a[48:51], v[64:67]\n"
      "ds_read_b128 a[56:59], v215 offset:1024\n"
      "v_fma_f32 v147, -v147, s94, s94\n"
      "s_waitcnt lgkmcnt(4)\n"
      "v_mfma_f32_16x16x32_bf16 v[72:75], a[64:67], a[4:7], 0\n"
      "ds_read_b128 a[12:15], v214 offset:4096\n"
      "v_fma_f32 v148, -v148, s94, s94\n"
      "v_mfma_f32_16x16x32_bf16 v[72:75], a[72:75], a[20:23], v[72:75]\n"
      "ds_read_b128 a[28:31], v215 offset:4096\n"
      "v_fma_f32 v149, -v149, s94, s94\n"
      "v_mfma_f32_16x16x32_bf16 v[72:75], a[80:83], a[36:39], v[72:75]\n"
      "ds_read_b128 a[44:47], v214 offset:5120\n"
      "v_fma_f32 v150, -v150, s94, s94\n"
      "v_mfma_f32_16x16x32_bf16 v[72:75], a[88:91], a[52:55], v[72:75]\n"
      "ds_read_b128 a[60:63], v215 offset:5120\n"
      "v_fma_f32 v151, -v151, s94, s94\n"
      "s_waitcnt lgkmcnt(4)\n"
      "v_mfma_f32_16x16x32_bf16 v[80:83], a[64:67], a[8:11], 0\n"
      "ds_read_b128 a[96:99], v198 offset:0\n"
      "v_fma_f32 v152, -v152, s94, s94\n"
      "v_mfma_f32_16x16x32_bf16 v[80:83], a[72:75], a[24:27], v[80:83]\n"
      "ds_read_b128 a[100:103], v198 offset:4096\n"
      "v_fma_f32 v153, -v153, s94, s94\n"
      "v_mfma_f32_16x16x32_bf16 v[80:83], a[80:83], a[40:43], v[80:83]\n"
      "ds_read_b128 a[104:107], v199 offset:0\n"
      "v_fma_f32 v154, -v154, s94, s94\n"
      "v_mfma_f32_16x16x32_bf16 v[80:83], a[88:91], a[56:59], v[80:83]\n"
      "ds_read_b128 a[108:111], v199 offset:4096\n"
      "v_fma_f32 v155, -v155, s94, s94\n"
      "s_waitcnt lgkmcnt(4)\n"
      "v_mfma_f32_16x16x32_bf16 v[88:91], a[64:67], a[12:15], 0\n"
      "ds_read_b128 a[112:115], v198 offset:1024\n"
      "v_fma_f32 v156, -v156, s94, s94\n"
      "v_mfma_f32_16x16x32_bf16 v[88:91], a[72:75], a[28:31], v[88:91]\n"
      "ds_read_b128 a[116:119], v198 offset:5120\n"
      "v_fma_f32 v157, -v157, s94, s94\n"
      "v_mfma_f32_16x16x32_bf16 v[88:91], a[80:83], a[44:47], v[88:91]\n"
      "ds_read_b128 a[120:123], v199 offset:1024\n"
      "v_fma_f32 v158, -v158, s94, s94\n"
      "v_mfma_f32_16x16x32_bf16 v[88:91], a[88:91], a[60:63], v[88:91]\n"
      "ds_read_b128 a[124:127], v199 offset:5120\n"
      "v_fma_f32 v159, -v159, s94, s94\n"
      "s_mov_b32 m0, s62\n"
      "v_add_u32 v248, s60, v228\n"
      "buffer_load_dwordx4 v229, s[56:59], s60 offen offset:1024 nt lds\n"
      "buffer_load_dwordx4 v231, s[56:59], s60 offen offset:3072 nt lds\n"
      "v_add_u32 v249, s60, v230\n"
      "s_waitcnt lgkmcnt(0)\n"
      "v_mfma_f32_16x16x32_bf16 v[96:99], a[96:99], a[0:3], v[144:147]\n"
      "s_add_u32 s40, s40, 0x7f4a7c15\n"
      "v_mfma_f32_16x16x32_bf16 v[96:99], a[104:107], a[16:19], v[96:99]\n"
      "s_lshl_b32 s69, s44, 3\n"
      "v_max3_f32 v188, v64, v65, v66\n"
      "v_max3_f32 v189, v72, v73, v74\n"
      "v_mfma_f32_16x16x32_bf16 v[96:99], a[112:115], a[32:35], v[96:99]\n"
      "s_lshr_b32 s74, s50, 12\n"
      "v_max3_f32 v190, v80, v81, v82\n"
      "v_max3_f32 v191, v88, v89, v90\n"
      "v_mfma_f32_16x16x32_bf16 v[96:99], a[120:123], a[48:51], v[96:99]\n"
      "s_add_u32 s69, s69, s74\n"
      "v_max_f32 v188, v188, v67\n"
      "v_max_f32 v189, v189, v75\n"
      "v_mfma_f32_16x16x32_bf16 v[104:107], a[96:99], a[4:7], v[152:155]\n"
      "s_mul_i32 s69, s69, 4608\n"
      "v_max_f32 v190, v190, v83\n"
      "v_max_f32 v191, v191, v91\n"
      "v_mfma_f32_16x16x32_bf16 v[104:107], a[104:107], a[20:23], v[104:107]\n"
      "s_add_u32 s69, s69, 81920\n"
      "v_mov_b32 v192, v188\n"
      "v_mov_b32 v193, v189\n"
      "v_mfma_f32_16x16x32_bf16 v[104:107], a[112:115], a[36:39], v[104:107]\n"
      "s_add_u32 s24, s18, s69\n"
      "v_mov_b32 v194, v190\n"
      "v_mov_b32 v195, v191\n"
      "v_mfma_f32_16x16x32_bf16 v[104:107], a[120:123], a[52:55], v[104:107]\n"
      "s_addc_u32 s25, s19, 0\n"
      "v_permlane16_swap_b32 v188, v192\n"
      "v_permlane16_swap_b32 v189, v193\n"
      "v_mfma_f32_16x16x32_bf16 v[112:115], a[96:99], a[8:11], v[144:147]\n"
      "s_movk_i32 s26, 23040\n"
      "v_permlane16_swap_b32 v190, v194\n"
      "v_permlane16_swap_b32 v191, v195\n"
      "v_mfma_f32_16x16x32_bf16 v[112:115], a[104:107], a[24:27], v[112:115]\n"
      "s_mul_i32 s78, s43, 18432\n"
      "v_max_f32 v188, v188, v192\n"
      "v_max_f32 v189, v189, v193\n"
      "v_mfma_f32_16x16x32_bf16 v[112:115], a[112:115], a[40:43], v[112:115]\n"
      "s_and_b32 s69, s50, 4096\n"
      "v_max_f32 v190, v190, v194\n"
      "v_max_f32 v191, v191, v195\n"
      "v_mfma_f32_16x16x32_bf16 v[112:115], a[120:123], a[56:59], v[112:115]\n"
      "s_lshl_b32 s69, s69, 2\n"
      "v_mov_b32 v192, v188\n"
      "v_mov_b32 v193, v189\n"
      "v_mfma_f32_16x16x32_bf16 v[120:123], a[96:99], a[12:15], v[152:155]\n"
      "s_and_b32 s74, s50, 8192\n"
      "v_mov_b32 v194, v190\n"
      "v_mov_b32 v195, v191\n"
      "v_mfma_f32_16x16x32_bf16 v[120:123], a[104:107], a[28:31], v[120:123]\n"
      "s_add_u32 s74, s74, 4096\n"
      "v_permlane32_swap_b32 v188, v192\n"
      "v_permlane32_swap_b32 v189, v193\n"
      "v_mfma_f32_16x16x32_bf16 v[120:123], a[112:115], a[44:47], v[120:123]\n"
      "s_add_u32 s69, s69, s74\n"
      "v_permlane32_swap_b32 v190, v194\n"
      "v_permlane32_swap_b32 v191, v195\n"
      "v_mfma_f32_16x16x32_bf16 v[120:123], a[120:123], a[60:63], v[120:123]\n"
      "s_lshl_b32 s74, s43, 7\n"
      "v_max_f32 v188, v188, v192\n"
      "v_max_f32 v189, v189, v193\n"
      "v_mfma_f32_16x16x32_bf16 v[100:103], a[100:103], a[0:3], v[148:151]\n"
      "s_add_u32 s69, s69, s74\n"
      "v_max_f32 v190, v190, v194\n"
      "v_max_f32 v191, v191, v195\n"
      "v_mfma_f32_16x16x32_bf16 v[100:103], a[108:111], a[16:19], v[100:103]\n"
      "s_lshl_b32 s74, s44, 15\n"
      "v_xor_b32 v176, 0x80000000, v188\n"
      "v_xor_b32 v177, 0x80000000, v189\n"
      "v_mfma_f32_16x16x32_bf16 v[100:103], a[116:119], a[32:35], v[100:103]\n"
      "s_add_u32 s69, s69, s74\n"
      "v_xor_b32 v178, 0x80000000, v190\n"
      "v_xor_b32 v179, 0x80000000, v191\n"
      "v_mfma_f32_16x16x32_bf16 v[100:103], a[124:127], a[48:51], v[100:103]\n"
      "s_add_u32 s30, s16, s69\n"
      "v_mov_b32 v160, v176\n"
      "v_mov_b32 v161, v176\n"
      "v_mfma_f32_16x16x32_bf16 v[108:111], a[100:103], a[4:7], v[156:159]\n"
      "s_addc_u32 s31, s17, 0\n"
      "v_mov_b32 v162, v176\n"
      "v_mov_b32 v163, v176\n"
      "v_mfma_f32_16x16x32_bf16 v[108:111], a[108:111], a[20:23], v[108:111]\n"
      "v_mov_b32 v164, v177\n"
      "v_mov_b32 v165, v177\n"
      "v_mfma_f32_16x16x32_bf16 v[108:111], a[116:119], a[36:39], v[108:111]\n"
      "v_mov_b32 v166, v177\n"
      "v_mov_b32 v167, v177\n"
      "v_mfma_f32_16x16x32_bf16 v[108:111], a[124:127], a[52:55], v[108:111]\n"
      "v_mov_b32 v168, v178\n"
      "v_mov_b32 v169, v178\n"
      "v_mfma_f32_16x16x32_bf16 v[116:119], a[100:103], a[8:11], v[148:151]\n"
      "v_mov_b32 v170, v178\n"
      "v_mov_b32 v171, v178\n"
      "v_mfma_f32_16x16x32_bf16 v[116:119], a[108:111], a[24:27], v[116:119]\n"
      "v_mov_b32 v172, v179\n"
      "v_mov_b32 v173, v179\n"
      "v_mfma_f32_16x16x32_bf16 v[116:119], a[116:119], a[40:43], v[116:119]\n"
      "v_mov_b32 v174, v179\n"
      "v_mov_b32 v175, v179\n"
      "v_mfma_f32_16x16x32_bf16 v[116:119], a[124:127], a[56:59], v[116:119]\n"
      "v_mov_b32 v209, 139248\n"
      "v_lshl_add_u32 v232, s50, 1, v241\n"
      "v_mfma_f32_16x16x32_bf16 v[124:127], a[100:103], a[12:15], v[156:159]\n"
      "v_add_u32 v232, 0x10000, v232\n"
      "v_xor_b32 v233, 0x4000, v232\n"
      "v_mfma_f32_16x16x32_bf16 v[124:127], a[108:111], a[28:31], v[124:127]\n"
      "v_add_u32 v234, 0x10000, v232\n"
      "v_xor_b32 v235, 0x4000, v234\n"
      "v_mfma_f32_16x16x32_bf16 v[124:127], a[116:119], a[44:47], v[124:127]\n"
      "v_lshrrev_b32 v240, 1, v241\n"
      "v_add_u32 v240, 0x1000, v240\n"
      "v_mfma_f32_16x16x32_bf16 v[124:127], a[124:127], a[60:63], v[124:127]\n"
      "v_mov_b32 v196, 0\n"
      "v_pk_add_f32 v[96:97], v[96:97], v[160:161]\n"
      "v_pk_add_f32 v[98:99], v[98:99], v[160:161]\n"
      "v_pk_add_f32 v[104:105], v[104:105], v[164:165]\n"
      "v_pk_add_f32 v[106:107], v[106:107], v[164:165]\n"
      "v_pk_add_f32 v[112:113], v[112:113], v[168:169]\n"
      "v_pk_add_f32 v[114:115], v[114:115], v[168:169]\n"
      "v_pk_add_f32 v[120:121], v[120:121], v[172:173]\n"
      "v_pk_add_f32 v[122:123], v[122:123], v[172:173]\n"
      "v_pk_add_f32 v[100:101], v[100:101], v[160:161]\n"
      "v_pk_add_f32 v[102:103], v[102:103], v[160:161]\n"
      "v_pk_add_f32 v[108:109], v[108:109], v[164:165]\n"
      "v_pk_add_f32 v[110:111], v[110:111], v[164:165]\n"
      "v_pk_add_f32 v[116:117], v[116:117], v[168:169]\n"
      "v_pk_add_f32 v[118:119], v[118:119], v[168:169]\n"
      "v_pk_add_f32 v[124:125], v[124:125], v[172:173]\n"
      "v_pk_add_f32 v[126:127], v[126:127], v[172:173]\n"
      "v_mov_b32 v224, v244\n"
      "v_mov_b32 v225, v245\n"
      "v_mov_b32 v226, v246\n"
      "v_mov_b32 v227, v247\n"
      "v_xor_b32 v220, 0x2000, v244\n"
      "v_xor_b32 v221, 0x2000, v245\n"
      "v_xor_b32 v222, 0x2000, v246\n"
      "v_xor_b32 v223, 0x2000, v247\n"
      "s_lshl_b32 s76, s43, 3\n"
      "v_mov_b32 v237, s40\n"
      "v_mov_b32 v238, s76\n"
      "v_xor_b32 v239, 8, v238\n"
      "s_memrealtime s[68:69]\n"
      "s_waitcnt vmcnt(12) lgkmcnt(0)\n"
      "s_sub_u32 s67, s68, s88\n"
      "s_cmp_lt_i32 s67, -8\n"
      "s_cbranch_scc1 GW0_%=\n"
      "GX0_%=:\n"
      "s_barrier\n"
      "ds_read_b128 a[64:67], v242 offset:0\n"
      "ds_read_b128 a[68:71], v242 offset:4096\n"
      "ds_read_b128 a[72:75], v243 offset:0\n"
      "ds_read_b128 a[76:79], v243 offset:4096\n"
      "ds_read_b128 a[80:83], v242 offset:1024\n"
      "ds_read_b128 a[84:87], v242 offset:5120\n"
      "ds_read_b128 a[88:91], v243 offset:1024\n"
      "ds_read_b128 a[92:95], v243 offset:5120\n"
      "ds_write_b32 v209, v196\n"
      "s_add_u32 s38, s38, s38\n"
      "s_memrealtime s[68:69]\n"
      "s_xor_b64 s[56:57], s[56:57], s[32:33]\n"
      "s_add_u32 s61, s61, 1\n"
      "s_add_u32 s62, s62, 0x4000\n"
      "s_and_b32 s62, s62, 0x1ffff\n"
      "v_readlane_b32 s60, v219, s61\n"
      "s_waitcnt lgkmcnt(0)\n"
      "s_sub_u32 s67, s68, s66\n"
      "s_cmp_gt_i32 s67, s70\n"
      "s_cselect_b32 s69, s77, s86\n"
      "s_cselect_b32 s70, 60, s70\n"
      "s_cselect_b32 s90, s80, s90\n"
      "s_add_u32 s66, s66, s69\n"
      "s_sub_u32 s76, s68, s83\n"
      "s_mov_b32 s83, s82\n"
      "LOOP_%=:\n"
      "v_mfma_f32_16x16x32_bf16 v[64:67], a[64:67], a[0:3], v[160:163]\n"
      "s_mov_b32 s82, s68\n"
      "v_exp_f32 v188, v96\n"
      "v_mfma_f32_16x16x32_bf16 v[64:67], a[72:75], a[16:19], v[64:67]\n"
      "s_add_u32 s65, s68, s90\n"
      "v_add_f32 v180, v180, v184\n"
      "v_add_f32 v181, v181, v185\n"
      "v_mfma_f32_16x16x32_bf16 v[64:67], a[80:83], a[32:35], v[64:67]\n"
      "s_max_u32 s88, s66, s65\n"
      "v_exp_f32 v189, v97\n"
      "v_mfma_f32_16x16x32_bf16 v[64:67], a[88:91], a[48:51], v[64:67]\n"
      "s_getreg_b32 s87, hwreg(HW_REG_IB_STS, 0, 4)\n"
      "v_add_f32 v182, v182, v186\n"
      "v_add_f32 v183, v183, v187\n"
      "v_mfma_f32_16x16x32_bf16 v[72:75], a[64:67], a[4:7], v[164:167]\n"
      "s_cmp_lt_i32 s76, 104\n"
      "v_exp_f32 v190, v98\n"
      "v_mfma_f32_16x16x32_bf16 v[72:75], a[72:75], a[20:23], v[72:75]\n"
      "s_cselect_b32 s76, 15, 4\n"
      "v_exp_f32 v191, v99\n"
      "v_mfma_f32_16x16x32_bf16 v[72:75], a[80:83], a[36:39], v[72:75]\n"
      "s_cmp_gt_u32 s87, s76\n"
      "v_cvt_pk_bf16_f32 v144, v188, v189\n"
      "v_cvt_pk_bf16_f32 v145, v190, v191\n"
      "v_mfma_f32_16x16x32_bf16 v[72:75], a[88:91], a[52:55], v[72:75]\n"
      "s_addc_u32 s89, s89, 0\n"
      "v_exp_f32 v192, v100\n"
      "v_mfma_f32_16x16x32_bf16 v[80:83], a[64:67], a[8:11], v[168:171]\n"
      "s_add_u32 s39, s39, s39\n"
      "v_exp_f32 v193, v101\n"
      "v_mfma_f32_16x16x32_bf16 v[80:83], a[72:75], a[24:27], v[80:83]\n"
      "s_memrealtime s[68:69]\n"
      "v_exp_f32 v194, v102\n"
      "v_mfma_f32_16x16x32_bf16 v[80:83], a[80:83], a[40:43], v[80:83]\n"
      "v_exp_f32 v195, v103\n"
      "v_mfma_f32_16x16x32_bf16 v[80:83], a[88:91], a[56:59], v[80:83]\n"
      "s_mov_b32 m0, s62\n"
      "v_cvt_pk_bf16_f32 v146, v192, v193\n"
      "v_cvt_pk_bf16_f32 v147, v194, v195\n"
      "v_mfma_f32_16x16x32_bf16 v[88:91], a[64:67], a[12:15], v[172:175]\n"
      "buffer_load_dwordx4 v228, s[56:59], s60 offen nt lds\n"
      "v_mfma_f32_16x16x32_bf16 v[184:187], a[128:131], v[144:147], 0\n"
      "buffer_load_dwordx4 v229, s[56:59], s60 offen offset:1024 nt lds\n"
      "v_mfma_f32_16x16x32_bf16 v[88:91], a[72:75], a[28:31], v[88:91]\n"
      "buffer_load_dwordx4 v230, s[56:59], s60 offen offset:2048 nt lds\n"
      "v_mfma_f32_16x16x32_bf16 v[88:91], a[80:83], a[44:47], v[88:91]\n"
      "buffer_load_dwordx4 v231, s[56:59], s60 offen offset:3072 nt lds\n"
      "s_cbranch_scc1 SP1_%=\n"
      "HD1_%=:\n"
      "s_cmp_lg_u32 s54, 0\n"
      "s_waitcnt vmcnt(12) lgkmcnt(0)\n"
      "s_cbranch_scc1 W1_%=\n"
      "s_waitcnt vmcnt(8)\n"
      "W1_%=:\n"
      "s_sub_u32 s67, s68, s88\n"
      "s_cmp_lt_i32 s67, -8\n"
      "s_cbranch_scc1 GW4_%=\n"
      "GX4_%=:\n"
      "s_barrier\n"
      "v_mfma_f32_16x16x32_bf16 v[88:91], a[88:91], a[60:63], v[88:91]\n"
      "s_memrealtime s[68:69]\n"
      "ds_read_b64_tr_b16 a[96:97], v244 offset:0\n"
      "ds_read_b64_tr_b16 a[98:99], v244 offset:4096\n"
      "v_mfma_f32_16x16x32_bf16 v[68:71], a[68:71], a[0:3], v[160:163]\n"
      "s_xor_b64 s[56:57], s[56:57], s[32:33]\n"
      "ds_read_b64_tr_b16 a[100:101], v245 offset:0\n"
      "ds_read_b64_tr_b16 a[102:103], v245 offset:4096\n"
      "v_mfma_f32_16x16x32_bf16 v[68:71], a[76:79], a[16:19], v[68:71]\n"
      "s_add_u32 s61, s61, 1\n"
      "ds_read_b64_tr_b16 a[104:105], v246 offset:0\n"
      "ds_read_b64_tr_b16 a[106:107], v246 offset:4096\n"
      "v_mfma_f32_16x16x32_bf16 v[68:71], a[84:87], a[32:35], v[68:71]\n"
      "s_add_u32 s62, s62, 0x4000\n"
      "ds_read_b64_tr_b16 a[108:109], v247 offset:0\n"
      "ds_read_b64_tr_b16 a[110:111], v247 offset:4096\n"
      "v_mfma_f32_16x16x32_bf16 v[68:71], a[92:95], a[48:51], v[68:71]\n"
      "s_and_b32 s62, s62, 0x1ffff\n"
      "v_exp_f32 v188, v104\n"
      "v_mfma_f32_16x16x32_bf16 v[76:79], a[68:71], a[4:7], v[164:167]\n"
      "v_readlane_b32 s60, v219, s61\n"
      "v_exp_f32 v189, v105\n"
      "v_mfma_f32_16x16x32_bf16 v[76:79], a[76:79], a[20:23], v[76:79]\n"
      "v_exp_f32 v190, v106\n"
      "v_mfma_f32_16x16x32_bf16 v[76:79], a[84:87], a[36:39], v[76:79]\n"
      "v_exp_f32 v191, v107\n"
      "v_mfma_f32_16x16x32_bf16 v[76:79], a[92:95], a[52:55], v[76:79]\n"
      "v_cvt_pk_bf16_f32 v148, v188, v189\n"
      "v_cvt_pk_bf16_f32 v149, v190, v191\n"
      "v_mfma_f32_16x16x32_bf16 v[84:87], a[68:71], a[8:11], v[168:171]\n"
      "v_exp_f32 v192, v108\n"
      "v_mfma_f32_16x16x32_bf16 v[84:87], a[76:79], a[24:27], v[84:87]\n"
      "v_exp_f32 v193, v109\n"
      "s_waitcnt lgkmcnt(0)\n"
      "v_mfma_f32_16x16x32_bf16 v[84:87], a[84:87], a[40:43], v[84:87]\n"
      "s_sub_u32 s67, s68, s66\n"
      "s_cmp_gt_i32 s67, s70\n"
      "v_cvt_pk_bf16_f32 v150, v192, v193\n"
      "v_mfma_f32_16x16x32_bf16 v[84:87], a[92:95], a[56:59], v[84:87]\n"
      "s_cselect_b32 s69, s77, s86\n"
      "v_exp_f32 v194, v110\n"
      "v_mfma_f32_16x16x32_bf16 v[92:95], a[68:71], a[12:15], v[172:175]\n"
      "s_cselect_b32 s70, 60, s70\n"
      "ds_read_b64_tr_b16 a[112:113], v244 offset:1024\n"
      "ds_read_b64_tr_b16 a[114:115], v244 offset:5120\n"
      "v_mfma_f32_16x16x32_bf16 v[92:95], a[76:79], a[28:31], v[92:95]\n"
      "s_cselect_b32 s90, s80, s90\n"
      "v_exp_f32 v195, v111\n"
      "v_mfma_f32_16x16x32_bf16 v[92:95], a[84:87], a[44:47], v[92:95]\n"
      "s_add_u32 s66, s66, s69\n"
      "ds_read_b64_tr_b16 a[116:117], v245 offset:1024\n"
      "v_cvt_pk_bf16_f32 v151, v194, v195\n"
      "v_mfma_f32_16x16x32_bf16 v[92:95], a[92:95], a[60:63], v[92:95]\n"
      "s_sub_u32 s76, s68, s83\n"
      "v_exp_f32 v188, v112\n"
      "v_mfma_f32_16x16x32_bf16 v[184:187], a[132:135], v[148:151], v[184:187]\n"
      "s_mov_b32 s83, s82\n"
      "ds_read_b64_tr_b16 a[118:119], v245 offset:5120\n"
      "ds_read_b64_tr_b16 a[120:121], v246 offset:1024\n"
      "v_mfma_f32_16x16x32_bf16 v[0:3], a[96:99], v[128:131], v[0:3]\n"
      "s_mov_b32 s82, s68\n"
      "v_exp_f32 v189, v113\n"
      "v_mfma_f32_16x16x32_bf16 v[4:7], a[96:99], v[132:135], v[4:7]\n"
      "s_add_u32 s65, s68, s90\n"
      "ds_read_b64_tr_b16 a[122:123], v246 offset:5120\n"
      "v_cvt_pk_bf16_f32 v152, v188, v189\n"
      "v_mfma_f32_16x16x32_bf16 a[144:147], a[96:99], v[136:139], a[144:147]\n"
      "s_max_u32 s88, s66, s65\n"
      "v_exp_f32 v190, v114\n"
      "v_mfma_f32_16x16x32_bf16 a[148:151], a[96:99], v[140:143], a[148:151]\n"
      "s_getreg_b32 s87, hwreg(HW_REG_IB_STS, 0, 4)\n"
      "ds_read_b64_tr_b16 a[124:125], v247 offset:1024\n"
      "ds_read_b64_tr_b16 a[126:127], v247 offset:5120\n"
      "v_mfma_f32_16x16x32_bf16 a[156:159], a[100:103], v[140:143], a[156:159]\n"
      "s_cmp_lt_i32 s76, 104\n"
      "v_exp_f32 v191, v115\n"
      "v_mfma_f32_16x16x32_bf16 a[152:155], a[100:103], v[136:139], a[152:155]\n"
      "s_cselect_b32 s76, 15, 4\n"
      "v_exp_f32 v192, v116\n"
      "v_mfma_f32_16x16x32_bf16 v[12:15], a[100:103], v[132:135], v[12:15]\n"
      "s_cmp_gt_u32 s87, s76\n"
      "v_exp_f32 v193, v117\n"
      "v_mfma_f32_16x16x32_bf16 v[8:11], a[100:103], v[128:131], v[8:11]\n"
      "s_addc_u32 s89, s89, 0\n"
      "v_cvt_pk_bf16_f32 v153, v190, v191\n"
      "v_cvt_pk_bf16_f32 v154, v192, v193\n"
      "v_mfma_f32_16x16x32_bf16 v[16:19], a[104:107], v[128:131], v[16:19]\n"
      "s_add_u32 s38, s38, s38\n"
      "v_exp_f32 v194, v118\n"
      "v_mfma_f32_16x16x32_bf16 v[20:23], a[104:107], v[132:135], v[20:23]\n"
      "s_memrealtime s[68:69]\n"
      "v_exp_f32 v195, v119\n"
      "v_mfma_f32_16x16x32_bf16 a[160:163], a[104:107], v[136:139], a[160:163]\n"
      "v_exp_f32 v188, v120\n"
      "v_mfma_f32_16x16x32_bf16 a[164:167], a[104:107], v[140:143], a[164:167]\n"
      "s_mov_b32 m0, s62\n"
      "v_exp_f32 v189, v121\n"
      "v_mfma_f32_16x16x32_bf16 a[172:175], a[108:111], v[140:143], a[172:175]\n"
      "buffer_load_dwordx4 v228, s[56:59], s60 offen nt lds\n"
      "v_mfma_f32_16x16x32_bf16 a[168:171], a[108:111], v[136:139], a[168:171]\n"
      "buffer_load_dwordx4 v229, s[56:59], s60 offen offset:1024 nt lds\n"
      "v_mfma_f32_16x16x32_bf16 v[28:31], a[108:111], v[132:135], v[28:31]\n"
      "buffer_load_dwordx4 v230, s[56:59], s60 offen offset:2048 nt lds\n"
      "v_mfma_f32_16x16x32_bf16 v[24:27], a[108:111], v[128:131], v[24:27]\n"
      "buffer_load_dwordx4 v231, s[56:59], s60 offen offset:3072 nt lds\n"
      "s_cbranch_scc1 SP2_%=\n"
      "HD2_%=:\n"
      "s_cmp_lg_u32 s54, 0\n"
      "s_waitcnt vmcnt(12) lgkmcnt(0)\n"
      "s_cbranch_scc1 W2_%=\n"
      "s_waitcnt vmcnt(8)\n"
      "W2_%=:\n"
      "s_sub_u32 s67, s68, s88\n"
      "s_cmp_lt_i32 s67, -8\n"
      "s_cbranch_scc1 GW5_%=\n"
      "GX5_%=:\n"
      "s_barrier\n"
      "v_mfma_f32_16x16x32_bf16 v[32:35], a[112:115], v[128:131], v[32:35]\n"
      "s_memrealtime s[68:69]\n"
      "ds_read_b128 a[64:67], v242 offset:32768\n"
      "ds_read_b128 a[72:75], v243 offset:32768\n"
      "v_mfma_f32_16x16x32_bf16 v[36:39], a[112:115], v[132:135], v[36:39]\n"
      "s_xor_b64 s[56:57], s[56:57], s[32:33]\n"
      "ds_read_b128 a[80:83], v242 offset:33792\n"
      "ds_read_b128 a[88:91], v243 offset:33792\n"
      "v_mfma_f32_16x16x32_bf16 a[176:179], a[112:115], v[136:139], a[176:179]\n"
      "s_add_u32 s61, s61, 1\n"
      "v_exp_f32 v190, v122\n"
      "v_mfma_f32_16x16x32_bf16 a[180:183], a[112:115], v[140:143], a[180:183]\n"
      "s_add_u32 s62, s62, 0x4000\n"
      "v_cvt_pk_bf16_f32 v155, v194, v195\n"
      "v_cvt_pk_bf16_f32 v156, v188, v189\n"
      "v_mfma_f32_16x16x32_bf16 a[188:191], a[116:119], v[140:143], a[188:191]\n"
      "s_and_b32 s62, s62, 0x1ffff\n"
      "v_exp_f32 v191, v123\n"
      "v_mfma_f32_16x16x32_bf16 v[184:187], a[136:139], v[152:155], v[184:187]\n"
      "v_cvt_pk_bf16_f32 v157, v190, v191\n"
      "v_readlane_b32 s60, v219, s61\n"
      "v_mfma_f32_16x16x32_bf16 a[184:187], a[116:119], v[136:139], a[184:187]\n"
      "v_exp_f32 v192, v124\n"
      "v_mfma_f32_16x16x32_bf16 v[44:47], a[116:119], v[132:135], v[44:47]\n"
      "v_exp_f32 v193, v125\n"
      "v_mfma_f32_16x16x32_bf16 v[40:43], a[116:119], v[128:131], v[40:43]\n"
      "v_exp_f32 v194, v126\n"
      "v_mfma_f32_16x16x32_bf16 v[48:51], a[120:123], v[128:131], v[48:51]\n"
      "v_exp_f32 v195, v127\n"
      "v_mfma_f32_16x16x32_bf16 v[52:55], a[120:123], v[132:135], v[52:55]\n"
      "v_cvt_pk_bf16_f32 v158, v192, v193\n"
      "v_cvt_pk_bf16_f32 v159, v194, v195\n"
      "s_waitcnt lgkmcnt(0)\n"
      "v_mfma_f32_16x16x32_bf16 a[192:195], a[120:123], v[136:139], a[192:195]\n"
      "s_sub_u32 s67, s68, s66\n"
      "s_cmp_gt_i32 s67, s70\n"
      "ds_read_b128 a[68:71], v242 offset:36864\n"
      "v_mfma_f32_16x16x32_bf16 v[184:187], a[140:143], v[156:159], v[184:187]\n"
      "s_cselect_b32 s69, s77, s86\n"
      "ds_read_b128 a[76:79], v243 offset:36864\n"
      "ds_read_b128 a[84:87], v242 offset:37888\n"
      "v_mfma_f32_16x16x32_bf16 a[196:199], a[120:123], v[140:143], a[196:199]\n"
      "s_cselect_b32 s70, 60, s70\n"
      "ds_read_b128 a[92:95], v243 offset:37888\n"
      "v_mfma_f32_16x16x32_bf16 a[204:207], a[124:127], v[140:143], a[204:207]\n"
      "s_cselect_b32 s90, s80, s90\n"
      "v_max3_f32 v196, v184, v185, v186\n"
      "v_max_f32 v196, v196, v187\n"
      "v_mfma_f32_16x16x32_bf16 a[200:203], a[124:127], v[136:139], a[200:203]\n"
      "s_add_u32 s66, s66, s69\n"
      "v_cmp_nge_f32 vcc, 0x4b800000, v196\n"
      "v_mfma_f32_16x16x32_bf16 v[60:63], a[124:127], v[132:135], v[60:63]\n"
      "s_sub_u32 s76, s68, s83\n"
      "v_mfma_f32_16x16x32_bf16 v[56:59], a[124:127], v[128:131], v[56:59]\n"
      "s_mov_b32 s83, s82\n"
      "s_cbranch_vccnz FS1_%=\n"
      "FR1_%=:\n"
      "v_mfma_f32_16x16x32_bf16 v[96:99], a[64:67], a[0:3], v[160:163]\n"
      "s_mov_b32 s82, s68\n"
      "v_exp_f32 v188, v64\n"
      "v_mfma_f32_16x16x32_bf16 v[96:99], a[72:75], a[16:19], v[96:99]\n"
      "s_add_u32 s65, s68, s90\n"
      "v_add_f32 v180, v180, v184\n"
      "v_add_f32 v181, v181, v185\n"
      "v_mfma_f32_16x16x32_bf16 v[96:99], a[80:83], a[32:35], v[96:99]\n"
      "s_max_u32 s88, s66, s65\n"
      "v_exp_f32 v189, v65\n"
      "v_mfma_f32_16x16x32_bf16 v[96:99], a[88:91], a[48:51], v[96:99]\n"
      "s_getreg_b32 s87, hwreg(HW_REG_IB_STS, 0, 4)\n"
      "v_add_f32 v182, v182, v186\n"
      "v_add_f32 v183, v183, v187\n"
      "v_mfma_f32_16x16x32_bf16 v[104:107], a[64:67], a[4:7], v[164:167]\n"
      "s_cmp_lt_i32 s76, 104\n"
      "v_exp_f32 v190, v66\n"
      "v_mfma_f32_16x16x32_bf16 v[104:107], a[72:75], a[20:23], v[104:107]\n"
      "s_cselect_b32 s76, 15, 4\n"
      "v_exp_f32 v191, v67\n"
      "v_mfma_f32_16x16x32_bf16 v[104:107], a[80:83], a[36:39], v[104:107]\n"
      "s_cmp_gt_u32 s87, s76\n"
      "v_cvt_pk_bf16_f32 v128, v188, v189\n"
      "v_cvt_pk_bf16_f32 v129, v190, v191\n"
      "v_mfma_f32_16x16x32_bf16 v[104:107], a[88:91], a[52:55], v[104:107]\n"
      "s_addc_u32 s89, s89, 0\n"
      "v_exp_f32 v192, v68\n"
      "v_mfma_f32_16x16x32_bf16 v[112:115], a[64:67], a[8:11], v[168:171]\n"
      "s_add_u32 s39, s39, s39\n"
      "v_exp_f32 v193, v69\n"
      "v_mfma_f32_16x16x32_bf16 v[112:115], a[72:75], a[24:27], v[112:115]\n"
      "s_memrealtime s[68:69]\n"
      "v_exp_f32 v194, v70\n"
      "v_mfma_f32_16x16x32_bf16 v[112:115], a[80:83], a[40:43], v[112:115]\n"
      "v_exp_f32 v195, v71\n"
      "v_mfma_f32_16x16x32_bf16 v[112:115], a[88:91], a[56:59], v[112:115]\n"
      "s_mov_b32 m0, s62\n"
      "v_cvt_pk_bf16_f32 v130, v192, v193\n"
      "v_cvt_pk_bf16_f32 v131, v194, v195\n"
      "v_mfma_f32_16x16x32_bf16 v[120:123], a[64:67], a[12:15], v[172:175]\n"
      "buffer_load_dwordx4 v228, s[56:59], s60 offen nt lds\n"
      "v_mfma_f32_16x16x32_bf16 v[184:187], a[128:131], v[128:131], 0\n"
      "buffer_load_dwordx4 v229, s[56:59], s60 offen offset:1024 nt lds\n"
      "v_mfma_f32_16x16x32_bf16 v[120:123], a[72:75], a[28:31], v[120:123]\n"
      "buffer_load_dwordx4 v230, s[56:59], s60 offen offset:2048 nt lds\n"
      "v_mfma_f32_16x16x32_bf16 v[120:123], a[80:83], a[44:47], v[120:123]\n"
      "buffer_load_dwordx4 v231, s[56:59], s60 offen offset:3072 nt lds\n"
      "s_cbranch_scc1 SP3_%=\n"
      "HD3_%=:\n"
      "s_cmp_lg_u32 s54, 0\n"
      "s_waitcnt vmcnt(12) lgkmcnt(0)\n"
      "s_cbranch_scc1 W3_%=\n"
      "s_waitcnt vmcnt(8)\n"
      "W3_%=:\n"
      "s_sub_u32 s67, s68, s88\n"
      "s_cmp_lt_i32 s67, -8\n"
      "s_cbranch_scc1 GW6_%=\n"
      "GX6_%=:\n"
      "s_barrier\n"
      "v_mfma_f32_16x16x32_bf16 v[120:123], a[88:91], a[60:63], v[120:123]\n"
      "s_memrealtime s[68:69]\n"
      "ds_read_b64_tr_b16 a[96:97], v244 offset:32768\n"
      "ds_read_b64_tr_b16 a[98:99], v244 offset:36864\n"
      "v_mfma_f32_16x16x32_bf16 v[100:103], a[68:71], a[0:3], v[160:163]\n"
      "s_xor_b64 s[56:57], s[56:57], s[32:33]\n"
      "ds_read_b64_tr_b16 a[100:101], v245 offset:32768\n"
      "ds_read_b64_tr_b16 a[102:103], v245 offset:36864\n"
      "v_mfma_f32_16x16x32_bf16 v[100:103], a[76:79], a[16:19], v[100:103]\n"
      "s_add_u32 s61, s61, 1\n"
      "ds_read_b64_tr_b16 a[104:105], v246 offset:32768\n"
      "ds_read_b64_tr_b16 a[106:107], v246 offset:36864\n"
      "v_mfma_f32_16x16x32_bf16 v[100:103], a[84:87], a[32:35], v[100:103]\n"
      "s_add_u32 s62, s62, 0x4000\n"
      "ds_read_b64_tr_b16 a[108:109], v247 offset:32768\n"
      "ds_read_b64_tr_b16 a[110:111], v247 offset:36864\n"
      "v_mfma_f32_16x16x32_bf16 v[100:103], a[92:95], a[48:51], v[100:103]\n"
      "s_and_b32 s62, s62, 0x1ffff\n"
      "v_exp_f32 v188, v72\n"
      "v_mfma_f32_16x16x32_bf16 v[108:111], a[68:71], a[4:7], v[164:167]\n"
      "v_readlane_b32 s60, v219, s61\n"
      "v_exp_f32 v189, v73\n"
      "v_mfma_f32_16x16x32_bf16 v[108:111], a[76:79], a[20:23], v[108:111]\n"
      "v_xor_b32 v242, 0x10000, v242\n"
      "v_xor_b32 v243, 0x10000, v243\n"
      "v_mfma_f32_16x16x32_bf16 v[108:111], a[84:87], a[36:39], v[108:111]\n"
      "v_exp_f32 v190, v74\n"
      "v_mfma_f32_16x16x32_bf16 v[108:111], a[92:95], a[52:55], v[108:111]\n"
      "v_exp_f32 v191, v75\n"
      "v_mfma_f32_16x16x32_bf16 v[116:119], a[68:71], a[8:11], v[168:171]\n"
      "v_cvt_pk_bf16_f32 v132, v188, v189\n"
      "v_cvt_pk_bf16_f32 v133, v190, v191\n"
      "v_mfma_f32_16x16x32_bf16 v[116:119], a[76:79], a[24:27], v[116:119]\n"
      "v_exp_f32 v192, v76\n"
      "s_waitcnt lgkmcnt(0)\n"
      "v_mfma_f32_16x16x32_bf16 v[116:119], a[84:87], a[40:43], v[116:119]\n"
      "s_sub_u32 s67, s68, s66\n"
      "s_cmp_gt_i32 s67, s70\n"
      "ds_read_b64_tr_b16 a[112:113], v244 offset:33792\n"
      "v_mfma_f32_16x16x32_bf16 v[116:119], a[92:95], a[56:59], v[116:119]\n"
      "s_cselect_b32 s69, s77, s86\n"
      "v_exp_f32 v193, v77\n"
      "v_mfma_f32_16x16x32_bf16 v[124:127], a[68:71], a[12:15], v[172:175]\n"
      "s_cselect_b32 s70, 60, s70\n"
      "ds_read_b64_tr_b16 a[114:115], v244 offset:37888\n"
      "v_cvt_pk_bf16_f32 v134, v192, v193\n"
      "v_mfma_f32_16x16x32_bf16 v[124:127], a[76:79], a[28:31], v[124:127]\n"
      "s_cselect_b32 s90, s80, s90\n"
      "v_exp_f32 v194, v78\n"
      "v_mfma_f32_16x16x32_bf16 v[124:127], a[84:87], a[44:47], v[124:127]\n"
      "s_add_u32 s66, s66, s69\n"
      "ds_read_b64_tr_b16 a[116:117], v245 offset:33792\n"
      "ds_read_b64_tr_b16 a[118:119], v245 offset:37888\n"
      "v_mfma_f32_16x16x32_bf16 v[124:127], a[92:95], a[60:63], v[124:127]\n"
      "s_sub_u32 s76, s68, s83\n"
      "v_exp_f32 v195, v79\n"
      "v_mfma_f32_16x16x32_bf16 v[0:3], a[96:99], v[144:147], v[0:3]\n"
      "s_mov_b32 s83, s82\n"
      "ds_read_b64_tr_b16 a[120:121], v246 offset:33792\n"
      "v_cvt_pk_bf16_f32 v135, v194, v195\n"
      "v_mfma_f32_16x16x32_bf16 v[4:7], a[96:99], v[148:151], v[4:7]\n"
      "s_mov_b32 s82, s68\n"
      "v_exp_f32 v188, v80\n"
      "v_mfma_f32_16x16x32_bf16 v[184:187], a[132:135], v[132:135], v[184:187]\n"
      "s_add_u32 s65, s68, s90\n"
      "ds_read_b64_tr_b16 a[122:123], v246 offset:37888\n"
      "ds_read_b64_tr_b16 a[124:125], v247 offset:33792\n"
      "v_mfma_f32_16x16x32_bf16 a[144:147], a[96:99], v[152:155], a[144:147]\n"
      "s_max_u32 s88, s66, s65\n"
      "v_exp_f32 v189, v81\n"
      "v_mfma_f32_16x16x32_bf16 a[148:151], a[96:99], v[156:159], a[148:151]\n"
      "s_getreg_b32 s87, hwreg(HW_REG_IB_STS, 0, 4)\n"
      "ds_read_b64_tr_b16 a[126:127], v247 offset:37888\n"
      "v_cvt_pk_bf16_f32 v136, v188, v189\n"
      "v_mfma_f32_16x16x32_bf16 a[156:159], a[100:103], v[156:159], a[156:159]\n"
      "s_cmp_lt_i32 s76, 104\n"
      "v_exp_f32 v190, v82\n"
      "v_mfma_f32_16x16x32_bf16 a[152:155], a[100:103], v[152:155], a[152:155]\n"
      "s_cselect_b32 s76, 15, 4\n"
      "v_exp_f32 v191, v83\n"
      "v_mfma_f32_16x16x32_bf16 v[12:15], a[100:103], v[148:151], v[12:15]\n"
      "s_cmp_gt_u32 s87, s76\n"
      "v_exp_f32 v192, v84\n"
      "v_mfma_f32_16x16x32_bf16 v[8:11], a[100:103], v[144:147], v[8:11]\n"
      "s_addc_u32 s89, s89, 0\n"
      "v_exp_f32 v193, v85\n"
      "v_mfma_f32_16x16x32_bf16 v[16:19], a[104:107], v[144:147], v[16:19]\n"
      "s_add_u32 s38, s38, s38\n"
      "v_cvt_pk_bf16_f32 v137, v190, v191\n"
      "v_cvt_pk_bf16_f32 v138, v192, v193\n"
      "v_mfma_f32_16x16x32_bf16 v[20:23], a[104:107], v[148:151], v[20:23]\n"
      "s_memrealtime s[68:69]\n"
      "v_exp_f32 v194, v86\n"
      "v_mfma_f32_16x16x32_bf16 a[160:163], a[104:107], v[152:155], a[160:163]\n"
      "v_exp_f32 v195, v87\n"
      "v_mfma_f32_16x16x32_bf16 a[164:167], a[104:107], v[156:159], a[164:167]\n"
      "s_mov_b32 m0, s62\n"
      "v_exp_f32 v188, v88\n"
      "v_mfma_f32_16x16x32_bf16 a[172:175], a[108:111], v[156:159], a[172:175]\n"
      "buffer_load_dwordx4 v228, s[56:59], s60 offen nt lds\n"
      "v_mfma_f32_16x16x32_bf16 a[168:171], a[108:111], v[152:155], a[168:171]\n"
      "buffer_load_dwordx4 v229, s[56:59], s60 offen offset:1024 nt lds\n"
      "v_mfma_f32_16x16x32_bf16 v[28:31], a[108:111], v[148:151], v[28:31]\n"
      "buffer_load_dwordx4 v230, s[56:59], s60 offen offset:2048 nt lds\n"
      "v_mfma_f32_16x16x32_bf16 v[24:27], a[108:111], v[144:147], v[24:27]\n"
      "buffer_load_dwordx4 v231, s[56:59], s60 offen offset:3072 nt lds\n"
      "HD0_%=:\n"
      "s_cmp_lg_u32 s54, 0\n"
      "s_waitcnt vmcnt(12) lgkmcnt(0)\n"
      "s_cbranch_scc1 W4_%=\n"
      "s_waitcnt vmcnt(8)\n"
      "W4_%=:\n"
      "s_sub_u32 s67, s68, s88\n"
      "s_cmp_lt_i32 s67, -8\n"
      "s_cbranch_scc1 GW7_%=\n"
      "GX7_%=:\n"
      "s_barrier\n"
      "v_mfma_f32_16x16x32_bf16 v[32:35], a[112:115], v[144:147], v[32:35]\n"
      "s_memrealtime s[68:69]\n"
      "ds_read_b128 a[64:67], v242 offset:0\n"
      "ds_read_b128 a[72:75], v243 offset:0\n"
      "v_mfma_f32_16x16x32_bf16 v[36:39], a[112:115], v[148:151], v[36:39]\n"
      "s_xor_b64 s[56:57], s[56:57], s[32:33]\n"
      "ds_read_b128 a[80:83], v242 offset:1024\n"
      "ds_read_b128 a[88:91], v243 offset:1024\n"
      "v_mfma_f32_16x16x32_bf16 a[176:179], a[112:115], v[152:155], a[176:179]\n"
      "s_add_u32 s61, s61, 1\n"
      "v_exp_f32 v189, v89\n"
      "v_mfma_f32_16x16x32_bf16 a[180:183], a[112:115], v[156:159], a[180:183]\n"
      "s_add_u32 s62, s62, 0x4000\n"
      "v_cvt_pk_bf16_f32 v139, v194, v195\n"
      "v_cvt_pk_bf16_f32 v140, v188, v189\n"
      "v_mfma_f32_16x16x32_bf16 a[188:191], a[116:119], v[156:159], a[188:191]\n"
      "s_and_b32 s62, s62, 0x1ffff\n"
      "v_exp_f32 v190, v90\n"
      "v_mfma_f32_16x16x32_bf16 v[184:187], a[136:139], v[136:139], v[184:187]\n"
      "v_readlane_b32 s60, v219, s61\n"
      "v_exp_f32 v191, v91\n"
      "v_mfma_f32_16x16x32_bf16 a[184:187], a[116:119], v[152:155], a[184:187]\n"
      "v_exp_f32 v192, v92\n"
      "v_mfma_f32_16x16x32_bf16 v[44:47], a[116:119], v[148:151], v[44:47]\n"
      "v_exp_f32 v193, v93\n"
      "v_mfma_f32_16x16x32_bf16 v[40:43], a[116:119], v[144:147], v[40:43]\n"
      "v_cvt_pk_bf16_f32 v141, v190, v191\n"
      "v_cvt_pk_bf16_f32 v142, v192, v193\n"
      "v_mfma_f32_16x16x32_bf16 v[48:51], a[120:123], v[144:147], v[48:51]\n"
      "v_exp_f32 v194, v94\n"
      "v_mfma_f32_16x16x32_bf16 v[52:55], a[120:123], v[148:151], v[52:55]\n"
      "v_exp_f32 v195, v95\n"
      "s_waitcnt lgkmcnt(0)\n"
      "v_mfma_f32_16x16x32_bf16 a[192:195], a[120:123], v[152:155], a[192:195]\n"
      "s_sub_u32 s67, s68, s66\n"
      "s_cmp_gt_i32 s67, s70\n"
      "v_cvt_pk_bf16_f32 v143, v194, v195\n"
      "v_mfma_f32_16x16x32_bf16 a[196:199], a[120:123], v[156:159], a[196:199]\n"
      "s_cselect_b32 s69, s77, s86\n"
      "ds_read_b128 a[68:71], v242 offset:4096\n"
      "ds_read_b128 a[76:79], v243 offset:4096\n"
      "v_mfma_f32_16x16x32_bf16 v[184:187], a[140:143], v[140:143], v[184:187]\n"
      "s_cselect_b32 s70, 60, s70\n"
      "ds_read_b128 a[84:87], v242 offset:5120\n"
      "ds_read_b128 a[92:95], v243 offset:5120\n"
      "v_mfma_f32_16x16x32_bf16 a[204:207], a[124:127], v[156:159], a[204:207]\n"
      "s_cselect_b32 s90, s80, s90\n"
      "v_xor_b32 v244, 0x10000, v244\n"
      "v_xor_b32 v245, 0x10000, v245\n"
      "v_mfma_f32_16x16x32_bf16 a[200:203], a[124:127], v[152:155], a[200:203]\n"
      "s_add_u32 s66, s66, s69\n"
      "v_xor_b32 v246, 0x10000, v246\n"
      "v_xor_b32 v247, 0x10000, v247\n"
      "v_mfma_f32_16x16x32_bf16 v[60:63], a[124:127], v[148:151], v[60:63]\n"
      "s_sub_u32 s76, s68, s83\n"
      "v_max3_f32 v196, v184, v185, v186\n"
      "v_max_f32 v196, v196, v187\n"
      "v_mfma_f32_16x16x32_bf16 v[56:59], a[124:127], v[144:147], v[56:59]\n"
      "s_mov_b32 s83, s82\n"
      "v_cmp_nge_f32 vcc, 0x4b800000, v196\n"
      "s_cbranch_vccnz FS2_%=\n"
      "FR2_%=:\n"
      "s_branch LOOP_%=\n"
      "SP1_%=:\n"
      "s_ff1_i32_b32 s64, s39\n"
      "s_lshl1_add_u32 s64, s64, 1\n"
      "s_bitcmp1_b64 s[52:53], s64\n"
      "s_cbranch_scc0 SH1_%=\n"
      "s_swappc_b64 s[84:85], s[84:85]\n"
      "SH1_%=:\n"
      "s_cmp_lg_u32 s64, 7\n"
      "s_cbranch_scc1 SK_%=\n"
      "v_accvgpr_write_b32 a208, v128\n"
      "v_accvgpr_write_b32 a209, v129\n"
      "v_accvgpr_write_b32 a210, v130\n"
      "v_accvgpr_write_b32 a211, v131\n"
      "v_accvgpr_write_b32 a212, v132\n"
      "v_accvgpr_write_b32 a213, v133\n"
      "v_accvgpr_write_b32 a214, v134\n"
      "v_accvgpr_write_b32 a215, v135\n"
      "v_accvgpr_write_b32 a216, v136\n"
      "v_accvgpr_write_b32 a217, v137\n"
      "v_accvgpr_write_b32 a218, v138\n"
      "v_accvgpr_write_b32 a219, v139\n"
      "v_accvgpr_write_b32 a220, v140\n"
      "v_accvgpr_write_b32 a221, v141\n"
      "v_accvgpr_write_b32 a222, v142\n"
      "v_accvgpr_write_b32 a223, v143\n"
      "SK_%=:\n"
      "s_cmp_lt_u32 s64, 63\n"
      "s_cbranch_scc1 HD1_%=\n"
      "s_xor_b64 s[56:57], s[56:57], s[32:33]\n"
      "s_add_u32 s61, s61, 1\n"
      "s_add_u32 s62, s62, 0x4000\n"
      "s_and_b32 s62, s62, 0x1ffff\n"
      "v_readlane_b32 s74, v219, 60\n"
      "v_readlane_b32 s76, v219, 62\n"
      "s_mov_b32 s61, 0\n"
      "s_add_u32 s74, s74, 0x400\n"
      "v_add_u32 v228, s74, v229\n"
      "v_add_u32 v230, s74, v231\n"
      "v_add_u32 v229, s76, v229\n"
      "v_add_u32 v231, s76, v231\n"
      "s_branch HD1_%=\n"
      "SP2_%=:\n"
      "s_cmp_eq_u32 s38, 0\n"
      "s_cbranch_scc1 L62_%=\n"
      "s_ff1_i32_b32 s64, s38\n"
      "s_lshl1_add_u32 s64, s64, 0\n"
      "s_bitcmp1_b64 s[52:53], s64\n"
      "s_cbranch_scc0 SH2_%=\n"
      "s_swappc_b64 s[84:85], s[84:85]\n"
      "SH2_%=:\n"
      "s_branch HD2_%=\n"
      "L62_%=:\n"
      "s_xor_b64 s[56:57], s[56:57], s[32:33]\n"
      "s_mov_b32 s61, 0\n"
      "v_xor_b32 v228, 0x80, v228\n"
      "v_xor_b32 v229, 0x80, v229\n"
      "v_xor_b32 v230, 0x80, v230\n"
      "v_xor_b32 v231, 0x80, v231\n"
      "s_mov_b32 s38, 0x80000000\n"
      "s_branch HD2_%=\n"
      "SP3_%=:\n"
      "s_cmp_eq_u32 s39, 0\n"
      "s_cbranch_scc1 NH_%=\n"
      "s_ff1_i32_b32 s64, s39\n"
      "s_lshl1_add_u32 s64, s64, 1\n"
      "s_bitcmp1_b64 s[52:53], s64\n"
      "s_cbranch_scc0 SH3_%=\n"
      "s_swappc_b64 s[84:85], s[84:85]\n"
      "SH3_%=:\n"
      "s_bitcmp1_b64 s[34:35], s64\n"
      "s_cbranch_scc0 HD3_%=\n"
      "NH_%=:\n"
      "s_cmp_lg_u32 s54, 0\n"
      "s_waitcnt vmcnt(12) lgkmcnt(0)\n"
      "s_cbranch_scc1 W5_%=\n"
      "s_waitcnt vmcnt(8)\n"
      "W5_%=:\n"
      "s_barrier\n"
      "v_mfma_f32_16x16x32_bf16 v[120:123], a[88:91], a[60:63], v[120:123]\n"
      "s_sub_u32 s54, s54, 1\n"
      "ds_read_b64_tr_b16 a[96:97], v244 offset:32768\n"
      "ds_read_b64_tr_b16 a[98:99], v244 offset:36864\n"
      "v_mfma_f32_16x16x32_bf16 v[100:103], a[68:71], a[0:3], v[160:163]\n"
      "ds_read_b64_tr_b16 a[100:101], v245 offset:32768\n"
      "ds_read_b64_tr_b16 a[102:103], v245 offset:36864\n"
      "v_mfma_f32_16x16x32_bf16 v[100:103], a[76:79], a[16:19], v[100:103]\n"
      "ds_read_b64_tr_b16 a[104:105], v246 offset:32768\n"
      "ds_read_b64_tr_b16 a[106:107], v246 offset:36864\n"
      "v_mfma_f32_16x16x32_bf16 v[100:103], a[84:87], a[32:35], v[100:103]\n"
      "ds_read_b64_tr_b16 a[108:109], v247 offset:32768\n"
      "ds_read_b64_tr_b16 a[110:111], v247 offset:36864\n"
      "v_mfma_f32_16x16x32_bf16 v[100:103], a[92:95], a[48:51], v[100:103]\n"
      "v_exp_f32 v188, v72\n"
      "v_mfma_f32_16x16x32_bf16 v[108:111], a[68:71], a[4:7], v[164:167]\n"
      "v_exp_f32 v189, v73\n"
      "v_mfma_f32_16x16x32_bf16 v[108:111], a[76:79], a[20:23], v[108:111]\n"
      "v_xor_b32 v242, 0x10000, v242\n"
      "v_xor_b32 v243, 0x10000, v243\n"
      "v_mfma_f32_16x16x32_bf16 v[108:111], a[84:87], a[36:39], v[108:111]\n"
      "v_exp_f32 v190, v74\n"
      "v_mfma_f32_16x16x32_bf16 v[108:111], a[92:95], a[52:55], v[108:111]\n"
      "v_exp_f32 v191, v75\n"
      "v_mfma_f32_16x16x32_bf16 v[116:119], a[68:71], a[8:11], v[168:171]\n"
      "v_cvt_pk_bf16_f32 v132, v188, v189\n"
      "v_cvt_pk_bf16_f32 v133, v190, v191\n"
      "v_mfma_f32_16x16x32_bf16 v[116:119], a[76:79], a[24:27], v[116:119]\n"
      "v_exp_f32 v192, v76\n"
      "s_waitcnt lgkmcnt(0)\n"
      "v_mfma_f32_16x16x32_bf16 v[116:119], a[84:87], a[40:43], v[116:119]\n"
      "ds_read_b64_tr_b16 a[112:113], v244 offset:33792\n"
      "v_mfma_f32_16x16x32_bf16 v[116:119], a[92:95], a[56:59], v[116:119]\n"
      "v_exp_f32 v193, v77\n"
      "v_mfma_f32_16x16x32_bf16 v[124:127], a[68:71], a[12:15], v[172:175]\n"
      "ds_read_b64_tr_b16 a[114:115], v244 offset:37888\n"
      "v_cvt_pk_bf16_f32 v134, v192, v193\n"
      "v_mfma_f32_16x16x32_bf16 v[124:127], a[76:79], a[28:31], v[124:127]\n"
      "v_exp_f32 v194, v78\n"
      "v_mfma_f32_16x16x32_bf16 v[124:127], a[84:87], a[44:47], v[124:127]\n"
      "ds_read_b64_tr_b16 a[116:117], v245 offset:33792\n"
      "ds_read_b64_tr_b16 a[118:119], v245 offset:37888\n"
      "v_mfma_f32_16x16x32_bf16 v[124:127], a[92:95], a[60:63], v[124:127]\n"
      "v_exp_f32 v195, v79\n"
      "v_mfma_f32_16x16x32_bf16 v[0:3], a[96:99], v[144:147], v[0:3]\n"
      "ds_read_b64_tr_b16 a[120:121], v246 offset:33792\n"
      "v_cvt_pk_bf16_f32 v135, v194, v195\n"
      "v_mfma_f32_16x16x32_bf16 v[4:7], a[96:99], v[148:151], v[4:7]\n"
      "v_exp_f32 v188, v80\n"
      "v_mfma_f32_16x16x32_bf16 v[184:187], a[132:135], v[132:135], v[184:187]\n"
      "ds_read_b64_tr_b16 a[122:123], v246 offset:37888\n"
      "ds_read_b64_tr_b16 a[124:125], v247 offset:33792\n"
      "v_mfma_f32_16x16x32_bf16 a[144:147], a[96:99], v[152:155], a[144:147]\n"
      "v_exp_f32 v189, v81\n"
      "v_mfma_f32_16x16x32_bf16 a[148:151], a[96:99], v[156:159], a[148:151]\n"
      "ds_read_b64_tr_b16 a[126:127], v247 offset:37888\n"
      "v_cvt_pk_bf16_f32 v136, v188, v189\n"
      "v_mfma_f32_16x16x32_bf16 a[156:159], a[100:103], v[156:159], a[156:159]\n"
      "v_exp_f32 v190, v82\n"
      "v_mfma_f32_16x16x32_bf16 a[152:155], a[100:103], v[152:155], a[152:155]\n"
      "v_exp_f32 v191, v83\n"
      "v_mfma_f32_16x16x32_bf16 v[12:15], a[100:103], v[148:151], v[12:15]\n"
      "v_exp_f32 v192, v84\n"
      "v_mfma_f32_16x16x32_bf16 v[8:11], a[100:103], v[144:147], v[8:11]\n"
      "v_exp_f32 v193, v85\n"
      "v_mfma_f32_16x16x32_bf16 v[16:19], a[104:107], v[144:147], v[16:19]\n"
      "s_add_u32 s38, s38, s38\n"
      "v_cvt_pk_bf16_f32 v137, v190, v191\n"
      "v_cvt_pk_bf16_f32 v138, v192, v193\n"
      "v_mfma_f32_16x16x32_bf16 v[20:23], a[104:107], v[148:151], v[20:23]\n"
      "s_memrealtime s[68:69]\n"
      "v_exp_f32 v194, v86\n"
      "v_mfma_f32_16x16x32_bf16 a[160:163], a[104:107], v[152:155], a[160:163]\n"
      "v_exp_f32 v195, v87\n"
      "v_mfma_f32_16x16x32_bf16 a[164:167], a[104:107], v[156:159], a[164:167]\n"
      "v_exp_f32 v188, v88\n"
      "v_mfma_f32_16x16x32_bf16 a[172:175], a[108:111], v[156:159], a[172:175]\n"
      "s_nop 0\n"
      "v_mfma_f32_16x16x32_bf16 a[168:171], a[108:111], v[152:155], a[168:171]\n"
      "s_nop 0\n"
      "v_mfma_f32_16x16x32_bf16 v[28:31], a[108:111], v[148:151], v[28:31]\n"
      "s_nop 0\n"
      "v_mfma_f32_16x16x32_bf16 v[24:27], a[108:111], v[144:147], v[24:27]\n"
      "s_nop 0\n"
      "s_cbranch_scc0 MH_%=\n"
      "END_%=:\n"
      "s_add_u32 m0, s62, 0x4000\n"
      "s_waitcnt lgkmcnt(0)\n"
      "buffer_load_dwordx4 v248, s[56:59], 0 offen nt lds\n"
      "buffer_load_dwordx4 v249, s[56:59], 0 offen offset:2048 nt lds\n"
      "v_mfma_f32_16x16x32_bf16 v[32:35], a[112:115], v[144:147], v[32:35]\n"
      "v_cvt_pk_bf16_f32 v139, v194, v195\n"
      "v_mov_b32 v236, s68\n"
      "v_mfma_f32_16x16x32_bf16 v[36:39], a[112:115], v[148:151], v[36:39]\n"
      "s_mov_b64 exec, 1\n"
      "global_store_dwordx2 v238, v[236:237], s[46:47] offset:8 sc1\n"
      "s_mov_b64 exec, -1\n"
      "v_mfma_f32_16x16x32_bf16 v[184:187], a[136:139], v[136:139], v[184:187]\n"
      "s_mov_b32 s82, s68\n"
      "v_exp_f32 v189, v89\n"
      "v_mfma_f32_16x16x32_bf16 a[176:179], a[112:115], v[152:155], a[176:179]\n"
      "v_exp_f32 v190, v90\n"
      "v_mfma_f32_16x16x32_bf16 a[180:183], a[112:115], v[156:159], a[180:183]\n"
      "v_exp_f32 v191, v91\n"
      "v_mfma_f32_16x16x32_bf16 a[188:191], a[116:119], v[156:159], a[188:191]\n"
      "v_cvt_pk_bf16_f32 v140, v188, v189\n"
      "v_cvt_pk_bf16_f32 v141, v190, v191\n"
      "s_waitcnt vmcnt(7)\n"
      "s_barrier\n"
      "v_mfma_f32_16x16x32_bf16 a[184:187], a[116:119], v[152:155], a[184:187]\n"
      "ds_read_b64_tr_b16 a[80:81], v224 offset:0\n"
      "ds_read_b64_tr_b16 a[82:83], v224 offset:4096\n"
      "v_mfma_f32_16x16x32_bf16 v[44:47], a[116:119], v[148:151], v[44:47]\n"
      "ds_read_b64_tr_b16 a[84:85], v225 offset:0\n"
      "ds_read_b64_tr_b16 a[86:87], v225 offset:4096\n"
      "v_mfma_f32_16x16x32_bf16 v[40:43], a[116:119], v[144:147], v[40:43]\n"
      "s_mov_b32 s74, 0xffff0000\n"
      "ds_read_b64_tr_b16 a[88:89], v226 offset:0\n"
      "ds_read_b64_tr_b16 a[90:91], v226 offset:4096\n"
      "v_mfma_f32_16x16x32_bf16 v[48:51], a[120:123], v[144:147], v[48:51]\n"
      "s_mov_b32 s75, 0xffff0000\n"
      "ds_read_b64_tr_b16 a[92:93], v227 offset:0\n"
      "ds_read_b64_tr_b16 a[94:95], v227 offset:4096\n"
      "v_mfma_f32_16x16x32_bf16 v[52:55], a[120:123], v[148:151], v[52:55]\n"
      "s_mov_b32 s94, 0\n"
      "v_exp_f32 v192, v92\n"
      "v_mfma_f32_16x16x32_bf16 a[192:195], a[120:123], v[152:155], a[192:195]\n"
      "s_mov_b32 s95, -1\n"
      "ds_read_b64_tr_b16 a[16:17], v224 offset:1024\n"
      "ds_read_b64_tr_b16 a[18:19], v224 offset:5120\n"
      "v_mfma_f32_16x16x32_bf16 a[196:199], a[120:123], v[156:159], a[196:199]\n"
      "v_exp_f32 v193, v93\n"
      "v_mfma_f32_16x16x32_bf16 a[204:207], a[124:127], v[156:159], a[204:207]\n"
      "ds_read_b64_tr_b16 a[20:21], v225 offset:1024\n"
      "v_cvt_pk_bf16_f32 v142, v192, v193\n"
      "v_mfma_f32_16x16x32_bf16 a[200:203], a[124:127], v[152:155], a[200:203]\n"
      "v_exp_f32 v194, v94\n"
      "v_mfma_f32_16x16x32_bf16 v[60:63], a[124:127], v[148:151], v[60:63]\n"
      "ds_read_b64_tr_b16 a[22:23], v225 offset:5120\n"
      "ds_read_b64_tr_b16 a[24:25], v226 offset:1024\n"
      "v_mfma_f32_16x16x32_bf16 v[56:59], a[124:127], v[144:147], v[56:59]\n"
      "v_exp_f32 v195, v95\n"
      "ds_read_b64_tr_b16 a[26:27], v226 offset:5120\n"
      "v_cvt_pk_bf16_f32 v143, v194, v195\n"
      "s_nop 1\n"
      "v_mfma_f32_16x16x32_bf16 v[184:187], a[140:143], v[140:143], v[184:187]\n"
      "ds_read_b64_tr_b16 a[28:29], v227 offset:1024\n"
      "ds_read_b64_tr_b16 a[30:31], v227 offset:5120\n"
      "ds_read_b64_tr_b16 a[0:1], v220 offset:1024\n"
      "ds_read_b64_tr_b16 a[2:3], v220 offset:5120\n"
      "ds_read_b64_tr_b16 a[4:5], v221 offset:1024\n"
      "ds_read_b64_tr_b16 a[6:7], v221 offset:5120\n"
      "ds_read_b64_tr_b16 a[8:9], v222 offset:1024\n"
      "ds_read_b64_tr_b16 a[10:11], v222 offset:5120\n"
      "ds_read_b64_tr_b16 a[12:13], v223 offset:1024\n"
      "ds_read_b64_tr_b16 a[14:15], v223 offset:5120\n"
      "v_max3_f32 v196, v184, v185, v186\n"
      "v_max_f32 v196, v196, v187\n"
      "v_cmp_nge_f32 vcc, 0x4b800000, v196\n"
      "s_cbranch_vccnz FS3_%=\n"
      "FR3_%=:\n"
      "s_waitcnt lgkmcnt(15)\n"
      "v_mfma_f32_16x16x32_bf16 a[176:179], a[80:83], v[136:139], a[176:179]\n"
      "v_add_f32 v180, v180, v184\n"
      "v_add_f32 v181, v181, v185\n"
      "v_mfma_f32_16x16x32_bf16 a[180:183], a[80:83], v[140:143], a[180:183]\n"
      "v_add_f32 v182, v182, v186\n"
      "v_add_f32 v183, v183, v187\n"
      "v_mfma_f32_16x16x32_bf16 a[184:187], a[84:87], v[136:139], a[184:187]\n"
      "v_exp_f32 v188, v112\n"
      "v_mfma_f32_16x16x32_bf16 a[188:191], a[84:87], v[140:143], a[188:191]\n"
      "ds_write_b128 v232, a[176:179] offset:0\n"
      "v_exp_f32 v189, v113\n"
      "v_mfma_f32_16x16x32_bf16 a[192:195], a[88:91], v[136:139], a[192:195]\n"
      "v_exp_f32 v190, v114\n"
      "v_mfma_f32_16x16x32_bf16 a[196:199], a[88:91], v[140:143], a[196:199]\n"
      "v_exp_f32 v191, v115\n"
      "v_mfma_f32_16x16x32_bf16 a[200:203], a[92:95], v[136:139], a[200:203]\n"
      "ds_write_b128 v232, a[180:183] offset:1024\n"
      "v_exp_f32 v192, v116\n"
      "v_mfma_f32_16x16x32_bf16 a[204:207], a[92:95], v[140:143], a[204:207]\n"
      "v_exp_f32 v193, v117\n"
      "v_exp_f32 v194, v118\n"
      "v_cvt_pk_bf16_f32 v152, v188, v189\n"
      "v_exp_f32 v195, v119\n"
      "v_cvt_pk_bf16_f32 v153, v190, v191\n"
      "ds_write_b128 v232, a[184:187] offset:2048\n"
      "v_exp_f32 v188, v120\n"
      "v_cvt_pk_bf16_f32 v154, v192, v193\n"
      "v_mfma_f32_16x16x32_bf16 v[32:35], a[80:83], v[128:131], v[32:35]\n"
      "v_exp_f32 v189, v121\n"
      "v_cvt_pk_bf16_f32 v155, v194, v195\n"
      "v_exp_f32 v190, v122\n"
      "v_cvt_pk_bf16_f32 v156, v188, v189\n"
      "v_mfma_f32_16x16x32_bf16 v[184:187], a[136:139], v[152:155], 0\n"
      "ds_write_b128 v234, v[152:155] offset:0\n"
      "v_exp_f32 v191, v123\n"
      "v_mfma_f32_16x16x32_bf16 v[36:39], a[80:83], v[132:135], v[36:39]\n"
      "v_exp_f32 v192, v124\n"
      "v_cvt_pk_bf16_f32 v157, v190, v191\n"
      "v_exp_f32 v193, v125\n"
      "v_exp_f32 v194, v126\n"
      "v_cvt_pk_bf16_f32 v158, v192, v193\n"
      "ds_write_b128 v232, a[188:191] offset:3072\n"
      "v_mfma_f32_16x16x32_bf16 v[40:43], a[84:87], v[128:131], v[40:43]\n"
      "v_exp_f32 v195, v127\n"
      "v_exp_f32 v188, v96\n"
      "v_cvt_pk_bf16_f32 v159, v194, v195\n"
      "v_exp_f32 v189, v97\n"
      "v_mfma_f32_16x16x32_bf16 v[44:47], a[84:87], v[132:135], v[44:47]\n"
      "v_exp_f32 v190, v98\n"
      "ds_write_b128 v234, v[156:159] offset:1024\n"
      "v_cvt_pk_bf16_f32 v144, v188, v189\n"
      "v_mfma_f32_16x16x32_bf16 v[184:187], a[140:143], v[156:159], v[184:187]\n"
      "v_exp_f32 v191, v99\n"
      "v_exp_f32 v192, v100\n"
      "v_cvt_pk_bf16_f32 v145, v190, v191\n"
      "v_mfma_f32_16x16x32_bf16 v[48:51], a[88:91], v[128:131], v[48:51]\n"
      "v_exp_f32 v193, v101\n"
      "ds_write_b128 v232, a[192:195] offset:4096\n"
      "v_exp_f32 v194, v102\n"
      "v_cvt_pk_bf16_f32 v146, v192, v193\n"
      "v_exp_f32 v195, v103\n"
      "v_mfma_f32_16x16x32_bf16 v[52:55], a[88:91], v[132:135], v[52:55]\n"
      "v_exp_f32 v188, v104\n"
      "v_cvt_pk_bf16_f32 v147, v194, v195\n"
      "v_exp_f32 v189, v105\n"
      "ds_write_b128 v232, a[196:199] offset:5120\n"
      "v_exp_f32 v190, v106\n"
      "v_cvt_pk_bf16_f32 v148, v188, v189\n"
      "v_mfma_f32_16x16x32_bf16 v[184:187], a[128:131], v[144:147], v[184:187]\n"
      "v_exp_f32 v191, v107\n"
      "v_exp_f32 v192, v108\n"
      "v_cvt_pk_bf16_f32 v149, v190, v191\n"
      "v_exp_f32 v193, v109\n"
      "ds_write_b128 v232, a[200:203] offset:6144\n"
      "v_mfma_f32_16x16x32_bf16 v[56:59], a[92:95], v[128:131], v[56:59]\n"
      "v_exp_f32 v194, v110\n"
      "v_cvt_pk_bf16_f32 v150, v192, v193\n"
      "v_exp_f32 v195, v111\n"
      "v_cndmask_b32 v229, v180, v181, s[74:75]\n"
      "v_cvt_pk_bf16_f32 v151, v194, v195\n"
      "v_mfma_f32_16x16x32_bf16 v[60:63], a[92:95], v[132:135], v[60:63]\n"
      "v_cndmask_b32 v200, v182, v183, s[74:75]\n"
      "ds_write_b128 v232, a[204:207] offset:7168\n"
      "v_cndmask_b32 v228, v176, v177, s[74:75]\n"
      "v_mfma_f32_16x16x32_bf16 v[184:187], a[132:135], v[148:151], v[184:187]\n"
      "s_nop 7\n"
      "v_max3_f32 v196, v184, v185, v186\n"
      "v_max_f32 v196, v196, v187\n"
      "v_cmp_nge_f32 vcc, 0x4b800000, v196\n"
      "s_cbranch_vccnz FS8_%=\n"
      "FR8_%=:\n"
      "v_cndmask_b32 v202, v186, v187, s[74:75]\n"
      "v_cndmask_b32 v201, v184, v185, s[74:75]\n"
      "v_add_f32 v200, v200, v202\n"
      "ds_write_b32 v234, v200 offset:4096\n"
      "v_add_f32 v229, v229, v201\n"
      "s_waitcnt lgkmcnt(8)\n"
      "v_mfma_f32_16x16x32_bf16 v[32:35], a[16:19], v[144:147], v[32:35]\n"
      "v_mfma_f32_16x16x32_bf16 v[36:39], a[16:19], v[148:151], v[36:39]\n"
      "v_mfma_f32_16x16x32_bf16 v[40:43], a[20:23], v[144:147], v[40:43]\n"
      "s_waitcnt lgkmcnt(0)\n"
      "s_barrier\n"
      "v_mfma_f32_16x16x32_bf16 v[44:47], a[20:23], v[148:151], v[44:47]\n"
      "ds_read_b32 v208, v209\n"
      "ds_read_b128 v[160:163], v235 offset:0\n"
      "v_mfma_f32_16x16x32_bf16 v[48:51], a[24:27], v[144:147], v[48:51]\n"
      "ds_read_b128 v[164:167], v235 offset:1024\n"
      "ds_read_b128 v[64:67], v233 offset:0\n"
      "v_mfma_f32_16x16x32_bf16 v[52:55], a[24:27], v[148:151], v[52:55]\n"
      "ds_read_b128 v[68:71], v233 offset:1024\n"
      "ds_read_b128 v[72:75], v233 offset:2048\n"
      "ds_read_b128 v[76:79], v233 offset:3072\n"
      "v_mfma_f32_16x16x32_bf16 v[56:59], a[28:31], v[144:147], v[56:59]\n"
      "ds_read_b128 v[80:83], v233 offset:4096\n"
      "ds_read_b128 v[84:87], v233 offset:5120\n"
      "v_mfma_f32_16x16x32_bf16 v[60:63], a[28:31], v[148:151], v[60:63]\n"
      "ds_read_b128 v[88:91], v233 offset:6144\n"
      "ds_read_b128 v[92:95], v233 offset:7168\n"
      "ds_read_b32 v204, v235 offset:4096\n"
      "s_waitcnt lgkmcnt(11)\n"
      "v_cmp_ne_u32 vcc, 0, v208\n"
      "s_cbranch_vccnz SLOW_%=\n"
      "AGAIN_%=:\n"
      "s_waitcnt lgkmcnt(9)\n"
      "v_mfma_f32_16x16x32_bf16 v[32:35], a[0:3], v[160:163], v[32:35]\n"
      "v_mfma_f32_16x16x32_bf16 v[36:39], a[0:3], v[164:167], v[36:39]\n"
      "v_mfma_f32_16x16x32_bf16 v[40:43], a[4:7], v[160:163], v[40:43]\n"
      "v_mfma_f32_16x16x32_bf16 v[44:47], a[4:7], v[164:167], v[44:47]\n"
      "v_mfma_f32_16x16x32_bf16 v[48:51], a[8:11], v[160:163], v[48:51]\n"
      "v_mfma_f32_16x16x32_bf16 v[52:55], a[8:11], v[164:167], v[52:55]\n"
      "v_mfma_f32_16x16x32_bf16 v[56:59], a[12:15], v[160:163], v[56:59]\n"
      "v_mfma_f32_16x16x32_bf16 v[60:63], a[12:15], v[164:167], v[60:63]\n"
      "global_load_dwordx2 v[206:207], v239, s[46:47] offset:8 sc1\n"
      "s_waitcnt lgkmcnt(7)\n"
      "v_pk_add_f32 v[32:33], v[32:33], v[64:65]\n"
      "v_pk_add_f32 v[34:35], v[34:35], v[66:67]\n"
      "v_pk_add_f32 v[36:37], v[36:37], v[68:69]\n"
      "v_pk_add_f32 v[38:39], v[38:39], v[70:71]\n"
      "v_cvt_pk_bf16_f32 v212, v32, v33\n"
      "v_cvt_pk_bf16_f32 v213, v34, v35\n"
      "v_cvt_pk_bf16_f32 v214, v36, v37\n"
      "v_cvt_pk_bf16_f32 v215, v38, v39\n"
      "buffer_store_dwordx4 v[212:215], v241, s[24:27], s78 offen offset:0 sc1\n"
      "s_waitcnt lgkmcnt(5)\n"
      "v_pk_add_f32 v[40:41], v[40:41], v[72:73]\n"
      "v_pk_add_f32 v[42:43], v[42:43], v[74:75]\n"
      "v_pk_add_f32 v[44:45], v[44:45], v[76:77]\n"
      "v_pk_add_f32 v[46:47], v[46:47], v[78:79]\n"
      "v_cvt_pk_bf16_f32 v216, v40, v41\n"
      "v_cvt_pk_bf16_f32 v217, v42, v43\n"
      "v_cvt_pk_bf16_f32 v218, v44, v45\n"
      "v_cvt_pk_bf16_f32 v219, v46, v47\n"
      "buffer_store_dwordx4 v[216:219], v241, s[24:27], s78 offen offset:1024 sc1\n"
      "s_waitcnt lgkmcnt(3)\n"
      "v_pk_add_f32 v[48:49], v[48:49], v[80:81]\n"
      "v_pk_add_f32 v[50:51], v[50:51], v[82:83]\n"
      "v_pk_add_f32 v[52:53], v[52:53], v[84:85]\n"
      "v_pk_add_f32 v[54:55], v[54:55], v[86:87]\n"
      "v_cvt_pk_bf16_f32 v220, v48, v49\n"
      "v_cvt_pk_bf16_f32 v221, v50, v51\n"
      "v_cvt_pk_bf16_f32 v222, v52, v53\n"
      "v_cvt_pk_bf16_f32 v223, v54, v55\n"
      "buffer_store_dwordx4 v[220:223], v241, s[24:27], s78 offen offset:2048 sc1\n"
      "s_waitcnt lgkmcnt(0)\n"
      "v_pk_add_f32 v[56:57], v[56:57], v[88:89]\n"
      "v_pk_add_f32 v[58:59], v[58:59], v[90:91]\n"
      "v_pk_add_f32 v[60:61], v[60:61], v[92:93]\n"
      "v_pk_add_f32 v[62:63], v[62:63], v[94:95]\n"
      "v_cvt_pk_bf16_f32 v224, v56, v57\n"
      "v_cvt_pk_bf16_f32 v225, v58, v59\n"
      "v_cvt_pk_bf16_f32 v226, v60, v61\n"
      "v_cvt_pk_bf16_f32 v227, v62, v63\n"
      "buffer_store_dwordx4 v[224:227], v241, s[24:27], s78 offen offset:3072 sc1\n"
      "v_add_f32 v229, v229, v204\n"
      "v_cndmask_b32 v230, v228, v229, s[94:95]\n"
      "v_add_u32 v231, s40, v230\n"
      "v_add3_u32 v231, v231, v212, v213\n"
      "v_add3_u32 v231, v231, v214, v215\n"
      "v_add3_u32 v231, v231, v216, v217\n"
      "v_add3_u32 v231, v231, v218, v219\n"
      "v_add3_u32 v231, v231, v220, v221\n"
      "v_add3_u32 v231, v231, v222, v223\n"
      "v_add3_u32 v231, v231, v224, v225\n"
      "v_add3_u32 v231, v231, v226, v227\n"
      "buffer_store_dwordx2 v[230:231], v240, s[24:27], s78 offen sc1\n"
      "v_xor_b32 v244, 0x10000, v244\n"
      "v_xor_b32 v245, 0x10000, v245\n"
      "v_xor_b32 v246, 0x10000, v246\n"
      "v_xor_b32 v247, 0x10000, v247\n"
      "s_waitcnt vmcnt(6)\n"
      "s_barrier\n"
      "ds_read_b64_tr_b16 a[64:65], v244 offset:16384\n"
      "ds_read_b64_tr_b16 a[66:67], v244 offset:20480\n"
      "ds_read_b64_tr_b16 a[68:69], v245 offset:16384\n"
      "ds_read_b64_tr_b16 a[70:71], v245 offset:20480\n"
      "ds_read_b64_tr_b16 a[72:73], v246 offset:16384\n"
      "ds_read_b64_tr_b16 a[74:75], v246 offset:20480\n"
      "ds_read_b64_tr_b16 a[76:77], v247 offset:16384\n"
      "ds_read_b64_tr_b16 a[78:79], v247 offset:20480\n"
      "s_waitcnt lgkmcnt(4)\n"
      "v_mfma_f32_16x16x32_bf16 a[144:147], a[64:67], v[136:139], a[144:147]\n"
      "ds_read_b64_tr_b16 a[96:97], v244 offset:17408\n"
      "ds_read_b64_tr_b16 a[98:99], v244 offset:21504\n"
      "v_mfma_f32_16x16x32_bf16 a[148:151], a[64:67], v[140:143], a[148:151]\n"
      "ds_read_b64_tr_b16 a[100:101], v245 offset:17408\n"
      "ds_read_b64_tr_b16 a[102:103], v245 offset:21504\n"
      "v_mfma_f32_16x16x32_bf16 a[152:155], a[68:71], v[136:139], a[152:155]\n"
      "ds_read_b64_tr_b16 a[104:105], v246 offset:17408\n"
      "ds_read_b64_tr_b16 a[106:107], v246 offset:21504\n"
      "v_mfma_f32_16x16x32_bf16 a[156:159], a[68:71], v[140:143], a[156:159]\n"
      "ds_read_b64_tr_b16 a[108:109], v247 offset:17408\n"
      "ds_read_b64_tr_b16 a[110:111], v247 offset:21504\n"
      "s_waitcnt lgkmcnt(8)\n"
      "v_mfma_f32_16x16x32_bf16 a[160:163], a[72:75], v[136:139], a[160:163]\n"
      "ds_read_b64_tr_b16 a[80:81], v244 offset:32768\n"
      "ds_read_b64_tr_b16 a[82:83], v244 offset:36864\n"
      "v_mfma_f32_16x16x32_bf16 a[164:167], a[72:75], v[140:143], a[164:167]\n"
      "ds_read_b64_tr_b16 a[84:85], v245 offset:32768\n"
      "ds_read_b64_tr_b16 a[86:87], v245 offset:36864\n"
      "v_mfma_f32_16x16x32_bf16 a[168:171], a[76:79], v[136:139], a[168:171]\n"
      "ds_read_b64_tr_b16 a[88:89], v246 offset:32768\n"
      "ds_read_b64_tr_b16 a[90:91], v246 offset:36864\n"
      "v_mfma_f32_16x16x32_bf16 a[172:175], a[76:79], v[140:143], a[172:175]\n"
      "ds_read_b64_tr_b16 a[92:93], v247 offset:32768\n"
      "ds_read_b64_tr_b16 a[94:95], v247 offset:36864\n"
      "s_waitcnt lgkmcnt(8)\n"
      "v_mfma_f32_16x16x32_bf16 a[144:147], a[96:99], v[152:155], a[144:147]\n"
      "s_sub_u32 s76, 18432, s78\n"
      "v_mfma_f32_16x16x32_bf16 a[148:151], a[96:99], v[156:159], a[148:151]\n"
      "s_mov_b32 s63, 0x40000\n"
      "v_mfma_f32_16x16x32_bf16 a[152:155], a[100:103], v[152:155], a[152:155]\n"
      "v_mov_b32 v198, 0\n"
      "v_mfma_f32_16x16x32_bf16 a[156:159], a[100:103], v[156:159], a[156:159]\n"
      "v_and_b32 v197, 0xf0, v241\n"
      "v_mfma_f32_16x16x32_bf16 a[160:163], a[104:107], v[152:155], a[160:163]\n"
      "v_lshrrev_b32 v196, 3, v241\n"
      "v_mfma_f32_16x16x32_bf16 a[164:167], a[104:107], v[156:159], a[164:167]\n"
      "v_and_b32 v196, 32, v196\n"
      "v_mfma_f32_16x16x32_bf16 a[168:171], a[108:111], v[152:155], a[168:171]\n"
      "v_lshl_or_b32 v196, v197, 4, v196\n"
      "v_mfma_f32_16x16x32_bf16 a[172:175], a[108:111], v[156:159], a[172:175]\n"
      "v_lshrrev_b32 v199, 5, v241\n"
      "s_waitcnt lgkmcnt(0)\n"
      "v_mfma_f32_16x16x32_bf16 a[144:147], a[80:83], a[216:219], a[144:147]\n"
      "v_and_or_b32 v196, v199, 16, v196\n"
      "v_mfma_f32_16x16x32_bf16 a[148:151], a[80:83], a[220:223], a[148:151]\n"
      "v_mov_b32 v191, s40\n"
      "v_mfma_f32_16x16x32_bf16 a[152:155], a[84:87], a[216:219], a[152:155]\n"
      "v_mfma_f32_16x16x32_bf16 a[156:159], a[84:87], a[220:223], a[156:159]\n"
      "v_mfma_f32_16x16x32_bf16 a[160:163], a[88:91], a[216:219], a[160:163]\n"
      "v_mfma_f32_16x16x32_bf16 a[164:167], a[88:91], a[220:223], a[164:167]\n"
      "v_mfma_f32_16x16x32_bf16 a[168:171], a[92:95], a[216:219], a[168:171]\n"
      "v_mfma_f32_16x16x32_bf16 a[172:175], a[92:95], a[220:223], a[172:175]\n"
      "v_mfma_f32_16x16x32_bf16 v[0:3], a[64:67], v[128:131], v[0:3]\n"
      "v_mfma_f32_16x16x32_bf16 v[4:7], a[64:67], v[132:135], v[4:7]\n"
      "ds_write_b128 v232, a[144:147] offset:32768\n"
      "v_mfma_f32_16x16x32_bf16 v[8:11], a[68:71], v[128:131], v[8:11]\n"
      "v_mfma_f32_16x16x32_bf16 v[12:15], a[68:71], v[132:135], v[12:15]\n"
      "s_waitcnt vmcnt(0)\n"
      "v_subrev_u32 v205, s82, v206\n"
      "v_cmp_ne_u32 vcc, s40, v207\n"
      "s_cbranch_vccnz LAG_%=\n"
      "v_cmp_lt_i32 vcc, 10, v205\n"
      "s_cbranch_vccnz LAG_%=\n"
      "READ_%=:\n"
      "buffer_load_dwordx2 v[160:161], v240, s[24:27], s76 offen sc1\n"
      "buffer_load_dwordx4 v[96:99], v241, s[24:27], s76 offen offset:0 sc1\n"
      "buffer_load_dwordx4 v[100:103], v241, s[24:27], s76 offen offset:1024 sc1\n"
      "buffer_load_dwordx4 v[104:107], v241, s[24:27], s76 offen offset:2048 sc1\n"
      "buffer_load_dwordx4 v[108:111], v241, s[24:27], s76 offen offset:3072 sc1\n"
      "v_mfma_f32_16x16x32_bf16 v[16:19], a[72:75], v[128:131], v[16:19]\n"
      "ds_write_b128 v232, a[148:151] offset:33792\n"
      "v_mfma_f32_16x16x32_bf16 v[20:23], a[72:75], v[132:135], v[20:23]\n"
      "v_mfma_f32_16x16x32_bf16 v[24:27], a[76:79], v[128:131], v[24:27]\n"
      "v_mfma_f32_16x16x32_bf16 v[28:31], a[76:79], v[132:135], v[28:31]\n"
      "ds_write_b128 v232, a[152:155] offset:34816\n"
      "v_mfma_f32_16x16x32_bf16 v[0:3], a[96:99], v[144:147], v[0:3]\n"
      "v_mfma_f32_16x16x32_bf16 v[4:7], a[96:99], v[148:151], v[4:7]\n"
      "v_mfma_f32_16x16x32_bf16 v[8:11], a[100:103], v[144:147], v[8:11]\n"
      "ds_write_b128 v232, a[156:159] offset:35840\n"
      "v_mfma_f32_16x16x32_bf16 v[12:15], a[100:103], v[148:151], v[12:15]\n"
      "v_mfma_f32_16x16x32_bf16 v[16:19], a[104:107], v[144:147], v[16:19]\n"
      "v_mfma_f32_16x16x32_bf16 v[20:23], a[104:107], v[148:151], v[20:23]\n"
      "ds_write_b128 v232, a[160:163] offset:36864\n"
      "v_mfma_f32_16x16x32_bf16 v[24:27], a[108:111], v[144:147], v[24:27]\n"
      "v_mfma_f32_16x16x32_bf16 v[28:31], a[108:111], v[148:151], v[28:31]\n"
      "v_mfma_f32_16x16x32_bf16 v[0:3], a[80:83], a[208:211], v[0:3]\n"
      "ds_write_b128 v232, a[164:167] offset:37888\n"
      "v_mfma_f32_16x16x32_bf16 v[4:7], a[80:83], a[212:215], v[4:7]\n"
      "v_mfma_f32_16x16x32_bf16 v[8:11], a[84:87], a[208:211], v[8:11]\n"
      "v_mfma_f32_16x16x32_bf16 v[12:15], a[84:87], a[212:215], v[12:15]\n"
      "ds_write_b128 v232, a[168:171] offset:38912\n"
      "v_mfma_f32_16x16x32_bf16 v[16:19], a[88:91], a[208:211], v[16:19]\n"
      "v_mfma_f32_16x16x32_bf16 v[20:23], a[88:91], a[212:215], v[20:23]\n"
      "v_mfma_f32_16x16x32_bf16 v[24:27], a[92:95], a[208:211], v[24:27]\n"
      "ds_write_b128 v232, a[172:175] offset:39936\n"
      "v_mfma_f32_16x16x32_bf16 v[28:31], a[92:95], a[212:215], v[28:31]\n"
      "s_waitcnt lgkmcnt(0)\n"
      "s_barrier\n"
      "ds_read_b128 v[64:67], v233 offset:32768\n"
      "ds_read_b128 v[68:71], v233 offset:33792\n"
      "ds_read_b128 v[72:75], v233 offset:34816\n"
      "ds_read_b128 v[76:79], v233 offset:35840\n"
      "ds_read_b128 v[80:83], v233 offset:36864\n"
      "ds_read_b128 v[84:87], v233 offset:37888\n"
      "ds_read_b128 v[88:91], v233 offset:38912\n"
      "ds_read_b128 v[92:95], v233 offset:39936\n"
      "s_waitcnt lgkmcnt(6)\n"
      "v_pk_add_f32 v[0:1], v[0:1], v[64:65]\n"
      "v_pk_add_f32 v[2:3], v[2:3], v[66:67]\n"
      "v_pk_add_f32 v[4:5], v[4:5], v[68:69]\n"
      "v_pk_add_f32 v[6:7], v[6:7], v[70:71]\n"
      "s_waitcnt lgkmcnt(4)\n"
      "v_pk_add_f32 v[8:9], v[8:9], v[72:73]\n"
      "v_pk_add_f32 v[10:11], v[10:11], v[74:75]\n"
      "v_pk_add_f32 v[12:13], v[12:13], v[76:77]\n"
      "v_pk_add_f32 v[14:15], v[14:15], v[78:79]\n"
      "s_waitcnt lgkmcnt(2)\n"
      "v_pk_add_f32 v[16:17], v[16:17], v[80:81]\n"
      "v_pk_add_f32 v[18:19], v[18:19], v[82:83]\n"
      "v_pk_add_f32 v[20:21], v[20:21], v[84:85]\n"
      "v_pk_add_f32 v[22:23], v[22:23], v[86:87]\n"
      "s_waitcnt lgkmcnt(0)\n"
      "v_pk_add_f32 v[24:25], v[24:25], v[88:89]\n"
      "v_pk_add_f32 v[26:27], v[26:27], v[90:91]\n"
      "v_pk_add_f32 v[28:29], v[28:29], v[92:93]\n"
      "v_pk_add_f32 v[30:31], v[30:31], v[94:95]\n"
      "TRY_%=:\n"
      "s_waitcnt vmcnt(4)\n"
      "v_mov_b32 v162, v160\n"
      "v_add_u32 v231, s40, v160\n"
      "s_nop 0\n"
      "v_permlane32_swap_b32 v160, v162\n"
      "v_sub_f32 v168, v160, v228\n"
      "v_min_f32 v172, 0, -v168\n"
      "v_min_f32 v168, 0, v168\n"
      "v_exp_f32 v168, v168\n"
      "v_exp_f32 v172, v172\n"
      "v_mul_f32 v163, v229, v168\n"
      "v_fmac_f32 v163, v162, v172\n"
      "v_rcp_f32 v163, v163\n"
      "s_nop 0\n"
      "v_mul_f32 v168, v168, v163\n"
      "v_mul_f32 v172, v172, v163\n"
      "v_mov_b32 v169, v168\n"
      "v_mov_b32 v173, v172\n"
      "s_nop 0\n"
      "v_permlane16_swap_b32 v168, v169\n"
      "v_permlane16_swap_b32 v172, v173\n"
      "v_pk_mul_f32 v[32:33], v[0:1], v[168:169] op_sel:[0,0] op_sel_hi:[1,0]\n"
      "v_pk_mul_f32 v[34:35], v[2:3], v[168:169] op_sel:[0,0] op_sel_hi:[1,0]\n"
      "v_pk_mul_f32 v[36:37], v[4:5], v[168:169] op_sel:[0,1] op_sel_hi:[1,1]\n"
      "v_pk_mul_f32 v[38:39], v[6:7], v[168:169] op_sel:[0,1] op_sel_hi:[1,1]\n"
      "v_pk_mul_f32 v[40:41], v[8:9], v[168:169] op_sel:[0,0] op_sel_hi:[1,0]\n"
      "v_pk_mul_f32 v[42:43], v[10:11], v[168:169] op_sel:[0,0] op_sel_hi:[1,0]\n"
      "v_pk_mul_f32 v[44:45], v[12:13], v[168:169] op_sel:[0,1] op_sel_hi:[1,1]\n"
      "v_pk_mul_f32 v[46:47], v[14:15], v[168:169] op_sel:[0,1] op_sel_hi:[1,1]\n"
      "v_pk_mul_f32 v[48:49], v[16:17], v[168:169] op_sel:[0,0] op_sel_hi:[1,0]\n"
      "v_pk_mul_f32 v[50:51], v[18:19], v[168:169] op_sel:[0,0] op_sel_hi:[1,0]\n"
      "v_pk_mul_f32 v[52:53], v[20:21], v[168:169] op_sel:[0,1] op_sel_hi:[1,1]\n"
      "v_pk_mul_f32 v[54:55], v[22:23], v[168:169] op_sel:[0,1] op_sel_hi:[1,1]\n"
      "v_pk_mul_f32 v[56:57], v[24:25], v[168:169] op_sel:[0,0] op_sel_hi:[1,0]\n"
      "v_pk_mul_f32 v[58:59], v[26:27], v[168:169] op_sel:[0,0] op_sel_hi:[1,0]\n"
      "v_pk_mul_f32 v[60:61], v[28:29], v[168:169] op_sel:[0,1] op_sel_hi:[1,1]\n"
      "v_pk_mul_f32 v[62:63], v[30:31], v[168:169] op_sel:[0,1] op_sel_hi:[1,1]\n"
      "s_waitcnt vmcnt(3)\n"
      "v_add3_u32 v231, v231, v96, v97\n"
      "v_add3_u32 v231, v231, v98, v99\n"
      "s_waitcnt vmcnt(2)\n"
      "v_add3_u32 v231, v231, v100, v101\n"
      "v_add3_u32 v231, v231, v102, v103\n"
      "s_waitcnt vmcnt(1)\n"
      "v_add3_u32 v231, v231, v104, v105\n"
      "v_add3_u32 v231, v231, v106, v107\n"
      "s_waitcnt vmcnt(0)\n"
      "v_add3_u32 v231, v231, v108, v109\n"
      "v_add3_u32 v231, v231, v110, v111\n"
      "v_cmp_ne_u32 vcc, v231, v161\n"
      "s_cbranch_vccnz RETRY_%=\n"
      "GOT_%=:\n"
      "v_lshlrev_b32 v0, 16, v96\n"
      "v_and_b32 v1, 0xffff0000, v96\n"
      "v_lshlrev_b32 v2, 16, v97\n"
      "v_and_b32 v3, 0xffff0000, v97\n"
      "v_lshlrev_b32 v8, 16, v100\n"
      "v_and_b32 v9, 0xffff0000, v100\n"
      "v_lshlrev_b32 v10, 16, v101\n"
      "v_and_b32 v11, 0xffff0000, v101\n"
      "v_pk_fma_f32 v[32:33], v[0:1], v[172:173], v[32:33] op_sel:[0,0,0] op_sel_hi:[1,0,1]\n"
      "v_pk_fma_f32 v[34:35], v[2:3], v[172:173], v[34:35] op_sel:[0,0,0] op_sel_hi:[1,0,1]\n"
      "v_pk_fma_f32 v[40:41], v[8:9], v[172:173], v[40:41] op_sel:[0,0,0] op_sel_hi:[1,0,1]\n"
      "v_pk_fma_f32 v[42:43], v[10:11], v[172:173], v[42:43] op_sel:[0,0,0] op_sel_hi:[1,0,1]\n"
      "v_cvt_pk_bf16_f32 v112, v32, v33\n"
      "v_cvt_pk_bf16_f32 v114, v40, v41\n"
      "v_cvt_pk_bf16_f32 v113, v34, v35\n"
      "v_cvt_pk_bf16_f32 v115, v42, v43\n"
      "v_permlane16_swap_b32 v112, v114\n"
      "s_nop 0\n"
      "v_permlane16_swap_b32 v113, v115\n"
      "global_store_dwordx4 v196, v[112:115], s[30:31] offset:-4096 sc0 sc1 nt\n"
      "v_lshlrev_b32 v16, 16, v104\n"
      "v_and_b32 v17, 0xffff0000, v104\n"
      "v_lshlrev_b32 v18, 16, v105\n"
      "v_and_b32 v19, 0xffff0000, v105\n"
      "v_lshlrev_b32 v24, 16, v108\n"
      "v_and_b32 v25, 0xffff0000, v108\n"
      "v_lshlrev_b32 v26, 16, v109\n"
      "v_and_b32 v27, 0xffff0000, v109\n"
      "v_pk_fma_f32 v[48:49], v[16:17], v[172:173], v[48:49] op_sel:[0,0,0] op_sel_hi:[1,0,1]\n"
      "v_pk_fma_f32 v[50:51], v[18:19], v[172:173], v[50:51] op_sel:[0,0,0] op_sel_hi:[1,0,1]\n"
      "v_pk_fma_f32 v[56:57], v[24:25], v[172:173], v[56:57] op_sel:[0,0,0] op_sel_hi:[1,0,1]\n"
      "v_pk_fma_f32 v[58:59], v[26:27], v[172:173], v[58:59] op_sel:[0,0,0] op_sel_hi:[1,0,1]\n"
      "v_cvt_pk_bf16_f32 v116, v48, v49\n"
      "v_cvt_pk_bf16_f32 v118, v56, v57\n"
      "v_cvt_pk_bf16_f32 v117, v50, v51\n"
      "v_cvt_pk_bf16_f32 v119, v58, v59\n"
      "v_permlane16_swap_b32 v116, v118\n"
      "s_nop 0\n"
      "v_permlane16_swap_b32 v117, v119\n"
      "global_store_dwordx4 v196, v[116:119], s[30:31] offset:-4032 sc0 sc1 nt\n"
      "v_lshlrev_b32 v4, 16, v98\n"
      "v_and_b32 v5, 0xffff0000, v98\n"
      "v_lshlrev_b32 v6, 16, v99\n"
      "v_and_b32 v7, 0xffff0000, v99\n"
      "v_lshlrev_b32 v12, 16, v102\n"
      "v_and_b32 v13, 0xffff0000, v102\n"
      "v_lshlrev_b32 v14, 16, v103\n"
      "v_and_b32 v15, 0xffff0000, v103\n"
      "v_pk_fma_f32 v[36:37], v[4:5], v[172:173], v[36:37] op_sel:[0,1,0] op_sel_hi:[1,1,1]\n"
      "v_pk_fma_f32 v[38:39], v[6:7], v[172:173], v[38:39] op_sel:[0,1,0] op_sel_hi:[1,1,1]\n"
      "v_pk_fma_f32 v[44:45], v[12:13], v[172:173], v[44:45] op_sel:[0,1,0] op_sel_hi:[1,1,1]\n"
      "v_pk_fma_f32 v[46:47], v[14:15], v[172:173], v[46:47] op_sel:[0,1,0] op_sel_hi:[1,1,1]\n"
      "v_cvt_pk_bf16_f32 v120, v36, v37\n"
      "v_cvt_pk_bf16_f32 v122, v44, v45\n"
      "v_cvt_pk_bf16_f32 v121, v38, v39\n"
      "v_cvt_pk_bf16_f32 v123, v46, v47\n"
      "v_permlane16_swap_b32 v120, v122\n"
      "s_nop 0\n"
      "v_permlane16_swap_b32 v121, v123\n"
      "global_store_dwordx4 v196, v[120:123], s[30:31] offset:0 sc0 sc1 nt\n"
      "v_lshlrev_b32 v20, 16, v106\n"
      "v_and_b32 v21, 0xffff0000, v106\n"
      "v_lshlrev_b32 v22, 16, v107\n"
      "v_and_b32 v23, 0xffff0000, v107\n"
      "v_lshlrev_b32 v28, 16, v110\n"
      "v_and_b32 v29, 0xffff0000, v110\n"
      "v_lshlrev_b32 v30, 16, v111\n"
      "v_and_b32 v31, 0xffff0000, v111\n"
      "v_pk_fma_f32 v[52:53], v[20:21], v[172:173], v[52:53] op_sel:[0,1,0] op_sel_hi:[1,1,1]\n"
      "v_pk_fma_f32 v[54:55], v[22:23], v[172:173], v[54:55] op_sel:[0,1,0] op_sel_hi:[1,1,1]\n"
      "v_pk_fma_f32 v[60:61], v[28:29], v[172:173], v[60:61] op_sel:[0,1,0] op_sel_hi:[1,1,1]\n"
      "v_pk_fma_f32 v[62:63], v[30:31], v[172:173], v[62:63] op_sel:[0,1,0] op_sel_hi:[1,1,1]\n"
      "v_cvt_pk_bf16_f32 v124, v52, v53\n"
      "v_cvt_pk_bf16_f32 v126, v60, v61\n"
      "v_cvt_pk_bf16_f32 v125, v54, v55\n"
      "v_cvt_pk_bf16_f32 v127, v62, v63\n"
      "v_permlane16_swap_b32 v124, v126\n"
      "s_nop 0\n"
      "v_permlane16_swap_b32 v125, v127\n"
      "global_store_dwordx4 v196, v[124:127], s[30:31] offset:64 sc0 sc1 nt\n"
      "s_mov_b64 exec, 1\n"
      "global_store_dword v198, v191, s[46:47] sc1\n"
      "s_mov_b64 exec, -1\n"
      "ENDE_%=:\n"
      ".subsection 1\n"
      "MH_%=:\n"
      "s_cmp_lg_u32 s54, 0\n"
      "s_waitcnt vmcnt(12) lgkmcnt(0)\n"
      "s_cbranch_scc1 W6_%=\n"
      "s_waitcnt vmcnt(8)\n"
      "W6_%=:\n"
      "s_barrier\n"
      "v_mfma_f32_16x16x32_bf16 v[32:35], a[112:115], v[144:147], v[32:35]\n"
      "s_xor_b64 s[56:57], s[56:57], s[32:33]\n"
      "ds_read_b128 a[64:67], v242 offset:0\n"
      "ds_read_b128 a[72:75], v243 offset:0\n"
      "v_mfma_f32_16x16x32_bf16 v[36:39], a[112:115], v[148:151], v[36:39]\n"
      "s_add_u32 s61, s61, 1\n"
      "ds_read_b128 a[80:83], v242 offset:1024\n"
      "ds_read_b128 a[88:91], v243 offset:1024\n"
      "v_mfma_f32_16x16x32_bf16 a[176:179], a[112:115], v[152:155], a[176:179]\n"
      "s_add_u32 s62, s62, 0x4000\n"
      "v_exp_f32 v189, v89\n"
      "v_mfma_f32_16x16x32_bf16 a[180:183], a[112:115], v[156:159], a[180:183]\n"
      "s_and_b32 s62, s62, 0x1ffff\n"
      "v_cvt_pk_bf16_f32 v139, v194, v195\n"
      "v_cvt_pk_bf16_f32 v140, v188, v189\n"
      "v_mfma_f32_16x16x32_bf16 a[188:191], a[116:119], v[156:159], a[188:191]\n"
      "v_readlane_b32 s60, v219, s61\n"
      "v_exp_f32 v190, v90\n"
      "v_mfma_f32_16x16x32_bf16 v[184:187], a[136:139], v[136:139], v[184:187]\n"
      "s_mov_b32 s83, s82\n"
      "v_exp_f32 v191, v91\n"
      "v_mfma_f32_16x16x32_bf16 a[184:187], a[116:119], v[152:155], a[184:187]\n"
      "v_exp_f32 v192, v92\n"
      "v_mfma_f32_16x16x32_bf16 v[44:47], a[116:119], v[148:151], v[44:47]\n"
      "s_add_u32 s66, s66, s86\n"
      "v_exp_f32 v193, v93\n"
      "v_mfma_f32_16x16x32_bf16 v[40:43], a[116:119], v[144:147], v[40:43]\n"
      "s_add_u32 s82, s82, s86\n"
      "v_cvt_pk_bf16_f32 v141, v190, v191\n"
      "v_cvt_pk_bf16_f32 v142, v192, v193\n"
      "v_mfma_f32_16x16x32_bf16 v[48:51], a[120:123], v[144:147], v[48:51]\n"
      "s_add_u32 s65, s65, s86\n"
      "v_exp_f32 v194, v94\n"
      "v_mfma_f32_16x16x32_bf16 v[52:55], a[120:123], v[148:151], v[52:55]\n"
      "s_max_u32 s88, s66, s65\n"
      "v_exp_f32 v195, v95\n"
      "s_waitcnt lgkmcnt(0)\n"
      "v_mfma_f32_16x16x32_bf16 a[192:195], a[120:123], v[152:155], a[192:195]\n"
      "v_cvt_pk_bf16_f32 v143, v194, v195\n"
      "v_mfma_f32_16x16x32_bf16 a[196:199], a[120:123], v[156:159], a[196:199]\n"
      "ds_read_b128 a[68:71], v242 offset:4096\n"
      "ds_read_b128 a[76:79], v243 offset:4096\n"
      "v_mfma_f32_16x16x32_bf16 v[184:187], a[140:143], v[140:143], v[184:187]\n"
      "s_mov_b32 m0, s62\n"
      "ds_read_b128 a[84:87], v242 offset:5120\n"
      "ds_read_b128 a[92:95], v243 offset:5120\n"
      "v_mfma_f32_16x16x32_bf16 a[204:207], a[124:127], v[156:159], a[204:207]\n"
      "buffer_load_dwordx4 v228, s[56:59], s60 offen nt lds\n"
      "v_xor_b32 v244, 0x10000, v244\n"
      "v_xor_b32 v245, 0x10000, v245\n"
      "v_mfma_f32_16x16x32_bf16 a[200:203], a[124:127], v[152:155], a[200:203]\n"
      "buffer_load_dwordx4 v229, s[56:59], s60 offen offset:1024 nt lds\n"
      "v_xor_b32 v246, 0x10000, v246\n"
      "v_xor_b32 v247, 0x10000, v247\n"
      "v_mfma_f32_16x16x32_bf16 v[60:63], a[124:127], v[148:151], v[60:63]\n"
      "buffer_load_dwordx4 v230, s[56:59], s60 offen offset:2048 nt lds\n"
      "v_max3_f32 v196, v184, v185, v186\n"
      "v_max_f32 v196, v196, v187\n"
      "v_mfma_f32_16x16x32_bf16 v[56:59], a[124:127], v[144:147], v[56:59]\n"
      "buffer_load_dwordx4 v231, s[56:59], s60 offen offset:3072 nt lds\n"
      "v_cmp_nge_f32 vcc, 0x4b800000, v196\n"
      "s_cbranch_vccnz FS11_%=\n"
      "FR11_%=:\n"
      "v_mfma_f32_16x16x32_bf16 v[64:67], a[64:67], a[0:3], v[160:163]\n"
      "v_exp_f32 v188, v96\n"
      "v_mfma_f32_16x16x32_bf16 v[64:67], a[72:75], a[16:19], v[64:67]\n"
      "v_add_f32 v180, v180, v184\n"
      "v_add_f32 v181, v181, v185\n"
      "v_mfma_f32_16x16x32_bf16 v[64:67], a[80:83], a[32:35], v[64:67]\n"
      "v_exp_f32 v189, v97\n"
      "v_mfma_f32_16x16x32_bf16 v[64:67], a[88:91], a[48:51], v[64:67]\n"
      "v_add_f32 v182, v182, v186\n"
      "v_add_f32 v183, v183, v187\n"
      "v_mfma_f32_16x16x32_bf16 v[72:75], a[64:67], a[4:7], v[164:167]\n"
      "v_exp_f32 v190, v98\n"
      "v_mfma_f32_16x16x32_bf16 v[72:75], a[72:75], a[20:23], v[72:75]\n"
      "v_exp_f32 v191, v99\n"
      "v_mfma_f32_16x16x32_bf16 v[72:75], a[80:83], a[36:39], v[72:75]\n"
      "v_cvt_pk_bf16_f32 v144, v188, v189\n"
      "v_cvt_pk_bf16_f32 v145, v190, v191\n"
      "v_mfma_f32_16x16x32_bf16 v[72:75], a[88:91], a[52:55], v[72:75]\n"
      "v_exp_f32 v192, v100\n"
      "v_mfma_f32_16x16x32_bf16 v[80:83], a[64:67], a[8:11], v[168:171]\n"
      "s_add_u32 s39, s39, s39\n"
      "v_exp_f32 v193, v101\n"
      "v_mfma_f32_16x16x32_bf16 v[80:83], a[72:75], a[24:27], v[80:83]\n"
      "s_memrealtime s[68:69]\n"
      "v_exp_f32 v194, v102\n"
      "v_mfma_f32_16x16x32_bf16 v[80:83], a[80:83], a[40:43], v[80:83]\n"
      "v_exp_f32 v195, v103\n"
      "v_mfma_f32_16x16x32_bf16 v[80:83], a[88:91], a[56:59], v[80:83]\n"
      "v_cvt_pk_bf16_f32 v146, v192, v193\n"
      "v_cvt_pk_bf16_f32 v147, v194, v195\n"
      "v_mfma_f32_16x16x32_bf16 v[88:91], a[64:67], a[12:15], v[172:175]\n"
      "s_nop 0\n"
      "v_mfma_f32_16x16x32_bf16 v[184:187], a[128:131], v[144:147], 0\n"
      "s_nop 0\n"
      "v_mfma_f32_16x16x32_bf16 v[88:91], a[72:75], a[28:31], v[88:91]\n"
      "s_nop 0\n"
      "v_mfma_f32_16x16x32_bf16 v[88:91], a[80:83], a[44:47], v[88:91]\n"
      "s_nop 0\n"
      "s_cbranch_scc1 SP1_%=\n"
      "s_branch HD1_%=\n"
      "HOOK_%=:\n"
      "buffer_load_dword v250, v251, s[20:23], 0 offen sc1\n"
      "s_swappc_b64 s[84:85], s[84:85]\n"
      "s_waitcnt vmcnt(12)\n"
      "v_subrev_u32 v250, s71, v250\n"
      "v_min_u32 v250, 0x258, v250\n"
      "s_nop 1\n"
      "v_add_u32_dpp v250, v250, v250 quad_perm:[1,0,3,2] row_mask:0xf bank_mask:0xf\n"
      "s_nop 1\n"
      "v_add_u32_dpp v250, v250, v250 quad_perm:[2,3,0,1] row_mask:0xf bank_mask:0xf\n"
      "s_nop 1\n"
      "v_add_u32_dpp v250, v250, v250 row_half_mirror row_mask:0xf bank_mask:0xf\n"
      "s_nop 0\n"
      "v_readfirstlane_b32 s67, v250\n"
      "s_sub_u32 s66, s66, s81\n"
      "s_lshr_b32 s67, s67, 3\n"
      "s_add_u32 s66, s66, s67\n"
      "s_max_u32 s88, s66, s65\n"
      "s_mov_b32 s86, 57\n"
      "s_mov_b32 s77, 171\n"
      "v_bfe_u32 v251, v241, 2, 7\n"
      "v_add_u32 v251, s79, v251\n"
      "buffer_load_dword v250, v251, s[20:23], 0 offen sc1\n"
      "s_swappc_b64 s[84:85], s[84:85]\n"
      "s_mov_b32 s86, 55\n"
      "s_mov_b32 s77, 165\n"
      "s_swappc_b64 s[84:85], s[84:85]\n"
      "s_mov_b32 s89, 0\n"
      "s_swappc_b64 s[84:85], s[84:85]\n"
      "s_mov_b32 s86, 54\n"
      "s_mov_b32 s77, 162\n"
      "s_swappc_b64 s[84:85], s[84:85]\n"
      "s_cmp_lt_u32 s89, 2\n"
      "s_cselect_b32 s67, 2048, s72\n"
      "s_mov_b32 s89, 0\n"
      "s_cmp_lt_u32 s67, 2048\n"
      "s_cbranch_scc0 NV0_%=\n"
      "v_and_b32 v216, 0x1f0, v241\n"
      "v_lshlrev_b32 v216, 4, v216\n"
      "v_mov_b32 v215, s82\n"
      "s_lshr_b32 s67, s67, 4\n"
      "s_add_u32 s67, s67, 8192\n"
      "buffer_store_dword v215, v216, s[20:23], s67 offen sc1\n"
      "NV0_%=:\n"
      "s_swappc_b64 s[84:85], s[84:85]\n"
      "buffer_load_dword v250, v251, s[20:23], 0 offen sc1\n"
      "s_swappc_b64 s[84:85], s[84:85]\n"
      "s_mov_b32 s86, 53\n"
      "s_mov_b32 s77, 159\n"
      "s_min_i32 s70, s70, 150\n"
      "s_waitcnt vmcnt(12)\n"
      "v_sub_u32 v250, s82, v250\n"
      "v_cmp_gt_u32 vcc, 1600, v250\n"
      "s_movk_i32 s76, 45\n"
      "s_nop 2\n"
      "s_bcnt1_i32_b32 s67, vcc_lo\n"
      "s_cmp_eq_u32 s70, 60\n"
      "s_cselect_b32 s67, 0, s67\n"
      "s_cmp_ge_u32 s67, 12\n"
      "s_cbranch_scc1 SM0_%=\n"
      "s_swappc_b64 s[84:85], s[84:85]\n"
      "s_cmp_lt_u32 s89, 4\n"
      "s_cselect_b32 s67, 2048, s72\n"
      "s_mov_b32 s89, 0\n"
      "s_cmp_lt_u32 s67, 2048\n"
      "s_cbranch_scc0 NV1_%=\n"
      "v_and_b32 v216, 0x1f0, v241\n"
      "v_lshlrev_b32 v216, 4, v216\n"
      "v_mov_b32 v215, s82\n"
      "s_lshr_b32 s67, s67, 4\n"
      "s_add_u32 s67, s67, 8192\n"
      "buffer_store_dword v215, v216, s[20:23], s67 offen sc1\n"
      "NV1_%=:\n"
      "s_swappc_b64 s[84:85], s[84:85]\n"
      "buffer_load_dword v250, v251, s[20:23], 0 offen sc1\n"
      "s_swappc_b64 s[84:85], s[84:85]\n"
      "s_waitcnt vmcnt(12)\n"
      "v_sub_u32 v250, s82, v250\n"
      "v_cmp_gt_u32 vcc, 1600, v250\n"
      "s_movk_i32 s76, 65\n"
      "s_nop 2\n"
      "s_bcnt1_i32_b32 s67, vcc_lo\n"
      "s_cmp_eq_u32 s70, 60\n"
      "s_cselect_b32 s67, 0, s67\n"
      "s_cmp_ge_u32 s67, 12\n"
      "s_cbranch_scc1 SM1_%=\n"
      "s_swappc_b64 s[84:85], s[84:85]\n"
      "s_getpc_b64 s[74:75]\n"
      "PF_%=:\n"
      "s_sub_u32 s74, s74, PF_%=-END_%=\n"
      "s_subb_u32 s75, s75, 0\n"
      "s_lshl_b32 s76, %[blk], 7\n"
      "s_and_b32 s76, s76, 0x1c00\n"
      "s_min_u32 s76, s76, ENDE_%=-END_%=-1024\n"
      "v_add_u32 v200, s76, v241\n"
      "global_load_dwordx4 v[200:203], v200, s[74:75]\n"
      "v_mov_b32 v212, 0\n"
      "global_load_dword v213, v212, s[30:31] nt\n"
      "buffer_load_dword v214, off, s[24:27], s78 nt\n"
      "s_swappc_b64 s[84:85], s[84:85]\n"
      "SM1_%=:\n"
      "s_add_u32 s66, s66, s76\n"
      "s_max_u32 s88, s66, s65\n"
      "s_min_i32 s70, s70, 150\n"
      "s_branch SM2_%=\n"
      "SM0_%=:\n"
      "s_or_b32 s39, s39, 0xc0000000\n"
      "s_mov_b32 s52, 0xaaaaaaaa\n"
      "s_mov_b32 s53, 0xaaaaaaaa\n"
      "s_add_u32 s66, s66, s76\n"
      "s_max_u32 s88, s66, s65\n"
      "s_min_i32 s70, s70, 150\n"
      "s_mov_b32 s86, 59\n"
      "s_mov_b32 s77, 177\n"
      "s_swappc_b64 s[84:85], s[84:85]\n"
      "s_mov_b32 s86, 57\n"
      "s_mov_b32 s77, 171\n"
      "s_swappc_b64 s[84:85], s[84:85]\n"
      "SM2_%=:\n"
      "s_mov_b32 s86, 55\n"
      "s_mov_b32 s77, 165\n"
      "s_mov_b32 s52, 0x0\n"
      "s_mov_b32 s53, 0x80000\n"
      "s_swappc_b64 s[84:85], s[84:85]\n"
      "s_getpc_b64 s[74:75]\n"
      "PG_%=:\n"
      "s_sub_u32 s74, s74, PG_%=-END_%=\n"
      "s_subb_u32 s75, s75, 0\n"
      "s_lshl_b32 s76, %[blk], 7\n"
      "s_and_b32 s76, s76, 0x1c00\n"
      "s_min_u32 s76, s76, ENDE_%=-END_%=-1024\n"
      "v_add_u32 v200, s76, v241\n"
      "global_load_dwordx4 v[200:203], v200, s[74:75]\n"
      "v_mov_b32 v212, 0\n"
      "global_load_dword v213, v212, s[30:31] nt\n"
      "buffer_load_dword v214, off, s[24:27], s78 nt\n"
      "s_swappc_b64 s[84:85], s[84:85]\n"
      "GW0_%=:\n"
      "s_memrealtime s[68:69]\n"
      "s_waitcnt lgkmcnt(0)\n"
      "s_sub_u32 s67, s68, s88\n"
      "s_cmp_ge_i32 s67, 0\n"
      "s_cbranch_scc1 GX0_%=\n"
      "s_branch GW0_%=\n"
      "FS1_%=:\n"
      "s_call_b64 s[96:97], FB1_%=\n"
      "s_branch FR1_%=\n"
      "FS2_%=:\n"
      "s_call_b64 s[96:97], FB0_%=\n"
      "s_branch FR2_%=\n"
      "FS3_%=:\n"
      "s_call_b64 s[96:97], FB0_%=\n"
      "s_branch FR3_%=\n"
      "GW4_%=:\n"
      "s_memrealtime s[68:69]\n"
      "s_waitcnt lgkmcnt(0)\n"
      "s_sub_u32 s67, s68, s88\n"
      "s_cmp_ge_i32 s67, 0\n"
      "s_cbranch_scc1 GX4_%=\n"
      "s_branch GW4_%=\n"
      "GW5_%=:\n"
      "s_memrealtime s[68:69]\n"
      "s_waitcnt lgkmcnt(0)\n"
      "s_sub_u32 s67, s68, s88\n"
      "s_cmp_ge_i32 s67, 0\n"
      "s_cbranch_scc1 GX5_%=\n"
      "s_branch GW5_%=\n"
      "GW6_%=:\n"
      "s_memrealtime s[68:69]\n"
      "s_waitcnt lgkmcnt(0)\n"
      "s_sub_u32 s67, s68, s88\n"
      "s_cmp_ge_i32 s67, 0\n"
      "s_cbranch_scc1 GX6_%=\n"
      "s_branch GW6_%=\n"
      "GW7_%=:\n"
      "s_memrealtime s[68:69]\n"
      "s_waitcnt lgkmcnt(0)\n"
      "s_sub_u32 s67, s68, s88\n"
      "s_cmp_ge_i32 s67, 0\n"
      "s_cbranch_scc1 GX7_%=\n"
      "s_branch GW7_%=\n"
      "FS8_%=:\n"
      "s_call_b64 s[96:97], FB1_%=\n"
      "s_branch FR8_%=\n"
      "LAG_%=:\n"
      "s_memrealtime s[68:69]\n"
      "s_waitcnt lgkmcnt(0)\n"
      "s_mov_b32 s88, s68\n"
      "LG1_%=:\n"
      "v_cmp_ne_u32 vcc, s40, v207\n"
      "s_cbranch_vccz LG2_%=\n"
      "s_sub_u32 s63, s63, 1\n"
      "s_cmp_eq_u32 s63, 0\n"
      "s_cbranch_scc1 READ_%=\n"
      "s_sleep 4\n"
      "global_load_dwordx2 v[206:207], v239, s[46:47] offset:8 sc1\n"
      "s_waitcnt vmcnt(0)\n"
      "s_branch LG1_%=\n"
      "LG2_%=:\n"
      "v_readfirstlane_b32 s67, v206\n"
      "s_sub_u32 s88, s88, s82\n"
      "s_min_u32 s88, s88, 400\n"
      "s_add_u32 s88, s88, s67\n"
      "LG3_%=:\n"
      "s_memrealtime s[68:69]\n"
      "s_waitcnt lgkmcnt(0)\n"
      "s_sub_u32 s67, s88, s68\n"
      "s_sub_u32 s67, s67, 11\n"
      "s_cmp_lt_u32 s67, 400\n"
      "s_cbranch_scc1 LG3_%=\n"
      "s_branch READ_%=\n"
      "RETRY_%=:\n"
      "s_sub_u32 s63, s63, 1\n"
      "s_cbranch_scc1 GOT_%=\n"
      "buffer_load_dwordx2 v[160:161], v240, s[24:27], s76 offen sc1\n"
      "buffer_load_dwordx4 v[96:99], v241, s[24:27], s76 offen offset:0 sc1\n"
      "buffer_load_dwordx4 v[100:103], v241, s[24:27], s76 offen offset:1024 sc1\n"
      "buffer_load_dwordx4 v[104:107], v241, s[24:27], s76 offen offset:2048 sc1\n"
      "buffer_load_dwordx4 v[108:111], v241, s[24:27], s76 offen offset:3072 sc1\n"
      "s_branch TRY_%=\n"
      "FS11_%=:\n"
      "s_call_b64 s[96:97], FB0_%=\n"
      "s_branch FR11_%=\n"
      "COLD_%=:\n"
      "FB0_%=:\n"
      "v_mov_b32 v196, 139248\n"
      "s_nop 7\n"
      "ds_write_b32 v196, v196\n"
      "v_max3_f32 v188, v64, v65, 0\n"
      "v_max3_f32 v189, v72, v73, 0\n"
      "v_max3_f32 v190, v80, v81, 0\n"
      "v_max3_f32 v191, v88, v89, 0\n"
      "v_max3_f32 v188, v188, v66, v67\n"
      "v_max3_f32 v189, v189, v74, v75\n"
      "v_max3_f32 v190, v190, v82, v83\n"
      "v_max3_f32 v191, v191, v90, v91\n"
      "v_max3_f32 v188, v188, v68, v69\n"
      "v_max3_f32 v189, v189, v76, v77\n"
      "v_max3_f32 v190, v190, v84, v85\n"
      "v_max3_f32 v191, v191, v92, v93\n"
      "v_max3_f32 v188, v188, v70, v71\n"
      "v_max3_f32 v189, v189, v78, v79\n"
      "v_max3_f32 v190, v190, v86, v87\n"
      "v_max3_f32 v191, v191, v94, v95\n"
      "v_mov_b32 v192, v188\n"
      "v_mov_b32 v193, v189\n"
      "v_mov_b32 v194, v190\n"
      "v_mov_b32 v195, v191\n"
      "v_permlane16_swap_b32 v188, v192\n"
      "v_permlane16_swap_b32 v189, v193\n"
      "v_permlane16_swap_b32 v190, v194\n"
      "v_permlane16_swap_b32 v191, v195\n"
      "v_max_f32 v188, v188, v192\n"
      "v_max_f32 v189, v189, v193\n"
      "v_max_f32 v190, v190, v194\n"
      "v_max_f32 v191, v191, v195\n"
      "v_mov_b32 v192, v188\n"
      "v_mov_b32 v193, v189\n"
      "v_mov_b32 v194, v190\n"
      "v_mov_b32 v195, v191\n"
      "v_permlane32_swap_b32 v188, v192\n"
      "v_permlane32_swap_b32 v189, v193\n"
      "v_permlane32_swap_b32 v190, v194\n"
      "v_permlane32_swap_b32 v191, v195\n"
      "v_max_f32 v188, v188, v192\n"
      "v_max_f32 v189, v189, v193\n"
      "v_max_f32 v190, v190, v194\n"
      "v_max_f32 v191, v191, v195\n"
      "RS0_%=:\n"
      "v_exp_f32 v192, -v188\n"
      "v_exp_f32 v193, -v189\n"
      "v_exp_f32 v194, -v190\n"
      "v_exp_f32 v195, -v191\n"
      "v_sub_f32 v176, v176, v188\n"
      "v_sub_f32 v177, v177, v189\n"
      "v_sub_f32 v178, v178, v190\n"
      "v_sub_f32 v179, v179, v191\n"
      "v_mov_b32 v160, v176\n"
      "v_mov_b32 v161, v176\n"
      "v_mov_b32 v162, v176\n"
      "v_mov_b32 v163, v176\n"
      "v_mov_b32 v164, v177\n"
      "v_mov_b32 v165, v177\n"
      "v_mov_b32 v166, v177\n"
      "v_mov_b32 v167, v177\n"
      "v_mov_b32 v168, v178\n"
      "v_mov_b32 v169, v178\n"
      "v_mov_b32 v170, v178\n"
      "v_mov_b32 v171, v178\n"
      "v_mov_b32 v172, v179\n"
      "v_mov_b32 v173, v179\n"
      "v_mov_b32 v174, v179\n"
      "v_mov_b32 v175, v179\n"
      "v_sub_f32 v64, v64, v188\n"
      "v_sub_f32 v65, v65, v188\n"
      "v_sub_f32 v66, v66, v188\n"
      "v_sub_f32 v67, v67, v188\n"
      "v_sub_f32 v68, v68, v188\n"
      "v_sub_f32 v69, v69, v188\n"
      "v_sub_f32 v70, v70, v188\n"
      "v_sub_f32 v71, v71, v188\n"
      "v_sub_f32 v72, v72, v189\n"
      "v_sub_f32 v73, v73, v189\n"
      "v_sub_f32 v74, v74, v189\n"
      "v_sub_f32 v75, v75, v189\n"
      "v_sub_f32 v76, v76, v189\n"
      "v_sub_f32 v77, v77, v189\n"
      "v_sub_f32 v78, v78, v189\n"
      "v_sub_f32 v79, v79, v189\n"
      "v_sub_f32 v80, v80, v190\n"
      "v_sub_f32 v81, v81, v190\n"
      "v_sub_f32 v82, v82, v190\n"
      "v_sub_f32 v83, v83, v190\n"
      "v_sub_f32 v84, v84, v190\n"
      "v_sub_f32 v85, v85, v190\n"
      "v_sub_f32 v86, v86, v190\n"
      "v_sub_f32 v87, v87, v190\n"
      "v_sub_f32 v88, v88, v191\n"
      "v_sub_f32 v89, v89, v191\n"
      "v_sub_f32 v90, v90, v191\n"
      "v_sub_f32 v91, v91, v191\n"
      "v_sub_f32 v92, v92, v191\n"
      "v_sub_f32 v93, v93, v191\n"
      "v_sub_f32 v94, v94, v191\n"
      "v_sub_f32 v95, v95, v191\n"
      "v_sub_f32 v96, v96, v188\n"
      "v_sub_f32 v97, v97, v188\n"
      "v_sub_f32 v98, v98, v188\n"
      "v_sub_f32 v99, v99, v188\n"
      "v_sub_f32 v100, v100, v188\n"
      "v_sub_f32 v101, v101, v188\n"
      "v_sub_f32 v102, v102, v188\n"
      "v_sub_f32 v103, v103, v188\n"
      "v_sub_f32 v104, v104, v189\n"
      "v_sub_f32 v105, v105, v189\n"
      "v_sub_f32 v106, v106, v189\n"
      "v_sub_f32 v107, v107, v189\n"
      "v_sub_f32 v108, v108, v189\n"
      "v_sub_f32 v109, v109, v189\n"
      "v_sub_f32 v110, v110, v189\n"
      "v_sub_f32 v111, v111, v189\n"
      "v_sub_f32 v112, v112, v190\n"
      "v_sub_f32 v113, v113, v190\n"
      "v_sub_f32 v114, v114, v190\n"
      "v_sub_f32 v115, v115, v190\n"
      "v_sub_f32 v116, v116, v190\n"
      "v_sub_f32 v117, v117, v190\n"
      "v_sub_f32 v118, v118, v190\n"
      "v_sub_f32 v119, v119, v190\n"
      "v_sub_f32 v120, v120, v191\n"
      "v_sub_f32 v121, v121, v191\n"
      "v_sub_f32 v122, v122, v191\n"
      "v_sub_f32 v123, v123, v191\n"
      "v_sub_f32 v124, v124, v191\n"
      "v_sub_f32 v125, v125, v191\n"
      "v_sub_f32 v126, v126, v191\n"
      "v_sub_f32 v127, v127, v191\n"
      "v_mul_f32 v0, v0, v192\n"
      "v_mul_f32 v1, v1, v192\n"
      "v_mul_f32 v2, v2, v192\n"
      "v_mul_f32 v3, v3, v192\n"
      "v_mul_f32 v4, v4, v193\n"
      "v_mul_f32 v5, v5, v193\n"
      "v_mul_f32 v6, v6, v193\n"
      "v_mul_f32 v7, v7, v193\n"
      "v_mul_f32 v8, v8, v192\n"
      "v_mul_f32 v9, v9, v192\n"
      "v_mul_f32 v10, v10, v192\n"
      "v_mul_f32 v11, v11, v192\n"
      "v_mul_f32 v12, v12, v193\n"
      "v_mul_f32 v13, v13, v193\n"
      "v_mul_f32 v14, v14, v193\n"
      "v_mul_f32 v15, v15, v193\n"
      "v_mul_f32 v16, v16, v192\n"
      "v_mul_f32 v17, v17, v192\n"
      "v_mul_f32 v18, v18, v192\n"
      "v_mul_f32 v19, v19, v192\n"
      "v_mul_f32 v20, v20, v193\n"
      "v_mul_f32 v21, v21, v193\n"
      "v_mul_f32 v22, v22, v193\n"
      "v_mul_f32 v23, v23, v193\n"
      "v_mul_f32 v24, v24, v192\n"
      "v_mul_f32 v25, v25, v192\n"
      "v_mul_f32 v26, v26, v192\n"
      "v_mul_f32 v27, v27, v192\n"
      "v_mul_f32 v28, v28, v193\n"
      "v_mul_f32 v29, v29, v193\n"
      "v_mul_f32 v30, v30, v193\n"
      "v_mul_f32 v31, v31, v193\n"
      "v_mul_f32 v32, v32, v192\n"
      "v_mul_f32 v33, v33, v192\n"
      "v_mul_f32 v34, v34, v192\n"
      "v_mul_f32 v35, v35, v192\n"
      "v_mul_f32 v36, v36, v193\n"
      "v_mul_f32 v37, v37, v193\n"
      "v_mul_f32 v38, v38, v193\n"
      "v_mul_f32 v39, v39, v193\n"
      "v_mul_f32 v40, v40, v192\n"
      "v_mul_f32 v41, v41, v192\n"
      "v_mul_f32 v42, v42, v192\n"
      "v_mul_f32 v43, v43, v192\n"
      "v_mul_f32 v44, v44, v193\n"
      "v_mul_f32 v45, v45, v193\n"
      "v_mul_f32 v46, v46, v193\n"
      "v_mul_f32 v47, v47, v193\n"
      "v_mul_f32 v48, v48, v192\n"
      "v_mul_f32 v49, v49, v192\n"
      "v_mul_f32 v50, v50, v192\n"
      "v_mul_f32 v51, v51, v192\n"
      "v_mul_f32 v52, v52, v193\n"
      "v_mul_f32 v53, v53, v193\n"
      "v_mul_f32 v54, v54, v193\n"
      "v_mul_f32 v55, v55, v193\n"
      "v_mul_f32 v56, v56, v192\n"
      "v_mul_f32 v57, v57, v192\n"
      "v_mul_f32 v58, v58, v192\n"
      "v_mul_f32 v59, v59, v192\n"
      "v_mul_f32 v60, v60, v193\n"
      "v_mul_f32 v61, v61, v193\n"
      "v_mul_f32 v62, v62, v193\n"
      "v_mul_f32 v63, v63, v193\n"
      "v_accvgpr_read_b32 v196, a144\n"
      "v_accvgpr_read_b32 v197, a145\n"
      "v_accvgpr_read_b32 v198, a146\n"
      "v_accvgpr_read_b32 v199, a147\n"
      "v_mul_f32 v196, v196, v194\n"
      "v_mul_f32 v197, v197, v194\n"
      "v_mul_f32 v198, v198, v194\n"
      "v_mul_f32 v199, v199, v194\n"
      "v_accvgpr_write_b32 a144, v196\n"
      "v_accvgpr_write_b32 a145, v197\n"
      "v_accvgpr_write_b32 a146, v198\n"
      "v_accvgpr_write_b32 a147, v199\n"
      "v_accvgpr_read_b32 v196, a148\n"
      "v_accvgpr_read_b32 v197, a149\n"
      "v_accvgpr_read_b32 v198, a150\n"
      "v_accvgpr_read_b32 v199, a151\n"
      "v_mul_f32 v196, v196, v195\n"
      "v_mul_f32 v197, v197, v195\n"
      "v_mul_f32 v198, v198, v195\n"
      "v_mul_f32 v199, v199, v195\n"
      "v_accvgpr_write_b32 a148, v196\n"
      "v_accvgpr_write_b32 a149, v197\n"
      "v_accvgpr_write_b32 a150, v198\n"
      "v_accvgpr_write_b32 a151, v199\n"
      "v_accvgpr_read_b32 v196, a152\n"
      "v_accvgpr_read_b32 v197, a153\n"
      "v_accvgpr_read_b32 v198, a154\n"
      "v_accvgpr_read_b32 v199, a155\n"
      "v_mul_f32 v196, v196, v194\n"
      "v_mul_f32 v197, v197, v194\n"
      "v_mul_f32 v198, v198, v194\n"
      "v_mul_f32 v199, v199, v194\n"
      "v_accvgpr_write_b32 a152, v196\n"
      "v_accvgpr_write_b32 a153, v197\n"
      "v_accvgpr_write_b32 a154, v198\n"
      "v_accvgpr_write_b32 a155, v199\n"
      "v_accvgpr_read_b32 v196, a156\n"
      "v_accvgpr_read_b32 v197, a157\n"
      "v_accvgpr_read_b32 v198, a158\n"
      "v_accvgpr_read_b32 v199, a159\n"
      "v_mul_f32 v196, v196, v195\n"
      "v_mul_f32 v197, v197, v195\n"
      "v_mul_f32 v198, v198, v195\n"
      "v_mul_f32 v199, v199, v195\n"
      "v_accvgpr_write_b32 a156, v196\n"
      "v_accvgpr_write_b32 a157, v197\n"
      "v_accvgpr_write_b32 a158, v198\n"
      "v_accvgpr_write_b32 a159, v199\n"
      "v_accvgpr_read_b32 v196, a160\n"
      "v_accvgpr_read_b32 v197, a161\n"
      "v_accvgpr_read_b32 v198, a162\n"
      "v_accvgpr_read_b32 v199, a163\n"
      "v_mul_f32 v196, v196, v194\n"
      "v_mul_f32 v197, v197, v194\n"
      "v_mul_f32 v198, v198, v194\n"
      "v_mul_f32 v199, v199, v194\n"
      "v_accvgpr_write_b32 a160, v196\n"
      "v_accvgpr_write_b32 a161, v197\n"
      "v_accvgpr_write_b32 a162, v198\n"
      "v_accvgpr_write_b32 a163, v199\n"
      "v_accvgpr_read_b32 v196, a164\n"
      "v_accvgpr_read_b32 v197, a165\n"
      "v_accvgpr_read_b32 v198, a166\n"
      "v_accvgpr_read_b32 v199, a167\n"
      "v_mul_f32 v196, v196, v195\n"
      "v_mul_f32 v197, v197, v195\n"
      "v_mul_f32 v198, v198, v195\n"
      "v_mul_f32 v199, v199, v195\n"
      "v_accvgpr_write_b32 a164, v196\n"
      "v_accvgpr_write_b32 a165, v197\n"
      "v_accvgpr_write_b32 a166, v198\n"
      "v_accvgpr_write_b32 a167, v199\n"
      "v_accvgpr_read_b32 v196, a168\n"
      "v_accvgpr_read_b32 v197, a169\n"
      "v_accvgpr_read_b32 v198, a170\n"
      "v_accvgpr_read_b32 v199, a171\n"
      "v_mul_f32 v196, v196, v194\n"
      "v_mul_f32 v197, v197, v194\n"
      "v_mul_f32 v198, v198, v194\n"
      "v_mul_f32 v199, v199, v194\n"
      "v_accvgpr_write_b32 a168, v196\n"
      "v_accvgpr_write_b32 a169, v197\n"
      "v_accvgpr_write_b32 a170, v198\n"
      "v_accvgpr_write_b32 a171, v199\n"
      "v_accvgpr_read_b32 v196, a172\n"
      "v_accvgpr_read_b32 v197, a173\n"
      "v_accvgpr_read_b32 v198, a174\n"
      "v_accvgpr_read_b32 v199, a175\n"
      "v_mul_f32 v196, v196, v195\n"
      "v_mul_f32 v197, v197, v195\n"
      "v_mul_f32 v198, v198, v195\n"
      "v_mul_f32 v199, v199, v195\n"
      "v_accvgpr_write_b32 a172, v196\n"
      "v_accvgpr_write_b32 a173, v197\n"
      "v_accvgpr_write_b32 a174, v198\n"
      "v_accvgpr_write_b32 a175, v199\n"
      "v_accvgpr_read_b32 v196, a176\n"
      "v_accvgpr_read_b32 v197, a177\n"
      "v_accvgpr_read_b32 v198, a178\n"
      "v_accvgpr_read_b32 v199, a179\n"
      "v_mul_f32 v196, v196, v194\n"
      "v_mul_f32 v197, v197, v194\n"
      "v_mul_f32 v198, v198, v194\n"
      "v_mul_f32 v199, v199, v194\n"
      "v_accvgpr_write_b32 a176, v196\n"
      "v_accvgpr_write_b32 a177, v197\n"
      "v_accvgpr_write_b32 a178, v198\n"
      "v_accvgpr_write_b32 a179, v199\n"
      "v_accvgpr_read_b32 v196, a180\n"
      "v_accvgpr_read_b32 v197, a181\n"
      "v_accvgpr_read_b32 v198, a182\n"
      "v_accvgpr_read_b32 v199, a183\n"
      "v_mul_f32 v196, v196, v195\n"
      "v_mul_f32 v197, v197, v195\n"
      "v_mul_f32 v198, v198, v195\n"
      "v_mul_f32 v199, v199, v195\n"
      "v_accvgpr_write_b32 a180, v196\n"
      "v_accvgpr_write_b32 a181, v197\n"
      "v_accvgpr_write_b32 a182, v198\n"
      "v_accvgpr_write_b32 a183, v199\n"
      "v_accvgpr_read_b32 v196, a184\n"
      "v_accvgpr_read_b32 v197, a185\n"
      "v_accvgpr_read_b32 v198, a186\n"
      "v_accvgpr_read_b32 v199, a187\n"
      "v_mul_f32 v196, v196, v194\n"
      "v_mul_f32 v197, v197, v194\n"
      "v_mul_f32 v198, v198, v194\n"
      "v_mul_f32 v199, v199, v194\n"
      "v_accvgpr_write_b32 a184, v196\n"
      "v_accvgpr_write_b32 a185, v197\n"
      "v_accvgpr_write_b32 a186, v198\n"
      "v_accvgpr_write_b32 a187, v199\n"
      "v_accvgpr_read_b32 v196, a188\n"
      "v_accvgpr_read_b32 v197, a189\n"
      "v_accvgpr_read_b32 v198, a190\n"
      "v_accvgpr_read_b32 v199, a191\n"
      "v_mul_f32 v196, v196, v195\n"
      "v_mul_f32 v197, v197, v195\n"
      "v_mul_f32 v198, v198, v195\n"
      "v_mul_f32 v199, v199, v195\n"
      "v_accvgpr_write_b32 a188, v196\n"
      "v_accvgpr_write_b32 a189, v197\n"
      "v_accvgpr_write_b32 a190, v198\n"
      "v_accvgpr_write_b32 a191, v199\n"
      "v_accvgpr_read_b32 v196, a192\n"
      "v_accvgpr_read_b32 v197, a193\n"
      "v_accvgpr_read_b32 v198, a194\n"
      "v_accvgpr_read_b32 v199, a195\n"
      "v_mul_f32 v196, v196, v194\n"
      "v_mul_f32 v197, v197, v194\n"
      "v_mul_f32 v198, v198, v194\n"
      "v_mul_f32 v199, v199, v194\n"
      "v_accvgpr_write_b32 a192, v196\n"
      "v_accvgpr_write_b32 a193, v197\n"
      "v_accvgpr_write_b32 a194, v198\n"
      "v_accvgpr_write_b32 a195, v199\n"
      "v_accvgpr_read_b32 v196, a196\n"
      "v_accvgpr_read_b32 v197, a197\n"
      "v_accvgpr_read_b32 v198, a198\n"
      "v_accvgpr_read_b32 v199, a199\n"
      "v_mul_f32 v196, v196, v195\n"
      "v_mul_f32 v197, v197, v195\n"
      "v_mul_f32 v198, v198, v195\n"
      "v_mul_f32 v199, v199, v195\n"
      "v_accvgpr_write_b32 a196, v196\n"
      "v_accvgpr_write_b32 a197, v197\n"
      "v_accvgpr_write_b32 a198, v198\n"
      "v_accvgpr_write_b32 a199, v199\n"
      "v_accvgpr_read_b32 v196, a200\n"
      "v_accvgpr_read_b32 v197, a201\n"
      "v_accvgpr_read_b32 v198, a202\n"
      "v_accvgpr_read_b32 v199, a203\n"
      "v_mul_f32 v196, v196, v194\n"
      "v_mul_f32 v197, v197, v194\n"
      "v_mul_f32 v198, v198, v194\n"
      "v_mul_f32 v199, v199, v194\n"
      "v_accvgpr_write_b32 a200, v196\n"
      "v_accvgpr_write_b32 a201, v197\n"
      "v_accvgpr_write_b32 a202, v198\n"
      "v_accvgpr_write_b32 a203, v199\n"
      "v_accvgpr_read_b32 v196, a204\n"
      "v_accvgpr_read_b32 v197, a205\n"
      "v_accvgpr_read_b32 v198, a206\n"
      "v_accvgpr_read_b32 v199, a207\n"
      "v_mul_f32 v196, v196, v195\n"
      "v_mul_f32 v197, v197, v195\n"
      "v_mul_f32 v198, v198, v195\n"
      "v_mul_f32 v199, v199, v195\n"
      "v_accvgpr_write_b32 a204, v196\n"
      "v_accvgpr_write_b32 a205, v197\n"
      "v_accvgpr_write_b32 a206, v198\n"
      "v_accvgpr_write_b32 a207, v199\n"
      "v_mul_f32 v180, v180, v192\n"
      "v_mul_f32 v181, v181, v193\n"
      "v_mul_f32 v182, v182, v194\n"
      "v_mul_f32 v183, v183, v195\n"
      "v_accvgpr_read_b32 v197, a208\n"
      "v_lshlrev_b32 v196, 16, v197\n"
      "v_and_b32 v197, 0xffff0000, v197\n"
      "v_mul_f32 v196, v196, v192\n"
      "v_mul_f32 v197, v197, v192\n"
      "v_cvt_pk_bf16_f32 v196, v196, v197\n"
      "v_accvgpr_write_b32 a208, v196\n"
      "v_accvgpr_read_b32 v197, a209\n"
      "v_lshlrev_b32 v196, 16, v197\n"
      "v_and_b32 v197, 0xffff0000, v197\n"
      "v_mul_f32 v196, v196, v192\n"
      "v_mul_f32 v197, v197, v192\n"
      "v_cvt_pk_bf16_f32 v196, v196, v197\n"
      "v_accvgpr_write_b32 a209, v196\n"
      "v_accvgpr_read_b32 v197, a210\n"
      "v_lshlrev_b32 v196, 16, v197\n"
      "v_and_b32 v197, 0xffff0000, v197\n"
      "v_mul_f32 v196, v196, v192\n"
      "v_mul_f32 v197, v197, v192\n"
      "v_cvt_pk_bf16_f32 v196, v196, v197\n"
      "v_accvgpr_write_b32 a210, v196\n"
      "v_accvgpr_read_b32 v197, a211\n"
      "v_lshlrev_b32 v196, 16, v197\n"
      "v_and_b32 v197, 0xffff0000, v197\n"
      "v_mul_f32 v196, v196, v192\n"
      "v_mul_f32 v197, v197, v192\n"
      "v_cvt_pk_bf16_f32 v196, v196, v197\n"
      "v_accvgpr_write_b32 a211, v196\n"
      "v_accvgpr_read_b32 v197, a212\n"
      "v_lshlrev_b32 v196, 16, v197\n"
      "v_and_b32 v197, 0xffff0000, v197\n"
      "v_mul_f32 v196, v196, v193\n"
      "v_mul_f32 v197, v197, v193\n"
      "v_cvt_pk_bf16_f32 v196, v196, v197\n"
      "v_accvgpr_write_b32 a212, v196\n"
      "v_accvgpr_read_b32 v197, a213\n"
      "v_lshlrev_b32 v196, 16, v197\n"
      "v_and_b32 v197, 0xffff0000, v197\n"
      "v_mul_f32 v196, v196, v193\n"
      "v_mul_f32 v197, v197, v193\n"
      "v_cvt_pk_bf16_f32 v196, v196, v197\n"
      "v_accvgpr_write_b32 a213, v196\n"
      "v_accvgpr_read_b32 v197, a214\n"
      "v_lshlrev_b32 v196, 16, v197\n"
      "v_and_b32 v197, 0xffff0000, v197\n"
      "v_mul_f32 v196, v196, v193\n"
      "v_mul_f32 v197, v197, v193\n"
      "v_cvt_pk_bf16_f32 v196, v196, v197\n"
      "v_accvgpr_write_b32 a214, v196\n"
      "v_accvgpr_read_b32 v197, a215\n"
      "v_lshlrev_b32 v196, 16, v197\n"
      "v_and_b32 v197, 0xffff0000, v197\n"
      "v_mul_f32 v196, v196, v193\n"
      "v_mul_f32 v197, v197, v193\n"
      "v_cvt_pk_bf16_f32 v196, v196, v197\n"
      "v_accvgpr_write_b32 a215, v196\n"
      "v_accvgpr_read_b32 v197, a216\n"
      "v_lshlrev_b32 v196, 16, v197\n"
      "v_and_b32 v197, 0xffff0000, v197\n"
      "v_mul_f32 v196, v196, v194\n"
      "v_mul_f32 v197, v197, v194\n"
      "v_cvt_pk_bf16_f32 v196, v196, v197\n"
      "v_accvgpr_write_b32 a216, v196\n"
      "v_accvgpr_read_b32 v197, a217\n"
      "v_lshlrev_b32 v196, 16, v197\n"
      "v_and_b32 v197, 0xffff0000, v197\n"
      "v_mul_f32 v196, v196, v194\n"
      "v_mul_f32 v197, v197, v194\n"
      "v_cvt_pk_bf16_f32 v196, v196, v197\n"
      "v_accvgpr_write_b32 a217, v196\n"
      "v_accvgpr_read_b32 v197, a218\n"
      "v_lshlrev_b32 v196, 16, v197\n"
      "v_and_b32 v197, 0xffff0000, v197\n"
      "v_mul_f32 v196, v196, v194\n"
      "v_mul_f32 v197, v197, v194\n"
      "v_cvt_pk_bf16_f32 v196, v196, v197\n"
      "v_accvgpr_write_b32 a218, v196\n"
      "v_accvgpr_read_b32 v197, a219\n"
      "v_lshlrev_b32 v196, 16, v197\n"
      "v_and_b32 v197, 0xffff0000, v197\n"
      "v_mul_f32 v196, v196, v194\n"
      "v_mul_f32 v197, v197, v194\n"
      "v_cvt_pk_bf16_f32 v196, v196, v197\n"
      "v_accvgpr_write_b32 a219, v196\n"
      "v_accvgpr_read_b32 v197, a220\n"
      "v_lshlrev_b32 v196, 16, v197\n"
      "v_and_b32 v197, 0xffff0000, v197\n"
      "v_mul_f32 v196, v196, v195\n"
      "v_mul_f32 v197, v197, v195\n"
      "v_cvt_pk_bf16_f32 v196, v196, v197\n"
      "v_accvgpr_write_b32 a220, v196\n"
      "v_accvgpr_read_b32 v197, a221\n"
      "v_lshlrev_b32 v196, 16, v197\n"
      "v_and_b32 v197, 0xffff0000, v197\n"
      "v_mul_f32 v196, v196, v195\n"
      "v_mul_f32 v197, v197, v195\n"
      "v_cvt_pk_bf16_f32 v196, v196, v197\n"
      "v_accvgpr_write_b32 a221, v196\n"
      "v_accvgpr_read_b32 v197, a222\n"
      "v_lshlrev_b32 v196, 16, v197\n"
      "v_and_b32 v197, 0xffff0000, v197\n"
      "v_mul_f32 v196, v196, v195\n"
      "v_mul_f32 v197, v197, v195\n"
      "v_cvt_pk_bf16_f32 v196, v196, v197\n"
      "v_accvgpr_write_b32 a222, v196\n"
      "v_accvgpr_read_b32 v197, a223\n"
      "v_lshlrev_b32 v196, 16, v197\n"
      "v_and_b32 v197, 0xffff0000, v197\n"
      "v_mul_f32 v196, v196, v195\n"
      "v_mul_f32 v197, v197, v195\n"
      "v_cvt_pk_bf16_f32 v196, v196, v197\n"
      "v_accvgpr_write_b32 a223, v196\n"
      "v_exp_f32 v188, v64\n"
      "v_exp_f32 v189, v65\n"
      "v_exp_f32 v190, v66\n"
      "v_exp_f32 v191, v67\n"
      "v_cvt_pk_bf16_f32 v128, v188, v189\n"
      "v_cvt_pk_bf16_f32 v129, v190, v191\n"
      "v_exp_f32 v192, v68\n"
      "v_exp_f32 v193, v69\n"
      "v_exp_f32 v194, v70\n"
      "v_exp_f32 v195, v71\n"
      "v_cvt_pk_bf16_f32 v130, v192, v193\n"
      "v_cvt_pk_bf16_f32 v131, v194, v195\n"
      "v_exp_f32 v188, v72\n"
      "v_exp_f32 v189, v73\n"
      "v_exp_f32 v190, v74\n"
      "v_exp_f32 v191, v75\n"
      "v_cvt_pk_bf16_f32 v132, v188, v189\n"
      "v_cvt_pk_bf16_f32 v133, v190, v191\n"
      "v_exp_f32 v192, v76\n"
      "v_exp_f32 v193, v77\n"
      "v_exp_f32 v194, v78\n"
      "v_exp_f32 v195, v79\n"
      "v_cvt_pk_bf16_f32 v134, v192, v193\n"
      "v_cvt_pk_bf16_f32 v135, v194, v195\n"
      "v_exp_f32 v188, v80\n"
      "v_exp_f32 v189, v81\n"
      "v_exp_f32 v190, v82\n"
      "v_exp_f32 v191, v83\n"
      "v_cvt_pk_bf16_f32 v136, v188, v189\n"
      "v_cvt_pk_bf16_f32 v137, v190, v191\n"
      "v_exp_f32 v192, v84\n"
      "v_exp_f32 v193, v85\n"
      "v_exp_f32 v194, v86\n"
      "v_exp_f32 v195, v87\n"
      "v_cvt_pk_bf16_f32 v138, v192, v193\n"
      "v_cvt_pk_bf16_f32 v139, v194, v195\n"
      "v_exp_f32 v188, v88\n"
      "v_exp_f32 v189, v89\n"
      "v_exp_f32 v190, v90\n"
      "v_exp_f32 v191, v91\n"
      "v_cvt_pk_bf16_f32 v140, v188, v189\n"
      "v_cvt_pk_bf16_f32 v141, v190, v191\n"
      "v_exp_f32 v192, v92\n"
      "v_exp_f32 v193, v93\n"
      "v_exp_f32 v194, v94\n"
      "v_exp_f32 v195, v95\n"
      "v_cvt_pk_bf16_f32 v142, v192, v193\n"
      "v_cvt_pk_bf16_f32 v143, v194, v195\n"
      "s_nop 1\n"
      "v_mfma_f32_16x16x32_bf16 v[184:187], a[128:131], v[128:131], 0\n"
      "v_mfma_f32_16x16x32_bf16 v[184:187], a[132:135], v[132:135], v[184:187]\n"
      "v_mfma_f32_16x16x32_bf16 v[184:187], a[136:139], v[136:139], v[184:187]\n"
      "v_mfma_f32_16x16x32_bf16 v[184:187], a[140:143], v[140:143], v[184:187]\n"
      "s_nop 7\n"
      "s_setpc_b64 s[96:97]\n"
      "FB1_%=:\n"
      "v_mov_b32 v196, 139248\n"
      "s_nop 7\n"
      "ds_write_b32 v196, v196\n"
      "v_max3_f32 v188, v96, v97, 0\n"
      "v_max3_f32 v189, v104, v105, 0\n"
      "v_max3_f32 v190, v112, v113, 0\n"
      "v_max3_f32 v191, v120, v121, 0\n"
      "v_max3_f32 v188, v188, v98, v99\n"
      "v_max3_f32 v189, v189, v106, v107\n"
      "v_max3_f32 v190, v190, v114, v115\n"
      "v_max3_f32 v191, v191, v122, v123\n"
      "v_max3_f32 v188, v188, v100, v101\n"
      "v_max3_f32 v189, v189, v108, v109\n"
      "v_max3_f32 v190, v190, v116, v117\n"
      "v_max3_f32 v191, v191, v124, v125\n"
      "v_max3_f32 v188, v188, v102, v103\n"
      "v_max3_f32 v189, v189, v110, v111\n"
      "v_max3_f32 v190, v190, v118, v119\n"
      "v_max3_f32 v191, v191, v126, v127\n"
      "v_mov_b32 v192, v188\n"
      "v_mov_b32 v193, v189\n"
      "v_mov_b32 v194, v190\n"
      "v_mov_b32 v195, v191\n"
      "v_permlane16_swap_b32 v188, v192\n"
      "v_permlane16_swap_b32 v189, v193\n"
      "v_permlane16_swap_b32 v190, v194\n"
      "v_permlane16_swap_b32 v191, v195\n"
      "v_max_f32 v188, v188, v192\n"
      "v_max_f32 v189, v189, v193\n"
      "v_max_f32 v190, v190, v194\n"
      "v_max_f32 v191, v191, v195\n"
      "v_mov_b32 v192, v188\n"
      "v_mov_b32 v193, v189\n"
      "v_mov_b32 v194, v190\n"
      "v_mov_b32 v195, v191\n"
      "v_permlane32_swap_b32 v188, v192\n"
      "v_permlane32_swap_b32 v189, v193\n"
      "v_permlane32_swap_b32 v190, v194\n"
      "v_permlane32_swap_b32 v191, v195\n"
      "v_max_f32 v188, v188, v192\n"
      "v_max_f32 v189, v189, v193\n"
      "v_max_f32 v190, v190, v194\n"
      "v_max_f32 v191, v191, v195\n"
      "RS1_%=:\n"
      "v_exp_f32 v192, -v188\n"
      "v_exp_f32 v193, -v189\n"
      "v_exp_f32 v194, -v190\n"
      "v_exp_f32 v195, -v191\n"
      "v_sub_f32 v176, v176, v188\n"
      "v_sub_f32 v177, v177, v189\n"
      "v_sub_f32 v178, v178, v190\n"
      "v_sub_f32 v179, v179, v191\n"
      "v_mov_b32 v160, v176\n"
      "v_mov_b32 v161, v176\n"
      "v_mov_b32 v162, v176\n"
      "v_mov_b32 v163, v176\n"
      "v_mov_b32 v164, v177\n"
      "v_mov_b32 v165, v177\n"
      "v_mov_b32 v166, v177\n"
      "v_mov_b32 v167, v177\n"
      "v_mov_b32 v168, v178\n"
      "v_mov_b32 v169, v178\n"
      "v_mov_b32 v170, v178\n"
      "v_mov_b32 v171, v178\n"
      "v_mov_b32 v172, v179\n"
      "v_mov_b32 v173, v179\n"
      "v_mov_b32 v174, v179\n"
      "v_mov_b32 v175, v179\n"
      "v_sub_f32 v64, v64, v188\n"
      "v_sub_f32 v65, v65, v188\n"
      "v_sub_f32 v66, v66, v188\n"
      "v_sub_f32 v67, v67, v188\n"
      "v_sub_f32 v68, v68, v188\n"
      "v_sub_f32 v69, v69, v188\n"
      "v_sub_f32 v70, v70, v188\n"
      "v_sub_f32 v71, v71, v188\n"
      "v_sub_f32 v72, v72, v189\n"
      "v_sub_f32 v73, v73, v189\n"
      "v_sub_f32 v74, v74, v189\n"
      "v_sub_f32 v75, v75, v189\n"
      "v_sub_f32 v76, v76, v189\n"
      "v_sub_f32 v77, v77, v189\n"
      "v_sub_f32 v78, v78, v189\n"
      "v_sub_f32 v79, v79, v189\n"
      "v_sub_f32 v80, v80, v190\n"
      "v_sub_f32 v81, v81, v190\n"
      "v_sub_f32 v82, v82, v190\n"
      "v_sub_f32 v83, v83, v190\n"
      "v_sub_f32 v84, v84, v190\n"
      "v_sub_f32 v85, v85, v190\n"
      "v_sub_f32 v86, v86, v190\n"
      "v_sub_f32 v87, v87, v190\n"
      "v_sub_f32 v88, v88, v191\n"
      "v_sub_f32 v89, v89, v191\n"
      "v_sub_f32 v90, v90, v191\n"
      "v_sub_f32 v91, v91, v191\n"
      "v_sub_f32 v92, v92, v191\n"
      "v_sub_f32 v93, v93, v191\n"
      "v_sub_f32 v94, v94, v191\n"
      "v_sub_f32 v95, v95, v191\n"
      "v_sub_f32 v96, v96, v188\n"
      "v_sub_f32 v97, v97, v188\n"
      "v_sub_f32 v98, v98, v188\n"
      "v_sub_f32 v99, v99, v188\n"
      "v_sub_f32 v100, v100, v188\n"
      "v_sub_f32 v101, v101, v188\n"
      "v_sub_f32 v102, v102, v188\n"
      "v_sub_f32 v103, v103, v188\n"
      "v_sub_f32 v104, v104, v189\n"
      "v_sub_f32 v105, v105, v189\n"
      "v_sub_f32 v106, v106, v189\n"
      "v_sub_f32 v107, v107, v189\n"
      "v_sub_f32 v108, v108, v189\n"
      "v_sub_f32 v109, v109, v189\n"
      "v_sub_f32 v110, v110, v189\n"
      "v_sub_f32 v111, v111, v189\n"
      "v_sub_f32 v112, v112, v190\n"
      "v_sub_f32 v113, v113, v190\n"
      "v_sub_f32 v114, v114, v190\n"
      "v_sub_f32 v115, v115, v190\n"
      "v_sub_f32 v116, v116, v190\n"
      "v_sub_f32 v117, v117, v190\n"
      "v_sub_f32 v118, v118, v190\n"
      "v_sub_f32 v119, v119, v190\n"
      "v_sub_f32 v120, v120, v191\n"
      "v_sub_f32 v121, v121, v191\n"
      "v_sub_f32 v122, v122, v191\n"
      "v_sub_f32 v123, v123, v191\n"
      "v_sub_f32 v124, v124, v191\n"
      "v_sub_f32 v125, v125, v191\n"
      "v_sub_f32 v126, v126, v191\n"
      "v_sub_f32 v127, v127, v191\n"
      "v_mul_f32 v0, v0, v192\n"
      "v_mul_f32 v1, v1, v192\n"
      "v_mul_f32 v2, v2, v192\n"
      "v_mul_f32 v3, v3, v192\n"
      "v_mul_f32 v4, v4, v193\n"
      "v_mul_f32 v5, v5, v193\n"
      "v_mul_f32 v6, v6, v193\n"
      "v_mul_f32 v7, v7, v193\n"
      "v_mul_f32 v8, v8, v192\n"
      "v_mul_f32 v9, v9, v192\n"
      "v_mul_f32 v10, v10, v192\n"
      "v_mul_f32 v11, v11, v192\n"
      "v_mul_f32 v12, v12, v193\n"
      "v_mul_f32 v13, v13, v193\n"
      "v_mul_f32 v14, v14, v193\n"
      "v_mul_f32 v15, v15, v193\n"
      "v_mul_f32 v16, v16, v192\n"
      "v_mul_f32 v17, v17, v192\n"
      "v_mul_f32 v18, v18, v192\n"
      "v_mul_f32 v19, v19, v192\n"
      "v_mul_f32 v20, v20, v193\n"
      "v_mul_f32 v21, v21, v193\n"
      "v_mul_f32 v22, v22, v193\n"
      "v_mul_f32 v23, v23, v193\n"
      "v_mul_f32 v24, v24, v192\n"
      "v_mul_f32 v25, v25, v192\n"
      "v_mul_f32 v26, v26, v192\n"
      "v_mul_f32 v27, v27, v192\n"
      "v_mul_f32 v28, v28, v193\n"
      "v_mul_f32 v29, v29, v193\n"
      "v_mul_f32 v30, v30, v193\n"
      "v_mul_f32 v31, v31, v193\n"
      "v_mul_f32 v32, v32, v192\n"
      "v_mul_f32 v33, v33, v192\n"
      "v_mul_f32 v34, v34, v192\n"
      "v_mul_f32 v35, v35, v192\n"
      "v_mul_f32 v36, v36, v193\n"
      "v_mul_f32 v37, v37, v193\n"
      "v_mul_f32 v38, v38, v193\n"
      "v_mul_f32 v39, v39, v193\n"
      "v_mul_f32 v40, v40, v192\n"
      "v_mul_f32 v41, v41, v192\n"
      "v_mul_f32 v42, v42, v192\n"
      "v_mul_f32 v43, v43, v192\n"
      "v_mul_f32 v44, v44, v193\n"
      "v_mul_f32 v45, v45, v193\n"
      "v_mul_f32 v46, v46, v193\n"
      "v_mul_f32 v47, v47, v193\n"
      "v_mul_f32 v48, v48, v192\n"
      "v_mul_f32 v49, v49, v192\n"
      "v_mul_f32 v50, v50, v192\n"
      "v_mul_f32 v51, v51, v192\n"
      "v_mul_f32 v52, v52, v193\n"
      "v_mul_f32 v53, v53, v193\n"
      "v_mul_f32 v54, v54, v193\n"
      "v_mul_f32 v55, v55, v193\n"
      "v_mul_f32 v56, v56, v192\n"
      "v_mul_f32 v57, v57, v192\n"
      "v_mul_f32 v58, v58, v192\n"
      "v_mul_f32 v59, v59, v192\n"
      "v_mul_f32 v60, v60, v193\n"
      "v_mul_f32 v61, v61, v193\n"
      "v_mul_f32 v62, v62, v193\n"
      "v_mul_f32 v63, v63, v193\n"
      "v_accvgpr_read_b32 v196, a144\n"
      "v_accvgpr_read_b32 v197, a145\n"
      "v_accvgpr_read_b32 v198, a146\n"
      "v_accvgpr_read_b32 v199, a147\n"
      "v_mul_f32 v196, v196, v194\n"
      "v_mul_f32 v197, v197, v194\n"
      "v_mul_f32 v198, v198, v194\n"
      "v_mul_f32 v199, v199, v194\n"
      "v_accvgpr_write_b32 a144, v196\n"
      "v_accvgpr_write_b32 a145, v197\n"
      "v_accvgpr_write_b32 a146, v198\n"
      "v_accvgpr_write_b32 a147, v199\n"
      "v_accvgpr_read_b32 v196, a148\n"
      "v_accvgpr_read_b32 v197, a149\n"
      "v_accvgpr_read_b32 v198, a150\n"
      "v_accvgpr_read_b32 v199, a151\n"
      "v_mul_f32 v196, v196, v195\n"
      "v_mul_f32 v197, v197, v195\n"
      "v_mul_f32 v198, v198, v195\n"
      "v_mul_f32 v199, v199, v195\n"
      "v_accvgpr_write_b32 a148, v196\n"
      "v_accvgpr_write_b32 a149, v197\n"
      "v_accvgpr_write_b32 a150, v198\n"
      "v_accvgpr_write_b32 a151, v199\n"
      "v_accvgpr_read_b32 v196, a152\n"
      "v_accvgpr_read_b32 v197, a153\n"
      "v_accvgpr_read_b32 v198, a154\n"
      "v_accvgpr_read_b32 v199, a155\n"
      "v_mul_f32 v196, v196, v194\n"
      "v_mul_f32 v197, v197, v194\n"
      "v_mul_f32 v198, v198, v194\n"
      "v_mul_f32 v199, v199, v194\n"
      "v_accvgpr_write_b32 a152, v196\n"
      "v_accvgpr_write_b32 a153, v197\n"
      "v_accvgpr_write_b32 a154, v198\n"
      "v_accvgpr_write_b32 a155, v199\n"
      "v_accvgpr_read_b32 v196, a156\n"
      "v_accvgpr_read_b32 v197, a157\n"
      "v_accvgpr_read_b32 v198, a158\n"
      "v_accvgpr_read_b32 v199, a159\n"
      "v_mul_f32 v196, v196, v195\n"
      "v_mul_f32 v197, v197, v195\n"
      "v_mul_f32 v198, v198, v195\n"
      "v_mul_f32 v199, v199, v195\n"
      "v_accvgpr_write_b32 a156, v196\n"
      "v_accvgpr_write_b32 a157, v197\n"
      "v_accvgpr_write_b32 a158, v198\n"
      "v_accvgpr_write_b32 a159, v199\n"
      "v_accvgpr_read_b32 v196, a160\n"
      "v_accvgpr_read_b32 v197, a161\n"
      "v_accvgpr_read_b32 v198, a162\n"
      "v_accvgpr_read_b32 v199, a163\n"
      "v_mul_f32 v196, v196, v194\n"
      "v_mul_f32 v197, v197, v194\n"
      "v_mul_f32 v198, v198, v194\n"
      "v_mul_f32 v199, v199, v194\n"
      "v_accvgpr_write_b32 a160, v196\n"
      "v_accvgpr_write_b32 a161, v197\n"
      "v_accvgpr_write_b32 a162, v198\n"
      "v_accvgpr_write_b32 a163, v199\n"
      "v_accvgpr_read_b32 v196, a164\n"
      "v_accvgpr_read_b32 v197, a165\n"
      "v_accvgpr_read_b32 v198, a166\n"
      "v_accvgpr_read_b32 v199, a167\n"
      "v_mul_f32 v196, v196, v195\n"
      "v_mul_f32 v197, v197, v195\n"
      "v_mul_f32 v198, v198, v195\n"
      "v_mul_f32 v199, v199, v195\n"
      "v_accvgpr_write_b32 a164, v196\n"
      "v_accvgpr_write_b32 a165, v197\n"
      "v_accvgpr_write_b32 a166, v198\n"
      "v_accvgpr_write_b32 a167, v199\n"
      "v_accvgpr_read_b32 v196, a168\n"
      "v_accvgpr_read_b32 v197, a169\n"
      "v_accvgpr_read_b32 v198, a170\n"
      "v_accvgpr_read_b32 v199, a171\n"
      "v_mul_f32 v196, v196, v194\n"
      "v_mul_f32 v197, v197, v194\n"
      "v_mul_f32 v198, v198, v194\n"
      "v_mul_f32 v199, v199, v194\n"
      "v_accvgpr_write_b32 a168, v196\n"
      "v_accvgpr_write_b32 a169, v197\n"
      "v_accvgpr_write_b32 a170, v198\n"
      "v_accvgpr_write_b32 a171, v199\n"
      "v_accvgpr_read_b32 v196, a172\n"
      "v_accvgpr_read_b32 v197, a173\n"
      "v_accvgpr_read_b32 v198, a174\n"
      "v_accvgpr_read_b32 v199, a175\n"
      "v_mul_f32 v196, v196, v195\n"
      "v_mul_f32 v197, v197, v195\n"
      "v_mul_f32 v198, v198, v195\n"
      "v_mul_f32 v199, v199, v195\n"
      "v_accvgpr_write_b32 a172, v196\n"
      "v_accvgpr_write_b32 a173, v197\n"
      "v_accvgpr_write_b32 a174, v198\n"
      "v_accvgpr_write_b32 a175, v199\n"
      "v_accvgpr_read_b32 v196, a176\n"
      "v_accvgpr_read_b32 v197, a177\n"
      "v_accvgpr_read_b32 v198, a178\n"
      "v_accvgpr_read_b32 v199, a179\n"
      "v_mul_f32 v196, v196, v194\n"
      "v_mul_f32 v197, v197, v194\n"
      "v_mul_f32 v198, v198, v194\n"
      "v_mul_f32 v199, v199, v194\n"
      "v_accvgpr_write_b32 a176, v196\n"
      "v_accvgpr_write_b32 a177, v197\n"
      "v_accvgpr_write_b32 a178, v198\n"
      "v_accvgpr_write_b32 a179, v199\n"
      "v_accvgpr_read_b32 v196, a180\n"
      "v_accvgpr_read_b32 v197, a181\n"
      "v_accvgpr_read_b32 v198, a182\n"
      "v_accvgpr_read_b32 v199, a183\n"
      "v_mul_f32 v196, v196, v195\n"
      "v_mul_f32 v197, v197, v195\n"
      "v_mul_f32 v198, v198, v195\n"
      "v_mul_f32 v199, v199, v195\n"
      "v_accvgpr_write_b32 a180, v196\n"
      "v_accvgpr_write_b32 a181, v197\n"
      "v_accvgpr_write_b32 a182, v198\n"
      "v_accvgpr_write_b32 a183, v199\n"
      "v_accvgpr_read_b32 v196, a184\n"
      "v_accvgpr_read_b32 v197, a185\n"
      "v_accvgpr_read_b32 v198, a186\n"
      "v_accvgpr_read_b32 v199, a187\n"
      "v_mul_f32 v196, v196, v194\n"
      "v_mul_f32 v197, v197, v194\n"
      "v_mul_f32 v198, v198, v194\n"
      "v_mul_f32 v199, v199, v194\n"
      "v_accvgpr_write_b32 a184, v196\n"
      "v_accvgpr_write_b32 a185, v197\n"
      "v_accvgpr_write_b32 a186, v198\n"
      "v_accvgpr_write_b32 a187, v199\n"
      "v_accvgpr_read_b32 v196, a188\n"
      "v_accvgpr_read_b32 v197, a189\n"
      "v_accvgpr_read_b32 v198, a190\n"
      "v_accvgpr_read_b32 v199, a191\n"
      "v_mul_f32 v196, v196, v195\n"
      "v_mul_f32 v197, v197, v195\n"
      "v_mul_f32 v198, v198, v195\n"
      "v_mul_f32 v199, v199, v195\n"
      "v_accvgpr_write_b32 a188, v196\n"
      "v_accvgpr_write_b32 a189, v197\n"
      "v_accvgpr_write_b32 a190, v198\n"
      "v_accvgpr_write_b32 a191, v199\n"
      "v_accvgpr_read_b32 v196, a192\n"
      "v_accvgpr_read_b32 v197, a193\n"
      "v_accvgpr_read_b32 v198, a194\n"
      "v_accvgpr_read_b32 v199, a195\n"
      "v_mul_f32 v196, v196, v194\n"
      "v_mul_f32 v197, v197, v194\n"
      "v_mul_f32 v198, v198, v194\n"
      "v_mul_f32 v199, v199, v194\n"
      "v_accvgpr_write_b32 a192, v196\n"
      "v_accvgpr_write_b32 a193, v197\n"
      "v_accvgpr_write_b32 a194, v198\n"
      "v_accvgpr_write_b32 a195, v199\n"
      "v_accvgpr_read_b32 v196, a196\n"
      "v_accvgpr_read_b32 v197, a197\n"
      "v_accvgpr_read_b32 v198, a198\n"
      "v_accvgpr_read_b32 v199, a199\n"
      "v_mul_f32 v196, v196, v195\n"
      "v_mul_f32 v197, v197, v195\n"
      "v_mul_f32 v198, v198, v195\n"
      "v_mul_f32 v199, v199, v195\n"
      "v_accvgpr_write_b32 a196, v196\n"
      "v_accvgpr_write_b32 a197, v197\n"
      "v_accvgpr_write_b32 a198, v198\n"
      "v_accvgpr_write_b32 a199, v199\n"
      "v_accvgpr_read_b32 v196, a200\n"
      "v_accvgpr_read_b32 v197, a201\n"
      "v_accvgpr_read_b32 v198, a202\n"
      "v_accvgpr_read_b32 v199, a203\n"
      "v_mul_f32 v196, v196, v194\n"
      "v_mul_f32 v197, v197, v194\n"
      "v_mul_f32 v198, v198, v194\n"
      "v_mul_f32 v199, v199, v194\n"
      "v_accvgpr_write_b32 a200, v196\n"
      "v_accvgpr_write_b32 a201, v197\n"
      "v_accvgpr_write_b32 a202, v198\n"
      "v_accvgpr_write_b32 a203, v199\n"
      "v_accvgpr_read_b32 v196, a204\n"
      "v_accvgpr_read_b32 v197, a205\n"
      "v_accvgpr_read_b32 v198, a206\n"
      "v_accvgpr_read_b32 v199, a207\n"
      "v_mul_f32 v196, v196, v195\n"
      "v_mul_f32 v197, v197, v195\n"
      "v_mul_f32 v198, v198, v195\n"
      "v_mul_f32 v199, v199, v195\n"
      "v_accvgpr_write_b32 a204, v196\n"
      "v_accvgpr_write_b32 a205, v197\n"
      "v_accvgpr_write_b32 a206, v198\n"
      "v_accvgpr_write_b32 a207, v199\n"
      "v_mul_f32 v180, v180, v192\n"
      "v_mul_f32 v181, v181, v193\n"
      "v_mul_f32 v182, v182, v194\n"
      "v_mul_f32 v183, v183, v195\n"
      "v_lshlrev_b32 v196, 16, v128\n"
      "v_and_b32 v197, 0xffff0000, v128\n"
      "v_mul_f32 v196, v196, v192\n"
      "v_mul_f32 v197, v197, v192\n"
      "v_cvt_pk_bf16_f32 v128, v196, v197\n"
      "v_lshlrev_b32 v196, 16, v129\n"
      "v_and_b32 v197, 0xffff0000, v129\n"
      "v_mul_f32 v196, v196, v192\n"
      "v_mul_f32 v197, v197, v192\n"
      "v_cvt_pk_bf16_f32 v129, v196, v197\n"
      "v_lshlrev_b32 v196, 16, v130\n"
      "v_and_b32 v197, 0xffff0000, v130\n"
      "v_mul_f32 v196, v196, v192\n"
      "v_mul_f32 v197, v197, v192\n"
      "v_cvt_pk_bf16_f32 v130, v196, v197\n"
      "v_lshlrev_b32 v196, 16, v131\n"
      "v_and_b32 v197, 0xffff0000, v131\n"
      "v_mul_f32 v196, v196, v192\n"
      "v_mul_f32 v197, v197, v192\n"
      "v_cvt_pk_bf16_f32 v131, v196, v197\n"
      "v_lshlrev_b32 v196, 16, v132\n"
      "v_and_b32 v197, 0xffff0000, v132\n"
      "v_mul_f32 v196, v196, v193\n"
      "v_mul_f32 v197, v197, v193\n"
      "v_cvt_pk_bf16_f32 v132, v196, v197\n"
      "v_lshlrev_b32 v196, 16, v133\n"
      "v_and_b32 v197, 0xffff0000, v133\n"
      "v_mul_f32 v196, v196, v193\n"
      "v_mul_f32 v197, v197, v193\n"
      "v_cvt_pk_bf16_f32 v133, v196, v197\n"
      "v_lshlrev_b32 v196, 16, v134\n"
      "v_and_b32 v197, 0xffff0000, v134\n"
      "v_mul_f32 v196, v196, v193\n"
      "v_mul_f32 v197, v197, v193\n"
      "v_cvt_pk_bf16_f32 v134, v196, v197\n"
      "v_lshlrev_b32 v196, 16, v135\n"
      "v_and_b32 v197, 0xffff0000, v135\n"
      "v_mul_f32 v196, v196, v193\n"
      "v_mul_f32 v197, v197, v193\n"
      "v_cvt_pk_bf16_f32 v135, v196, v197\n"
      "v_lshlrev_b32 v196, 16, v136\n"
      "v_and_b32 v197, 0xffff0000, v136\n"
      "v_mul_f32 v196, v196, v194\n"
      "v_mul_f32 v197, v197, v194\n"
      "v_cvt_pk_bf16_f32 v136, v196, v197\n"
      "v_lshlrev_b32 v196, 16, v137\n"
      "v_and_b32 v197, 0xffff0000, v137\n"
      "v_mul_f32 v196, v196, v194\n"
      "v_mul_f32 v197, v197, v194\n"
      "v_cvt_pk_bf16_f32 v137, v196, v197\n"
      "v_lshlrev_b32 v196, 16, v138\n"
      "v_and_b32 v197, 0xffff0000, v138\n"
      "v_mul_f32 v196, v196, v194\n"
      "v_mul_f32 v197, v197, v194\n"
      "v_cvt_pk_bf16_f32 v138, v196, v197\n"
      "v_lshlrev_b32 v196, 16, v139\n"
      "v_and_b32 v197, 0xffff0000, v139\n"
      "v_mul_f32 v196, v196, v194\n"
      "v_mul_f32 v197, v197, v194\n"
      "v_cvt_pk_bf16_f32 v139, v196, v197\n"
      "v_lshlrev_b32 v196, 16, v140\n"
      "v_and_b32 v197, 0xffff0000, v140\n"
      "v_mul_f32 v196, v196, v195\n"
      "v_mul_f32 v197, v197, v195\n"
      "v_cvt_pk_bf16_f32 v140, v196, v197\n"
      "v_lshlrev_b32 v196, 16, v141\n"
      "v_and_b32 v197, 0xffff0000, v141\n"
      "v_mul_f32 v196, v196, v195\n"
      "v_mul_f32 v197, v197, v195\n"
      "v_cvt_pk_bf16_f32 v141, v196, v197\n"
      "v_lshlrev_b32 v196, 16, v142\n"
      "v_and_b32 v197, 0xffff0000, v142\n"
      "v_mul_f32 v196, v196, v195\n"
      "v_mul_f32 v197, v197, v195\n"
      "v_cvt_pk_bf16_f32 v142, v196, v197\n"
      "v_lshlrev_b32 v196, 16, v143\n"
      "v_and_b32 v197, 0xffff0000, v143\n"
      "v_mul_f32 v196, v196, v195\n"
      "v_mul_f32 v197, v197, v195\n"
      "v_cvt_pk_bf16_f32 v143, v196, v197\n"
      "v_accvgpr_read_b32 v197, a208\n"
      "v_lshlrev_b32 v196, 16, v197\n"
      "v_and_b32 v197, 0xffff0000, v197\n"
      "v_mul_f32 v196, v196, v192\n"
      "v_mul_f32 v197, v197, v192\n"
      "v_cvt_pk_bf16_f32 v196, v196, v197\n"
      "v_accvgpr_write_b32 a208, v196\n"
      "v_accvgpr_read_b32 v197, a209\n"
      "v_lshlrev_b32 v196, 16, v197\n"
      "v_and_b32 v197, 0xffff0000, v197\n"
      "v_mul_f32 v196, v196, v192\n"
      "v_mul_f32 v197, v197, v192\n"
      "v_cvt_pk_bf16_f32 v196, v196, v197\n"
      "v_accvgpr_write_b32 a209, v196\n"
      "v_accvgpr_read_b32 v197, a210\n"
      "v_lshlrev_b32 v196, 16, v197\n"
      "v_and_b32 v197, 0xffff0000, v197\n"
      "v_mul_f32 v196, v196, v192\n"
      "v_mul_f32 v197, v197, v192\n"
      "v_cvt_pk_bf16_f32 v196, v196, v197\n"
      "v_accvgpr_write_b32 a210, v196\n"
      "v_accvgpr_read_b32 v197, a211\n"
      "v_lshlrev_b32 v196, 16, v197\n"
      "v_and_b32 v197, 0xffff0000, v197\n"
      "v_mul_f32 v196, v196, v192\n"
      "v_mul_f32 v197, v197, v192\n"
      "v_cvt_pk_bf16_f32 v196, v196, v197\n"
      "v_accvgpr_write_b32 a211, v196\n"
      "v_accvgpr_read_b32 v197, a212\n"
      "v_lshlrev_b32 v196, 16, v197\n"
      "v_and_b32 v197, 0xffff0000, v197\n"
      "v_mul_f32 v196, v196, v193\n"
      "v_mul_f32 v197, v197, v193\n"
      "v_cvt_pk_bf16_f32 v196, v196, v197\n"
      "v_accvgpr_write_b32 a212, v196\n"
      "v_accvgpr_read_b32 v197, a213\n"
      "v_lshlrev_b32 v196, 16, v197\n"
      "v_and_b32 v197, 0xffff0000, v197\n"
      "v_mul_f32 v196, v196, v193\n"
      "v_mul_f32 v197, v197, v193\n"
      "v_cvt_pk_bf16_f32 v196, v196, v197\n"
      "v_accvgpr_write_b32 a213, v196\n"
      "v_accvgpr_read_b32 v197, a214\n"
      "v_lshlrev_b32 v196, 16, v197\n"
      "v_and_b32 v197, 0xffff0000, v197\n"
      "v_mul_f32 v196, v196, v193\n"
      "v_mul_f32 v197, v197, v193\n"
      "v_cvt_pk_bf16_f32 v196, v196, v197\n"
      "v_accvgpr_write_b32 a214, v196\n"
      "v_accvgpr_read_b32 v197, a215\n"
      "v_lshlrev_b32 v196, 16, v197\n"
      "v_and_b32 v197, 0xffff0000, v197\n"
      "v_mul_f32 v196, v196, v193\n"
      "v_mul_f32 v197, v197, v193\n"
      "v_cvt_pk_bf16_f32 v196, v196, v197\n"
      "v_accvgpr_write_b32 a215, v196\n"
      "v_accvgpr_read_b32 v197, a216\n"
      "v_lshlrev_b32 v196, 16, v197\n"
      "v_and_b32 v197, 0xffff0000, v197\n"
      "v_mul_f32 v196, v196, v194\n"
      "v_mul_f32 v197, v197, v194\n"
      "v_cvt_pk_bf16_f32 v196, v196, v197\n"
      "v_accvgpr_write_b32 a216, v196\n"
      "v_accvgpr_read_b32 v197, a217\n"
      "v_lshlrev_b32 v196, 16, v197\n"
      "v_and_b32 v197, 0xffff0000, v197\n"
      "v_mul_f32 v196, v196, v194\n"
      "v_mul_f32 v197, v197, v194\n"
      "v_cvt_pk_bf16_f32 v196, v196, v197\n"
      "v_accvgpr_write_b32 a217, v196\n"
      "v_accvgpr_read_b32 v197, a218\n"
      "v_lshlrev_b32 v196, 16, v197\n"
      "v_and_b32 v197, 0xffff0000, v197\n"
      "v_mul_f32 v196, v196, v194\n"
      "v_mul_f32 v197, v197, v194\n"
      "v_cvt_pk_bf16_f32 v196, v196, v197\n"
      "v_accvgpr_write_b32 a218, v196\n"
      "v_accvgpr_read_b32 v197, a219\n"
      "v_lshlrev_b32 v196, 16, v197\n"
      "v_and_b32 v197, 0xffff0000, v197\n"
      "v_mul_f32 v196, v196, v194\n"
      "v_mul_f32 v197, v197, v194\n"
      "v_cvt_pk_bf16_f32 v196, v196, v197\n"
      "v_accvgpr_write_b32 a219, v196\n"
      "v_accvgpr_read_b32 v197, a220\n"
      "v_lshlrev_b32 v196, 16, v197\n"
      "v_and_b32 v197, 0xffff0000, v197\n"
      "v_mul_f32 v196, v196, v195\n"
      "v_mul_f32 v197, v197, v195\n"
      "v_cvt_pk_bf16_f32 v196, v196, v197\n"
      "v_accvgpr_write_b32 a220, v196\n"
      "v_accvgpr_read_b32 v197, a221\n"
      "v_lshlrev_b32 v196, 16, v197\n"
      "v_and_b32 v197, 0xffff0000, v197\n"
      "v_mul_f32 v196, v196, v195\n"
      "v_mul_f32 v197, v197, v195\n"
      "v_cvt_pk_bf16_f32 v196, v196, v197\n"
      "v_accvgpr_write_b32 a221, v196\n"
      "v_accvgpr_read_b32 v197, a222\n"
      "v_lshlrev_b32 v196, 16, v197\n"
      "v_and_b32 v197, 0xffff0000, v197\n"
      "v_mul_f32 v196, v196, v195\n"
      "v_mul_f32 v197, v197, v195\n"
      "v_cvt_pk_bf16_f32 v196, v196, v197\n"
      "v_accvgpr_write_b32 a222, v196\n"
      "v_accvgpr_read_b32 v197, a223\n"
      "v_lshlrev_b32 v196, 16, v197\n"
      "v_and_b32 v197, 0xffff0000, v197\n"
      "v_mul_f32 v196, v196, v195\n"
      "v_mul_f32 v197, v197, v195\n"
      "v_cvt_pk_bf16_f32 v196, v196, v197\n"
      "v_accvgpr_write_b32 a223, v196\n"
      "v_exp_f32 v188, v96\n"
      "v_exp_f32 v189, v97\n"
      "v_exp_f32 v190, v98\n"
      "v_exp_f32 v191, v99\n"
      "v_cvt_pk_bf16_f32 v144, v188, v189\n"
      "v_cvt_pk_bf16_f32 v145, v190, v191\n"
      "v_exp_f32 v192, v100\n"
      "v_exp_f32 v193, v101\n"
      "v_exp_f32 v194, v102\n"
      "v_exp_f32 v195, v103\n"
      "v_cvt_pk_bf16_f32 v146, v192, v193\n"
      "v_cvt_pk_bf16_f32 v147, v194, v195\n"
      "v_exp_f32 v188, v104\n"
      "v_exp_f32 v189, v105\n"
      "v_exp_f32 v190, v106\n"
      "v_exp_f32 v191, v107\n"
      "v_cvt_pk_bf16_f32 v148, v188, v189\n"
      "v_cvt_pk_bf16_f32 v149, v190, v191\n"
      "v_exp_f32 v192, v108\n"
      "v_exp_f32 v193, v109\n"
      "v_exp_f32 v194, v110\n"
      "v_exp_f32 v195, v111\n"
      "v_cvt_pk_bf16_f32 v150, v192, v193\n"
      "v_cvt_pk_bf16_f32 v151, v194, v195\n"
      "v_exp_f32 v188, v112\n"
      "v_exp_f32 v189, v113\n"
      "v_exp_f32 v190, v114\n"
      "v_exp_f32 v191, v115\n"
      "v_cvt_pk_bf16_f32 v152, v188, v189\n"
      "v_cvt_pk_bf16_f32 v153, v190, v191\n"
      "v_exp_f32 v192, v116\n"
      "v_exp_f32 v193, v117\n"
      "v_exp_f32 v194, v118\n"
      "v_exp_f32 v195, v119\n"
      "v_cvt_pk_bf16_f32 v154, v192, v193\n"
      "v_cvt_pk_bf16_f32 v155, v194, v195\n"
      "v_exp_f32 v188, v120\n"
      "v_exp_f32 v189, v121\n"
      "v_exp_f32 v190, v122\n"
      "v_exp_f32 v191, v123\n"
      "v_cvt_pk_bf16_f32 v156, v188, v189\n"
      "v_cvt_pk_bf16_f32 v157, v190, v191\n"
      "v_exp_f32 v192, v124\n"
      "v_exp_f32 v193, v125\n"
      "v_exp_f32 v194, v126\n"
      "v_exp_f32 v195, v127\n"
      "v_cvt_pk_bf16_f32 v158, v192, v193\n"
      "v_cvt_pk_bf16_f32 v159, v194, v195\n"
      "s_nop 1\n"
      "v_mfma_f32_16x16x32_bf16 v[184:187], a[128:131], v[144:147], 0\n"
      "v_mfma_f32_16x16x32_bf16 v[184:187], a[132:135], v[148:151], v[184:187]\n"
      "v_mfma_f32_16x16x32_bf16 v[184:187], a[136:139], v[152:155], v[184:187]\n"
      "v_mfma_f32_16x16x32_bf16 v[184:187], a[140:143], v[156:159], v[184:187]\n"
      "s_nop 7\n"
      "s_setpc_b64 s[96:97]\n"
      "SLOW_%=:\n"
      "ds_write_b128 v234, v[176:179] offset:5120\n"
      "s_waitcnt lgkmcnt(0)\n"
      "s_barrier\n"
      "ds_read_b128 v[192:195], v235 offset:5120\n"
      "s_waitcnt lgkmcnt(0)\n"
      "v_sub_f32 v188, v176, v194\n"
      "v_sub_f32 v189, v177, v195\n"
      "v_sub_f32 v190, v178, v192\n"
      "v_sub_f32 v191, v179, v193\n"
      "v_max_f32 v188, 0, v188\n"
      "v_max_f32 v189, 0, v189\n"
      "v_max_f32 v190, 0, v190\n"
      "v_max_f32 v191, 0, v191\n"
      "s_call_b64 s[96:97], RS1_%=\n"
      "ds_write_b128 v232, a[176:179] offset:0\n"
      "ds_write_b128 v232, a[180:183] offset:1024\n"
      "ds_write_b128 v232, a[184:187] offset:2048\n"
      "ds_write_b128 v232, a[188:191] offset:3072\n"
      "ds_write_b128 v232, a[192:195] offset:4096\n"
      "ds_write_b128 v232, a[196:199] offset:5120\n"
      "ds_write_b128 v232, a[200:203] offset:6144\n"
      "ds_write_b128 v232, a[204:207] offset:7168\n"
      "ds_write_b128 v234, v[152:155] offset:0\n"
      "ds_write_b128 v234, v[156:159] offset:1024\n"
      "v_cndmask_b32 v229, v180, v181, s[74:75]\n"
      "v_cndmask_b32 v200, v182, v183, s[74:75]\n"
      "v_cndmask_b32 v228, v176, v177, s[74:75]\n"
      "v_cndmask_b32 v202, v186, v187, s[74:75]\n"
      "v_cndmask_b32 v201, v184, v185, s[74:75]\n"
      "v_add_f32 v200, v200, v202\n"
      "ds_write_b32 v234, v200 offset:4096\n"
      "v_add_f32 v229, v229, v201\n"
      "s_waitcnt lgkmcnt(0)\n"
      "s_barrier\n"
      "ds_read_b32 v208, v209\n"
      "ds_read_b128 v[160:163], v235 offset:0\n"
      "ds_read_b128 v[164:167], v235 offset:1024\n"
      "ds_read_b128 v[64:67], v233 offset:0\n"
      "ds_read_b128 v[68:71], v233 offset:1024\n"
      "ds_read_b128 v[72:75], v233 offset:2048\n"
      "ds_read_b128 v[76:79], v233 offset:3072\n"
      "ds_read_b128 v[80:83], v233 offset:4096\n"
      "ds_read_b128 v[84:87], v233 offset:5120\n"
      "ds_read_b128 v[88:91], v233 offset:6144\n"
      "ds_read_b128 v[92:95], v233 offset:7168\n"
      "ds_read_b32 v204, v235 offset:4096\n"
      "s_branch AGAIN_%=\n"
      ".subsection 0\n"
      : "+{v0}"(tid)
      : [blk] "s"(blockIdx.x), [kp] "s"(__builtin_amdgcn_kernarg_segment_ptr()), "s"(lds0)
      : "v1", "v2", "v3", "v4", "v5", "v6", "v7", "v8", "v9", "v10", "v11", "v12", "v13", "v14", "v15", "v16", "v17", "v18", "v19", "v20", "v21", "v22", "v23", "v24", "v25", "v26", "v27", "v28", "v29", "v30", "v31", "v32", "v33", "v34", "v35", "v36", "v37", "v38", "v39", "v40", "v41", "v42", "v43", "v44", "v45", "v46", "v47", "v48", "v49", "v50", "v51", "v52", "v53", "v54", "v55", "v56", "v57", "v58", "v59", "v60", "v61", "v62", "v63", "v64", "v65", "v66", "v67", "v68", "v69", "v70", "v71", "v72", "v73", "v74", "v75", "v76", "v77", "v78", "v79", "v80", "v81", "v82", "v83", "v84", "v85", "v86", "v87", "v88", "v89", "v90", "v91", "v92", "v93", "v94", "v95", "v96", "v97", "v98", "v99", "v100", "v101", "v102", "v103", "v104", "v105", "v106", "v107", "v108", "v109", "v110", "v111", "v112", "v113", "v114", "v115", "v116", "v117", "v118", "v119", "v120", "v121", "v122", "v123", "v124", "v125", "v126", "v127", "v128", "v129", "v130", "v131", "v132", "v133", "v134", "v135", "v136", "v137", "v138", "v139", "v140", "v141", "v142", "v143", "v144", "v145", "v146", "v147", "v148", "v149", "v150", "v151", "v152", "v153", "v154", "v155", "v156", "v157", "v158", "v159", "v160", "v161", "v162", "v163", "v164", "v165", "v166", "v167", "v168", "v169", "v170", "v171", "v172", "v173", "v174", "v175", "v176", "v177", "v178", "v179", "v180", "v181", "v182", "v183", "v184", "v185", "v186", "v187", "v188", "v189", "v190", "v191", "v192", "v193", "v194", "v195", "v196", "v197", "v198", "v199", "v200", "v201", "v202", "v203", "v204", "v205", "v206", "v207", "v208", "v209", "v210", "v211", "v212", "v213", "v214", "v215", "v216", "v217", "v218", "v219", "v220", "v221", "v222", "v223", "v224", "v225", "v226", "v227", "v228", "v229", "v230", "v231", "v232", "v233", "v234", "v235", "v236", "v237", "v238", "v239", "v240", "v241", "v242", "v243", "v244", "v245", "v246", "v247", "v248", "v249", "v250", "v251", "a0", "a1", "a2", "a3", "a4", "a5", "a6", "a7", "a8", "a9", "a10", "a11", "a12", "a13", "a14", "a15", "a16", "a17", "a18", "a19", "a20", "a21", "a22", "a23", "a24", "a25", "a26", "a27", "a28", "a29", "a30", "a31", "a32", "a33", "a34", "a35", "a36", "a37", "a38", "a39", "a40", "a41", "a42", "a43", "a44", "a45", "a46", "a47", "a48", "a49", "a50", "a51", "a52", "a53", "a54", "a55", "a56", "a57", "a58", "a59", "a60", "a61", "a62", "a63", "a64", "a65", "a66", "a67", "a68", "a69", "a70", "a71", "a72", "a73", "a74", "a75", "a76", "a77", "a78", "a79", "a80", "a81", "a82", "a83", "a84", "a85", "a86", "a87", "a88", "a89", "a90", "a91", "a92", "a93", "a94", "a95", "a96", "a97", "a98", "a99", "a100", "a101", "a102", "a103", "a104", "a105", "a106", "a107", "a108", "a109", "a110", "a111", "a112", "a113", "a114", "a115", "a116", "a117", "a118", "a119", "a120", "a121", "a122", "a123", "a124", "a125", "a126", "a127", "a128", "a129", "a130", "a131", "a132", "a133", "a134", "a135", "a136", "a137", "a138", "a139", "a140", "a141", "a142", "a143", "a144", "a145", "a146", "a147", "a148", "a149", "a150", "a151", "a152", "a153", "a154", "a155", "a156", "a157", "a158", "a159", "a160", "a161", "a162", "a163", "a164", "a165", "a166", "a167", "a168", "a169", "a170", "a171", "a172", "a173", "a174", "a175", "a176", "a177", "a178", "a179", "a180", "a181", "a182", "a183", "a184", "a185", "a186", "a187", "a188", "a189", "a190", "a191", "a192", "a193", "a194", "a195", "a196", "a197", "a198", "a199", "a200", "a201", "a202", "a203", "a204", "a205", "a206", "a207", "a208", "a209", "a210", "a211", "a212", "a213", "a214", "a215", "a216", "a217", "a218", "a219", "a220", "a221", "a222", "a223", "s4", "s5", "s6", "s7", "s8", "s9", "s10", "s11", "s12", "s13", "s14", "s15", "s16", "s17", "s18", "s19", "s20", "s21", "s22", "s23", "s24", "s25", "s26", "s27", "s28", "s29", "s30", "s31", "s32", "s33", "s34", "s35", "s38", "s39", "s40", "s43", "s44", "s45", "s46", "s47", "s50", "s52", "s53", "s54", "s56", "s57", "s58", "s59", "s60", "s61", "s62", "s63", "s64", "s65", "s66", "s67", "s68", "s69", "s70", "s71", "s72", "s74", "s75", "s76", "s77", "s78", "s79", "s80", "s81", "s82", "s83", "s84", "s85", "s86", "s87", "s88", "s89", "s90", "s91", "s92", "s93", "s94", "s95", "s96", "s97", "vcc", "scc", "memory");
}

}

extern "C" void kernel_launch(void* const* d_in, const int* in_sizes, int n_in, void* d_out, int out_size,
                              void* d_ws, size_t ws_size, hipStream_t stream) {
  if (n_in != 6 || ws_size < WS_END) return;
  const __hip_bfloat16* q = (const __hip_bfloat16*)d_in[0];
  const __hip_bfloat16* kc = (const __hip_bfloat16*)d_in[1];
  const __hip_bfloat16* vc = (const __hip_bfloat16*)d_in[2];
  const __hip_bfloat16* kt = (const __hip_bfloat16*)d_in[3];
  const __hip_bfloat16* vt = (const __hip_bfloat16*)d_in[4];
  const __hip_bfloat16* tm = (const __hip_bfloat16*)d_in[5];
  hipLaunchKernelGGL(tree_attn_main, dim3(B_ * HKV_ * 2), dim3(256), 0, stream, q, kc, vc, kt, vt, tm,
                     (__hip_bfloat16*)d_out, (char*)d_ws);
}
```

```cpp
#include <hip/hip_runtime.h>
#include <hip/hip_bf16.h>
#include <stdint.h>

#pragma clang diagnostic ignored "-Wint-to-pointer-cast"
#pragma clang diagnostic ignored "-Winline-asm"

namespace {

constexpr int B_ = 16, HKV_ = 8, D_ = 128;
constexpr int SLOT = 64 * D_ * 2;
constexpr int LDS_BYTES = 10 * SLOT;

constexpr size_t WS_EPOCH = 0;
constexpr size_t WS_REF = WS_EPOCH + 128 * 4 * 128;
constexpr size_t WS_X = WS_REF + 16384;
constexpr size_t XW_BYTES = 4608;
constexpr size_t WS_END = WS_X + (size_t)128 * 2 * 4 * XW_BYTES;

__global__ __launch_bounds__(256, 1)
void tree_attn_main(const __hip_bfloat16*, const __hip_bfloat16*, const __hip_bfloat16*, const __hip_bfloat16*,
                    const __hip_bfloat16*, const __hip_bfloat16*, __hip_bfloat16*, char*) {
  __shared__ __attribute__((aligned(1024))) char smem[LDS_BYTES];
  const unsigned lds0 = (unsigned)(uintptr_t)smem;
  unsigned tid = threadIdx.x;
  asm volatile(
      "s_load_dwordx16 s[4:19], %[kp], 0x0\n"
      "s_getpc_b64 s[60:61]\n"
      "v_lshlrev_b32 v241, 4, v0\n"
      "s_lshl_b32 s62, %[blk], 9\n"
      "s_and_b32 s62, s62, 0x3000\n"
      "s_add_u32 s60, s60, s62\n"
      "s_addc_u32 s61, s61, 0\n"
      "global_load_dwordx4 v[236:239], v241, s[60:61]\n"
      "v_and_b32 v1, 0xc0, v0\n"
      "v_lshlrev_b32 v248, 1, v0\n"
      "v_readfirstlane_b32 s50, v1\n"
      "v_and_b32 v248, 0x60, v248\n"
      "v_and_b32 v249, 0x70, v241\n"
      "v_xor_b32 v248, v248, v249\n"
      "v_lshl_or_b32 v248, v1, 6, v248\n"
      "v_and_b32 v1, 56, v0\n"
      "v_lshl_or_b32 v248, v1, 5, v248\n"
      "s_mov_b32 s64, 0x14100400\n"
      "s_mov_b32 s65, 0x54504440\n"
      "s_lshl_b32 s66, %[blk], 3\n"
      "s_lshr_b64 s[64:65], s[64:65], s66\n"
      "s_mov_b32 s66, 0x3020100\n"
      "s_mov_b32 s67, 0x83828180\n"
      "s_and_b32 s65, %[blk], 56\n"
      "s_lshr_b64 s[66:67], s[66:67], s65\n"
      "s_or_b32 s64, s64, s66\n"
      "s_lshr_b32 s65, %[blk], 3\n"
      "s_and_b32 s65, s65, 24\n"
      "s_lshr_b32 s65, 0x28082000, s65\n"
      "s_or_b32 s64, s64, s65\n"
      "s_and_b32 s63, s64, 0x80\n"
      "s_and_b32 s44, s64, 127\n"
      "v_or_b32 v248, s63, v248\n"
      "v_xor_b32 v249, 0x80, v248\n"
      "s_lshl_b32 s64, s44, 20\n"
      "s_lshl_b32 s62, s44, 15\n"
      "s_lshl_b32 s50, s50, 6\n"
      "s_add_u32 m0, s50, 131072\n"
      "s_mov_b32 s58, 0x120000\n"
      "s_mov_b32 s59, 0x20000\n"
      "s_movk_i32 s22, 0x4000\n"
      "s_waitcnt lgkmcnt(0)\n"
      "s_add_u32 s56, s4, s62\n"
      "s_addc_u32 s57, s5, 0\n"
      "buffer_load_dwordx4 v248, s[56:59], 0 offen nt lds\n"
      "buffer_load_dwordx4 v248, s[56:59], 0 offen offset:2048 nt lds\n"
      "s_bitset1_b32 m0, 10\n"
      "s_memrealtime s[82:83]\n"
      "s_add_u32 s20, s6, s64\n"
      "s_addc_u32 s21, s7, 0\n"
      "s_add_u32 s32, s8, s64\n"
      "s_addc_u32 s33, s9, 0\n"
      "s_load_dword s92, s[20:21], 0x0\n"
      "s_load_dword s92, s[10:11], 0x0\n"
      "s_load_dword s92, s[32:33], 0x0\n"
      "buffer_load_dwordx4 v249, s[56:59], 0 offen nt lds\n"
      "buffer_load_dwordx4 v249, s[56:59], 0 offen offset:2048 nt lds\n"
      "s_add_u32 m0, s50, 147456\n"
      "s_load_dword s92, s[18:19], 0x0\n"
      "buffer_load_dwordx4 v248, s[56:59], s22 offen nt lds\n"
      "buffer_load_dwordx4 v248, s[56:59], s22 offen offset:2048 nt lds\n"
      "s_bitset1_b32 m0, 10\n"
      "s_nop 0\n"
      "buffer_load_dwordx4 v249, s[56:59], s22 offen nt lds\n"
      "buffer_load_dwordx4 v249, s[56:59], s22 offen offset:2048 nt lds\n"
      "s_mov_b64 s[56:57], s[20:21]\n"
      "s_load_dword s92, s[14:15], 0x0\n"
      "s_load_dword s92, s[12:13], 0x0\n"
      "s_lshr_b32 s74, s50, 2\n"
      "s_lshr_b32 s75, s50, 5\n"
      "s_xor_b32 s75, s75, s63\n"
      "s_and_b32 s75, s75, 0x80\n"
      "s_and_b32 s76, s74, 0x800\n"
      "s_add_u32 s75, s75, s76\n"
      "s_add_u32 s70, s63, s50\n"
      "s_sub_u32 s70, s70, s75\n"
      "s_lshr_b32 s75, s50, 1\n"
      "s_lshr_b32 s71, s62, 2\n"
      "s_sub_u32 s71, s71, s75\n"
      "s_ashr_i32 s72, s71, 31\n"
      "s_mov_b32 s23, s59\n"
      "s_mov_b64 s[26:27], s[22:23]\n"
      "s_sub_u32 s20, s56, s70\n"
      "s_subb_u32 s21, s57, 0\n"
      "s_add_u32 m0, s74, 24576\n"
      "s_add_u32 s24, s10, s71\n"
      "buffer_load_dwordx4 v248, s[20:23], 0 offen nt lds\n"
      "s_addc_u32 s25, s11, s72\n"
      "s_add_u32 m0, s75, 16384\n"
      "s_nop 0\n"
      "buffer_load_dwordx4 v248, s[24:27], 0 offen nt lds\n"
      "s_bitset1_b32 m0, 10\n"
      "s_nop 0\n"
      "buffer_load_dwordx4 v249, s[24:27], 0 offen nt lds\n"
      "s_lshr_b32 s43, s63, 7\n"
      "v_mov_b64 v[180:181], 0\n"
      "v_mov_b64 v[182:183], 0\n"
      "v_mov_b64 v[184:185], 0\n"
      "v_mov_b64 v[186:187], 0\n"
      "v_lshlrev_b32 v3, 6, v0\n"
      "v_and_b32 v3, 0x3c0, v3\n"
      "v_lshrrev_b32 v4, 1, v0\n"
      "v_and_or_b32 v211, v4, 24, v3\n"
      "v_mov_b64 v[200:201], 0\n"
      "v_mov_b64 v[202:203], 0\n"
      "v_mov_b64 v[204:205], 0\n"
      "v_mov_b64 v[206:207], 0\n"
      "s_lshl_b32 s76, s44, 9\n"
      "s_lshr_b32 s67, s50, 5\n"
      "s_add_u32 s76, s76, s67\n"
      "s_add_u32 s46, s18, s76\n"
      "s_addc_u32 s47, s19, 0\n"
      "s_mul_i32 s76, s43, 0x3000\n"
      "s_lshl_b32 s67, s44, 8\n"
      "s_and_b32 s67, s67, 0x7800\n"
      "s_add_u32 s14, s14, s67\n"
      "s_addc_u32 s15, s15, 0\n"
      "s_cmp_lg_u32 s76, s50\n"
      "s_cbranch_scc1 NOMASK_%=\n"
      "global_load_dwordx2 v[200:201], v211, s[14:15] offset:0\n"
      "global_load_dwordx2 v[202:203], v211, s[14:15] offset:32\n"
      "global_load_dwordx2 v[204:205], v211, s[14:15] offset:1024\n"
      "global_load_dwordx2 v[206:207], v211, s[14:15] offset:1056\n"
      "NOMASK_%=:\n"
      "global_load_dword v208, v180, s[46:47] sc1\n"
      "s_lshl_b32 s69, s43, 13\n"
      "s_xor_b32 s69, s69, 0x2000\n"
      "v_mov_b32 v188, v241\n"
      "v_add_u32 v189, s69, v241\n"
      "v_and_b32 v241, 0x3f0, v241\n"
      "s_add_u32 s69, s50, 81920\n"
      "v_add_u32 v190, s69, v241\n"
      "s_mul_i32 s69, s43, 0x8000\n"
      "s_bitcmp1_b32 s44, 0\n"
      "s_cselect_b32 s66, 0x8000, 0\n"
      "s_xor_b32 s69, s69, s66\n"
      "v_mfma_f32_32x32x16_bf16 a[144:159], v[180:183], v[180:183], 0\n"
      "v_and_b32 v228, 0xff, v248\n"
      "v_xor_b32 v228, s69, v228\n"
      "v_bfe_i32 v2, v0, 3, 1\n"
      "v_and_b32 v2, 0x1000, v2\n"
      "v_xor_b32 v228, v228, v2\n"
      "v_bfe_i32 v2, v0, 4, 1\n"
      "v_and_b32 v2, 0x20400, v2\n"
      "v_xor_b32 v228, v228, v2\n"
      "ds_write_b128 v188, v[180:183] offset:28672\n"
      "v_bfe_i32 v2, v0, 5, 1\n"
      "v_and_b32 v2, 0x100, v2\n"
      "v_xor_b32 v228, v228, v2\n"
      "v_bfe_i32 v2, v0, 6, 1\n"
      "v_and_b32 v2, 0x400, v2\n"
      "v_xor_b32 v228, v228, v2\n"
      "v_bfe_i32 v2, v0, 7, 1\n"
      "v_and_b32 v2, 0x2000, v2\n"
      "v_mfma_f32_32x32x16_bf16 a[160:175], v[180:183], v[180:183], 0\n"
      "v_xor_b32 v228, v228, v2\n"
      "v_xor_b32 v231, 0x280, v228\n"
      "v_xor_b32 v230, 0x200, v228\n"
      "v_xor_b32 v229, 0x80, v228\n"
      "v_add_u32 v228, 0xc00, v228\n"
      "v_add_u32 v229, 0x800, v229\n"
      "v_add_u32 v230, 0x400, v230\n"
      "s_and_b32 s91, s50, 8192\n"
      "ds_write_b128 v189, v[180:183] offset:49152\n"
      "v_lshlrev_b32 v4, 8, v0\n"
      "v_and_b32 v4, 0x800, v4\n"
      "v_lshlrev_b32 v5, 7, v0\n"
      "v_and_b32 v6, 0x380, v5\n"
      "v_or3_b32 v4, v4, v6, s91\n"
      "v_and_b32 v6, 0x60, v241\n"
      "v_and_b32 v2, 48, v0\n"
      "v_xor_b32 v6, v6, v2\n"
      "v_mfma_f32_32x32x16_bf16 a[176:191], v[180:183], v[180:183], 0\n"
      "v_or_b32 v242, v4, v6\n"
      "v_xor_b32 v243, 64, v242\n"
      "v_lshlrev_b32 v4, 6, v0\n"
      "v_and_b32 v6, 0x800, v4\n"
      "v_lshlrev_b32 v2, 5, v0\n"
      "v_and_b32 v2, 0x380, v2\n"
      "s_add_u32 s69, s91, 16384\n"
      "v_or3_b32 v6, v6, v2, s69\n"
      "ds_write_b128 v189, v[180:183] offset:53248\n"
      "v_lshlrev_b32 v2, 3, v0\n"
      "v_and_or_b32 v6, v2, 24, v6\n"
      "v_lshlrev_b32 v2, 2, v0\n"
      "v_and_b32 v2, 0x60, v2\n"
      "v_xor_b32 v244, v6, v2\n"
      "v_xor_b32 v245, 0x20, v244\n"
      "v_xor_b32 v246, 0x40, v244\n"
      "v_xor_b32 v247, 0x60, v244\n"
      "v_mfma_f32_32x32x16_bf16 a[192:207], v[180:183], v[180:183], 0\n"
      "v_and_b32 v5, 0x1e00, v5\n"
      "v_and_or_b32 v250, v241, 48, v5\n"
      "s_lshl_b32 s69, %[blk], 9\n"
      "s_and_b32 s69, s69, 0x1e00\n"
      "v_and_b32 v4, 0x1c0, v4\n"
      "v_or_b32 v251, s69, v4\n"
      "s_and_b32 s69, s50, 4096\n"
      "s_lshl_b32 s69, s69, 2\n"
      "ds_write_b128 v190, v[180:183] offset:0\n"
      "s_add_u32 s69, s69, 131072\n"
      "s_sub_u32 s91, 16384, s91\n"
      "v_add_u32 v196, s69, v242\n"
      "v_add_u32 v198, s91, v242\n"
      "v_xor_b32 v214, 0x2000, v196\n"
      "v_add_u32 v197, s69, v243\n"
      "v_add_u32 v199, s91, v243\n"
      "v_xor_b32 v215, 0x2000, v197\n"
      "v_mfma_f32_32x32x16_bf16 v[128:143], v[180:183], v[180:183], 0\n"
      "s_add_u32 s69, s50, 131072\n"
      "v_add_u32 v210, s69, v241\n"
      "v_and_b32 v3, 3, v0\n"
      "v_mov_b32 v4, 0x3f803f80\n"
      "s_mov_b32 s94, 0x3e0293ee\n"
      "s_mov_b32 s95, s94\n"
      "v_cmp_eq_u32 vcc, 0, v3\n"
      "s_nop 1\n"
      "ds_write_b128 v190, v[180:183] offset:2048\n"
      "v_cndmask_b32 v5, 0, v4, vcc\n"
      "v_accvgpr_write_b32 a128, v5\n"
      "v_accvgpr_write_b32 a129, v5\n"
      "v_accvgpr_write_b32 a130, v5\n"
      "v_accvgpr_write_b32 a131, v5\n"
      "v_cmp_eq_u32 vcc, 1, v3\n"
      "s_nop 1\n"
      "v_cndmask_b32 v5, 0, v4, vcc\n"
      "v_mfma_f32_32x32x16_bf16 v[48:63], v[180:183], v[180:183], 0\n"
      "v_accvgpr_write_b32 a132, v5\n"
      "v_accvgpr_write_b32 a133, v5\n"
      "v_accvgpr_write_b32 a134, v5\n"
      "v_accvgpr_write_b32 a135, v5\n"
      "v_cmp_eq_u32 vcc, 2, v3\n"
      "s_nop 1\n"
      "v_cndmask_b32 v5, 0, v4, vcc\n"
      "v_accvgpr_write_b32 a136, v5\n"
      "v_mfma_f32_32x32x16_bf16 v[32:47], v[180:183], v[180:183], 0\n"
      "v_accvgpr_write_b32 a137, v5\n"
      "v_accvgpr_write_b32 a138, v5\n"
      "v_accvgpr_write_b32 a139, v5\n"
      "v_cmp_eq_u32 vcc, 3, v3\n"
      "s_nop 1\n"
      "v_cndmask_b32 v5, 0, v4, vcc\n"
      "v_accvgpr_write_b32 a140, v5\n"
      "v_accvgpr_write_b32 a141, v5\n"
      "v_mfma_f32_32x32x16_bf16 v[16:31], v[180:183], v[180:183], 0\n"
      "v_accvgpr_write_b32 a142, v5\n"
      "v_accvgpr_write_b32 a143, v5\n"
      "s_sub_u32 s56, s56, 0xc00\n"
      "s_subb_u32 s57, s57, 0\n"
      "s_sub_u32 s32, s32, 0xc00\n"
      "s_subb_u32 s33, s33, 0\n"
      "s_xor_b64 s[32:33], s[32:33], s[56:57]\n"
      "s_mov_b32 s69, 0\n"
      "s_bitcmp1_b32 s44, 4\n"
      "s_cselect_b32 s66, 0x10000, 0\n"
      "s_xor_b32 s69, s69, s66\n"
      "s_bitcmp1_b32 s44, 0\n"
      "s_cselect_b32 s66, 0x800, 0\n"
      "s_xor_b32 s69, s69, s66\n"
      "s_bitcmp1_b32 s44, 2\n"
      "s_cselect_b32 s66, 0x40800, 0\n"
      "s_xor_b32 s69, s69, s66\n"
      "s_bitcmp1_b32 s44, 4\n"
      "s_cselect_b32 s66, 0x4000, 0\n"
      "s_xor_b32 s69, s69, s66\n"
      "s_mov_b32 s60, s69\n"
      "s_mov_b32 s62, s50\n"
      "v_and_b32 v3, 1, v0\n"
      "v_bfe_u32 v4, v0, 1, 5\n"
      "v_lshlrev_b32 v3, 1, v3\n"
      "v_sub_u32 v4, v4, v3\n"
      "v_mov_b32 v219, s60\n"
      "v_bfe_i32 v2, v4, 0, 1\n"
      "v_and_b32 v2, 0x10000, v2\n"
      "v_xor_b32 v219, v219, v2\n"
      "v_bfe_i32 v2, v4, 1, 1\n"
      "v_and_b32 v2, 0x800, v2\n"
      "v_xor_b32 v219, v219, v2\n"
      "v_bfe_i32 v2, v4, 2, 1\n"
      "v_and_b32 v2, 0x40800, v2\n"
      "v_xor_b32 v219, v219, v2\n"
      "v_bfe_i32 v2, v4, 3, 1\n"
      "v_and_b32 v2, 0x80000, v2\n"
      "v_xor_b32 v219, v219, v2\n"
      "v_bfe_i32 v2, v4, 4, 1\n"
      "v_and_b32 v2, 0x4000, v2\n"
      "v_xor_b32 v219, v219, v2\n"
      "v_writelane_b32 v219, 0, 1\n"
      "s_mov_b32 s61, 0\n"
      "v_mfma_f32_32x32x16_bf16 v[0:15], v[180:183], v[180:183], 0\n"
      "s_mov_b64 s[30:31], s[22:23]\n"
      "s_add_u32 s28, s12, s71\n"
      "s_addc_u32 s29, s13, s72\n"
      "s_lshl_b32 s69, s43, 13\n"
      "s_add_u32 s45, s69, s75\n"
      "s_mov_b32 m0, s62\n"
      "s_nop 0\n"
      "buffer_load_dwordx4 v228, s[56:59], s60 offen nt lds\n"
      "buffer_load_dwordx4 v229, s[56:59], s60 offen offset:1024 nt lds\n"
      "buffer_load_dwordx4 v230, s[56:59], s60 offen offset:2048 nt lds\n"
      "buffer_load_dwordx4 v231, s[56:59], s60 offen offset:3072 nt lds\n"
      "s_xor_b64 s[56:57], s[56:57], s[32:33]\n"
      "s_add_u32 s61, s61, 1\n"
      "s_add_u32 s62, s62, 0x4000\n"
      "s_and_b32 s62, s62, 0x1ffff\n"
      "v_readlane_b32 s60, v219, s61\n"
      "s_xor_b64 s[56:57], s[56:57], s[32:33]\n"
      "s_add_u32 s61, s61, 1\n"
      "s_add_u32 s62, s62, 0x4000\n"
      "s_and_b32 s62, s62, 0x1ffff\n"
      "v_readlane_b32 s60, v219, s61\n"
      "s_waitcnt vmcnt(8)\n"
      "s_memrealtime s[68:69]\n"
      "ds_read_b128 v[64:67], v210 offset:0\n"
      "ds_read_b128 v[68:71], v210 offset:1024\n"
      "ds_read_b128 v[72:75], v210 offset:2048\n"
      "ds_read_b128 v[76:79], v210 offset:3072\n"
      "ds_read_b128 v[80:83], v210 offset:16384\n"
      "ds_read_b128 v[84:87], v210 offset:17408\n"
      "ds_read_b128 v[88:91], v210 offset:18432\n"
      "ds_read_b128 v[92:95], v210 offset:19456\n"
      "s_add_u32 s20, s18, 65536\n"
      "s_addc_u32 s21, s19, 0\n"
      "s_lshl_b32 s75, %[blk], 6\n"
      "s_xor_b32 s74, s50, 4096\n"
      "s_add_u32 s75, s75, s74\n"
      "s_waitcnt lgkmcnt(0)\n"
      "s_sub_u32 s67, s68, s82\n"
      "s_min_u32 s67, s67, 165\n"
      "s_add_u32 s93, s82, s67\n"
      "s_sub_u32 s71, s68, 400\n"
      "s_cmp_lt_u32 s75, 512\n"
      "s_cbranch_scc0 NOLEAD_%=\n"
      "v_mov_b32 v188, s68\n"
      "v_mov_b32 v189, s68\n"
      "v_mov_b32 v190, s68\n"
      "v_mov_b32 v191, s68\n"
      "buffer_store_dwordx4 v[188:191], v250, s[20:23], s75 offen sc1\n"
      "s_nop 1\n"
      "NOLEAD_%=:\n"
      "s_mov_b32 m0, s62\n"
      "s_nop 0\n"
      "buffer_load_dwordx4 v228, s[56:59], s60 offen nt lds\n"
      "buffer_load_dwordx4 v229, s[56:59], s60 offen offset:1024 nt lds\n"
      "buffer_load_dwordx4 v230, s[56:59], s60 offen offset:2048 nt lds\n"
      "buffer_load_dwordx4 v231, s[56:59], s60 offen offset:3072 nt lds\n"
      "s_xor_b64 s[56:57], s[56:57], s[32:33]\n"
      "s_add_u32 s61, s61, 1\n"
      "s_add_u32 s62, s62, 0x4000\n"
      "s_and_b32 s62, s62, 0x1ffff\n"
      "v_readlane_b32 s60, v219, s61\n"
      "s_xor_b64 s[56:57], s[56:57], s[32:33]\n"
      "s_add_u32 s61, s61, 1\n"
      "s_add_u32 s62, s62, 0x4000\n"
      "s_and_b32 s62, s62, 0x1ffff\n"
      "v_readlane_b32 s60, v219, s61\n"
      "s_add_u32 m0, s45, 49152\n"
      "s_nop 0\n"
      "buffer_load_dwordx4 v248, s[28:31], 0 offen nt lds\n"
      "s_bitset1_b32 m0, 10\n"
      "s_nop 0\n"
      "buffer_load_dwordx4 v249, s[28:31], 0 offen nt lds\n"
      "v_lshlrev_b32 v188, 16, v64\n"
      "v_and_b32 v189, 0xffff0000, v64\n"
      "v_lshlrev_b32 v190, 16, v65\n"
      "v_and_b32 v191, 0xffff0000, v65\n"
      "v_lshlrev_b32 v192, 16, v66\n"
      "v_and_b32 v193, 0xffff0000, v66\n"
      "v_lshlrev_b32 v194, 16, v67\n"
      "v_and_b32 v195, 0xffff0000, v67\n"
      "v_pk_mul_f32 v[188:189], v[188:189], s[94:95]\n"
      "v_pk_mul_f32 v[190:191], v[190:191], s[94:95]\n"
      "v_pk_mul_f32 v[192:193], v[192:193], s[94:95]\n"
      "v_pk_mul_f32 v[194:195], v[194:195], s[94:95]\n"
      "v_cvt_pk_bf16_f32 v64, v188, v189\n"
      "v_cvt_pk_bf16_f32 v65, v190, v191\n"
      "v_cvt_pk_bf16_f32 v66, v192, v193\n"
      "v_cvt_pk_bf16_f32 v67, v194, v195\n"
      "ds_write_b128 v210, v[64:67] offset:0\n"
      "v_lshlrev_b32 v188, 16, v68\n"
      "v_and_b32 v189, 0xffff0000, v68\n"
      "v_lshlrev_b32 v190, 16, v69\n"
      "v_and_b32 v191, 0xffff0000, v69\n"
      "v_lshlrev_b32 v192, 16, v70\n"
      "v_and_b32 v193, 0xffff0000, v70\n"
      "v_lshlrev_b32 v194, 16, v71\n"
      "v_and_b32 v195, 0xffff0000, v71\n"
      "v_pk_mul_f32 v[188:189], v[188:189], s[94:95]\n"
      "v_pk_mul_f32 v[190:191], v[190:191], s[94:95]\n"
      "v_pk_mul_f32 v[192:193], v[192:193], s[94:95]\n"
      "v_pk_mul_f32 v[194:195], v[194:195], s[94:95]\n"
      "v_cvt_pk_bf16_f32 v68, v188, v189\n"
      "v_cvt_pk_bf16_f32 v69, v190, v191\n"
      "v_cvt_pk_bf16_f32 v70, v192, v193\n"
      "v_cvt_pk_bf16_f32 v71, v194, v195\n"
      "ds_write_b128 v210, v[68:71] offset:1024\n"
      "v_lshlrev_b32 v188, 16, v72\n"
      "v_and_b32 v189, 0xffff0000, v72\n"
      "v_lshlrev_b32 v190, 16, v73\n"
      "v_and_b32 v191, 0xffff0000, v73\n"
      "v_lshlrev_b32 v192, 16, v74\n"
      "v_and_b32 v193, 0xffff0000, v74\n"
      "v_lshlrev_b32 v194, 16, v75\n"
      "v_and_b32 v195, 0xffff0000, v75\n"
      "v_pk_mul_f32 v[188:189], v[188:189], s[94:95]\n"
      "v_pk_mul_f32 v[190:191], v[190:191], s[94:95]\n"
      "v_pk_mul_f32 v[192:193], v[192:193], s[94:95]\n"
      "v_pk_mul_f32 v[194:195], v[194:195], s[94:95]\n"
      "v_cvt_pk_bf16_f32 v72, v188, v189\n"
      "v_cvt_pk_bf16_f32 v73, v190, v191\n"
      "v_cvt_pk_bf16_f32 v74, v192, v193\n"
      "v_cvt_pk_bf16_f32 v75, v194, v195\n"
      "ds_write_b128 v210, v[72:75] offset:2048\n"
      "v_lshlrev_b32 v188, 16, v76\n"
      "v_and_b32 v189, 0xffff0000, v76\n"
      "v_lshlrev_b32 v190, 16, v77\n"
      "v_and_b32 v191, 0xffff0000, v77\n"
      "v_lshlrev_b32 v192, 16, v78\n"
      "v_and_b32 v193, 0xffff0000, v78\n"
      "v_lshlrev_b32 v194, 16, v79\n"
      "v_and_b32 v195, 0xffff0000, v79\n"
      "v_pk_mul_f32 v[188:189], v[188:189], s[94:95]\n"
      "v_pk_mul_f32 v[190:191], v[190:191], s[94:95]\n"
      "v_pk_mul_f32 v[192:193], v[192:193], s[94:95]\n"
      "v_pk_mul_f32 v[194:195], v[194:195], s[94:95]\n"
      "v_cvt_pk_bf16_f32 v76, v188, v189\n"
      "v_cvt_pk_bf16_f32 v77, v190, v191\n"
      "v_cvt_pk_bf16_f32 v78, v192, v193\n"
      "v_cvt_pk_bf16_f32 v79, v194, v195\n"
      "ds_write_b128 v210, v[76:79] offset:3072\n"
      "v_lshlrev_b32 v188, 16, v80\n"
      "v_and_b32 v189, 0xffff0000, v80\n"
      "v_lshlrev_b32 v190, 16, v81\n"
      "v_and_b32 v191, 0xffff0000, v81\n"
      "v_lshlrev_b32 v192, 16, v82\n"
      "v_and_b32 v193, 0xffff0000, v82\n"
      "v_lshlrev_b32 v194, 16, v83\n"
      "v_and_b32 v195, 0xffff0000, v83\n"
      "v_pk_mul_f32 v[188:189], v[188:189], s[94:95]\n"
      "v_pk_mul_f32 v[190:191], v[190:191], s[94:95]\n"
      "v_pk_mul_f32 v[192:193], v[192:193], s[94:95]\n"
      "v_pk_mul_f32 v[194:195], v[194:195], s[94:95]\n"
      "v_cvt_pk_bf16_f32 v80, v188, v189\n"
      "v_cvt_pk_bf16_f32 v81, v190, v191\n"
      "v_cvt_pk_bf16_f32 v82, v192, v193\n"
      "v_cvt_pk_bf16_f32 v83, v194, v195\n"
      "ds_write_b128 v210, v[80:83] offset:16384\n"
      "v_lshlrev_b32 v188, 16, v84\n"
      "v_and_b32 v189, 0xffff0000, v84\n"
      "v_lshlrev_b32 v190, 16, v85\n"
      "v_and_b32 v191, 0xffff0000, v85\n"
      "v_lshlrev_b32 v192, 16, v86\n"
      "v_and_b32 v193, 0xffff0000, v86\n"
      "v_lshlrev_b32 v194, 16, v87\n"
      "v_and_b32 v195, 0xffff0000, v87\n"
      "v_pk_mul_f32 v[188:189], v[188:189], s[94:95]\n"
      "v_pk_mul_f32 v[190:191], v[190:191], s[94:95]\n"
      "v_pk_mul_f32 v[192:193], v[192:193], s[94:95]\n"
      "v_pk_mul_f32 v[194:195], v[194:195], s[94:95]\n"
      "v_cvt_pk_bf16_f32 v84, v188, v189\n"
      "v_cvt_pk_bf16_f32 v85, v190, v191\n"
      "v_cvt_pk_bf16_f32 v86, v192, v193\n"
      "v_cvt_pk_bf16_f32 v87, v194, v195\n"
      "ds_write_b128 v210, v[84:87] offset:17408\n"
      "v_lshlrev_b32 v188, 16, v88\n"
      "v_and_b32 v189, 0xffff0000, v88\n"
      "v_lshlrev_b32 v190, 16, v89\n"
      "v_and_b32 v191, 0xffff0000, v89\n"
      "v_lshlrev_b32 v192, 16, v90\n"
      "v_and_b32 v193, 0xffff0000, v90\n"
      "v_lshlrev_b32 v194, 16, v91\n"
      "v_and_b32 v195, 0xffff0000, v91\n"
      "v_pk_mul_f32 v[188:189], v[188:189], s[94:95]\n"
      "v_pk_mul_f32 v[190:191], v[190:191], s[94:95]\n"
      "v_pk_mul_f32 v[192:193], v[192:193], s[94:95]\n"
      "v_pk_mul_f32 v[194:195], v[194:195], s[94:95]\n"
      "v_cvt_pk_bf16_f32 v88, v188, v189\n"
      "v_cvt_pk_bf16_f32 v89, v190, v191\n"
      "v_cvt_pk_bf16_f32 v90, v192, v193\n"
      "v_cvt_pk_bf16_f32 v91, v194, v195\n"
      "ds_write_b128 v210, v[88:91] offset:18432\n"
      "v_lshlrev_b32 v188, 16, v92\n"
      "v_and_b32 v189, 0xffff0000, v92\n"
      "v_lshlrev_b32 v190, 16, v93\n"
      "v_and_b32 v191, 0xffff0000, v93\n"
      "v_lshlrev_b32 v192, 16, v94\n"
      "v_and_b32 v193, 0xffff0000, v94\n"
      "v_lshlrev_b32 v194, 16, v95\n"
      "v_and_b32 v195, 0xffff0000, v95\n"
      "v_pk_mul_f32 v[188:189], v[188:189], s[94:95]\n"
      "v_pk_mul_f32 v[190:191], v[190:191], s[94:95]\n"
      "v_pk_mul_f32 v[192:193], v[192:193], s[94:95]\n"
      "v_pk_mul_f32 v[194:195], v[194:195], s[94:95]\n"
      "v_cvt_pk_bf16_f32 v92, v188, v189\n"
      "v_cvt_pk_bf16_f32 v93, v190, v191\n"
      "v_cvt_pk_bf16_f32 v94, v192, v193\n"
      "v_cvt_pk_bf16_f32 v95, v194, v195\n"
      "ds_write_b128 v210, v[92:95] offset:19456\n"
      "s_mov_b32 m0, s62\n"
      "s_nop 0\n"
      "buffer_load_dwordx4 v228, s[56:59], s60 offen nt lds\n"
      "buffer_load_dwordx4 v229, s[56:59], s60 offen offset:1024 nt lds\n"
      "buffer_load_dwordx4 v230, s[56:59], s60 offen offset:2048 nt lds\n"
      "buffer_load_dwordx4 v231, s[56:59], s60 offen offset:3072 nt lds\n"
      "s_xor_b64 s[56:57], s[56:57], s[32:33]\n"
      "s_add_u32 s61, s61, 1\n"
      "s_add_u32 s62, s62, 0x4000\n"
      "s_and_b32 s62, s62, 0x1ffff\n"
      "v_readlane_b32 s60, v219, s61\n"
      "s_cmp_eq_u32 s50, 0\n"
      "s_cselect_b32 s38, 0x44554001, 1\n"
      "s_cselect_b32 s39, 0x2a040083, 3\n"
      "s_cselect_b32 s52, 0x31109890, 0\n"
      "s_cselect_b32 s53, 0x80011, 0\n"
      "s_mov_b64 s[34:35], 0\n"
      "s_bitset1_b32 s39, 29\n"
      "s_and_b32 s69, %[blk], 7\n"
      "s_mul_i32 s69, s69, 5\n"
      "s_lshr_b32 s74, %[blk], 4\n"
      "s_add_u32 s69, s69, s74\n"
      "s_and_b32 s69, s69, 15\n"
      "s_mul_i32 s74, s69, 16\n"
      "s_lshr_b32 s74, s74, 6\n"
      "s_lshl2_add_u32 s76, s74, 9\n"
      "s_bitset1_b64 s[34:35], s76\n"
      "s_lshl_b32 s74, s74, 1\n"
      "s_sub_u32 s74, 28, s74\n"
      "s_bitset1_b32 s39, s74\n"
      "s_mul_i32 s74, s69, 20\n"
      "s_lshr_b32 s74, s74, 6\n"
      "s_lshl2_add_u32 s76, s74, 25\n"
      "s_bitset1_b64 s[34:35], s76\n"
      "s_lshl_b32 s74, s74, 1\n"
      "s_sub_u32 s74, 20, s74\n"
      "s_bitset1_b32 s39, s74\n"
      "s_mul_i32 s74, s69, 20\n"
      "s_lshr_b32 s74, s74, 6\n"
      "s_lshl2_add_u32 s76, s74, 45\n"
      "s_bitset1_b64 s[34:35], s76\n"
      "s_lshl_b32 s74, s74, 1\n"
      "s_sub_u32 s74, 10, s74\n"
      "s_bitset1_b32 s39, s74\n"
      "s_mov_b32 s54, 3\n"
      "s_mov_b32 s70, 250\n"
      "s_mov_b32 s86, 59\n"
      "s_mov_b32 s77, 177\n"
      "s_lshl_b32 s91, s50, 16\n"
      "s_sub_u32 s80, 46, s91\n"
      "s_sub_u32 s90, 34, s91\n"
      "s_sub_u32 s72, s75, 18432\n"
      "s_and_b32 s79, %[blk], 31\n"
      "s_lshl_b32 s79, s79, 8\n"
      "s_add_u32 s79, s79, 8192\n"
      "s_mul_i32 s69, %[blk], 53\n"
      "s_lshr_b32 s69, s69, 8\n"
      "s_add_u32 s66, s93, s69\n"
      "s_add_u32 s66, s66, 112\n"
      "s_sub_u32 s81, s93, s71\n"
      "s_add_u32 s81, s81, -15\n"
      "s_sub_u32 s66, s66, s91\n"
      "s_mov_b32 s88, s66\n"
      "s_mov_b32 s65, s66\n"
      "s_getpc_b64 s[84:85]\n"
      "PC_%=:\n"
      "s_add_u32 s84, s84, HOOK_%=-PC_%=\n"
      "s_addc_u32 s85, s85, 0\n"
      "s_waitcnt vmcnt(14) lgkmcnt(0)\n"
      "s_barrier\n"
      "ds_read_b128 a[64:67], v198 offset:8192\n"
      "ds_read_b128 a[72:75], v199 offset:8192\n"
      "ds_read_b128 a[80:83], v198 offset:9216\n"
      "ds_read_b128 a[88:91], v199 offset:9216\n"
      "ds_read_b128 a[0:3], v196 offset:0\n"
      "ds_read_b128 a[16:19], v197 offset:0\n"
      "ds_read_b128 a[32:35], v196 offset:1024\n"
      "ds_read_b128 a[48:51], v197 offset:1024\n"
      "ds_read_b128 a[4:7], v196 offset:4096\n"
      "ds_read_b128 a[20:23], v197 offset:4096\n"
      "ds_read_b128 a[36:39], v196 offset:5120\n"
      "ds_read_b128 a[52:55], v197 offset:5120\n"
      "v_readfirstlane_b32 s40, v208\n"
      "s_mov_b32 s94, 0xceabfb8f\n"
      "v_lshlrev_b32 v144, 16, v200\n"
      "v_and_b32 v145, 0xffff0000, v200\n"
      "v_lshlrev_b32 v146, 16, v201\n"
      "v_and_b32 v147, 0xffff0000, v201\n"
      "v_lshlrev_b32 v148, 16, v202\n"
      "v_and_b32 v149, 0xffff0000, v202\n"
      "v_lshlrev_b32 v150, 16, v203\n"
      "v_and_b32 v151, 0xffff0000, v203\n"
      "v_lshlrev_b32 v152, 16, v204\n"
      "v_and_b32 v153, 0xffff0000, v204\n"
      "v_lshlrev_b32 v154, 16, v205\n"
      "v_and_b32 v155, 0xffff0000, v205\n"
      "v_lshlrev_b32 v156, 16, v206\n"
      "v_and_b32 v157, 0xffff0000, v206\n"
      "v_lshlrev_b32 v158, 16, v207\n"
      "v_and_b32 v159, 0xffff0000, v207\n"
      "s_waitcnt lgkmcnt(4)\n"
      "v_mfma_f32_16x16x32_bf16 v[64:67], a[64:67], a[0:3], 0\n"
      "ds_read_b128 a[8:11], v214 offset:0\n"
      "v_fma_f32 v144, -v144, s94, s94\n"
      "v_mfma_f32_16x16x32_bf16 v[64:67], a[72:75], a[16:19], v[64:67]\n"
      "ds_read_b128 a[24:27], v215 offset:0\n"
      "v_fma_f32 v145, -v145, s94, s94\n"
      "v_mfma_f32_16x16x32_bf16 v[64:67], a[80:83], a[32:35], v[64:67]\n"
      "ds_read_b128 a[40:43], v214 offset:1024\n"
      "v_fma_f32 v146, -v146, s94, s94\n"
      "v_mfma_f32_16x16x32_bf16 v[64:67], a[88:91], a[48:51], v[64:67]\n"
      "ds_read_b128 a[56:59], v215 offset:1024\n"
      "v_fma_f32 v147, -v147, s94, s94\n"
      "s_waitcnt lgkmcnt(4)\n"
      "v_mfma_f32_16x16x32_bf16 v[72:75], a[64:67], a[4:7], 0\n"
      "ds_read_b128 a[12:15], v214 offset:4096\n"
      "v_fma_f32 v148, -v148, s94, s94\n"
      "v_mfma_f32_16x16x32_bf16 v[72:75], a[72:75], a[20:23], v[72:75]\n"
      "ds_read_b128 a[28:31], v215 offset:4096\n"
      "v_fma_f32 v149, -v149, s94, s94\n"
      "v_mfma_f32_16x16x32_bf16 v[72:75], a[80:83], a[36:39], v[72:75]\n"
      "ds_read_b128 a[44:47], v214 offset:5120\n"
      "v_fma_f32 v150, -v150, s94, s94\n"
      "v_mfma_f32_16x16x32_bf16 v[72:75], a[88:91], a[52:55], v[72:75]\n"
      "ds_read_b128 a[60:63], v215 offset:5120\n"
      "v_fma_f32 v151, -v151, s94, s94\n"
      "s_waitcnt lgkmcnt(4)\n"
      "v_mfma_f32_16x16x32_bf16 v[80:83], a[64:67], a[8:11], 0\n"
      "ds_read_b128 a[96:99], v198 offset:0\n"
      "v_fma_f32 v152, -v152, s94, s94\n"
      "v_mfma_f32_16x16x32_bf16 v[80:83], a[72:75], a[24:27], v[80:83]\n"
      "ds_read_b128 a[100:103], v198 offset:4096\n"
      "v_fma_f32 v153, -v153, s94, s94\n"
      "v_mfma_f32_16x16x32_bf16 v[80:83], a[80:83], a[40:43], v[80:83]\n"
      "ds_read_b128 a[104:107], v199 offset:0\n"
      "v_fma_f32 v154, -v154, s94, s94\n"
      "v_mfma_f32_16x16x32_bf16 v[80:83], a[88:91], a[56:59], v[80:83]\n"
      "ds_read_b128 a[108:111], v199 offset:4096\n"
      "v_fma_f32 v155, -v155, s94, s94\n"
      "s_waitcnt lgkmcnt(4)\n"
      "v_mfma_f32_16x16x32_bf16 v[88:91], a[64:67], a[12:15], 0\n"
      "ds_read_b128 a[112:115], v198 offset:1024\n"
      "v_fma_f32 v156, -v156, s94, s94\n"
      "v_mfma_f32_16x16x32_bf16 v[88:91], a[72:75], a[28:31], v[88:91]\n"
      "ds_read_b128 a[116:119], v198 offset:5120\n"
      "v_fma_f32 v157, -v157, s94, s94\n"
      "v_mfma_f32_16x16x32_bf16 v[88:91], a[80:83], a[44:47], v[88:91]\n"
      "ds_read_b128 a[120:123], v199 offset:1024\n"
      "v_fma_f32 v158, -v158, s94, s94\n"
      "v_mfma_f32_16x16x32_bf16 v[88:91], a[88:91], a[60:63], v[88:91]\n"
      "ds_read_b128 a[124:127], v199 offset:5120\n"
      "v_fma_f32 v159, -v159, s94, s94\n"
      "s_mov_b32 m0, s62\n"
      "v_add_u32 v248, s60, v228\n"
      "buffer_load_dwordx4 v229, s[56:59], s60 offen offset:1024 nt lds\n"
      "buffer_load_dwordx4 v231, s[56:59], s60 offen offset:3072 nt lds\n"
      "v_add_u32 v249, s60, v230\n"
      "s_waitcnt lgkmcnt(0)\n"
      "v_mfma_f32_16x16x32_bf16 v[96:99], a[96:99], a[0:3], v[144:147]\n"
      "s_add_u32 s40, s40, 0x7f4a7c15\n"
      "v_mfma_f32_16x16x32_bf16 v[96:99], a[104:107], a[16:19], v[96:99]\n"
      "s_lshl_b32 s69, s44, 3\n"
      "v_max3_f32 v188, v64, v65, v66\n"
      "v_max3_f32 v189, v72, v73, v74\n"
      "v_mfma_f32_16x16x32_bf16 v[96:99], a[112:115], a[32:35], v[96:99]\n"
      "s_lshr_b32 s74, s50, 12\n"
      "v_max3_f32 v190, v80, v81, v82\n"
      "v_max3_f32 v191, v88, v89, v90\n"
      "v_mfma_f32_16x16x32_bf16 v[96:99], a[120:123], a[48:51], v[96:99]\n"
      "s_add_u32 s69, s69, s74\n"
      "v_max_f32 v188, v188, v67\n"
      "v_max_f32 v189, v189, v75\n"
      "v_mfma_f32_16x16x32_bf16 v[104:107], a[96:99], a[4:7], v[152:155]\n"
      "s_mul_i32 s69, s69, 4608\n"
      "v_max_f32 v190, v190, v83\n"
      "v_max_f32 v191, v191, v91\n"
      "v_mfma_f32_16x16x32_bf16 v[104:107], a[104:107], a[20:23], v[104:107]\n"
      "s_add_u32 s69, s69, 81920\n"
      "v_mov_b32 v192, v188\n"
      "v_mov_b32 v193, v189\n"
      "v_mfma_f32_16x16x32_bf16 v[104:107], a[112:115], a[36:39], v[104:107]\n"
      "s_add_u32 s24, s18, s69\n"
      "v_mov_b32 v194, v190\n"
      "v_mov_b32 v195, v191\n"
      "v_mfma_f32_16x16x32_bf16 v[104:107], a[120:123], a[52:55], v[104:107]\n"
      "s_addc_u32 s25, s19, 0\n"
      "v_permlane16_swap_b32 v188, v192\n"
      "v_permlane16_swap_b32 v189, v193\n"
      "v_mfma_f32_16x16x32_bf16 v[112:115], a[96:99], a[8:11], v[144:147]\n"
      "s_movk_i32 s26, 23040\n"
      "v_permlane16_swap_b32 v190, v194\n"
      "v_permlane16_swap_b32 v191, v195\n"
      "v_mfma_f32_16x16x32_bf16 v[112:115], a[104:107], a[24:27], v[112:115]\n"
      "s_mul_i32 s78, s43, 18432\n"
      "v_max_f32 v188, v188, v192\n"
      "v_max_f32 v189, v189, v193\n"
      "v_mfma_f32_16x16x32_bf16 v[112:115], a[112:115], a[40:43], v[112:115]\n"
      "s_and_b32 s69, s50, 4096\n"
      "v_max_f32 v190, v190, v194\n"
      "v_max_f32 v191, v191, v195\n"
      "v_mfma_f32_16x16x32_bf16 v[112:115], a[120:123], a[56:59], v[112:115]\n"
      "s_lshl_b32 s69, s69, 2\n"
      "v_mov_b32 v192, v188\n"
      "v_mov_b32 v193, v189\n"
      "v_mfma_f32_16x16x32_bf16 v[120:123], a[96:99], a[12:15], v[152:155]\n"
      "s_and_b32 s74, s50, 8192\n"
      "v_mov_b32 v194, v190\n"
      "v_mov_b32 v195, v191\n"
      "v_mfma_f32_16x16x32_bf16 v[120:123], a[104:107], a[28:31], v[120:123]\n"
      "s_add_u32 s74, s74, 4096\n"
      "v_permlane32_swap_b32 v188, v192\n"
      "v_permlane32_swap_b32 v189, v193\n"
      "v_mfma_f32_16x16x32_bf16 v[120:123], a[112:115], a[44:47], v[120:123]\n"
      "s_add_u32 s69, s69, s74\n"
      "v_permlane32_swap_b32 v190, v194\n"
      "v_permlane32_swap_b32 v191, v195\n"
      "v_mfma_f32_16x16x32_bf16 v[120:123], a[120:123], a[60:63], v[120:123]\n"
      "s_lshl_b32 s74, s43, 7\n"
      "v_max_f32 v188, v188, v192\n"
      "v_max_f32 v189, v189, v193\n"
      "v_mfma_f32_16x16x32_bf16 v[100:103], a[100:103], a[0:3], v[148:151]\n"
      "s_add_u32 s69, s69, s74\n"
      "v_max_f32 v190, v190, v194\n"
      "v_max_f32 v191, v191, v195\n"
      "v_mfma_f32_16x16x32_bf16 v[100:103], a[108:111], a[16:19], v[100:103]\n"
      "s_lshl_b32 s74, s44, 15\n"
      "v_xor_b32 v176, 0x80000000, v188\n"
      "v_xor_b32 v177, 0x80000000, v189\n"
      "v_mfma_f32_16x16x32_bf16 v[100:103], a[116:119], a[32:35], v[100:103]\n"
      "s_add_u32 s69, s69, s74\n"
      "v_xor_b32 v178, 0x80000000, v190\n"
      "v_xor_b32 v179, 0x80000000, v191\n"
      "v_mfma_f32_16x16x32_bf16 v[100:103], a[124:127], a[48:51], v[100:103]\n"
      "s_add_u32 s30, s16, s69\n"
      "v_mov_b32 v160, v176\n"
      "v_mov_b32 v161, v176\n"
      "v_mfma_f32_16x16x32_bf16 v[108:111], a[100:103], a[4:7], v[156:159]\n"
      "s_addc_u32 s31, s17, 0\n"
      "v_mov_b32 v162, v176\n"
      "v_mov_b32 v163, v176\n"
      "v_mfma_f32_16x16x32_bf16 v[108:111], a[108:111], a[20:23], v[108:111]\n"
      "v_mov_b32 v164, v177\n"
      "v_mov_b32 v165, v177\n"
      "v_mfma_f32_16x16x32_bf16 v[108:111], a[116:119], a[36:39], v[108:111]\n"
      "v_mov_b32 v166, v177\n"
      "v_mov_b32 v167, v177\n"
      "v_mfma_f32_16x16x32_bf16 v[108:111], a[124:127], a[52:55], v[108:111]\n"
      "v_mov_b32 v168, v178\n"
      "v_mov_b32 v169, v178\n"
      "v_mfma_f32_16x16x32_bf16 v[116:119], a[100:103], a[8:11], v[148:151]\n"
      "v_mov_b32 v170, v178\n"
      "v_mov_b32 v171, v178\n"
      "v_mfma_f32_16x16x32_bf16 v[116:119], a[108:111], a[24:27], v[116:119]\n"
      "v_mov_b32 v172, v179\n"
      "v_mov_b32 v173, v179\n"
      "v_mfma_f32_16x16x32_bf16 v[116:119], a[116:119], a[40:43], v[116:119]\n"
      "v_mov_b32 v174, v179\n"
      "v_mov_b32 v175, v179\n"
      "v_mfma_f32_16x16x32_bf16 v[116:119], a[124:127], a[56:59], v[116:119]\n"
      "v_mov_b32 v209, 139248\n"
      "v_lshl_add_u32 v232, s50, 1, v241\n"
      "v_mfma_f32_16x16x32_bf16 v[124:127], a[100:103], a[12:15], v[156:159]\n"
      "v_add_u32 v232, 0x10000, v232\n"
      "v_xor_b32 v233, 0x4000, v232\n"
      "v_mfma_f32_16x16x32_bf16 v[124:127], a[108:111], a[28:31], v[124:127]\n"
      "v_add_u32 v234, 0x10000, v232\n"
      "v_xor_b32 v235, 0x4000, v234\n"
      "v_mfma_f32_16x16x32_bf16 v[124:127], a[116:119], a[44:47], v[124:127]\n"
      "v_lshrrev_b32 v240, 1, v241\n"
      "v_add_u32 v240, 0x1000, v240\n"
      "v_mfma_f32_16x16x32_bf16 v[124:127], a[124:127], a[60:63], v[124:127]\n"
      "v_mov_b32 v196, 0\n"
      "v_pk_add_f32 v[96:97], v[96:97], v[160:161]\n"
      "v_pk_add_f32 v[98:99], v[98:99], v[160:161]\n"
      "v_pk_add_f32 v[104:105], v[104:105], v[164:165]\n"
      "v_pk_add_f32 v[106:107], v[106:107], v[164:165]\n"
      "v_pk_add_f32 v[112:113], v[112:113], v[168:169]\n"
      "v_pk_add_f32 v[114:115], v[114:115], v[168:169]\n"
      "v_pk_add_f32 v[120:121], v[120:121], v[172:173]\n"
      "v_pk_add_f32 v[122:123], v[122:123], v[172:173]\n"
      "v_pk_add_f32 v[100:101], v[100:101], v[160:161]\n"
      "v_pk_add_f32 v[102:103], v[102:103], v[160:161]\n"
      "v_pk_add_f32 v[108:109], v[108:109], v[164:165]\n"
      "v_pk_add_f32 v[110:111], v[110:111], v[164:165]\n"
      "v_pk_add_f32 v[116:117], v[116:117], v[168:169]\n"
      "v_pk_add_f32 v[118:119], v[118:119], v[168:169]\n"
      "v_pk_add_f32 v[124:125], v[124:125], v[172:173]\n"
      "v_pk_add_f32 v[126:127], v[126:127], v[172:173]\n"
      "v_mov_b32 v224, v244\n"
      "v_mov_b32 v225, v245\n"
      "v_mov_b32 v226, v246\n"
      "v_mov_b32 v227, v247\n"
      "v_xor_b32 v220, 0x2000, v244\n"
      "v_xor_b32 v221, 0x2000, v245\n"
      "v_xor_b32 v222, 0x2000, v246\n"
      "v_xor_b32 v223, 0x2000, v247\n"
      "s_lshl_b32 s76, s43, 3\n"
      "v_mov_b32 v237, s40\n"
      "v_mov_b32 v238, s76\n"
      "v_xor_b32 v239, 8, v238\n"
      "s_memrealtime s[68:69]\n"
      "s_waitcnt vmcnt(12) lgkmcnt(0)\n"
      "s_sub_u32 s67, s68, s88\n"
      "s_cmp_lt_i32 s67, -8\n"
      "s_cbranch_scc1 GW0_%=\n"
      "GX0_%=:\n"
      "s_barrier\n"
      "ds_read_b128 a[64:67], v242 offset:0\n"
      "ds_read_b128 a[68:71], v242 offset:4096\n"
      "ds_read_b128 a[72:75], v243 offset:0\n"
      "ds_read_b128 a[76:79], v243 offset:4096\n"
      "ds_read_b128 a[80:83], v242 offset:1024\n"
      "ds_read_b128 a[84:87], v242 offset:5120\n"
      "ds_read_b128 a[88:91], v243 offset:1024\n"
      "ds_read_b128 a[92:95], v243 offset:5120\n"
      "ds_write_b32 v209, v196\n"
      "s_add_u32 s38, s38, s38\n"
      "s_memrealtime s[68:69]\n"
      "s_xor_b64 s[56:57], s[56:57], s[32:33]\n"
      "s_add_u32 s61, s61, 1\n"
      "s_add_u32 s62, s62, 0x4000\n"
      "s_and_b32 s62, s62, 0x1ffff\n"
      "v_readlane_b32 s60, v219, s61\n"
      "s_waitcnt lgkmcnt(0)\n"
      "s_sub_u32 s67, s68, s66\n"
      "s_cmp_gt_i32 s67, s70\n"
      "s_cselect_b32 s69, s77, s86\n"
      "s_cselect_b32 s70, 60, s70\n"
      "s_cselect_b32 s90, s80, s90\n"
      "s_add_u32 s66, s66, s69\n"
      "s_sub_u32 s76, s68, s83\n"
      "s_mov_b32 s83, s82\n"
      "LOOP_%=:\n"
      "v_mfma_f32_16x16x32_bf16 v[64:67], a[64:67], a[0:3], v[160:163]\n"
      "s_mov_b32 s82, s68\n"
      "v_exp_f32 v188, v96\n"
      "v_mfma_f32_16x16x32_bf16 v[64:67], a[72:75], a[16:19], v[64:67]\n"
      "s_add_u32 s65, s68, s90\n"
      "v_add_f32 v180, v180, v184\n"
      "v_add_f32 v181, v181, v185\n"
      "v_mfma_f32_16x16x32_bf16 v[64:67], a[80:83], a[32:35], v[64:67]\n"
      "s_max_u32 s88, s66, s65\n"
      "v_exp_f32 v189, v97\n"
      "v_mfma_f32_16x16x32_bf16 v[64:67], a[88:91], a[48:51], v[64:67]\n"
      "s_getreg_b32 s87, hwreg(HW_REG_IB_STS, 0, 4)\n"
      "v_add_f32 v182, v182, v186\n"
      "v_add_f32 v183, v183, v187\n"
      "v_mfma_f32_16x16x32_bf16 v[72:75], a[64:67], a[4:7], v[164:167]\n"
      "s_cmp_lt_i32 s76, 104\n"
      "v_exp_f32 v190, v98\n"
      "v_mfma_f32_16x16x32_bf16 v[72:75], a[72:75], a[20:23], v[72:75]\n"
      "s_cselect_b32 s76, 15, 4\n"
      "v_exp_f32 v191, v99\n"
      "v_mfma_f32_16x16x32_bf16 v[72:75], a[80:83], a[36:39], v[72:75]\n"
      "s_cmp_gt_u32 s87, s76\n"
      "v_cvt_pk_bf16_f32 v144, v188, v189\n"
      "v_cvt_pk_bf16_f32 v145, v190, v191\n"
      "v_mfma_f32_16x16x32_bf16 v[72:75], a[88:91], a[52:55], v[72:75]\n"
      "s_addc_u32 s89, s89, 0\n"
      "v_exp_f32 v192, v100\n"
      "v_mfma_f32_16x16x32_bf16 v[80:83], a[64:67], a[8:11], v[168:171]\n"
      "s_add_u32 s39, s39, s39\n"
      "v_exp_f32 v193, v101\n"
      "v_mfma_f32_16x16x32_bf16 v[80:83], a[72:75], a[24:27], v[80:83]\n"
      "s_memrealtime s[68:69]\n"
      "v_exp_f32 v194, v102\n"
      "v_mfma_f32_16x16x32_bf16 v[80:83], a[80:83], a[40:43], v[80:83]\n"
      "v_exp_f32 v195, v103\n"
      "v_mfma_f32_16x16x32_bf16 v[80:83], a[88:91], a[56:59], v[80:83]\n"
      "s_mov_b32 m0, s62\n"
      "v_cvt_pk_bf16_f32 v146, v192, v193\n"
      "v_cvt_pk_bf16_f32 v147, v194, v195\n"
      "v_mfma_f32_16x16x32_bf16 v[88:91], a[64:67], a[12:15], v[172:175]\n"
      "buffer_load_dwordx4 v228, s[56:59], s60 offen nt lds\n"
      "v_mfma_f32_16x16x32_bf16 v[184:187], a[128:131], v[144:147], 0\n"
      "buffer_load_dwordx4 v229, s[56:59], s60 offen offset:1024 nt lds\n"
      "v_mfma_f32_16x16x32_bf16 v[88:91], a[72:75], a[28:31], v[88:91]\n"
      "buffer_load_dwordx4 v230, s[56:59], s60 offen offset:2048 nt lds\n"
      "v_mfma_f32_16x16x32_bf16 v[88:91], a[80:83], a[44:47], v[88:91]\n"
      "buffer_load_dwordx4 v231, s[56:59], s60 offen offset:3072 nt lds\n"
      "s_cbranch_scc1 SP1_%=\n"
      "HD1_%=:\n"
      "s_cmp_lg_u32 s54, 0\n"
      "s_waitcnt vmcnt(12) lgkmcnt(0)\n"
      "s_cbranch_scc1 W1_%=\n"
      "s_waitcnt vmcnt(8)\n"
      "W1_%=:\n"
      "s_sub_u32 s67, s68, s88\n"
      "s_cmp_lt_i32 s67, -8\n"
      "s_cbranch_scc1 GW4_%=\n"
      "GX4_%=:\n"
      "s_barrier\n"
      "v_mfma_f32_16x16x32_bf16 v[88:91], a[88:91], a[60:63], v[88:91]\n"
      "s_memrealtime s[68:69]\n"
      "ds_read_b64_tr_b16 a[96:97], v244 offset:0\n"
      "ds_read_b64_tr_b16 a[98:99], v244 offset:4096\n"
      "v_mfma_f32_16x16x32_bf16 v[68:71], a[68:71], a[0:3], v[160:163]\n"
      "s_xor_b64 s[56:57], s[56:57], s[32:33]\n"
      "ds_read_b64_tr_b16 a[100:101], v245 offset:0\n"
      "ds_read_b64_tr_b16 a[102:103], v245 offset:4096\n"
      "v_mfma_f32_16x16x32_bf16 v[68:71], a[76:79], a[16:19], v[68:71]\n"
      "s_add_u32 s61, s61, 1\n"
      "ds_read_b64_tr_b16 a[104:105], v246 offset:0\n"
      "ds_read_b64_tr_b16 a[106:107], v246 offset:4096\n"
      "v_mfma_f32_16x16x32_bf16 v[68:71], a[84:87], a[32:35], v[68:71]\n"
      "s_add_u32 s62, s62, 0x4000\n"
      "ds_read_b64_tr_b16 a[108:109], v247 offset:0\n"
      "ds_read_b64_tr_b16 a[110:111], v247 offset:4096\n"
      "v_mfma_f32_16x16x32_bf16 v[68:71], a[92:95], a[48:51], v[68:71]\n"
      "s_and_b32 s62, s62, 0x1ffff\n"
      "v_exp_f32 v188, v104\n"
      "v_mfma_f32_16x16x32_bf16 v[76:79], a[68:71], a[4:7], v[164:167]\n"
      "v_readlane_b32 s60, v219, s61\n"
      "v_exp_f32 v189, v105\n"
      "v_mfma_f32_16x16x32_bf16 v[76:79], a[76:79], a[20:23], v[76:79]\n"
      "v_exp_f32 v190, v106\n"
      "v_mfma_f32_16x16x32_bf16 v[76:79], a[84:87], a[36:39], v[76:79]\n"
      "v_exp_f32 v191, v107\n"
      "v_mfma_f32_16x16x32_bf16 v[76:79], a[92:95], a[52:55], v[76:79]\n"
      "v_cvt_pk_bf16_f32 v148, v188, v189\n"
      "v_cvt_pk_bf16_f32 v149, v190, v191\n"
      "v_mfma_f32_16x16x32_bf16 v[84:87], a[68:71], a[8:11], v[168:171]\n"
      "v_exp_f32 v192, v108\n"
      "v_mfma_f32_16x16x32_bf16 v[84:87], a[76:79], a[24:27], v[84:87]\n"
      "v_exp_f32 v193, v109\n"
      "s_waitcnt lgkmcnt(0)\n"
      "v_mfma_f32_16x16x32_bf16 v[84:87], a[84:87], a[40:43], v[84:87]\n"
      "s_sub_u32 s67, s68, s66\n"
      "s_cmp_gt_i32 s67, s70\n"
      "v_cvt_pk_bf16_f32 v150, v192, v193\n"
      "v_mfma_f32_16x16x32_bf16 v[84:87], a[92:95], a[56:59], v[84:87]\n"
      "s_cselect_b32 s69, s77, s86\n"
      "v_exp_f32 v194, v110\n"
      "v_mfma_f32_16x16x32_bf16 v[92:95], a[68:71], a[12:15], v[172:175]\n"
      "s_cselect_b32 s70, 60, s70\n"
      "ds_read_b64_tr_b16 a[112:113], v244 offset:1024\n"
      "ds_read_b64_tr_b16 a[114:115], v244 offset:5120\n"
      "v_mfma_f32_16x16x32_bf16 v[92:95], a[76:79], a[28:31], v[92:95]\n"
      "s_cselect_b32 s90, s80, s90\n"
      "v_exp_f32 v195, v111\n"
      "v_mfma_f32_16x16x32_bf16 v[92:95], a[84:87], a[44:47], v[92:95]\n"
      "s_add_u32 s66, s66, s69\n"
      "ds_read_b64_tr_b16 a[116:117], v245 offset:1024\n"
      "v_cvt_pk_bf16_f32 v151, v194, v195\n"
      "v_mfma_f32_16x16x32_bf16 v[92:95], a[92:95], a[60:63], v[92:95]\n"
      "s_sub_u32 s76, s68, s83\n"
      "v_exp_f32 v188, v112\n"
      "v_mfma_f32_16x16x32_bf16 v[184:187], a[132:135], v[148:151], v[184:187]\n"
      "s_mov_b32 s83, s82\n"
      "ds_read_b64_tr_b16 a[118:119], v245 offset:5120\n"
      "ds_read_b64_tr_b16 a[120:121], v246 offset:1024\n"
      "v_mfma_f32_16x16x32_bf16 v[0:3], a[96:99], v[128:131], v[0:3]\n"
      "s_mov_b32 s82, s68\n"
      "v_exp_f32 v189, v113\n"
      "v_mfma_f32_16x16x32_bf16 v[4:7], a[96:99], v[132:135], v[4:7]\n"
      "s_add_u32 s65, s68, s90\n"
      "ds_read_b64_tr_b16 a[122:123], v246 offset:5120\n"
      "v_cvt_pk_bf16_f32 v152, v188, v189\n"
      "v_mfma_f32_16x16x32_bf16 a[144:147], a[96:99], v[136:139], a[144:147]\n"
      "s_max_u32 s88, s66, s65\n"
      "v_exp_f32 v190, v114\n"
      "v_mfma_f32_16x16x32_bf16 a[148:151], a[96:99], v[140:143], a[148:151]\n"
      "s_getreg_b32 s87, hwreg(HW_REG_IB_STS, 0, 4)\n"
      "ds_read_b64_tr_b16 a[124:125], v247 offset:1024\n"
      "ds_read_b64_tr_b16 a[126:127], v247 offset:5120\n"
      "v_mfma_f32_16x16x32_bf16 a[156:159], a[100:103], v[140:143], a[156:159]\n"
      "s_cmp_lt_i32 s76, 104\n"
      "v_exp_f32 v191, v115\n"
      "v_mfma_f32_16x16x32_bf16 a[152:155], a[100:103], v[136:139], a[152:155]\n"
      "s_cselect_b32 s76, 15, 4\n"
      "v_exp_f32 v192, v116\n"
      "v_mfma_f32_16x16x32_bf16 v[12:15], a[100:103], v[132:135], v[12:15]\n"
      "s_cmp_gt_u32 s87, s76\n"
      "v_exp_f32 v193, v117\n"
      "v_mfma_f32_16x16x32_bf16 v[8:11], a[100:103], v[128:131], v[8:11]\n"
      "s_addc_u32 s89, s89, 0\n"
      "v_cvt_pk_bf16_f32 v153, v190, v191\n"
      "v_cvt_pk_bf16_f32 v154, v192, v193\n"
      "v_mfma_f32_16x16x32_bf16 v[16:19], a[104:107], v[128:131], v[16:19]\n"
      "s_add_u32 s38, s38, s38\n"
      "v_exp_f32 v194, v118\n"
      "v_mfma_f32_16x16x32_bf16 v[20:23], a[104:107], v[132:135], v[20:23]\n"
      "s_memrealtime s[68:69]\n"
      "v_exp_f32 v195, v119\n"
      "v_mfma_f32_16x16x32_bf16 a[160:163], a[104:107], v[136:139], a[160:163]\n"
      "v_exp_f32 v188, v120\n"
      "v_mfma_f32_16x16x32_bf16 a[164:167], a[104:107], v[140:143], a[164:167]\n"
      "s_mov_b32 m0, s62\n"
      "v_exp_f32 v189, v121\n"
      "v_mfma_f32_16x16x32_bf16 a[172:175], a[108:111], v[140:143], a[172:175]\n"
      "buffer_load_dwordx4 v228, s[56:59], s60 offen nt lds\n"
      "v_mfma_f32_16x16x32_bf16 a[168:171], a[108:111], v[136:139], a[168:171]\n"
      "buffer_load_dwordx4 v229, s[56:59], s60 offen offset:1024 nt lds\n"
      "v_mfma_f32_16x16x32_bf16 v[28:31], a[108:111], v[132:135], v[28:31]\n"
      "buffer_load_dwordx4 v230, s[56:59], s60 offen offset:2048 nt lds\n"
      "v_mfma_f32_16x16x32_bf16 v[24:27], a[108:111], v[128:131], v[24:27]\n"
      "buffer_load_dwordx4 v231, s[56:59], s60 offen offset:3072 nt lds\n"
      "s_cbranch_scc1 SP2_%=\n"
      "HD2_%=:\n"
      "s_cmp_lg_u32 s54, 0\n"
      "s_waitcnt vmcnt(12) lgkmcnt(0)\n"
      "s_cbranch_scc1 W2_%=\n"
      "s_waitcnt vmcnt(8)\n"
      "W2_%=:\n"
      "s_sub_u32 s67, s68, s88\n"
      "s_cmp_lt_i32 s67, -8\n"
      "s_cbranch_scc1 GW5_%=\n"
      "GX5_%=:\n"
      "s_barrier\n"
      "v_mfma_f32_16x16x32_bf16 v[32:35], a[112:115], v[128:131], v[32:35]\n"
      "s_memrealtime s[68:69]\n"
      "ds_read_b128 a[64:67], v242 offset:32768\n"
      "ds_read_b128 a[72:75], v243 offset:32768\n"
      "v_mfma_f32_16x16x32_bf16 v[36:39], a[112:115], v[132:135], v[36:39]\n"
      "s_xor_b64 s[56:57], s[56:57], s[32:33]\n"
      "ds_read_b128 a[80:83], v242 offset:33792\n"
      "ds_read_b128 a[88:91], v243 offset:33792\n"
      "v_mfma_f32_16x16x32_bf16 a[176:179], a[112:115], v[136:139], a[176:179]\n"
      "s_add_u32 s61, s61, 1\n"
      "v_exp_f32 v190, v122\n"
      "v_mfma_f32_16x16x32_bf16 a[180:183], a[112:115], v[140:143], a[180:183]\n"
      "s_add_u32 s62, s62, 0x4000\n"
      "v_cvt_pk_bf16_f32 v155, v194, v195\n"
      "v_cvt_pk_bf16_f32 v156, v188, v189\n"
      "v_mfma_f32_16x16x32_bf16 a[188:191], a[116:119], v[140:143], a[188:191]\n"
      "s_and_b32 s62, s62, 0x1ffff\n"
      "v_exp_f32 v191, v123\n"
      "v_mfma_f32_16x16x32_bf16 v[184:187], a[136:139], v[152:155], v[184:187]\n"
      "v_cvt_pk_bf16_f32 v157, v190, v191\n"
      "v_readlane_b32 s60, v219, s61\n"
      "v_mfma_f32_16x16x32_bf16 a[184:187], a[116:119], v[136:139], a[184:187]\n"
      "v_exp_f32 v192, v124\n"
      "v_mfma_f32_16x16x32_bf16 v[44:47], a[116:119], v[132:135], v[44:47]\n"
      "v_exp_f32 v193, v125\n"
      "v_mfma_f32_16x16x32_bf16 v[40:43], a[116:119], v[128:131], v[40:43]\n"
      "v_exp_f32 v194, v126\n"
      "v_mfma_f32_16x16x32_bf16 v[48:51], a[120:123], v[128:131], v[48:51]\n"
      "v_exp_f32 v195, v127\n"
      "v_mfma_f32_16x16x32_bf16 v[52:55], a[120:123], v[132:135], v[52:55]\n"
      "v_cvt_pk_bf16_f32 v158, v192, v193\n"
      "v_cvt_pk_bf16_f32 v159, v194, v195\n"
      "s_waitcnt lgkmcnt(0)\n"
      "v_mfma_f32_16x16x32_bf16 a[192:195], a[120:123], v[136:139], a[192:195]\n"
      "s_sub_u32 s67, s68, s66\n"
      "s_cmp_gt_i32 s67, s70\n"
      "ds_read_b128 a[68:71], v242 offset:36864\n"
      "v_mfma_f32_16x16x32_bf16 v[184:187], a[140:143], v[156:159], v[184:187]\n"
      "s_cselect_b32 s69, s77, s86\n"
      "ds_read_b128 a[76:79], v243 offset:36864\n"
      "ds_read_b128 a[84:87], v242 offset:37888\n"
      "v_mfma_f32_16x16x32_bf16 a[196:199], a[120:123], v[140:143], a[196:199]\n"
      "s_cselect_b32 s70, 60, s70\n"
      "ds_read_b128 a[92:95], v243 offset:37888\n"
      "v_mfma_f32_16x16x32_bf16 a[204:207], a[124:127], v[140:143], a[204:207]\n"
      "s_cselect_b32 s90, s80, s90\n"
      "v_max3_f32 v196, v184, v185, v186\n"
      "v_max_f32 v196, v196, v187\n"
      "v_mfma_f32_16x16x32_bf16 a[200:203], a[124:127], v[136:139], a[200:203]\n"
      "s_add_u32 s66, s66, s69\n"
      "v_cmp_nge_f32 vcc, 0x4b800000, v196\n"
      "v_mfma_f32_16x16x32_bf16 v[60:63], a[124:127], v[132:135], v[60:63]\n"
      "s_sub_u32 s76, s68, s83\n"
      "v_mfma_f32_16x16x32_bf16 v[56:59], a[124:127], v[128:131], v[56:59]\n"
      "s_mov_b32 s83, s82\n"
      "s_cbranch_vccnz FS1_%=\n"
      "FR1_%=:\n"
      "v_mfma_f32_16x16x32_bf16 v[96:99], a[64:67], a[0:3], v[160:163]\n"
      "s_mov_b32 s82, s68\n"
      "v_exp_f32 v188, v64\n"
      "v_mfma_f32_16x16x32_bf16 v[96:99], a[72:75], a[16:19], v[96:99]\n"
      "s_add_u32 s65, s68, s90\n"
      "v_add_f32 v180, v180, v184\n"
      "v_add_f32 v181, v181, v185\n"
      "v_mfma_f32_16x16x32_bf16 v[96:99], a[80:83], a[32:35], v[96:99]\n"
      "s_max_u32 s88, s66, s65\n"
      "v_exp_f32 v189, v65\n"
      "v_mfma_f32_16x16x32_bf16 v[96:99], a[88:91], a[48:51], v[96:99]\n"
      "s_getreg_b32 s87, hwreg(HW_REG_IB_STS, 0, 4)\n"
      "v_add_f32 v182, v182, v186\n"
      "v_add_f32 v183, v183, v187\n"
      "v_mfma_f32_16x16x32_bf16 v[104:107], a[64:67], a[4:7], v[164:167]\n"
      "s_cmp_lt_i32 s76, 104\n"
      "v_exp_f32 v190, v66\n"
      "v_mfma_f32_16x16x32_bf16 v[104:107], a[72:75], a[20:23], v[104:107]\n"
      "s_cselect_b32 s76, 15, 4\n"
      "v_exp_f32 v191, v67\n"
      "v_mfma_f32_16x16x32_bf16 v[104:107], a[80:83], a[36:39], v[104:107]\n"
      "s_cmp_gt_u32 s87, s76\n"
      "v_cvt_pk_bf16_f32 v128, v188, v189\n"
      "v_cvt_pk_bf16_f32 v129, v190, v191\n"
      "v_mfma_f32_16x16x32_bf16 v[104:107], a[88:91], a[52:55], v[104:107]\n"
      "s_addc_u32 s89, s89, 0\n"
      "v_exp_f32 v192, v68\n"
      "v_mfma_f32_16x16x32_bf16 v[112:115], a[64:67], a[8:11], v[168:171]\n"
      "s_add_u32 s39, s39, s39\n"
      "v_exp_f32 v193, v69\n"
      "v_mfma_f32_16x16x32_bf16 v[112:115], a[72:75], a[24:27], v[112:115]\n"
      "s_memrealtime s[68:69]\n"
      "v_exp_f32 v194, v70\n"
      "v_mfma_f32_16x16x32_bf16 v[112:115], a[80:83], a[40:43], v[112:115]\n"
      "v_exp_f32 v195, v71\n"
      "v_mfma_f32_16x16x32_bf16 v[112:115], a[88:91], a[56:59], v[112:115]\n"
      "s_mov_b32 m0, s62\n"
      "v_cvt_pk_bf16_f32 v130, v192, v193\n"
      "v_cvt_pk_bf16_f32 v131, v194, v195\n"
      "v_mfma_f32_16x16x32_bf16 v[120:123], a[64:67], a[12:15], v[172:175]\n"
      "buffer_load_dwordx4 v228, s[56:59], s60 offen nt lds\n"
      "v_mfma_f32_16x16x32_bf16 v[184:187], a[128:131], v[128:131], 0\n"
      "buffer_load_dwordx4 v229, s[56:59], s60 offen offset:1024 nt lds\n"
      "v_mfma_f32_16x16x32_bf16 v[120:123], a[72:75], a[28:31], v[120:123]\n"
      "buffer_load_dwordx4 v230, s[56:59], s60 offen offset:2048 nt lds\n"
      "v_mfma_f32_16x16x32_bf16 v[120:123], a[80:83], a[44:47], v[120:123]\n"
      "buffer_load_dwordx4 v231, s[56:59], s60 offen offset:3072 nt lds\n"
      "s_cbranch_scc1 SP3_%=\n"
      "HD3_%=:\n"
      "s_cmp_lg_u32 s54, 0\n"
      "s_waitcnt vmcnt(12) lgkmcnt(0)\n"
      "s_cbranch_scc1 W3_%=\n"
      "s_waitcnt vmcnt(8)\n"
      "W3_%=:\n"
      "s_sub_u32 s67, s68, s88\n"
      "s_cmp_lt_i32 s67, -8\n"
      "s_cbranch_scc1 GW6_%=\n"
      "GX6_%=:\n"
      "s_barrier\n"
      "v_mfma_f32_16x16x32_bf16 v[120:123], a[88:91], a[60:63], v[120:123]\n"
      "s_memrealtime s[68:69]\n"
      "ds_read_b64_tr_b16 a[96:97], v244 offset:32768\n"
      "ds_read_b64_tr_b16 a[98:99], v244 offset:36864\n"
      "v_mfma_f32_16x16x32_bf16 v[100:103], a[68:71], a[0:3], v[160:163]\n"
      "s_xor_b64 s[56:57], s[56:57], s[32:33]\n"
      "ds_read_b64_tr_b16 a[100:101], v245 offset:32768\n"
      "ds_read_b64_tr_b16 a[102:103], v245 offset:36864\n"
      "v_mfma_f32_16x16x32_bf16 v[100:103], a[76:79], a[16:19], v[100:103]\n"
      "s_add_u32 s61, s61, 1\n"
      "ds_read_b64_tr_b16 a[104:105], v246 offset:32768\n"
      "ds_read_b64_tr_b16 a[106:107], v246 offset:36864\n"
      "v_mfma_f32_16x16x32_bf16 v[100:103], a[84:87], a[32:35], v[100:103]\n"
      "s_add_u32 s62, s62, 0x4000\n"
      "ds_read_b64_tr_b16 a[108:109], v247 offset:32768\n"
      "ds_read_b64_tr_b16 a[110:111], v247 offset:36864\n"
      "v_mfma_f32_16x16x32_bf16 v[100:103], a[92:95], a[48:51], v[100:103]\n"
      "s_and_b32 s62, s62, 0x1ffff\n"
      "v_exp_f32 v188, v72\n"
      "v_mfma_f32_16x16x32_bf16 v[108:111], a[68:71], a[4:7], v[164:167]\n"
      "v_readlane_b32 s60, v219, s61\n"
      "v_exp_f32 v189, v73\n"
      "v_mfma_f32_16x16x32_bf16 v[108:111], a[76:79], a[20:23], v[108:111]\n"
      "v_xor_b32 v242, 0x10000, v242\n"
      "v_xor_b32 v243, 0x10000, v243\n"
      "v_mfma_f32_16x16x32_bf16 v[108:111], a[84:87], a[36:39], v[108:111]\n"
      "v_exp_f32 v190, v74\n"
      "v_mfma_f32_16x16x32_bf16 v[108:111], a[92:95], a[52:55], v[108:111]\n"
      "v_exp_f32 v191, v75\n"
      "v_mfma_f32_16x16x32_bf16 v[116:119], a[68:71], a[8:11], v[168:171]\n"
      "v_cvt_pk_bf16_f32 v132, v188, v189\n"
      "v_cvt_pk_bf16_f32 v133, v190, v191\n"
      "v_mfma_f32_16x16x32_bf16 v[116:119], a[76:79], a[24:27], v[116:119]\n"
      "v_exp_f32 v192, v76\n"
      "s_waitcnt lgkmcnt(0)\n"
      "v_mfma_f32_16x16x32_bf16 v[116:119], a[84:87], a[40:43], v[116:119]\n"
      "s_sub_u32 s67, s68, s66\n"
      "s_cmp_gt_i32 s67, s70\n"
      "ds_read_b64_tr_b16 a[112:113], v244 offset:33792\n"
      "v_mfma_f32_16x16x32_bf16 v[116:119], a[92:95], a[56:59], v[116:119]\n"
      "s_cselect_b32 s69, s77, s86\n"
      "v_exp_f32 v193, v77\n"
      "v_mfma_f32_16x16x32_bf16 v[124:127], a[68:71], a[12:15], v[172:175]\n"
      "s_cselect_b32 s70, 60, s70\n"
      "ds_read_b64_tr_b16 a[114:115], v244 offset:37888\n"
      "v_cvt_pk_bf16_f32 v134, v192, v193\n"
      "v_mfma_f32_16x16x32_bf16 v[124:127], a[76:79], a[28:31], v[124:127]\n"
      "s_cselect_b32 s90, s80, s90\n"
      "v_exp_f32 v194, v78\n"
      "v_mfma_f32_16x16x32_bf16 v[124:127], a[84:87], a[44:47], v[124:127]\n"
      "s_add_u32 s66, s66, s69\n"
      "ds_read_b64_tr_b16 a[116:117], v245 offset:33792\n"
      "ds_read_b64_tr_b16 a[118:119], v245 offset:37888\n"
      "v_mfma_f32_16x16x32_bf16 v[124:127], a[92:95], a[60:63], v[124:127]\n"
      "s_sub_u32 s76, s68, s83\n"
      "v_exp_f32 v195, v79\n"
      "v_mfma_f32_16x16x32_bf16 v[0:3], a[96:99], v[144:147], v[0:3]\n"
      "s_mov_b32 s83, s82\n"
      "ds_read_b64_tr_b16 a[120:121], v246 offset:33792\n"
      "v_cvt_pk_bf16_f32 v135, v194, v195\n"
      "v_mfma_f32_16x16x32_bf16 v[4:7], a[96:99], v[148:151], v[4:7]\n"
      "s_mov_b32 s82, s68\n"
      "v_exp_f32 v188, v80\n"
      "v_mfma_f32_16x16x32_bf16 v[184:187], a[132:135], v[132:135], v[184:187]\n"
      "s_add_u32 s65, s68, s90\n"
      "ds_read_b64_tr_b16 a[122:123], v246 offset:37888\n"
      "ds_read_b64_tr_b16 a[124:125], v247 offset:33792\n"
      "v_mfma_f32_16x16x32_bf16 a[144:147], a[96:99], v[152:155], a[144:147]\n"
      "s_max_u32 s88, s66, s65\n"
      "v_exp_f32 v189, v81\n"
      "v_mfma_f32_16x16x32_bf16 a[148:151], a[96:99], v[156:159], a[148:151]\n"
      "s_getreg_b32 s87, hwreg(HW_REG_IB_STS, 0, 4)\n"
      "ds_read_b64_tr_b16 a[126:127], v247 offset:37888\n"
      "v_cvt_pk_bf16_f32 v136, v188, v189\n"
      "v_mfma_f32_16x16x32_bf16 a[156:159], a[100:103], v[156:159], a[156:159]\n"
      "s_cmp_lt_i32 s76, 104\n"
      "v_exp_f32 v190, v82\n"
      "v_mfma_f32_16x16x32_bf16 a[152:155], a[100:103], v[152:155], a[152:155]\n"
      "s_cselect_b32 s76, 15, 4\n"
      "v_exp_f32 v191, v83\n"
      "v_mfma_f32_16x16x32_bf16 v[12:15], a[100:103], v[148:151], v[12:15]\n"
      "s_cmp_gt_u32 s87, s76\n"
      "v_exp_f32 v192, v84\n"
      "v_mfma_f32_16x16x32_bf16 v[8:11], a[100:103], v[144:147], v[8:11]\n"
      "s_addc_u32 s89, s89, 0\n"
      "v_exp_f32 v193, v85\n"
      "v_mfma_f32_16x16x32_bf16 v[16:19], a[104:107], v[144:147], v[16:19]\n"
      "s_add_u32 s38, s38, s38\n"
      "v_cvt_pk_bf16_f32 v137, v190, v191\n"
      "v_cvt_pk_bf16_f32 v138, v192, v193\n"
      "v_mfma_f32_16x16x32_bf16 v[20:23], a[104:107], v[148:151], v[20:23]\n"
      "s_memrealtime s[68:69]\n"
      "v_exp_f32 v194, v86\n"
      "v_mfma_f32_16x16x32_bf16 a[160:163], a[104:107], v[152:155], a[160:163]\n"
      "v_exp_f32 v195, v87\n"
      "v_mfma_f32_16x16x32_bf16 a[164:167], a[104:107], v[156:159], a[164:167]\n"
      "s_mov_b32 m0, s62\n"
      "v_exp_f32 v188, v88\n"
      "v_mfma_f32_16x16x32_bf16 a[172:175], a[108:111], v[156:159], a[172:175]\n"
      "buffer_load_dwordx4 v228, s[56:59], s60 offen nt lds\n"
      "v_mfma_f32_16x16x32_bf16 a[168:171], a[108:111], v[152:155], a[168:171]\n"
      "buffer_load_dwordx4 v229, s[56:59], s60 offen offset:1024 nt lds\n"
      "v_mfma_f32_16x16x32_bf16 v[28:31], a[108:111], v[148:151], v[28:31]\n"
      "buffer_load_dwordx4 v230, s[56:59], s60 offen offset:2048 nt lds\n"
      "v_mfma_f32_16x16x32_bf16 v[24:27], a[108:111], v[144:147], v[24:27]\n"
      "buffer_load_dwordx4 v231, s[56:59], s60 offen offset:3072 nt lds\n"
      "HD0_%=:\n"
      "s_cmp_lg_u32 s54, 0\n"
      "s_waitcnt vmcnt(12) lgkmcnt(0)\n"
      "s_cbranch_scc1 W4_%=\n"
      "s_waitcnt vmcnt(8)\n"
      "W4_%=:\n"
      "s_sub_u32 s67, s68, s88\n"
      "s_cmp_lt_i32 s67, -8\n"
      "s_cbranch_scc1 GW7_%=\n"
      "GX7_%=:\n"
      "s_barrier\n"
      "v_mfma_f32_16x16x32_bf16 v[32:35], a[112:115], v[144:147], v[32:35]\n"
      "s_memrealtime s[68:69]\n"
      "ds_read_b128 a[64:67], v242 offset:0\n"
      "ds_read_b128 a[72:75], v243 offset:0\n"
      "v_mfma_f32_16x16x32_bf16 v[36:39], a[112:115], v[148:151], v[36:39]\n"
      "s_xor_b64 s[56:57], s[56:57], s[32:33]\n"
      "ds_read_b128 a[80:83], v242 offset:1024\n"
      "ds_read_b128 a[88:91], v243 offset:1024\n"
      "v_mfma_f32_16x16x32_bf16 a[176:179], a[112:115], v[152:155], a[176:179]\n"
      "s_add_u32 s61, s61, 1\n"
      "v_exp_f32 v189, v89\n"
      "v_mfma_f32_16x16x32_bf16 a[180:183], a[112:115], v[156:159], a[180:183]\n"
      "s_add_u32 s62, s62, 0x4000\n"
      "v_cvt_pk_bf16_f32 v139, v194, v195\n"
      "v_cvt_pk_bf16_f32 v140, v188, v189\n"
      "v_mfma_f32_16x16x32_bf16 a[188:191], a[116:119], v[156:159], a[188:191]\n"
      "s_and_b32 s62, s62, 0x1ffff\n"
      "v_exp_f32 v190, v90\n"
      "v_mfma_f32_16x16x32_bf16 v[184:187], a[136:139], v[136:139], v[184:187]\n"
      "v_readlane_b32 s60, v219, s61\n"
      "v_exp_f32 v191, v91\n"
      "v_mfma_f32_16x16x32_bf16 a[184:187], a[116:119], v[152:155], a[184:187]\n"
      "v_exp_f32 v192, v92\n"
      "v_mfma_f32_16x16x32_bf16 v[44:47], a[116:119], v[148:151], v[44:47]\n"
      "v_exp_f32 v193, v93\n"
      "v_mfma_f32_16x16x32_bf16 v[40:43], a[116:119], v[144:147], v[40:43]\n"
      "v_cvt_pk_bf16_f32 v141, v190, v191\n"
      "v_cvt_pk_bf16_f32 v142, v192, v193\n"
      "v_mfma_f32_16x16x32_bf16 v[48:51], a[120:123], v[144:147], v[48:51]\n"
      "v_exp_f32 v194, v94\n"
      "v_mfma_f32_16x16x32_bf16 v[52:55], a[120:123], v[148:151], v[52:55]\n"
      "v_exp_f32 v195, v95\n"
      "s_waitcnt lgkmcnt(0)\n"
      "v_mfma_f32_16x16x32_bf16 a[192:195], a[120:123], v[152:155], a[192:195]\n"
      "s_sub_u32 s67, s68, s66\n"
      "s_cmp_gt_i32 s67, s70\n"
      "v_cvt_pk_bf16_f32 v143, v194, v195\n"
      "v_mfma_f32_16x16x32_bf16 a[196:199], a[120:123], v[156:159], a[196:199]\n"
      "s_cselect_b32 s69, s77, s86\n"
      "ds_read_b128 a[68:71], v242 offset:4096\n"
      "ds_read_b128 a[76:79], v243 offset:4096\n"
      "v_mfma_f32_16x16x32_bf16 v[184:187], a[140:143], v[140:143], v[184:187]\n"
      "s_cselect_b32 s70, 60, s70\n"
      "ds_read_b128 a[84:87], v242 offset:5120\n"
      "ds_read_b128 a[92:95], v243 offset:5120\n"
      "v_mfma_f32_16x16x32_bf16 a[204:207], a[124:127], v[156:159], a[204:207]\n"
      "s_cselect_b32 s90, s80, s90\n"
      "v_xor_b32 v244, 0x10000, v244\n"
      "v_xor_b32 v245, 0x10000, v245\n"
      "v_mfma_f32_16x16x32_bf16 a[200:203], a[124:127], v[152:155], a[200:203]\n"
      "s_add_u32 s66, s66, s69\n"
      "v_xor_b32 v246, 0x10000, v246\n"
      "v_xor_b32 v247, 0x10000, v247\n"
      "v_mfma_f32_16x16x32_bf16 v[60:63], a[124:127], v[148:151], v[60:63]\n"
      "s_sub_u32 s76, s68, s83\n"
      "v_max3_f32 v196, v184, v185, v186\n"
      "v_max_f32 v196, v196, v187\n"
      "v_mfma_f32_16x16x32_bf16 v[56:59], a[124:127], v[144:147], v[56:59]\n"
      "s_mov_b32 s83, s82\n"
      "v_cmp_nge_f32 vcc, 0x4b800000, v196\n"
      "s_cbranch_vccnz FS2_%=\n"
      "FR2_%=:\n"
      "s_branch LOOP_%=\n"
      "SP1_%=:\n"
      "s_ff1_i32_b32 s64, s39\n"
      "s_lshl1_add_u32 s64, s64, 1\n"
      "s_bitcmp1_b64 s[52:53], s64\n"
      "s_cbranch_scc0 SH1_%=\n"
      "s_swappc_b64 s[84:85], s[84:85]\n"
      "SH1_%=:\n"
      "s_cmp_lg_u32 s64, 7\n"
      "s_cbranch_scc1 SK_%=\n"
      "v_accvgpr_write_b32 a208, v128\n"
      "v_accvgpr_write_b32 a209, v129\n"
      "v_accvgpr_write_b32 a210, v130\n"
      "v_accvgpr_write_b32 a211, v131\n"
      "v_accvgpr_write_b32 a212, v132\n"
      "v_accvgpr_write_b32 a213, v133\n"
      "v_accvgpr_write_b32 a214, v134\n"
      "v_accvgpr_write_b32 a215, v135\n"
      "v_accvgpr_write_b32 a216, v136\n"
      "v_accvgpr_write_b32 a217, v137\n"
      "v_accvgpr_write_b32 a218, v138\n"
      "v_accvgpr_write_b32 a219, v139\n"
      "v_accvgpr_write_b32 a220, v140\n"
      "v_accvgpr_write_b32 a221, v141\n"
      "v_accvgpr_write_b32 a222, v142\n"
      "v_accvgpr_write_b32 a223, v143\n"
      "SK_%=:\n"
      "s_cmp_lt_u32 s64, 63\n"
      "s_cbranch_scc1 HD1_%=\n"
      "s_xor_b64 s[56:57], s[56:57], s[32:33]\n"
      "s_add_u32 s61, s61, 1\n"
      "s_add_u32 s62, s62, 0x4000\n"
      "s_and_b32 s62, s62, 0x1ffff\n"
      "v_readlane_b32 s74, v219, 60\n"
      "v_readlane_b32 s76, v219, 62\n"
      "s_mov_b32 s61, 0\n"
      "s_add_u32 s74, s74, 0x400\n"
      "v_add_u32 v228, s74, v229\n"
      "v_add_u32 v230, s74, v231\n"
      "v_add_u32 v229, s76, v229\n"
      "v_add_u32 v231, s76, v231\n"
      "s_branch HD1_%=\n"
      "SP2_%=:\n"
      "s_cmp_eq_u32 s38, 0\n"
      "s_cbranch_scc1 L62_%=\n"
      "s_ff1_i32_b32 s64, s38\n"
      "s_lshl1_add_u32 s64, s64, 0\n"
      "s_bitcmp1_b64 s[52:53], s64\n"
      "s_cbranch_scc0 SH2_%=\n"
      "s_swappc_b64 s[84:85], s[84:85]\n"
      "SH2_%=:\n"
      "s_branch HD2_%=\n"
      "L62_%=:\n"
      "s_xor_b64 s[56:57], s[56:57], s[32:33]\n"
      "s_mov_b32 s61, 0\n"
      "v_xor_b32 v228, 0x80, v228\n"
      "v_xor_b32 v229, 0x80, v229\n"
      "v_xor_b32 v230, 0x80, v230\n"
      "v_xor_b32 v231, 0x80, v231\n"
      "s_mov_b32 s38, 0x80000000\n"
      "s_branch HD2_%=\n"
      "SP3_%=:\n"
      "s_cmp_eq_u32 s39, 0\n"
      "s_cbranch_scc1 NH_%=\n"
      "s_ff1_i32_b32 s64, s39\n"
      "s_lshl1_add_u32 s64, s64, 1\n"
      "s_bitcmp1_b64 s[52:53], s64\n"
      "s_cbranch_scc0 SH3_%=\n"
      "s_swappc_b64 s[84:85], s[84:85]\n"
      "SH3_%=:\n"
      "s_bitcmp1_b64 s[34:35], s64\n"
      "s_cbranch_scc0 HD3_%=\n"
      "NH_%=:\n"
      "s_cmp_lg_u32 s54, 0\n"
      "s_waitcnt vmcnt(12) lgkmcnt(0)\n"
      "s_cbranch_scc1 W5_%=\n"
      "s_waitcnt vmcnt(8)\n"
      "W5_%=:\n"
      "s_barrier\n"
      "v_mfma_f32_16x16x32_bf16 v[120:123], a[88:91], a[60:63], v[120:123]\n"
      "s_sub_u32 s54, s54, 1\n"
      "ds_read_b64_tr_b16 a[96:97], v244 offset:32768\n"
      "ds_read_b64_tr_b16 a[98:99], v244 offset:36864\n"
      "v_mfma_f32_16x16x32_bf16 v[100:103], a[68:71], a[0:3], v[160:163]\n"
      "ds_read_b64_tr_b16 a[100:101], v245 offset:32768\n"
      "ds_read_b64_tr_b16 a[102:103], v245 offset:36864\n"
      "v_mfma_f32_16x16x32_bf16 v[100:103], a[76:79], a[16:19], v[100:103]\n"
      "ds_read_b64_tr_b16 a[104:105], v246 offset:32768\n"
      "ds_read_b64_tr_b16 a[106:107], v246 offset:36864\n"
      "v_mfma_f32_16x16x32_bf16 v[100:103], a[84:87], a[32:35], v[100:103]\n"
      "ds_read_b64_tr_b16 a[108:109], v247 offset:32768\n"
      "ds_read_b64_tr_b16 a[110:111], v247 offset:36864\n"
      "v_mfma_f32_16x16x32_bf16 v[100:103], a[92:95], a[48:51], v[100:103]\n"
      "v_exp_f32 v188, v72\n"
      "v_mfma_f32_16x16x32_bf16 v[108:111], a[68:71], a[4:7], v[164:167]\n"
      "v_exp_f32 v189, v73\n"
      "v_mfma_f32_16x16x32_bf16 v[108:111], a[76:79], a[20:23], v[108:111]\n"
      "v_xor_b32 v242, 0x10000, v242\n"
      "v_xor_b32 v243, 0x10000, v243\n"
      "v_mfma_f32_16x16x32_bf16 v[108:111], a[84:87], a[36:39], v[108:111]\n"
      "v_exp_f32 v190, v74\n"
      "v_mfma_f32_16x16x32_bf16 v[108:111], a[92:95], a[52:55], v[108:111]\n"
      "v_exp_f32 v191, v75\n"
      "v_mfma_f32_16x16x32_bf16 v[116:119], a[68:71], a[8:11], v[168:171]\n"
      "v_cvt_pk_bf16_f32 v132, v188, v189\n"
      "v_cvt_pk_bf16_f32 v133, v190, v191\n"
      "v_mfma_f32_16x16x32_bf16 v[116:119], a[76:79], a[24:27], v[116:119]\n"
      "v_exp_f32 v192, v76\n"
      "s_waitcnt lgkmcnt(0)\n"
      "v_mfma_f32_16x16x32_bf16 v[116:119], a[84:87], a[40:43], v[116:119]\n"
      "ds_read_b64_tr_b16 a[112:113], v244 offset:33792\n"
      "v_mfma_f32_16x16x32_bf16 v[116:119], a[92:95], a[56:59], v[116:119]\n"
      "v_exp_f32 v193, v77\n"
      "v_mfma_f32_16x16x32_bf16 v[124:127], a[68:71], a[12:15], v[172:175]\n"
      "ds_read_b64_tr_b16 a[114:115], v244 offset:37888\n"
      "v_cvt_pk_bf16_f32 v134, v192, v193\n"
      "v_mfma_f32_16x16x32_bf16 v[124:127], a[76:79], a[28:31], v[124:127]\n"
      "v_exp_f32 v194, v78\n"
      "v_mfma_f32_16x16x32_bf16 v[124:127], a[84:87], a[44:47], v[124:127]\n"
      "ds_read_b64_tr_b16 a[116:117], v245 offset:33792\n"
      "ds_read_b64_tr_b16 a[118:119], v245 offset:37888\n"
      "v_mfma_f32_16x16x32_bf16 v[124:127], a[92:95], a[60:63], v[124:127]\n"
      "v_exp_f32 v195, v79\n"
      "v_mfma_f32_16x16x32_bf16 v[0:3], a[96:99], v[144:147], v[0:3]\n"
      "ds_read_b64_tr_b16 a[120:121], v246 offset:33792\n"
      "v_cvt_pk_bf16_f32 v135, v194, v195\n"
      "v_mfma_f32_16x16x32_bf16 v[4:7], a[96:99], v[148:151], v[4:7]\n"
      "v_exp_f32 v188, v80\n"
      "v_mfma_f32_16x16x32_bf16 v[184:187], a[132:135], v[132:135], v[184:187]\n"
      "ds_read_b64_tr_b16 a[122:123], v246 offset:37888\n"
      "ds_read_b64_tr_b16 a[124:125], v247 offset:33792\n"
      "v_mfma_f32_16x16x32_bf16 a[144:147], a[96:99], v[152:155], a[144:147]\n"
      "v_exp_f32 v189, v81\n"
      "v_mfma_f32_16x16x32_bf16 a[148:151], a[96:99], v[156:159], a[148:151]\n"
      "ds_read_b64_tr_b16 a[126:127], v247 offset:37888\n"
      "v_cvt_pk_bf16_f32 v136, v188, v189\n"
      "v_mfma_f32_16x16x32_bf16 a[156:159], a[100:103], v[156:159], a[156:159]\n"
      "v_exp_f32 v190, v82\n"
      "v_mfma_f32_16x16x32_bf16 a[152:155], a[100:103], v[152:155], a[152:155]\n"
      "v_exp_f32 v191, v83\n"
      "v_mfma_f32_16x16x32_bf16 v[12:15], a[100:103], v[148:151], v[12:15]\n"
      "v_exp_f32 v192, v84\n"
      "v_mfma_f32_16x16x32_bf16 v[8:11], a[100:103], v[144:147], v[8:11]\n"
      "v_exp_f32 v193, v85\n"
      "v_mfma_f32_16x16x32_bf16 v[16:19], a[104:107], v[144:147], v[16:19]\n"
      "s_add_u32 s38, s38, s38\n"
      "v_cvt_pk_bf16_f32 v137, v190, v191\n"
      "v_cvt_pk_bf16_f32 v138, v192, v193\n"
      "v_mfma_f32_16x16x32_bf16 v[20:23], a[104:107], v[148:151], v[20:23]\n"
      "s_memrealtime s[68:69]\n"
      "v_exp_f32 v194, v86\n"
      "v_mfma_f32_16x16x32_bf16 a[160:163], a[104:107], v[152:155], a[160:163]\n"
      "v_exp_f32 v195, v87\n"
      "v_mfma_f32_16x16x32_bf16 a[164:167], a[104:107], v[156:159], a[164:167]\n"
      "v_exp_f32 v188, v88\n"
      "v_mfma_f32_16x16x32_bf16 a[172:175], a[108:111], v[156:159], a[172:175]\n"
      "s_nop 0\n"
      "v_mfma_f32_16x16x32_bf16 a[168:171], a[108:111], v[152:155], a[168:171]\n"
      "s_nop 0\n"
      "v_mfma_f32_16x16x32_bf16 v[28:31], a[108:111], v[148:151], v[28:31]\n"
      "s_nop 0\n"
      "v_mfma_f32_16x16x32_bf16 v[24:27], a[108:111], v[144:147], v[24:27]\n"
      "s_nop 0\n"
      "s_cbranch_scc0 MH_%=\n"
      "END_%=:\n"
      "s_add_u32 m0, s62, 0x4000\n"
      "s_waitcnt lgkmcnt(0)\n"
      "buffer_load_dwordx4 v248, s[56:59], 0 offen nt lds\n"
      "buffer_load_dwordx4 v249, s[56:59], 0 offen offset:2048 nt lds\n"
      "v_mfma_f32_16x16x32_bf16 v[32:35], a[112:115], v[144:147], v[32:35]\n"
      "v_cvt_pk_bf16_f32 v139, v194, v195\n"
      "v_mov_b32 v236, s68\n"
      "v_mfma_f32_16x16x32_bf16 v[36:39], a[112:115], v[148:151], v[36:39]\n"
      "s_mov_b64 exec, 1\n"
      "global_store_dwordx2 v238, v[236:237], s[46:47] offset:8 sc1\n"
      "s_mov_b64 exec, -1\n"
      "v_mfma_f32_16x16x32_bf16 v[184:187], a[136:139], v[136:139], v[184:187]\n"
      "s_mov_b32 s82, s68\n"
      "v_exp_f32 v189, v89\n"
      "v_mfma_f32_16x16x32_bf16 a[176:179], a[112:115], v[152:155], a[176:179]\n"
      "v_exp_f32 v190, v90\n"
      "v_mfma_f32_16x16x32_bf16 a[180:183], a[112:115], v[156:159], a[180:183]\n"
      "v_exp_f32 v191, v91\n"
      "v_mfma_f32_16x16x32_bf16 a[188:191], a[116:119], v[156:159], a[188:191]\n"
      "v_cvt_pk_bf16_f32 v140, v188, v189\n"
      "v_cvt_pk_bf16_f32 v141, v190, v191\n"
      "s_waitcnt vmcnt(7)\n"
      "s_barrier\n"
      "v_mfma_f32_16x16x32_bf16 a[184:187], a[116:119], v[152:155], a[184:187]\n"
      "ds_read_b64_tr_b16 a[80:81], v224 offset:0\n"
      "ds_read_b64_tr_b16 a[82:83], v224 offset:4096\n"
      "v_mfma_f32_16x16x32_bf16 v[44:47], a[116:119], v[148:151], v[44:47]\n"
      "ds_read_b64_tr_b16 a[84:85], v225 offset:0\n"
      "ds_read_b64_tr_b16 a[86:87], v225 offset:4096\n"
      "v_mfma_f32_16x16x32_bf16 v[40:43], a[116:119], v[144:147], v[40:43]\n"
      "s_mov_b32 s74, 0xffff0000\n"
      "ds_read_b64_tr_b16 a[88:89], v226 offset:0\n"
      "ds_read_b64_tr_b16 a[90:91], v226 offset:4096\n"
      "v_mfma_f32_16x16x32_bf16 v[48:51], a[120:123], v[144:147], v[48:51]\n"
      "s_mov_b32 s75, 0xffff0000\n"
      "ds_read_b64_tr_b16 a[92:93], v227 offset:0\n"
      "ds_read_b64_tr_b16 a[94:95], v227 offset:4096\n"
      "v_mfma_f32_16x16x32_bf16 v[52:55], a[120:123], v[148:151], v[52:55]\n"
      "s_mov_b32 s94, 0\n"
      "v_exp_f32 v192, v92\n"
      "v_mfma_f32_16x16x32_bf16 a[192:195], a[120:123], v[152:155], a[192:195]\n"
      "s_mov_b32 s95, -1\n"
      "ds_read_b64_tr_b16 a[16:17], v224 offset:1024\n"
      "ds_read_b64_tr_b16 a[18:19], v224 offset:5120\n"
      "v_mfma_f32_16x16x32_bf16 a[196:199], a[120:123], v[156:159], a[196:199]\n"
      "v_exp_f32 v193, v93\n"
      "v_mfma_f32_16x16x32_bf16 a[204:207], a[124:127], v[156:159], a[204:207]\n"
      "ds_read_b64_tr_b16 a[20:21], v225 offset:1024\n"
      "v_cvt_pk_bf16_f32 v142, v192, v193\n"
      "v_mfma_f32_16x16x32_bf16 a[200:203], a[124:127], v[152:155], a[200:203]\n"
      "v_exp_f32 v194, v94\n"
      "v_mfma_f32_16x16x32_bf16 v[60:63], a[124:127], v[148:151], v[60:63]\n"
      "ds_read_b64_tr_b16 a[22:23], v225 offset:5120\n"
      "ds_read_b64_tr_b16 a[24:25], v226 offset:1024\n"
      "v_mfma_f32_16x16x32_bf16 v[56:59], a[124:127], v[144:147], v[56:59]\n"
      "v_exp_f32 v195, v95\n"
      "ds_read_b64_tr_b16 a[26:27], v226 offset:5120\n"
      "v_cvt_pk_bf16_f32 v143, v194, v195\n"
      "s_nop 1\n"
      "v_mfma_f32_16x16x32_bf16 v[184:187], a[140:143], v[140:143], v[184:187]\n"
      "ds_read_b64_tr_b16 a[28:29], v227 offset:1024\n"
      "ds_read_b64_tr_b16 a[30:31], v227 offset:5120\n"
      "ds_read_b64_tr_b16 a[0:1], v220 offset:1024\n"
      "ds_read_b64_tr_b16 a[2:3], v220 offset:5120\n"
      "ds_read_b64_tr_b16 a[4:5], v221 offset:1024\n"
      "ds_read_b64_tr_b16 a[6:7], v221 offset:5120\n"
      "ds_read_b64_tr_b16 a[8:9], v222 offset:1024\n"
      "ds_read_b64_tr_b16 a[10:11], v222 offset:5120\n"
      "ds_read_b64_tr_b16 a[12:13], v223 offset:1024\n"
      "ds_read_b64_tr_b16 a[14:15], v223 offset:5120\n"
      "v_max3_f32 v196, v184, v185, v186\n"
      "v_max_f32 v196, v196, v187\n"
      "v_cmp_nge_f32 vcc, 0x4b800000, v196\n"
      "s_cbranch_vccnz FS3_%=\n"
      "FR3_%=:\n"
      "s_waitcnt lgkmcnt(15)\n"
      "v_mfma_f32_16x16x32_bf16 a[176:179], a[80:83], v[136:139], a[176:179]\n"
      "v_add_f32 v180, v180, v184\n"
      "v_add_f32 v181, v181, v185\n"
      "v_mfma_f32_16x16x32_bf16 a[180:183], a[80:83], v[140:143], a[180:183]\n"
      "v_add_f32 v182, v182, v186\n"
      "v_add_f32 v183, v183, v187\n"
      "v_mfma_f32_16x16x32_bf16 a[184:187], a[84:87], v[136:139], a[184:187]\n"
      "v_exp_f32 v188, v112\n"
      "v_mfma_f32_16x16x32_bf16 a[188:191], a[84:87], v[140:143], a[188:191]\n"
      "ds_write_b128 v232, a[176:179] offset:0\n"
      "v_exp_f32 v189, v113\n"
      "v_mfma_f32_16x16x32_bf16 a[192:195], a[88:91], v[136:139], a[192:195]\n"
      "v_exp_f32 v190, v114\n"
      "v_mfma_f32_16x16x32_bf16 a[196:199], a[88:91], v[140:143], a[196:199]\n"
      "v_exp_f32 v191, v115\n"
      "v_mfma_f32_16x16x32_bf16 a[200:203], a[92:95], v[136:139], a[200:203]\n"
      "ds_write_b128 v232, a[180:183] offset:1024\n"
      "v_exp_f32 v192, v116\n"
      "v_mfma_f32_16x16x32_bf16 a[204:207], a[92:95], v[140:143], a[204:207]\n"
      "v_exp_f32 v193, v117\n"
      "v_exp_f32 v194, v118\n"
      "v_cvt_pk_bf16_f32 v152, v188, v189\n"
      "v_exp_f32 v195, v119\n"
      "v_cvt_pk_bf16_f32 v153, v190, v191\n"
      "ds_write_b128 v232, a[184:187] offset:2048\n"
      "v_exp_f32 v188, v120\n"
      "v_cvt_pk_bf16_f32 v154, v192, v193\n"
      "v_mfma_f32_16x16x32_bf16 v[32:35], a[80:83], v[128:131], v[32:35]\n"
      "v_exp_f32 v189, v121\n"
      "v_cvt_pk_bf16_f32 v155, v194, v195\n"
      "v_exp_f32 v190, v122\n"
      "v_cvt_pk_bf16_f32 v156, v188, v189\n"
      "v_mfma_f32_16x16x32_bf16 v[184:187], a[136:139], v[152:155], 0\n"
      "ds_write_b128 v234, v[152:155] offset:0\n"
      "v_exp_f32 v191, v123\n"
      "v_mfma_f32_16x16x32_bf16 v[36:39], a[80:83], v[132:135], v[36:39]\n"
      "v_exp_f32 v192, v124\n"
      "v_cvt_pk_bf16_f32 v157, v190, v191\n"
      "v_exp_f32 v193, v125\n"
      "v_exp_f32 v194, v126\n"
      "v_cvt_pk_bf16_f32 v158, v192, v193\n"
      "ds_write_b128 v232, a[188:191] offset:3072\n"
      "v_mfma_f32_16x16x32_bf16 v[40:43], a[84:87], v[128:131], v[40:43]\n"
      "v_exp_f32 v195, v127\n"
      "v_exp_f32 v188, v96\n"
      "v_cvt_pk_bf16_f32 v159, v194, v195\n"
      "v_exp_f32 v189, v97\n"
      "v_mfma_f32_16x16x32_bf16 v[44:47], a[84:87], v[132:135], v[44:47]\n"
      "v_exp_f32 v190, v98\n"
      "ds_write_b128 v234, v[156:159] offset:1024\n"
      "v_cvt_pk_bf16_f32 v144, v188, v189\n"
      "v_mfma_f32_16x16x32_bf16 v[184:187], a[140:143], v[156:159], v[184:187]\n"
      "v_exp_f32 v191, v99\n"
      "v_exp_f32 v192, v100\n"
      "v_cvt_pk_bf16_f32 v145, v190, v191\n"
      "v_mfma_f32_16x16x32_bf16 v[48:51], a[88:91], v[128:131], v[48:51]\n"
      "v_exp_f32 v193, v101\n"
      "ds_write_b128 v232, a[192:195] offset:4096\n"
      "v_exp_f32 v194, v102\n"
      "v_cvt_pk_bf16_f32 v146, v192, v193\n"
      "v_exp_f32 v195, v103\n"
      "v_mfma_f32_16x16x32_bf16 v[52:55], a[88:91], v[132:135], v[52:55]\n"
      "v_exp_f32 v188, v104\n"
      "v_cvt_pk_bf16_f32 v147, v194, v195\n"
      "v_exp_f32 v189, v105\n"
      "ds_write_b128 v232, a[196:199] offset:5120\n"
      "v_exp_f32 v190, v106\n"
      "v_cvt_pk_bf16_f32 v148, v188, v189\n"
      "v_mfma_f32_16x16x32_bf16 v[184:187], a[128:131], v[144:147], v[184:187]\n"
      "v_exp_f32 v191, v107\n"
      "v_exp_f32 v192, v108\n"
      "v_cvt_pk_bf16_f32 v149, v190, v191\n"
      "v_exp_f32 v193, v109\n"
      "ds_write_b128 v232, a[200:203] offset:6144\n"
      "v_mfma_f32_16x16x32_bf16 v[56:59], a[92:95], v[128:131], v[56:59]\n"
      "v_exp_f32 v194, v110\n"
      "v_cvt_pk_bf16_f32 v150, v192, v193\n"
      "v_exp_f32 v195, v111\n"
      "v_cndmask_b32 v229, v180, v181, s[74:75]\n"
      "v_cvt_pk_bf16_f32 v151, v194, v195\n"
      "v_mfma_f32_16x16x32_bf16 v[60:63], a[92:95], v[132:135], v[60:63]\n"
      "v_cndmask_b32 v200, v182, v183, s[74:75]\n"
      "ds_write_b128 v232, a[204:207] offset:7168\n"
      "v_cndmask_b32 v228, v176, v177, s[74:75]\n"
      "v_mfma_f32_16x16x32_bf16 v[184:187], a[132:135], v[148:151], v[184:187]\n"
      "s_nop 7\n"
      "v_max3_f32 v196, v184, v185, v186\n"
      "v_max_f32 v196, v196, v187\n"
      "v_cmp_nge_f32 vcc, 0x4b800000, v196\n"
      "s_cbranch_vccnz FS8_%=\n"
      "FR8_%=:\n"
      "v_cndmask_b32 v202, v186, v187, s[74:75]\n"
      "v_cndmask_b32 v201, v184, v185, s[74:75]\n"
      "v_add_f32 v200, v200, v202\n"
      "ds_write_b32 v234, v200 offset:4096\n"
      "v_add_f32 v229, v229, v201\n"
      "s_waitcnt lgkmcnt(8)\n"
      "v_mfma_f32_16x16x32_bf16 v[32:35], a[16:19], v[144:147], v[32:35]\n"
      "v_mfma_f32_16x16x32_bf16 v[36:39], a[16:19], v[148:151], v[36:39]\n"
      "v_mfma_f32_16x16x32_bf16 v[40:43], a[20:23], v[144:147], v[40:43]\n"
      "v_mfma_f32_16x16x32_bf16 v[44:47], a[20:23], v[148:151], v[44:47]\n"
      "v_mfma_f32_16x16x32_bf16 v[48:51], a[24:27], v[144:147], v[48:51]\n"
      "s_waitcnt lgkmcnt(0)\n"
      "s_barrier\n"
      "v_mfma_f32_16x16x32_bf16 v[52:55], a[24:27], v[148:151], v[52:55]\n"
      "ds_read_b32 v208, v209\n"
      "ds_read_b128 v[160:163], v235 offset:0\n"
      "ds_read_b128 v[164:167], v235 offset:1024\n"
      "ds_read_b128 v[64:67], v233 offset:0\n"
      "v_mfma_f32_16x16x32_bf16 v[56:59], a[28:31], v[144:147], v[56:59]\n"
      "ds_read_b128 v[68:71], v233 offset:1024\n"
      "ds_read_b128 v[72:75], v233 offset:2048\n"
      "ds_read_b128 v[76:79], v233 offset:3072\n"
      "ds_read_b128 v[80:83], v233 offset:4096\n"
      "v_mfma_f32_16x16x32_bf16 v[60:63], a[28:31], v[148:151], v[60:63]\n"
      "ds_read_b128 v[84:87], v233 offset:5120\n"
      "ds_read_b128 v[88:91], v233 offset:6144\n"
      "ds_read_b128 v[92:95], v233 offset:7168\n"
      "ds_read_b32 v204, v235 offset:4096\n"
      "s_waitcnt lgkmcnt(11)\n"
      "v_cmp_ne_u32 vcc, 0, v208\n"
      "s_cbranch_vccnz SLOW_%=\n"
      "AGAIN_%=:\n"
      "s_waitcnt lgkmcnt(9)\n"
      "v_mfma_f32_16x16x32_bf16 v[32:35], a[0:3], v[160:163], v[32:35]\n"
      "v_mfma_f32_16x16x32_bf16 v[36:39], a[0:3], v[164:167], v[36:39]\n"
      "v_mfma_f32_16x16x32_bf16 v[40:43], a[4:7], v[160:163], v[40:43]\n"
      "v_mfma_f32_16x16x32_bf16 v[44:47], a[4:7], v[164:167], v[44:47]\n"
      "v_mfma_f32_16x16x32_bf16 v[48:51], a[8:11], v[160:163], v[48:51]\n"
      "v_mfma_f32_16x16x32_bf16 v[52:55], a[8:11], v[164:167], v[52:55]\n"
      "v_mfma_f32_16x16x32_bf16 v[56:59], a[12:15], v[160:163], v[56:59]\n"
      "v_mfma_f32_16x16x32_bf16 v[60:63], a[12:15], v[164:167], v[60:63]\n"
      "global_load_dwordx2 v[206:207], v239, s[46:47] offset:8 sc1\n"
      "s_waitcnt lgkmcnt(7)\n"
      "v_pk_add_f32 v[32:33], v[32:33], v[64:65]\n"
      "v_pk_add_f32 v[34:35], v[34:35], v[66:67]\n"
      "v_pk_add_f32 v[36:37], v[36:37], v[68:69]\n"
      "v_pk_add_f32 v[38:39], v[38:39], v[70:71]\n"
      "v_cvt_pk_bf16_f32 v212, v32, v33\n"
      "v_cvt_pk_bf16_f32 v213, v34, v35\n"
      "v_cvt_pk_bf16_f32 v214, v36, v37\n"
      "v_cvt_pk_bf16_f32 v215, v38, v39\n"
      "buffer_store_dwordx4 v[212:215], v241, s[24:27], s78 offen offset:0 sc1\n"
      "s_waitcnt lgkmcnt(5)\n"
      "v_pk_add_f32 v[40:41], v[40:41], v[72:73]\n"
      "v_pk_add_f32 v[42:43], v[42:43], v[74:75]\n"
      "v_pk_add_f32 v[44:45], v[44:45], v[76:77]\n"
      "v_pk_add_f32 v[46:47], v[46:47], v[78:79]\n"
      "v_cvt_pk_bf16_f32 v216, v40, v41\n"
      "v_cvt_pk_bf16_f32 v217, v42, v43\n"
      "v_cvt_pk_bf16_f32 v218, v44, v45\n"
      "v_cvt_pk_bf16_f32 v219, v46, v47\n"
      "buffer_store_dwordx4 v[216:219], v241, s[24:27], s78 offen offset:1024 sc1\n"
      "s_waitcnt lgkmcnt(3)\n"
      "v_pk_add_f32 v[48:49], v[48:49], v[80:81]\n"
      "v_pk_add_f32 v[50:51], v[50:51], v[82:83]\n"
      "v_pk_add_f32 v[52:53], v[52:53], v[84:85]\n"
      "v_pk_add_f32 v[54:55], v[54:55], v[86:87]\n"
      "v_cvt_pk_bf16_f32 v220, v48, v49\n"
      "v_cvt_pk_bf16_f32 v221, v50, v51\n"
      "v_cvt_pk_bf16_f32 v222, v52, v53\n"
      "v_cvt_pk_bf16_f32 v223, v54, v55\n"
      "buffer_store_dwordx4 v[220:223], v241, s[24:27], s78 offen offset:2048 sc1\n"
      "s_waitcnt lgkmcnt(0)\n"
      "v_pk_add_f32 v[56:57], v[56:57], v[88:89]\n"
      "v_pk_add_f32 v[58:59], v[58:59], v[90:91]\n"
      "v_pk_add_f32 v[60:61], v[60:61], v[92:93]\n"
      "v_pk_add_f32 v[62:63], v[62:63], v[94:95]\n"
      "v_cvt_pk_bf16_f32 v224, v56, v57\n"
      "v_cvt_pk_bf16_f32 v225, v58, v59\n"
      "v_cvt_pk_bf16_f32 v226, v60, v61\n"
      "v_cvt_pk_bf16_f32 v227, v62, v63\n"
      "buffer_store_dwordx4 v[224:227], v241, s[24:27], s78 offen offset:3072 sc1\n"
      "v_add_f32 v229, v229, v204\n"
      "v_cndmask_b32 v230, v228, v229, s[94:95]\n"
      "v_add_u32 v231, s40, v230\n"
      "v_add3_u32 v231, v231, v212, v213\n"
      "v_add3_u32 v231, v231, v214, v215\n"
      "v_add3_u32 v231, v231, v216, v217\n"
      "v_add3_u32 v231, v231, v218, v219\n"
      "v_add3_u32 v231, v231, v220, v221\n"
      "v_add3_u32 v231, v231, v222, v223\n"
      "v_add3_u32 v231, v231, v224, v225\n"
      "v_add3_u32 v231, v231, v226, v227\n"
      "buffer_store_dwordx2 v[230:231], v240, s[24:27], s78 offen sc1\n"
      "v_xor_b32 v244, 0x10000, v244\n"
      "v_xor_b32 v245, 0x10000, v245\n"
      "v_xor_b32 v246, 0x10000, v246\n"
      "v_xor_b32 v247, 0x10000, v247\n"
      "s_waitcnt vmcnt(6)\n"
      "s_barrier\n"
      "ds_read_b64_tr_b16 a[64:65], v244 offset:16384\n"
      "ds_read_b64_tr_b16 a[66:67], v244 offset:20480\n"
      "ds_read_b64_tr_b16 a[68:69], v245 offset:16384\n"
      "ds_read_b64_tr_b16 a[70:71], v245 offset:20480\n"
      "ds_read_b64_tr_b16 a[72:73], v246 offset:16384\n"
      "ds_read_b64_tr_b16 a[74:75], v246 offset:20480\n"
      "ds_read_b64_tr_b16 a[76:77], v247 offset:16384\n"
      "ds_read_b64_tr_b16 a[78:79], v247 offset:20480\n"
      "s_waitcnt lgkmcnt(4)\n"
      "v_mfma_f32_16x16x32_bf16 a[144:147], a[64:67], v[136:139], a[144:147]\n"
      "ds_read_b64_tr_b16 a[96:97], v244 offset:17408\n"
      "ds_read_b64_tr_b16 a[98:99], v244 offset:21504\n"
      "v_mfma_f32_16x16x32_bf16 a[148:151], a[64:67], v[140:143], a[148:151]\n"
      "ds_read_b64_tr_b16 a[100:101], v245 offset:17408\n"
      "ds_read_b64_tr_b16 a[102:103], v245 offset:21504\n"
      "v_mfma_f32_16x16x32_bf16 a[152:155], a[68:71], v[136:139], a[152:155]\n"
      "ds_read_b64_tr_b16 a[104:105], v246 offset:17408\n"
      "ds_read_b64_tr_b16 a[106:107], v246 offset:21504\n"
      "v_mfma_f32_16x16x32_bf16 a[156:159], a[68:71], v[140:143], a[156:159]\n"
      "ds_read_b64_tr_b16 a[108:109], v247 offset:17408\n"
      "ds_read_b64_tr_b16 a[110:111], v247 offset:21504\n"
      "s_waitcnt lgkmcnt(8)\n"
      "v_mfma_f32_16x16x32_bf16 a[160:163], a[72:75], v[136:139], a[160:163]\n"
      "ds_read_b64_tr_b16 a[80:81], v244 offset:32768\n"
      "ds_read_b64_tr_b16 a[82:83], v244 offset:36864\n"
      "v_mfma_f32_16x16x32_bf16 a[164:167], a[72:75], v[140:143], a[164:167]\n"
      "ds_read_b64_tr_b16 a[84:85], v245 offset:32768\n"
      "ds_read_b64_tr_b16 a[86:87], v245 offset:36864\n"
      "v_mfma_f32_16x16x32_bf16 a[168:171], a[76:79], v[136:139], a[168:171]\n"
      "ds_read_b64_tr_b16 a[88:89], v246 offset:32768\n"
      "ds_read_b64_tr_b16 a[90:91], v246 offset:36864\n"
      "v_mfma_f32_16x16x32_bf16 a[172:175], a[76:79], v[140:143], a[172:175]\n"
      "ds_read_b64_tr_b16 a[92:93], v247 offset:32768\n"
      "ds_read_b64_tr_b16 a[94:95], v247 offset:36864\n"
      "s_waitcnt lgkmcnt(8)\n"
      "v_mfma_f32_16x16x32_bf16 a[144:147], a[96:99], v[152:155], a[144:147]\n"
      "s_sub_u32 s76, 18432, s78\n"
      "v_mfma_f32_16x16x32_bf16 a[148:151], a[96:99], v[156:159], a[148:151]\n"
      "s_mov_b32 s63, 0x40000\n"
      "v_mfma_f32_16x16x32_bf16 a[152:155], a[100:103], v[152:155], a[152:155]\n"
      "v_mov_b32 v198, 0\n"
      "v_mfma_f32_16x16x32_bf16 a[156:159], a[100:103], v[156:159], a[156:159]\n"
      "v_and_b32 v197, 0xf0, v241\n"
      "v_mfma_f32_16x16x32_bf16 a[160:163], a[104:107], v[152:155], a[160:163]\n"
      "v_lshrrev_b32 v196, 3, v241\n"
      "v_mfma_f32_16x16x32_bf16 a[164:167], a[104:107], v[156:159], a[164:167]\n"
      "v_and_b32 v196, 32, v196\n"
      "v_mfma_f32_16x16x32_bf16 a[168:171], a[108:111], v[152:155], a[168:171]\n"
      "v_lshl_or_b32 v196, v197, 4, v196\n"
      "v_mfma_f32_16x16x32_bf16 a[172:175], a[108:111], v[156:159], a[172:175]\n"
      "v_lshrrev_b32 v199, 5, v241\n"
      "s_waitcnt lgkmcnt(0)\n"
      "v_mfma_f32_16x16x32_bf16 a[144:147], a[80:83], a[216:219], a[144:147]\n"
      "v_and_or_b32 v196, v199, 16, v196\n"
      "v_mfma_f32_16x16x32_bf16 a[148:151], a[80:83], a[220:223], a[148:151]\n"
      "v_mov_b32 v191, s40\n"
      "v_mfma_f32_16x16x32_bf16 a[152:155], a[84:87], a[216:219], a[152:155]\n"
      "v_mfma_f32_16x16x32_bf16 a[156:159], a[84:87], a[220:223], a[156:159]\n"
      "v_mfma_f32_16x16x32_bf16 a[160:163], a[88:91], a[216:219], a[160:163]\n"
      "v_mfma_f32_16x16x32_bf16 a[164:167], a[88:91], a[220:223], a[164:167]\n"
      "v_mfma_f32_16x16x32_bf16 a[168:171], a[92:95], a[216:219], a[168:171]\n"
      "v_mfma_f32_16x16x32_bf16 a[172:175], a[92:95], a[220:223], a[172:175]\n"
      "v_mfma_f32_16x16x32_bf16 v[0:3], a[64:67], v[128:131], v[0:3]\n"
      "v_mfma_f32_16x16x32_bf16 v[4:7], a[64:67], v[132:135], v[4:7]\n"
      "ds_write_b128 v232, a[144:147] offset:32768\n"
      "v_mfma_f32_16x16x32_bf16 v[8:11], a[68:71], v[128:131], v[8:11]\n"
      "v_mfma_f32_16x16x32_bf16 v[12:15], a[68:71], v[132:135], v[12:15]\n"
      "v_mfma_f32_16x16x32_bf16 v[16:19], a[72:75], v[128:131], v[16:19]\n"
      "ds_write_b128 v232, a[148:151] offset:33792\n"
      "v_mfma_f32_16x16x32_bf16 v[20:23], a[72:75], v[132:135], v[20:23]\n"
      "v_mfma_f32_16x16x32_bf16 v[24:27], a[76:79], v[128:131], v[24:27]\n"
      "v_mfma_f32_16x16x32_bf16 v[28:31], a[76:79], v[132:135], v[28:31]\n"
      "ds_write_b128 v232, a[152:155] offset:34816\n"
      "v_mfma_f32_16x16x32_bf16 v[0:3], a[96:99], v[144:147], v[0:3]\n"
      "v_mfma_f32_16x16x32_bf16 v[4:7], a[96:99], v[148:151], v[4:7]\n"
      "v_mfma_f32_16x16x32_bf16 v[8:11], a[100:103], v[144:147], v[8:11]\n"
      "ds_write_b128 v232, a[156:159] offset:35840\n"
      "v_mfma_f32_16x16x32_bf16 v[12:15], a[100:103], v[148:151], v[12:15]\n"
      "v_mfma_f32_16x16x32_bf16 v[16:19], a[104:107], v[144:147], v[16:19]\n"
      "v_mfma_f32_16x16x32_bf16 v[20:23], a[104:107], v[148:151], v[20:23]\n"
      "ds_write_b128 v232, a[160:163] offset:36864\n"
      "v_mfma_f32_16x16x32_bf16 v[24:27], a[108:111], v[144:147], v[24:27]\n"
      "v_mfma_f32_16x16x32_bf16 v[28:31], a[108:111], v[148:151], v[28:31]\n"
      "v_mfma_f32_16x16x32_bf16 v[0:3], a[80:83], a[208:211], v[0:3]\n"
      "ds_write_b128 v232, a[164:167] offset:37888\n"
      "v_mfma_f32_16x16x32_bf16 v[4:7], a[80:83], a[212:215], v[4:7]\n"
      "v_mfma_f32_16x16x32_bf16 v[8:11], a[84:87], a[208:211], v[8:11]\n"
      "v_mfma_f32_16x16x32_bf16 v[12:15], a[84:87], a[212:215], v[12:15]\n"
      "ds_write_b128 v232, a[168:171] offset:38912\n"
      "v_mfma_f32_16x16x32_bf16 v[16:19], a[88:91], a[208:211], v[16:19]\n"
      "v_mfma_f32_16x16x32_bf16 v[20:23], a[88:91], a[212:215], v[20:23]\n"
      "v_mfma_f32_16x16x32_bf16 v[24:27], a[92:95], a[208:211], v[24:27]\n"
      "ds_write_b128 v232, a[172:175] offset:39936\n"
      "v_mfma_f32_16x16x32_bf16 v[28:31], a[92:95], a[212:215], v[28:31]\n"
      "s_waitcnt vmcnt(0)\n"
      "v_subrev_u32 v205, s82, v206\n"
      "v_cmp_ne_u32 vcc, s40, v207\n"
      "s_cbranch_vccnz LAG_%=\n"
      "v_cmp_lt_i32 vcc, 10, v205\n"
      "s_cbranch_vccnz LAG_%=\n"
      "READ_%=:\n"
      "buffer_load_dwordx2 v[160:161], v240, s[24:27], s76 offen sc1\n"
      "buffer_load_dwordx4 v[96:99], v241, s[24:27], s76 offen offset:0 sc1\n"
      "buffer_load_dwordx4 v[100:103], v241, s[24:27], s76 offen offset:1024 sc1\n"
      "buffer_load_dwordx4 v[104:107], v241, s[24:27], s76 offen offset:2048 sc1\n"
      "buffer_load_dwordx4 v[108:111], v241, s[24:27], s76 offen offset:3072 sc1\n"
      "s_waitcnt lgkmcnt(0)\n"
      "s_barrier\n"
      "ds_read_b128 v[64:67], v233 offset:32768\n"
      "ds_read_b128 v[68:71], v233 offset:33792\n"
      "ds_read_b128 v[72:75], v233 offset:34816\n"
      "ds_read_b128 v[76:79], v233 offset:35840\n"
      "ds_read_b128 v[80:83], v233 offset:36864\n"
      "ds_read_b128 v[84:87], v233 offset:37888\n"
      "ds_read_b128 v[88:91], v233 offset:38912\n"
      "ds_read_b128 v[92:95], v233 offset:39936\n"
      "s_waitcnt lgkmcnt(6)\n"
      "v_pk_add_f32 v[0:1], v[0:1], v[64:65]\n"
      "v_pk_add_f32 v[2:3], v[2:3], v[66:67]\n"
      "v_pk_add_f32 v[4:5], v[4:5], v[68:69]\n"
      "v_pk_add_f32 v[6:7], v[6:7], v[70:71]\n"
      "s_waitcnt lgkmcnt(4)\n"
      "v_pk_add_f32 v[8:9], v[8:9], v[72:73]\n"
      "v_pk_add_f32 v[10:11], v[10:11], v[74:75]\n"
      "v_pk_add_f32 v[12:13], v[12:13], v[76:77]\n"
      "v_pk_add_f32 v[14:15], v[14:15], v[78:79]\n"
      "s_waitcnt lgkmcnt(2)\n"
      "v_pk_add_f32 v[16:17], v[16:17], v[80:81]\n"
      "v_pk_add_f32 v[18:19], v[18:19], v[82:83]\n"
      "v_pk_add_f32 v[20:21], v[20:21], v[84:85]\n"
      "v_pk_add_f32 v[22:23], v[22:23], v[86:87]\n"
      "s_waitcnt lgkmcnt(0)\n"
      "v_pk_add_f32 v[24:25], v[24:25], v[88:89]\n"
      "v_pk_add_f32 v[26:27], v[26:27], v[90:91]\n"
      "v_pk_add_f32 v[28:29], v[28:29], v[92:93]\n"
      "v_pk_add_f32 v[30:31], v[30:31], v[94:95]\n"
      "TRY_%=:\n"
      "s_waitcnt vmcnt(4)\n"
      "v_mov_b32 v162, v160\n"
      "v_add_u32 v231, s40, v160\n"
      "s_nop 0\n"
      "v_permlane32_swap_b32 v160, v162\n"
      "v_sub_f32 v168, v160, v228\n"
      "v_min_f32 v172, 0, -v168\n"
      "v_min_f32 v168, 0, v168\n"
      "v_exp_f32 v168, v168\n"
      "v_exp_f32 v172, v172\n"
      "v_mul_f32 v163, v229, v168\n"
      "v_fmac_f32 v163, v162, v172\n"
      "v_rcp_f32 v163, v163\n"
      "s_nop 0\n"
      "v_mul_f32 v168, v168, v163\n"
      "v_mul_f32 v172, v172, v163\n"
      "v_mov_b32 v169, v168\n"
      "v_mov_b32 v173, v172\n"
      "s_nop 0\n"
      "v_permlane16_swap_b32 v168, v169\n"
      "v_permlane16_swap_b32 v172, v173\n"
      "v_pk_mul_f32 v[32:33], v[0:1], v[168:169] op_sel:[0,0] op_sel_hi:[1,0]\n"
      "v_pk_mul_f32 v[34:35], v[2:3], v[168:169] op_sel:[0,0] op_sel_hi:[1,0]\n"
      "v_pk_mul_f32 v[36:37], v[4:5], v[168:169] op_sel:[0,1] op_sel_hi:[1,1]\n"
      "v_pk_mul_f32 v[38:39], v[6:7], v[168:169] op_sel:[0,1] op_sel_hi:[1,1]\n"
      "v_pk_mul_f32 v[40:41], v[8:9], v[168:169] op_sel:[0,0] op_sel_hi:[1,0]\n"
      "v_pk_mul_f32 v[42:43], v[10:11], v[168:169] op_sel:[0,0] op_sel_hi:[1,0]\n"
      "v_pk_mul_f32 v[44:45], v[12:13], v[168:169] op_sel:[0,1] op_sel_hi:[1,1]\n"
      "v_pk_mul_f32 v[46:47], v[14:15], v[168:169] op_sel:[0,1] op_sel_hi:[1,1]\n"
      "v_pk_mul_f32 v[48:49], v[16:17], v[168:169] op_sel:[0,0] op_sel_hi:[1,0]\n"
      "v_pk_mul_f32 v[50:51], v[18:19], v[168:169] op_sel:[0,0] op_sel_hi:[1,0]\n"
      "v_pk_mul_f32 v[52:53], v[20:21], v[168:169] op_sel:[0,1] op_sel_hi:[1,1]\n"
      "v_pk_mul_f32 v[54:55], v[22:23], v[168:169] op_sel:[0,1] op_sel_hi:[1,1]\n"
      "v_pk_mul_f32 v[56:57], v[24:25], v[168:169] op_sel:[0,0] op_sel_hi:[1,0]\n"
      "v_pk_mul_f32 v[58:59], v[26:27], v[168:169] op_sel:[0,0] op_sel_hi:[1,0]\n"
      "v_pk_mul_f32 v[60:61], v[28:29], v[168:169] op_sel:[0,1] op_sel_hi:[1,1]\n"
      "v_pk_mul_f32 v[62:63], v[30:31], v[168:169] op_sel:[0,1] op_sel_hi:[1,1]\n"
      "s_waitcnt vmcnt(3)\n"
      "v_add3_u32 v231, v231, v96, v97\n"
      "v_add3_u32 v231, v231, v98, v99\n"
      "s_waitcnt vmcnt(2)\n"
      "v_add3_u32 v231, v231, v100, v101\n"
      "v_add3_u32 v231, v231, v102, v103\n"
      "s_waitcnt vmcnt(1)\n"
      "v_add3_u32 v231, v231, v104, v105\n"
      "v_add3_u32 v231, v231, v106, v107\n"
      "s_waitcnt vmcnt(0)\n"
      "v_add3_u32 v231, v231, v108, v109\n"
      "v_add3_u32 v231, v231, v110, v111\n"
      "v_cmp_ne_u32 vcc, v231, v161\n"
      "s_cbranch_vccnz RETRY_%=\n"
      "GOT_%=:\n"
      "v_lshlrev_b32 v0, 16, v96\n"
      "v_and_b32 v1, 0xffff0000, v96\n"
      "v_lshlrev_b32 v2, 16, v97\n"
      "v_and_b32 v3, 0xffff0000, v97\n"
      "v_lshlrev_b32 v8, 16, v100\n"
      "v_and_b32 v9, 0xffff0000, v100\n"
      "v_lshlrev_b32 v10, 16, v101\n"
      "v_and_b32 v11, 0xffff0000, v101\n"
      "v_pk_fma_f32 v[32:33], v[0:1], v[172:173], v[32:33] op_sel:[0,0,0] op_sel_hi:[1,0,1]\n"
      "v_pk_fma_f32 v[34:35], v[2:3], v[172:173], v[34:35] op_sel:[0,0,0] op_sel_hi:[1,0,1]\n"
      "v_pk_fma_f32 v[40:41], v[8:9], v[172:173], v[40:41] op_sel:[0,0,0] op_sel_hi:[1,0,1]\n"
      "v_pk_fma_f32 v[42:43], v[10:11], v[172:173], v[42:43] op_sel:[0,0,0] op_sel_hi:[1,0,1]\n"
      "v_cvt_pk_bf16_f32 v112, v32, v33\n"
      "v_cvt_pk_bf16_f32 v114, v40, v41\n"
      "v_cvt_pk_bf16_f32 v113, v34, v35\n"
      "v_cvt_pk_bf16_f32 v115, v42, v43\n"
      "v_permlane16_swap_b32 v112, v114\n"
      "s_nop 0\n"
      "v_permlane16_swap_b32 v113, v115\n"
      "global_store_dwordx4 v196, v[112:115], s[30:31] offset:-4096 sc0 sc1 nt\n"
      "v_lshlrev_b32 v16, 16, v104\n"
      "v_and_b32 v17, 0xffff0000, v104\n"
      "v_lshlrev_b32 v18, 16, v105\n"
      "v_and_b32 v19, 0xffff0000, v105\n"
      "v_lshlrev_b32 v24, 16, v108\n"
      "v_and_b32 v25, 0xffff0000, v108\n"
      "v_lshlrev_b32 v26, 16, v109\n"
      "v_and_b32 v27, 0xffff0000, v109\n"
      "v_pk_fma_f32 v[48:49], v[16:17], v[172:173], v[48:49] op_sel:[0,0,0] op_sel_hi:[1,0,1]\n"
      "v_pk_fma_f32 v[50:51], v[18:19], v[172:173], v[50:51] op_sel:[0,0,0] op_sel_hi:[1,0,1]\n"
      "v_pk_fma_f32 v[56:57], v[24:25], v[172:173], v[56:57] op_sel:[0,0,0] op_sel_hi:[1,0,1]\n"
      "v_pk_fma_f32 v[58:59], v[26:27], v[172:173], v[58:59] op_sel:[0,0,0] op_sel_hi:[1,0,1]\n"
      "v_cvt_pk_bf16_f32 v116, v48, v49\n"
      "v_cvt_pk_bf16_f32 v118, v56, v57\n"
      "v_cvt_pk_bf16_f32 v117, v50, v51\n"
      "v_cvt_pk_bf16_f32 v119, v58, v59\n"
      "v_permlane16_swap_b32 v116, v118\n"
      "s_nop 0\n"
      "v_permlane16_swap_b32 v117, v119\n"
      "global_store_dwordx4 v196, v[116:119], s[30:31] offset:-4032 sc0 sc1 nt\n"
      "v_lshlrev_b32 v4, 16, v98\n"
      "v_and_b32 v5, 0xffff0000, v98\n"
      "v_lshlrev_b32 v6, 16, v99\n"
      "v_and_b32 v7, 0xffff0000, v99\n"
      "v_lshlrev_b32 v12, 16, v102\n"
      "v_and_b32 v13, 0xffff0000, v102\n"
      "v_lshlrev_b32 v14, 16, v103\n"
      "v_and_b32 v15, 0xffff0000, v103\n"
      "v_pk_fma_f32 v[36:37], v[4:5], v[172:173], v[36:37] op_sel:[0,1,0] op_sel_hi:[1,1,1]\n"
      "v_pk_fma_f32 v[38:39], v[6:7], v[172:173], v[38:39] op_sel:[0,1,0] op_sel_hi:[1,1,1]\n"
      "v_pk_fma_f32 v[44:45], v[12:13], v[172:173], v[44:45] op_sel:[0,1,0] op_sel_hi:[1,1,1]\n"
      "v_pk_fma_f32 v[46:47], v[14:15], v[172:173], v[46:47] op_sel:[0,1,0] op_sel_hi:[1,1,1]\n"
      "v_cvt_pk_bf16_f32 v120, v36, v37\n"
      "v_cvt_pk_bf16_f32 v122, v44, v45\n"
      "v_cvt_pk_bf16_f32 v121, v38, v39\n"
      "v_cvt_pk_bf16_f32 v123, v46, v47\n"
      "v_permlane16_swap_b32 v120, v122\n"
      "s_nop 0\n"
      "v_permlane16_swap_b32 v121, v123\n"
      "global_store_dwordx4 v196, v[120:123], s[30:31] offset:0 sc0 sc1 nt\n"
      "v_lshlrev_b32 v20, 16, v106\n"
      "v_and_b32 v21, 0xffff0000, v106\n"
      "v_lshlrev_b32 v22, 16, v107\n"
      "v_and_b32 v23, 0xffff0000, v107\n"
      "v_lshlrev_b32 v28, 16, v110\n"
      "v_and_b32 v29, 0xffff0000, v110\n"
      "v_lshlrev_b32 v30, 16, v111\n"
      "v_and_b32 v31, 0xffff0000, v111\n"
      "v_pk_fma_f32 v[52:53], v[20:21], v[172:173], v[52:53] op_sel:[0,1,0] op_sel_hi:[1,1,1]\n"
      "v_pk_fma_f32 v[54:55], v[22:23], v[172:173], v[54:55] op_sel:[0,1,0] op_sel_hi:[1,1,1]\n"
      "v_pk_fma_f32 v[60:61], v[28:29], v[172:173], v[60:61] op_sel:[0,1,0] op_sel_hi:[1,1,1]\n"
      "v_pk_fma_f32 v[62:63], v[30:31], v[172:173], v[62:63] op_sel:[0,1,0] op_sel_hi:[1,1,1]\n"
      "v_cvt_pk_bf16_f32 v124, v52, v53\n"
      "v_cvt_pk_bf16_f32 v126, v60, v61\n"
      "v_cvt_pk_bf16_f32 v125, v54, v55\n"
      "v_cvt_pk_bf16_f32 v127, v62, v63\n"
      "v_permlane16_swap_b32 v124, v126\n"
      "s_nop 0\n"
      "v_permlane16_swap_b32 v125, v127\n"
      "global_store_dwordx4 v196, v[124:127], s[30:31] offset:64 sc0 sc1 nt\n"
      "s_mov_b64 exec, 1\n"
      "global_store_dword v198, v191, s[46:47] sc1\n"
      "s_mov_b64 exec, -1\n"
      "ENDE_%=:\n"
      ".subsection 1\n"
      "MH_%=:\n"
      "s_cmp_lg_u32 s54, 0\n"
      "s_waitcnt vmcnt(12) lgkmcnt(0)\n"
      "s_cbranch_scc1 W6_%=\n"
      "s_waitcnt vmcnt(8)\n"
      "W6_%=:\n"
      "s_barrier\n"
      "v_mfma_f32_16x16x32_bf16 v[32:35], a[112:115], v[144:147], v[32:35]\n"
      "s_xor_b64 s[56:57], s[56:57], s[32:33]\n"
      "ds_read_b128 a[64:67], v242 offset:0\n"
      "ds_read_b128 a[72:75], v243 offset:0\n"
      "v_mfma_f32_16x16x32_bf16 v[36:39], a[112:115], v[148:151], v[36:39]\n"
      "s_add_u32 s61, s61, 1\n"
      "ds_read_b128 a[80:83], v242 offset:1024\n"
      "ds_read_b128 a[88:91], v243 offset:1024\n"
      "v_mfma_f32_16x16x32_bf16 a[176:179], a[112:115], v[152:155], a[176:179]\n"
      "s_add_u32 s62, s62, 0x4000\n"
      "v_exp_f32 v189, v89\n"
      "v_mfma_f32_16x16x32_bf16 a[180:183], a[112:115], v[156:159], a[180:183]\n"
      "s_and_b32 s62, s62, 0x1ffff\n"
      "v_cvt_pk_bf16_f32 v139, v194, v195\n"
      "v_cvt_pk_bf16_f32 v140, v188, v189\n"
      "v_mfma_f32_16x16x32_bf16 a[188:191], a[116:119], v[156:159], a[188:191]\n"
      "v_readlane_b32 s60, v219, s61\n"
      "v_exp_f32 v190, v90\n"
      "v_mfma_f32_16x16x32_bf16 v[184:187], a[136:139], v[136:139], v[184:187]\n"
      "s_mov_b32 s83, s82\n"
      "v_exp_f32 v191, v91\n"
      "v_mfma_f32_16x16x32_bf16 a[184:187], a[116:119], v[152:155], a[184:187]\n"
      "v_exp_f32 v192, v92\n"
      "v_mfma_f32_16x16x32_bf16 v[44:47], a[116:119], v[148:151], v[44:47]\n"
      "s_add_u32 s66, s66, s86\n"
      "v_exp_f32 v193, v93\n"
      "v_mfma_f32_16x16x32_bf16 v[40:43], a[116:119], v[144:147], v[40:43]\n"
      "s_add_u32 s82, s82, s86\n"
      "v_cvt_pk_bf16_f32 v141, v190, v191\n"
      "v_cvt_pk_bf16_f32 v142, v192, v193\n"
      "v_mfma_f32_16x16x32_bf16 v[48:51], a[120:123], v[144:147], v[48:51]\n"
      "s_add_u32 s65, s65, s86\n"
      "v_exp_f32 v194, v94\n"
      "v_mfma_f32_16x16x32_bf16 v[52:55], a[120:123], v[148:151], v[52:55]\n"
      "s_max_u32 s88, s66, s65\n"
      "v_exp_f32 v195, v95\n"
      "s_waitcnt lgkmcnt(0)\n"
      "v_mfma_f32_16x16x32_bf16 a[192:195], a[120:123], v[152:155], a[192:195]\n"
      "v_cvt_pk_bf16_f32 v143, v194, v195\n"
      "v_mfma_f32_16x16x32_bf16 a[196:199], a[120:123], v[156:159], a[196:199]\n"
      "ds_read_b128 a[68:71], v242 offset:4096\n"
      "ds_read_b128 a[76:79], v243 offset:4096\n"
      "v_mfma_f32_16x16x32_bf16 v[184:187], a[140:143], v[140:143], v[184:187]\n"
      "s_mov_b32 m0, s62\n"
      "ds_read_b128 a[84:87], v242 offset:5120\n"
      "ds_read_b128 a[92:95], v243 offset:5120\n"
      "v_mfma_f32_16x16x32_bf16 a[204:207], a[124:127], v[156:159], a[204:207]\n"
      "buffer_load_dwordx4 v228, s[56:59], s60 offen nt lds\n"
      "v_xor_b32 v244, 0x10000, v244\n"
      "v_xor_b32 v245, 0x10000, v245\n"
      "v_mfma_f32_16x16x32_bf16 a[200:203], a[124:127], v[152:155], a[200:203]\n"
      "buffer_load_dwordx4 v229, s[56:59], s60 offen offset:1024 nt lds\n"
      "v_xor_b32 v246, 0x10000, v246\n"
      "v_xor_b32 v247, 0x10000, v247\n"
      "v_mfma_f32_16x16x32_bf16 v[60:63], a[124:127], v[148:151], v[60:63]\n"
      "buffer_load_dwordx4 v230, s[56:59], s60 offen offset:2048 nt lds\n"
      "v_max3_f32 v196, v184, v185, v186\n"
      "v_max_f32 v196, v196, v187\n"
      "v_mfma_f32_16x16x32_bf16 v[56:59], a[124:127], v[144:147], v[56:59]\n"
      "buffer_load_dwordx4 v231, s[56:59], s60 offen offset:3072 nt lds\n"
      "v_cmp_nge_f32 vcc, 0x4b800000, v196\n"
      "s_cbranch_vccnz FS11_%=\n"
      "FR11_%=:\n"
      "v_mfma_f32_16x16x32_bf16 v[64:67], a[64:67], a[0:3], v[160:163]\n"
      "v_exp_f32 v188, v96\n"
      "v_mfma_f32_16x16x32_bf16 v[64:67], a[72:75], a[16:19], v[64:67]\n"
      "v_add_f32 v180, v180, v184\n"
      "v_add_f32 v181, v181, v185\n"
      "v_mfma_f32_16x16x32_bf16 v[64:67], a[80:83], a[32:35], v[64:67]\n"
      "v_exp_f32 v189, v97\n"
      "v_mfma_f32_16x16x32_bf16 v[64:67], a[88:91], a[48:51], v[64:67]\n"
      "v_add_f32 v182, v182, v186\n"
      "v_add_f32 v183, v183, v187\n"
      "v_mfma_f32_16x16x32_bf16 v[72:75], a[64:67], a[4:7], v[164:167]\n"
      "v_exp_f32 v190, v98\n"
      "v_mfma_f32_16x16x32_bf16 v[72:75], a[72:75], a[20:23], v[72:75]\n"
      "v_exp_f32 v191, v99\n"
      "v_mfma_f32_16x16x32_bf16 v[72:75], a[80:83], a[36:39], v[72:75]\n"
      "v_cvt_pk_bf16_f32 v144, v188, v189\n"
      "v_cvt_pk_bf16_f32 v145, v190, v191\n"
      "v_mfma_f32_16x16x32_bf16 v[72:75], a[88:91], a[52:55], v[72:75]\n"
      "v_exp_f32 v192, v100\n"
      "v_mfma_f32_16x16x32_bf16 v[80:83], a[64:67], a[8:11], v[168:171]\n"
      "s_add_u32 s39, s39, s39\n"
      "v_exp_f32 v193, v101\n"
      "v_mfma_f32_16x16x32_bf16 v[80:83], a[72:75], a[24:27], v[80:83]\n"
      "s_memrealtime s[68:69]\n"
      "v_exp_f32 v194, v102\n"
      "v_mfma_f32_16x16x32_bf16 v[80:83], a[80:83], a[40:43], v[80:83]\n"
      "v_exp_f32 v195, v103\n"
      "v_mfma_f32_16x16x32_bf16 v[80:83], a[88:91], a[56:59], v[80:83]\n"
      "v_cvt_pk_bf16_f32 v146, v192, v193\n"
      "v_cvt_pk_bf16_f32 v147, v194, v195\n"
      "v_mfma_f32_16x16x32_bf16 v[88:91], a[64:67], a[12:15], v[172:175]\n"
      "s_nop 0\n"
      "v_mfma_f32_16x16x32_bf16 v[184:187], a[128:131], v[144:147], 0\n"
      "s_nop 0\n"
      "v_mfma_f32_16x16x32_bf16 v[88:91], a[72:75], a[28:31], v[88:91]\n"
      "s_nop 0\n"
      "v_mfma_f32_16x16x32_bf16 v[88:91], a[80:83], a[44:47], v[88:91]\n"
      "s_nop 0\n"
      "s_cbranch_scc1 SP1_%=\n"
      "s_branch HD1_%=\n"
      "HOOK_%=:\n"
      "buffer_load_dword v250, v251, s[20:23], 0 offen sc1\n"
      "s_swappc_b64 s[84:85], s[84:85]\n"
      "s_waitcnt vmcnt(12)\n"
      "v_subrev_u32 v250, s71, v250\n"
      "v_min_u32 v250, 0x258, v250\n"
      "s_nop 1\n"
      "v_add_u32_dpp v250, v250, v250 quad_perm:[1,0,3,2] row_mask:0xf bank_mask:0xf\n"
      "s_nop 1\n"
      "v_add_u32_dpp v250, v250, v250 quad_perm:[2,3,0,1] row_mask:0xf bank_mask:0xf\n"
      "s_nop 1\n"
      "v_add_u32_dpp v250, v250, v250 row_half_mirror row_mask:0xf bank_mask:0xf\n"
      "s_nop 0\n"
      "v_readfirstlane_b32 s67, v250\n"
      "s_sub_u32 s66, s66, s81\n"
      "s_lshr_b32 s67, s67, 3\n"
      "s_add_u32 s66, s66, s67\n"
      "s_max_u32 s88, s66, s65\n"
      "s_mov_b32 s86, 57\n"
      "s_mov_b32 s77, 171\n"
      "v_bfe_u32 v251, v241, 2, 7\n"
      "v_add_u32 v251, s79, v251\n"
      "buffer_load_dword v250, v251, s[20:23], 0 offen sc1\n"
      "s_swappc_b64 s[84:85], s[84:85]\n"
      "s_mov_b32 s86, 55\n"
      "s_mov_b32 s77, 165\n"
      "s_swappc_b64 s[84:85], s[84:85]\n"
      "s_mov_b32 s89, 0\n"
      "s_swappc_b64 s[84:85], s[84:85]\n"
      "s_mov_b32 s86, 54\n"
      "s_mov_b32 s77, 162\n"
      "s_swappc_b64 s[84:85], s[84:85]\n"
      "s_cmp_lt_u32 s89, 2\n"
      "s_cselect_b32 s67, 2048, s72\n"
      "s_mov_b32 s89, 0\n"
      "s_cmp_lt_u32 s67, 2048\n"
      "s_cbranch_scc0 NV0_%=\n"
      "v_and_b32 v216, 0x1f0, v241\n"
      "v_lshlrev_b32 v216, 4, v216\n"
      "v_mov_b32 v215, s82\n"
      "s_lshr_b32 s67, s67, 4\n"
      "s_add_u32 s67, s67, 8192\n"
      "buffer_store_dword v215, v216, s[20:23], s67 offen sc1\n"
      "NV0_%=:\n"
      "s_swappc_b64 s[84:85], s[84:85]\n"
      "buffer_load_dword v250, v251, s[20:23], 0 offen sc1\n"
      "s_swappc_b64 s[84:85], s[84:85]\n"
      "s_mov_b32 s86, 53\n"
      "s_mov_b32 s77, 159\n"
      "s_min_i32 s70, s70, 150\n"
      "s_waitcnt vmcnt(12)\n"
      "v_sub_u32 v250, s82, v250\n"
      "v_cmp_gt_u32 vcc, 1600, v250\n"
      "s_movk_i32 s76, 45\n"
      "s_nop 2\n"
      "s_bcnt1_i32_b32 s67, vcc_lo\n"
      "s_cmp_eq_u32 s70, 60\n"
      "s_cselect_b32 s67, 0, s67\n"
      "s_cmp_ge_u32 s67, 12\n"
      "s_cbranch_scc1 SM0_%=\n"
      "s_swappc_b64 s[84:85], s[84:85]\n"
      "s_cmp_lt_u32 s89, 4\n"
      "s_cselect_b32 s67, 2048, s72\n"
      "s_mov_b32 s89, 0\n"
      "s_cmp_lt_u32 s67, 2048\n"
      "s_cbranch_scc0 NV1_%=\n"
      "v_and_b32 v216, 0x1f0, v241\n"
      "v_lshlrev_b32 v216, 4, v216\n"
      "v_mov_b32 v215, s82\n"
      "s_lshr_b32 s67, s67, 4\n"
      "s_add_u32 s67, s67, 8192\n"
      "buffer_store_dword v215, v216, s[20:23], s67 offen sc1\n"
      "NV1_%=:\n"
      "s_swappc_b64 s[84:85], s[84:85]\n"
      "buffer_load_dword v250, v251, s[20:23], 0 offen sc1\n"
      "s_swappc_b64 s[84:85], s[84:85]\n"
      "s_waitcnt vmcnt(12)\n"
      "v_sub_u32 v250, s82, v250\n"
      "v_cmp_gt_u32 vcc, 1600, v250\n"
      "s_movk_i32 s76, 65\n"
      "s_nop 2\n"
      "s_bcnt1_i32_b32 s67, vcc_lo\n"
      "s_cmp_eq_u32 s70, 60\n"
      "s_cselect_b32 s67, 0, s67\n"
      "s_cmp_ge_u32 s67, 12\n"
      "s_cbranch_scc1 SM1_%=\n"
      "s_swappc_b64 s[84:85], s[84:85]\n"
      "s_getpc_b64 s[74:75]\n"
      "PF_%=:\n"
      "s_sub_u32 s74, s74, PF_%=-END_%=\n"
      "s_subb_u32 s75, s75, 0\n"
      "s_lshl_b32 s76, %[blk], 7\n"
      "s_and_b32 s76, s76, 0x1c00\n"
      "s_min_u32 s76, s76, ENDE_%=-END_%=-1024\n"
      "v_add_u32 v200, s76, v241\n"
      "global_load_dwordx4 v[200:203], v200, s[74:75]\n"
      "v_mov_b32 v212, 0\n"
      "global_load_dword v213, v212, s[30:31] nt\n"
      "buffer_load_dword v214, off, s[24:27], s78 nt\n"
      "s_swappc_b64 s[84:85], s[84:85]\n"
      "SM1_%=:\n"
      "s_add_u32 s66, s66, s76\n"
      "s_max_u32 s88, s66, s65\n"
      "s_min_i32 s70, s70, 150\n"
      "s_branch SM2_%=\n"
      "SM0_%=:\n"
      "s_or_b32 s39, s39, 0xc0000000\n"
      "s_mov_b32 s52, 0xaaaaaaaa\n"
      "s_mov_b32 s53, 0xaaaaaaaa\n"
      "s_add_u32 s66, s66, s76\n"
      "s_max_u32 s88, s66, s65\n"
      "s_min_i32 s70, s70, 150\n"
      "s_mov_b32 s86, 59\n"
      "s_mov_b32 s77, 177\n"
      "s_swappc_b64 s[84:85], s[84:85]\n"
      "s_mov_b32 s86, 57\n"
      "s_mov_b32 s77, 171\n"
      "s_swappc_b64 s[84:85], s[84:85]\n"
      "SM2_%=:\n"
      "s_mov_b32 s86, 55\n"
      "s_mov_b32 s77, 165\n"
      "s_mov_b32 s52, 0x0\n"
      "s_mov_b32 s53, 0x80000\n"
      "s_swappc_b64 s[84:85], s[84:85]\n"
      "s_getpc_b64 s[74:75]\n"
      "PG_%=:\n"
      "s_sub_u32 s74, s74, PG_%=-END_%=\n"
      "s_subb_u32 s75, s75, 0\n"
      "s_lshl_b32 s76, %[blk], 7\n"
      "s_and_b32 s76, s76, 0x1c00\n"
      "s_min_u32 s76, s76, ENDE_%=-END_%=-1024\n"
      "v_add_u32 v200, s76, v241\n"
      "global_load_dwordx4 v[200:203], v200, s[74:75]\n"
      "v_mov_b32 v212, 0\n"
      "global_load_dword v213, v212, s[30:31] nt\n"
      "buffer_load_dword v214, off, s[24:27], s78 nt\n"
      "s_swappc_b64 s[84:85], s[84:85]\n"
      "GW0_%=:\n"
      "s_memrealtime s[68:69]\n"
      "s_waitcnt lgkmcnt(0)\n"
      "s_sub_u32 s67, s68, s88\n"
      "s_cmp_ge_i32 s67, 0\n"
      "s_cbranch_scc1 GX0_%=\n"
      "s_branch GW0_%=\n"
      "FS1_%=:\n"
      "s_call_b64 s[96:97], FB1_%=\n"
      "s_branch FR1_%=\n"
      "FS2_%=:\n"
      "s_call_b64 s[96:97], FB0_%=\n"
      "s_branch FR2_%=\n"
      "FS3_%=:\n"
      "s_call_b64 s[96:97], FB0_%=\n"
      "s_branch FR3_%=\n"
      "GW4_%=:\n"
      "s_memrealtime s[68:69]\n"
      "s_waitcnt lgkmcnt(0)\n"
      "s_sub_u32 s67, s68, s88\n"
      "s_cmp_ge_i32 s67, 0\n"
      "s_cbranch_scc1 GX4_%=\n"
      "s_branch GW4_%=\n"
      "GW5_%=:\n"
      "s_memrealtime s[68:69]\n"
      "s_waitcnt lgkmcnt(0)\n"
      "s_sub_u32 s67, s68, s88\n"
      "s_cmp_ge_i32 s67, 0\n"
      "s_cbranch_scc1 GX5_%=\n"
      "s_branch GW5_%=\n"
      "GW6_%=:\n"
      "s_memrealtime s[68:69]\n"
      "s_waitcnt lgkmcnt(0)\n"
      "s_sub_u32 s67, s68, s88\n"
      "s_cmp_ge_i32 s67, 0\n"
      "s_cbranch_scc1 GX6_%=\n"
      "s_branch GW6_%=\n"
      "GW7_%=:\n"
      "s_memrealtime s[68:69]\n"
      "s_waitcnt lgkmcnt(0)\n"
      "s_sub_u32 s67, s68, s88\n"
      "s_cmp_ge_i32 s67, 0\n"
      "s_cbranch_scc1 GX7_%=\n"
      "s_branch GW7_%=\n"
      "FS8_%=:\n"
      "s_call_b64 s[96:97], FB1_%=\n"
      "s_branch FR8_%=\n"
      "LAG_%=:\n"
      "s_memrealtime s[68:69]\n"
      "s_waitcnt lgkmcnt(0)\n"
      "s_mov_b32 s88, s68\n"
      "LG1_%=:\n"
      "v_cmp_ne_u32 vcc, s40, v207\n"
      "s_cbranch_vccz LG2_%=\n"
      "s_sub_u32 s63, s63, 1\n"
      "s_cmp_eq_u32 s63, 0\n"
      "s_cbranch_scc1 READ_%=\n"
      "s_sleep 4\n"
      "global_load_dwordx2 v[206:207], v239, s[46:47] offset:8 sc1\n"
      "s_waitcnt vmcnt(0)\n"
      "s_branch LG1_%=\n"
      "LG2_%=:\n"
      "v_readfirstlane_b32 s67, v206\n"
      "s_sub_u32 s88, s88, s82\n"
      "s_min_u32 s88, s88, 400\n"
      "s_add_u32 s88, s88, s67\n"
      "LG3_%=:\n"
      "s_memrealtime s[68:69]\n"
      "s_waitcnt lgkmcnt(0)\n"
      "s_sub_u32 s67, s88, s68\n"
      "s_sub_u32 s67, s67, 11\n"
      "s_cmp_lt_u32 s67, 400\n"
      "s_cbranch_scc1 LG3_%=\n"
      "s_branch READ_%=\n"
      "RETRY_%=:\n"
      "s_sub_u32 s63, s63, 1\n"
      "s_cbranch_scc1 GOT_%=\n"
      "buffer_load_dwordx2 v[160:161], v240, s[24:27], s76 offen sc1\n"
      "buffer_load_dwordx4 v[96:99], v241, s[24:27], s76 offen offset:0 sc1\n"
      "buffer_load_dwordx4 v[100:103], v241, s[24:27], s76 offen offset:1024 sc1\n"
      "buffer_load_dwordx4 v[104:107], v241, s[24:27], s76 offen offset:2048 sc1\n"
      "buffer_load_dwordx4 v[108:111], v241, s[24:27], s76 offen offset:3072 sc1\n"
      "s_branch TRY_%=\n"
      "FS11_%=:\n"
      "s_call_b64 s[96:97], FB0_%=\n"
      "s_branch FR11_%=\n"
      "COLD_%=:\n"
      "FB0_%=:\n"
      "v_mov_b32 v196, 139248\n"
      "s_nop 7\n"
      "ds_write_b32 v196, v196\n"
      "v_max3_f32 v188, v64, v65, 0\n"
      "v_max3_f32 v189, v72, v73, 0\n"
      "v_max3_f32 v190, v80, v81, 0\n"
      "v_max3_f32 v191, v88, v89, 0\n"
      "v_max3_f32 v188, v188, v66, v67\n"
      "v_max3_f32 v189, v189, v74, v75\n"
      "v_max3_f32 v190, v190, v82, v83\n"
      "v_max3_f32 v191, v191, v90, v91\n"
      "v_max3_f32 v188, v188, v68, v69\n"
      "v_max3_f32 v189, v189, v76, v77\n"
      "v_max3_f32 v190, v190, v84, v85\n"
      "v_max3_f32 v191, v191, v92, v93\n"
      "v_max3_f32 v188, v188, v70, v71\n"
      "v_max3_f32 v189, v189, v78, v79\n"
      "v_max3_f32 v190, v190, v86, v87\n"
      "v_max3_f32 v191, v191, v94, v95\n"
      "v_mov_b32 v192, v188\n"
      "v_mov_b32 v193, v189\n"
      "v_mov_b32 v194, v190\n"
      "v_mov_b32 v195, v191\n"
      "v_permlane16_swap_b32 v188, v192\n"
      "v_permlane16_swap_b32 v189, v193\n"
      "v_permlane16_swap_b32 v190, v194\n"
      "v_permlane16_swap_b32 v191, v195\n"
      "v_max_f32 v188, v188, v192\n"
      "v_max_f32 v189, v189, v193\n"
      "v_max_f32 v190, v190, v194\n"
      "v_max_f32 v191, v191, v195\n"
      "v_mov_b32 v192, v188\n"
      "v_mov_b32 v193, v189\n"
      "v_mov_b32 v194, v190\n"
      "v_mov_b32 v195, v191\n"
      "v_permlane32_swap_b32 v188, v192\n"
      "v_permlane32_swap_b32 v189, v193\n"
      "v_permlane32_swap_b32 v190, v194\n"
      "v_permlane32_swap_b32 v191, v195\n"
      "v_max_f32 v188, v188, v192\n"
      "v_max_f32 v189, v189, v193\n"
      "v_max_f32 v190, v190, v194\n"
      "v_max_f32 v191, v191, v195\n"
      "RS0_%=:\n"
      "v_exp_f32 v192, -v188\n"
      "v_exp_f32 v193, -v189\n"
      "v_exp_f32 v194, -v190\n"
      "v_exp_f32 v195, -v191\n"
      "v_sub_f32 v176, v176, v188\n"
      "v_sub_f32 v177, v177, v189\n"
      "v_sub_f32 v178, v178, v190\n"
      "v_sub_f32 v179, v179, v191\n"
      "v_mov_b32 v160, v176\n"
      "v_mov_b32 v161, v176\n"
      "v_mov_b32 v162, v176\n"
      "v_mov_b32 v163, v176\n"
      "v_mov_b32 v164, v177\n"
      "v_mov_b32 v165, v177\n"
      "v_mov_b32 v166, v177\n"
      "v_mov_b32 v167, v177\n"
      "v_mov_b32 v168, v178\n"
      "v_mov_b32 v169, v178\n"
      "v_mov_b32 v170, v178\n"
      "v_mov_b32 v171, v178\n"
      "v_mov_b32 v172, v179\n"
      "v_mov_b32 v173, v179\n"
      "v_mov_b32 v174, v179\n"
      "v_mov_b32 v175, v179\n"
      "v_sub_f32 v64, v64, v188\n"
      "v_sub_f32 v65, v65, v188\n"
      "v_sub_f32 v66, v66, v188\n"
      "v_sub_f32 v67, v67, v188\n"
      "v_sub_f32 v68, v68, v188\n"
      "v_sub_f32 v69, v69, v188\n"
      "v_sub_f32 v70, v70, v188\n"
      "v_sub_f32 v71, v71, v188\n"
      "v_sub_f32 v72, v72, v189\n"
      "v_sub_f32 v73, v73, v189\n"
      "v_sub_f32 v74, v74, v189\n"
      "v_sub_f32 v75, v75, v189\n"
      "v_sub_f32 v76, v76, v189\n"
      "v_sub_f32 v77, v77, v189\n"
      "v_sub_f32 v78, v78, v189\n"
      "v_sub_f32 v79, v79, v189\n"
      "v_sub_f32 v80, v80, v190\n"
      "v_sub_f32 v81, v81, v190\n"
      "v_sub_f32 v82, v82, v190\n"
      "v_sub_f32 v83, v83, v190\n"
      "v_sub_f32 v84, v84, v190\n"
      "v_sub_f32 v85, v85, v190\n"
      "v_sub_f32 v86, v86, v190\n"
      "v_sub_f32 v87, v87, v190\n"
      "v_sub_f32 v88, v88, v191\n"
      "v_sub_f32 v89, v89, v191\n"
      "v_sub_f32 v90, v90, v191\n"
      "v_sub_f32 v91, v91, v191\n"
      "v_sub_f32 v92, v92, v191\n"
      "v_sub_f32 v93, v93, v191\n"
      "v_sub_f32 v94, v94, v191\n"
      "v_sub_f32 v95, v95, v191\n"
      "v_sub_f32 v96, v96, v188\n"
      "v_sub_f32 v97, v97, v188\n"
      "v_sub_f32 v98, v98, v188\n"
      "v_sub_f32 v99, v99, v188\n"
      "v_sub_f32 v100, v100, v188\n"
      "v_sub_f32 v101, v101, v188\n"
      "v_sub_f32 v102, v102, v188\n"
      "v_sub_f32 v103, v103, v188\n"
      "v_sub_f32 v104, v104, v189\n"
      "v_sub_f32 v105, v105, v189\n"
      "v_sub_f32 v106, v106, v189\n"
      "v_sub_f32 v107, v107, v189\n"
      "v_sub_f32 v108, v108, v189\n"
      "v_sub_f32 v109, v109, v189\n"
      "v_sub_f32 v110, v110, v189\n"
      "v_sub_f32 v111, v111, v189\n"
      "v_sub_f32 v112, v112, v190\n"
      "v_sub_f32 v113, v113, v190\n"
      "v_sub_f32 v114, v114, v190\n"
      "v_sub_f32 v115, v115, v190\n"
      "v_sub_f32 v116, v116, v190\n"
      "v_sub_f32 v117, v117, v190\n"
      "v_sub_f32 v118, v118, v190\n"
      "v_sub_f32 v119, v119, v190\n"
      "v_sub_f32 v120, v120, v191\n"
      "v_sub_f32 v121, v121, v191\n"
      "v_sub_f32 v122, v122, v191\n"
      "v_sub_f32 v123, v123, v191\n"
      "v_sub_f32 v124, v124, v191\n"
      "v_sub_f32 v125, v125, v191\n"
      "v_sub_f32 v126, v126, v191\n"
      "v_sub_f32 v127, v127, v191\n"
      "v_mul_f32 v0, v0, v192\n"
      "v_mul_f32 v1, v1, v192\n"
      "v_mul_f32 v2, v2, v192\n"
      "v_mul_f32 v3, v3, v192\n"
      "v_mul_f32 v4, v4, v193\n"
      "v_mul_f32 v5, v5, v193\n"
      "v_mul_f32 v6, v6, v193\n"
      "v_mul_f32 v7, v7, v193\n"
      "v_mul_f32 v8, v8, v192\n"
      "v_mul_f32 v9, v9, v192\n"
      "v_mul_f32 v10, v10, v192\n"
      "v_mul_f32 v11, v11, v192\n"
      "v_mul_f32 v12, v12, v193\n"
      "v_mul_f32 v13, v13, v193\n"
      "v_mul_f32 v14, v14, v193\n"
      "v_mul_f32 v15, v15, v193\n"
      "v_mul_f32 v16, v16, v192\n"
      "v_mul_f32 v17, v17, v192\n"
      "v_mul_f32 v18, v18, v192\n"
      "v_mul_f32 v19, v19, v192\n"
      "v_mul_f32 v20, v20, v193\n"
      "v_mul_f32 v21, v21, v193\n"
      "v_mul_f32 v22, v22, v193\n"
      "v_mul_f32 v23, v23, v193\n"
      "v_mul_f32 v24, v24, v192\n"
      "v_mul_f32 v25, v25, v192\n"
      "v_mul_f32 v26, v26, v192\n"
      "v_mul_f32 v27, v27, v192\n"
      "v_mul_f32 v28, v28, v193\n"
      "v_mul_f32 v29, v29, v193\n"
      "v_mul_f32 v30, v30, v193\n"
      "v_mul_f32 v31, v31, v193\n"
      "v_mul_f32 v32, v32, v192\n"
      "v_mul_f32 v33, v33, v192\n"
      "v_mul_f32 v34, v34, v192\n"
      "v_mul_f32 v35, v35, v192\n"
      "v_mul_f32 v36, v36, v193\n"
      "v_mul_f32 v37, v37, v193\n"
      "v_mul_f32 v38, v38, v193\n"
      "v_mul_f32 v39, v39, v193\n"
      "v_mul_f32 v40, v40, v192\n"
      "v_mul_f32 v41, v41, v192\n"
      "v_mul_f32 v42, v42, v192\n"
      "v_mul_f32 v43, v43, v192\n"
      "v_mul_f32 v44, v44, v193\n"
      "v_mul_f32 v45, v45, v193\n"
      "v_mul_f32 v46, v46, v193\n"
      "v_mul_f32 v47, v47, v193\n"
      "v_mul_f32 v48, v48, v192\n"
      "v_mul_f32 v49, v49, v192\n"
      "v_mul_f32 v50, v50, v192\n"
      "v_mul_f32 v51, v51, v192\n"
      "v_mul_f32 v52, v52, v193\n"
      "v_mul_f32 v53, v53, v193\n"
      "v_mul_f32 v54, v54, v193\n"
      "v_mul_f32 v55, v55, v193\n"
      "v_mul_f32 v56, v56, v192\n"
      "v_mul_f32 v57, v57, v192\n"
      "v_mul_f32 v58, v58, v192\n"
      "v_mul_f32 v59, v59, v192\n"
      "v_mul_f32 v60, v60, v193\n"
      "v_mul_f32 v61, v61, v193\n"
      "v_mul_f32 v62, v62, v193\n"
      "v_mul_f32 v63, v63, v193\n"
      "v_accvgpr_read_b32 v196, a144\n"
      "v_accvgpr_read_b32 v197, a145\n"
      "v_accvgpr_read_b32 v198, a146\n"
      "v_accvgpr_read_b32 v199, a147\n"
      "v_mul_f32 v196, v196, v194\n"
      "v_mul_f32 v197, v197, v194\n"
      "v_mul_f32 v198, v198, v194\n"
      "v_mul_f32 v199, v199, v194\n"
      "v_accvgpr_write_b32 a144, v196\n"
      "v_accvgpr_write_b32 a145, v197\n"
      "v_accvgpr_write_b32 a146, v198\n"
      "v_accvgpr_write_b32 a147, v199\n"
      "v_accvgpr_read_b32 v196, a148\n"
      "v_accvgpr_read_b32 v197, a149\n"
      "v_accvgpr_read_b32 v198, a150\n"
      "v_accvgpr_read_b32 v199, a151\n"
      "v_mul_f32 v196, v196, v195\n"
      "v_mul_f32 v197, v197, v195\n"
      "v_mul_f32 v198, v198, v195\n"
      "v_mul_f32 v199, v199, v195\n"
      "v_accvgpr_write_b32 a148, v196\n"
      "v_accvgpr_write_b32 a149, v197\n"
      "v_accvgpr_write_b32 a150, v198\n"
      "v_accvgpr_write_b32 a151, v199\n"
      "v_accvgpr_read_b32 v196, a152\n"
      "v_accvgpr_read_b32 v197, a153\n"
      "v_accvgpr_read_b32 v198, a154\n"
      "v_accvgpr_read_b32 v199, a155\n"
      "v_mul_f32 v196, v196, v194\n"
      "v_mul_f32 v197, v197, v194\n"
      "v_mul_f32 v198, v198, v194\n"
      "v_mul_f32 v199, v199, v194\n"
      "v_accvgpr_write_b32 a152, v196\n"
      "v_accvgpr_write_b32 a153, v197\n"
      "v_accvgpr_write_b32 a154, v198\n"
      "v_accvgpr_write_b32 a155, v199\n"
      "v_accvgpr_read_b32 v196, a156\n"
      "v_accvgpr_read_b32 v197, a157\n"
      "v_accvgpr_read_b32 v198, a158\n"
      "v_accvgpr_read_b32 v199, a159\n"
      "v_mul_f32 v196, v196, v195\n"
      "v_mul_f32 v197, v197, v195\n"
      "v_mul_f32 v198, v198, v195\n"
      "v_mul_f32 v199, v199, v195\n"
      "v_accvgpr_write_b32 a156, v196\n"
      "v_accvgpr_write_b32 a157, v197\n"
      "v_accvgpr_write_b32 a158, v198\n"
      "v_accvgpr_write_b32 a159, v199\n"
      "v_accvgpr_read_b32 v196, a160\n"
      "v_accvgpr_read_b32 v197, a161\n"
      "v_accvgpr_read_b32 v198, a162\n"
      "v_accvgpr_read_b32 v199, a163\n"
      "v_mul_f32 v196, v196, v194\n"
      "v_mul_f32 v197, v197, v194\n"
      "v_mul_f32 v198, v198, v194\n"
      "v_mul_f32 v199, v199, v194\n"
      "v_accvgpr_write_b32 a160, v196\n"
      "v_accvgpr_write_b32 a161, v197\n"
      "v_accvgpr_write_b32 a162, v198\n"
      "v_accvgpr_write_b32 a163, v199\n"
      "v_accvgpr_read_b32 v196, a164\n"
      "v_accvgpr_read_b32 v197, a165\n"
      "v_accvgpr_read_b32 v198, a166\n"
      "v_accvgpr_read_b32 v199, a167\n"
      "v_mul_f32 v196, v196, v195\n"
      "v_mul_f32 v197, v197, v195\n"
      "v_mul_f32 v198, v198, v195\n"
      "v_mul_f32 v199, v199, v195\n"
      "v_accvgpr_write_b32 a164, v196\n"
      "v_accvgpr_write_b32 a165, v197\n"
      "v_accvgpr_write_b32 a166, v198\n"
      "v_accvgpr_write_b32 a167, v199\n"
      "v_accvgpr_read_b32 v196, a168\n"
      "v_accvgpr_read_b32 v197, a169\n"
      "v_accvgpr_read_b32 v198, a170\n"
      "v_accvgpr_read_b32 v199, a171\n"
      "v_mul_f32 v196, v196, v194\n"
      "v_mul_f32 v197, v197, v194\n"
      "v_mul_f32 v198, v198, v194\n"
      "v_mul_f32 v199, v199, v194\n"
      "v_accvgpr_write_b32 a168, v196\n"
      "v_accvgpr_write_b32 a169, v197\n"
      "v_accvgpr_write_b32 a170, v198\n"
      "v_accvgpr_write_b32 a171, v199\n"
      "v_accvgpr_read_b32 v196, a172\n"
      "v_accvgpr_read_b32 v197, a173\n"
      "v_accvgpr_read_b32 v198, a174\n"
      "v_accvgpr_read_b32 v199, a175\n"
      "v_mul_f32 v196, v196, v195\n"
      "v_mul_f32 v197, v197, v195\n"
      "v_mul_f32 v198, v198, v195\n"
      "v_mul_f32 v199, v199, v195\n"
      "v_accvgpr_write_b32 a172, v196\n"
      "v_accvgpr_write_b32 a173, v197\n"
      "v_accvgpr_write_b32 a174, v198\n"
      "v_accvgpr_write_b32 a175, v199\n"
      "v_accvgpr_read_b32 v196, a176\n"
      "v_accvgpr_read_b32 v197, a177\n"
      "v_accvgpr_read_b32 v198, a178\n"
      "v_accvgpr_read_b32 v199, a179\n"
      "v_mul_f32 v196, v196, v194\n"
      "v_mul_f32 v197, v197, v194\n"
      "v_mul_f32 v198, v198, v194\n"
      "v_mul_f32 v199, v199, v194\n"
      "v_accvgpr_write_b32 a176, v196\n"
      "v_accvgpr_write_b32 a177, v197\n"
      "v_accvgpr_write_b32 a178, v198\n"
      "v_accvgpr_write_b32 a179, v199\n"
      "v_accvgpr_read_b32 v196, a180\n"
      "v_accvgpr_read_b32 v197, a181\n"
      "v_accvgpr_read_b32 v198, a182\n"
      "v_accvgpr_read_b32 v199, a183\n"
      "v_mul_f32 v196, v196, v195\n"
      "v_mul_f32 v197, v197, v195\n"
      "v_mul_f32 v198, v198, v195\n"
      "v_mul_f32 v199, v199, v195\n"
      "v_accvgpr_write_b32 a180, v196\n"
      "v_accvgpr_write_b32 a181, v197\n"
      "v_accvgpr_write_b32 a182, v198\n"
      "v_accvgpr_write_b32 a183, v199\n"
      "v_accvgpr_read_b32 v196, a184\n"
      "v_accvgpr_read_b32 v197, a185\n"
      "v_accvgpr_read_b32 v198, a186\n"
      "v_accvgpr_read_b32 v199, a187\n"
      "v_mul_f32 v196, v196, v194\n"
      "v_mul_f32 v197, v197, v194\n"
      "v_mul_f32 v198, v198, v194\n"
      "v_mul_f32 v199, v199, v194\n"
      "v_accvgpr_write_b32 a184, v196\n"
      "v_accvgpr_write_b32 a185, v197\n"
      "v_accvgpr_write_b32 a186, v198\n"
      "v_accvgpr_write_b32 a187, v199\n"
      "v_accvgpr_read_b32 v196, a188\n"
      "v_accvgpr_read_b32 v197, a189\n"
      "v_accvgpr_read_b32 v198, a190\n"
      "v_accvgpr_read_b32 v199, a191\n"
      "v_mul_f32 v196, v196, v195\n"
      "v_mul_f32 v197, v197, v195\n"
      "v_mul_f32 v198, v198, v195\n"
      "v_mul_f32 v199, v199, v195\n"
      "v_accvgpr_write_b32 a188, v196\n"
      "v_accvgpr_write_b32 a189, v197\n"
      "v_accvgpr_write_b32 a190, v198\n"
      "v_accvgpr_write_b32 a191, v199\n"
      "v_accvgpr_read_b32 v196, a192\n"
      "v_accvgpr_read_b32 v197, a193\n"
      "v_accvgpr_read_b32 v198, a194\n"
      "v_accvgpr_read_b32 v199, a195\n"
      "v_mul_f32 v196, v196, v194\n"
      "v_mul_f32 v197, v197, v194\n"
      "v_mul_f32 v198, v198, v194\n"
      "v_mul_f32 v199, v199, v194\n"
      "v_accvgpr_write_b32 a192, v196\n"
      "v_accvgpr_write_b32 a193, v197\n"
      "v_accvgpr_write_b32 a194, v198\n"
      "v_accvgpr_write_b32 a195, v199\n"
      "v_accvgpr_read_b32 v196, a196\n"
      "v_accvgpr_read_b32 v197, a197\n"
      "v_accvgpr_read_b32 v198, a198\n"
      "v_accvgpr_read_b32 v199, a199\n"
      "v_mul_f32 v196, v196, v195\n"
      "v_mul_f32 v197, v197, v195\n"
      "v_mul_f32 v198, v198, v195\n"
      "v_mul_f32 v199, v199, v195\n"
      "v_accvgpr_write_b32 a196, v196\n"
      "v_accvgpr_write_b32 a197, v197\n"
      "v_accvgpr_write_b32 a198, v198\n"
      "v_accvgpr_write_b32 a199, v199\n"
      "v_accvgpr_read_b32 v196, a200\n"
      "v_accvgpr_read_b32 v197, a201\n"
      "v_accvgpr_read_b32 v198, a202\n"
      "v_accvgpr_read_b32 v199, a203\n"
      "v_mul_f32 v196, v196, v194\n"
      "v_mul_f32 v197, v197, v194\n"
      "v_mul_f32 v198, v198, v194\n"
      "v_mul_f32 v199, v199, v194\n"
      "v_accvgpr_write_b32 a200, v196\n"
      "v_accvgpr_write_b32 a201, v197\n"
      "v_accvgpr_write_b32 a202, v198\n"
      "v_accvgpr_write_b32 a203, v199\n"
      "v_accvgpr_read_b32 v196, a204\n"
      "v_accvgpr_read_b32 v197, a205\n"
      "v_accvgpr_read_b32 v198, a206\n"
      "v_accvgpr_read_b32 v199, a207\n"
      "v_mul_f32 v196, v196, v195\n"
      "v_mul_f32 v197, v197, v195\n"
      "v_mul_f32 v198, v198, v195\n"
      "v_mul_f32 v199, v199, v195\n"
      "v_accvgpr_write_b32 a204, v196\n"
      "v_accvgpr_write_b32 a205, v197\n"
      "v_accvgpr_write_b32 a206, v198\n"
      "v_accvgpr_write_b32 a207, v199\n"
      "v_mul_f32 v180, v180, v192\n"
      "v_mul_f32 v181, v181, v193\n"
      "v_mul_f32 v182, v182, v194\n"
      "v_mul_f32 v183, v183, v195\n"
      "v_accvgpr_read_b32 v197, a208\n"
      "v_lshlrev_b32 v196, 16, v197\n"
      "v_and_b32 v197, 0xffff0000, v197\n"
      "v_mul_f32 v196, v196, v192\n"
      "v_mul_f32 v197, v197, v192\n"
      "v_cvt_pk_bf16_f32 v196, v196, v197\n"
      "v_accvgpr_write_b32 a208, v196\n"
      "v_accvgpr_read_b32 v197, a209\n"
      "v_lshlrev_b32 v196, 16, v197\n"
      "v_and_b32 v197, 0xffff0000, v197\n"
      "v_mul_f32 v196, v196, v192\n"
      "v_mul_f32 v197, v197, v192\n"
      "v_cvt_pk_bf16_f32 v196, v196, v197\n"
      "v_accvgpr_write_b32 a209, v196\n"
      "v_accvgpr_read_b32 v197, a210\n"
      "v_lshlrev_b32 v196, 16, v197\n"
      "v_and_b32 v197, 0xffff0000, v197\n"
      "v_mul_f32 v196, v196, v192\n"
      "v_mul_f32 v197, v197, v192\n"
      "v_cvt_pk_bf16_f32 v196, v196, v197\n"
      "v_accvgpr_write_b32 a210, v196\n"
      "v_accvgpr_read_b32 v197, a211\n"
      "v_lshlrev_b32 v196, 16, v197\n"
      "v_and_b32 v197, 0xffff0000, v197\n"
      "v_mul_f32 v196, v196, v192\n"
      "v_mul_f32 v197, v197, v192\n"
      "v_cvt_pk_bf16_f32 v196, v196, v197\n"
      "v_accvgpr_write_b32 a211, v196\n"
      "v_accvgpr_read_b32 v197, a212\n"
      "v_lshlrev_b32 v196, 16, v197\n"
      "v_and_b32 v197, 0xffff0000, v197\n"
      "v_mul_f32 v196, v196, v193\n"
      "v_mul_f32 v197, v197, v193\n"
      "v_cvt_pk_bf16_f32 v196, v196, v197\n"
      "v_accvgpr_write_b32 a212, v196\n"
      "v_accvgpr_read_b32 v197, a213\n"
      "v_lshlrev_b32 v196, 16, v197\n"
      "v_and_b32 v197, 0xffff0000, v197\n"
      "v_mul_f32 v196, v196, v193\n"
      "v_mul_f32 v197, v197, v193\n"
      "v_cvt_pk_bf16_f32 v196, v196, v197\n"
      "v_accvgpr_write_b32 a213, v196\n"
      "v_accvgpr_read_b32 v197, a214\n"
      "v_lshlrev_b32 v196, 16, v197\n"
      "v_and_b32 v197, 0xffff0000, v197\n"
      "v_mul_f32 v196, v196, v193\n"
      "v_mul_f32 v197, v197, v193\n"
      "v_cvt_pk_bf16_f32 v196, v196, v197\n"
      "v_accvgpr_write_b32 a214, v196\n"
      "v_accvgpr_read_b32 v197, a215\n"
      "v_lshlrev_b32 v196, 16, v197\n"
      "v_and_b32 v197, 0xffff0000, v197\n"
      "v_mul_f32 v196, v196, v193\n"
      "v_mul_f32 v197, v197, v193\n"
      "v_cvt_pk_bf16_f32 v196, v196, v197\n"
      "v_accvgpr_write_b32 a215, v196\n"
      "v_accvgpr_read_b32 v197, a216\n"
      "v_lshlrev_b32 v196, 16, v197\n"
      "v_and_b32 v197, 0xffff0000, v197\n"
      "v_mul_f32 v196, v196, v194\n"
      "v_mul_f32 v197, v197, v194\n"
      "v_cvt_pk_bf16_f32 v196, v196, v197\n"
      "v_accvgpr_write_b32 a216, v196\n"
      "v_accvgpr_read_b32 v197, a217\n"
      "v_lshlrev_b32 v196, 16, v197\n"
      "v_and_b32 v197, 0xffff0000, v197\n"
      "v_mul_f32 v196, v196, v194\n"
      "v_mul_f32 v197, v197, v194\n"
      "v_cvt_pk_bf16_f32 v196, v196, v197\n"
      "v_accvgpr_write_b32 a217, v196\n"
      "v_accvgpr_read_b32 v197, a218\n"
      "v_lshlrev_b32 v196, 16, v197\n"
      "v_and_b32 v197, 0xffff0000, v197\n"
      "v_mul_f32 v196, v196, v194\n"
      "v_mul_f32 v197, v197, v194\n"
      "v_cvt_pk_bf16_f32 v196, v196, v197\n"
      "v_accvgpr_write_b32 a218, v196\n"
      "v_accvgpr_read_b32 v197, a219\n"
      "v_lshlrev_b32 v196, 16, v197\n"
      "v_and_b32 v197, 0xffff0000, v197\n"
      "v_mul_f32 v196, v196, v194\n"
      "v_mul_f32 v197, v197, v194\n"
      "v_cvt_pk_bf16_f32 v196, v196, v197\n"
      "v_accvgpr_write_b32 a219, v196\n"
      "v_accvgpr_read_b32 v197, a220\n"
      "v_lshlrev_b32 v196, 16, v197\n"
      "v_and_b32 v197, 0xffff0000, v197\n"
      "v_mul_f32 v196, v196, v195\n"
      "v_mul_f32 v197, v197, v195\n"
      "v_cvt_pk_bf16_f32 v196, v196, v197\n"
      "v_accvgpr_write_b32 a220, v196\n"
      "v_accvgpr_read_b32 v197, a221\n"
      "v_lshlrev_b32 v196, 16, v197\n"
      "v_and_b32 v197, 0xffff0000, v197\n"
      "v_mul_f32 v196, v196, v195\n"
      "v_mul_f32 v197, v197, v195\n"
      "v_cvt_pk_bf16_f32 v196, v196, v197\n"
      "v_accvgpr_write_b32 a221, v196\n"
      "v_accvgpr_read_b32 v197, a222\n"
      "v_lshlrev_b32 v196, 16, v197\n"
      "v_and_b32 v197, 0xffff0000, v197\n"
      "v_mul_f32 v196, v196, v195\n"
      "v_mul_f32 v197, v197, v195\n"
      "v_cvt_pk_bf16_f32 v196, v196, v197\n"
      "v_accvgpr_write_b32 a222, v196\n"
      "v_accvgpr_read_b32 v197, a223\n"
      "v_lshlrev_b32 v196, 16, v197\n"
      "v_and_b32 v197, 0xffff0000, v197\n"
      "v_mul_f32 v196, v196, v195\n"
      "v_mul_f32 v197, v197, v195\n"
      "v_cvt_pk_bf16_f32 v196, v196, v197\n"
      "v_accvgpr_write_b32 a223, v196\n"
      "v_exp_f32 v188, v64\n"
      "v_exp_f32 v189, v65\n"
      "v_exp_f32 v190, v66\n"
      "v_exp_f32 v191, v67\n"
      "v_cvt_pk_bf16_f32 v128, v188, v189\n"
      "v_cvt_pk_bf16_f32 v129, v190, v191\n"
      "v_exp_f32 v192, v68\n"
      "v_exp_f32 v193, v69\n"
      "v_exp_f32 v194, v70\n"
      "v_exp_f32 v195, v71\n"
      "v_cvt_pk_bf16_f32 v130, v192, v193\n"
      "v_cvt_pk_bf16_f32 v131, v194, v195\n"
      "v_exp_f32 v188, v72\n"
      "v_exp_f32 v189, v73\n"
      "v_exp_f32 v190, v74\n"
      "v_exp_f32 v191, v75\n"
      "v_cvt_pk_bf16_f32 v132, v188, v189\n"
      "v_cvt_pk_bf16_f32 v133, v190, v191\n"
      "v_exp_f32 v192, v76\n"
      "v_exp_f32 v193, v77\n"
      "v_exp_f32 v194, v78\n"
      "v_exp_f32 v195, v79\n"
      "v_cvt_pk_bf16_f32 v134, v192, v193\n"
      "v_cvt_pk_bf16_f32 v135, v194, v195\n"
      "v_exp_f32 v188, v80\n"
      "v_exp_f32 v189, v81\n"
      "v_exp_f32 v190, v82\n"
      "v_exp_f32 v191, v83\n"
      "v_cvt_pk_bf16_f32 v136, v188, v189\n"
      "v_cvt_pk_bf16_f32 v137, v190, v191\n"
      "v_exp_f32 v192, v84\n"
      "v_exp_f32 v193, v85\n"
      "v_exp_f32 v194, v86\n"
      "v_exp_f32 v195, v87\n"
      "v_cvt_pk_bf16_f32 v138, v192, v193\n"
      "v_cvt_pk_bf16_f32 v139, v194, v195\n"
      "v_exp_f32 v188, v88\n"
      "v_exp_f32 v189, v89\n"
      "v_exp_f32 v190, v90\n"
      "v_exp_f32 v191, v91\n"
      "v_cvt_pk_bf16_f32 v140, v188, v189\n"
      "v_cvt_pk_bf16_f32 v141, v190, v191\n"
      "v_exp_f32 v192, v92\n"
      "v_exp_f32 v193, v93\n"
      "v_exp_f32 v194, v94\n"
      "v_exp_f32 v195, v95\n"
      "v_cvt_pk_bf16_f32 v142, v192, v193\n"
      "v_cvt_pk_bf16_f32 v143, v194, v195\n"
      "s_nop 1\n"
      "v_mfma_f32_16x16x32_bf16 v[184:187], a[128:131], v[128:131], 0\n"
      "v_mfma_f32_16x16x32_bf16 v[184:187], a[132:135], v[132:135], v[184:187]\n"
      "v_mfma_f32_16x16x32_bf16 v[184:187], a[136:139], v[136:139], v[184:187]\n"
      "v_mfma_f32_16x16x32_bf16 v[184:187], a[140:143], v[140:143], v[184:187]\n"
      "s_nop 7\n"
      "s_setpc_b64 s[96:97]\n"
      "FB1_%=:\n"
      "v_mov_b32 v196, 139248\n"
      "s_nop 7\n"
      "ds_write_b32 v196, v196\n"
      "v_max3_f32 v188, v96, v97, 0\n"
      "v_max3_f32 v189, v104, v105, 0\n"
      "v_max3_f32 v190, v112, v113, 0\n"
      "v_max3_f32 v191, v120, v121, 0\n"
      "v_max3_f32 v188, v188, v98, v99\n"
      "v_max3_f32 v189, v189, v106, v107\n"
      "v_max3_f32 v190, v190, v114, v115\n"
      "v_max3_f32 v191, v191, v122, v123\n"
      "v_max3_f32 v188, v188, v100, v101\n"
      "v_max3_f32 v189, v189, v108, v109\n"
      "v_max3_f32 v190, v190, v116, v117\n"
      "v_max3_f32 v191, v191, v124, v125\n"
      "v_max3_f32 v188, v188, v102, v103\n"
      "v_max3_f32 v189, v189, v110, v111\n"
      "v_max3_f32 v190, v190, v118, v119\n"
      "v_max3_f32 v191, v191, v126, v127\n"
      "v_mov_b32 v192, v188\n"
      "v_mov_b32 v193, v189\n"
      "v_mov_b32 v194, v190\n"
      "v_mov_b32 v195, v191\n"
      "v_permlane16_swap_b32 v188, v192\n"
      "v_permlane16_swap_b32 v189, v193\n"
      "v_permlane16_swap_b32 v190, v194\n"
      "v_permlane16_swap_b32 v191, v195\n"
      "v_max_f32 v188, v188, v192\n"
      "v_max_f32 v189, v189, v193\n"
      "v_max_f32 v190, v190, v194\n"
      "v_max_f32 v191, v191, v195\n"
      "v_mov_b32 v192, v188\n"
      "v_mov_b32 v193, v189\n"
      "v_mov_b32 v194, v190\n"
      "v_mov_b32 v195, v191\n"
      "v_permlane32_swap_b32 v188, v192\n"
      "v_permlane32_swap_b32 v189, v193\n"
      "v_permlane32_swap_b32 v190, v194\n"
      "v_permlane32_swap_b32 v191, v195\n"
      "v_max_f32 v188, v188, v192\n"
      "v_max_f32 v189, v189, v193\n"
      "v_max_f32 v190, v190, v194\n"
      "v_max_f32 v191, v191, v195\n"
      "RS1_%=:\n"
      "v_exp_f32 v192, -v188\n"
      "v_exp_f32 v193, -v189\n"
      "v_exp_f32 v194, -v190\n"
      "v_exp_f32 v195, -v191\n"
      "v_sub_f32 v176, v176, v188\n"
      "v_sub_f32 v177, v177, v189\n"
      "v_sub_f32 v178, v178, v190\n"
      "v_sub_f32 v179, v179, v191\n"
      "v_mov_b32 v160, v176\n"
      "v_mov_b32 v161, v176\n"
      "v_mov_b32 v162, v176\n"
      "v_mov_b32 v163, v176\n"
      "v_mov_b32 v164, v177\n"
      "v_mov_b32 v165, v177\n"
      "v_mov_b32 v166, v177\n"
      "v_mov_b32 v167, v177\n"
      "v_mov_b32 v168, v178\n"
      "v_mov_b32 v169, v178\n"
      "v_mov_b32 v170, v178\n"
      "v_mov_b32 v171, v178\n"
      "v_mov_b32 v172, v179\n"
      "v_mov_b32 v173, v179\n"
      "v_mov_b32 v174, v179\n"
      "v_mov_b32 v175, v179\n"
      "v_sub_f32 v64, v64, v188\n"
      "v_sub_f32 v65, v65, v188\n"
      "v_sub_f32 v66, v66, v188\n"
      "v_sub_f32 v67, v67, v188\n"
      "v_sub_f32 v68, v68, v188\n"
      "v_sub_f32 v69, v69, v188\n"
      "v_sub_f32 v70, v70, v188\n"
      "v_sub_f32 v71, v71, v188\n"
      "v_sub_f32 v72, v72, v189\n"
      "v_sub_f32 v73, v73, v189\n"
      "v_sub_f32 v74, v74, v189\n"
      "v_sub_f32 v75, v75, v189\n"
      "v_sub_f32 v76, v76, v189\n"
      "v_sub_f32 v77, v77, v189\n"
      "v_sub_f32 v78, v78, v189\n"
      "v_sub_f32 v79, v79, v189\n"
      "v_sub_f32 v80, v80, v190\n"
      "v_sub_f32 v81, v81, v190\n"
      "v_sub_f32 v82, v82, v190\n"
      "v_sub_f32 v83, v83, v190\n"
      "v_sub_f32 v84, v84, v190\n"
      "v_sub_f32 v85, v85, v190\n"
      "v_sub_f32 v86, v86, v190\n"
      "v_sub_f32 v87, v87, v190\n"
      "v_sub_f32 v88, v88, v191\n"
      "v_sub_f32 v89, v89, v191\n"
      "v_sub_f32 v90, v90, v191\n"
      "v_sub_f32 v91, v91, v191\n"
      "v_sub_f32 v92, v92, v191\n"
      "v_sub_f32 v93, v93, v191\n"
      "v_sub_f32 v94, v94, v191\n"
      "v_sub_f32 v95, v95, v191\n"
      "v_sub_f32 v96, v96, v188\n"
      "v_sub_f32 v97, v97, v188\n"
      "v_sub_f32 v98, v98, v188\n"
      "v_sub_f32 v99, v99, v188\n"
      "v_sub_f32 v100, v100, v188\n"
      "v_sub_f32 v101, v101, v188\n"
      "v_sub_f32 v102, v102, v188\n"
      "v_sub_f32 v103, v103, v188\n"
      "v_sub_f32 v104, v104, v189\n"
      "v_sub_f32 v105, v105, v189\n"
      "v_sub_f32 v106, v106, v189\n"
      "v_sub_f32 v107, v107, v189\n"
      "v_sub_f32 v108, v108, v189\n"
      "v_sub_f32 v109, v109, v189\n"
      "v_sub_f32 v110, v110, v189\n"
      "v_sub_f32 v111, v111, v189\n"
      "v_sub_f32 v112, v112, v190\n"
      "v_sub_f32 v113, v113, v190\n"
      "v_sub_f32 v114, v114, v190\n"
      "v_sub_f32 v115, v115, v190\n"
      "v_sub_f32 v116, v116, v190\n"
      "v_sub_f32 v117, v117, v190\n"
      "v_sub_f32 v118, v118, v190\n"
      "v_sub_f32 v119, v119, v190\n"
      "v_sub_f32 v120, v120, v191\n"
      "v_sub_f32 v121, v121, v191\n"
      "v_sub_f32 v122, v122, v191\n"
      "v_sub_f32 v123, v123, v191\n"
      "v_sub_f32 v124, v124, v191\n"
      "v_sub_f32 v125, v125, v191\n"
      "v_sub_f32 v126, v126, v191\n"
      "v_sub_f32 v127, v127, v191\n"
      "v_mul_f32 v0, v0, v192\n"
      "v_mul_f32 v1, v1, v192\n"
      "v_mul_f32 v2, v2, v192\n"
      "v_mul_f32 v3, v3, v192\n"
      "v_mul_f32 v4, v4, v193\n"
      "v_mul_f32 v5, v5, v193\n"
      "v_mul_f32 v6, v6, v193\n"
      "v_mul_f32 v7, v7, v193\n"
      "v_mul_f32 v8, v8, v192\n"
      "v_mul_f32 v9, v9, v192\n"
      "v_mul_f32 v10, v10, v192\n"
      "v_mul_f32 v11, v11, v192\n"
      "v_mul_f32 v12, v12, v193\n"
      "v_mul_f32 v13, v13, v193\n"
      "v_mul_f32 v14, v14, v193\n"
      "v_mul_f32 v15, v15, v193\n"
      "v_mul_f32 v16, v16, v192\n"
      "v_mul_f32 v17, v17, v192\n"
      "v_mul_f32 v18, v18, v192\n"
      "v_mul_f32 v19, v19, v192\n"
      "v_mul_f32 v20, v20, v193\n"
      "v_mul_f32 v21, v21, v193\n"
      "v_mul_f32 v22, v22, v193\n"
      "v_mul_f32 v23, v23, v193\n"
      "v_mul_f32 v24, v24, v192\n"
      "v_mul_f32 v25, v25, v192\n"
      "v_mul_f32 v26, v26, v192\n"
      "v_mul_f32 v27, v27, v192\n"
      "v_mul_f32 v28, v28, v193\n"
      "v_mul_f32 v29, v29, v193\n"
      "v_mul_f32 v30, v30, v193\n"
      "v_mul_f32 v31, v31, v193\n"
      "v_mul_f32 v32, v32, v192\n"
      "v_mul_f32 v33, v33, v192\n"
      "v_mul_f32 v34, v34, v192\n"
      "v_mul_f32 v35, v35, v192\n"
      "v_mul_f32 v36, v36, v193\n"
      "v_mul_f32 v37, v37, v193\n"
      "v_mul_f32 v38, v38, v193\n"
      "v_mul_f32 v39, v39, v193\n"
      "v_mul_f32 v40, v40, v192\n"
      "v_mul_f32 v41, v41, v192\n"
      "v_mul_f32 v42, v42, v192\n"
      "v_mul_f32 v43, v43, v192\n"
      "v_mul_f32 v44, v44, v193\n"
      "v_mul_f32 v45, v45, v193\n"
      "v_mul_f32 v46, v46, v193\n"
      "v_mul_f32 v47, v47, v193\n"
      "v_mul_f32 v48, v48, v192\n"
      "v_mul_f32 v49, v49, v192\n"
      "v_mul_f32 v50, v50, v192\n"
      "v_mul_f32 v51, v51, v192\n"
      "v_mul_f32 v52, v52, v193\n"
      "v_mul_f32 v53, v53, v193\n"
      "v_mul_f32 v54, v54, v193\n"
      "v_mul_f32 v55, v55, v193\n"
      "v_mul_f32 v56, v56, v192\n"
      "v_mul_f32 v57, v57, v192\n"
      "v_mul_f32 v58, v58, v192\n"
      "v_mul_f32 v59, v59, v192\n"
      "v_mul_f32 v60, v60, v193\n"
      "v_mul_f32 v61, v61, v193\n"
      "v_mul_f32 v62, v62, v193\n"
      "v_mul_f32 v63, v63, v193\n"
      "v_accvgpr_read_b32 v196, a144\n"
      "v_accvgpr_read_b32 v197, a145\n"
      "v_accvgpr_read_b32 v198, a146\n"
      "v_accvgpr_read_b32 v199, a147\n"
      "v_mul_f32 v196, v196, v194\n"
      "v_mul_f32 v197, v197, v194\n"
      "v_mul_f32 v198, v198, v194\n"
      "v_mul_f32 v199, v199, v194\n"
      "v_accvgpr_write_b32 a144, v196\n"
      "v_accvgpr_write_b32 a145, v197\n"
      "v_accvgpr_write_b32 a146, v198\n"
      "v_accvgpr_write_b32 a147, v199\n"
      "v_accvgpr_read_b32 v196, a148\n"
      "v_accvgpr_read_b32 v197, a149\n"
      "v_accvgpr_read_b32 v198, a150\n"
      "v_accvgpr_read_b32 v199, a151\n"
      "v_mul_f32 v196, v196, v195\n"
      "v_mul_f32 v197, v197, v195\n"
      "v_mul_f32 v198, v198, v195\n"
      "v_mul_f32 v199, v199, v195\n"
      "v_accvgpr_write_b32 a148, v196\n"
      "v_accvgpr_write_b32 a149, v197\n"
      "v_accvgpr_write_b32 a150, v198\n"
      "v_accvgpr_write_b32 a151, v199\n"
      "v_accvgpr_read_b32 v196, a152\n"
      "v_accvgpr_read_b32 v197, a153\n"
      "v_accvgpr_read_b32 v198, a154\n"
      "v_accvgpr_read_b32 v199, a155\n"
      "v_mul_f32 v196, v196, v194\n"
      "v_mul_f32 v197, v197, v194\n"
      "v_mul_f32 v198, v198, v194\n"
      "v_mul_f32 v199, v199, v194\n"
      "v_accvgpr_write_b32 a152, v196\n"
      "v_accvgpr_write_b32 a153, v197\n"
      "v_accvgpr_write_b32 a154, v198\n"
      "v_accvgpr_write_b32 a155, v199\n"
      "v_accvgpr_read_b32 v196, a156\n"
      "v_accvgpr_read_b32 v197, a157\n"
      "v_accvgpr_read_b32 v198, a158\n"
      "v_accvgpr_read_b32 v199, a159\n"
      "v_mul_f32 v196, v196, v195\n"
      "v_mul_f32 v197, v197, v195\n"
      "v_mul_f32 v198, v198, v195\n"
      "v_mul_f32 v199, v199, v195\n"
      "v_accvgpr_write_b32 a156, v196\n"
      "v_accvgpr_write_b32 a157, v197\n"
      "v_accvgpr_write_b32 a158, v198\n"
      "v_accvgpr_write_b32 a159, v199\n"
      "v_accvgpr_read_b32 v196, a160\n"
      "v_accvgpr_read_b32 v197, a161\n"
      "v_accvgpr_read_b32 v198, a162\n"
      "v_accvgpr_read_b32 v199, a163\n"
      "v_mul_f32 v196, v196, v194\n"
      "v_mul_f32 v197, v197, v194\n"
      "v_mul_f32 v198, v198, v194\n"
      "v_mul_f32 v199, v199, v194\n"
      "v_accvgpr_write_b32 a160, v196\n"
      "v_accvgpr_write_b32 a161, v197\n"
      "v_accvgpr_write_b32 a162, v198\n"
      "v_accvgpr_write_b32 a163, v199\n"
      "v_accvgpr_read_b32 v196, a164\n"
      "v_accvgpr_read_b32 v197, a165\n"
      "v_accvgpr_read_b32 v198, a166\n"
      "v_accvgpr_read_b32 v199, a167\n"
      "v_mul_f32 v196, v196, v195\n"
      "v_mul_f32 v197, v197, v195\n"
      "v_mul_f32 v198, v198, v195\n"
      "v_mul_f32 v199, v199, v195\n"
      "v_accvgpr_write_b32 a164, v196\n"
      "v_accvgpr_write_b32 a165, v197\n"
      "v_accvgpr_write_b32 a166, v198\n"
      "v_accvgpr_write_b32 a167, v199\n"
      "v_accvgpr_read_b32 v196, a168\n"
      "v_accvgpr_read_b32 v197, a169\n"
      "v_accvgpr_read_b32 v198, a170\n"
      "v_accvgpr_read_b32 v199, a171\n"
      "v_mul_f32 v196, v196, v194\n"
      "v_mul_f32 v197, v197, v194\n"
      "v_mul_f32 v198, v198, v194\n"
      "v_mul_f32 v199, v199, v194\n"
      "v_accvgpr_write_b32 a168, v196\n"
      "v_accvgpr_write_b32 a169, v197\n"
      "v_accvgpr_write_b32 a170, v198\n"
      "v_accvgpr_write_b32 a171, v199\n"
      "v_accvgpr_read_b32 v196, a172\n"
      "v_accvgpr_read_b32 v197, a173\n"
      "v_accvgpr_read_b32 v198, a174\n"
      "v_accvgpr_read_b32 v199, a175\n"
      "v_mul_f32 v196, v196, v195\n"
      "v_mul_f32 v197, v197, v195\n"
      "v_mul_f32 v198, v198, v195\n"
      "v_mul_f32 v199, v199, v195\n"
      "v_accvgpr_write_b32 a172, v196\n"
      "v_accvgpr_write_b32 a173, v197\n"
      "v_accvgpr_write_b32 a174, v198\n"
      "v_accvgpr_write_b32 a175, v199\n"
      "v_accvgpr_read_b32 v196, a176\n"
      "v_accvgpr_read_b32 v197, a177\n"
      "v_accvgpr_read_b32 v198, a178\n"
      "v_accvgpr_read_b32 v199, a179\n"
      "v_mul_f32 v196, v196, v194\n"
      "v_mul_f32 v197, v197, v194\n"
      "v_mul_f32 v198, v198, v194\n"
      "v_mul_f32 v199, v199, v194\n"
      "v_accvgpr_write_b32 a176, v196\n"
      "v_accvgpr_write_b32 a177, v197\n"
      "v_accvgpr_write_b32 a178, v198\n"
      "v_accvgpr_write_b32 a179, v199\n"
      "v_accvgpr_read_b32 v196, a180\n"
      "v_accvgpr_read_b32 v197, a181\n"
      "v_accvgpr_read_b32 v198, a182\n"
      "v_accvgpr_read_b32 v199, a183\n"
      "v_mul_f32 v196, v196, v195\n"
      "v_mul_f32 v197, v197, v195\n"
      "v_mul_f32 v198, v198, v195\n"
      "v_mul_f32 v199, v199, v195\n"
      "v_accvgpr_write_b32 a180, v196\n"
      "v_accvgpr_write_b32 a181, v197\n"
      "v_accvgpr_write_b32 a182, v198\n"
      "v_accvgpr_write_b32 a183, v199\n"
      "v_accvgpr_read_b32 v196, a184\n"
      "v_accvgpr_read_b32 v197, a185\n"
      "v_accvgpr_read_b32 v198, a186\n"
      "v_accvgpr_read_b32 v199, a187\n"
      "v_mul_f32 v196, v196, v194\n"
      "v_mul_f32 v197, v197, v194\n"
      "v_mul_f32 v198, v198, v194\n"
      "v_mul_f32 v199, v199, v194\n"
      "v_accvgpr_write_b32 a184, v196\n"
      "v_accvgpr_write_b32 a185, v197\n"
      "v_accvgpr_write_b32 a186, v198\n"
      "v_accvgpr_write_b32 a187, v199\n"
      "v_accvgpr_read_b32 v196, a188\n"
      "v_accvgpr_read_b32 v197, a189\n"
      "v_accvgpr_read_b32 v198, a190\n"
      "v_accvgpr_read_b32 v199, a191\n"
      "v_mul_f32 v196, v196, v195\n"
      "v_mul_f32 v197, v197, v195\n"
      "v_mul_f32 v198, v198, v195\n"
      "v_mul_f32 v199, v199, v195\n"
      "v_accvgpr_write_b32 a188, v196\n"
      "v_accvgpr_write_b32 a189, v197\n"
      "v_accvgpr_write_b32 a190, v198\n"
      "v_accvgpr_write_b32 a191, v199\n"
      "v_accvgpr_read_b32 v196, a192\n"
      "v_accvgpr_read_b32 v197, a193\n"
      "v_accvgpr_read_b32 v198, a194\n"
      "v_accvgpr_read_b32 v199, a195\n"
      "v_mul_f32 v196, v196, v194\n"
      "v_mul_f32 v197, v197, v194\n"
      "v_mul_f32 v198, v198, v194\n"
      "v_mul_f32 v199, v199, v194\n"
      "v_accvgpr_write_b32 a192, v196\n"
      "v_accvgpr_write_b32 a193, v197\n"
      "v_accvgpr_write_b32 a194, v198\n"
      "v_accvgpr_write_b32 a195, v199\n"
      "v_accvgpr_read_b32 v196, a196\n"
      "v_accvgpr_read_b32 v197, a197\n"
      "v_accvgpr_read_b32 v198, a198\n"
      "v_accvgpr_read_b32 v199, a199\n"
      "v_mul_f32 v196, v196, v195\n"
      "v_mul_f32 v197, v197, v195\n"
      "v_mul_f32 v198, v198, v195\n"
      "v_mul_f32 v199, v199, v195\n"
      "v_accvgpr_write_b32 a196, v196\n"
      "v_accvgpr_write_b32 a197, v197\n"
      "v_accvgpr_write_b32 a198, v198\n"
      "v_accvgpr_write_b32 a199, v199\n"
      "v_accvgpr_read_b32 v196, a200\n"
      "v_accvgpr_read_b32 v197, a201\n"
      "v_accvgpr_read_b32 v198, a202\n"
      "v_accvgpr_read_b32 v199, a203\n"
      "v_mul_f32 v196, v196, v194\n"
      "v_mul_f32 v197, v197, v194\n"
      "v_mul_f32 v198, v198, v194\n"
      "v_mul_f32 v199, v199, v194\n"
      "v_accvgpr_write_b32 a200, v196\n"
      "v_accvgpr_write_b32 a201, v197\n"
      "v_accvgpr_write_b32 a202, v198\n"
      "v_accvgpr_write_b32 a203, v199\n"
      "v_accvgpr_read_b32 v196, a204\n"
      "v_accvgpr_read_b32 v197, a205\n"
      "v_accvgpr_read_b32 v198, a206\n"
      "v_accvgpr_read_b32 v199, a207\n"
      "v_mul_f32 v196, v196, v195\n"
      "v_mul_f32 v197, v197, v195\n"
      "v_mul_f32 v198, v198, v195\n"
      "v_mul_f32 v199, v199, v195\n"
      "v_accvgpr_write_b32 a204, v196\n"
      "v_accvgpr_write_b32 a205, v197\n"
      "v_accvgpr_write_b32 a206, v198\n"
      "v_accvgpr_write_b32 a207, v199\n"
      "v_mul_f32 v180, v180, v192\n"
      "v_mul_f32 v181, v181, v193\n"
      "v_mul_f32 v182, v182, v194\n"
      "v_mul_f32 v183, v183, v195\n"
      "v_lshlrev_b32 v196, 16, v128\n"
      "v_and_b32 v197, 0xffff0000, v128\n"
      "v_mul_f32 v196, v196, v192\n"
      "v_mul_f32 v197, v197, v192\n"
      "v_cvt_pk_bf16_f32 v128, v196, v197\n"
      "v_lshlrev_b32 v196, 16, v129\n"
      "v_and_b32 v197, 0xffff0000, v129\n"
      "v_mul_f32 v196, v196, v192\n"
      "v_mul_f32 v197, v197, v192\n"
      "v_cvt_pk_bf16_f32 v129, v196, v197\n"
      "v_lshlrev_b32 v196, 16, v130\n"
      "v_and_b32 v197, 0xffff0000, v130\n"
      "v_mul_f32 v196, v196, v192\n"
      "v_mul_f32 v197, v197, v192\n"
      "v_cvt_pk_bf16_f32 v130, v196, v197\n"
      "v_lshlrev_b32 v196, 16, v131\n"
      "v_and_b32 v197, 0xffff0000, v131\n"
      "v_mul_f32 v196, v196, v192\n"
      "v_mul_f32 v197, v197, v192\n"
      "v_cvt_pk_bf16_f32 v131, v196, v197\n"
      "v_lshlrev_b32 v196, 16, v132\n"
      "v_and_b32 v197, 0xffff0000, v132\n"
      "v_mul_f32 v196, v196, v193\n"
      "v_mul_f32 v197, v197, v193\n"
      "v_cvt_pk_bf16_f32 v132, v196, v197\n"
      "v_lshlrev_b32 v196, 16, v133\n"
      "v_and_b32 v197, 0xffff0000, v133\n"
      "v_mul_f32 v196, v196, v193\n"
      "v_mul_f32 v197, v197, v193\n"
      "v_cvt_pk_bf16_f32 v133, v196, v197\n"
      "v_lshlrev_b32 v196, 16, v134\n"
      "v_and_b32 v197, 0xffff0000, v134\n"
      "v_mul_f32 v196, v196, v193\n"
      "v_mul_f32 v197, v197, v193\n"
      "v_cvt_pk_bf16_f32 v134, v196, v197\n"
      "v_lshlrev_b32 v196, 16, v135\n"
      "v_and_b32 v197, 0xffff0000, v135\n"
      "v_mul_f32 v196, v196, v193\n"
      "v_mul_f32 v197, v197, v193\n"
      "v_cvt_pk_bf16_f32 v135, v196, v197\n"
      "v_lshlrev_b32 v196, 16, v136\n"
      "v_and_b32 v197, 0xffff0000, v136\n"
      "v_mul_f32 v196, v196, v194\n"
      "v_mul_f32 v197, v197, v194\n"
      "v_cvt_pk_bf16_f32 v136, v196, v197\n"
      "v_lshlrev_b32 v196, 16, v137\n"
      "v_and_b32 v197, 0xffff0000, v137\n"
      "v_mul_f32 v196, v196, v194\n"
      "v_mul_f32 v197, v197, v194\n"
      "v_cvt_pk_bf16_f32 v137, v196, v197\n"
      "v_lshlrev_b32 v196, 16, v138\n"
      "v_and_b32 v197, 0xffff0000, v138\n"
      "v_mul_f32 v196, v196, v194\n"
      "v_mul_f32 v197, v197, v194\n"
      "v_cvt_pk_bf16_f32 v138, v196, v197\n"
      "v_lshlrev_b32 v196, 16, v139\n"
      "v_and_b32 v197, 0xffff0000, v139\n"
      "v_mul_f32 v196, v196, v194\n"
      "v_mul_f32 v197, v197, v194\n"
      "v_cvt_pk_bf16_f32 v139, v196, v197\n"
      "v_lshlrev_b32 v196, 16, v140\n"
      "v_and_b32 v197, 0xffff0000, v140\n"
      "v_mul_f32 v196, v196, v195\n"
      "v_mul_f32 v197, v197, v195\n"
      "v_cvt_pk_bf16_f32 v140, v196, v197\n"
      "v_lshlrev_b32 v196, 16, v141\n"
      "v_and_b32 v197, 0xffff0000, v141\n"
      "v_mul_f32 v196, v196, v195\n"
      "v_mul_f32 v197, v197, v195\n"
      "v_cvt_pk_bf16_f32 v141, v196, v197\n"
      "v_lshlrev_b32 v196, 16, v142\n"
      "v_and_b32 v197, 0xffff0000, v142\n"
      "v_mul_f32 v196, v196, v195\n"
      "v_mul_f32 v197, v197, v195\n"
      "v_cvt_pk_bf16_f32 v142, v196, v197\n"
      "v_lshlrev_b32 v196, 16, v143\n"
      "v_and_b32 v197, 0xffff0000, v143\n"
      "v_mul_f32 v196, v196, v195\n"
      "v_mul_f32 v197, v197, v195\n"
      "v_cvt_pk_bf16_f32 v143, v196, v197\n"
      "v_accvgpr_read_b32 v197, a208\n"
      "v_lshlrev_b32 v196, 16, v197\n"
      "v_and_b32 v197, 0xffff0000, v197\n"
      "v_mul_f32 v196, v196, v192\n"
      "v_mul_f32 v197, v197, v192\n"
      "v_cvt_pk_bf16_f32 v196, v196, v197\n"
      "v_accvgpr_write_b32 a208, v196\n"
      "v_accvgpr_read_b32 v197, a209\n"
      "v_lshlrev_b32 v196, 16, v197\n"
      "v_and_b32 v197, 0xffff0000, v197\n"
      "v_mul_f32 v196, v196, v192\n"
      "v_mul_f32 v197, v197, v192\n"
      "v_cvt_pk_bf16_f32 v196, v196, v197\n"
      "v_accvgpr_write_b32 a209, v196\n"
      "v_accvgpr_read_b32 v197, a210\n"
      "v_lshlrev_b32 v196, 16, v197\n"
      "v_and_b32 v197, 0xffff0000, v197\n"
      "v_mul_f32 v196, v196, v192\n"
      "v_mul_f32 v197, v197, v192\n"
      "v_cvt_pk_bf16_f32 v196, v196, v197\n"
      "v_accvgpr_write_b32 a210, v196\n"
      "v_accvgpr_read_b32 v197, a211\n"
      "v_lshlrev_b32 v196, 16, v197\n"
      "v_and_b32 v197, 0xffff0000, v197\n"
      "v_mul_f32 v196, v196, v192\n"
      "v_mul_f32 v197, v197, v192\n"
      "v_cvt_pk_bf16_f32 v196, v196, v197\n"
      "v_accvgpr_write_b32 a211, v196\n"
      "v_accvgpr_read_b32 v197, a212\n"
      "v_lshlrev_b32 v196, 16, v197\n"
      "v_and_b32 v197, 0xffff0000, v197\n"
      "v_mul_f32 v196, v196, v193\n"
      "v_mul_f32 v197, v197, v193\n"
      "v_cvt_pk_bf16_f32 v196, v196, v197\n"
      "v_accvgpr_write_b32 a212, v196\n"
      "v_accvgpr_read_b32 v197, a213\n"
      "v_lshlrev_b32 v196, 16, v197\n"
      "v_and_b32 v197, 0xffff0000, v197\n"
      "v_mul_f32 v196, v196, v193\n"
      "v_mul_f32 v197, v197, v193\n"
      "v_cvt_pk_bf16_f32 v196, v196, v197\n"
      "v_accvgpr_write_b32 a213, v196\n"
      "v_accvgpr_read_b32 v197, a214\n"
      "v_lshlrev_b32 v196, 16, v197\n"
      "v_and_b32 v197, 0xffff0000, v197\n"
      "v_mul_f32 v196, v196, v193\n"
      "v_mul_f32 v197, v197, v193\n"
      "v_cvt_pk_bf16_f32 v196, v196, v197\n"
      "v_accvgpr_write_b32 a214, v196\n"
      "v_accvgpr_read_b32 v197, a215\n"
      "v_lshlrev_b32 v196, 16, v197\n"
      "v_and_b32 v197, 0xffff0000, v197\n"
      "v_mul_f32 v196, v196, v193\n"
      "v_mul_f32 v197, v197, v193\n"
      "v_cvt_pk_bf16_f32 v196, v196, v197\n"
      "v_accvgpr_write_b32 a215, v196\n"
      "v_accvgpr_read_b32 v197, a216\n"
      "v_lshlrev_b32 v196, 16, v197\n"
      "v_and_b32 v197, 0xffff0000, v197\n"
      "v_mul_f32 v196, v196, v194\n"
      "v_mul_f32 v197, v197, v194\n"
      "v_cvt_pk_bf16_f32 v196, v196, v197\n"
      "v_accvgpr_write_b32 a216, v196\n"
      "v_accvgpr_read_b32 v197, a217\n"
      "v_lshlrev_b32 v196, 16, v197\n"
      "v_and_b32 v197, 0xffff0000, v197\n"
      "v_mul_f32 v196, v196, v194\n"
      "v_mul_f32 v197, v197, v194\n"
      "v_cvt_pk_bf16_f32 v196, v196, v197\n"
      "v_accvgpr_write_b32 a217, v196\n"
      "v_accvgpr_read_b32 v197, a218\n"
      "v_lshlrev_b32 v196, 16, v197\n"
      "v_and_b32 v197, 0xffff0000, v197\n"
      "v_mul_f32 v196, v196, v194\n"
      "v_mul_f32 v197, v197, v194\n"
      "v_cvt_pk_bf16_f32 v196, v196, v197\n"
      "v_accvgpr_write_b32 a218, v196\n"
      "v_accvgpr_read_b32 v197, a219\n"
      "v_lshlrev_b32 v196, 16, v197\n"
      "v_and_b32 v197, 0xffff0000, v197\n"
      "v_mul_f32 v196, v196, v194\n"
      "v_mul_f32 v197, v197, v194\n"
      "v_cvt_pk_bf16_f32 v196, v196, v197\n"
      "v_accvgpr_write_b32 a219, v196\n"
      "v_accvgpr_read_b32 v197, a220\n"
      "v_lshlrev_b32 v196, 16, v197\n"
      "v_and_b32 v197, 0xffff0000, v197\n"
      "v_mul_f32 v196, v196, v195\n"
      "v_mul_f32 v197, v197, v195\n"
      "v_cvt_pk_bf16_f32 v196, v196, v197\n"
      "v_accvgpr_write_b32 a220, v196\n"
      "v_accvgpr_read_b32 v197, a221\n"
      "v_lshlrev_b32 v196, 16, v197\n"
      "v_and_b32 v197, 0xffff0000, v197\n"
      "v_mul_f32 v196, v196, v195\n"
      "v_mul_f32 v197, v197, v195\n"
      "v_cvt_pk_bf16_f32 v196, v196, v197\n"
      "v_accvgpr_write_b32 a221, v196\n"
      "v_accvgpr_read_b32 v197, a222\n"
      "v_lshlrev_b32 v196, 16, v197\n"
      "v_and_b32 v197, 0xffff0000, v197\n"
      "v_mul_f32 v196, v196, v195\n"
      "v_mul_f32 v197, v197, v195\n"
      "v_cvt_pk_bf16_f32 v196, v196, v197\n"
      "v_accvgpr_write_b32 a222, v196\n"
      "v_accvgpr_read_b32 v197, a223\n"
      "v_lshlrev_b32 v196, 16, v197\n"
      "v_and_b32 v197, 0xffff0000, v197\n"
      "v_mul_f32 v196, v196, v195\n"
      "v_mul_f32 v197, v197, v195\n"
      "v_cvt_pk_bf16_f32 v196, v196, v197\n"
      "v_accvgpr_write_b32 a223, v196\n"
      "v_exp_f32 v188, v96\n"
      "v_exp_f32 v189, v97\n"
      "v_exp_f32 v190, v98\n"
      "v_exp_f32 v191, v99\n"
      "v_cvt_pk_bf16_f32 v144, v188, v189\n"
      "v_cvt_pk_bf16_f32 v145, v190, v191\n"
      "v_exp_f32 v192, v100\n"
      "v_exp_f32 v193, v101\n"
      "v_exp_f32 v194, v102\n"
      "v_exp_f32 v195, v103\n"
      "v_cvt_pk_bf16_f32 v146, v192, v193\n"
      "v_cvt_pk_bf16_f32 v147, v194, v195\n"
      "v_exp_f32 v188, v104\n"
      "v_exp_f32 v189, v105\n"
      "v_exp_f32 v190, v106\n"
      "v_exp_f32 v191, v107\n"
      "v_cvt_pk_bf16_f32 v148, v188, v189\n"
      "v_cvt_pk_bf16_f32 v149, v190, v191\n"
      "v_exp_f32 v192, v108\n"
      "v_exp_f32 v193, v109\n"
      "v_exp_f32 v194, v110\n"
      "v_exp_f32 v195, v111\n"
      "v_cvt_pk_bf16_f32 v150, v192, v193\n"
      "v_cvt_pk_bf16_f32 v151, v194, v195\n"
      "v_exp_f32 v188, v112\n"
      "v_exp_f32 v189, v113\n"
      "v_exp_f32 v190, v114\n"
      "v_exp_f32 v191, v115\n"
      "v_cvt_pk_bf16_f32 v152, v188, v189\n"
      "v_cvt_pk_bf16_f32 v153, v190, v191\n"
      "v_exp_f32 v192, v116\n"
      "v_exp_f32 v193, v117\n"
      "v_exp_f32 v194, v118\n"
      "v_exp_f32 v195, v119\n"
      "v_cvt_pk_bf16_f32 v154, v192, v193\n"
      "v_cvt_pk_bf16_f32 v155, v194, v195\n"
      "v_exp_f32 v188, v120\n"
      "v_exp_f32 v189, v121\n"
      "v_exp_f32 v190, v122\n"
      "v_exp_f32 v191, v123\n"
      "v_cvt_pk_bf16_f32 v156, v188, v189\n"
      "v_cvt_pk_bf16_f32 v157, v190, v191\n"
      "v_exp_f32 v192, v124\n"
      "v_exp_f32 v193, v125\n"
      "v_exp_f32 v194, v126\n"
      "v_exp_f32 v195, v127\n"
      "v_cvt_pk_bf16_f32 v158, v192, v193\n"
      "v_cvt_pk_bf16_f32 v159, v194, v195\n"
      "s_nop 1\n"
      "v_mfma_f32_16x16x32_bf16 v[184:187], a[128:131], v[144:147], 0\n"
      "v_mfma_f32_16x16x32_bf16 v[184:187], a[132:135], v[148:151], v[184:187]\n"
      "v_mfma_f32_16x16x32_bf16 v[184:187], a[136:139], v[152:155], v[184:187]\n"
      "v_mfma_f32_16x16x32_bf16 v[184:187], a[140:143], v[156:159], v[184:187]\n"
      "s_nop 7\n"
      "s_setpc_b64 s[96:97]\n"
      "SLOW_%=:\n"
      "ds_write_b128 v234, v[176:179] offset:5120\n"
      "s_waitcnt lgkmcnt(0)\n"
      "s_barrier\n"
      "ds_read_b128 v[192:195], v235 offset:5120\n"
      "s_waitcnt lgkmcnt(0)\n"
      "v_sub_f32 v188, v176, v194\n"
      "v_sub_f32 v189, v177, v195\n"
      "v_sub_f32 v190, v178, v192\n"
      "v_sub_f32 v191, v179, v193\n"
      "v_max_f32 v188, 0, v188\n"
      "v_max_f32 v189, 0, v189\n"
      "v_max_f32 v190, 0, v190\n"
      "v_max_f32 v191, 0, v191\n"
      "s_call_b64 s[96:97], RS1_%=\n"
      "ds_write_b128 v232, a[176:179] offset:0\n"
      "ds_write_b128 v232, a[180:183] offset:1024\n"
      "ds_write_b128 v232, a[184:187] offset:2048\n"
      "ds_write_b128 v232, a[188:191] offset:3072\n"
      "ds_write_b128 v232, a[192:195] offset:4096\n"
      "ds_write_b128 v232, a[196:199] offset:5120\n"
      "ds_write_b128 v232, a[200:203] offset:6144\n"
      "ds_write_b128 v232, a[204:207] offset:7168\n"
      "ds_write_b128 v234, v[152:155] offset:0\n"
      "ds_write_b128 v234, v[156:159] offset:1024\n"
      "v_cndmask_b32 v229, v180, v181, s[74:75]\n"
      "v_cndmask_b32 v200, v182, v183, s[74:75]\n"
      "v_cndmask_b32 v228, v176, v177, s[74:75]\n"
      "v_cndmask_b32 v202, v186, v187, s[74:75]\n"
      "v_cndmask_b32 v201, v184, v185, s[74:75]\n"
      "v_add_f32 v200, v200, v202\n"
      "ds_write_b32 v234, v200 offset:4096\n"
      "v_add_f32 v229, v229, v201\n"
      "s_waitcnt lgkmcnt(0)\n"
      "s_barrier\n"
      "ds_read_b32 v208, v209\n"
      "ds_read_b128 v[160:163], v235 offset:0\n"
      "ds_read_b128 v[164:167], v235 offset:1024\n"
      "ds_read_b128 v[64:67], v233 offset:0\n"
      "ds_read_b128 v[68:71], v233 offset:1024\n"
      "ds_read_b128 v[72:75], v233 offset:2048\n"
      "ds_read_b128 v[76:79], v233 offset:3072\n"
      "ds_read_b128 v[80:83], v233 offset:4096\n"
      "ds_read_b128 v[84:87], v233 offset:5120\n"
      "ds_read_b128 v[88:91], v233 offset:6144\n"
      "ds_read_b128 v[92:95], v233 offset:7168\n"
      "ds_read_b32 v204, v235 offset:4096\n"
      "s_branch AGAIN_%=\n"
      ".subsection 0\n"
      : "+{v0}"(tid)
      : [blk] "s"(blockIdx.x), [kp] "s"(__builtin_amdgcn_kernarg_segment_ptr()), "s"(lds0)
      : "v1", "v2", "v3", "v4", "v5", "v6", "v7", "v8", "v9", "v10", "v11", "v12", "v13", "v14", "v15", "v16", "v17", "v18", "v19", "v20", "v21", "v22", "v23", "v24", "v25", "v26", "v27", "v28", "v29", "v30", "v31", "v32", "v33", "v34", "v35", "v36", "v37", "v38", "v39", "v40", "v41", "v42", "v43", "v44", "v45", "v46", "v47", "v48", "v49", "v50", "v51", "v52", "v53", "v54", "v55", "v56", "v57", "v58", "v59", "v60", "v61", "v62", "v63", "v64", "v65", "v66", "v67", "v68", "v69", "v70", "v71", "v72", "v73", "v74", "v75", "v76", "v77", "v78", "v79", "v80", "v81", "v82", "v83", "v84", "v85", "v86", "v87", "v88", "v89", "v90", "v91", "v92", "v93", "v94", "v95", "v96", "v97", "v98", "v99", "v100", "v101", "v102", "v103", "v104", "v105", "v106", "v107", "v108", "v109", "v110", "v111", "v112", "v113", "v114", "v115", "v116", "v117", "v118", "v119", "v120", "v121", "v122", "v123", "v124", "v125", "v126", "v127", "v128", "v129", "v130", "v131", "v132", "v133", "v134", "v135", "v136", "v137", "v138", "v139", "v140", "v141", "v142", "v143", "v144", "v145", "v146", "v147", "v148", "v149", "v150", "v151", "v152", "v153", "v154", "v155", "v156", "v157", "v158", "v159", "v160", "v161", "v162", "v163", "v164", "v165", "v166", "v167", "v168", "v169", "v170", "v171", "v172", "v173", "v174", "v175", "v176", "v177", "v178", "v179", "v180", "v181", "v182", "v183", "v184", "v185", "v186", "v187", "v188", "v189", "v190", "v191", "v192", "v193", "v194", "v195", "v196", "v197", "v198", "v199", "v200", "v201", "v202", "v203", "v204", "v205", "v206", "v207", "v208", "v209", "v210", "v211", "v212", "v213", "v214", "v215", "v216", "v217", "v218", "v219", "v220", "v221", "v222", "v223", "v224", "v225", "v226", "v227", "v228", "v229", "v230", "v231", "v232", "v233", "v234", "v235", "v236", "v237", "v238", "v239", "v240", "v241", "v242", "v243", "v244", "v245", "v246", "v247", "v248", "v249", "v250", "v251", "a0", "a1", "a2", "a3", "a4", "a5", "a6", "a7", "a8", "a9", "a10", "a11", "a12", "a13", "a14", "a15", "a16", "a17", "a18", "a19", "a20", "a21", "a22", "a23", "a24", "a25", "a26", "a27", "a28", "a29", "a30", "a31", "a32", "a33", "a34", "a35", "a36", "a37", "a38", "a39", "a40", "a41", "a42", "a43", "a44", "a45", "a46", "a47", "a48", "a49", "a50", "a51", "a52", "a53", "a54", "a55", "a56", "a57", "a58", "a59", "a60", "a61", "a62", "a63", "a64", "a65", "a66", "a67", "a68", "a69", "a70", "a71", "a72", "a73", "a74", "a75", "a76", "a77", "a78", "a79", "a80", "a81", "a82", "a83", "a84", "a85", "a86", "a87", "a88", "a89", "a90", "a91", "a92", "a93", "a94", "a95", "a96", "a97", "a98", "a99", "a100", "a101", "a102", "a103", "a104", "a105", "a106", "a107", "a108", "a109", "a110", "a111", "a112", "a113", "a114", "a115", "a116", "a117", "a118", "a119", "a120", "a121", "a122", "a123", "a124", "a125", "a126", "a127", "a128", "a129", "a130", "a131", "a132", "a133", "a134", "a135", "a136", "a137", "a138", "a139", "a140", "a141", "a142", "a143", "a144", "a145", "a146", "a147", "a148", "a149", "a150", "a151", "a152", "a153", "a154", "a155", "a156", "a157", "a158", "a159", "a160", "a161", "a162", "a163", "a164", "a165", "a166", "a167", "a168", "a169", "a170", "a171", "a172", "a173", "a174", "a175", "a176", "a177", "a178", "a179", "a180", "a181", "a182", "a183", "a184", "a185", "a186", "a187", "a188", "a189", "a190", "a191", "a192", "a193", "a194", "a195", "a196", "a197", "a198", "a199", "a200", "a201", "a202", "a203", "a204", "a205", "a206", "a207", "a208", "a209", "a210", "a211", "a212", "a213", "a214", "a215", "a216", "a217", "a218", "a219", "a220", "a221", "a222", "a223", "s4", "s5", "s6", "s7", "s8", "s9", "s10", "s11", "s12", "s13", "s14", "s15", "s16", "s17", "s18", "s19", "s20", "s21", "s22", "s23", "s24", "s25", "s26", "s27", "s28", "s29", "s30", "s31", "s32", "s33", "s34", "s35", "s38", "s39", "s40", "s43", "s44", "s45", "s46", "s47", "s50", "s52", "s53", "s54", "s56", "s57", "s58", "s59", "s60", "s61", "s62", "s63", "s64", "s65", "s66", "s67", "s68", "s69", "s70", "s71", "s72", "s74", "s75", "s76", "s77", "s78", "s79", "s80", "s81", "s82", "s83", "s84", "s85", "s86", "s87", "s88", "s89", "s90", "s91", "s92", "s93", "s94", "s95", "s96", "s97", "vcc", "scc", "memory");
}

}

extern "C" void kernel_launch(void* const* d_in, const int* in_sizes, int n_in, void* d_out, int out_size,
                              void* d_ws, size_t ws_size, hipStream_t stream) {
  if (n_in != 6 || ws_size < WS_END) return;
  const __hip_bfloat16* q = (const __hip_bfloat16*)d_in[0];
  const __hip_bfloat16* kc = (const __hip_bfloat16*)d_in[1];
  const __hip_bfloat16* vc = (const __hip_bfloat16*)d_in[2];
  const __hip_bfloat16* kt = (const __hip_bfloat16*)d_in[3];
  const __hip_bfloat16* vt = (const __hip_bfloat16*)d_in[4];
  const __hip_bfloat16* tm = (const __hip_bfloat16*)d_in[5];
  hipLaunchKernelGGL(tree_attn_main, dim3(B_ * HKV_ * 2), dim3(256), 0, stream, q, kc, vc, kt, vt, tm,
                     (__hip_bfloat16*)d_out, (char*)d_ws);
}
```
